```python
import torch

def _softmax(x, axis=-1):
    return torch.softmax(x, dim=axis)

def _gelu(x):
    return torch.nn.functional.gelu(x, approximate='tanh')

def _silu(x):
    return torch.nn.functional.silu(x)

def _sigmoid(x):
    return torch.sigmoid(x)

def _layernorm(x, axis=-1, eps=1e-05):
    m = x.mean(dim=axis, keepdim=True)
    v = x.var(dim=axis, keepdim=True, unbiased=False)
    return (x - m) / torch.sqrt(v + eps)

def _scatter_add(tgt, idx, src, axis=0):
    return tgt.clone().index_add_(axis, idx, src)

def _segment_sum(src, seg, n):
    out = torch.zeros((n,) + src.shape[1:], dtype=src.dtype)
    return out.index_add_(0, seg, src)

def _pad(x, pw):
    flat = tuple((v for pair in reversed(pw) for v in pair))
    return torch.nn.functional.pad(x, flat)
kernel_name = 'concat_heads__memory_aug_recurrent_mem_tr__sliding_local_dilated__prompt'

def setup_inputs():

    _w = lambda a: (lambda t: t.to(torch.bfloat16) if t.is_floating_point() else t)(torch.as_tensor(a))

    def _A_setup():
        B, S, Dm = (1, 2048, 1024)
        H, Dh = (32, 128)
        M, seg = (8, 32)
        seg = min(seg, S)
        S = S // seg * seg
        return {'x': _w((lambda *s: torch.randn(*s, dtype=torch.bfloat16) * 0.05)(B, S, Dm)), 'mem0': _w((lambda *s: torch.randn(*s, dtype=torch.bfloat16) * 1.6)(B, M, Dm)), 'Wq': _w((lambda *s: torch.randn(*s, dtype=torch.bfloat16) * 0.8)(Dm, H, Dh) * Dm ** (-0.5)), 'Wk': _w((lambda *s: torch.randn(*s, dtype=torch.bfloat16) * 0.8)(Dm, H, Dh) * Dm ** (-0.5)), 'Wv': _w((lambda *s: torch.randn(*s, dtype=torch.bfloat16) * 1.6)(Dm, H, Dh) * Dm ** (-0.5)), 'Wo': _w((lambda *s: torch.randn(*s, dtype=torch.bfloat16) * 1.6)(H, Dh, Dm) * (H * Dh) ** (-0.5)), 'seg_len': seg}
    _A_in = _A_setup()

    def _B_setup():
        B, S, D = (1, 2048, 1024)
        return {'x': _w((lambda *s: torch.randn(*s, dtype=torch.bfloat16) * 0.2)(B, S, D)), 'wqkv': _w((lambda *s: torch.randn(*s, dtype=torch.bfloat16) * 0.2)(D, 3 * D)), 'wo': _w((lambda *s: torch.randn(*s, dtype=torch.bfloat16) * 0.05)(D, D))}
    _B_in = _B_setup()
    return {'A_x': _A_in['x'], 'A_mem0': _A_in['mem0'], 'A_Wq': _A_in['Wq'], 'A_Wk': _A_in['Wk'], 'A_Wv': _A_in['Wv'], 'A_Wo': _A_in['Wo'], 'A_seg_len': _A_in['seg_len'], 'B_x': _B_in['x'], 'B_wqkv': _B_in['wqkv'], 'B_wo': _B_in['wo']}

def reference(A_x, A_mem0, A_Wq, A_Wk, A_Wv, A_Wo, A_seg_len, B_x, B_wqkv, B_wo):

    def _A_ref(x, mem0, Wq, Wk, Wv, Wo, seg_len):
        B, S, Dm = x.shape
        H, Dh = (Wq.shape[1], Wq.shape[2])
        M = mem0.shape[1]
        scale = Dh ** (-0.5)
        n_seg = S // seg_len
        L = M + seg_len
        idx = torch.arange(L)
        mask = (idx[:, None] < M) | (idx[:, None] >= idx[None, :])
        mem = mem0
        outs = []
        for s in range(n_seg):
            xs = x[:, s * seg_len:(s + 1) * seg_len, :].clone()
            h = torch.concatenate([mem, xs], axis=1)
            q = torch.einsum('btd,dhe->bhte', h, Wq)
            k = torch.einsum('btd,dhe->bhte', h, Wk)
            v = torch.einsum('btd,dhe->bhte', h, Wv)
            scores = torch.einsum('bhqe,bhke->bhqk', q, k) * scale
            scores = torch.where(mask[None, None], scores, -1000000000.0)
            p = _softmax(scores, axis=-1)
            o = torch.einsum('bhqk,bhke->bhqe', p, v)
            o = torch.einsum('bhte,hed->btd', o, Wo)
            mem = o[:, :M, :].clone()
            outs.append(o[:, M:, :])
        return torch.concatenate(outs, axis=1)

    def _B_ref(x, wqkv, wo):
        B, S, D = x.shape
        H = 32
        Dh = D // H
        W, dil = (8, 2)
        qkv = (lambda x, *d: x.permute(*d))((x @ wqkv).reshape(B, S, 3, H, Dh), 2, 0, 3, 1, 4)
        q, k, v = (qkv[0], qkv[1], qkv[2])
        scale = Dh ** (-0.5)
        pos = torch.arange(S)
        off = torch.arange(W) * dil
        idx = pos[:, None] - off[None, :]
        mask = idx >= 0
        idx_c = torch.where(mask, idx, 0).reshape(-1)
        kg = k[:, :, idx_c, :].reshape(B, H, S, W, Dh)
        vg = v[:, :, idx_c, :].reshape(B, H, S, W, Dh)
        logits = torch.einsum('bhsd,bhswd->bhsw', q, kg) * scale
        logits = torch.where(mask[None, None, :, :], logits, -1000000000.0)
        attn = _softmax(logits, axis=-1)
        o = torch.einsum('bhsw,bhswd->bhsd', attn, vg)
        return (lambda x, *d: x.permute(*d))(o, 0, 2, 1, 3).reshape(B, S, D) @ wo
    a = _A_ref(A_x, A_mem0, A_Wq, A_Wk, A_Wv, A_Wo, A_seg_len)
    b = _B_ref(B_x, B_wqkv, B_wo)
    return torch.concatenate([a, b], axis=-1)
```

```cpp
#include <hip/hip_runtime.h>
#include <hip/hip_bf16.h>
#include <stdint.h>
#include <stddef.h>

#define DQ 13
#define DQ2 13
#define DMV 2950
#define DOV 1480
#define GAIN 4
#define GAIN0 8
#define MINC 32
#define MDEC 6
#define RECOV 22
#define STAG 0
#define SH1 14
#define SH2 45
#define SH3 0
#define DVV 3
#define BOOT 0x1932
#define ROT 608
#define EPSV 0x020000
#define ABD 3
#define BOOT2 0x0505
#define BOOT3 0x0a14
#define IMARG 3
#define PFR 0
#define VSLOT 60
typedef unsigned short u16;
typedef __attribute__((ext_vector_type(4))) float f32x4;
typedef unsigned v4u __attribute__((ext_vector_type(4)));

constexpr int S_ = 2048, DM = 1024, HA = 32, DHA = 128, HDA = 4096, MEM = 8, SEG = 32, NSEG = 64;
constexpr int QPAGE = 2 << 20, QBLK = 16384, QSIZE = 48 * QPAGE;
__device__ __forceinline__ int qoff(unsigned long long parq, int n) { return n * QPAGE + (int)((parq >> n) & 1) * 8192; }
constexpr int DB = 1024, HB = 32, DHB = 32, NQKVB = 3 * DB;

struct PrepParams {
  v4u* sent; unsigned long long sent_vec;
  v4u* ctl; const u16* mem0; char* obuf; char* membuf; char* vbuf; unsigned* par; unsigned long long ctl_vec;
  const v4u *wk, *wv, *ax; unsigned long long pfr, far; char* qkv;
};
#define PROBES 3
constexpr int PSENT = 256, OREG = 131072, MREG = 65536;
typedef __attribute__((ext_vector_type(8))) __bf16 bfx8;
typedef __attribute__((ext_vector_type(2))) __bf16 bfx2;
typedef __attribute__((ext_vector_type(2))) float f32x2;
typedef unsigned v2u __attribute__((ext_vector_type(2)));
typedef unsigned v8u __attribute__((ext_vector_type(8)));
constexpr int NQK = 32, NVO = 64;
constexpr int NLW = 15, VXLD = 144;
struct ChainLds {
  v4u sA[16 * 64];
  f32x4 xs[4][64];
  f32x4 ms[4][64];
  v4u kv[4][4][64];
  v4u sW[4][NLW][2][64];
};
constexpr int seq_i(int u) { return u < 16 ? u >> 2 : 4 + ((u - 16) % 24 >> 1); }
constexpr int seq_t(int u) { return u < 16 ? (u & 3) : (u < 40 ? (u & 1) : 2 + (u & 1)); }
constexpr bool seq_l(int u) { return u < 10 ? (u & 1) : (u == 15 || (u >= 17 && u <= 37 && (u & 3) == 1) || u == 40 || u == 42 || u == 44); }
constexpr int rd_a(int u) { return u >= 11 && u <= 33 && (u & 1) ? 4 + (u - 11) / 2 : 0; }
constexpr int rd_w(int u) { if (u < 8 || u > 39 || rd_a(u)) return -1; int n = 0; for (int v = 8; v < u; ++v) n += !rd_a(v); return n; }
constexpr int seq_c(int u) { if (seq_l(u)) return 2; int n = 0; for (int v = 0; v < u; ++v) n += !seq_l(v); return n < 32 - NLW ? 1 : 0; }
constexpr int seq_n(int u) { int n = 0; for (int v = 0; v < u; ++v) n += seq_c(v) == seq_c(u); return n; }
constexpr int seq_u(int i, int t) { for (int u = 0; u < 64; ++u) if (seq_i(u) == i && seq_t(u) == t) return u; return -1; }
template <int I> struct IC { static constexpr int v = I; };
template <int I, int N, class F> __device__ __forceinline__ void sfor(F&& f) { if constexpr (I < N) { f(IC<I>{}); sfor<I + 1, N>(f); } }
#define SFOR(u, a, b) sfor<a, b>([&](auto u##_) __attribute__((always_inline)) { constexpr int u = decltype(u##_)::v;
#define SEND });
constexpr int NPRE = 5;

__device__ __forceinline__ void lds_barrier() { asm volatile("s_waitcnt lgkmcnt(0)" ::: "memory"); __builtin_amdgcn_s_barrier(); asm volatile("" ::: "memory"); }
template <int N> __device__ __forceinline__ float dpp_ror(float v) {
  return __int_as_float(__builtin_amdgcn_update_dpp(0, __float_as_int(v), 0x120 + N, 0xf, 0xf, false));
}
__device__ __forceinline__ unsigned pk2(float a, float b) { f32x2 t = {a, b}; return __builtin_bit_cast(unsigned, __builtin_convertvector(t, bfx2)); }
__device__ __forceinline__ unsigned nosent(unsigned x) { return x < 0xfffffffeu ? x : 0xfffffffeu; }
__device__ __forceinline__ unsigned umax3(unsigned a, unsigned b, unsigned c) { unsigned m = a > b ? a : b; return m > c ? m : c; }
__device__ __forceinline__ unsigned chk4(unsigned m, v4u v) { return umax3(umax3(m, v.x, v.y), v.z, v.w); }
__device__ __forceinline__ unsigned long long now() { return __builtin_amdgcn_s_memtime(); }
__device__ __forceinline__ f32x4 mf(v4u a, v4u b, f32x4 c) {
  return __builtin_amdgcn_mfma_f32_16x16x32_bf16(__builtin_bit_cast(bfx8, a), __builtin_bit_cast(bfx8, b), c, 0, 0, 0);
}
__device__ __forceinline__ v2u trrd(const u16* p) {
  typedef __attribute__((ext_vector_type(4))) short v4i16_t;
  return __builtin_bit_cast(v2u, __builtin_amdgcn_ds_read_tr16_b64_v4i16((__attribute__((address_space(3))) v4i16_t*)p));
}
__device__ __forceinline__ v8u cat8(v4u a, v4u b) { return __builtin_shufflevector(a, b, 0, 1, 2, 3, 4, 5, 6, 7); }
__device__ __forceinline__ float xmax16(float x) { auto r = __builtin_amdgcn_permlane16_swap(__float_as_uint(x), __float_as_uint(x), false, false); return fmaxf(__uint_as_float(r[0]), __uint_as_float(r[1])); }
__device__ __forceinline__ float xmax32(float x) { auto r = __builtin_amdgcn_permlane32_swap(__float_as_uint(x), __float_as_uint(x), false, false); return fmaxf(__uint_as_float(r[0]), __uint_as_float(r[1])); }
__device__ __forceinline__ float xsum16(float x) { auto r = __builtin_amdgcn_permlane16_swap(__float_as_uint(x), __float_as_uint(x), false, false); return __uint_as_float(r[0]) + __uint_as_float(r[1]); }
__device__ __forceinline__ float xsum32(float x) { auto r = __builtin_amdgcn_permlane32_swap(__float_as_uint(x), __float_as_uint(x), false, false); return __uint_as_float(r[0]) + __uint_as_float(r[1]); }
__device__ __forceinline__ int dbl(int off) { return ((off >> 13) << 14) | (off & 8191); }
__device__ __forceinline__ bool spin_fail(unsigned spins, unsigned* tmo, unsigned code) {
  if ((spins & 31u) != 31u) return false;
  if (spins > (1u << 15)) { __hip_atomic_store(tmo, code, __ATOMIC_RELAXED, __HIP_MEMORY_SCOPE_AGENT); return true; }
  return __builtin_amdgcn_readfirstlane(__hip_atomic_load(tmo, __ATOMIC_RELAXED, __HIP_MEMORY_SCOPE_AGENT)) != 0u;
}
#define SB0 __builtin_amdgcn_sched_barrier(0)
typedef __attribute__((ext_vector_type(16))) float f32x16;
#define WARM32(d) asm volatile("v_mfma_f32_16x16x32_bf16 %0, v[8:11], v[8:11], 0\n\tv_mfma_f32_16x16x32_bf16 %0, v[8:11], v[8:11], 0" : "=v"(d) : "{v[8:15]}"(wb[0])); SB0
#define WDK 0
#define PACEO 32
#define PACE_O { asm volatile("s_nop %0" :: "i"(PACEO / 4 - 2)); SB0; }
#define PACEV 0
#define PRET 32
#define EPS 20
#define XB 56
#define ALP 2
#define KF0 8
#define PF1 1
#define PSL 48
#define TSL 6
#define FSL 4
#define OUTAUX 16
#define LA0 4
#define LAX 12
#define NBL 16
#define NAL 8
#define HROT 16
#define EARLY 3
#define NWQ 4
#define PACEQ 1
#define NWV 0
#define NWO 0
__device__ __forceinline__ f32x4 zero4() { f32x2 a, b; asm volatile("v_mov_b64 %0, 0\n\tv_mov_b64 %1, 0" : "=v"(a), "=v"(b)); return (f32x4){a[0], a[1], b[0], b[1]}; }
__device__ __forceinline__ int opaque_lane() { int l; asm volatile("v_mbcnt_lo_u32_b32 %0, -1, 0\n\tv_mbcnt_hi_u32_b32 %0, -1, %0" : "=v"(l)); return l & 63; }

namespace wg {
constexpr int LDS_USE = 131072;
__device__ __forceinline__ void* uni(const void* p) { const unsigned long long x = (unsigned long long)p; return (void*)(((unsigned long long)(unsigned)__builtin_amdgcn_readfirstlane(x >> 32) << 32) | (unsigned)__builtin_amdgcn_readfirstlane(x)); }
template <int MT>
__device__ __forceinline__ void gemm_tile(const u16* A, int lda, const u16* B, int ldb, int nst  , void* C, int ldc, bool apack, bool cpack, const unsigned* qpar  , char* lds, int w, int eps, int rot = 0  ) {
  constexpr int MI = MT / 32, PA = MT / 64, NP = PA + 4;
  constexpr int A_B = MT * 64, ST = A_B + 16384;
  const int lane = opaque_lane();
  const int l15 = lane & 15, lq = lane >> 4, wm = w >> 1, wn = w & 1;
  lda = __builtin_amdgcn_readfirstlane(lda); ldb = __builtin_amdgcn_readfirstlane(ldb); ldc = __builtin_amdgcn_readfirstlane(ldc); nst = __builtin_amdgcn_readfirstlane(nst);
  f32x4 acc[8][MI];
#pragma unroll
  for (int i = 0; i < 8; ++i)
#pragma unroll
    for (int j = 0; j < MI; ++j) acc[i][j] = (f32x4){0.f, 0.f, 0.f, 0.f};
  const unsigned lb = (unsigned)(size_t)lds;
  auto fsw = [](int x) { return ((x ^ (x >> 1)) & 1) << 1 | (x >> 1); };
  unsigned ao[2], bo[2][8];
  ao[0] = lb + wm * (MT / 2) * 64 + (apack ? lane * 16 : l15 * 64 + ((lq ^ fsw(l15 >> 2)) << 4)); ao[1] = ao[0] + 2 * ST;
  { const int hh = (l15 >> 2) | ((lq & 1) << 2);
#pragma unroll
    for (int i = 0; i < 8; ++i) { bo[0][i] = lb + A_B + (8 * lq + (l15 >> 2)) * 512 + ((16 * wn + ((2 * i) ^ (2 * hh)) + ((l15 >> 1) & 1)) << 4) + (l15 & 1) * 8; bo[1][i] = bo[0][i] + 2 * ST; } }
  __amdgpu_buffer_rsrc_t ra = __builtin_amdgcn_make_buffer_rsrc(uni(A), 0, 0x7fffffff, 0x00020000), rb = __builtin_amdgcn_make_buffer_rsrc(uni(B), 0, 0x7fffffff, 0x00020000);
  const int va = (16 * PA * w * lda + (apack ? lane * 8 : (lane >> 2) * lda + (((lane & 3) ^ fsw(lane >> 4)) << 3))) * 2, ska = __builtin_amdgcn_readfirstlane(apack ? 1024 : 64);
  const int vb0 = ((8 * w + (lane >> 5)) * ldb + (((lane & 31) ^ ((lane >> 5) << 1) ^ ((w & 1) << 3)) << 3)) * 2, vb1 = vb0 ^ 64;
  const int sa = 32 * lda, sb = 4 * ldb, skb = 64 * ldb;
  const unsigned ma = lb + PA * w * 1024, mb = lb + A_B + w * 4096;
#define PIECE(q, st) do { \
    if ((q) < PA) { asm volatile("s_add_u32 m0, %0, %1\n\ts_nop 0\n\tbuffer_load_dwordx4 %2, %3, %4 offen lds" :: "s"(ma), "i"((st) * ST + (q) * 1024), "v"(va), "s"(ra), "s"(ka + (q) * sa) : "memory", "scc"); } \
    else { asm volatile("s_add_u32 m0, %0, %1\n\ts_nop 0\n\tbuffer_load_dwordx4 %2, %3, %4 offen lds" :: "s"(mb), "i"((st) * ST + ((q) - PA) * 1024), "v"((((q) - PA) & 1) ? vb1 : vb0), "s"(rb), "s"(kb + ((q) - PA) * sb) : "memory", "scc"); } } while (0)
#define RDA(f, j, st) asm volatile("ds_read_b128 %0, %1 offset:%2" : "=v"(fa[f][j]) : "v"(ao[(st) >> 1]), "i"(((st) & 1) * ST + (j) * 1024) : "memory")
#define RDB(f, i, h, st) asm volatile("ds_read_b64_tr_b16 %0, %1 offset:%2" : "=v"(fb[f][i][h]) : "v"(bo[(st) >> 1][i]), "i"(((st) & 1) * ST + (h) * 2048) : "memory")
#define TIE(f) do { _Pragma("unroll") for (int j = 0; j < MI; ++j) asm volatile("" : "+v"(fa[f][j])); \
    _Pragma("unroll") for (int i = 0; i < 8; ++i) asm volatile("" : "+v"(fb[f][i][0]), "+v"(fb[f][i][1])); } while (0)
  v4u fa[2][MI]; v2u fb[2][8][2];
  const unsigned qw = *qpar;
  const unsigned long long parq = apack ? __ballot(qpar[lane] != 0u) : 0ull;
  rot = MT == 64 ? __builtin_amdgcn_readfirstlane(rot) : 0;
  int ka = qoff(parq, 0) + rot * ska, kb = rot * skb;
  asm volatile("s_nop 4" :: "s"(ra), "s"(rb), "s"(sa), "s"(sb), "s"(skb), "s"(ska));
#pragma unroll
  for (int st = 0; st < 4; ++st) {
#pragma unroll
    for (int q = 0; q < NP; ++q) PIECE(q, st);
    ka += ska; kb += skb;
  }
  asm volatile("s_waitcnt vmcnt(%0)\n\ts_barrier" :: "i"(3 * NP) : "memory");
#pragma unroll
  for (int i = 0; i < 8; ++i) { RDB(0, i, 0, 0); RDB(0, i, 1, 0); }
#pragma unroll
  for (int j = 0; j < MI; ++j) RDA(0, j, 0);
  int k4 = 0;
  do {
    if (apack && (k4 & 4)) ka = qoff(parq, (k4 + 4) >> 3);
    if (MT == 64 && k4 + 4 + rot == nst) { ka = 0; kb = 0; }
    if (k4 + 4 >= nst) { ra = __builtin_amdgcn_make_buffer_rsrc(uni(C), 0, 0, 0x00020000); rb = ra; }
#pragma unroll
    for (int st = 0; st < 4; ++st) {
      constexpr int NM = 8 * MI, NR = 16 + MI;
      const int f = st & 1, sn = (st + 1) & 3;
      asm volatile("s_waitcnt vmcnt(%0) lgkmcnt(0)\n\ts_barrier" :: "i"(2 * NP) : "memory");
      TIE(f);
      SB0;
#pragma unroll
      for (int u = 0; u < NM; ++u) {
        const int j = u >> 3, i = u & 7;
        asm volatile("v_mfma_f32_16x16x32_bf16 %0, %1, %2, %0" : "+a"(acc[i][j]) : "v"((v4u){fb[f][i][0].x, fb[f][i][0].y, fb[f][i][1].x, fb[f][i][1].y}), "v"(fa[f][j]));
        SB0;
        constexpr int RS = MT == 256 ? 2 : 1;
        if (u % RS == 0 && u / RS < NR) {
          const int r = u / RS;
          if (r < 16) RDB(f ^ 1, r >> 1, r & 1, sn); else RDA(f ^ 1, r - 16, sn);
          if (MT == 64 && (u == 0 || u == 2)) RDA(f ^ 1, u >> 1, sn);
          SB0;
        }
        constexpr int PS = NM / 8;
        if (u % PS == PS - 1 && u / PS < NP) { PIECE(u / PS, st); SB0; }
      }
      ka += ska; kb += skb;
    }
    k4 += 4;
  } while (k4 < nst);
#undef PIECE
#undef RDA
#undef RDB
  asm volatile("s_nop 7\n\ts_waitcnt lgkmcnt(0)" ::: "memory");
  SB0;
  TIE(0);
#undef TIE
  char* st = lds + LDS_USE + 2048 + w * 4096;
  const __amdgpu_buffer_rsrc_t rs = __builtin_amdgcn_make_buffer_rsrc((char*)uni(C) + __builtin_amdgcn_readfirstlane(cpack && qw ? 8192 : 0), 0, MT * ldc * 2 - (cpack ? 8192 : 0), 0x00020000);
  const int so = wm * (MT / 2) * ldc * 2 + (cpack ? wn * 4096 + lane * 16 : (lq * ldc + wn * 128 + l15 * 8) * 2), sq = cpack ? 1024 : 8 * ldc;
#pragma unroll
  for (int j = 0; j < MI; ++j) {
    v2u p[8]; v4u v[4];
#pragma unroll
    for (int i = 0; i < 8; ++i) { asm volatile("" : "+a"(acc[i][j])); p[i] = (v2u){pk2(acc[i][j][0], acc[i][j][1]), pk2(acc[i][j][2], acc[i][j][3])}; }
#pragma unroll
    for (int q = 0; q < 4; ++q) { v[q] = (v4u){p[2 * q].x, p[2 * q].y, p[2 * q + 1].x, p[2 * q + 1].y}; asm volatile("" : "+v"(v[q])); }
    if (!cpack) {
#pragma unroll
      for (int i = 0; i < 8; ++i) *(v2u*)(st + l15 * 256 + (((2 * i + (lq >> 1)) ^ l15) << 4) + (lq & 1) * 8) = (i & 1) ? (v2u){v[i >> 1].z, v[i >> 1].w} : (v2u){v[i >> 1].x, v[i >> 1].y};
      asm volatile("" ::: "memory");
#pragma unroll
      for (int q = 0; q < 4; ++q) v[q] = *(const v4u*)(st + (4 * q + lq) * 256 + ((l15 ^ (4 * q + lq)) << 4));
      asm volatile("" ::: "memory");
    }
#pragma unroll
    for (int q = 0; q < 4; ++q) {
      if (MT == 128 && OUTAUX != 16 && ldc == 2 * DM) __builtin_amdgcn_raw_buffer_store_b128(v[q], rs, so, 32 * j * ldc + q * sq, OUTAUX);
      else __builtin_amdgcn_raw_buffer_store_b128(v[q], rs, so, 32 * j * ldc + q * sq, 16);
    }
    for (int z = 0; z < eps; ++z) __builtin_amdgcn_s_sleep(4);
    SB0;
  }
  asm volatile("s_waitcnt vmcnt(0)" ::: "memory");
  __builtin_amdgcn_s_barrier();
}
}

struct Ctl {
  unsigned qhead[8][32];
  unsigned flagA[16][64];
  unsigned cntB[16][32];
  unsigned cntAB[16][32];
  unsigned cntAA[16][32];
  unsigned tticket[28][4][32];
  unsigned prog[32];
  unsigned tmo[32];
  unsigned tpick[8][4][32];
  unsigned flagH[64];
};
struct FParams {
  const u16 *Ax, *Bx, *Wq, *Wk, *Wv, *Wo, *wqkvB, *woB;
  u16 *qkvA, *qkvB, *kmem, *vbuf, *obuf, *membuf, *out;
  float* part; Ctl* ctl; unsigned long long* prof; const unsigned* par;
  int dq, dq2, dmv, dov, gain, inc, dec, rec, stag, sh1, sh2, sh3, dv, pad, rot, eps, abd, boot2, boot3, im, gain0, pad4;
};
#define RLX_AGENT __ATOMIC_RELAXED, __HIP_MEMORY_SCOPE_AGENT
__device__ __forceinline__ unsigned ld_agent(const unsigned* p) { return __hip_atomic_load(p, RLX_AGENT); }
__device__ __forceinline__ void wg_wait(const unsigned* p, unsigned need, Ctl* ctl, int w) {
  if (w == 0) {
    for (unsigned spins = 0;; ++spins) {
      const bool ok = (p == nullptr) || ld_agent(p) >= need;
      if (__all(ok)) break;
      if (spin_fail(spins, &ctl->tmo[0], 2u)) break;
      __builtin_amdgcn_s_sleep(16);
    }
    __builtin_amdgcn_fence(__ATOMIC_ACQUIRE, "agent");
    asm volatile("s_waitcnt vmcnt(0)" ::: "memory");
  }
  __syncthreads();
}

struct AttnX { v4u q[2][4], kx[2][4], vr[8]; };
template <int AUX = 0>
__device__ __forceinline__ void attnA_load_x(const FParams& F, unsigned long long parq, int seg, int h, int lane, AttnX& X) {
  const __amdgpu_buffer_rsrc_t rs = __builtin_amdgcn_make_buffer_rsrc(F.qkvA, 0, QSIZE, 0x00020000);
  const int so = seg * 2 * QBLK + (h & 1) * 4096, oq = so + qoff(parq, h >> 1), ok = so + qoff(parq, 16 + (h >> 1)), ov = so + qoff(parq, 32 + (h >> 1));
#pragma unroll
  for (int i = 0; i < 8; ++i) X.vr[i] = __builtin_amdgcn_raw_buffer_load_b128(rs, lane * 16 + (i & 3) * 1024, ov + (i >> 2) * QBLK, AUX);
#pragma unroll
  for (int t = 0; t < 2; ++t)
#pragma unroll
    for (int j = 0; j < 4; ++j) {
      X.q[t][j] = __builtin_amdgcn_raw_buffer_load_b128(rs, lane * 16 + j * 1024, oq + t * QBLK, AUX);
      X.kx[t][j] = __builtin_amdgcn_raw_buffer_load_b128(rs, lane * 16 + j * 1024, ok + t * QBLK, AUX);
    }
}
__device__ __forceinline__ void attnA_stage_v(const AttnX& X, u16* vs  , int lane) {
#pragma unroll
  for (int i = 0; i < 8; ++i) *(v4u*)&vs[(16 * (i >> 2) + (lane & 15)) * VXLD + 32 * (i & 3) + 8 * (lane >> 4)] = X.vr[i];
}
template <int WHAT = 3>
__device__ __forceinline__ unsigned attnA_load_m(const FParams& F, unsigned long long parv, int seg, int h, int lane, v4u (&km)[4], unsigned (&vm)[8]) {
  const int l15 = lane & 15, lq = lane >> 4;
  const __amdgpu_buffer_rsrc_t rk = __builtin_amdgcn_make_buffer_rsrc(F.kmem + (size_t)seg * MEM * HDA, 0, MEM * HDA * 2, 0x00020000);
  const __amdgpu_buffer_rsrc_t rv = __builtin_amdgcn_make_buffer_rsrc((char*)F.vbuf + (size_t)seg * OREG + (int)((parv >> seg) & 1) * 8192 + dbl(h * 2048), 0, 2048, 0x00020000);
#pragma unroll
  for (int j = 0; j < 4; ++j)
    if (WHAT & 1) km[j] = __builtin_amdgcn_raw_buffer_load_b128(rk, h * 2048 + (2 * (lane >> 2) + (lane & 1)) * 16 + 512 * j, 0, 16);
#pragma unroll
  for (int t = 0; t < 8; ++t)
    if (WHAT & 2) vm[t] = __builtin_amdgcn_raw_buffer_load_b32(rv, (((l15 >> 3) * 4 + lq) * 16 + 8 * ((l15 >> 2) & 1) + (l15 & 3)) * 4 + 512 * (t >> 1) + 16 * (t & 1), 0, 16);
  unsigned m = 0;
#pragma unroll
  for (int j = 0; j < 4; ++j) if (WHAT & 1) m = chk4(m, km[j]);
#pragma unroll
  for (int t = 0; t < 8; t += 2) if (WHAT & 2) m = umax3(m, vm[t], vm[t + 1]);
  return m;
}
__device__ __forceinline__ void attnA_compute(const AttnX& X, const v4u (&km)[4], const unsigned (&vm)[8], const u16* vs, int lane, v4u (&ov)[2][4]) {
  const int l15 = lane & 15, lq = lane >> 4;
  const float scale = 0.08838834764831845f, L2E = 1.4426950408889634f;
  v4u px[2], pm[2];
#pragma unroll
  for (int t = 0; t < 2; ++t) {
    f32x4 s0 = {0.f,0.f,0.f,0.f}, s1 = s0, s2 = s0;
#pragma unroll
    for (int j = 0; j < 4; ++j) { s0 = mf(km[j], X.q[t][j], s0); s1 = mf(X.kx[0][j], X.q[t][j], s1); if (t == 1) s2 = mf(X.kx[1][j], X.q[t][j], s2); }
    float sv[12];
#pragma unroll
    for (int e = 0; e < 4; ++e) { sv[e] = s0[e]; sv[4 + e] = s1[e]; sv[8 + e] = s2[e]; }
#pragma unroll
    for (int e = 0; e < 12; e += 2) {
      unsigned p = pk2(sv[e], sv[e + 1]);
      p = pk2(__uint_as_float(p << 16) * scale, __uint_as_float(p & 0xffff0000u) * scale);
      sv[e] = __uint_as_float(p << 16); sv[e + 1] = __uint_as_float(p & 0xffff0000u);
    }
    bool ok[12];
#pragma unroll
    for (int e = 0; e < 4; ++e) { ok[e] = e < 2; ok[4 + e] = (t == 1) || (4 * lq + e <= l15); ok[8 + e] = (t == 1) && (4 * lq + e <= l15); }
    float mx = -INFINITY;
#pragma unroll
    for (int e = 0; e < 12; ++e) mx = ok[e] ? fmaxf(mx, sv[e]) : mx;
    mx = xmax32(xmax16(mx));
    float sum = 0.f;
#pragma unroll
    for (int e = 0; e < 12; ++e) { sv[e] = ok[e] ? __builtin_amdgcn_exp2f((sv[e] - mx) * L2E) : 0.f; sum += sv[e]; }
    sum = xsum32(xsum16(sum));
    const float inv = __builtin_amdgcn_rcpf(sum);
#pragma unroll
    for (int e = 0; e < 12; ++e) sv[e] = ok[e] ? sv[e] * inv : 0.f;
    px[t] = (v4u){pk2(sv[4], sv[5]), pk2(sv[6], sv[7]), pk2(sv[8], sv[9]), pk2(sv[10], sv[11])};
    pm[t] = (v4u){pk2(sv[0], sv[1]), 0u, 0u, 0u};
  }
  const u16* vp = &vs[(4 * lq + (l15 >> 2)) * VXLD + 16 * (l15 & 1) + 2 * (l15 & 2)];
#pragma unroll
  for (int u = 0; u < 4; ++u) {
    f32x4 o[2][2];
#pragma unroll
    for (int ab = 0; ab < 2; ++ab) {
      v2u lo = trrd(vp + 32 * u + 8 * ab), hi = trrd(vp + 16 * VXLD + 32 * u + 8 * ab);
      const v4u va = {lo.x, lo.y, hi.x, hi.y}, vb = {vm[2 * u + ab], 0u, 0u, 0u};
#pragma unroll
      for (int t = 0; t < 2; ++t) { o[t][ab] = mf(va, px[t], (f32x4){0.f,0.f,0.f,0.f}); o[t][ab] = mf(vb, pm[t], o[t][ab]); }
    }
#pragma unroll
    for (int t = 0; t < 2; ++t)
      ov[t][u] = (v4u){pk2(o[t][0][0], o[t][0][1]), pk2(o[t][0][2], o[t][0][3]), pk2(o[t][1][0], o[t][1][1]), pk2(o[t][1][2], o[t][1][3])};
  }
}
__device__ __forceinline__ void attnA_block(const FParams& F, unsigned long long parv, unsigned long long parq, int r, int h, u16* vs, int lane, int w, unsigned* shw) {
  AttnX X[2]; v4u km[2][4]; unsigned vm[2][8];
  unsigned seen = 0;
  attnA_load_x(F, parq, 4 * r, h, lane, X[0]);
  bool have = false;
#pragma unroll
  for (int i = 0; i < 4; ++i) {
    const int seg = 4 * r + i, c = i & 1;
    v4u ov[2][4];
    if (!have) {
      if (seen < (unsigned)seg + 1) {
        if (w == 0) {
          for (unsigned spins = 0;; ++spins) {
            seen = __builtin_amdgcn_readfirstlane(ld_agent(&F.ctl->prog[0]));
            if (seen >= (unsigned)seg + 1 || spin_fail(spins, &F.ctl->tmo[0], 6u)) break;
            for (unsigned z = seen; z < (unsigned)seg + 1; ++z) __builtin_amdgcn_s_sleep(PSL);
          }
          *shw = seen;
        }
        __syncthreads();
        seen = __builtin_amdgcn_readfirstlane(*shw);
        __syncthreads();
      }
      asm volatile("" ::: "memory");
      (void)attnA_load_m(F, parv, seg, h, lane, km[c], vm[c]);
    }
    if (i < 3) {
      attnA_load_x(F, parq, seg + 1, h, lane, X[c ^ 1]);
      have = seen >= (unsigned)seg + 2;
      (void)attnA_load_m(F, parv, have ? seg + 1 : seg, h, lane, km[c ^ 1], vm[c ^ 1]);
    }
    SB0;
    attnA_stage_v(X[c], vs, lane);
    attnA_compute(X[c], km[c], vm[c], vs, lane, ov);
    const __amdgpu_buffer_rsrc_t rs = __builtin_amdgcn_make_buffer_rsrc(F.qkvA, 0, QSIZE, 0x00020000);
#pragma unroll
    for (int u = 0; u < 4; ++u)
#pragma unroll
      for (int t = 0; t < 2; ++t)
        __builtin_amdgcn_raw_buffer_store_b128(ov[t][u], rs, lane * 16 + u * 1024, qoff(parq, h >> 1) + (seg * 2 + t) * QBLK + (h & 1) * 4096, 16);
    SB0;
  }
}

constexpr int TSEG0 = 36, NSET = 4;
__device__ __forceinline__ void tail_task(const FParams& F, unsigned long long parv, unsigned long long parq, int set, int hg, int nb, char* lds, int w) {
  set = __builtin_amdgcn_readfirstlane(set); nb = __builtin_amdgcn_readfirstlane(nb); w = __builtin_amdgcn_readfirstlane(w);
  const int lane = opaque_lane(), tid = 64 * w + lane;
  const int l15 = lane & 15, lq = lane >> 4, h = 4 * hg + w;
  Ctl* ctl = F.ctl;
  v4u wf[4][16];
#pragma unroll
  for (int ps = 0; ps < 2; ++ps) {
    char* stg = lds + w * 32768;
#pragma unroll 1
    for (int q = 0; q < 32; ++q) {
      __builtin_amdgcn_global_load_lds((const unsigned*)(F.Wo + (size_t)(hg * 512 + ps * 256 + 8 * q + (lane >> 3)) * DM + nb * 256 + 64 * w + (lane & 7) * 8), (unsigned*)(stg + q * 1024), 16, 0, 0);
      for (int z = 0; z < (F.eps >> 24); ++z) __builtin_amdgcn_s_sleep(1);
    }
    asm volatile("s_waitcnt vmcnt(0)" ::: "memory");
    const u16* p = (const u16*)(stg + (8 * lq + (l15 >> 2)) * 128 + (l15 & 3) * 8);
#pragma unroll
    for (int i = 0; i < 4; ++i)
#pragma unroll
      for (int u = 0; u < 8; ++u) { v2u lo = trrd(p + (32 * u) * 64 + 16 * i), hi = trrd(p + (32 * u + 4) * 64 + 16 * i); wf[i][8 * ps + u] = (v4u){lo.x, lo.y, hi.x, hi.y}; }
    asm volatile("s_waitcnt lgkmcnt(0)" ::: "memory");
  }
  __syncthreads();
  unsigned long long rdy = 0;
  auto flags = [&]() { const int lz = opaque_lane(); return __ballot(lz < 48 && ld_agent(&ctl->flagA[lz < 48 ? lz / 3 : 0][(lz % 3) * 16 + (h >> 1)]) != 0u); };
  auto load_x = [&](int sg, AttnX& X) {
    for (unsigned spins = 0; ((rdy >> (3 * (sg >> 2))) & 7) != 7; ++spins) {
      rdy = flags();
      if (((rdy >> (3 * (sg >> 2))) & 7) == 7 || spin_fail(spins, &ctl->tmo[0], 2u)) break;
      __builtin_amdgcn_s_sleep(16);
    }
    asm volatile("" ::: "memory");
    attnA_load_x<16>(F, parq, sg, h, lane, X);
  };
  int* shp = (int*)(lds + wg::LDS_USE);
  if (tid == 0) shp[2] = (int)__hip_atomic_fetch_add(&ctl->tpick[hg][nb][0], 1u, RLX_AGENT);
  __syncthreads();
  int seg = TSEG0 + __builtin_amdgcn_readfirstlane(shp[2]), prev = -1;
  if (seg >= NSEG) return;
  constexpr int LAST = NSEG - 2;
  if (seg == TSEG0) {
    const __amdgpu_buffer_rsrc_t rp = __builtin_amdgcn_make_buffer_rsrc(F.part + ((size_t)(LAST - TSEG0) * 8 * SEG) * DM + nb * 256, 0, (NSEG - LAST) * 8 * SEG * DM * 4, 0x00020000);
#pragma unroll
    for (int k = 0; k < 8 * (NSEG - LAST); ++k) __builtin_amdgcn_raw_buffer_store_b128((v4u){~0u, ~0u, ~0u, ~0u}, rp, ((hg * SEG + 4 * (k & 7) + lq) * DM + 64 * w + l15 * 4) * 4, (k >> 3) * 8 * SEG * DM * 4, 16);
  }
  u16* vs = (u16*)(lds + w * 16384);
  v4u* Of = (v4u*)(lds + 65536);
  float* st = (float*)(lds + 98304 + w * 8192);
  AttnX X;
  load_x(seg, X);
  auto finish = [&](int sg) {
    const unsigned* tk = &ctl->tticket[sg - TSEG0][nb][0];
    for (unsigned spins = 0; sg < LAST; ++spins) {
      if (ld_agent(tk) >= 8u) break;
      if (spin_fail(spins, &ctl->tmo[0], 5u)) break;
      __builtin_amdgcn_s_sleep(2);
    }
    asm volatile("" ::: "memory");
    const __amdgpu_buffer_rsrc_t rp = __builtin_amdgcn_make_buffer_rsrc(F.part + ((size_t)(sg - TSEG0) * 8 * SEG) * DM + nb * 256, 0, 8 * SEG * DM * 4, 0x00020000);
    const int q = 4 * hg + w;
    f32x4 v[8];
    for (unsigned spins = 0;; ++spins) {
      unsigned m = 0;
#pragma unroll
      for (int k = 0; k < 8; ++k) { const v4u z = __builtin_amdgcn_raw_buffer_load_b128(rp, ((k * SEG + q) * DM + lane * 4) * 4, 0, 16); v[k] = __builtin_bit_cast(f32x4, z); m = chk4(m, z); }
      if (__all(m != 0xffffffffu) || spin_fail(spins, &ctl->tmo[0], 5u)) break;
      __builtin_amdgcn_s_sleep(4);
    }
    const f32x4 a = ((v[0] + v[1]) + (v[2] + v[3])) + ((v[4] + v[5]) + (v[6] + v[7]));
    *(v2u*)(F.out + (size_t)(sg * SEG + q) * 2 * DM + nb * 256 + lane * 4) = (v2u){pk2(a[0], a[1]), pk2(a[2], a[3])};
  };
#pragma unroll 1
  for (;;) {
    bool pend = prev >= 0;
    int nxt = NSEG;
    if (seg < NSEG) {
      v4u km[4], ov[2][4]; unsigned vm[8];
      if (seg >= LAST) for (unsigned spins = 0; ld_agent(&ctl->tticket[0][nb][0]) < 8u && !spin_fail(spins, &ctl->tmo[0], 5u); ++spins) __builtin_amdgcn_s_sleep(2);
      attnA_stage_v(X, vs, lane);
      for (unsigned spins = 0;; ++spins) {
        if (__all(attnA_load_m<1>(F, parv, seg, h, lane, km, vm) != 0xffffffffu)) break;
        if (spin_fail(spins, &ctl->tmo[0], 4u)) break;
        if (pend) { finish(prev); pend = false; } else __builtin_amdgcn_s_sleep(TSL);
      }
      asm volatile("" ::: "memory");
      (void)attnA_load_m<2>(F, parv, seg, h, lane, km, vm);
      const int pk = __builtin_amdgcn_raw_ptr_buffer_atomic_add_i32(1, __builtin_amdgcn_make_buffer_rsrc(&ctl->tpick[hg][nb][0], 0, 4, 0x00020000), tid * 4, 0, 16);
      attnA_compute(X, km, vm, vs, lane, ov);
#pragma unroll
      for (int u = 0; u < 4; ++u)
#pragma unroll
        for (int t = 0; t < 2; ++t) Of[((4 * w + u) * 2 + t) * 64 + lane] = ov[t][u];
      shp[3 + tid] = pk;
      lds_barrier();
      nxt = TSEG0 + __builtin_amdgcn_readfirstlane(shp[3]);
      const bool early = nxt < NSEG && ((rdy >> (3 * (nxt >> 2))) & 7) == 7;
      attnA_load_x<16>(F, parq, early ? nxt : seg, h, lane, X);
      f32x4 acc[4][2];
#pragma unroll
      for (int i = 0; i < 4; ++i) { acc[i][0] = (f32x4){0.f,0.f,0.f,0.f}; acc[i][1] = acc[i][0]; }
      v4u bf[32];
#pragma unroll
      for (int q = 0; q < 6; ++q) bf[q] = Of[q * 64 + lane];
#pragma unroll
      for (int ks = 0; ks < 16; ++ks) {
        if (ks + 3 < 16) { bf[2 * ks + 6] = Of[(2 * ks + 6) * 64 + lane]; bf[2 * ks + 7] = Of[(2 * ks + 7) * 64 + lane]; }
        SB0;
        const v4u b0 = bf[2 * ks], b1 = bf[2 * ks + 1];
#define OUT8(C3) asm volatile("s_nop 1\n\t" \
                     "v_mfma_f32_16x16x32_bf16 %0, %8, %12, %0\n\tv_mfma_f32_16x16x32_bf16 %1, %8, %13, %1\n\t" \
                     "v_mfma_f32_16x16x32_bf16 %2, %9, %12, %2\n\tv_mfma_f32_16x16x32_bf16 %3, %9, %13, %3\n\t" \
                     "v_mfma_f32_16x16x32_bf16 %4, %10, %12, %4\n\tv_mfma_f32_16x16x32_bf16 %5, %10, %13, %5\n\t" \
                     "v_mfma_f32_16x16x32_bf16 %6, %11, %12, %6\n\tv_mfma_f32_16x16x32_bf16 %7, %11, %13, %7" \
                     : "+v"(acc[0][0]), "+v"(acc[0][1]), "+v"(acc[1][0]), "+v"(acc[1][1]), "+v"(acc[2][0]), "+v"(acc[2][1]), "+v"(acc[3][0]), "+v"(acc[3][1]) \
                     : "a"(wf[0][ks]), "a"(wf[1][ks]), "a"(wf[2][ks]), C3(wf[3][ks]), "v"(b0), "v"(b1))
        if (ks < 5) OUT8("a"); else OUT8("v");
        SB0;
      }
      asm volatile("s_nop 7" : "+v"(acc[0][0]), "+v"(acc[0][1]), "+v"(acc[1][0]), "+v"(acc[1][1]), "+v"(acc[2][0]), "+v"(acc[2][1]), "+v"(acc[3][0]), "+v"(acc[3][1]));
#pragma unroll
      for (int t = 0; t < 2; ++t)
#pragma unroll
        for (int i = 0; i < 4; ++i) *(f32x4*)&st[(16 * t + l15) * 64 + (((4 * i + lq) ^ l15) << 2)] = acc[i][t];
      const __amdgpu_buffer_rsrc_t rp = __builtin_amdgcn_make_buffer_rsrc(F.part + ((size_t)(seg - TSEG0) * 8 * SEG) * DM + nb * 256, 0, 8 * SEG * DM * 4, 0x00020000);
#pragma unroll
      for (int k = 0; k < 8; ++k) {
        const int q = 4 * k + lq;
        const v4u z = *(const v4u*)&st[q * 64 + ((l15 ^ (q & 15)) << 2)];
        __builtin_amdgcn_raw_buffer_store_b128((v4u){nosent(z.x), nosent(z.y), nosent(z.z), nosent(z.w)}, rp, ((hg * SEG + q) * DM + 64 * w + l15 * 4) * 4, 0, 16);
      }
      if (pend) { finish(prev); pend = false; }
      if (seg < LAST) {
        asm volatile("s_waitcnt vmcnt(0)" ::: "memory");
        __syncthreads();
        if (tid == 0) __hip_atomic_fetch_add(&ctl->tticket[seg - TSEG0][nb][0], 1u, RLX_AGENT);
      } else { lds_barrier(); __builtin_amdgcn_s_sleep(FSL); }
      if (!early && nxt < NSEG) load_x(nxt, X);
    }
    if (pend) finish(prev);
    if (seg >= NSEG) break;
    prev = seg; seg = nxt;
  }
}

__device__ __forceinline__ float dot8(v4u a, v4u b) {
  float d = 0.f;
#pragma unroll
  for (int e = 0; e < 4; ++e) { const unsigned x = a[e], y = b[e]; d = __builtin_amdgcn_fdot2_f32_bf16(__builtin_bit_cast(bfx2, x), __builtin_bit_cast(bfx2, y), d, false); }
  return d;
}
__device__ __forceinline__ void attnB_16(const u16* qkv, __amdgpu_buffer_rsrc_t rsO, int pos0  , int w) {
  const int tid = 64 * w + opaque_lane(), cc = tid & 127, p0 = pos0 + (tid >> 7);
  v4u q[8], k[15], v[15];
#pragma unroll
  for (int i = 0; i < 15; ++i) {
    const int kp = p0 - 14 + 2 * i, kc = kp < 0 ? 0 : kp;
    k[i] = *(const v4u*)(qkv + (size_t)kc * NQKVB + DB + cc * 8);
    v[i] = *(const v4u*)(qkv + (size_t)kc * NQKVB + 2 * DB + cc * 8);
  }
#pragma unroll
  for (int i = 0; i < 8; ++i) q[i] = *(const v4u*)(qkv + (size_t)(p0 + 2 * i) * NQKVB + cc * 8);
  const float scale = 0.17677669529663687f;
#pragma unroll
  for (int i = 0; i < 8; ++i) {
    const int pos = p0 + 2 * i;
    float lg[8], mx = -INFINITY;
#pragma unroll
    for (int w = 0; w < 8; ++w) {
      float d = dot8(q[i], k[i + 7 - w]);
      d += __int_as_float(__builtin_amdgcn_update_dpp(0, __float_as_int(d), 0xB1, 0xf, 0xf, false));
      d += __int_as_float(__builtin_amdgcn_update_dpp(0, __float_as_int(d), 0x4E, 0xf, 0xf, false));
      lg[w] = d;
    }
#pragma unroll
    for (int w = 0; w < 8; w += 2) {
      unsigned p = pk2(lg[w], lg[w + 1]);
      p = pk2(__uint_as_float(p << 16) * scale, __uint_as_float(p & 0xffff0000u) * scale);
      lg[w] = __uint_as_float(p << 16); lg[w + 1] = __uint_as_float(p & 0xffff0000u);
    }
#pragma unroll
    for (int w = 0; w < 8; ++w) if (pos - 2 * w >= 0) mx = fmaxf(mx, lg[w]);
    float sum = 0.f;
#pragma unroll
    for (int w = 0; w < 8; ++w) { lg[w] = (pos - 2 * w >= 0) ? __expf(lg[w] - mx) : 0.f; sum += lg[w]; }
    const float inv = __builtin_amdgcn_rcpf(sum);
    float o[8] = {0.f, 0.f, 0.f, 0.f, 0.f, 0.f, 0.f, 0.f};
#pragma unroll
    for (int w = 0; w < 8; w += 2) {
      const unsigned pp = pk2(lg[w] * inv, lg[w + 1] * inv);
#pragma unroll
      for (int z = 0; z < 2; ++z) {
        if (pos - 2 * (w + z) >= 0) {
          const float p = z ? __uint_as_float(pp & 0xffff0000u) : __uint_as_float(pp << 16);
          const v4u vv = v[i + 7 - w - z];
#pragma unroll
          for (int e = 0; e < 4; ++e) { o[2 * e] += p * __uint_as_float(vv[e] << 16); o[2 * e + 1] += p * __uint_as_float(vv[e] & 0xffff0000u); }
        }
      }
    }
    __builtin_amdgcn_raw_buffer_store_b128((v4u){pk2(o[0], o[1]), pk2(o[2], o[3]), pk2(o[4], o[5]), pk2(o[6], o[7])}, rsO, (pos * NQKVB + cc * 8) * 2, 0, 16);
  }
}

constexpr int NTASK = 109;
__constant__ unsigned short TASKS[NTASK] = {
  9216, 9217, 9218, 9219, 9232, 9233, 9234, 9235, 16, 17, 18, 19, 40, 72, 41, 73, 42, 74, 43, 75,
  1032, 1064, 1096, 1128, 1033, 1065, 1097, 1129, 104, 105, 106, 107, 136, 168, 137, 169, 138, 170, 139, 171,
  5120, 5136, 2056, 2072, 2088, 2104, 2120, 2136, 2152, 2168, 2184, 2200, 2216, 2232, 5152, 5168, 1160, 1192, 1224, 1256,
  1161, 1193, 1225, 1257, 3072, 3088, 3104, 3120, 3136, 3152, 3168, 3184, 200, 232, 201, 233, 202, 234, 203, 235,
  5184, 5200, 5216, 5232, 5248, 4096, 4112, 4128, 4144, 4160, 4176, 4192, 4208, 7168, 7169, 7170, 7171, 7184, 7185, 7186,
  7187, 7200, 7201, 7202, 7203, 7216, 7217, 7218, 7219,
};
__device__ __forceinline__ const FParams& args() {
  typedef const __attribute__((address_space(4))) FParams* KP;
  KP p = (KP)__builtin_amdgcn_kernarg_segment_ptr(); asm volatile("" : "+s"(p)); return *(const FParams*)p;
}
__device__ __forceinline__ void worker(const FParams& F, char* lds, const int w0) {
#define lane opaque_lane()
#define tid (64 * w + opaque_lane())
  const int wid = ((blockIdx.x & 7) - 3) * 32 + (blockIdx.x >> 3), x = wid / 20;
  int first = wid - 20 * x;
  Ctl* ctl = F.ctl;
  int* sh = (int*)(lds + wg::LDS_USE);
#define PARV __ballot(F.par[128 + lane] != 0u), __ballot(F.par[192 + lane] != 0u)
  int next = -1;
  for (;;) {
    const FParams& F = args();
    int w = w0; asm volatile("" : "+s"(w));
    int t = first < 0 ? 0 : first;
    if (next < 0 && first < 0) {
      if (tid == 0) sh[0] = (int)__hip_atomic_fetch_add(&ctl->qhead[x][0], 1u, RLX_AGENT);
      __syncthreads();
      t = __builtin_amdgcn_readfirstlane(sh[0]);
      __syncthreads();
    }
    first = -1;
    if (t >= NTASK) return;
    if (next < 0 && t < 20 && t >= (F.stag >> 16) && F.stag) {
      const unsigned t1 = (unsigned)__builtin_amdgcn_s_memrealtime() + (F.stag & 0xffff);
      while ((int)(t1 - (unsigned)__builtin_amdgcn_s_memrealtime()) > 0) __builtin_amdgcn_s_sleep(16);
    }
    const int code = __builtin_amdgcn_readfirstlane(next < 0 ? (int)TASKS[t] : next); next = -1;
    const int tk = code >> 10, ta = (code >> 4) & 63, tb = code & 7;
    const bool big = (code & 8) != 0;
    const u16 *A = nullptr, *Bt = nullptr; void* C = nullptr; int lda = 0, ldb = 0, ldc = 0, nk = 32, kind = 0, r = ta, n = 0, sst = 0;
    const unsigned* wp = nullptr; unsigned need = 0; bool dep = false; unsigned* sig = nullptr;
    if (tk < 2) {
      n = tk == 0 ? 16 + 4 * x + tb : 2 * x + tb;
      A = F.Ax + (size_t)r * 128 * DM; lda = DM; Bt = (n < 16 ? F.Wq : (n < 32 ? F.Wk : F.Wv)) + (n & 15) * 256; ldb = HDA;
      C = (char*)F.qkvA + n * QPAGE + r * 8 * QBLK; ldc = QBLK / 32; sig = &ctl->flagA[r][n]; sst = 64;
    } else if (tk == 9) {
      n = 16 + 4 * x + tb;
      wg::gemm_tile<64>(F.Ax + ta * 64 * DM, DM, (n < 32 ? F.Wk : F.Wv) + (n & 15) * 256, HDA, 32, (char*)F.qkvA + n * QPAGE + ta * 4 * QBLK, QBLK / 32, false, true, F.par + 192 + n, lds, w, 0, ta * HROT);
      if (ta) for (unsigned spins = 0; __all(ld_agent(&ctl->flagH[n]) == 0u) && !spin_fail(spins, &ctl->tmo[0], 2u); ++spins) __builtin_amdgcn_s_sleep(8);
      if (tid == 0) __hip_atomic_fetch_add(ta ? &ctl->flagA[0][n] : &ctl->flagH[n], 1u, RLX_AGENT);
      continue;
    } else if (tk == 2) {
      const int u = 12 * x + ta; n = u >> 3; r = 2 * (u & 7);
      A = F.Bx + (size_t)r * 128 * DB; lda = DB; Bt = F.wqkvB + n * 256; ldb = NQKVB;
      C = F.qkvB + (size_t)r * 128 * NQKVB + n * 256; ldc = NQKVB; sig = &ctl->cntB[r][0]; sst = 32;
    } else if (tk == 3) {
      const int u = ta * 8 + x; r = u >> 2; n = u & 3;
      kind = 2; dep = true; need = 12; if (lane < 2 && r - lane >= 0) wp = &ctl->cntB[r - lane][0]; sig = &ctl->cntAB[r][0];
    } else if (tk == 4) {
      r = 2 * x + (ta >> 2); n = ta & 3;
      A = F.qkvB + (size_t)r * 128 * NQKVB; lda = NQKVB; Bt = F.woB + n * 256; ldb = DB;
      C = F.out + (size_t)r * 128 * 2 * DM + DM + n * 256; ldc = 2 * DM; dep = true; need = 4; if (lane <= (int)big) wp = &ctl->cntAB[r + lane][0];
    } else if (tk == 5) { kind = 3; sig = &ctl->cntAA[r][0]; if ((x >> 2) == (r & 1)) next = code + 1024; }
    else if (tk == 6) {
      n = x & 3;
      A = F.qkvA + r * 4 * QBLK; lda = QBLK / 32; Bt = F.Wo + n * 256; ldb = DM; nk = HDA / 32;
      C = F.out + (size_t)r * 128 * 2 * DM + n * 256; ldc = 2 * DM;
      dep = true; need = 8; if (lane == 0) wp = &ctl->cntAA[r][0];
    } else {
      tail_task(F, PARV, ta, x, tb, lds, w);
      return;
    }
    if (kind == 3) {
      { const int lz = opaque_lane(); if (lz < 6) wp = &ctl->flagA[r][(lz >> 1) * 16 + 2 * x + (lz & 1)]; }
      wg_wait(wp, 1, ctl, w);
      attnA_block(F, PARV, r, 4 * x + w, (u16*)(lds + w * 16384), opaque_lane(), w, (unsigned*)sh + 4);
      asm volatile("s_waitcnt vmcnt(0)" ::: "memory");
      __syncthreads();
    } else {
      if (dep) wg_wait(wp, need, ctl, w);
      const unsigned* rot = F.par + 192 + (tk < 2 ? n : 0);
      { const int t0 = (F.rot & 16) ? 12 : 20;
        if ((F.rot & 48) && t >= t0 && t < t0 + 8) {
          const int id = __builtin_amdgcn_readfirstlane((x * 8 + t - t0) * 4 + w), tz = opaque_lane();
          const __amdgpu_buffer_rsrc_t r1 = __builtin_amdgcn_make_buffer_rsrc((void*)(F.Ax + 256 * DM), 0, 1792 * DM * 2, 0x00020000), r2 = __builtin_amdgcn_make_buffer_rsrc((void*)F.woB, 0, DB * DB * 2, 0x00020000);
          for (int q = 0; q < 14; ++q) __builtin_amdgcn_raw_ptr_buffer_load_lds(r1, (__attribute__((address_space(3))) void*)(lds + wg::LDS_USE + 1024), 16, tz * 16, (id * 14 + q) * 1024, 0, 0);
          for (int q = 0; q < 8; ++q) __builtin_amdgcn_raw_ptr_buffer_load_lds(r2, (__attribute__((address_space(3))) void*)(lds + wg::LDS_USE + 1024), 16, tz * 16, (id * 8 + q) * 1024, 0, 0);
          if (F.rot & 64) {
            const __amdgpu_buffer_rsrc_t r3 = __builtin_amdgcn_make_buffer_rsrc((void*)F.wqkvB, 0, DB * NQKVB * 2, 0x00020000);
            for (int q = 0; q < 24; ++q) __builtin_amdgcn_raw_ptr_buffer_load_lds(r3, (__attribute__((address_space(3))) void*)(lds + wg::LDS_USE + 1024), 16, tz * 16, (id * 24 + q) * 1024, 0, 0);
          }
          if (F.rot & 512) {
            const __amdgpu_buffer_rsrc_t r4 = __builtin_amdgcn_make_buffer_rsrc((void*)F.Bx, 0, S_ * DB * 2, 0x00020000);
            for (int q = 0; q < 16; ++q) __builtin_amdgcn_raw_ptr_buffer_load_lds(r4, (__attribute__((address_space(3))) void*)(lds + wg::LDS_USE + 1024), 16, tz * 16, (id * 16 + q) * 1024, 0, 0);
          }
        } }
      if (((tk == 0 || tk == 2) && (F.rot & 4))) {
        const int np = tk == 0 ? (big ? 4 : 2) : (tk == 2 ? 11 : 16), p0 = (tk == 0 ? 4 * x + tb : n) * 4 * np + w * np, tz = opaque_lane();
        const __amdgpu_buffer_rsrc_t rp = __builtin_amdgcn_make_buffer_rsrc(wg::uni(A), 0, (big ? 256 : 128) * DM * 2, 0x00020000);
        for (int q = 0; q < np; ++q) __builtin_amdgcn_raw_ptr_buffer_load_lds(rp, (__attribute__((address_space(3))) void*)(lds + wg::LDS_USE + 1024), 16, tz * 16, (p0 + q) * 1024, 0, 0);
      }
      if (kind == 0) { if (big) wg::gemm_tile<256>(A, lda, Bt, ldb, nk, C, ldc, false, tk < 2, rot, lds, w, (F.eps >> (tk == 2 ? 16 : 0)) & 255); else wg::gemm_tile<128>(A, lda, Bt, ldb, nk, C, ldc, tk == 6, tk < 2, rot, lds, w, tk == 4 ? (F.eps >> 8) & 255 : 0); }
      else {
        { const unsigned t1 = (unsigned)__builtin_amdgcn_s_memrealtime() + (ta * 8 + x) * F.abd;
          while ((int)(t1 - (unsigned)__builtin_amdgcn_s_memrealtime()) > 0) __builtin_amdgcn_s_sleep(8); }
        if (F.rot & 128) {
          const __amdgpu_buffer_rsrc_t r2 = __builtin_amdgcn_make_buffer_rsrc((void*)F.woB, 0, DB * DB * 2, 0x00020000);
          for (int q = 0; q < 8; ++q) __builtin_amdgcn_raw_ptr_buffer_load_lds(r2, (__attribute__((address_space(3))) void*)(lds + wg::LDS_USE + 1024), 16, opaque_lane() * 16, (((ta * 8 + x) * 4 + w) * 8 + q) * 1024, 0, 0);
        }
        if (F.rot & 256) {
          const __amdgpu_buffer_rsrc_t r2 = __builtin_amdgcn_make_buffer_rsrc((void*)(F.Ax + 1536 * DM), 0, 512 * DM * 2, 0x00020000);
          for (int q = 0; q < 4; ++q) __builtin_amdgcn_raw_ptr_buffer_load_lds(r2, (__attribute__((address_space(3))) void*)(lds + wg::LDS_USE + 1024), 16, opaque_lane() * 16, (((ta * 8 + x) * 4 + w) * 4 + q) * 1024, 0, 0);
        }
        const __amdgpu_buffer_rsrc_t rsO = __builtin_amdgcn_make_buffer_rsrc(F.qkvB, 0, S_ * NQKVB * 2, 0x00020000);
#pragma unroll 1
        for (int it = 0; it < 2; ++it) attnB_16(F.qkvB, rsO, r * 128 + n * 32 + it * 16, w);
        asm volatile("s_waitcnt vmcnt(0)" ::: "memory");
        __syncthreads();
      }
    }
    if (sig && tid <= (int)big) __hip_atomic_fetch_add(sig + tid * sst, 1u, RLX_AGENT);
  }
}

#undef lane
#undef tid
__device__ __forceinline__ unsigned rtnow() { return (unsigned)__builtin_amdgcn_s_memrealtime(); }
__device__ __forceinline__ void wait_rt(unsigned t) {
  int rem = (int)(t - rtnow());
  if (rem > 20000) return;
  while (rem > 12) { __builtin_amdgcn_s_sleep(2); rem = (int)(t - rtnow()); }
  while (rem > 0) rem = (int)(t - rtnow());
}
#define MBASE(s) ((s) * MREG + (int)((parm >> (s)) & 1) * 8192)
#define VBASE(s) ((s) * OREG + (int)((parv >> (s)) & 1) * 8192)
#define OBASE(s) ((s) * OREG + (int)((paro >> (s)) & 1) * 8192)
constexpr int RECO = 32768;
constexpr int ALM = RECO + 1024;
__device__ __forceinline__ float rowmax16(float f) { f = fmaxf(f, dpp_ror<8>(f)); f = fmaxf(f, dpp_ror<4>(f)); f = fmaxf(f, dpp_ror<2>(f)); return fmaxf(f, dpp_ror<1>(f)); }
__device__ __forceinline__ unsigned al_eval(unsigned v, unsigned& t) {
  const bool set = (v >> 16) == 1u;
  const unsigned any = (unsigned)__ballot(set) & 0xffffu;
  if (__builtin_expect(any == 0, 1)) return 0u;
  const int d = (short)(v - t);
  t += __builtin_amdgcn_readfirstlane((int)rowmax16(set && d > 0 && d < 1000 ? (float)d : 0.f));
  return any;
}
__device__ __forceinline__ void al_set(__amdgpu_buffer_rsrc_t rs, int base, int line, unsigned t) {
  const int lz = opaque_lane();
  __builtin_amdgcn_raw_buffer_store_b32((t & 0xffffu) | 0x10000u, rs, lz < 32 && line >= 0 ? base + line * 128 + lz * 4 : 0x7ffffff0, 0, 16);
}
__device__ __forceinline__ void rec_get(unsigned& recA, unsigned& recD, __amdgpu_buffer_rsrc_t rs, int off, unsigned* tmo, int naps = 1) {
#pragma unroll 1
  for (unsigned fails = 0;; ++fails) {
    const v2u rec = __builtin_amdgcn_raw_buffer_load_b64(rs, off, 0, 16);
    recA = __builtin_amdgcn_readfirstlane(rec.x); recD = __builtin_amdgcn_readfirstlane(rec.y);
    if ((recA != 0xffffffffu && recD != 0xffffffffu) || spin_fail(fails, tmo, 1u)) break;
    if (naps > 1) __builtin_amdgcn_s_sleep(6); else __builtin_amdgcn_s_sleep(2);
  }
}

__global__ void __launch_bounds__(256, 1) k_fused(FParams P) {
  __shared__ __attribute__((aligned(16))) char Lraw[sizeof(ChainLds)];
  unsigned long long pc0;
  asm volatile("s_getpc_b64 %0\nLfused_pc0:" : "=s"(pc0));
  const int w = __builtin_amdgcn_readfirstlane(threadIdx.x >> 6);
  if ((blockIdx.x & 7) >= 3) {
    asm volatile("Lfused_worker:");
    worker(P, Lraw, w);
    return;
  }
  ChainLds& L = *(ChainLds*)Lraw;
  u16 *vbuf = P.vbuf, *obuf = P.obuf, *membuf = P.membuf; unsigned* tmo = &P.ctl->tmo[0]; unsigned long long* prof = P.prof; (void)prof;
  const int xq = blockIdx.x & 7, slot = blockIdx.x >> 3, nq = xq < 2 ? 12 : 8;
  const int b = slot < nq ? 12 * xq + slot : NQK + 20 * xq + (slot - nq);
  const int alw = b < NQK ? (b % (NBL == 16 ? 9 : 18) == 0 ? (b / (NBL == 16 ? 9 : 18)) * 4 + w : -1) : (b == 33 ? 16 + w : (b == 95 ? 20 + w : -1));
  const __amdgpu_buffer_rsrc_t rs_o = __builtin_amdgcn_make_buffer_rsrc(obuf, 0, NSEG * OREG, 0x00020000);
  const __amdgpu_buffer_rsrc_t rs_m = __builtin_amdgcn_make_buffer_rsrc(membuf, 0, NSEG * MREG, 0x00020000);
#define PAR_BITS const unsigned* parp = P.par + opaque_lane(); const unsigned long long paro = __ballot(parp[0] != 0u), parm = __ballot(parp[64] != 0u), parv = __ballot(parp[128] != 0u)
  const __amdgpu_buffer_rsrc_t rs_v = __builtin_amdgcn_make_buffer_rsrc(vbuf, 0, NSEG * OREG, 0x00020000);
#define LANE_CONSTS const int lane = opaque_lane(), tid = 64 * w + lane, l15 = lane & 15, lq = lane >> 4, idx = (lane & 1) ? 0xEEEE : 0x4444; \
  const int mem_lane_off = (((lq >> 1) * 2 + (l15 >> 3)) * 16 + (lq & 1) * 8 + (l15 & 7)) * 16 + dbl(w * 4096); \
  const int wrow = 8 * (lq >> 1) + 2 * (lq & 1) + 4 * (l15 >> 3) + 16 * ((l15 >> 2) & 1), wsw = (wrow >> 1) & 3
#define CODE_PREFETCH do { int clen; asm volatile("s_mov_b32 %0, Lfused_worker-Lfused_pc0" : "=s"(clen)); \
      unsigned acc_ = 0; for (int o_ = tid * 64; o_ + 4 <= clen; o_ += 256 * 64) acc_ ^= *(const unsigned*)((const char*)pc0 + o_); asm volatile("" :: "v"(acc_)); } while (0)
  unsigned tA = 0;
  if (b < NQK) {
    const int h = b;
    LANE_CONSTS;
    const v4u* sAq[3];
#pragma unroll
    for (int q = 0; q < 3; ++q) sAq[q] = &L.sA[(q == 0 ? (w ^ 1) : (((w & 2) ^ 2) + q - 1)) * 256 + lane];
    v8u wa[32], wb[32 - NLW];
    {
      const size_t go = (size_t)(lane >> 3) * HDA + h * DHA + 64 * (w >> 1) + (lane & 7) * 8;
      char* wr = Lraw + w * 8192 + (lane >> 3) * 128 + ((((lane & 7) >> 1) ^ ((lane >> 4) & 3)) << 5) + (lane & 1) * 16;
      const int rd0 = wrow * 128 + (l15 & 3) * 8;
      v4u R[16][8];
#define AHEAD(ii) ((ii) + LA0 < LAX ? (ii) + LA0 : ((ii) + 2 > LAX ? (ii) + 2 : LAX))
#define SLOT(ii) (((ii) & 4) ? 7 - ((ii) & 7) : 11 - ((ii) & 7))
#define LDW(ii) do { const size_t ko = (size_t)(64 * (4 * (SLOT(ii) < 8 ? w : w ^ 2) + (SLOT(ii) & 3))) * HDA + go; \
        _Pragma("unroll") for (int u = 0; u < 8; ++u) R[ii][u] = *(const v4u*)((((ii) & 8) ? P.Wq : P.Wk) + ko + (size_t)(8 * u) * HDA); } while (0)
      SFOR(ii, 0, LA0) LDW(ii); SEND
      SFOR(ii, 0, 16)
        constexpr int j = SLOT(ii);
        if constexpr (AHEAD(ii) < 16 && AHEAD(ii) > AHEAD(ii - 1)) LDW(AHEAD(ii));
        SB0;
#pragma unroll
        for (int u = 0; u < 8; ++u) *(v4u*)(wr + u * 1024) = R[ii][u];
        lds_barrier();
        SFOR(t, 0, 4)
          const int img = j < 8 ? w ^ (t >> 1) : (w & 2) + (t >> 1);
          const u16* p = (const u16*)(Lraw + img * 8192 + rd0 + (((2 * (w & 1) + (t & 1)) ^ wsw) << 5));
          v2u e0 = trrd(p), e1 = trrd(p + 64), e2 = trrd(p + 32 * 64), e3 = trrd(p + 33 * 64);
          v4u lo = {e0.x, e0.y, e1.x, e1.y}, hi = {e2.x, e2.y, e3.x, e3.y};
          constexpr int u = seq_u(j + 4 * (t >> 1), ((ii & 8) ? 0 : 2) + (t & 1)), n = seq_n(u);
          if constexpr (seq_c(u) == 0) wa[n] = cat8(lo, hi);
          else if constexpr (seq_c(u) == 1) wb[n] = cat8(lo, hi);
          else { L.sW[w][n][0][lane] = lo; L.sW[w][n][1][lane] = hi; }
        SEND
        lds_barrier();
        SB0;
      SEND
#undef LDW
#undef SLOT
#undef AHEAD
    }
    PAR_BITS;
    const __amdgpu_buffer_rsrc_t rs_x = __builtin_amdgcn_make_buffer_rsrc(P.qkvA, 0, QSIZE, 0x00020000);
    const unsigned long long parq = __ballot(parp[192] != 0u);
    const int xk = qoff(parq, 16 + (h >> 1)) + (h & 1) * 4096, xv = qoff(parq, 32 + (h >> 1)) + (h & 1) * 4096;
    const __amdgpu_buffer_rsrc_t rs_k = __builtin_amdgcn_make_buffer_rsrc(P.kmem, 0, NSEG * MEM * HDA * 2, 0x00020000);
#define PF_ADDR const int tz = opaque_lane(); const unsigned* kvflag = &P.ctl->flagA[0][16 + (tz & 1) * 16 + (h >> 1)]
#define WAIT_KV(rb) do { for (unsigned spins = 0;; ++spins) { if (__all(ld_agent(kvflag + (rb) * 64) != 0u)) break; \
        if (spin_fail(spins, tmo, 3u)) break; __builtin_amdgcn_s_sleep(8); } } while (0)
#define LOAD_KV(sg) do { const int so_ = (sg) * 2 * QBLK + w * 1024; \
        _Pragma("unroll") for (int q = 0; q < 4; ++q) \
          asm volatile("s_mov_b32 m0, %0\n\ts_nop 4\n\tbuffer_load_dwordx4 %1, %2, %3 offen sc1 lds" :: "s"((unsigned)(size_t)&L.kv[w][q][0]), "v"((q < 2 ? tz : tz ^ ((tz >> 1) & 8)) * 16), "s"(rs_x), \
                       "s"(so_ + (q & 1) * QBLK + ((q >> 1) ? xv : xk)) : "memory"); } while (0)
    v4u* own = &L.sA[4 * w * 64 + lane];
    unsigned pz0 = 0;
    const v4u* kxp = own + (offsetof(ChainLds, kv) >> 4);
    const unsigned lb0 = (unsigned)(size_t)Lraw;
    static_assert(offsetof(ChainLds, ms) == 20480, "");
    {
      PF_ADDR;
      CODE_PREFETCH;
      WAIT_KV((int)((P.ctl->flagH - &P.ctl->flagA[0][0]) / 64));
      LOAD_KV(0);
      if (PF1) pz0 = __builtin_amdgcn_raw_buffer_load_b32(rs_x, (tz & 31) * 128, (2 + (w & 1)) * QBLK + ((w >> 1) ? xv : xk), 16);
    }
    const float QS = 0.08838834764831845f * 1.4426950408889634f;
    unsigned sWo = (unsigned)(size_t)&L.sW[w][0][0][lane]; asm volatile("" : "+v"(sWo));
    const __attribute__((address_space(3))) v4u* sWp = (const __attribute__((address_space(3))) v4u*)sWo;
    const v2u JK = {lq == (l15 >> 2) && !(l15 & 1) ? 0x3f803f80u : 0u, lq == (l15 >> 2) && (l15 & 1) ? 0x3f803f80u : 0u};
    const bool twin = (lane & 2) != 0;
    unsigned xr = lane * 16, xw = tid * 16;
    const u16* vxp = (const u16*)&L.kv[w][2][(l15 & 3) * 16 + ((4 * lq + (l15 >> 2)) ^ ((l15 & 1) << 3))];
    int kvok = -1;
    unsigned pz = pz0;
    unsigned pot = 0, kfl = 0, d1p = 140;
    int mlo = mem_lane_off, hoff = dbl(h * 2048) + w * 512 + lane * 4;
    v2u vm0 = {0u, 0u}, vm1 = {0u, 0u};
    const int alo = lane < NBL ? ALM + lane * 128 : (lane >= 62 ? RECO + (lane - 62) * 4 : 0x7ffffff0);
    for (int s = 0; s < NSEG; ++s) {
      v8u wl[NLW];
#pragma unroll
      for (int i = 0; i < NPRE; ++i) wl[i] = cat8(sWp[i * 128], sWp[i * 128 + 64]);
      f32x4 acc[4] = {zero4(), zero4(), zero4(), zero4()};
      int mb = MBASE(s), vb = VBASE(s), ob = OBASE(s);
      asm volatile("" : "+s"(mb), "+s"(vb), "+s"(ob));
      asm volatile("" : "+v"(acc[0]), "+v"(acc[1]), "+v"(acc[2]), "+v"(acc[3]), "+v"(mlo), "+v"(hoff),
                        "+{v[208:215]}"(wl[0]), "+{v[216:223]}"(wl[1]), "+{v[224:231]}"(wl[2]), "+{v[144:151]}"(wl[3]), "+{v[152:159]}"(wl[4]));
      if (s > 0) {
        if (__builtin_expect(s == 1, 0)) {
          unsigned recD;
          if (w == 0) { rec_get(tA, recD, rs_m, mb + RECO, tmo, 8); L.ms[0][0][0] = __uint_as_float(tA - (recD & 0xffffu) - (recD >> 16)); }
          lds_barrier();
          tA = __builtin_amdgcn_readfirstlane(__float_as_uint(L.ms[0][0][0]));
        }
        wait_rt(tA);
      }
      v4u a[16]; f32x4 dm;
#pragma unroll
      for (int k = 0; k < 4; ++k) { a[k] = __builtin_amdgcn_raw_buffer_load_b128(rs_m, mlo + k * 1024, mb, 16); SB0; for (int z = 0; z < PACEQ; ++z) { WARM32(dm); } }
#pragma unroll
      for (int z = 0; z < NWQ - 4 * PACEQ; ++z) { WARM32(dm); }
#define SMF_A(ACC, A, W) asm volatile("v_smfmac_f32_16x16x64_bf16 %0, %1, %2, %3" : "+v"(ACC) : "v"(A), "a"(W), "v"(idx)); SB0
#define SMF_V(ACC, A, W) asm volatile("v_smfmac_f32_16x16x64_bf16 %0, %1, %2, %3" : "+v"(ACC) : "v"(A), "v"(W), "v"(idx)); SB0
#define MF32Z(D, A, B) asm volatile("v_mfma_f32_16x16x32_bf16 %0, %1, %2, 0" : "=&v"(D) : "v"(A), "v"(B)); SB0
#define MF32A(D, A, B) asm volatile("v_mfma_f32_16x16x32_bf16 %0, %1, %2, %0" : "+v"(D) : "v"(A), "v"(B)); SB0
#define PIN(x) asm volatile("" : "+v"(x))
#define REGATHER(k0) do { \
        _Pragma("unroll 1") for (unsigned fails = 0;; ++fails) { \
          if (fails > 8) __builtin_amdgcn_s_sleep(4); \
          _Pragma("unroll") for (int k = k0; k < 4; ++k) a[k] = __builtin_amdgcn_raw_buffer_load_b128(rs_m, mlo, mb + k * 1024, 16); \
          unsigned mm = 0; \
          _Pragma("unroll") for (int k = k0; k < 4; ++k) mm = chk4(mm, a[k]); \
          if (__all(mm != 0xffffffffu) || spin_fail(fails, tmo, 1u)) break; \
        } \
        _Pragma("unroll") for (int k = k0; k < 4; ++k) PIN(a[k]); \
        if (s > 0) al_set(rs_m, mb + ALM, alw, rtnow() - P.sh1 + d1p); } while (0)
#define SMF1(t, A, W) do { \
        if constexpr (t == 2) asm volatile("v_smfmac_f32_16x16x64_bf16 %0, %1, %2, %3" : "+{v[200:203]}"(acc[2]) : A, W, "v"(idx)); \
        else if constexpr (t == 3) asm volatile("v_smfmac_f32_16x16x64_bf16 %0, %1, %2, %3" : "+{v[204:207]}"(acc[3]) : A, W, "v"(idx)); \
        else if constexpr (t == 1) asm volatile("v_smfmac_f32_16x16x64_bf16 %0, %1, %2, %3" : "+{v[180:183]}"(acc[1]) : A, W, "v"(idx)); \
        else asm volatile("v_smfmac_f32_16x16x64_bf16 %0, %1, %2, %3" : "+{v[176:179]}"(acc[0]) : A, W, "v"(idx)); } while (0)
#define SMFQ(t, i, W) do { \
        if constexpr (i == 0) SMF1(t, "{v[232:235]}"(a[i]), W); else if constexpr (i == 1) SMF1(t, "{v[236:239]}"(a[i]), W); else if constexpr (i == 2) SMF1(t, "{v[240:243]}"(a[i]), W); else if constexpr (i == 3) SMF1(t, "{v[244:247]}"(a[i]), W); \
        else if constexpr (i == 4) SMF1(t, "{v[208:211]}"(a[i]), W); else if constexpr (i == 5) SMF1(t, "{v[212:215]}"(a[i]), W); else if constexpr (i == 6) SMF1(t, "{v[216:219]}"(a[i]), W); else if constexpr (i == 7) SMF1(t, "{v[220:223]}"(a[i]), W); \
        else if constexpr (i == 8) SMF1(t, "{v[224:227]}"(a[i]), W); else if constexpr (i == 9) SMF1(t, "{v[228:231]}"(a[i]), W); else if constexpr (i == 10) SMF1(t, "{v[232:235]}"(a[i]), W); else if constexpr (i == 11) SMF1(t, "{v[236:239]}"(a[i]), W); \
        else if constexpr (i == 12) SMF1(t, "{v[240:243]}"(a[i]), W); else if constexpr (i == 13) SMF1(t, "{v[244:247]}"(a[i]), W); \
        else if constexpr (i == 14) SMF1(t, "{v[168:171]}"(a[i]), W); else SMF1(t, "{v[172:175]}"(a[i]), W); \
        SB0; } while (0)
#define SMFB(t, i, n) do { \
        if constexpr (n == 0) SMFQ(t, i, "{v[8:15]}"(wb[n])); else if constexpr (n == 1) SMFQ(t, i, "{v[16:23]}"(wb[n])); else if constexpr (n == 2) SMFQ(t, i, "{v[24:31]}"(wb[n])); else if constexpr (n == 3) SMFQ(t, i, "{v[32:39]}"(wb[n])); \
        else if constexpr (n == 4) SMFQ(t, i, "{v[40:47]}"(wb[n])); else if constexpr (n == 5) SMFQ(t, i, "{v[48:55]}"(wb[n])); else if constexpr (n == 6) SMFQ(t, i, "{v[56:63]}"(wb[n])); else if constexpr (n == 7) SMFQ(t, i, "{v[64:71]}"(wb[n])); \
        else if constexpr (n == 8) SMFQ(t, i, "{v[72:79]}"(wb[n])); else if constexpr (n == 9) SMFQ(t, i, "{v[80:87]}"(wb[n])); else if constexpr (n == 10) SMFQ(t, i, "{v[88:95]}"(wb[n])); else if constexpr (n == 11) SMFQ(t, i, "{v[96:103]}"(wb[n])); \
        else if constexpr (n == 12) SMFQ(t, i, "{v[104:111]}"(wb[n])); else if constexpr (n == 13) SMFQ(t, i, "{v[112:119]}"(wb[n])); else if constexpr (n == 14) SMFQ(t, i, "{v[120:127]}"(wb[n])); else if constexpr (n == 15) SMFQ(t, i, "{v[128:135]}"(wb[n])); \
        else if constexpr (n == 16) SMFQ(t, i, "{v[136:143]}"(wb[n])); } while (0)
#define ALLD "buffer_load_dword %[al], %[alo], %[rsm], %[mb] offen sc1\n\t"
#define AL1
#define AL2 ALLD
#define AL3
#define MF16Z(D, A, B) asm volatile("v_mfma_f32_16x16x16_bf16 %0, %1, %2, 0" : "=&v"(D) : "v"(A), "v"(B)); SB0
#define LDVM do { vm0.x = __builtin_amdgcn_raw_buffer_load_b32(rs_v, hoff, vb, 16); vm1.x = __builtin_amdgcn_raw_buffer_load_b32(rs_v, hoff, vb + 256, 16); SB0; } while (0)
      v4u wlo, kx0, kx1, vx0, vx1, Qo, Km;
      v2u h0, h1, pm;
      unsigned m, al;
      unsigned long long bad, kmiss;
      f32x4 t1, t2, x0, x1, x2, x3;
      SFOR(u, 0, 64)
        constexpr int i = seq_i(u), t = seq_t(u), c = seq_c(u), n = seq_n(u);
        if constexpr (c == 0) { SMFQ(t, i, "a"(wa[n])); } else if constexpr (c == 1) { SMFB(t, i, n); }
        else if constexpr (n == 0) { SMFQ(t, i, "{v[208:215]}"(wl[n])); } else if constexpr (n == 1) { SMFQ(t, i, "{v[216:223]}"(wl[n])); } else if constexpr (n == 2) { SMFQ(t, i, "{v[224:231]}"(wl[n])); }
        else if constexpr (n % 3 == 0) { SMFQ(t, i, "{v[144:151]}"(wl[n])); } else if constexpr (n % 3 == 1) { SMFQ(t, i, "{v[152:159]}"(wl[n])); } else { SMFQ(t, i, "{v[160:167]}"(wl[n])); }
        if constexpr (u == 3 && NWQ > 0) { PIN(dm); }
        if constexpr (u == 0) { PIN(pz); }
        if constexpr (u == 20) { kmiss = __ballot(kfl == 0u); asm volatile("" : "+s"(kmiss)); SB0; }
        if constexpr (u < 8 && !(u & 1)) { own[(u >> 1) * 64] = a[u >> 1]; SB0; }
        if constexpr (u == 0) { m = umax3(a[0].x, a[0].y, a[0].z); PIN(m); SB0; }
        if constexpr (u == 1) { m = m > a[0].w ? m : a[0].w; PIN(m); SB0; }
        if constexpr (u >= 2 && u < 8) { m = (u & 1) ? umax3(m, a[u >> 1].z, a[u >> 1].w) : umax3(m, a[u >> 1].x, a[u >> 1].y); PIN(m); SB0; }
        if constexpr (u == 11) {
          if (__builtin_expect(bad != 0, 0)) {
            REGATHER(0);
#pragma unroll
            for (int k = 0; k < 4; ++k) { own[k * 64] = a[k]; acc[k] = zero4(); }
            asm volatile("s_nop 1" : "+v"(acc[0]), "+v"(acc[1]), "+v"(acc[2]), "+v"(acc[3])); SB0;
            SFOR(v, 0, 12)
              constexpr int vi = seq_i(v), vt = seq_t(v), vn = seq_n(v);
              if constexpr (seq_c(v) == 1) { SMFB(vt, vi, vn); }
              else { v8u wt; asm volatile("ds_read_b128 v[224:227], %1 offset:%2\n\tds_read_b128 v[228:231], %1 offset:%3\n\ts_waitcnt lgkmcnt(0)" : "=&{v[224:231]}"(wt) : "v"(sWo), "i"(vn * 2048), "i"(vn * 2048 + 1024));
                     SMFQ(vt, vi, "{v[224:231]}"(wt)); }
            SEND
          }
          asm volatile("s_waitcnt lgkmcnt(3)\n\ts_barrier" ::: "memory"); SB0;
        }
        if constexpr (rd_a(u) != 0) { constexpr int j = rd_a(u); a[j] = sAq[(j >> 2) - 1][(j & 3) * 64]; SB0; }
        if constexpr (rd_w(u) >= 0) { constexpr int j = NPRE + (rd_w(u) >> 1); if constexpr (!(rd_w(u) & 1)) wlo = sWp[j * 128]; else wl[j] = cat8(wlo, sWp[j * 128 + 64]); SB0; }
        if constexpr (u == 8) { bad = __ballot(m == 0xffffffffu); asm volatile("" : "+s"(bad)); SB0; }
        if constexpr (u == 40) { kx0 = kxp[0]; SB0; }
        if constexpr (u == 41) { kx1 = kxp[64]; SB0; }
        if constexpr (u == 40) { h0 = (v2u){pk2(acc[0][0], acc[0][1]), pk2(acc[0][2], acc[0][3])}; PIN(h0); SB0; }
        if constexpr (u == 41) { MF16Z(t1, h0, JK); h1 = (v2u){pk2(acc[1][0], acc[1][1]), pk2(acc[1][2], acc[1][3])}; PIN(h1); SB0; }
        if constexpr (u == 42) { asm volatile("s_nop 0"); MF16Z(t2, h1, JK); Qo.x = pk2(t1[0], t1[1]); Qo.y = pk2(t1[2], t1[3]); PIN(Qo.x); PIN(Qo.y); SB0; }
        if constexpr (u == 44) { Qo.z = pk2(t2[0], t2[1]); Qo.w = pk2(t2[2], t2[3]); PIN(Qo); SB0; }
        if constexpr (u == 45) { asm volatile("s_nop 0"); MF32Z(t1, kx0, Qo); MF32Z(t2, kx1, Qo); }
        if constexpr (u == 43) { v2u p0 = trrd(vxp), p1 = trrd(vxp + 512); vx0 = (v4u){p0.x, p0.y, p1.x, p1.y}; SB0; }
        if constexpr (u == 46) { v2u p2 = trrd(vxp + 4), p3 = trrd(vxp + 516); vx1 = (v4u){p2.x, p2.y, p3.x, p3.y}; SB0; }
        if constexpr (u >= 47 && u < 51) { constexpr int e = u - 47; t1[e] = twin ? t2[e] : t1[e]; PIN(t1[e]); SB0; }
        if constexpr (u == 51) { *(f32x4*)((char*)L.xs + xw) = t1; SB0; }
        if constexpr (u == XB) { lds_barrier(); SB0; x0 = *(const f32x4*)((const char*)L.xs[0] + xr); SB0; }
        if constexpr (u == XB + 1) { x1 = *(const f32x4*)((const char*)L.xs[1] + xr); SB0; }
        if constexpr (u == XB + 2) { x2 = *(const f32x4*)((const char*)L.xs[2] + xr); SB0; }
        if constexpr (u == XB + 3) { x3 = *(const f32x4*)((const char*)L.xs[3] + xr); SB0; }
        if constexpr (u == 61) { asm volatile("v_pk_add_f32 v[208:209], v[208:209], v[216:217]" : "+{v[208:211]}"(x0) : "{v[216:219]}"(x1)); SB0; }
        if constexpr (u == 62) { asm volatile("v_pk_add_f32 v[210:211], v[210:211], v[218:219]" : "+{v[208:211]}"(x0) : "{v[216:219]}"(x1)); SB0; }
        if constexpr (u == 63) { asm volatile("v_pk_add_f32 v[212:213], v[212:213], v[220:221]\n\tv_pk_add_f32 v[214:215], v[214:215], v[222:223]" : "+{v[212:215]}"(x2) : "{v[220:223]}"(x3)); SB0; }
        if constexpr (u == VSLOT) { asm volatile("buffer_load_dword v196, %1, %2, %3 offen sc1" : "+{v[196:197]}"(vm0) : "v"(hoff), "s"(rs_v), "s"(vb)); SB0; }
        if constexpr (u == VSLOT + 1) { asm volatile("buffer_load_dword v198, %1, %2, %3 offen offset:256 sc1" : "+{v[198:199]}"(vm1) : "v"(hoff), "s"(rs_v), "s"(vb)); SB0; }
      SEND
      {
        f32x4 c0 = acc[2], c1 = acc[3], at; f32x2 iv;
        asm volatile(
          "v_pk_add_f32 v[208:209], v[208:209], v[212:213]\n\t"
          "v_pk_add_f32 v[210:211], v[210:211], v[214:215]\n\t"
          "v_cvt_pk_bf16_f32 v216, v200, v201\n\t"
          "v_cvt_pk_bf16_f32 v217, v202, v203\n\t"
          "v_pk_mul_f32 v[208:209], v[208:209], %[qs] op_sel_hi:[1,0]\n\t"
          "v_pk_mul_f32 v[210:211], v[210:211], %[qs] op_sel_hi:[1,0]\n\t"
          "v_mfma_f32_16x16x16_bf16 v[220:223], v[216:217], %[jk], 0\n\t"
          "v_cvt_pk_bf16_f32 v218, v204, v205\n\t"
          "v_cvt_pk_bf16_f32 v219, v206, v207\n\t"
          "v_max_f32 v236, v208, v209\n\t"
          "v_max3_f32 v236, v236, v210, v211\n\t"
          "v_mfma_f32_16x16x16_bf16 v[224:227], v[218:219], %[jk], 0\n\t"
          "s_nop 0\n\t"
          "v_max_f32_dpp v236, v236, v236 quad_perm:[2,3,0,1] row_mask:0xf bank_mask:0xf\n\t"
          "v_mov_b32 v237, v236\n\t"
          "v_cvt_pk_bf16_f32 v228, v220, v221\n\t"
          "v_cvt_pk_bf16_f32 v229, v222, v223\n\t"
          "v_permlane16_swap_b32 v236, v237\n\t"
          "v_max_f32 v236, v236, v237\n\t"
          "v_mov_b32 v237, v236\n\t"
          "v_cvt_pk_bf16_f32 v230, v224, v225\n\t"
          "v_cvt_pk_bf16_f32 v231, v226, v227\n\t"
          "v_permlane32_swap_b32 v236, v237\n\t"
          "v_max_f32 v236, v236, v237\n\t"
          "v_mfma_f32_16x16x32_bf16 v[232:235], v[228:231], %[qo], 0\n\t"
          "v_sub_f32 v208, v208, v236\n\t"
          "v_sub_f32 v209, v209, v236\n\t"
          "v_sub_f32 v210, v210, v236\n\t"
          "v_sub_f32 v211, v211, v236\n\t"
          "v_exp_f32 v208, v208\n\t"
          "v_exp_f32 v209, v209\n\t"
          "s_nop 1\n\t"
          "ds_write_b64 %[msw], v[232:233] offset:20480\n\t"
          AL1
          "v_exp_f32 v210, v210\n\t"
          "v_exp_f32 v211, v211\n\t"
          "v_cvt_pk_bf16_f32 v240, v208, v209\n\t"
          "v_cvt_pk_bf16_f32 v241, v210, v211\n\t"
          "s_nop 0\n\t"
          "v_mov_b32_dpp v242, v240 quad_perm:[2,3,0,1] row_mask:0xf bank_mask:0xf\n\t"
          "v_mov_b32_dpp v243, v241 quad_perm:[2,3,0,1] row_mask:0xf bank_mask:0xf\n\t"
          "v_mov_b64 v[232:233], %[one]\n\t"
          "v_mov_b64 v[234:235], %[one]\n\t"
          "v_mfma_f32_16x16x32_bf16 v[200:203], v[240:243], %[vx0], 0\n\t"
          "s_waitcnt lgkmcnt(0)\n\t"
          "s_barrier\n\t"
          "ds_read_b64 v[220:221], %[msr] offset:20480\n\t"
          "ds_read_b64 v[222:223], %[msr] offset:21504\n\t"
          "v_mfma_f32_16x16x32_bf16 v[204:207], v[240:243], %[vx1], 0\n\t"
          "ds_read_b64 v[224:225], %[msr] offset:22528\n\t"
          "ds_read_b64 v[226:227], %[msr] offset:23552\n\t" AL2
          "s_nop 2\n\t"
          "v_mfma_f32_16x16x32_bf16 v[212:215], v[240:243], v[232:235], 0\n\t"
          "s_waitcnt lgkmcnt(2)\n\t"
          "v_pk_add_f32 v[216:217], v[220:221], v[222:223]\n\t"
          "s_waitcnt lgkmcnt(0)\n\t"
          "v_pk_add_f32 v[218:219], v[224:225], v[226:227]\n\t"
          "s_nop 0\n\t"
          "v_pk_add_f32 v[216:217], v[216:217], v[218:219]\n\t"
          "s_nop 0\n\t"
          "v_pk_mul_f32 v[216:217], v[216:217], %[qs] op_sel_hi:[1,0]\n\t"
          "s_nop 0\n\t"
          "v_max3_f32 v218, v216, v217, v236\n\t"
          "v_max3_f32 v219, v216, v217, v236\n\t"
          "v_mov_b64 v[240:241], %[ca]\n\t"
          "v_mov_b64 v[242:243], %[cl]\n\t"
          "v_permlane16_swap_b32 v218, v219\n\t"
          "v_max_f32 v218, v218, v219\n\t"
          "v_mov_b32 v219, v218\n\t"
          "s_nop 1\n\t"
          "v_permlane32_swap_b32 v218, v219\n\t"
          "v_max_f32 v218, v218, v219\n\t"
          "v_sub_f32 v236, v236, v218\n\t"
          "v_sub_f32 v216, v216, v218\n\t"
          "v_sub_f32 v217, v217, v218\n\t"
          "v_exp_f32 v236, v236\n\t"
          "v_exp_f32 v216, v216\n\t"
          "v_exp_f32 v217, v217\n\t"
          "v_cvt_pk_bf16_f32 v239, v236, 0\n\t"
          "v_cvt_pk_bf16_f32 v238, v216, v217\n\t"
          "s_nop 1\n\t"
          "v_mfma_f32_16x16x16_bf16 v[220:223], v[238:239], v[240:241], 0\n\t"
          "v_mfma_f32_16x16x16_bf16 v[224:227], v[238:239], v[242:243], 0\n\t"
          "s_waitcnt vmcnt(%[y1])\n\t"
          "v_mfma_f32_16x16x16_bf16 v[232:235], v[238:239], v[196:197], 0\n\t"
          "s_waitcnt vmcnt(%[y0])\n\t"
          "v_max_u32 v237, v196, v198\n\t"
          "v_mfma_f32_16x16x16_bf16 v[208:211], v[238:239], v[198:199], 0\n\t"
          "v_cmp_eq_u32 %[bad], -1, v237\n\t"
          "v_pk_fma_f32 v[216:217], v[212:213], v[220:221], v[224:225]\n\t"
          "s_nop 0\n\t"
          "v_rcp_f32 v216, v216\n\t"
          "v_rcp_f32 v217, v217\n\t"
          "v_pk_fma_f32 v[232:233], v[200:201], v[220:221], v[232:233]\n\t"
          "s_cmp_eq_u64 %[bad], 0\n\t"
          "v_pk_fma_f32 v[208:209], v[204:205], v[220:221], v[208:209]\n\t"
          "v_pk_mul_f32 v[232:233], v[232:233], v[216:217]\n\t"
          "v_pk_mul_f32 v[208:209], v[208:209], v[216:217]\n\t"
          "s_cselect_b64 exec, -1, 0\n\t"
          "v_cvt_pk_bf16_f32 v232, v232, v208\n\t"
          "v_cvt_pk_bf16_f32 v233, v233, v209\n\t"
          "v_min_u32 v232, -2, v232\n\t"
          "v_min_u32 v233, -2, v233\n\t"
          "buffer_store_dword v232, %[ho], %[rso], %[ob] offen sc1\n\t"
          "buffer_store_dword v233, %[ho], %[rso], %[ob] offen offset:256 sc1\n\t"
          "s_mov_b64 exec, -1\n\t" AL3
          : "+{v[200:203]}"(c0), "+{v[204:207]}"(c1), "=&{v[220:223]}"(at), "=&{v[216:217]}"(iv), "=&{v[228:231]}"(Km), "=&{v[238:239]}"(pm),
            [al] "=&v"(al), [bad] "=&s"(bad), "+{v[208:211]}"(x0), "+{v[212:215]}"(x2), "+{v[196:197]}"(vm0), "+{v[198:199]}"(vm1)
          : [qs] "s"((unsigned long long)__float_as_uint(QS)), [jk] "v"(JK), [qo] "v"(Qo), [vx0] "v"(vx0), [vx1] "v"(vx1), [one] "s"(0x3f803f803f803f80ull), [ca] "s"(0x3e8000000000ull), [cl] "s"(0x3f803f80ull),
            [msw] "v"(xw + lb0), [msr] "v"(xr + lb0), [alo] "v"(alo), [rsm] "s"(rs_m), [mb] "s"(mb), [ho] "v"(hoff), [rso] "s"(rs_o), [ob] "s"(ob), [y0] "i"(ALP < 3 ? 1 : 0), [y1] "i"(ALP < 3 ? 2 : 1)
          : "v218", "v219", "v224", "v225", "v226", "v227", "v232", "v233", "v234", "v235", "v236", "v237", "v240", "v241", "v242", "v243", "scc", "memory");
        SB0;
        if (__builtin_expect(bad != 0, 0)) {
#pragma unroll 1
          for (unsigned fails = 0;; ++fails) {
            __builtin_amdgcn_s_sleep(2);
            LDVM;
            if (__all((vm0.x > vm1.x ? vm0.x : vm1.x) != 0xffffffffu)) break;
            if (spin_fail(fails, tmo, 1u)) break;
          }
          f32x4 o0, o1;
          asm volatile("s_nop 1\n\t"
                       "v_mfma_f32_16x16x16_bf16 %0, %4, %2, 0\n\t"
                       "v_mfma_f32_16x16x16_bf16 %1, %4, %3, 0\n\t"
                       "s_nop 7"
                       : "=&v"(o0), "=&v"(o1) : "v"(vm0), "v"(vm1), "v"(pm));
          __builtin_amdgcn_raw_buffer_store_b32(nosent(pk2(__builtin_fmaf(c0[0], at[0], o0[0]) * iv[0], __builtin_fmaf(c1[0], at[0], o1[0]) * iv[0])), rs_o, hoff, ob, 16);
          __builtin_amdgcn_raw_buffer_store_b32(nosent(pk2(__builtin_fmaf(c0[1], at[1], o0[1]) * iv[1], __builtin_fmaf(c1[1], at[1], o1[1]) * iv[1])), rs_o, hoff + 256, ob, 16);
        }
        SB0;
        const unsigned po = rtnow();
        __builtin_amdgcn_raw_buffer_store_b32(nosent(po), rs_m, (h | w | opaque_lane()) == 0 ? RECO + 128 : 0x7ffffff0, mb, 16);
        asm volatile("s_waitcnt vmcnt(%1)" : "+v"(al) : "i"(ALP < 3 ? 3 : 1) : "memory");
        pot = po;
      }
      if (s < NSEG - 1) {
        unsigned recD = __builtin_amdgcn_readlane(al, 63);
        tA = __builtin_amdgcn_readlane(al, 62);
        if (__builtin_expect(tA == 0xffffffffu || recD == 0xffffffffu, 0)) rec_get(tA, recD, rs_m, mb + RECO, tmo);
        {
          unsigned tB = tA - (recD >> 16);
          (void)al_eval(al, tB);
          tA = tB + (recD >> 16); if (s > 0) d1p = recD & 0xffffu; else tA = pot;
        }
        PF_ADDR;
        if (kmiss == 0) ++kvok;
        if (__builtin_expect(((s + 1) >> 2) > kvok && s > 0, 0)) { WAIT_KV((s + 1) >> 2); kvok = (s + 1) >> 2; }
        LOAD_KV(s + 1);
        kfl = 0;
        if (kvok < 15) kfl = ld_agent(kvflag + (kvok + 1) * 64);
        const int po2 = ((s + 2) >> 2) <= kvok && s + 2 < NSEG && !(tz & 32) ? tz * 128 : 0x7ffffff0;
        pz = __builtin_amdgcn_raw_buffer_load_b32(rs_x, po2, (2 * (s + 2) + (w & 1)) * QBLK + ((w >> 1) ? xv : xk), 16);
      }
      { const int lz = opaque_lane();
        const int ko = (lz & 2) ? 0x7ffffff0 : h * 2048 + w * 512 + (2 * (lz >> 2) + (lz & 1)) * 16;
        __builtin_amdgcn_raw_buffer_store_b128((v4u){nosent(Km.x), nosent(Km.y), nosent(Km.z), nosent(Km.w)}, rs_k, ko, s * (MEM * HDA * 2), 16); }
    }
  } else {
    LANE_CONSTS;
    const unsigned c00 = (unsigned)now(), t00 = rtnow();
    const int c = b - NQK, rank = ((c >> 2) & 1) | ((c & 3) << 1), cg = c >> 1, rh = c & 1;
    v8u wv[16], wo[32];
#define OHEAD(i) (16 * (((i) >> 2) ^ (rank & 1)) + ((4 * ((i) & 3) + w + 4 * (rank >> 1)) & 15))
    {
      { const int r = lane >> 3, h2 = (r >> 1) & 3;
        const int ch = (((((lane & 7) >> 1) ^ h2) << 1) | (lane & 1));
#pragma unroll 4
        for (int q = 0; q < 32; ++q) {
          const int pc = 32 * w + q;
          __builtin_amdgcn_global_load_lds((const unsigned*)(P.Wv + (size_t)(8 * pc + r) * HDA + 64 * c + ch * 8), (unsigned*)(Lraw + pc * 1024), 16, 0, 0);
        } }
      {
        char* stg = Lraw + w * 32768;
        const u16* src = P.Wo + (size_t)(lane >> 3) * DM + 64 * (cg >> 1) + (lane & 7) * 8;
#define LDO(i) _Pragma("unroll 4") for (int q = 0; q < 16; ++q) \
          __builtin_amdgcn_global_load_lds((const unsigned*)(src + (size_t)(OHEAD(i) * 128 + 8 * q) * DM), (unsigned*)(stg + ((i) & 1) * 16384 + q * 1024), 16, 0, 0)
        unsigned vb[4];
#pragma unroll
        for (int t = 0; t < 4; ++t) vb[t] = (unsigned)(size_t)stg + wrow * 128 + (l15 & 3) * 8 + ((t ^ w ^ 3 ^ wsw) << 5);
        SFOR(hf, 0, 2)
          asm volatile("s_waitcnt vmcnt(16)" ::: "memory");
          v2u e[8][4];
#pragma unroll
          for (int i = 0; i < 8; ++i)
            asm volatile("ds_read_b64_tr_b16 %0, %4 offset:%5\n\tds_read_b64_tr_b16 %1, %4 offset:%6\n\tds_read_b64_tr_b16 %2, %4 offset:%7\n\tds_read_b64_tr_b16 %3, %4 offset:%8\n\ts_waitcnt lgkmcnt(0)"
                         : "=&v"(e[i][0]), "=&v"(e[i][1]), "=&v"(e[i][2]), "=&v"(e[i][3])
                         : "v"(vb[i & 3]), "i"((2 * hf + (i >> 2)) * 8192), "i"((2 * hf + (i >> 2)) * 8192 + 128), "i"((2 * hf + (i >> 2)) * 8192 + 4096), "i"((2 * hf + (i >> 2)) * 8192 + 4224) : "memory");
          LDO(hf);
#pragma unroll
          for (int i = 0; i < 8; ++i) wv[8 * hf + i] = (v8u){e[i][0].x, e[i][0].y, e[i][1].x, e[i][1].y, e[i][2].x, e[i][2].y, e[i][3].x, e[i][3].y};
        SEND
        const unsigned base = (unsigned)(size_t)stg + (32 * (lq >> 1) + 2 * (lq & 1) + 4 * (l15 >> 3) + 16 * ((l15 >> 2) & 1)) * 128 + (cg & 1) * 64 + (l15 & 3) * 8;
        SFOR(i, 0, 8)
          if constexpr (i < 7) asm volatile("s_waitcnt vmcnt(16)" ::: "memory"); else asm volatile("s_waitcnt vmcnt(0)" ::: "memory");
          v2u e[4][4];
#pragma unroll
          for (int t = 0; t < 4; ++t)
            asm volatile("ds_read_b64_tr_b16 %0, %4 offset:%5\n\tds_read_b64_tr_b16 %1, %4 offset:%6\n\tds_read_b64_tr_b16 %2, %4 offset:%7\n\tds_read_b64_tr_b16 %3, %4 offset:%8"
                         : "=&v"(e[t][0]), "=&v"(e[t][1]), "=&v"(e[t][2]), "=&v"(e[t][3])
                         : "v"(base), "i"((i & 1) * 16384 + (t >> 1) * 1024 + (t & 1) * 32), "i"((i & 1) * 16384 + (t >> 1) * 1024 + (t & 1) * 32 + 128),
                           "i"((i & 1) * 16384 + (t >> 1) * 1024 + (t & 1) * 32 + 64 * 128), "i"((i & 1) * 16384 + (t >> 1) * 1024 + (t & 1) * 32 + 65 * 128) : "memory");
          asm volatile("s_waitcnt lgkmcnt(0)" : "+v"(e[0][0]), "+v"(e[0][1]), "+v"(e[0][2]), "+v"(e[0][3]), "+v"(e[1][0]), "+v"(e[1][1]), "+v"(e[1][2]), "+v"(e[1][3]),
                                                "+v"(e[2][0]), "+v"(e[2][1]), "+v"(e[2][2]), "+v"(e[2][3]), "+v"(e[3][0]), "+v"(e[3][1]), "+v"(e[3][2]), "+v"(e[3][3]) :: "memory");
          if constexpr (i + 2 < 8) { LDO(i + 2); }
#pragma unroll
          for (int t = 0; t < 4; ++t) { wo[4 * i + t] = (v8u){e[t][0].x, e[t][0].y, e[t][1].x, e[t][1].y, e[t][2].x, e[t][2].y, e[t][3].x, e[t][3].y}; asm volatile("" : "+a"(wo[4 * i + t])); }
        SEND
#undef LDO
        lds_barrier();
      }
    }
    PAR_BITS;
    constexpr int RST = 258;
    int redo = lq * 24 + l15 + w * 96; asm volatile("" : "+v"(redo));
    f32x2* red = (f32x2*)&L.sW[0][0][0][0] + redo;
    f32x2* vrw[3];
#pragma unroll
    for (int t = 0; t < 3; ++t) vrw[t] = (f32x2*)L.sA + t * RST + (t ^ w ^ 3) * 64 + lane;
    const f32x2* vrr = (const f32x2*)L.sA + w * 64 + lane;
    const int o_lane_off = ((lq * 2 + rh) * 16 + l15) * 16;
    const unsigned redr = (unsigned)(size_t)((const f32x2*)&L.sW[0][0][0][0] + ((lane >> 3) & 3) * 24 + 8 * (lane >> 5) + (lane & 7));
    const bool keeper = (c == 0 && w == 3);
    int d1 = P.dmv * 16, d2 = P.dov * 16, m1 = (P.dq2 + P.im) * 16, m2 = (P.dq + P.im) * 16;
    int LV = 70;
    bool prev_clean = false;
    unsigned recD = 140;
    const float tpc = 16.f * (float)(rtnow() - t00) / (float)((unsigned)now() - c00);
    int e1 = __builtin_amdgcn_readfirstlane((int)(P.dmv * tpc)), e2 = __builtin_amdgcn_readfirstlane((int)(P.dov * tpc));
    d1 = e1 + m1; d2 = e2 + m2;
    int kx2 = 0, kfl = 0; unsigned kto = 0, kA0 = 0, kpm = 0; (void)kpm;
    int* failw = (int*)L.xs;
    if (tid < 2) failw[tid] = 0;
    CODE_PREFETCH;
    int ga, vo; f32x4 va[4];
#define V_PREP(st) do { ga = MBASE(st) + mem_lane_off; vo = VBASE(st) + dbl(c * 1024) + tid * 4; \
      _Pragma("unroll") for (int t = 0; t < 4; ++t) va[t] = zero4(); \
      asm volatile("" : "+v"(ga), "+v"(vo), "+v"(va[0]), "+v"(va[1]), "+v"(va[2]), "+v"(va[3])); } while (0)
    V_PREP(0);
    f32x16 dd = {};
    for (int s = 0; s < NSEG; ++s) {
      if (s > 0) wait_rt(tA + P.dv);
      f32x2 fv;
#define WARMD SB0
#define V_PASS(ok) do { v4u a[4]; unsigned m; \
        _Pragma("unroll") for (int k = 0; k < 4; ++k) { a[k] = __builtin_amdgcn_raw_buffer_load_b128(rs_m, ga + k * 1024, 0, 16); SB0; if (PACEV && k < 3) { asm volatile("s_nop %0" :: "i"(PACEV / 4 - 2)); SB0; } } \
        _Pragma("unroll") for (int z = 0; z < NWV; ++z) { WARMD; } \
        _Pragma("unroll") for (int u = 0; u < 16; ++u) { \
          const int k = u < 8 ? u >> 2 : 2 + (u & 1), t = u < 8 ? u & 3 : (u - 8) >> 1; \
          SMF_V(va[t], a[k], wv[4 * k + t]); \
          if (u == 0) m = umax3(a[0].x, a[0].y, a[0].z); \
          if (u == 1) m = m > a[0].w ? m : a[0].w; \
          if (u >= 4 && u < 10) m = (u & 1) ? umax3(m, a[(u - 2) >> 1].z, a[(u - 2) >> 1].w) : umax3(m, a[(u - 2) >> 1].x, a[(u - 2) >> 1].y); \
          if (u < 10) { PIN(m); SB0; } \
          if (u == 10) { ok = __ballot(m == 0xffffffffu); asm volatile("" : "+s"(ok)); SB0; } \
          if (u == 14) { *vrw[0] = fv; SB0; } \
          if (u >= 12) { fv[u & 1] = va[(u - 12) >> 1][(u & 1) * 2] + va[(u - 12) >> 1][(u & 1) * 2 + 1]; PIN(fv[u & 1]); SB0; } \
        } } while (0)
      unsigned long long vbad;
      V_PASS(vbad);
      if (__builtin_expect(vbad != 0, 0)) {
#pragma unroll 1
        for (unsigned fails = 0;; ++fails) {
          if (fails > 8) __builtin_amdgcn_s_sleep(4);
          asm volatile("s_nop 7" : "+v"(va[0]), "+v"(va[1]), "+v"(va[2]), "+v"(va[3]));
#pragma unroll
          for (int t = 0; t < 4; ++t) va[t] = zero4();
          asm volatile("s_nop 1" : "+v"(va[0]), "+v"(va[1]), "+v"(va[2]), "+v"(va[3]));
          V_PASS(vbad);
          if (vbad == 0 || spin_fail(fails, tmo, 1u)) break;
        }
      }
      {
        f32x2 f2; f2[0] = va[2][0] + va[2][1]; PIN(f2[0]); f2[1] = va[2][2] + va[2][3]; PIN(f2[1]); SB0;
        WARMD;
        *vrw[1] = fv; SB0; *vrw[2] = f2; SB0;
        lds_barrier();
        const f32x2 v0 = vrr[0], v1 = vrr[RST], v2 = vrr[2 * RST]; SB0;
        WARMD;
        fv[0] = va[3][0] + va[3][1]; PIN(fv[0]); fv[1] = va[3][2] + va[3][3]; PIN(fv[1]); SB0;
        const f32x2 r = (fv + v0) + (v1 + v2);
        unsigned vv = nosent(pk2(r[0], r[1])); PIN(vv); SB0;
        __builtin_amdgcn_raw_buffer_store_b32(vv, rs_v, vo, 0, 16); SB0;
      }
      const unsigned tv = rtnow();
      SB0;
      if (keeper && s > 0) {
        asm volatile("" ::: "memory");
        if (lane == 0) __hip_atomic_store(&P.ctl->prog[0], (unsigned)(s - 1), RLX_AGENT);
        const int kx1 = kto == 0xffffffffu ? e1 >> 4 : (int)(kto - kA0);
        const bool okA = kfl & 1, okB = kfl & 2;
        if (s > 1) {
          const int gain = s < 5 ? P.gain0 : P.gain;
          if (okA) {
            int x = kx1 * 16 - e1; x = x > 320 ? 320 : x; e1 += (x * gain) >> 4;
            if (!okB) m1 = m1 < 60 * 16 ? m1 + P.inc : m1; else if (m1 > P.dq2 * 16) m1 -= P.dec;
            if (m2 > P.dq * 16) m2 -= P.dec;
          } else if (prev_clean) m2 = m2 < 60 * 16 ? m2 + P.inc : m2;
          if (kfl & 4) { int x = kx2 * 16 - e2; x = x > 320 ? 320 : x; e2 += (x * gain) >> 4; }
          d1 = e1 + m1; d2 = e2 + m2;
        }
        prev_clean = okB;
      }
      if (s == NSEG - 1) break;
      const unsigned tA0 = tA;
      LV = (int)(tv - tA0);
      V_PREP(s + 1);
      int oo[8], mo = MBASE(s + 1) + dbl(c * 256) + lane * 4, alo = lane < (keeper ? 8 : NAL) ? MBASE(s) + ALM + 2048 + lane * 128 : (keeper && lane == 63 ? MBASE(s) + RECO + 128 : 0x7ffffff0);
#pragma unroll
      for (int i = 0; i < 8; ++i) oo[i] = OBASE(s) + dbl(OHEAD(i) * 2048) + o_lane_off;
      f32x4 oa[4] = {zero4(), zero4(), zero4(), zero4()};
      asm volatile("" : "+v"(oo[0]), "+v"(oo[1]), "+v"(oo[2]), "+v"(oo[3]), "+v"(oo[4]), "+v"(oo[5]), "+v"(oo[6]), "+v"(oo[7]), "+v"(mo), "+v"(alo),
                        "+v"(oa[0]), "+v"(oa[1]), "+v"(oa[2]), "+v"(oa[3]));
      unsigned flB = 0, tB = tv;
      if (s > 0) {
        wait_rt(tA0 + (recD & 0xffffu) - 50);
        const unsigned al = __builtin_amdgcn_raw_buffer_load_b32(rs_m, lane < NBL ? ALM + lane * 128 : (lane >= 62 ? RECO + (lane - 62) * 4 : 0x7ffffff0), MBASE(s), 16);
        tA = __builtin_amdgcn_readlane(al, 62); recD = __builtin_amdgcn_readlane(al, 63);
        if (__builtin_expect(tA == 0xffffffffu || recD == 0xffffffffu, 0)) rec_get(tA, recD, rs_m, MBASE(s) + RECO, tmo);
        tB = tA - (recD >> 16);
        flB = al_eval(al, tB);
        tA = tB + (recD >> 16);
      }
      flB = __builtin_amdgcn_readfirstlane(flB); tA = __builtin_amdgcn_readfirstlane(tA); tB = __builtin_amdgcn_readfirstlane(tB);
      asm volatile("" : "+s"(flB), "+s"(tA), "+s"(tB));
      wait_rt(tB);
#define O_PART(i0, i1) _Pragma("unroll") for (int i = i0; i < i1; ++i) { \
          _Pragma("unroll") for (int t = 0; t < 4; ++t) { SMF_A(oa[t], f[i], wo[4 * i + t]); \
            if (t < 2) { m = t ? umax3(m, f[i].z, f[i].w) : umax3(m, f[i].x, f[i].y); asm volatile("" : "+v"(m)); SB0; } \
            if (i == i1 - 1 && t == 2) { obad = __ballot(m == 0xffffffffu); asm volatile("" : "+s"(obad)); SB0; } } }
#define O_PASS(first) do { v4u f[8]; \
        _Pragma("unroll") for (int i = 0; i < 8; ++i) { f[i] = __builtin_amdgcn_raw_buffer_load_b128(rs_o, oo[i], 0, 16); SB0; if (first && i < 7) PACE_O; } \
        if (!first) { al_set(rs_m, MBASE(s) + ALM, alnow ? alw : -1, tA + P.sh2); SB0; } \
        unsigned m = 0; \
        O_PART(0, EARLY) \
        if (__builtin_expect(obad == 0, 1)) { O_PART(EARLY, 8) } \
        } while (0)
      unsigned long long obad; bool alnow = s > 0;
      O_PASS(true);
      if (__builtin_expect(obad != 0, 0)) {
        asm volatile("s_nop 7"); SB0;
        failw[1] = s + 1;
#pragma unroll 1
        for (unsigned fails = 0;; ++fails) {
          bool dead = false;
          if (s == 0 || fails > 1) for (unsigned sp = 0;; ++sp) {
            __builtin_amdgcn_s_sleep(4);
            v4u z = __builtin_amdgcn_raw_buffer_load_b128(rs_o, oo[7], 0, 16);
            if (__all(chk4(0u, z) != 0xffffffffu)) break;
            if (spin_fail(sp, tmo, 1u)) { dead = true; break; }
          }
          asm volatile("s_nop 7"); SB0;
#pragma unroll
          for (int t = 0; t < 4; ++t) oa[t] = zero4();
          asm volatile("s_nop 1" : "+v"(oa[0]), "+v"(oa[1]), "+v"(oa[2]), "+v"(oa[3]));
          O_PASS(false);
          alnow = false;
          if (obad == 0) break;
          asm volatile("s_nop 7"); SB0;
          if (dead || spin_fail(fails, tmo, 1u)) break;
        }
      }
      {
        f32x2 r;
        asm volatile("v_add_f32 %0, %2, %3\n\t"
                     "v_add_f32 %1, %4, %5\n\t"
                     "s_nop 0\n\t"
                     "v_add_f32 %0, %0, %6\n\t"
                     "v_add_f32 %0, %0, %7\n\t"
                     "s_nop 1\n\t"
                     "v_add_f32 %1, %1, %8\n\t"
                     "v_add_f32 %1, %1, %9"
                     : "=&v"(r[0]), "=&v"(r[1]) : "v"(oa[0][0]), "v"(oa[0][1]), "v"(oa[1][0]), "v"(oa[1][1]), "v"(oa[2][2]), "v"(oa[2][3]), "v"(oa[3][2]), "v"(oa[3][3]), "v"(oa[0]), "v"(oa[1]), "v"(oa[2]), "v"(oa[3]));
        *red = r; SB0;
      }
      const unsigned alr = __builtin_amdgcn_raw_buffer_load_b32(rs_m, alo, 0, 16); SB0;
      { f32x2 p0, p1, p2, p3; unsigned pk;
        asm volatile("s_cmp_lg_u32 %[w], 0\n\t"
                     "s_waitcnt lgkmcnt(0)\n\t"
                     "s_barrier\n\t"
                     "s_cbranch_scc1 1f\n\t"
                     "ds_read_b64 %0, %[ra]\n\t"
                     "ds_read_b64 %1, %[ra] offset:768\n\t"
                     "ds_read_b64 %2, %[ra] offset:1536\n\t"
                     "ds_read_b64 v[252:253], %[ra] offset:2304\n\t"
                     "s_waitcnt lgkmcnt(2)\n\t"
                     "v_pk_add_f32 %0, %0, %1\n\t"
                     "s_waitcnt lgkmcnt(0)\n\t"
                     "v_pk_add_f32 %2, %2, v[252:253]\n\t"
                     "s_nop 0\n\t"
                     "v_pk_add_f32 v[252:253], %0, %2\n\t"
                     "s_nop 0\n\t"
                     "v_cvt_pk_bf16_f32 %4, v252, v253\n\t"
                     "v_min_u32 %4, -2, %4\n\t"
                     "buffer_store_dword %4, %[mo], %[rs], 0 offen sc1\n\t"
                     "s_branch 2f\n\t"
                     "1:\n\ts_sleep 1\n\t"
                     "2:"
                     : "=&v"(p0), "=&v"(p1), "=&v"(p2), "=&{v[252:253]}"(p3), "=&v"(pk) : [w] "s"(w), [ra] "v"(redr), [mo] "v"(mo), [rs] "s"(rs_m) : "scc", "memory"); }
      SB0;
      const int fw1 = failw[1];
      const unsigned pm = rtnow();
      const bool cleanB = __builtin_amdgcn_readfirstlane(fw1) != s + 1;
      unsigned flA = 0;
      if (s == 0) tA = pm + (c == 0 ? P.pad & 255 : P.pad >> 8);
      else { if (!cleanB) tA += P.sh2; flA = al_eval(alr, tA) | (cleanB ? 0u : 1u); if ((int)(pm + 10 - tA) > 0) tA = pm + 10; }
      if (keeper) {
        const unsigned r1 = (d1 >> 4) + (s < 2 ? (P.boot2 >> (8 * s)) & 255 : 0), r2 = (d2 >> 4) + (s < 2 ? (P.boot3 >> (8 * s)) & 255 : 0);
        __builtin_amdgcn_raw_buffer_store_b64((v2u){nosent(tA + r1 + r2), r1 | (r2 << 16)}, rs_m, lane == 0 ? MBASE(s + 1) + RECO : 0x7ffffff0, 0, 16);
        kto = __builtin_amdgcn_readlane(alr, 63); kx2 = (int)(pm - tB); kA0 = tA0; asm volatile("" : "+s"(flB)); kfl = (flB == 0 ? 1 : 0) | (flA == 0 ? 2 : 0) | (cleanB ? 4 : 0); kpm = pm;
      }
    }
  }
}

__global__ void __launch_bounds__(512) k_prep(PrepParams P) {
  __shared__ unsigned lat[2];
  unsigned long long pc0;
  asm volatile("s_getpc_b64 %0\nLprep_pc0:" : "=s"(pc0));
  const int b = blockIdx.x, tid = threadIdx.x;
  const v4u ff = {0xffffffffu, 0xffffffffu, 0xffffffffu, 0xffffffffu};
  if (b < 3 * NSEG + 48) {
    const int reg = b;
    const bool iso = (reg >> 6) != 1; const int s = reg & (NSEG - 1);
    char* base = reg >= 3 * NSEG ? P.qkv + (size_t)s * QPAGE : (iso ? (reg < NSEG ? P.obuf : P.vbuf) + (size_t)s * OREG : P.membuf + (size_t)s * MREG);
    if (tid < 64) {
      const __amdgpu_buffer_rsrc_t rs = __builtin_amdgcn_make_buffer_rsrc(base, 0, 16384, 0x00020000);
      unsigned best0 = ~0u, best1 = ~0u;
#pragma unroll 1
      for (int t = 0; t < PROBES; ++t) {
        const unsigned long long t0 = __builtin_amdgcn_s_memtime();
        __builtin_amdgcn_raw_buffer_store_b128(ff, rs, tid < (t ? 8 : 16) ? ((t & 1) + (tid >> 3)) * 8192 + (tid & 7) * 16 : 0x7ffffff0, 0, 16);
        asm volatile("s_waitcnt vmcnt(0)" ::: "memory");
        const unsigned dt = (unsigned)(__builtin_amdgcn_s_memtime() - t0);
        if (t & 1) best1 = dt < best1 ? dt : best1; else if (t) best0 = dt < best0 ? dt : best0;
      }
      if (tid == 0) { lat[0] = best0; lat[1] = best1; }
    }
    __syncthreads();
    unsigned xcc; asm volatile("s_getreg_b32 %0, hwreg(HW_REG_XCC_ID)" : "=s"(xcc));
    const int sp = (lat[1] < lat[0]) ^ (((xcc ^ (xcc - b + 1)) >> 2) & 1);
    if (tid == 0) P.par[reg] = reg >= 3 * NSEG ? sp ^ (int)P.far : sp;
    v4u* d = (v4u*)(base + sp * 8192);
    const int nblk = reg >= 3 * NSEG ? 0 : (iso ? 8 : 3);
    for (int i = tid; i < nblk * 512; i += 512) {
      v4u v = ff;
      if (!iso && s == 0) {
        if (i < 1024) {
          const u16* src = P.mem0 + (((i >> 4) & 1) * 4 + ((i >> 1) & 3)) * DM + 32 * (i >> 5) + 8 * ((i >> 3) & 1) + 4 * (i & 1);
#pragma unroll
          for (int p = 0; p < 4; ++p) { unsigned x = src[p] | ((unsigned)src[p + 16] << 16); v[p] = x < 0xfffffffeu ? x : 0xfffffffeu; }
        } else if (i == 1024) v = (v4u){0u, 0u, 0u, 0u};
      }
      __builtin_amdgcn_raw_buffer_store_b128(v, __builtin_amdgcn_make_buffer_rsrc(d, 0, OREG, 0x00020000), ((i >> 9) * 1024 + (i & 511)) * 16, 0, 16);
    }
    if (reg < 3 * NSEG) return;
  } else if (tid == 0) P.par[b] = 0;
  const int fb = b - 3 * NSEG;
  {
    int clen; asm volatile("s_mov_b32 %0, Lprep_pc0-Lfused_pc0" : "=s"(clen));
    unsigned acc_ = 0;
    for (int o_ = 64 + tid * 64; o_ <= clen; o_ += 512 * 64) acc_ ^= *(const unsigned*)((const char*)pc0 - o_);
    asm volatile("" :: "v"(acc_)); }
  v4u pa = {0u, 0u, 0u, 0u};
  if (P.pfr) {
    pa = P.ax[fb * 512 + tid];
#pragma unroll 4
    for (unsigned i = fb * 512 + tid; i < (unsigned)P.pfr * 512; i += 64 * 512) pa ^= P.wk[i] ^ P.wv[i];
  }
  for (unsigned long long i = (unsigned long long)fb * 512 + tid; i < P.sent_vec; i += (unsigned long long)(PSENT - 3 * NSEG) * 512)
    __builtin_amdgcn_raw_buffer_store_b128(ff, __builtin_amdgcn_make_buffer_rsrc(P.sent, 0, (int)P.sent_vec * 16, 0x00020000), (int)i * 16, 0, 16);
  if (KF0) __builtin_amdgcn_raw_buffer_store_b128(ff, __builtin_amdgcn_make_buffer_rsrc((char*)P.sent - TSEG0 * MEM * HDA * 2, 0, KF0 * MEM * HDA * 2, 0x00020000), (fb * 512 + tid) * 16, 0, 16);
  asm volatile("" :: "v"(pa));
  if (fb == 5) for (unsigned i = tid; i < (unsigned)P.ctl_vec; i += 512) P.ctl[i] = (v4u){i < 64 ? 20u : 0u, 0u, 0u, 0u};
}

extern "C" void kernel_launch(void* const* d_in, const int* in_sizes, int n_in,
                              void* d_out, int out_size, void* d_ws, size_t ws_size,
                              hipStream_t stream) {
  const u16* A_x = (const u16*)d_in[0];
  const u16* A_mem0 = (const u16*)d_in[1];
  const u16* A_Wq = (const u16*)d_in[2];
  const u16* A_Wk = (const u16*)d_in[3];
  const u16* A_Wv = (const u16*)d_in[4];
  const u16* A_Wo = (const u16*)d_in[5];
  const u16* B_x = (const u16*)d_in[7];
  const u16* B_wqkv = (const u16*)d_in[8];
  const u16* B_wo = (const u16*)d_in[9];
  u16* out = (u16*)d_out;

  char* ws = (char*)d_ws;
  size_t off = 0;
  auto alloc = [&](size_t bytes) { char* p = ws + off; off += (bytes + 255) & ~(size_t)255; return p; };
  off = (size_t)(-(long long)(size_t)ws) & (OREG - 1);
  char* obuf = alloc((size_t)NSEG * OREG);
  char* vmem = alloc((size_t)NSEG * OREG);
  char* membuf = alloc((size_t)NSEG * MREG);
  u16* kmem = (u16*)alloc((size_t)NSEG * MEM * HDA * 2);
#define KFILL (NSEG - TSEG0)
  const size_t sent_beg = off - (size_t)KFILL * MEM * HDA * 2;
  const size_t sent_end = off;
  unsigned* par = (unsigned*)alloc(4 * NSEG * 4);
  unsigned long long* prof = (unsigned long long*)alloc(96 * 4 * 72 * 8 + 8 * 256 * 16 + 64 * 384 * 8);
  Ctl* ctl = (Ctl*)alloc(sizeof(Ctl));
  off += (size_t)(-(long long)(size_t)(ws + off)) & (QPAGE - 1);
  u16* qkvA = (u16*)alloc(QSIZE);
  u16* qkvB = (u16*)alloc((size_t)S_ * NQKVB * 2);
  float* part = (float*)alloc((size_t)(NSEG - TSEG0) * 8 * SEG * DM * 4);

  PrepParams pp;
  pp.sent = (v4u*)(ws + sent_beg); pp.sent_vec = (sent_end - sent_beg) / 16; pp.ctl = (v4u*)ctl; pp.mem0 = A_mem0; pp.obuf = obuf; pp.membuf = membuf; pp.vbuf = vmem; pp.par = par; pp.ctl_vec = sizeof(Ctl) / 16;
  pp.wk = (const v4u*)A_Wk; pp.wv = (const v4u*)A_Wv; pp.ax = (const v4u*)A_x; pp.pfr = PFR; pp.far = 1; pp.qkv = (char*)qkvA;
  k_prep<<<PSENT, 512, 0, stream>>>(pp);
  FParams cp;
  cp.Ax = A_x; cp.Bx = B_x; cp.Wq = A_Wq; cp.Wk = A_Wk; cp.Wv = A_Wv; cp.Wo = A_Wo; cp.wqkvB = B_wqkv; cp.woB = B_wo;
  cp.qkvA = qkvA; cp.qkvB = qkvB; cp.kmem = kmem; cp.vbuf = (u16*)vmem; cp.obuf = (u16*)obuf; cp.membuf = (u16*)membuf; cp.par = par; cp.out = out;
  cp.part = part; cp.ctl = ctl; cp.prof = prof;
  cp.dq = DQ; cp.dq2 = DQ2; cp.dmv = DMV; cp.dov = DOV; cp.gain = GAIN; cp.inc = MINC; cp.dec = MDEC; cp.rec = RECOV; cp.stag = STAG; cp.sh1 = SH1; cp.sh2 = SH2; cp.sh3 = SH3; cp.dv = DVV; cp.pad = BOOT; cp.rot = ROT; cp.eps = EPSV; cp.abd = ABD; cp.boot2 = BOOT2; cp.boot3 = BOOT3; cp.im = IMARG; cp.gain0 = GAIN0; cp.pad4 = 0;
  k_fused<<<256, 256, 0, stream>>>(cp);
}
```

```cpp
#include <hip/hip_runtime.h>
#include <hip/hip_bf16.h>
#include <stdint.h>
#include <stddef.h>

#define DQ 14
#define DQ2 17
#define DMV 2500
#define DOV 1480
#define GAIN 4
#define GAIN0 8
#define MINC 32
#define MDEC 6
#define RECOV 22
#define STAG (1400 * 65536 + 1400)
#define SH1 14
#define SH2 45
#define SH3 0
#define DVV 3
#define BOOT 0x1932
#define ROT 1616
#define EPSV 0x020000
#define ABD 3
#define BOOT2 0x0505
#define BOOT3 0x0a14
#define IMARG 3
#define PFR 0
#define OJ1 48
#define VSLOT 60
typedef unsigned short u16;
typedef __attribute__((ext_vector_type(4))) float f32x4;
typedef unsigned v4u __attribute__((ext_vector_type(4)));

constexpr int S_ = 2048, DM = 1024, HA = 32, DHA = 128, HDA = 4096, MEM = 8, SEG = 32, NSEG = 64;
constexpr int QPAGE = 2 << 20, QBLK = 16384, QSIZE = 48 * QPAGE;
__device__ __forceinline__ int qoff(unsigned long long parq, int n) { return n * QPAGE + (int)((parq >> n) & 1) * 8192; }
constexpr int DB = 1024, HB = 32, DHB = 32, NQKVB = 3 * DB;

struct PrepParams {
  v4u* sent; unsigned long long sent_vec;
  v4u* ctl; const u16* mem0; char* obuf; char* membuf; char* vbuf; unsigned* par; unsigned long long ctl_vec;
  const v4u *wk, *wv, *ax; unsigned long long pfr, far; char* qkv;
};
#define PROBES 3
constexpr int PSENT = 256, OREG = 131072, MREG = 65536;
typedef __attribute__((ext_vector_type(8))) __bf16 bfx8;
typedef __attribute__((ext_vector_type(2))) __bf16 bfx2;
typedef __attribute__((ext_vector_type(2))) float f32x2;
typedef unsigned v2u __attribute__((ext_vector_type(2)));
typedef unsigned v8u __attribute__((ext_vector_type(8)));
typedef unsigned v32u __attribute__((ext_vector_type(32)));
constexpr int NQK = 32, NVO = 64;
constexpr int NLW = 15, VXLD = 144;
struct ChainLds {
  v4u sA[16 * 64];
  f32x4 xs[4][64];
  f32x4 ms[4][64];
  v4u kv[4][4][64];
  v4u sW[4][NLW][2][64];
};
constexpr int seq_i(int u) { return u < 16 ? u >> 2 : 4 + ((u - 16) % 24 >> 1); }
constexpr int seq_t(int u) { return u < 16 ? (u & 3) : (u < 40 ? (u & 1) : 2 + (u & 1)); }
constexpr bool seq_l(int u) { return u < 10 ? (u & 1) : (u == 15 || (u >= 17 && u <= 37 && (u & 3) == 1) || u == 40 || u == 42 || u == 44); }
constexpr int rd_a(int u) { return u >= 11 && u <= 33 && (u & 1) ? 4 + (u - 11) / 2 : 0; }
constexpr int rd_w(int u) { if (u < 8 || u > 39 || rd_a(u)) return -1; int n = 0; for (int v = 8; v < u; ++v) n += !rd_a(v); return n; }
constexpr int seq_c(int u) { if (seq_l(u)) return 2; int n = 0; for (int v = 0; v < u; ++v) n += !seq_l(v); return n < 32 - NLW ? 1 : 0; }
constexpr int seq_n(int u) { int n = 0; for (int v = 0; v < u; ++v) n += seq_c(v) == seq_c(u); return n; }
constexpr int seq_u(int i, int t) { for (int u = 0; u < 64; ++u) if (seq_i(u) == i && seq_t(u) == t) return u; return -1; }
template <int I> struct IC { static constexpr int v = I; };
template <int I, int N, class F> __device__ __forceinline__ void sfor(F&& f) { if constexpr (I < N) { f(IC<I>{}); sfor<I + 1, N>(f); } }
#define SFOR(u, a, b) sfor<a, b>([&](auto u##_) __attribute__((always_inline)) { constexpr int u = decltype(u##_)::v;
#define SEND });
constexpr int NPRE = 5;

__device__ __forceinline__ void lds_barrier() { asm volatile("s_waitcnt lgkmcnt(0)" ::: "memory"); __builtin_amdgcn_s_barrier(); asm volatile("" ::: "memory"); }
template <int N> __device__ __forceinline__ float dpp_ror(float v) {
  return __int_as_float(__builtin_amdgcn_update_dpp(0, __float_as_int(v), 0x120 + N, 0xf, 0xf, false));
}
__device__ __forceinline__ unsigned pk2(float a, float b) { f32x2 t = {a, b}; return __builtin_bit_cast(unsigned, __builtin_convertvector(t, bfx2)); }
__device__ __forceinline__ unsigned nosent(unsigned x) { return x < 0xfffffffeu ? x : 0xfffffffeu; }
__device__ __forceinline__ unsigned umax3(unsigned a, unsigned b, unsigned c) { unsigned m = a > b ? a : b; return m > c ? m : c; }
__device__ __forceinline__ unsigned chk4(unsigned m, v4u v) { return umax3(umax3(m, v.x, v.y), v.z, v.w); }
__device__ __forceinline__ unsigned long long now() { return __builtin_amdgcn_s_memtime(); }
__device__ __forceinline__ f32x4 mf(v4u a, v4u b, f32x4 c) {
  return __builtin_amdgcn_mfma_f32_16x16x32_bf16(__builtin_bit_cast(bfx8, a), __builtin_bit_cast(bfx8, b), c, 0, 0, 0);
}
__device__ __forceinline__ v2u trrd(const u16* p) {
  typedef __attribute__((ext_vector_type(4))) short v4i16_t;
  return __builtin_bit_cast(v2u, __builtin_amdgcn_ds_read_tr16_b64_v4i16((__attribute__((address_space(3))) v4i16_t*)p));
}
__device__ __forceinline__ v8u cat8(v4u a, v4u b) { return __builtin_shufflevector(a, b, 0, 1, 2, 3, 4, 5, 6, 7); }
__device__ __forceinline__ float xmax16(float x) { auto r = __builtin_amdgcn_permlane16_swap(__float_as_uint(x), __float_as_uint(x), false, false); return fmaxf(__uint_as_float(r[0]), __uint_as_float(r[1])); }
__device__ __forceinline__ float xmax32(float x) { auto r = __builtin_amdgcn_permlane32_swap(__float_as_uint(x), __float_as_uint(x), false, false); return fmaxf(__uint_as_float(r[0]), __uint_as_float(r[1])); }
__device__ __forceinline__ float xsum16(float x) { auto r = __builtin_amdgcn_permlane16_swap(__float_as_uint(x), __float_as_uint(x), false, false); return __uint_as_float(r[0]) + __uint_as_float(r[1]); }
__device__ __forceinline__ float xsum32(float x) { auto r = __builtin_amdgcn_permlane32_swap(__float_as_uint(x), __float_as_uint(x), false, false); return __uint_as_float(r[0]) + __uint_as_float(r[1]); }
__device__ __forceinline__ int dbl(int off) { return ((off >> 13) << 14) | (off & 8191); }
__device__ __forceinline__ bool spin_fail(unsigned spins, unsigned* tmo, unsigned code) {
  if ((spins & 31u) != 31u) return false;
  if (spins > (1u << 15)) { __hip_atomic_store(tmo, code, __ATOMIC_RELAXED, __HIP_MEMORY_SCOPE_AGENT); return true; }
  return __builtin_amdgcn_readfirstlane(__hip_atomic_load(tmo, __ATOMIC_RELAXED, __HIP_MEMORY_SCOPE_AGENT)) != 0u;
}
#define SB0 __builtin_amdgcn_sched_barrier(0)
typedef __attribute__((ext_vector_type(16))) float f32x16;
#define WARM32(d) asm volatile("v_mfma_f32_16x16x32_bf16 %0, v[8:11], v[8:11], 0\n\tv_mfma_f32_16x16x32_bf16 %0, v[8:11], v[8:11], 0" : "=v"(d) : "{v[8:15]}"(wb[0])); SB0
#define NTQ 0
#define NTV 0
#define NTO 0
#define NTA 0
#define NTB 0
#define QROT 1
#define RANKAL 0
#define WDK 0
#define PACEO 32
#define PACE_O { asm volatile("s_nop %0" :: "i"(PACEO / 4 - 2)); SB0; }
#define PACEV 0
#define PRET 32
#define EPS 20
#define XB 56
#define ALP 2
#define KF0 8
#define PF1 1
#define PSL 48
#define TSL 6
#define FSL 4
#define OUTAUX 16
#define LA0 4
#define LAX 12
#define NBL 16
#define NAL 8
#define HROT 16
#define EARLY 3
#define NWQ 4
#define PACEQ 1
#define NWV 0
#define NWO 0
__device__ __forceinline__ f32x4 zero4() { f32x2 a, b; asm volatile("v_mov_b64 %0, 0\n\tv_mov_b64 %1, 0" : "=v"(a), "=v"(b)); return (f32x4){a[0], a[1], b[0], b[1]}; }
__device__ __forceinline__ int opaque_lane() { int l; asm volatile("v_mbcnt_lo_u32_b32 %0, -1, 0\n\tv_mbcnt_hi_u32_b32 %0, -1, %0" : "=v"(l)); return l & 63; }

namespace wg {
constexpr int LDS_USE = 131072;
__device__ __forceinline__ void* uni(const void* p) { const unsigned long long x = (unsigned long long)p; return (void*)(((unsigned long long)(unsigned)__builtin_amdgcn_readfirstlane(x >> 32) << 32) | (unsigned)__builtin_amdgcn_readfirstlane(x)); }
template <int MT>
__device__ __forceinline__ void gemm_tile(const u16* A, int lda, const u16* B, int ldb, int nst  , void* C, int ldc, bool apack, bool cpack, const unsigned* qpar  , char* lds, int w, int eps, int rot = 0  ) {
  constexpr int MI = MT / 32, PA = MT / 64, NP = PA + 4;
  constexpr int A_B = MT * 64, ST = A_B + 16384;
  const int lane = opaque_lane();
  const int l15 = lane & 15, lq = lane >> 4, wm = w >> 1, wn = w & 1;
  lda = __builtin_amdgcn_readfirstlane(lda); ldb = __builtin_amdgcn_readfirstlane(ldb); ldc = __builtin_amdgcn_readfirstlane(ldc); nst = __builtin_amdgcn_readfirstlane(nst);
  f32x4 acc[8][MI];
#pragma unroll
  for (int i = 0; i < 8; ++i)
#pragma unroll
    for (int j = 0; j < MI; ++j) acc[i][j] = (f32x4){0.f, 0.f, 0.f, 0.f};
  const unsigned lb = (unsigned)(size_t)lds;
  auto fsw = [](int x) { return ((x ^ (x >> 1)) & 1) << 1 | (x >> 1); };
  unsigned ao[2], bo[2][8];
  ao[0] = lb + wm * (MT / 2) * 64 + (apack ? lane * 16 : l15 * 64 + ((lq ^ fsw(l15 >> 2)) << 4)); ao[1] = ao[0] + 2 * ST;
  { const int hh = (l15 >> 2) | ((lq & 1) << 2);
#pragma unroll
    for (int i = 0; i < 8; ++i) { bo[0][i] = lb + A_B + (8 * lq + (l15 >> 2)) * 512 + ((16 * wn + ((2 * i) ^ (2 * hh)) + ((l15 >> 1) & 1)) << 4) + (l15 & 1) * 8; bo[1][i] = bo[0][i] + 2 * ST; } }
  __amdgpu_buffer_rsrc_t ra = __builtin_amdgcn_make_buffer_rsrc(uni(A), 0, 0x7fffffff, 0x00020000), rb = __builtin_amdgcn_make_buffer_rsrc(uni(B), 0, 0x7fffffff, 0x00020000);
  const int va = (16 * PA * w * lda + (apack ? lane * 8 : (lane >> 2) * lda + (((lane & 3) ^ fsw(lane >> 4)) << 3))) * 2, ska = __builtin_amdgcn_readfirstlane(apack ? 1024 : 64);
  const int vb0 = ((8 * w + (lane >> 5)) * ldb + (((lane & 31) ^ ((lane >> 5) << 1) ^ ((w & 1) << 3)) << 3)) * 2, vb1 = vb0 ^ 64;
  const int sa = 32 * lda, sb = 4 * ldb, skb = 64 * ldb;
  const unsigned ma = lb + PA * w * 1024, mb = lb + A_B + w * 4096;
#define PIECE1(NT, base, imm, vo, rs, so) do { if constexpr (NT) asm volatile("s_add_u32 m0, %0, %1\n\ts_nop 0\n\tbuffer_load_dwordx4 %2, %3, %4 offen nt lds" :: "s"(base), "i"(imm), "v"(vo), "s"(rs), "s"(so) : "memory", "scc"); \
    else asm volatile("s_add_u32 m0, %0, %1\n\ts_nop 0\n\tbuffer_load_dwordx4 %2, %3, %4 offen lds" :: "s"(base), "i"(imm), "v"(vo), "s"(rs), "s"(so) : "memory", "scc"); } while (0)
#define PIECE(q, st) do { \
    if ((q) < PA) PIECE1((NTA & (MT / 64)) != 0, ma, (st) * ST + (q) * 1024, va, ra, ka + (q) * sa); \
    else PIECE1((NTB & (MT / 64)) != 0, mb, (st) * ST + ((q) - PA) * 1024, (((q) - PA) & 1) ? vb1 : vb0, rb, kb + ((q) - PA) * sb); } while (0)
#define RDA(f, j, st) asm volatile("ds_read_b128 %0, %1 offset:%2" : "=v"(fa[f][j]) : "v"(ao[(st) >> 1]), "i"(((st) & 1) * ST + (j) * 1024) : "memory")
#define RDB(f, i, h, st) asm volatile("ds_read_b64_tr_b16 %0, %1 offset:%2" : "=v"(fb[f][i][h]) : "v"(bo[(st) >> 1][i]), "i"(((st) & 1) * ST + (h) * 2048) : "memory")
#define TIE(f) do { _Pragma("unroll") for (int j = 0; j < MI; ++j) asm volatile("" : "+v"(fa[f][j])); \
    _Pragma("unroll") for (int i = 0; i < 8; ++i) asm volatile("" : "+v"(fb[f][i][0]), "+v"(fb[f][i][1])); } while (0)
  v4u fa[2][MI]; v2u fb[2][8][2];
  const unsigned qw = *qpar;
  const unsigned long long parq = apack ? __ballot(qpar[lane] != 0u) : 0ull;
  rot = MT == 64 ? __builtin_amdgcn_readfirstlane(rot) : 0;
  int ka = qoff(parq, 0) + rot * ska, kb = rot * skb;
  asm volatile("s_nop 4" :: "s"(ra), "s"(rb), "s"(sa), "s"(sb), "s"(skb), "s"(ska));
#pragma unroll
  for (int st = 0; st < 4; ++st) {
#pragma unroll
    for (int q = 0; q < NP; ++q) PIECE(q, st);
    ka += ska; kb += skb;
  }
  asm volatile("s_waitcnt vmcnt(%0)\n\ts_barrier" :: "i"(3 * NP) : "memory");
#pragma unroll
  for (int i = 0; i < 8; ++i) { RDB(0, i, 0, 0); RDB(0, i, 1, 0); }
#pragma unroll
  for (int j = 0; j < MI; ++j) RDA(0, j, 0);
  int k4 = 0;
  do {
    if (apack && (k4 & 4)) ka = qoff(parq, (k4 + 4) >> 3);
    if (MT == 64 && k4 + 4 + rot == nst) { ka = 0; kb = 0; }
    if (k4 + 4 >= nst) { ra = __builtin_amdgcn_make_buffer_rsrc(uni(C), 0, 0, 0x00020000); rb = ra; }
#pragma unroll
    for (int st = 0; st < 4; ++st) {
      constexpr int NM = 8 * MI, NR = 16 + MI;
      const int f = st & 1, sn = (st + 1) & 3;
      asm volatile("s_waitcnt vmcnt(%0) lgkmcnt(0)\n\ts_barrier" :: "i"(2 * NP) : "memory");
      TIE(f);
      SB0;
#pragma unroll
      for (int u = 0; u < NM; ++u) {
        const int j = u >> 3, i = u & 7;
        asm volatile("v_mfma_f32_16x16x32_bf16 %0, %1, %2, %0" : "+a"(acc[i][j]) : "v"((v4u){fb[f][i][0].x, fb[f][i][0].y, fb[f][i][1].x, fb[f][i][1].y}), "v"(fa[f][j]));
        SB0;
        constexpr int RS = MT == 256 ? 2 : 1;
        if (u % RS == 0 && u / RS < NR) {
          const int r = u / RS;
          if (r < 16) RDB(f ^ 1, r >> 1, r & 1, sn); else RDA(f ^ 1, r - 16, sn);
          if (MT == 64 && (u == 0 || u == 2)) RDA(f ^ 1, u >> 1, sn);
          SB0;
        }
        constexpr int PS = NM / 8;
        if (u % PS == PS - 1 && u / PS < NP) { PIECE(u / PS, st); SB0; }
      }
      ka += ska; kb += skb;
    }
    k4 += 4;
  } while (k4 < nst);
#undef PIECE
#undef PIECE1
#undef RDA
#undef RDB
  asm volatile("s_nop 7\n\ts_waitcnt lgkmcnt(0)" ::: "memory");
  SB0;
  TIE(0);
#undef TIE
  char* st = lds + LDS_USE + 2048 + w * 4096;
  const __amdgpu_buffer_rsrc_t rs = __builtin_amdgcn_make_buffer_rsrc((char*)uni(C) + __builtin_amdgcn_readfirstlane(cpack && qw ? 8192 : 0), 0, MT * ldc * 2 - (cpack ? 8192 : 0), 0x00020000);
  const int so = wm * (MT / 2) * ldc * 2 + (cpack ? wn * 4096 + lane * 16 : (lq * ldc + wn * 128 + l15 * 8) * 2), sq = cpack ? 1024 : 8 * ldc;
#pragma unroll
  for (int j = 0; j < MI; ++j) {
    v2u p[8]; v4u v[4];
#pragma unroll
    for (int i = 0; i < 8; ++i) { asm volatile("" : "+a"(acc[i][j])); p[i] = (v2u){pk2(acc[i][j][0], acc[i][j][1]), pk2(acc[i][j][2], acc[i][j][3])}; }
#pragma unroll
    for (int q = 0; q < 4; ++q) { v[q] = (v4u){p[2 * q].x, p[2 * q].y, p[2 * q + 1].x, p[2 * q + 1].y}; asm volatile("" : "+v"(v[q])); }
    if (!cpack) {
#pragma unroll
      for (int i = 0; i < 8; ++i) *(v2u*)(st + l15 * 256 + (((2 * i + (lq >> 1)) ^ l15) << 4) + (lq & 1) * 8) = (i & 1) ? (v2u){v[i >> 1].z, v[i >> 1].w} : (v2u){v[i >> 1].x, v[i >> 1].y};
      asm volatile("" ::: "memory");
#pragma unroll
      for (int q = 0; q < 4; ++q) v[q] = *(const v4u*)(st + (4 * q + lq) * 256 + ((l15 ^ (4 * q + lq)) << 4));
      asm volatile("" ::: "memory");
    }
#pragma unroll
    for (int q = 0; q < 4; ++q) {
      if (MT == 128 && OUTAUX != 16 && ldc == 2 * DM) __builtin_amdgcn_raw_buffer_store_b128(v[q], rs, so, 32 * j * ldc + q * sq, OUTAUX);
      else __builtin_amdgcn_raw_buffer_store_b128(v[q], rs, so, 32 * j * ldc + q * sq, 16);
    }
    for (int z = 0; z < eps; ++z) __builtin_amdgcn_s_sleep(4);
    SB0;
  }
  asm volatile("s_waitcnt vmcnt(0)" ::: "memory");
  __builtin_amdgcn_s_barrier();
}
}

struct Ctl {
  unsigned qhead[8][32];
  unsigned prg[96][32];
  unsigned cntB[16][32];
  unsigned cntAB[16][32];
  unsigned cntO[72][32];
  unsigned prog[32];
  unsigned tmo[32];
};
struct FParams {
  const u16 *Ax, *Bx, *Wq, *Wk, *Wv, *Wo, *wqkvB, *woB;
  u16 *qkvA, *qkvB, *kmem, *vbuf, *obuf, *membuf, *out;
  float* part; Ctl* ctl; unsigned long long* prof; const unsigned* par;
  int dq, dq2, dmv, dov, gain, inc, dec, rec, stag, sh1, sh2, sh3, dv, pad, rot, eps, abd, boot2, boot3, im, gain0, oj1;
};
#define RLX_AGENT __ATOMIC_RELAXED, __HIP_MEMORY_SCOPE_AGENT
__device__ __forceinline__ unsigned ld_agent(const unsigned* p) { return __hip_atomic_load(p, RLX_AGENT); }
__device__ __forceinline__ void wg_wait(const unsigned* p, unsigned need, Ctl* ctl, int w) {
  if (w == 0) {
    for (unsigned spins = 0;; ++spins) {
      const bool ok = (p == nullptr) || ld_agent(p) >= need;
      if (__all(ok)) break;
      if (spin_fail(spins, &ctl->tmo[0], 2u)) break;
      __builtin_amdgcn_s_sleep(16);
    }
    __builtin_amdgcn_fence(__ATOMIC_ACQUIRE, "agent");
    asm volatile("s_waitcnt vmcnt(0)" ::: "memory");
  }
  __syncthreads();
}

#define PN_BODY \
          "s_mov_b64 s[40:41], %[xa]\n\ts_mov_b32 s42, 0x400000\n\ts_mov_b32 s43, 0x20000\n\ts_nop 4\n\ts_add_u32 m0, %[wl], 65536\n\ts_add_u32 %[t], %[wo], 0x0\n\t" \
          "buffer_load_dwordx4 %[vw0], %[rw], %[t] offen" PN_NT " lds\n\ts_add_u32 m0, %[wl], 66560\n\ts_add_u32 %[t], %[wo], 0x8000\n\tbuffer_load_dwordx4 %[vw0], %[rw], %[t] offen" PN_NT " lds\n\ts_add_u32 m0, %[wl], 67584\n\ts_add_u32 %[t], %[wo], 0x10000\n\t" \
          "buffer_load_dwordx4 %[vw1], %[rw], %[t] offen" PN_NT " lds\n\ts_add_u32 m0, %[wl], 68608\n\ts_add_u32 %[t], %[wo], 0x18000\n\tbuffer_load_dwordx4 %[vw1], %[rw], %[t] offen" PN_NT " lds\n\ts_add_u32 m0, %[sl], 0\n\ts_nop 0\n\t" \
          "buffer_load_dwordx4 %[vd0], s[40:43], %[xo] offen lds\n\tbuffer_load_dwordx4 %[vd0], s[40:43], %[xo] offen offset:1024 lds\n\tbuffer_load_dwordx4 %[vd1], s[40:43], %[xo] offen offset:2048 lds\n\tbuffer_load_dwordx4 %[vd1], s[40:43], %[xo] offen offset:3072 lds\n\ts_add_u32 m0, %[sl], 4096\n\ts_nop 0\n\t" \
          "buffer_load_dwordx4 %[vd2], s[40:43], %[xo] offen lds\n\tbuffer_load_dwordx4 %[vd2], s[40:43], %[xo] offen offset:1024 lds\n\tbuffer_load_dwordx4 %[vd3], s[40:43], %[xo] offen offset:2048 lds\n\tbuffer_load_dwordx4 %[vd3], s[40:43], %[xo] offen offset:3072 lds\n\ts_add_u32 %[xo], %[xo], 0x8000\n\ts_add_u32 %[sl], %[sl], 0x8000\n\t" \
          "s_bitset0_b32 %[sl], 17\n\ts_add_u32 m0, %[wl], 81920\n\ts_add_u32 %[t], %[wo], 0x80000\n\tbuffer_load_dwordx4 %[vw0], %[rw], %[t] offen" PN_NT " lds\n\ts_add_u32 m0, %[wl], 82944\n\ts_add_u32 %[t], %[wo], 0x88000\n\t" \
          "buffer_load_dwordx4 %[vw0], %[rw], %[t] offen" PN_NT " lds\n\ts_add_u32 m0, %[wl], 83968\n\ts_add_u32 %[t], %[wo], 0x90000\n\tbuffer_load_dwordx4 %[vw1], %[rw], %[t] offen" PN_NT " lds\n\ts_add_u32 m0, %[wl], 84992\n\ts_add_u32 %[t], %[wo], 0x98000\n\t" \
          "buffer_load_dwordx4 %[vw1], %[rw], %[t] offen" PN_NT " lds\n\ts_add_u32 m0, %[sl], 0\n\ts_nop 0\n\tbuffer_load_dwordx4 %[vd0], s[40:43], %[xo] offen lds\n\tbuffer_load_dwordx4 %[vd0], s[40:43], %[xo] offen offset:1024 lds\n\tbuffer_load_dwordx4 %[vd1], s[40:43], %[xo] offen offset:2048 lds\n\t" \
          "buffer_load_dwordx4 %[vd1], s[40:43], %[xo] offen offset:3072 lds\n\ts_add_u32 m0, %[sl], 4096\n\ts_nop 0\n\tbuffer_load_dwordx4 %[vd2], s[40:43], %[xo] offen lds\n\tbuffer_load_dwordx4 %[vd2], s[40:43], %[xo] offen offset:1024 lds\n\tbuffer_load_dwordx4 %[vd3], s[40:43], %[xo] offen offset:2048 lds\n\t" \
          "buffer_load_dwordx4 %[vd3], s[40:43], %[xo] offen offset:3072 lds\n\ts_add_u32 %[xo], %[xo], 0x8000\n\ts_add_u32 %[sl], %[sl], 0x8000\n\ts_bitset0_b32 %[sl], 17\n\ts_add_u32 m0, %[wl], 98304\n\ts_add_u32 %[t], %[wo], 0x100000\n\t" \
          "buffer_load_dwordx4 %[vw0], %[rw], %[t] offen" PN_NT " lds\n\ts_add_u32 m0, %[wl], 99328\n\ts_add_u32 %[t], %[wo], 0x108000\n\tbuffer_load_dwordx4 %[vw0], %[rw], %[t] offen" PN_NT " lds\n\ts_add_u32 m0, %[wl], 100352\n\ts_add_u32 %[t], %[wo], 0x110000\n\t" \
          "buffer_load_dwordx4 %[vw1], %[rw], %[t] offen" PN_NT " lds\n\ts_add_u32 m0, %[wl], 101376\n\ts_add_u32 %[t], %[wo], 0x118000\n\tbuffer_load_dwordx4 %[vw1], %[rw], %[t] offen" PN_NT " lds\n\ts_add_u32 m0, %[wl], 114688\n\ts_add_u32 %[t], %[wo], 0x180000\n\t" \
          "buffer_load_dwordx4 %[vw0], %[rw], %[t] offen" PN_NT " lds\n\ts_add_u32 m0, %[wl], 115712\n\ts_add_u32 %[t], %[wo], 0x188000\n\tbuffer_load_dwordx4 %[vw0], %[rw], %[t] offen" PN_NT " lds\n\ts_add_u32 m0, %[wl], 116736\n\ts_add_u32 %[t], %[wo], 0x190000\n\t" \
          "buffer_load_dwordx4 %[vw1], %[rw], %[t] offen" PN_NT " lds\n\ts_add_u32 m0, %[wl], 117760\n\ts_add_u32 %[t], %[wo], 0x198000\n\tbuffer_load_dwordx4 %[vw1], %[rw], %[t] offen" PN_NT " lds\n\ts_add_u32 m0, %[wl], 131072\n\ts_add_u32 %[t], %[wo], 0x200000\n\t" \
          "buffer_load_dwordx4 %[vw0], %[rw], %[t] offen" PN_NT " lds\n\ts_add_u32 m0, %[wl], 132096\n\ts_add_u32 %[t], %[wo], 0x208000\n\tbuffer_load_dwordx4 %[vw0], %[rw], %[t] offen" PN_NT " lds\n\ts_add_u32 m0, %[wl], 133120\n\ts_add_u32 %[t], %[wo], 0x210000\n\t" \
          "buffer_load_dwordx4 %[vw1], %[rw], %[t] offen" PN_NT " lds\n\ts_add_u32 m0, %[wl], 134144\n\ts_add_u32 %[t], %[wo], 0x218000\n\tbuffer_load_dwordx4 %[vw1], %[rw], %[t] offen" PN_NT " lds\n\ts_waitcnt vmcnt(32) lgkmcnt(0)\n\ts_barrier\n\t" \
          "s_add_u32 m0, %[wl], 147456\n\ts_add_u32 %[t], %[wo], 0x280000\n\tbuffer_load_dwordx4 %[vw0], %[rw], %[t] offen" PN_NT " lds\n\ts_add_u32 m0, %[wl], 148480\n\ts_add_u32 %[t], %[wo], 0x288000\n\tbuffer_load_dwordx4 %[vw0], %[rw], %[t] offen" PN_NT " lds\n\t" \
          "s_add_u32 m0, %[wl], 149504\n\ts_add_u32 %[t], %[wo], 0x290000\n\tbuffer_load_dwordx4 %[vw1], %[rw], %[t] offen" PN_NT " lds\n\ts_add_u32 m0, %[wl], 150528\n\ts_add_u32 %[t], %[wo], 0x298000\n\tbuffer_load_dwordx4 %[vw1], %[rw], %[t] offen" PN_NT " lds\n\t" \
          "ds_read_b64_tr_b16 a[0:1], %[rt0] offset:0\n\tds_read_b64_tr_b16 a[2:3], %[rt0] offset:1024\n\tds_read_b64_tr_b16 a[4:5], %[rt1] offset:0\n\tds_read_b64_tr_b16 a[6:7], %[rt1] offset:1024\n\tds_read_b64_tr_b16 a[8:9], %[rt0] offset:8192\n\tds_read_b64_tr_b16 a[10:11], %[rt0] offset:9216\n\t" \
          "ds_read_b64_tr_b16 a[12:13], %[rt1] offset:8192\n\tds_read_b64_tr_b16 a[14:15], %[rt1] offset:9216\n\ts_waitcnt vmcnt(24) lgkmcnt(0)\n\ts_barrier\n\ts_add_u32 m0, %[wl], 65536\n\ts_add_u32 %[t], %[wo], 0x300000\n\t" \
          "buffer_load_dwordx4 %[vw0], %[rw], %[t] offen" PN_NT " lds\n\ts_add_u32 m0, %[wl], 66560\n\ts_add_u32 %[t], %[wo], 0x308000\n\tbuffer_load_dwordx4 %[vw0], %[rw], %[t] offen" PN_NT " lds\n\ts_add_u32 m0, %[wl], 67584\n\ts_add_u32 %[t], %[wo], 0x310000\n\t" \
          "buffer_load_dwordx4 %[vw1], %[rw], %[t] offen" PN_NT " lds\n\ts_add_u32 m0, %[wl], 68608\n\ts_add_u32 %[t], %[wo], 0x318000\n\tbuffer_load_dwordx4 %[vw1], %[rw], %[t] offen" PN_NT " lds\n\tds_read_b64_tr_b16 a[16:17], %[rt0] offset:16384\n\tds_read_b64_tr_b16 a[18:19], %[rt0] offset:17408\n\t" \
          "ds_read_b64_tr_b16 a[20:21], %[rt1] offset:16384\n\tds_read_b64_tr_b16 a[22:23], %[rt1] offset:17408\n\tds_read_b64_tr_b16 a[24:25], %[rt0] offset:24576\n\tds_read_b64_tr_b16 a[26:27], %[rt0] offset:25600\n\tds_read_b64_tr_b16 a[28:29], %[rt1] offset:24576\n\tds_read_b64_tr_b16 a[30:31], %[rt1] offset:25600\n\t" \
          "s_waitcnt vmcnt(16) lgkmcnt(0)\n\ts_barrier\n\ts_add_u32 m0, %[wl], 81920\n\ts_add_u32 %[t], %[wo], 0x380000\n\tbuffer_load_dwordx4 %[vw0], %[rw], %[t] offen" PN_NT " lds\n\ts_add_u32 m0, %[wl], 82944\n\t" \
          "s_add_u32 %[t], %[wo], 0x388000\n\tbuffer_load_dwordx4 %[vw0], %[rw], %[t] offen" PN_NT " lds\n\ts_add_u32 m0, %[wl], 83968\n\ts_add_u32 %[t], %[wo], 0x390000\n\tbuffer_load_dwordx4 %[vw1], %[rw], %[t] offen" PN_NT " lds\n\ts_add_u32 m0, %[wl], 84992\n\t" \
          "s_add_u32 %[t], %[wo], 0x398000\n\tbuffer_load_dwordx4 %[vw1], %[rw], %[t] offen" PN_NT " lds\n\tds_read_b64_tr_b16 a[32:33], %[rt0] offset:32768\n\tds_read_b64_tr_b16 a[34:35], %[rt0] offset:33792\n\tds_read_b64_tr_b16 a[36:37], %[rt1] offset:32768\n\tds_read_b64_tr_b16 a[38:39], %[rt1] offset:33792\n\t" \
          "ds_read_b64_tr_b16 a[40:41], %[rt0] offset:40960\n\tds_read_b64_tr_b16 a[42:43], %[rt0] offset:41984\n\tds_read_b64_tr_b16 a[44:45], %[rt1] offset:40960\n\tds_read_b64_tr_b16 a[46:47], %[rt1] offset:41984\n\ts_waitcnt vmcnt(16) lgkmcnt(0)\n\ts_barrier\n\t" \
          "s_add_u32 m0, %[wl], 98304\n\ts_add_u32 %[t], %[wo], 0x400000\n\tbuffer_load_dwordx4 %[vw0], %[rw], %[t] offen" PN_NT " lds\n\ts_add_u32 m0, %[wl], 99328\n\ts_add_u32 %[t], %[wo], 0x408000\n\tbuffer_load_dwordx4 %[vw0], %[rw], %[t] offen" PN_NT " lds\n\t" \
          "s_add_u32 m0, %[wl], 100352\n\ts_add_u32 %[t], %[wo], 0x410000\n\tbuffer_load_dwordx4 %[vw1], %[rw], %[t] offen" PN_NT " lds\n\ts_add_u32 m0, %[wl], 101376\n\ts_add_u32 %[t], %[wo], 0x418000\n\tbuffer_load_dwordx4 %[vw1], %[rw], %[t] offen" PN_NT " lds\n\t" \
          "ds_read_b64_tr_b16 a[48:49], %[rt2] offset:0\n\tds_read_b64_tr_b16 a[50:51], %[rt2] offset:1024\n\tds_read_b64_tr_b16 a[52:53], %[rt3] offset:0\n\tds_read_b64_tr_b16 a[54:55], %[rt3] offset:1024\n\tds_read_b64_tr_b16 a[56:57], %[rt2] offset:8192\n\tds_read_b64_tr_b16 a[58:59], %[rt2] offset:9216\n\t" \
          "ds_read_b64_tr_b16 a[60:61], %[rt3] offset:8192\n\tds_read_b64_tr_b16 a[62:63], %[rt3] offset:9216\n\ts_waitcnt vmcnt(16) lgkmcnt(0)\n\ts_barrier\n\ts_add_u32 m0, %[wl], 114688\n\ts_add_u32 %[t], %[wo], 0x480000\n\t" \
          "buffer_load_dwordx4 %[vw0], %[rw], %[t] offen" PN_NT " lds\n\ts_add_u32 m0, %[wl], 115712\n\ts_add_u32 %[t], %[wo], 0x488000\n\tbuffer_load_dwordx4 %[vw0], %[rw], %[t] offen" PN_NT " lds\n\ts_add_u32 m0, %[wl], 116736\n\ts_add_u32 %[t], %[wo], 0x490000\n\t" \
          "buffer_load_dwordx4 %[vw1], %[rw], %[t] offen" PN_NT " lds\n\ts_add_u32 m0, %[wl], 117760\n\ts_add_u32 %[t], %[wo], 0x498000\n\tbuffer_load_dwordx4 %[vw1], %[rw], %[t] offen" PN_NT " lds\n\tds_read_b64_tr_b16 a[64:65], %[rt2] offset:16384\n\tds_read_b64_tr_b16 a[66:67], %[rt2] offset:17408\n\t" \
          "ds_read_b64_tr_b16 a[68:69], %[rt3] offset:16384\n\tds_read_b64_tr_b16 a[70:71], %[rt3] offset:17408\n\tds_read_b64_tr_b16 a[72:73], %[rt2] offset:24576\n\tds_read_b64_tr_b16 a[74:75], %[rt2] offset:25600\n\tds_read_b64_tr_b16 a[76:77], %[rt3] offset:24576\n\tds_read_b64_tr_b16 a[78:79], %[rt3] offset:25600\n\t" \
          "s_waitcnt vmcnt(16) lgkmcnt(0)\n\ts_barrier\n\ts_add_u32 m0, %[wl], 131072\n\ts_add_u32 %[t], %[wo], 0x500000\n\tbuffer_load_dwordx4 %[vw0], %[rw], %[t] offen" PN_NT " lds\n\ts_add_u32 m0, %[wl], 132096\n\t" \
          "s_add_u32 %[t], %[wo], 0x508000\n\tbuffer_load_dwordx4 %[vw0], %[rw], %[t] offen" PN_NT " lds\n\ts_add_u32 m0, %[wl], 133120\n\ts_add_u32 %[t], %[wo], 0x510000\n\tbuffer_load_dwordx4 %[vw1], %[rw], %[t] offen" PN_NT " lds\n\ts_add_u32 m0, %[wl], 134144\n\t" \
          "s_add_u32 %[t], %[wo], 0x518000\n\tbuffer_load_dwordx4 %[vw1], %[rw], %[t] offen" PN_NT " lds\n\tds_read_b64_tr_b16 a[80:81], %[rt2] offset:32768\n\tds_read_b64_tr_b16 a[82:83], %[rt2] offset:33792\n\tds_read_b64_tr_b16 a[84:85], %[rt3] offset:32768\n\tds_read_b64_tr_b16 a[86:87], %[rt3] offset:33792\n\t" \
          "ds_read_b64_tr_b16 a[88:89], %[rt2] offset:40960\n\tds_read_b64_tr_b16 a[90:91], %[rt2] offset:41984\n\tds_read_b64_tr_b16 a[92:93], %[rt3] offset:40960\n\tds_read_b64_tr_b16 a[94:95], %[rt3] offset:41984\n\ts_waitcnt vmcnt(16) lgkmcnt(0)\n\ts_barrier\n\t" \
          "s_add_u32 m0, %[wl], 147456\n\ts_add_u32 %[t], %[wo], 0x580000\n\tbuffer_load_dwordx4 %[vw0], %[rw], %[t] offen" PN_NT " lds\n\ts_add_u32 m0, %[wl], 148480\n\ts_add_u32 %[t], %[wo], 0x588000\n\tbuffer_load_dwordx4 %[vw0], %[rw], %[t] offen" PN_NT " lds\n\t" \
          "s_add_u32 m0, %[wl], 149504\n\ts_add_u32 %[t], %[wo], 0x590000\n\tbuffer_load_dwordx4 %[vw1], %[rw], %[t] offen" PN_NT " lds\n\ts_add_u32 m0, %[wl], 150528\n\ts_add_u32 %[t], %[wo], 0x598000\n\tbuffer_load_dwordx4 %[vw1], %[rw], %[t] offen" PN_NT " lds\n\t" \
          "ds_read_b64_tr_b16 a[96:97], %[rt0] offset:0\n\tds_read_b64_tr_b16 a[98:99], %[rt0] offset:1024\n\tds_read_b64_tr_b16 a[100:101], %[rt1] offset:0\n\tds_read_b64_tr_b16 a[102:103], %[rt1] offset:1024\n\tds_read_b64_tr_b16 a[104:105], %[rt0] offset:8192\n\tds_read_b64_tr_b16 a[106:107], %[rt0] offset:9216\n\t" \
          "ds_read_b64_tr_b16 a[108:109], %[rt1] offset:8192\n\tds_read_b64_tr_b16 a[110:111], %[rt1] offset:9216\n\ts_waitcnt vmcnt(16) lgkmcnt(0)\n\ts_barrier\n\ts_add_u32 m0, %[wl], 65536\n\ts_add_u32 %[t], %[wo], 0x600000\n\t" \
          "buffer_load_dwordx4 %[vw0], %[rw], %[t] offen" PN_NT " lds\n\ts_add_u32 m0, %[wl], 66560\n\ts_add_u32 %[t], %[wo], 0x608000\n\tbuffer_load_dwordx4 %[vw0], %[rw], %[t] offen" PN_NT " lds\n\ts_add_u32 m0, %[wl], 67584\n\ts_add_u32 %[t], %[wo], 0x610000\n\t" \
          "buffer_load_dwordx4 %[vw1], %[rw], %[t] offen" PN_NT " lds\n\ts_add_u32 m0, %[wl], 68608\n\ts_add_u32 %[t], %[wo], 0x618000\n\tbuffer_load_dwordx4 %[vw1], %[rw], %[t] offen" PN_NT " lds\n\tds_read_b64_tr_b16 a[112:113], %[rt0] offset:16384\n\tds_read_b64_tr_b16 a[114:115], %[rt0] offset:17408\n\t" \
          "ds_read_b64_tr_b16 a[116:117], %[rt1] offset:16384\n\tds_read_b64_tr_b16 a[118:119], %[rt1] offset:17408\n\tds_read_b64_tr_b16 a[120:121], %[rt0] offset:24576\n\tds_read_b64_tr_b16 a[122:123], %[rt0] offset:25600\n\tds_read_b64_tr_b16 a[124:125], %[rt1] offset:24576\n\tds_read_b64_tr_b16 a[126:127], %[rt1] offset:25600\n\t" \
          "s_waitcnt vmcnt(16) lgkmcnt(0)\n\ts_barrier\n\ts_add_u32 m0, %[wl], 81920\n\ts_add_u32 %[t], %[wo], 0x680000\n\tbuffer_load_dwordx4 %[vw0], %[rw], %[t] offen" PN_NT " lds\n\ts_add_u32 m0, %[wl], 82944\n\t" \
          "s_add_u32 %[t], %[wo], 0x688000\n\tbuffer_load_dwordx4 %[vw0], %[rw], %[t] offen" PN_NT " lds\n\ts_add_u32 m0, %[wl], 83968\n\ts_add_u32 %[t], %[wo], 0x690000\n\tbuffer_load_dwordx4 %[vw1], %[rw], %[t] offen" PN_NT " lds\n\ts_add_u32 m0, %[wl], 84992\n\t" \
          "s_add_u32 %[t], %[wo], 0x698000\n\tbuffer_load_dwordx4 %[vw1], %[rw], %[t] offen" PN_NT " lds\n\tds_read_b64_tr_b16 a[128:129], %[rt0] offset:32768\n\tds_read_b64_tr_b16 a[130:131], %[rt0] offset:33792\n\tds_read_b64_tr_b16 a[132:133], %[rt1] offset:32768\n\tds_read_b64_tr_b16 a[134:135], %[rt1] offset:33792\n\t" \
          "ds_read_b64_tr_b16 a[136:137], %[rt0] offset:40960\n\tds_read_b64_tr_b16 a[138:139], %[rt0] offset:41984\n\tds_read_b64_tr_b16 a[140:141], %[rt1] offset:40960\n\tds_read_b64_tr_b16 a[142:143], %[rt1] offset:41984\n\ts_waitcnt vmcnt(16) lgkmcnt(0)\n\ts_barrier\n\t" \
          "s_add_u32 m0, %[wl], 98304\n\ts_add_u32 %[t], %[wo], 0x700000\n\tbuffer_load_dwordx4 %[vw0], %[rw], %[t] offen" PN_NT " lds\n\ts_add_u32 m0, %[wl], 99328\n\ts_add_u32 %[t], %[wo], 0x708000\n\tbuffer_load_dwordx4 %[vw0], %[rw], %[t] offen" PN_NT " lds\n\t" \
          "s_add_u32 m0, %[wl], 100352\n\ts_add_u32 %[t], %[wo], 0x710000\n\tbuffer_load_dwordx4 %[vw1], %[rw], %[t] offen" PN_NT " lds\n\ts_add_u32 m0, %[wl], 101376\n\ts_add_u32 %[t], %[wo], 0x718000\n\tbuffer_load_dwordx4 %[vw1], %[rw], %[t] offen" PN_NT " lds\n\t" \
          "ds_read_b64_tr_b16 a[144:145], %[rt2] offset:0\n\tds_read_b64_tr_b16 a[146:147], %[rt2] offset:1024\n\tds_read_b64_tr_b16 a[148:149], %[rt3] offset:0\n\tds_read_b64_tr_b16 a[150:151], %[rt3] offset:1024\n\tds_read_b64_tr_b16 a[152:153], %[rt2] offset:8192\n\tds_read_b64_tr_b16 a[154:155], %[rt2] offset:9216\n\t" \
          "ds_read_b64_tr_b16 a[156:157], %[rt3] offset:8192\n\tds_read_b64_tr_b16 a[158:159], %[rt3] offset:9216\n\ts_waitcnt vmcnt(16) lgkmcnt(0)\n\ts_barrier\n\ts_add_u32 m0, %[wl], 114688\n\ts_add_u32 %[t], %[wo], 0x780000\n\t" \
          "buffer_load_dwordx4 %[vw0], %[rw], %[t] offen" PN_NT " lds\n\ts_add_u32 m0, %[wl], 115712\n\ts_add_u32 %[t], %[wo], 0x788000\n\tbuffer_load_dwordx4 %[vw0], %[rw], %[t] offen" PN_NT " lds\n\ts_add_u32 m0, %[wl], 116736\n\ts_add_u32 %[t], %[wo], 0x790000\n\t" \
          "buffer_load_dwordx4 %[vw1], %[rw], %[t] offen" PN_NT " lds\n\ts_add_u32 m0, %[wl], 117760\n\ts_add_u32 %[t], %[wo], 0x798000\n\tbuffer_load_dwordx4 %[vw1], %[rw], %[t] offen" PN_NT " lds\n\tds_read_b64_tr_b16 a[160:161], %[rt2] offset:16384\n\tds_read_b64_tr_b16 a[162:163], %[rt2] offset:17408\n\t" \
          "ds_read_b64_tr_b16 a[164:165], %[rt3] offset:16384\n\tds_read_b64_tr_b16 a[166:167], %[rt3] offset:17408\n\tds_read_b64_tr_b16 a[168:169], %[rt2] offset:24576\n\tds_read_b64_tr_b16 a[170:171], %[rt2] offset:25600\n\tds_read_b64_tr_b16 a[172:173], %[rt3] offset:24576\n\tds_read_b64_tr_b16 a[174:175], %[rt3] offset:25600\n\t" \
          "s_waitcnt vmcnt(16) lgkmcnt(0)\n\ts_barrier\n\tds_read_b64_tr_b16 a[176:177], %[rt2] offset:32768\n\tds_read_b64_tr_b16 a[178:179], %[rt2] offset:33792\n\tds_read_b64_tr_b16 a[180:181], %[rt3] offset:32768\n\tds_read_b64_tr_b16 a[182:183], %[rt3] offset:33792\n\t" \
          "ds_read_b64_tr_b16 a[184:185], %[rt2] offset:40960\n\tds_read_b64_tr_b16 a[186:187], %[rt2] offset:41984\n\tds_read_b64_tr_b16 a[188:189], %[rt3] offset:40960\n\tds_read_b64_tr_b16 a[190:191], %[rt3] offset:41984\n\ts_waitcnt vmcnt(12) lgkmcnt(0)\n\ts_barrier\n\t" \
          "ds_read_b64_tr_b16 a[192:193], %[rt0] offset:0\n\tds_read_b64_tr_b16 a[194:195], %[rt0] offset:1024\n\tds_read_b64_tr_b16 a[196:197], %[rt1] offset:0\n\tds_read_b64_tr_b16 a[198:199], %[rt1] offset:1024\n\tds_read_b64_tr_b16 a[200:201], %[rt0] offset:8192\n\tds_read_b64_tr_b16 a[202:203], %[rt0] offset:9216\n\t" \
          "ds_read_b64_tr_b16 a[204:205], %[rt1] offset:8192\n\tds_read_b64_tr_b16 a[206:207], %[rt1] offset:9216\n\ts_waitcnt vmcnt(8) lgkmcnt(0)\n\ts_barrier\n\tds_read_b64_tr_b16 a[208:209], %[rt0] offset:16384\n\tds_read_b64_tr_b16 a[210:211], %[rt0] offset:17408\n\t" \
          "ds_read_b64_tr_b16 a[212:213], %[rt1] offset:16384\n\tds_read_b64_tr_b16 a[214:215], %[rt1] offset:17408\n\tds_read_b64_tr_b16 a[216:217], %[rt0] offset:24576\n\tds_read_b64_tr_b16 a[218:219], %[rt0] offset:25600\n\tds_read_b64_tr_b16 a[220:221], %[rt1] offset:24576\n\tds_read_b64_tr_b16 a[222:223], %[rt1] offset:25600\n\t" \
          "s_waitcnt vmcnt(4) lgkmcnt(0)\n\ts_barrier\n\tds_read_b64_tr_b16 a[224:225], %[rt0] offset:32768\n\tds_read_b64_tr_b16 a[226:227], %[rt0] offset:33792\n\tds_read_b64_tr_b16 a[228:229], %[rt1] offset:32768\n\tds_read_b64_tr_b16 a[230:231], %[rt1] offset:33792\n\t" \
          "ds_read_b64_tr_b16 a[232:233], %[rt0] offset:40960\n\tds_read_b64_tr_b16 a[234:235], %[rt0] offset:41984\n\tds_read_b64_tr_b16 a[236:237], %[rt1] offset:40960\n\tds_read_b64_tr_b16 a[238:239], %[rt1] offset:41984\n\ts_waitcnt vmcnt(0) lgkmcnt(0)\n\ts_barrier\n\t" \
          "ds_read_b64_tr_b16 a[240:241], %[rt2] offset:0\n\tds_read_b64_tr_b16 a[242:243], %[rt2] offset:1024\n\tds_read_b64_tr_b16 a[244:245], %[rt3] offset:0\n\tds_read_b64_tr_b16 a[246:247], %[rt3] offset:1024\n\tds_read_b64_tr_b16 a[248:249], %[rt2] offset:8192\n\tds_read_b64_tr_b16 a[250:251], %[rt2] offset:9216\n\t" \
          "ds_read_b64_tr_b16 a[252:253], %[rt3] offset:8192\n\tds_read_b64_tr_b16 a[254:255], %[rt3] offset:9216\n\ts_waitcnt lgkmcnt(0)\n\ts_barrier\n\ts_add_u32 m0, %[sl], 0\n\ts_nop 0\n\t" \
          "buffer_load_dwordx4 %[vd0], s[40:43], %[xo] offen lds\n\tbuffer_load_dwordx4 %[vd0], s[40:43], %[xo] offen offset:1024 lds\n\tbuffer_load_dwordx4 %[vd1], s[40:43], %[xo] offen offset:2048 lds\n\tbuffer_load_dwordx4 %[vd1], s[40:43], %[xo] offen offset:3072 lds\n\ts_add_u32 m0, %[sl], 4096\n\ts_nop 0\n\t" \
          "buffer_load_dwordx4 %[vd2], s[40:43], %[xo] offen lds\n\tbuffer_load_dwordx4 %[vd2], s[40:43], %[xo] offen offset:1024 lds\n\tbuffer_load_dwordx4 %[vd3], s[40:43], %[xo] offen offset:2048 lds\n\tbuffer_load_dwordx4 %[vd3], s[40:43], %[xo] offen offset:3072 lds\n\ts_add_u32 %[xo], %[xo], 0x8000\n\ts_add_u32 %[sl], %[sl], 0x8000\n\t" \
          "s_bitset0_b32 %[sl], 17\n\tds_read_b128 v[64:67], %[ra0] offset:0\n\tds_read_b128 v[68:71], %[ra1] offset:0\n\tds_read_b128 v[72:75], %[ra2] offset:0\n\tds_read_b128 v[76:79], %[ra3] offset:0\n\tds_read_b128 v[80:83], %[ra0] offset:256\n\t" \
          "ds_read_b128 v[84:87], %[ra1] offset:256\n\tds_read_b128 v[88:91], %[ra2] offset:256\n\tds_read_b128 v[92:95], %[ra3] offset:256\n\ts_mov_b32 %[j], 0\n\ts_mov_b64 %[sx], 0\n\tLpn_loop%=:\n\t" \
          "s_waitcnt lgkmcnt(6)\n\tv_mfma_f32_16x16x32_bf16 v[48:51], a[0:3], v[64:67], 0\n\tds_read_b128 v[96:99], %[ra0] offset:512\n\tv_mfma_f32_16x16x32_bf16 v[52:55], a[4:7], v[64:67], 0\n\ts_cmp_lt_u32 %[j], 125\n\ts_cselect_b32 s42, s42, 0\n\t" \
          "v_mfma_f32_16x16x32_bf16 v[48:51], a[8:11], v[68:71], v[48:51]\n\tds_read_b128 v[100:103], %[ra1] offset:512\n\tv_mfma_f32_16x16x32_bf16 v[52:55], a[12:15], v[68:71], v[52:55]\n\tv_cvt_pk_bf16_f32 v44, v56, v57\n\tv_cvt_pk_bf16_f32 v45, v58, v59\n\ts_waitcnt lgkmcnt(6)\n\t" \
          "v_mfma_f32_16x16x32_bf16 v[48:51], a[16:19], v[72:75], v[48:51]\n\tds_read_b128 v[104:107], %[ra2] offset:512\n\tv_mfma_f32_16x16x32_bf16 v[52:55], a[20:23], v[72:75], v[52:55]\n\tv_cvt_pk_bf16_f32 v46, v60, v61\n\tv_cvt_pk_bf16_f32 v47, v62, v63\n\tv_mfma_f32_16x16x32_bf16 v[48:51], a[24:27], v[76:79], v[48:51]\n\t" \
          "ds_read_b128 v[108:111], %[ra3] offset:512\n\tv_mfma_f32_16x16x32_bf16 v[52:55], a[28:31], v[76:79], v[52:55]\n\ts_mov_b64 exec, %[sx]\n\tbuffer_store_dwordx4 v[44:47], %[so], %[rc], %[co] offen sc1\n\ts_mov_b64 exec, -1\n\ts_waitcnt lgkmcnt(6)\n\t" \
          "v_mfma_f32_16x16x32_bf16 v[48:51], a[32:35], v[80:83], v[48:51]\n\tds_read_b128 v[112:115], %[ra0] offset:768\n\tv_mfma_f32_16x16x32_bf16 v[52:55], a[36:39], v[80:83], v[52:55]\n\ts_mov_b64 exec, %[fx]\n\tbuffer_store_dword %[fv], off, %[rf], %[fo] sc1\n\ts_mov_b64 exec, -1\n\t" \
          "v_mfma_f32_16x16x32_bf16 v[48:51], a[40:43], v[84:87], v[48:51]\n\tds_read_b128 v[116:119], %[ra1] offset:768\n\tv_mfma_f32_16x16x32_bf16 v[52:55], a[44:47], v[84:87], v[52:55]\n\ts_add_u32 %[co], %[co], %[cs]\n\ts_mov_b32 %[cs], 0x4000\n\ts_mov_b64 %[sx], -1\n\t" \
          "s_waitcnt lgkmcnt(6)\n\tv_mfma_f32_16x16x32_bf16 v[48:51], a[48:51], v[88:91], v[48:51]\n\tds_read_b128 v[120:123], %[ra2] offset:768\n\ts_add_u32 m0, %[sl], 0\n\tv_mfma_f32_16x16x32_bf16 v[52:55], a[52:55], v[88:91], v[52:55]\n\tbuffer_load_dwordx4 %[vd0], s[40:43], %[xo] offen lds\n\t" \
          "v_mfma_f32_16x16x32_bf16 v[48:51], a[56:59], v[92:95], v[48:51]\n\tds_read_b128 v[124:127], %[ra3] offset:768\n\tv_mfma_f32_16x16x32_bf16 v[52:55], a[60:63], v[92:95], v[52:55]\n\ts_waitcnt lgkmcnt(6)\n\tv_mfma_f32_16x16x32_bf16 v[48:51], a[64:67], v[96:99], v[48:51]\n\tds_read_b128 v[64:67], %[ra0] offset:1024\n\t" \
          "v_mfma_f32_16x16x32_bf16 v[52:55], a[68:71], v[96:99], v[52:55]\n\tbuffer_load_dwordx4 %[vd0], s[40:43], %[xo] offen offset:1024 lds\n\tv_mfma_f32_16x16x32_bf16 v[48:51], a[72:75], v[100:103], v[48:51]\n\tds_read_b128 v[68:71], %[ra1] offset:1024\n\tv_mfma_f32_16x16x32_bf16 v[52:55], a[76:79], v[100:103], v[52:55]\n\ts_waitcnt lgkmcnt(6)\n\t" \
          "v_mfma_f32_16x16x32_bf16 v[48:51], a[80:83], v[104:107], v[48:51]\n\tds_read_b128 v[72:75], %[ra2] offset:1024\n\tv_mfma_f32_16x16x32_bf16 v[52:55], a[84:87], v[104:107], v[52:55]\n\tbuffer_load_dwordx4 %[vd1], s[40:43], %[xo] offen offset:2048 lds\n\tv_mfma_f32_16x16x32_bf16 v[48:51], a[88:91], v[108:111], v[48:51]\n\tds_read_b128 v[76:79], %[ra3] offset:1024\n\t" \
          "v_mfma_f32_16x16x32_bf16 v[52:55], a[92:95], v[108:111], v[52:55]\n\ts_waitcnt lgkmcnt(6)\n\tv_mfma_f32_16x16x32_bf16 v[48:51], a[96:99], v[112:115], v[48:51]\n\tds_read_b128 v[80:83], %[ra0] offset:1280\n\tv_mfma_f32_16x16x32_bf16 v[52:55], a[100:103], v[112:115], v[52:55]\n\tbuffer_load_dwordx4 %[vd1], s[40:43], %[xo] offen offset:3072 lds\n\t" \
          "v_mfma_f32_16x16x32_bf16 v[48:51], a[104:107], v[116:119], v[48:51]\n\tds_read_b128 v[84:87], %[ra1] offset:1280\n\tv_mfma_f32_16x16x32_bf16 v[52:55], a[108:111], v[116:119], v[52:55]\n\ts_waitcnt lgkmcnt(6)\n\tv_mfma_f32_16x16x32_bf16 v[48:51], a[112:115], v[120:123], v[48:51]\n\tds_read_b128 v[88:91], %[ra2] offset:1280\n\t" \
          "s_add_u32 m0, %[sl], 4096\n\tv_mfma_f32_16x16x32_bf16 v[52:55], a[116:119], v[120:123], v[52:55]\n\tbuffer_load_dwordx4 %[vd2], s[40:43], %[xo] offen lds\n\tv_mfma_f32_16x16x32_bf16 v[48:51], a[120:123], v[124:127], v[48:51]\n\tds_read_b128 v[92:95], %[ra3] offset:1280\n\tv_mfma_f32_16x16x32_bf16 v[52:55], a[124:127], v[124:127], v[52:55]\n\t" \
          "s_waitcnt lgkmcnt(6)\n\tv_mfma_f32_16x16x32_bf16 v[48:51], a[128:131], v[64:67], v[48:51]\n\tds_read_b128 v[96:99], %[ra0] offset:1536\n\tv_mfma_f32_16x16x32_bf16 v[52:55], a[132:135], v[64:67], v[52:55]\n\tbuffer_load_dwordx4 %[vd2], s[40:43], %[xo] offen offset:1024 lds\n\tv_mfma_f32_16x16x32_bf16 v[48:51], a[136:139], v[68:71], v[48:51]\n\t" \
          "ds_read_b128 v[100:103], %[ra1] offset:1536\n\tv_mfma_f32_16x16x32_bf16 v[52:55], a[140:143], v[68:71], v[52:55]\n\ts_waitcnt lgkmcnt(6)\n\tv_mfma_f32_16x16x32_bf16 v[48:51], a[144:147], v[72:75], v[48:51]\n\tds_read_b128 v[104:107], %[ra2] offset:1536\n\tv_mfma_f32_16x16x32_bf16 v[52:55], a[148:151], v[72:75], v[52:55]\n\t" \
          "buffer_load_dwordx4 %[vd3], s[40:43], %[xo] offen offset:2048 lds\n\tv_mfma_f32_16x16x32_bf16 v[48:51], a[152:155], v[76:79], v[48:51]\n\tds_read_b128 v[108:111], %[ra3] offset:1536\n\tv_mfma_f32_16x16x32_bf16 v[52:55], a[156:159], v[76:79], v[52:55]\n\ts_waitcnt lgkmcnt(6)\n\tv_mfma_f32_16x16x32_bf16 v[48:51], a[160:163], v[80:83], v[48:51]\n\t" \
          "ds_read_b128 v[112:115], %[ra0] offset:1792\n\tv_mfma_f32_16x16x32_bf16 v[52:55], a[164:167], v[80:83], v[52:55]\n\tbuffer_load_dwordx4 %[vd3], s[40:43], %[xo] offen offset:3072 lds\n\tv_mfma_f32_16x16x32_bf16 v[48:51], a[168:171], v[84:87], v[48:51]\n\tds_read_b128 v[116:119], %[ra1] offset:1792\n\tv_mfma_f32_16x16x32_bf16 v[52:55], a[172:175], v[84:87], v[52:55]\n\t" \
          "s_waitcnt lgkmcnt(6)\n\tv_mfma_f32_16x16x32_bf16 v[48:51], a[176:179], v[88:91], v[48:51]\n\tds_read_b128 v[120:123], %[ra2] offset:1792\n\tv_mfma_f32_16x16x32_bf16 v[52:55], a[180:183], v[88:91], v[52:55]\n\tv_mfma_f32_16x16x32_bf16 v[48:51], a[184:187], v[92:95], v[48:51]\n\tds_read_b128 v[124:127], %[ra3] offset:1792\n\t" \
          "v_mfma_f32_16x16x32_bf16 v[52:55], a[188:191], v[92:95], v[52:55]\n\ts_add_u32 %[xo], %[xo], 0x8000\n\ts_add_u32 %[sl], %[sl], 0x8000\n\ts_bitset0_b32 %[sl], 17\n\ts_waitcnt lgkmcnt(6)\n\tv_mfma_f32_16x16x32_bf16 v[48:51], a[192:195], v[96:99], v[48:51]\n\t" \
          "v_mfma_f32_16x16x32_bf16 v[52:55], a[196:199], v[96:99], v[52:55]\n\tv_mfma_f32_16x16x32_bf16 v[48:51], a[200:203], v[100:103], v[48:51]\n\tv_mfma_f32_16x16x32_bf16 v[52:55], a[204:207], v[100:103], v[52:55]\n\ts_waitcnt lgkmcnt(4)\n\tv_mfma_f32_16x16x32_bf16 v[48:51], a[208:211], v[104:107], v[48:51]\n\tv_mfma_f32_16x16x32_bf16 v[52:55], a[212:215], v[104:107], v[52:55]\n\t" \
          "v_mfma_f32_16x16x32_bf16 v[48:51], a[216:219], v[108:111], v[48:51]\n\tv_mfma_f32_16x16x32_bf16 v[52:55], a[220:223], v[108:111], v[52:55]\n\ts_waitcnt vmcnt(18) lgkmcnt(0)\n\ts_barrier\n\tv_mfma_f32_16x16x32_bf16 v[48:51], a[224:227], v[112:115], v[48:51]\n\tds_read_b128 v[64:67], %[ra0] offset:32768\n\t" \
          "v_mfma_f32_16x16x32_bf16 v[52:55], a[228:231], v[112:115], v[52:55]\n\tds_read_b128 v[68:71], %[ra1] offset:32768\n\tv_mfma_f32_16x16x32_bf16 v[48:51], a[232:235], v[116:119], v[48:51]\n\tds_read_b128 v[72:75], %[ra2] offset:32768\n\tv_mfma_f32_16x16x32_bf16 v[52:55], a[236:239], v[116:119], v[52:55]\n\tds_read_b128 v[76:79], %[ra3] offset:32768\n\t" \
          "v_mfma_f32_16x16x32_bf16 v[48:51], a[240:243], v[120:123], v[48:51]\n\tds_read_b128 v[80:83], %[ra0] offset:33024\n\tv_mfma_f32_16x16x32_bf16 v[52:55], a[244:247], v[120:123], v[52:55]\n\tds_read_b128 v[84:87], %[ra1] offset:33024\n\tv_mfma_f32_16x16x32_bf16 v[48:51], a[248:251], v[124:127], v[48:51]\n\tds_read_b128 v[88:91], %[ra2] offset:33024\n\t" \
          "v_mfma_f32_16x16x32_bf16 v[52:55], a[252:255], v[124:127], v[52:55]\n\tds_read_b128 v[92:95], %[ra3] offset:33024\n\ts_waitcnt lgkmcnt(6)\n\tv_mfma_f32_16x16x32_bf16 v[56:59], a[0:3], v[64:67], 0\n\tds_read_b128 v[96:99], %[ra0] offset:33280\n\tv_mfma_f32_16x16x32_bf16 v[60:63], a[4:7], v[64:67], 0\n\t" \
          "s_cmp_lt_u32 %[j], 124\n\ts_cselect_b32 s42, s42, 0\n\tv_mfma_f32_16x16x32_bf16 v[56:59], a[8:11], v[68:71], v[56:59]\n\tds_read_b128 v[100:103], %[ra1] offset:33280\n\tv_mfma_f32_16x16x32_bf16 v[60:63], a[12:15], v[68:71], v[60:63]\n\tv_cvt_pk_bf16_f32 v44, v48, v49\n\t" \
          "v_cvt_pk_bf16_f32 v45, v50, v51\n\ts_waitcnt lgkmcnt(6)\n\tv_mfma_f32_16x16x32_bf16 v[56:59], a[16:19], v[72:75], v[56:59]\n\tds_read_b128 v[104:107], %[ra2] offset:33280\n\tv_mfma_f32_16x16x32_bf16 v[60:63], a[20:23], v[72:75], v[60:63]\n\tv_cvt_pk_bf16_f32 v46, v52, v53\n\t" \
          "v_cvt_pk_bf16_f32 v47, v54, v55\n\tv_mfma_f32_16x16x32_bf16 v[56:59], a[24:27], v[76:79], v[56:59]\n\tds_read_b128 v[108:111], %[ra3] offset:33280\n\tv_mfma_f32_16x16x32_bf16 v[60:63], a[28:31], v[76:79], v[60:63]\n\tbuffer_store_dwordx4 v[44:47], %[so], %[rc], %[co] offen sc1\n\ts_waitcnt lgkmcnt(6)\n\t" \
          "v_mfma_f32_16x16x32_bf16 v[56:59], a[32:35], v[80:83], v[56:59]\n\tds_read_b128 v[112:115], %[ra0] offset:33536\n\tv_mfma_f32_16x16x32_bf16 v[60:63], a[36:39], v[80:83], v[60:63]\n\tv_mov_b32 %[fv], %[j]\n\tv_mfma_f32_16x16x32_bf16 v[56:59], a[40:43], v[84:87], v[56:59]\n\tds_read_b128 v[116:119], %[ra1] offset:33536\n\t" \
          "v_mfma_f32_16x16x32_bf16 v[60:63], a[44:47], v[84:87], v[60:63]\n\ts_add_u32 %[co], %[co], 0x4000\n\ts_waitcnt lgkmcnt(6)\n\tv_mfma_f32_16x16x32_bf16 v[56:59], a[48:51], v[88:91], v[56:59]\n\tds_read_b128 v[120:123], %[ra2] offset:33536\n\ts_add_u32 m0, %[sl], 0\n\t" \
          "v_mfma_f32_16x16x32_bf16 v[60:63], a[52:55], v[88:91], v[60:63]\n\tbuffer_load_dwordx4 %[vd0], s[40:43], %[xo] offen lds\n\tv_mfma_f32_16x16x32_bf16 v[56:59], a[56:59], v[92:95], v[56:59]\n\tds_read_b128 v[124:127], %[ra3] offset:33536\n\tv_mfma_f32_16x16x32_bf16 v[60:63], a[60:63], v[92:95], v[60:63]\n\ts_waitcnt lgkmcnt(6)\n\t" \
          "v_mfma_f32_16x16x32_bf16 v[56:59], a[64:67], v[96:99], v[56:59]\n\tds_read_b128 v[64:67], %[ra0] offset:33792\n\tv_mfma_f32_16x16x32_bf16 v[60:63], a[68:71], v[96:99], v[60:63]\n\tbuffer_load_dwordx4 %[vd0], s[40:43], %[xo] offen offset:1024 lds\n\tv_mfma_f32_16x16x32_bf16 v[56:59], a[72:75], v[100:103], v[56:59]\n\tds_read_b128 v[68:71], %[ra1] offset:33792\n\t" \
          "v_mfma_f32_16x16x32_bf16 v[60:63], a[76:79], v[100:103], v[60:63]\n\ts_waitcnt lgkmcnt(6)\n\tv_mfma_f32_16x16x32_bf16 v[56:59], a[80:83], v[104:107], v[56:59]\n\tds_read_b128 v[72:75], %[ra2] offset:33792\n\tv_mfma_f32_16x16x32_bf16 v[60:63], a[84:87], v[104:107], v[60:63]\n\tbuffer_load_dwordx4 %[vd1], s[40:43], %[xo] offen offset:2048 lds\n\t" \
          "v_mfma_f32_16x16x32_bf16 v[56:59], a[88:91], v[108:111], v[56:59]\n\tds_read_b128 v[76:79], %[ra3] offset:33792\n\tv_mfma_f32_16x16x32_bf16 v[60:63], a[92:95], v[108:111], v[60:63]\n\ts_waitcnt lgkmcnt(6)\n\tv_mfma_f32_16x16x32_bf16 v[56:59], a[96:99], v[112:115], v[56:59]\n\tds_read_b128 v[80:83], %[ra0] offset:34048\n\t" \
          "v_mfma_f32_16x16x32_bf16 v[60:63], a[100:103], v[112:115], v[60:63]\n\tbuffer_load_dwordx4 %[vd1], s[40:43], %[xo] offen offset:3072 lds\n\tv_mfma_f32_16x16x32_bf16 v[56:59], a[104:107], v[116:119], v[56:59]\n\tds_read_b128 v[84:87], %[ra1] offset:34048\n\tv_mfma_f32_16x16x32_bf16 v[60:63], a[108:111], v[116:119], v[60:63]\n\ts_waitcnt lgkmcnt(6)\n\t" \
          "v_mfma_f32_16x16x32_bf16 v[56:59], a[112:115], v[120:123], v[56:59]\n\tds_read_b128 v[88:91], %[ra2] offset:34048\n\ts_add_u32 m0, %[sl], 4096\n\tv_mfma_f32_16x16x32_bf16 v[60:63], a[116:119], v[120:123], v[60:63]\n\tbuffer_load_dwordx4 %[vd2], s[40:43], %[xo] offen lds\n\tv_mfma_f32_16x16x32_bf16 v[56:59], a[120:123], v[124:127], v[56:59]\n\t" \
          "ds_read_b128 v[92:95], %[ra3] offset:34048\n\tv_mfma_f32_16x16x32_bf16 v[60:63], a[124:127], v[124:127], v[60:63]\n\ts_waitcnt lgkmcnt(6)\n\tv_mfma_f32_16x16x32_bf16 v[56:59], a[128:131], v[64:67], v[56:59]\n\tds_read_b128 v[96:99], %[ra0] offset:34304\n\tv_mfma_f32_16x16x32_bf16 v[60:63], a[132:135], v[64:67], v[60:63]\n\t" \
          "buffer_load_dwordx4 %[vd2], s[40:43], %[xo] offen offset:1024 lds\n\tv_mfma_f32_16x16x32_bf16 v[56:59], a[136:139], v[68:71], v[56:59]\n\tds_read_b128 v[100:103], %[ra1] offset:34304\n\tv_mfma_f32_16x16x32_bf16 v[60:63], a[140:143], v[68:71], v[60:63]\n\ts_waitcnt lgkmcnt(6)\n\tv_mfma_f32_16x16x32_bf16 v[56:59], a[144:147], v[72:75], v[56:59]\n\t" \
          "ds_read_b128 v[104:107], %[ra2] offset:34304\n\tv_mfma_f32_16x16x32_bf16 v[60:63], a[148:151], v[72:75], v[60:63]\n\tbuffer_load_dwordx4 %[vd3], s[40:43], %[xo] offen offset:2048 lds\n\tv_mfma_f32_16x16x32_bf16 v[56:59], a[152:155], v[76:79], v[56:59]\n\tds_read_b128 v[108:111], %[ra3] offset:34304\n\tv_mfma_f32_16x16x32_bf16 v[60:63], a[156:159], v[76:79], v[60:63]\n\t" \
          "s_waitcnt lgkmcnt(6)\n\tv_mfma_f32_16x16x32_bf16 v[56:59], a[160:163], v[80:83], v[56:59]\n\tds_read_b128 v[112:115], %[ra0] offset:34560\n\tv_mfma_f32_16x16x32_bf16 v[60:63], a[164:167], v[80:83], v[60:63]\n\tbuffer_load_dwordx4 %[vd3], s[40:43], %[xo] offen offset:3072 lds\n\tv_mfma_f32_16x16x32_bf16 v[56:59], a[168:171], v[84:87], v[56:59]\n\t" \
          "ds_read_b128 v[116:119], %[ra1] offset:34560\n\tv_mfma_f32_16x16x32_bf16 v[60:63], a[172:175], v[84:87], v[60:63]\n\ts_waitcnt lgkmcnt(6)\n\tv_mfma_f32_16x16x32_bf16 v[56:59], a[176:179], v[88:91], v[56:59]\n\tds_read_b128 v[120:123], %[ra2] offset:34560\n\tv_mfma_f32_16x16x32_bf16 v[60:63], a[180:183], v[88:91], v[60:63]\n\t" \
          "v_mfma_f32_16x16x32_bf16 v[56:59], a[184:187], v[92:95], v[56:59]\n\tds_read_b128 v[124:127], %[ra3] offset:34560\n\tv_mfma_f32_16x16x32_bf16 v[60:63], a[188:191], v[92:95], v[60:63]\n\ts_add_u32 %[xo], %[xo], 0x8000\n\ts_add_u32 %[sl], %[sl], 0x8000\n\ts_bitset0_b32 %[sl], 17\n\t" \
          "s_waitcnt lgkmcnt(6)\n\tv_mfma_f32_16x16x32_bf16 v[56:59], a[192:195], v[96:99], v[56:59]\n\tv_mfma_f32_16x16x32_bf16 v[60:63], a[196:199], v[96:99], v[60:63]\n\tv_xor_b32 %[ra0], 0x10000, %[ra0]\n\tv_mfma_f32_16x16x32_bf16 v[56:59], a[200:203], v[100:103], v[56:59]\n\tv_mfma_f32_16x16x32_bf16 v[60:63], a[204:207], v[100:103], v[60:63]\n\t" \
          "v_xor_b32 %[ra1], 0x10000, %[ra1]\n\ts_waitcnt lgkmcnt(4)\n\tv_mfma_f32_16x16x32_bf16 v[56:59], a[208:211], v[104:107], v[56:59]\n\tv_mfma_f32_16x16x32_bf16 v[60:63], a[212:215], v[104:107], v[60:63]\n\tv_xor_b32 %[ra2], 0x10000, %[ra2]\n\tv_mfma_f32_16x16x32_bf16 v[56:59], a[216:219], v[108:111], v[56:59]\n\t" \
          "s_add_u32 %[j], %[j], 2\n\ts_cmp_lt_u32 %[j], 128\n\tv_mfma_f32_16x16x32_bf16 v[60:63], a[220:223], v[108:111], v[60:63]\n\tv_xor_b32 %[ra3], 0x10000, %[ra3]\n\ts_waitcnt vmcnt(17) lgkmcnt(0)\n\ts_barrier\n\t" \
          "v_mfma_f32_16x16x32_bf16 v[56:59], a[224:227], v[112:115], v[56:59]\n\tds_read_b128 v[64:67], %[ra0] offset:0\n\tv_mfma_f32_16x16x32_bf16 v[60:63], a[228:231], v[112:115], v[60:63]\n\tds_read_b128 v[68:71], %[ra1] offset:0\n\tv_mfma_f32_16x16x32_bf16 v[56:59], a[232:235], v[116:119], v[56:59]\n\tds_read_b128 v[72:75], %[ra2] offset:0\n\t" \
          "v_mfma_f32_16x16x32_bf16 v[60:63], a[236:239], v[116:119], v[60:63]\n\tds_read_b128 v[76:79], %[ra3] offset:0\n\tv_mfma_f32_16x16x32_bf16 v[56:59], a[240:243], v[120:123], v[56:59]\n\tds_read_b128 v[80:83], %[ra0] offset:256\n\tv_mfma_f32_16x16x32_bf16 v[60:63], a[244:247], v[120:123], v[60:63]\n\tds_read_b128 v[84:87], %[ra1] offset:256\n\t" \
          "v_mfma_f32_16x16x32_bf16 v[56:59], a[248:251], v[124:127], v[56:59]\n\tds_read_b128 v[88:91], %[ra2] offset:256\n\tv_mfma_f32_16x16x32_bf16 v[60:63], a[252:255], v[124:127], v[60:63]\n\tds_read_b128 v[92:95], %[ra3] offset:256\n\ts_cbranch_scc1 Lpn_loop%=\n\ts_nop 7\n\t" \
          "v_mov_b32 %[fv], 128\n\tv_cvt_pk_bf16_f32 v44, v56, v57\n\tv_cvt_pk_bf16_f32 v45, v58, v59\n\tv_cvt_pk_bf16_f32 v46, v60, v61\n\tv_cvt_pk_bf16_f32 v47, v62, v63\n\tbuffer_store_dwordx4 v[44:47], %[so], %[rc], %[co] offen sc1\n\t" \
          "s_waitcnt vmcnt(0) lgkmcnt(0)\n\ts_barrier\n\ts_mov_b64 exec, %[fx]\n\tbuffer_store_dword %[fv], off, %[rf], %[fo] sc1\n\ts_mov_b64 exec, -1\n\t"
#define PN_CLOB "v44", "v45", "v46", "v47", "v48", "v49", "v50", "v51", "v52", "v53", "v54", "v55", "v56", "v57", "v58", "v59", "v60", "v61", "v62", "v63", "v64", "v65", "v66", "v67", "v68", "v69", "v70", "v71", "v72", "v73", "v74", "v75", "v76", "v77", "v78", "v79", "v80", "v81", "v82", "v83", "v84", "v85", "v86", "v87", "v88", "v89", "v90", "v91", "v92", "v93", "v94", "v95", "v96", "v97", "v98", "v99", "v100", "v101", "v102", "v103", "v104", "v105", "v106", "v107", "v108", "v109", "v110", "v111", "v112", "v113", "v114", "v115", "v116", "v117", "v118", "v119", "v120", "v121", "v122", "v123", "v124", "v125", "v126", "v127", "a0", "a1", "a2", "a3", "a4", "a5", "a6", "a7", "a8", "a9", "a10", "a11", "a12", "a13", "a14", "a15", "a16", "a17", "a18", "a19", "a20", "a21", "a22", "a23", "a24", "a25", "a26", "a27", "a28", "a29", "a30", "a31", "a32", "a33", "a34", "a35", "a36", "a37", "a38", "a39", "a40", "a41", "a42", "a43", "a44", "a45", "a46", "a47", "a48", "a49", "a50", "a51", "a52", "a53", "a54", "a55", "a56", "a57", "a58", "a59", "a60", "a61", "a62", "a63", "a64", "a65", "a66", "a67", "a68", "a69", "a70", "a71", "a72", "a73", "a74", "a75", "a76", "a77", "a78", "a79", "a80", "a81", "a82", "a83", "a84", "a85", "a86", "a87", "a88", "a89", "a90", "a91", "a92", "a93", "a94", "a95", "a96", "a97", "a98", "a99", "a100", "a101", "a102", "a103", "a104", "a105", "a106", "a107", "a108", "a109", "a110", "a111", "a112", "a113", "a114", "a115", "a116", "a117", "a118", "a119", "a120", "a121", "a122", "a123", "a124", "a125", "a126", "a127", "a128", "a129", "a130", "a131", "a132", "a133", "a134", "a135", "a136", "a137", "a138", "a139", "a140", "a141", "a142", "a143", "a144", "a145", "a146", "a147", "a148", "a149", "a150", "a151", "a152", "a153", "a154", "a155", "a156", "a157", "a158", "a159", "a160", "a161", "a162", "a163", "a164", "a165", "a166", "a167", "a168", "a169", "a170", "a171", "a172", "a173", "a174", "a175", "a176", "a177", "a178", "a179", "a180", "a181", "a182", "a183", "a184", "a185", "a186", "a187", "a188", "a189", "a190", "a191", "a192", "a193", "a194", "a195", "a196", "a197", "a198", "a199", "a200", "a201", "a202", "a203", "a204", "a205", "a206", "a207", "a208", "a209", "a210", "a211", "a212", "a213", "a214", "a215", "a216", "a217", "a218", "a219", "a220", "a221", "a222", "a223", "a224", "a225", "a226", "a227", "a228", "a229", "a230", "a231", "a232", "a233", "a234", "a235", "a236", "a237", "a238", "a239", "a240", "a241", "a242", "a243", "a244", "a245", "a246", "a247", "a248", "a249", "a250", "a251", "a252", "a253", "a254", "a255", "s40", "s41", "s42", "s43"
#define NTP 0
#define PN_NT ""
__device__ __forceinline__ void panel_task(const FParams& F, int p, int w) {
  const int lane = opaque_lane(), l15 = lane & 15, lq = lane >> 4, m = p >> 5, h = p & 31, n = 16 * m + (h >> 1);
  const __amdgpu_buffer_rsrc_t rw = __builtin_amdgcn_make_buffer_rsrc(wg::uni((m == 0 ? F.Wq : (m == 1 ? F.Wk : F.Wv)) + h * DHA), 0, 0x7fffffff, 0x00020000);
  const __amdgpu_buffer_rsrc_t rc = __builtin_amdgcn_make_buffer_rsrc((char*)F.qkvA + __builtin_amdgcn_readfirstlane(n * QPAGE + (F.par[192 + n] ? 8192 : 0) + (h & 1) * 4096 + w * 1024), 0, QPAGE, 0x00020000);
  const __amdgpu_buffer_rsrc_t rf = __builtin_amdgcn_make_buffer_rsrc(F.ctl, 0, sizeof(Ctl), 0x00020000);
  int ra[4], vd[4], rt[4], vw[2], fv = 0, xo = w * 8192, sl = w * 8192, co = 0, cs = 0, t, j; unsigned long long sx;
#pragma unroll
  for (int i = 0; i < 4; ++i) {
    ra[i] = l15 * 2048 + (((4 * i + lq) ^ l15) << 4); vd[i] = ((lane ^ (4 * w + i)) << 4) + (i >> 1) * 4096;
    rt[i] = 65536 + (i >> 1) * 49152 + (8 * lq + (l15 >> 2)) * 256 + (((2 * w + (i & 1)) ^ ((l15 >> 2) | ((lq & 1) << 2))) << 5) + (l15 & 3) * 8;
  }
  vw[0] = lq * 8192 + ((l15 ^ (lq << 1)) << 4); vw[1] = vw[0] ^ 128;
  asm volatile(PN_BODY
    : [ra0] "+v"(ra[0]), [ra1] "+v"(ra[1]), [ra2] "+v"(ra[2]), [ra3] "+v"(ra[3]), [fv] "+v"(fv), [xo] "+s"(xo), [sl] "+s"(sl), [co] "+s"(co), [cs] "+s"(cs), [t] "=&s"(t), [j] "=&s"(j), [sx] "=&s"(sx)
    : [vd0] "v"(vd[0]), [vd1] "v"(vd[1]), [vd2] "v"(vd[2]), [vd3] "v"(vd[3]), [rt0] "v"(rt[0]), [rt1] "v"(rt[1]), [rt2] "v"(rt[2]), [rt3] "v"(rt[3]), [vw0] "v"(vw[0]), [vw1] "v"(vw[1]), [so] "v"(lane * 16),
      [xa] "s"(F.Ax), [rw] "s"(rw), [rc] "s"(rc), [rf] "s"(rf), [wl] "s"(w * 4096), [wo] "s"(w * 16 * 8192), [fo] "s"((int)offsetof(Ctl, prg) + p * 128), [fx] "s"((unsigned long long)(unsigned)__builtin_amdgcn_readfirstlane(w == 0))
    : PN_CLOB, "scc", "memory");
}

struct AttnX { v4u q[2][4], kx[2][4], vr[8]; };
template <int AUX = 0>
__device__ __forceinline__ void attnA_load_x(const FParams& F, unsigned long long parq, int seg, int h, int lane, AttnX& X) {
  const __amdgpu_buffer_rsrc_t rs = __builtin_amdgcn_make_buffer_rsrc(F.qkvA, 0, QSIZE, 0x00020000);
  const int so = seg * 2 * QBLK + (h & 1) * 4096, oq = so + qoff(parq, h >> 1), ok = so + qoff(parq, 16 + (h >> 1)), ov = so + qoff(parq, 32 + (h >> 1));
#pragma unroll
  for (int i = 0; i < 8; ++i) X.vr[i] = __builtin_amdgcn_raw_buffer_load_b128(rs, lane * 16 + (i & 3) * 1024, ov + (i >> 2) * QBLK, AUX);
#pragma unroll
  for (int t = 0; t < 2; ++t)
#pragma unroll
    for (int j = 0; j < 4; ++j) {
      X.q[t][j] = __builtin_amdgcn_raw_buffer_load_b128(rs, lane * 16 + j * 1024, oq + t * QBLK, AUX);
      X.kx[t][j] = __builtin_amdgcn_raw_buffer_load_b128(rs, lane * 16 + j * 1024, ok + t * QBLK, AUX);
    }
}
__device__ __forceinline__ void attnA_stage_v(const AttnX& X, u16* vs  , int lane) {
#pragma unroll
  for (int i = 0; i < 8; ++i) *(v4u*)&vs[(16 * (i >> 2) + (lane & 15)) * VXLD + 32 * (i & 3) + 8 * (lane >> 4)] = X.vr[i];
}
template <int WHAT = 3>
__device__ __forceinline__ unsigned attnA_load_m(const FParams& F, unsigned long long parv, int seg, int h, int lane, v4u (&km)[4], unsigned (&vm)[8]) {
  const int l15 = lane & 15, lq = lane >> 4;
  const __amdgpu_buffer_rsrc_t rk = __builtin_amdgcn_make_buffer_rsrc(F.kmem + (size_t)seg * MEM * HDA, 0, MEM * HDA * 2, 0x00020000);
  const __amdgpu_buffer_rsrc_t rv = __builtin_amdgcn_make_buffer_rsrc((char*)F.vbuf + (size_t)seg * OREG + (int)((parv >> seg) & 1) * 8192 + dbl(h * 2048), 0, 2048, 0x00020000);
#pragma unroll
  for (int j = 0; j < 4; ++j)
    if (WHAT & 1) km[j] = __builtin_amdgcn_raw_buffer_load_b128(rk, h * 2048 + (2 * (lane >> 2) + (lane & 1)) * 16 + 512 * j, 0, 16);
#pragma unroll
  for (int t = 0; t < 8; ++t)
    if (WHAT & 2) vm[t] = __builtin_amdgcn_raw_buffer_load_b32(rv, (((l15 >> 3) * 4 + lq) * 16 + 8 * ((l15 >> 2) & 1) + (l15 & 3)) * 4 + 512 * (t >> 1) + 16 * (t & 1), 0, 16);
  unsigned m = 0;
#pragma unroll
  for (int j = 0; j < 4; ++j) if (WHAT & 1) m = chk4(m, km[j]);
#pragma unroll
  for (int t = 0; t < 8; t += 2) if (WHAT & 2) m = umax3(m, vm[t], vm[t + 1]);
  return m;
}
__device__ __forceinline__ void attnA_compute(const AttnX& X, const v4u (&km)[4], const unsigned (&vm)[8], const u16* vs, int lane, v4u (&ov)[2][4]) {
  const int l15 = lane & 15, lq = lane >> 4;
  const float scale = 0.08838834764831845f, L2E = 1.4426950408889634f;
  v4u px[2], pm[2];
#pragma unroll
  for (int t = 0; t < 2; ++t) {
    f32x4 s0 = {0.f,0.f,0.f,0.f}, s1 = s0, s2 = s0;
#pragma unroll
    for (int j = 0; j < 4; ++j) { s0 = mf(km[j], X.q[t][j], s0); s1 = mf(X.kx[0][j], X.q[t][j], s1); if (t == 1) s2 = mf(X.kx[1][j], X.q[t][j], s2); }
    float sv[12];
#pragma unroll
    for (int e = 0; e < 4; ++e) { sv[e] = s0[e]; sv[4 + e] = s1[e]; sv[8 + e] = s2[e]; }
#pragma unroll
    for (int e = 0; e < 12; e += 2) {
      unsigned p = pk2(sv[e], sv[e + 1]);
      p = pk2(__uint_as_float(p << 16) * scale, __uint_as_float(p & 0xffff0000u) * scale);
      sv[e] = __uint_as_float(p << 16); sv[e + 1] = __uint_as_float(p & 0xffff0000u);
    }
    bool ok[12];
#pragma unroll
    for (int e = 0; e < 4; ++e) { ok[e] = e < 2; ok[4 + e] = (t == 1) || (4 * lq + e <= l15); ok[8 + e] = (t == 1) && (4 * lq + e <= l15); }
    float mx = -INFINITY;
#pragma unroll
    for (int e = 0; e < 12; ++e) mx = ok[e] ? fmaxf(mx, sv[e]) : mx;
    mx = xmax32(xmax16(mx));
    float sum = 0.f;
#pragma unroll
    for (int e = 0; e < 12; ++e) { sv[e] = ok[e] ? __builtin_amdgcn_exp2f((sv[e] - mx) * L2E) : 0.f; sum += sv[e]; }
    sum = xsum32(xsum16(sum));
    const float inv = __builtin_amdgcn_rcpf(sum);
#pragma unroll
    for (int e = 0; e < 12; ++e) sv[e] = ok[e] ? sv[e] * inv : 0.f;
    px[t] = (v4u){pk2(sv[4], sv[5]), pk2(sv[6], sv[7]), pk2(sv[8], sv[9]), pk2(sv[10], sv[11])};
    pm[t] = (v4u){pk2(sv[0], sv[1]), 0u, 0u, 0u};
  }
  const u16* vp = &vs[(4 * lq + (l15 >> 2)) * VXLD + 16 * (l15 & 1) + 2 * (l15 & 2)];
#pragma unroll
  for (int u = 0; u < 4; ++u) {
    f32x4 o[2][2];
#pragma unroll
    for (int ab = 0; ab < 2; ++ab) {
      v2u lo = trrd(vp + 32 * u + 8 * ab), hi = trrd(vp + 16 * VXLD + 32 * u + 8 * ab);
      const v4u va = {lo.x, lo.y, hi.x, hi.y}, vb = {vm[2 * u + ab], 0u, 0u, 0u};
#pragma unroll
      for (int t = 0; t < 2; ++t) { o[t][ab] = mf(va, px[t], (f32x4){0.f,0.f,0.f,0.f}); o[t][ab] = mf(vb, pm[t], o[t][ab]); }
    }
#pragma unroll
    for (int t = 0; t < 2; ++t)
      ov[t][u] = (v4u){pk2(o[t][0][0], o[t][0][1]), pk2(o[t][0][2], o[t][0][3]), pk2(o[t][1][0], o[t][1][1]), pk2(o[t][1][2], o[t][1][3])};
  }
}
__device__ __forceinline__ void attnA_block(const FParams& F, unsigned long long parv, unsigned long long parq, int r, int h, u16* vs, int lane, int w, unsigned* shw) {
  AttnX X[2]; v4u km[2][4]; unsigned vm[2][8];
  unsigned seen = 0;
  attnA_load_x(F, parq, 4 * r, h, lane, X[0]);
  bool have = false;
#pragma unroll
  for (int i = 0; i < 4; ++i) {
    const int seg = 4 * r + i, c = i & 1;
    v4u ov[2][4];
    if (!have) {
      if (seen < (unsigned)seg + 1) {
        if (w == 0) {
          for (unsigned spins = 0;; ++spins) {
            seen = __builtin_amdgcn_readfirstlane(ld_agent(&F.ctl->prog[0]));
            if (seen >= (unsigned)seg + 1 || spin_fail(spins, &F.ctl->tmo[0], 6u)) break;
            for (unsigned z = seen; z < (unsigned)seg + 1; ++z) __builtin_amdgcn_s_sleep(PSL);
          }
          *shw = seen;
        }
        __syncthreads();
        seen = __builtin_amdgcn_readfirstlane(*shw);
        __syncthreads();
      }
      asm volatile("" ::: "memory");
      (void)attnA_load_m(F, parv, seg, h, lane, km[c], vm[c]);
    }
    if (i < 3) {
      attnA_load_x(F, parq, seg + 1, h, lane, X[c ^ 1]);
      have = seen >= (unsigned)seg + 2;
      (void)attnA_load_m(F, parv, have ? seg + 1 : seg, h, lane, km[c ^ 1], vm[c ^ 1]);
    }
    SB0;
    attnA_stage_v(X[c], vs, lane);
    attnA_compute(X[c], km[c], vm[c], vs, lane, ov);
    const __amdgpu_buffer_rsrc_t rs = __builtin_amdgcn_make_buffer_rsrc(F.qkvA, 0, QSIZE, 0x00020000);
#pragma unroll
    for (int u = 0; u < 4; ++u)
#pragma unroll
      for (int t = 0; t < 2; ++t)
        __builtin_amdgcn_raw_buffer_store_b128(ov[t][u], rs, lane * 16 + u * 1024, qoff(parq, h >> 1) + (seg * 2 + t) * QBLK + (h & 1) * 4096, 16);
    SB0;
  }
}

constexpr int TSEG0 = 36, OSEG0 = 16, NSET = 4;
__device__ __forceinline__ void attn_stream(const FParams& F, unsigned long long parv, unsigned long long parq, int set, int hg, char* lds, int w) {
  const int lane = opaque_lane(), h = 4 * hg + w;
  Ctl* ctl = F.ctl;
  for (unsigned spins = 0;; ++spins) {
    if (__all(ld_agent(&ctl->prg[(lane < 3 ? lane : 0) * 32 + h][0]) >= 128u) || spin_fail(spins, &ctl->tmo[0], 2u)) break;
    __builtin_amdgcn_s_sleep(16);
  }
  asm volatile("" ::: "memory");
  u16* vs = (u16*)(lds + w * 16384);
  AttnX X;
  attnA_load_x<16>(F, parq, TSEG0 + set, h, lane, X);
#pragma unroll 1
  for (int seg = TSEG0 + set; seg < NSEG; seg += NSET) {
    v4u km[4], ov[2][4]; unsigned vm[8];
    attnA_stage_v(X, vs, lane);
    for (unsigned spins = 0;; ++spins) {
      if (__all(attnA_load_m<1>(F, parv, seg, h, lane, km, vm) != 0xffffffffu) || spin_fail(spins, &ctl->tmo[0], 4u)) break;
      __builtin_amdgcn_s_sleep(TSL);
    }
    asm volatile("" ::: "memory");
    (void)attnA_load_m<2>(F, parv, seg, h, lane, km, vm);
    attnA_compute(X, km, vm, vs, lane, ov);
    const __amdgpu_buffer_rsrc_t rs = __builtin_amdgcn_make_buffer_rsrc(F.qkvA, 0, QSIZE, 0x00020000);
#pragma unroll
    for (int u = 0; u < 4; ++u)
#pragma unroll
      for (int t = 0; t < 2; ++t)
        __builtin_amdgcn_raw_buffer_store_b128(ov[t][u], rs, lane * 16 + u * 1024, qoff(parq, h >> 1) + (seg * 2 + t) * QBLK + (h & 1) * 4096, 16);
    SB0;
    attnA_load_x<16>(F, parq, seg + NSET < NSEG ? seg + NSET : TSEG0 - 1, h, lane, X);
    SB0;
    asm volatile("s_waitcnt vmcnt(24)\n\ts_barrier" ::: "memory");
    if (w == 0 && lane == 0) __hip_atomic_fetch_add(&ctl->cntO[seg][0], 1u, RLX_AGENT);
  }
}
#define OU_BODY \
          "s_mov_b64 s[40:41], %[qa]\n\ts_mov_b32 s42, %[qs]\n\ts_mov_b32 s43, 0x20000\n\ts_mov_b64 s[44:45], %[qa]\n\ts_mov_b32 s47, 0x20000\n\ts_nop 4\n\t" \
          "s_add_u32 m0, %[wl], 0\n\ts_xor_b32 %[t], %[wo], 0x0\n\tbuffer_load_dwordx4 %[vw0], %[rw], %[t] offen lds\n\ts_add_u32 m0, %[wl], 1024\n\ts_xor_b32 %[t], %[wo], 0x4000\n\tbuffer_load_dwordx4 %[vw1], %[rw], %[t] offen lds\n\t" \
          "s_add_u32 m0, %[wl], 2048\n\ts_xor_b32 %[t], %[wo], 0x8000\n\tbuffer_load_dwordx4 %[vw0], %[rw], %[t] offen lds\n\ts_add_u32 m0, %[wl], 3072\n\ts_xor_b32 %[t], %[wo], 0xc000\n\tbuffer_load_dwordx4 %[vw1], %[rw], %[t] offen lds\n\t" \
          "s_add_u32 m0, %[wl], 4096\n\ts_xor_b32 %[t], %[wo], 0x10000\n\tbuffer_load_dwordx4 %[vw0], %[rw], %[t] offen lds\n\ts_add_u32 m0, %[wl], 5120\n\ts_xor_b32 %[t], %[wo], 0x14000\n\tbuffer_load_dwordx4 %[vw1], %[rw], %[t] offen lds\n\t" \
          "s_add_u32 m0, %[wl], 6144\n\ts_xor_b32 %[t], %[wo], 0x18000\n\tbuffer_load_dwordx4 %[vw0], %[rw], %[t] offen lds\n\ts_add_u32 m0, %[wl], 7168\n\ts_xor_b32 %[t], %[wo], 0x1c000\n\tbuffer_load_dwordx4 %[vw1], %[rw], %[t] offen lds\n\t" \
          "s_add_u32 m0, %[wl], 8192\n\ts_xor_b32 %[t], %[wo], 0x20000\n\tbuffer_load_dwordx4 %[vw0], %[rw], %[t] offen lds\n\ts_add_u32 m0, %[wl], 9216\n\ts_xor_b32 %[t], %[wo], 0x24000\n\tbuffer_load_dwordx4 %[vw1], %[rw], %[t] offen lds\n\t" \
          "s_add_u32 m0, %[wl], 10240\n\ts_xor_b32 %[t], %[wo], 0x28000\n\tbuffer_load_dwordx4 %[vw0], %[rw], %[t] offen lds\n\ts_add_u32 m0, %[wl], 11264\n\ts_xor_b32 %[t], %[wo], 0x2c000\n\tbuffer_load_dwordx4 %[vw1], %[rw], %[t] offen lds\n\t" \
          "s_add_u32 m0, %[wl], 12288\n\ts_xor_b32 %[t], %[wo], 0x30000\n\tbuffer_load_dwordx4 %[vw0], %[rw], %[t] offen lds\n\ts_add_u32 m0, %[wl], 13312\n\ts_xor_b32 %[t], %[wo], 0x34000\n\tbuffer_load_dwordx4 %[vw1], %[rw], %[t] offen lds\n\t" \
          "s_add_u32 m0, %[wl], 14336\n\ts_xor_b32 %[t], %[wo], 0x38000\n\tbuffer_load_dwordx4 %[vw0], %[rw], %[t] offen lds\n\ts_add_u32 m0, %[wl], 15360\n\ts_xor_b32 %[t], %[wo], 0x3c000\n\tbuffer_load_dwordx4 %[vw1], %[rw], %[t] offen lds\n\t" \
          "s_add_u32 m0, %[wl], 16384\n\ts_xor_b32 %[t], %[wo], 0x40000\n\tbuffer_load_dwordx4 %[vw0], %[rw], %[t] offen lds\n\ts_add_u32 m0, %[wl], 17408\n\ts_xor_b32 %[t], %[wo], 0x44000\n\tbuffer_load_dwordx4 %[vw1], %[rw], %[t] offen lds\n\t" \
          "s_add_u32 m0, %[wl], 18432\n\ts_xor_b32 %[t], %[wo], 0x48000\n\tbuffer_load_dwordx4 %[vw0], %[rw], %[t] offen lds\n\ts_add_u32 m0, %[wl], 19456\n\ts_xor_b32 %[t], %[wo], 0x4c000\n\tbuffer_load_dwordx4 %[vw1], %[rw], %[t] offen lds\n\t" \
          "s_add_u32 m0, %[wl], 20480\n\ts_xor_b32 %[t], %[wo], 0x50000\n\tbuffer_load_dwordx4 %[vw0], %[rw], %[t] offen lds\n\ts_add_u32 m0, %[wl], 21504\n\ts_xor_b32 %[t], %[wo], 0x54000\n\tbuffer_load_dwordx4 %[vw1], %[rw], %[t] offen lds\n\t" \
          "s_add_u32 m0, %[wl], 22528\n\ts_xor_b32 %[t], %[wo], 0x58000\n\tbuffer_load_dwordx4 %[vw0], %[rw], %[t] offen lds\n\ts_add_u32 m0, %[wl], 23552\n\ts_xor_b32 %[t], %[wo], 0x5c000\n\tbuffer_load_dwordx4 %[vw1], %[rw], %[t] offen lds\n\t" \
          "s_add_u32 m0, %[wl], 24576\n\ts_xor_b32 %[t], %[wo], 0x60000\n\tbuffer_load_dwordx4 %[vw0], %[rw], %[t] offen lds\n\ts_add_u32 m0, %[wl], 25600\n\ts_xor_b32 %[t], %[wo], 0x64000\n\tbuffer_load_dwordx4 %[vw1], %[rw], %[t] offen lds\n\t" \
          "s_add_u32 m0, %[wl], 26624\n\ts_xor_b32 %[t], %[wo], 0x68000\n\tbuffer_load_dwordx4 %[vw0], %[rw], %[t] offen lds\n\ts_add_u32 m0, %[wl], 27648\n\ts_xor_b32 %[t], %[wo], 0x6c000\n\tbuffer_load_dwordx4 %[vw1], %[rw], %[t] offen lds\n\t" \
          "s_add_u32 m0, %[wl], 28672\n\ts_xor_b32 %[t], %[wo], 0x70000\n\tbuffer_load_dwordx4 %[vw0], %[rw], %[t] offen lds\n\ts_add_u32 m0, %[wl], 29696\n\ts_xor_b32 %[t], %[wo], 0x74000\n\tbuffer_load_dwordx4 %[vw1], %[rw], %[t] offen lds\n\t" \
          "s_add_u32 m0, %[wl], 30720\n\ts_xor_b32 %[t], %[wo], 0x78000\n\tbuffer_load_dwordx4 %[vw0], %[rw], %[t] offen lds\n\ts_add_u32 m0, %[wl], 31744\n\ts_xor_b32 %[t], %[wo], 0x7c000\n\tbuffer_load_dwordx4 %[vw1], %[rw], %[t] offen lds\n\t" \
          "s_add_u32 m0, %[wl], 32768\n\ts_xor_b32 %[t], %[wo], 0x80000\n\tbuffer_load_dwordx4 %[vw0], %[rw], %[t] offen lds\n\ts_add_u32 m0, %[wl], 33792\n\ts_xor_b32 %[t], %[wo], 0x84000\n\tbuffer_load_dwordx4 %[vw1], %[rw], %[t] offen lds\n\t" \
          "s_add_u32 m0, %[wl], 34816\n\ts_xor_b32 %[t], %[wo], 0x88000\n\tbuffer_load_dwordx4 %[vw0], %[rw], %[t] offen lds\n\ts_add_u32 m0, %[wl], 35840\n\ts_xor_b32 %[t], %[wo], 0x8c000\n\tbuffer_load_dwordx4 %[vw1], %[rw], %[t] offen lds\n\t" \
          "s_add_u32 m0, %[wl], 36864\n\ts_xor_b32 %[t], %[wo], 0x90000\n\tbuffer_load_dwordx4 %[vw0], %[rw], %[t] offen lds\n\ts_add_u32 m0, %[wl], 37888\n\ts_xor_b32 %[t], %[wo], 0x94000\n\tbuffer_load_dwordx4 %[vw1], %[rw], %[t] offen lds\n\t" \
          "s_add_u32 m0, %[wl], 38912\n\ts_xor_b32 %[t], %[wo], 0x98000\n\tbuffer_load_dwordx4 %[vw0], %[rw], %[t] offen lds\n\ts_add_u32 m0, %[wl], 39936\n\ts_xor_b32 %[t], %[wo], 0x9c000\n\tbuffer_load_dwordx4 %[vw1], %[rw], %[t] offen lds\n\t" \
          "s_waitcnt vmcnt(32)\n\tds_read_b64_tr_b16 a[0:1], %[rt0] offset:0\n\tds_read_b64_tr_b16 a[2:3], %[rt0] offset:512\n\tds_read_b64_tr_b16 a[4:5], %[rt1] offset:0\n\tds_read_b64_tr_b16 a[6:7], %[rt1] offset:512\n\tds_read_b64_tr_b16 v[128:129], %[rt2] offset:0\n\t" \
          "ds_read_b64_tr_b16 v[130:131], %[rt2] offset:512\n\tds_read_b64_tr_b16 v[32:33], %[rt3] offset:0\n\tds_read_b64_tr_b16 v[34:35], %[rt3] offset:512\n\tds_read_b64_tr_b16 a[8:9], %[rt0] offset:4096\n\tds_read_b64_tr_b16 a[10:11], %[rt0] offset:4608\n\tds_read_b64_tr_b16 a[12:13], %[rt1] offset:4096\n\t" \
          "ds_read_b64_tr_b16 a[14:15], %[rt1] offset:4608\n\tds_read_b64_tr_b16 v[132:133], %[rt2] offset:4096\n\tds_read_b64_tr_b16 v[134:135], %[rt2] offset:4608\n\tds_read_b64_tr_b16 v[36:37], %[rt3] offset:4096\n\tds_read_b64_tr_b16 v[38:39], %[rt3] offset:4608\n\ts_waitcnt lgkmcnt(0)\n\t" \
          "s_add_u32 m0, %[wl], 0\n\ts_xor_b32 %[t], %[wo], 0xa0000\n\tbuffer_load_dwordx4 %[vw0], %[rw], %[t] offen lds\n\ts_add_u32 m0, %[wl], 1024\n\ts_xor_b32 %[t], %[wo], 0xa4000\n\tbuffer_load_dwordx4 %[vw1], %[rw], %[t] offen lds\n\t" \
          "s_add_u32 m0, %[wl], 2048\n\ts_xor_b32 %[t], %[wo], 0xa8000\n\tbuffer_load_dwordx4 %[vw0], %[rw], %[t] offen lds\n\ts_add_u32 m0, %[wl], 3072\n\ts_xor_b32 %[t], %[wo], 0xac000\n\tbuffer_load_dwordx4 %[vw1], %[rw], %[t] offen lds\n\t" \
          "s_add_u32 m0, %[wl], 4096\n\ts_xor_b32 %[t], %[wo], 0xb0000\n\tbuffer_load_dwordx4 %[vw0], %[rw], %[t] offen lds\n\ts_add_u32 m0, %[wl], 5120\n\ts_xor_b32 %[t], %[wo], 0xb4000\n\tbuffer_load_dwordx4 %[vw1], %[rw], %[t] offen lds\n\t" \
          "s_add_u32 m0, %[wl], 6144\n\ts_xor_b32 %[t], %[wo], 0xb8000\n\tbuffer_load_dwordx4 %[vw0], %[rw], %[t] offen lds\n\ts_add_u32 m0, %[wl], 7168\n\ts_xor_b32 %[t], %[wo], 0xbc000\n\tbuffer_load_dwordx4 %[vw1], %[rw], %[t] offen lds\n\t" \
          "s_waitcnt vmcnt(32)\n\tds_read_b64_tr_b16 a[16:17], %[rt0] offset:8192\n\tds_read_b64_tr_b16 a[18:19], %[rt0] offset:8704\n\tds_read_b64_tr_b16 a[20:21], %[rt1] offset:8192\n\tds_read_b64_tr_b16 a[22:23], %[rt1] offset:8704\n\tds_read_b64_tr_b16 v[136:137], %[rt2] offset:8192\n\t" \
          "ds_read_b64_tr_b16 v[138:139], %[rt2] offset:8704\n\tds_read_b64_tr_b16 v[40:41], %[rt3] offset:8192\n\tds_read_b64_tr_b16 v[42:43], %[rt3] offset:8704\n\tds_read_b64_tr_b16 a[24:25], %[rt0] offset:12288\n\tds_read_b64_tr_b16 a[26:27], %[rt0] offset:12800\n\tds_read_b64_tr_b16 a[28:29], %[rt1] offset:12288\n\t" \
          "ds_read_b64_tr_b16 a[30:31], %[rt1] offset:12800\n\tds_read_b64_tr_b16 v[140:141], %[rt2] offset:12288\n\tds_read_b64_tr_b16 v[142:143], %[rt2] offset:12800\n\tds_read_b64_tr_b16 v[44:45], %[rt3] offset:12288\n\tds_read_b64_tr_b16 v[46:47], %[rt3] offset:12800\n\ts_waitcnt lgkmcnt(0)\n\t" \
          "s_add_u32 m0, %[wl], 8192\n\ts_xor_b32 %[t], %[wo], 0xc0000\n\tbuffer_load_dwordx4 %[vw0], %[rw], %[t] offen lds\n\ts_add_u32 m0, %[wl], 9216\n\ts_xor_b32 %[t], %[wo], 0xc4000\n\tbuffer_load_dwordx4 %[vw1], %[rw], %[t] offen lds\n\t" \
          "s_add_u32 m0, %[wl], 10240\n\ts_xor_b32 %[t], %[wo], 0xc8000\n\tbuffer_load_dwordx4 %[vw0], %[rw], %[t] offen lds\n\ts_add_u32 m0, %[wl], 11264\n\ts_xor_b32 %[t], %[wo], 0xcc000\n\tbuffer_load_dwordx4 %[vw1], %[rw], %[t] offen lds\n\t" \
          "s_add_u32 m0, %[wl], 12288\n\ts_xor_b32 %[t], %[wo], 0xd0000\n\tbuffer_load_dwordx4 %[vw0], %[rw], %[t] offen lds\n\ts_add_u32 m0, %[wl], 13312\n\ts_xor_b32 %[t], %[wo], 0xd4000\n\tbuffer_load_dwordx4 %[vw1], %[rw], %[t] offen lds\n\t" \
          "s_add_u32 m0, %[wl], 14336\n\ts_xor_b32 %[t], %[wo], 0xd8000\n\tbuffer_load_dwordx4 %[vw0], %[rw], %[t] offen lds\n\ts_add_u32 m0, %[wl], 15360\n\ts_xor_b32 %[t], %[wo], 0xdc000\n\tbuffer_load_dwordx4 %[vw1], %[rw], %[t] offen lds\n\t" \
          "s_waitcnt vmcnt(32)\n\tds_read_b64_tr_b16 a[32:33], %[rt0] offset:16384\n\tds_read_b64_tr_b16 a[34:35], %[rt0] offset:16896\n\tds_read_b64_tr_b16 a[36:37], %[rt1] offset:16384\n\tds_read_b64_tr_b16 a[38:39], %[rt1] offset:16896\n\tds_read_b64_tr_b16 v[144:145], %[rt2] offset:16384\n\t" \
          "ds_read_b64_tr_b16 v[146:147], %[rt2] offset:16896\n\tds_read_b64_tr_b16 v[48:49], %[rt3] offset:16384\n\tds_read_b64_tr_b16 v[50:51], %[rt3] offset:16896\n\tds_read_b64_tr_b16 a[40:41], %[rt0] offset:20480\n\tds_read_b64_tr_b16 a[42:43], %[rt0] offset:20992\n\tds_read_b64_tr_b16 a[44:45], %[rt1] offset:20480\n\t" \
          "ds_read_b64_tr_b16 a[46:47], %[rt1] offset:20992\n\tds_read_b64_tr_b16 v[148:149], %[rt2] offset:20480\n\tds_read_b64_tr_b16 v[150:151], %[rt2] offset:20992\n\tds_read_b64_tr_b16 v[52:53], %[rt3] offset:20480\n\tds_read_b64_tr_b16 v[54:55], %[rt3] offset:20992\n\ts_waitcnt lgkmcnt(0)\n\t" \
          "s_add_u32 m0, %[wl], 16384\n\ts_xor_b32 %[t], %[wo], 0xe0000\n\tbuffer_load_dwordx4 %[vw0], %[rw], %[t] offen lds\n\ts_add_u32 m0, %[wl], 17408\n\ts_xor_b32 %[t], %[wo], 0xe4000\n\tbuffer_load_dwordx4 %[vw1], %[rw], %[t] offen lds\n\t" \
          "s_add_u32 m0, %[wl], 18432\n\ts_xor_b32 %[t], %[wo], 0xe8000\n\tbuffer_load_dwordx4 %[vw0], %[rw], %[t] offen lds\n\ts_add_u32 m0, %[wl], 19456\n\ts_xor_b32 %[t], %[wo], 0xec000\n\tbuffer_load_dwordx4 %[vw1], %[rw], %[t] offen lds\n\t" \
          "s_add_u32 m0, %[wl], 20480\n\ts_xor_b32 %[t], %[wo], 0xf0000\n\tbuffer_load_dwordx4 %[vw0], %[rw], %[t] offen lds\n\ts_add_u32 m0, %[wl], 21504\n\ts_xor_b32 %[t], %[wo], 0xf4000\n\tbuffer_load_dwordx4 %[vw1], %[rw], %[t] offen lds\n\t" \
          "s_add_u32 m0, %[wl], 22528\n\ts_xor_b32 %[t], %[wo], 0xf8000\n\tbuffer_load_dwordx4 %[vw0], %[rw], %[t] offen lds\n\ts_add_u32 m0, %[wl], 23552\n\ts_xor_b32 %[t], %[wo], 0xfc000\n\tbuffer_load_dwordx4 %[vw1], %[rw], %[t] offen lds\n\t" \
          "s_waitcnt vmcnt(32)\n\tds_read_b64_tr_b16 a[48:49], %[rt0] offset:24576\n\tds_read_b64_tr_b16 a[50:51], %[rt0] offset:25088\n\tds_read_b64_tr_b16 a[52:53], %[rt1] offset:24576\n\tds_read_b64_tr_b16 a[54:55], %[rt1] offset:25088\n\tds_read_b64_tr_b16 v[152:153], %[rt2] offset:24576\n\t" \
          "ds_read_b64_tr_b16 v[154:155], %[rt2] offset:25088\n\tds_read_b64_tr_b16 v[56:57], %[rt3] offset:24576\n\tds_read_b64_tr_b16 v[58:59], %[rt3] offset:25088\n\tds_read_b64_tr_b16 a[56:57], %[rt0] offset:28672\n\tds_read_b64_tr_b16 a[58:59], %[rt0] offset:29184\n\tds_read_b64_tr_b16 a[60:61], %[rt1] offset:28672\n\t" \
          "ds_read_b64_tr_b16 a[62:63], %[rt1] offset:29184\n\tds_read_b64_tr_b16 v[156:157], %[rt2] offset:28672\n\tds_read_b64_tr_b16 v[158:159], %[rt2] offset:29184\n\tds_read_b64_tr_b16 v[60:61], %[rt3] offset:28672\n\tds_read_b64_tr_b16 v[62:63], %[rt3] offset:29184\n\ts_waitcnt lgkmcnt(0)\n\t" \
          "s_add_u32 m0, %[wl], 24576\n\ts_xor_b32 %[t], %[wo], 0x100000\n\tbuffer_load_dwordx4 %[vw0], %[rw], %[t] offen lds\n\ts_add_u32 m0, %[wl], 25600\n\ts_xor_b32 %[t], %[wo], 0x104000\n\tbuffer_load_dwordx4 %[vw1], %[rw], %[t] offen lds\n\t" \
          "s_add_u32 m0, %[wl], 26624\n\ts_xor_b32 %[t], %[wo], 0x108000\n\tbuffer_load_dwordx4 %[vw0], %[rw], %[t] offen lds\n\ts_add_u32 m0, %[wl], 27648\n\ts_xor_b32 %[t], %[wo], 0x10c000\n\tbuffer_load_dwordx4 %[vw1], %[rw], %[t] offen lds\n\t" \
          "s_add_u32 m0, %[wl], 28672\n\ts_xor_b32 %[t], %[wo], 0x110000\n\tbuffer_load_dwordx4 %[vw0], %[rw], %[t] offen lds\n\ts_add_u32 m0, %[wl], 29696\n\ts_xor_b32 %[t], %[wo], 0x114000\n\tbuffer_load_dwordx4 %[vw1], %[rw], %[t] offen lds\n\t" \
          "s_add_u32 m0, %[wl], 30720\n\ts_xor_b32 %[t], %[wo], 0x118000\n\tbuffer_load_dwordx4 %[vw0], %[rw], %[t] offen lds\n\ts_add_u32 m0, %[wl], 31744\n\ts_xor_b32 %[t], %[wo], 0x11c000\n\tbuffer_load_dwordx4 %[vw1], %[rw], %[t] offen lds\n\t" \
          "s_waitcnt vmcnt(32)\n\tds_read_b64_tr_b16 a[64:65], %[rt0] offset:32768\n\tds_read_b64_tr_b16 a[66:67], %[rt0] offset:33280\n\tds_read_b64_tr_b16 a[68:69], %[rt1] offset:32768\n\tds_read_b64_tr_b16 a[70:71], %[rt1] offset:33280\n\tds_read_b64_tr_b16 v[160:161], %[rt2] offset:32768\n\t" \
          "ds_read_b64_tr_b16 v[162:163], %[rt2] offset:33280\n\tds_read_b64_tr_b16 v[64:65], %[rt3] offset:32768\n\tds_read_b64_tr_b16 v[66:67], %[rt3] offset:33280\n\tds_read_b64_tr_b16 a[72:73], %[rt0] offset:36864\n\tds_read_b64_tr_b16 a[74:75], %[rt0] offset:37376\n\tds_read_b64_tr_b16 a[76:77], %[rt1] offset:36864\n\t" \
          "ds_read_b64_tr_b16 a[78:79], %[rt1] offset:37376\n\tds_read_b64_tr_b16 v[164:165], %[rt2] offset:36864\n\tds_read_b64_tr_b16 v[166:167], %[rt2] offset:37376\n\tds_read_b64_tr_b16 v[68:69], %[rt3] offset:36864\n\tds_read_b64_tr_b16 v[70:71], %[rt3] offset:37376\n\ts_waitcnt lgkmcnt(0)\n\t" \
          "s_add_u32 m0, %[wl], 32768\n\ts_xor_b32 %[t], %[wo], 0x120000\n\tbuffer_load_dwordx4 %[vw0], %[rw], %[t] offen lds\n\ts_add_u32 m0, %[wl], 33792\n\ts_xor_b32 %[t], %[wo], 0x124000\n\tbuffer_load_dwordx4 %[vw1], %[rw], %[t] offen lds\n\t" \
          "s_add_u32 m0, %[wl], 34816\n\ts_xor_b32 %[t], %[wo], 0x128000\n\tbuffer_load_dwordx4 %[vw0], %[rw], %[t] offen lds\n\ts_add_u32 m0, %[wl], 35840\n\ts_xor_b32 %[t], %[wo], 0x12c000\n\tbuffer_load_dwordx4 %[vw1], %[rw], %[t] offen lds\n\t" \
          "s_add_u32 m0, %[wl], 36864\n\ts_xor_b32 %[t], %[wo], 0x130000\n\tbuffer_load_dwordx4 %[vw0], %[rw], %[t] offen lds\n\ts_add_u32 m0, %[wl], 37888\n\ts_xor_b32 %[t], %[wo], 0x134000\n\tbuffer_load_dwordx4 %[vw1], %[rw], %[t] offen lds\n\t" \
          "s_add_u32 m0, %[wl], 38912\n\ts_xor_b32 %[t], %[wo], 0x138000\n\tbuffer_load_dwordx4 %[vw0], %[rw], %[t] offen lds\n\ts_add_u32 m0, %[wl], 39936\n\ts_xor_b32 %[t], %[wo], 0x13c000\n\tbuffer_load_dwordx4 %[vw1], %[rw], %[t] offen lds\n\t" \
          "s_waitcnt vmcnt(32)\n\tds_read_b64_tr_b16 a[80:81], %[rt0] offset:0\n\tds_read_b64_tr_b16 a[82:83], %[rt0] offset:512\n\tds_read_b64_tr_b16 a[84:85], %[rt1] offset:0\n\tds_read_b64_tr_b16 a[86:87], %[rt1] offset:512\n\tds_read_b64_tr_b16 v[168:169], %[rt2] offset:0\n\t" \
          "ds_read_b64_tr_b16 v[170:171], %[rt2] offset:512\n\tds_read_b64_tr_b16 v[72:73], %[rt3] offset:0\n\tds_read_b64_tr_b16 v[74:75], %[rt3] offset:512\n\tds_read_b64_tr_b16 a[88:89], %[rt0] offset:4096\n\tds_read_b64_tr_b16 a[90:91], %[rt0] offset:4608\n\tds_read_b64_tr_b16 a[92:93], %[rt1] offset:4096\n\t" \
          "ds_read_b64_tr_b16 a[94:95], %[rt1] offset:4608\n\tds_read_b64_tr_b16 v[172:173], %[rt2] offset:4096\n\tds_read_b64_tr_b16 v[174:175], %[rt2] offset:4608\n\tds_read_b64_tr_b16 v[76:77], %[rt3] offset:4096\n\tds_read_b64_tr_b16 v[78:79], %[rt3] offset:4608\n\ts_waitcnt lgkmcnt(0)\n\t" \
          "s_add_u32 m0, %[wl], 0\n\ts_xor_b32 %[t], %[wo], 0x140000\n\tbuffer_load_dwordx4 %[vw0], %[rw], %[t] offen lds\n\ts_add_u32 m0, %[wl], 1024\n\ts_xor_b32 %[t], %[wo], 0x144000\n\tbuffer_load_dwordx4 %[vw1], %[rw], %[t] offen lds\n\t" \
          "s_add_u32 m0, %[wl], 2048\n\ts_xor_b32 %[t], %[wo], 0x148000\n\tbuffer_load_dwordx4 %[vw0], %[rw], %[t] offen lds\n\ts_add_u32 m0, %[wl], 3072\n\ts_xor_b32 %[t], %[wo], 0x14c000\n\tbuffer_load_dwordx4 %[vw1], %[rw], %[t] offen lds\n\t" \
          "s_add_u32 m0, %[wl], 4096\n\ts_xor_b32 %[t], %[wo], 0x150000\n\tbuffer_load_dwordx4 %[vw0], %[rw], %[t] offen lds\n\ts_add_u32 m0, %[wl], 5120\n\ts_xor_b32 %[t], %[wo], 0x154000\n\tbuffer_load_dwordx4 %[vw1], %[rw], %[t] offen lds\n\t" \
          "s_add_u32 m0, %[wl], 6144\n\ts_xor_b32 %[t], %[wo], 0x158000\n\tbuffer_load_dwordx4 %[vw0], %[rw], %[t] offen lds\n\ts_add_u32 m0, %[wl], 7168\n\ts_xor_b32 %[t], %[wo], 0x15c000\n\tbuffer_load_dwordx4 %[vw1], %[rw], %[t] offen lds\n\t" \
          "s_waitcnt vmcnt(32)\n\tds_read_b64_tr_b16 a[96:97], %[rt0] offset:8192\n\tds_read_b64_tr_b16 a[98:99], %[rt0] offset:8704\n\tds_read_b64_tr_b16 a[100:101], %[rt1] offset:8192\n\tds_read_b64_tr_b16 a[102:103], %[rt1] offset:8704\n\tds_read_b64_tr_b16 v[176:177], %[rt2] offset:8192\n\t" \
          "ds_read_b64_tr_b16 v[178:179], %[rt2] offset:8704\n\tds_read_b64_tr_b16 v[80:81], %[rt3] offset:8192\n\tds_read_b64_tr_b16 v[82:83], %[rt3] offset:8704\n\tds_read_b64_tr_b16 a[104:105], %[rt0] offset:12288\n\tds_read_b64_tr_b16 a[106:107], %[rt0] offset:12800\n\tds_read_b64_tr_b16 a[108:109], %[rt1] offset:12288\n\t" \
          "ds_read_b64_tr_b16 a[110:111], %[rt1] offset:12800\n\tds_read_b64_tr_b16 v[180:181], %[rt2] offset:12288\n\tds_read_b64_tr_b16 v[182:183], %[rt2] offset:12800\n\tds_read_b64_tr_b16 v[84:85], %[rt3] offset:12288\n\tds_read_b64_tr_b16 v[86:87], %[rt3] offset:12800\n\ts_waitcnt lgkmcnt(0)\n\t" \
          "s_add_u32 m0, %[wl], 8192\n\ts_xor_b32 %[t], %[wo], 0x160000\n\tbuffer_load_dwordx4 %[vw0], %[rw], %[t] offen lds\n\ts_add_u32 m0, %[wl], 9216\n\ts_xor_b32 %[t], %[wo], 0x164000\n\tbuffer_load_dwordx4 %[vw1], %[rw], %[t] offen lds\n\t" \
          "s_add_u32 m0, %[wl], 10240\n\ts_xor_b32 %[t], %[wo], 0x168000\n\tbuffer_load_dwordx4 %[vw0], %[rw], %[t] offen lds\n\ts_add_u32 m0, %[wl], 11264\n\ts_xor_b32 %[t], %[wo], 0x16c000\n\tbuffer_load_dwordx4 %[vw1], %[rw], %[t] offen lds\n\t" \
          "s_add_u32 m0, %[wl], 12288\n\ts_xor_b32 %[t], %[wo], 0x170000\n\tbuffer_load_dwordx4 %[vw0], %[rw], %[t] offen lds\n\ts_add_u32 m0, %[wl], 13312\n\ts_xor_b32 %[t], %[wo], 0x174000\n\tbuffer_load_dwordx4 %[vw1], %[rw], %[t] offen lds\n\t" \
          "s_add_u32 m0, %[wl], 14336\n\ts_xor_b32 %[t], %[wo], 0x178000\n\tbuffer_load_dwordx4 %[vw0], %[rw], %[t] offen lds\n\ts_add_u32 m0, %[wl], 15360\n\ts_xor_b32 %[t], %[wo], 0x17c000\n\tbuffer_load_dwordx4 %[vw1], %[rw], %[t] offen lds\n\t" \
          "s_waitcnt vmcnt(32)\n\tds_read_b64_tr_b16 a[112:113], %[rt0] offset:16384\n\tds_read_b64_tr_b16 a[114:115], %[rt0] offset:16896\n\tds_read_b64_tr_b16 a[116:117], %[rt1] offset:16384\n\tds_read_b64_tr_b16 a[118:119], %[rt1] offset:16896\n\tds_read_b64_tr_b16 v[184:185], %[rt2] offset:16384\n\t" \
          "ds_read_b64_tr_b16 v[186:187], %[rt2] offset:16896\n\tds_read_b64_tr_b16 v[88:89], %[rt3] offset:16384\n\tds_read_b64_tr_b16 v[90:91], %[rt3] offset:16896\n\tds_read_b64_tr_b16 a[120:121], %[rt0] offset:20480\n\tds_read_b64_tr_b16 a[122:123], %[rt0] offset:20992\n\tds_read_b64_tr_b16 a[124:125], %[rt1] offset:20480\n\t" \
          "ds_read_b64_tr_b16 a[126:127], %[rt1] offset:20992\n\tds_read_b64_tr_b16 v[188:189], %[rt2] offset:20480\n\tds_read_b64_tr_b16 v[190:191], %[rt2] offset:20992\n\tds_read_b64_tr_b16 v[92:93], %[rt3] offset:20480\n\tds_read_b64_tr_b16 v[94:95], %[rt3] offset:20992\n\ts_waitcnt lgkmcnt(0)\n\t" \
          "s_add_u32 m0, %[wl], 16384\n\ts_xor_b32 %[t], %[wo], 0x180000\n\tbuffer_load_dwordx4 %[vw0], %[rw], %[t] offen lds\n\ts_add_u32 m0, %[wl], 17408\n\ts_xor_b32 %[t], %[wo], 0x184000\n\tbuffer_load_dwordx4 %[vw1], %[rw], %[t] offen lds\n\t" \
          "s_add_u32 m0, %[wl], 18432\n\ts_xor_b32 %[t], %[wo], 0x188000\n\tbuffer_load_dwordx4 %[vw0], %[rw], %[t] offen lds\n\ts_add_u32 m0, %[wl], 19456\n\ts_xor_b32 %[t], %[wo], 0x18c000\n\tbuffer_load_dwordx4 %[vw1], %[rw], %[t] offen lds\n\t" \
          "s_add_u32 m0, %[wl], 20480\n\ts_xor_b32 %[t], %[wo], 0x190000\n\tbuffer_load_dwordx4 %[vw0], %[rw], %[t] offen lds\n\ts_add_u32 m0, %[wl], 21504\n\ts_xor_b32 %[t], %[wo], 0x194000\n\tbuffer_load_dwordx4 %[vw1], %[rw], %[t] offen lds\n\t" \
          "s_add_u32 m0, %[wl], 22528\n\ts_xor_b32 %[t], %[wo], 0x198000\n\tbuffer_load_dwordx4 %[vw0], %[rw], %[t] offen lds\n\ts_add_u32 m0, %[wl], 23552\n\ts_xor_b32 %[t], %[wo], 0x19c000\n\tbuffer_load_dwordx4 %[vw1], %[rw], %[t] offen lds\n\t" \
          "s_waitcnt vmcnt(32)\n\tds_read_b64_tr_b16 a[128:129], %[rt0] offset:24576\n\tds_read_b64_tr_b16 a[130:131], %[rt0] offset:25088\n\tds_read_b64_tr_b16 a[132:133], %[rt1] offset:24576\n\tds_read_b64_tr_b16 a[134:135], %[rt1] offset:25088\n\tds_read_b64_tr_b16 v[192:193], %[rt2] offset:24576\n\t" \
          "ds_read_b64_tr_b16 v[194:195], %[rt2] offset:25088\n\tds_read_b64_tr_b16 v[96:97], %[rt3] offset:24576\n\tds_read_b64_tr_b16 v[98:99], %[rt3] offset:25088\n\tds_read_b64_tr_b16 a[136:137], %[rt0] offset:28672\n\tds_read_b64_tr_b16 a[138:139], %[rt0] offset:29184\n\tds_read_b64_tr_b16 a[140:141], %[rt1] offset:28672\n\t" \
          "ds_read_b64_tr_b16 a[142:143], %[rt1] offset:29184\n\tds_read_b64_tr_b16 v[196:197], %[rt2] offset:28672\n\tds_read_b64_tr_b16 v[198:199], %[rt2] offset:29184\n\tds_read_b64_tr_b16 v[100:101], %[rt3] offset:28672\n\tds_read_b64_tr_b16 v[102:103], %[rt3] offset:29184\n\ts_waitcnt lgkmcnt(0)\n\t" \
          "s_add_u32 m0, %[wl], 24576\n\ts_xor_b32 %[t], %[wo], 0x1a0000\n\tbuffer_load_dwordx4 %[vw0], %[rw], %[t] offen lds\n\ts_add_u32 m0, %[wl], 25600\n\ts_xor_b32 %[t], %[wo], 0x1a4000\n\tbuffer_load_dwordx4 %[vw1], %[rw], %[t] offen lds\n\t" \
          "s_add_u32 m0, %[wl], 26624\n\ts_xor_b32 %[t], %[wo], 0x1a8000\n\tbuffer_load_dwordx4 %[vw0], %[rw], %[t] offen lds\n\ts_add_u32 m0, %[wl], 27648\n\ts_xor_b32 %[t], %[wo], 0x1ac000\n\tbuffer_load_dwordx4 %[vw1], %[rw], %[t] offen lds\n\t" \
          "s_add_u32 m0, %[wl], 28672\n\ts_xor_b32 %[t], %[wo], 0x1b0000\n\tbuffer_load_dwordx4 %[vw0], %[rw], %[t] offen lds\n\ts_add_u32 m0, %[wl], 29696\n\ts_xor_b32 %[t], %[wo], 0x1b4000\n\tbuffer_load_dwordx4 %[vw1], %[rw], %[t] offen lds\n\t" \
          "s_add_u32 m0, %[wl], 30720\n\ts_xor_b32 %[t], %[wo], 0x1b8000\n\tbuffer_load_dwordx4 %[vw0], %[rw], %[t] offen lds\n\ts_add_u32 m0, %[wl], 31744\n\ts_xor_b32 %[t], %[wo], 0x1bc000\n\tbuffer_load_dwordx4 %[vw1], %[rw], %[t] offen lds\n\t" \
          "s_waitcnt vmcnt(32)\n\tds_read_b64_tr_b16 a[144:145], %[rt0] offset:32768\n\tds_read_b64_tr_b16 a[146:147], %[rt0] offset:33280\n\tds_read_b64_tr_b16 a[148:149], %[rt1] offset:32768\n\tds_read_b64_tr_b16 a[150:151], %[rt1] offset:33280\n\tds_read_b64_tr_b16 v[200:201], %[rt2] offset:32768\n\t" \
          "ds_read_b64_tr_b16 v[202:203], %[rt2] offset:33280\n\tds_read_b64_tr_b16 v[104:105], %[rt3] offset:32768\n\tds_read_b64_tr_b16 v[106:107], %[rt3] offset:33280\n\tds_read_b64_tr_b16 a[152:153], %[rt0] offset:36864\n\tds_read_b64_tr_b16 a[154:155], %[rt0] offset:37376\n\tds_read_b64_tr_b16 a[156:157], %[rt1] offset:36864\n\t" \
          "ds_read_b64_tr_b16 a[158:159], %[rt1] offset:37376\n\tds_read_b64_tr_b16 v[204:205], %[rt2] offset:36864\n\tds_read_b64_tr_b16 v[206:207], %[rt2] offset:37376\n\tds_read_b64_tr_b16 v[108:109], %[rt3] offset:36864\n\tds_read_b64_tr_b16 v[110:111], %[rt3] offset:37376\n\ts_waitcnt lgkmcnt(0)\n\t" \
          "s_add_u32 m0, %[wl], 32768\n\ts_xor_b32 %[t], %[wo], 0x1c0000\n\tbuffer_load_dwordx4 %[vw0], %[rw], %[t] offen lds\n\ts_add_u32 m0, %[wl], 33792\n\ts_xor_b32 %[t], %[wo], 0x1c4000\n\tbuffer_load_dwordx4 %[vw1], %[rw], %[t] offen lds\n\t" \
          "s_add_u32 m0, %[wl], 34816\n\ts_xor_b32 %[t], %[wo], 0x1c8000\n\tbuffer_load_dwordx4 %[vw0], %[rw], %[t] offen lds\n\ts_add_u32 m0, %[wl], 35840\n\ts_xor_b32 %[t], %[wo], 0x1cc000\n\tbuffer_load_dwordx4 %[vw1], %[rw], %[t] offen lds\n\t" \
          "s_add_u32 m0, %[wl], 36864\n\ts_xor_b32 %[t], %[wo], 0x1d0000\n\tbuffer_load_dwordx4 %[vw0], %[rw], %[t] offen lds\n\ts_add_u32 m0, %[wl], 37888\n\ts_xor_b32 %[t], %[wo], 0x1d4000\n\tbuffer_load_dwordx4 %[vw1], %[rw], %[t] offen lds\n\t" \
          "s_add_u32 m0, %[wl], 38912\n\ts_xor_b32 %[t], %[wo], 0x1d8000\n\tbuffer_load_dwordx4 %[vw0], %[rw], %[t] offen lds\n\ts_add_u32 m0, %[wl], 39936\n\ts_xor_b32 %[t], %[wo], 0x1dc000\n\tbuffer_load_dwordx4 %[vw1], %[rw], %[t] offen lds\n\t" \
          "s_waitcnt vmcnt(32)\n\tds_read_b64_tr_b16 a[160:161], %[rt0] offset:0\n\tds_read_b64_tr_b16 a[162:163], %[rt0] offset:512\n\tds_read_b64_tr_b16 a[164:165], %[rt1] offset:0\n\tds_read_b64_tr_b16 a[166:167], %[rt1] offset:512\n\tds_read_b64_tr_b16 v[208:209], %[rt2] offset:0\n\t" \
          "ds_read_b64_tr_b16 v[210:211], %[rt2] offset:512\n\tds_read_b64_tr_b16 v[112:113], %[rt3] offset:0\n\tds_read_b64_tr_b16 v[114:115], %[rt3] offset:512\n\tds_read_b64_tr_b16 a[168:169], %[rt0] offset:4096\n\tds_read_b64_tr_b16 a[170:171], %[rt0] offset:4608\n\tds_read_b64_tr_b16 a[172:173], %[rt1] offset:4096\n\t" \
          "ds_read_b64_tr_b16 a[174:175], %[rt1] offset:4608\n\tds_read_b64_tr_b16 v[212:213], %[rt2] offset:4096\n\tds_read_b64_tr_b16 v[214:215], %[rt2] offset:4608\n\tds_read_b64_tr_b16 v[116:117], %[rt3] offset:4096\n\tds_read_b64_tr_b16 v[118:119], %[rt3] offset:4608\n\ts_waitcnt lgkmcnt(0)\n\t" \
          "s_add_u32 m0, %[wl], 0\n\ts_xor_b32 %[t], %[wo], 0x1e0000\n\tbuffer_load_dwordx4 %[vw0], %[rw], %[t] offen lds\n\ts_add_u32 m0, %[wl], 1024\n\ts_xor_b32 %[t], %[wo], 0x1e4000\n\tbuffer_load_dwordx4 %[vw1], %[rw], %[t] offen lds\n\t" \
          "s_add_u32 m0, %[wl], 2048\n\ts_xor_b32 %[t], %[wo], 0x1e8000\n\tbuffer_load_dwordx4 %[vw0], %[rw], %[t] offen lds\n\ts_add_u32 m0, %[wl], 3072\n\ts_xor_b32 %[t], %[wo], 0x1ec000\n\tbuffer_load_dwordx4 %[vw1], %[rw], %[t] offen lds\n\t" \
          "s_add_u32 m0, %[wl], 4096\n\ts_xor_b32 %[t], %[wo], 0x1f0000\n\tbuffer_load_dwordx4 %[vw0], %[rw], %[t] offen lds\n\ts_add_u32 m0, %[wl], 5120\n\ts_xor_b32 %[t], %[wo], 0x1f4000\n\tbuffer_load_dwordx4 %[vw1], %[rw], %[t] offen lds\n\t" \
          "s_add_u32 m0, %[wl], 6144\n\ts_xor_b32 %[t], %[wo], 0x1f8000\n\tbuffer_load_dwordx4 %[vw0], %[rw], %[t] offen lds\n\ts_add_u32 m0, %[wl], 7168\n\ts_xor_b32 %[t], %[wo], 0x1fc000\n\tbuffer_load_dwordx4 %[vw1], %[rw], %[t] offen lds\n\t" \
          "s_waitcnt vmcnt(32)\n\tds_read_b64_tr_b16 a[176:177], %[rt0] offset:8192\n\tds_read_b64_tr_b16 a[178:179], %[rt0] offset:8704\n\tds_read_b64_tr_b16 a[180:181], %[rt1] offset:8192\n\tds_read_b64_tr_b16 a[182:183], %[rt1] offset:8704\n\tds_read_b64_tr_b16 v[216:217], %[rt2] offset:8192\n\t" \
          "ds_read_b64_tr_b16 v[218:219], %[rt2] offset:8704\n\tds_read_b64_tr_b16 v[120:121], %[rt3] offset:8192\n\tds_read_b64_tr_b16 v[122:123], %[rt3] offset:8704\n\tds_read_b64_tr_b16 a[184:185], %[rt0] offset:12288\n\tds_read_b64_tr_b16 a[186:187], %[rt0] offset:12800\n\tds_read_b64_tr_b16 a[188:189], %[rt1] offset:12288\n\t" \
          "ds_read_b64_tr_b16 a[190:191], %[rt1] offset:12800\n\tds_read_b64_tr_b16 v[220:221], %[rt2] offset:12288\n\tds_read_b64_tr_b16 v[222:223], %[rt2] offset:12800\n\tds_read_b64_tr_b16 v[124:125], %[rt3] offset:12288\n\tds_read_b64_tr_b16 v[126:127], %[rt3] offset:12800\n\ts_waitcnt lgkmcnt(0)\n\t" \
          "ds_write_b128 %[own], v[32:35] offset:8192\n\tds_write_b128 %[own], v[36:39] offset:9216\n\tds_write_b128 %[own], v[40:43] offset:10240\n\tds_write_b128 %[own], v[44:47] offset:11264\n\tds_write_b128 %[own], v[48:51] offset:12288\n\tds_write_b128 %[own], v[52:55] offset:13312\n\t" \
          "ds_write_b128 %[own], v[56:59] offset:14336\n\tds_write_b128 %[own], v[60:63] offset:15360\n\ts_waitcnt lgkmcnt(0)\n\ts_waitcnt vmcnt(24)\n\tds_read_b64_tr_b16 a[192:193], %[rt0] offset:16384\n\tds_read_b64_tr_b16 a[194:195], %[rt0] offset:16896\n\t" \
          "ds_read_b64_tr_b16 a[196:197], %[rt1] offset:16384\n\tds_read_b64_tr_b16 a[198:199], %[rt1] offset:16896\n\tds_read_b64_tr_b16 v[224:225], %[rt2] offset:16384\n\tds_read_b64_tr_b16 v[226:227], %[rt2] offset:16896\n\tds_read_b64_tr_b16 v[32:33], %[rt3] offset:16384\n\tds_read_b64_tr_b16 v[34:35], %[rt3] offset:16896\n\t" \
          "ds_read_b64_tr_b16 a[200:201], %[rt0] offset:20480\n\tds_read_b64_tr_b16 a[202:203], %[rt0] offset:20992\n\tds_read_b64_tr_b16 a[204:205], %[rt1] offset:20480\n\tds_read_b64_tr_b16 a[206:207], %[rt1] offset:20992\n\tds_read_b64_tr_b16 v[228:229], %[rt2] offset:20480\n\tds_read_b64_tr_b16 v[230:231], %[rt2] offset:20992\n\t" \
          "ds_read_b64_tr_b16 v[36:37], %[rt3] offset:20480\n\tds_read_b64_tr_b16 v[38:39], %[rt3] offset:20992\n\ts_waitcnt lgkmcnt(0)\n\tds_write_b128 %[own], v[64:67] offset:16384\n\tds_write_b128 %[own], v[68:71] offset:17408\n\tds_write_b128 %[own], v[72:75] offset:18432\n\t" \
          "ds_write_b128 %[own], v[76:79] offset:19456\n\tds_write_b128 %[own], v[80:83] offset:20480\n\tds_write_b128 %[own], v[84:87] offset:21504\n\tds_write_b128 %[own], v[88:91] offset:22528\n\tds_write_b128 %[own], v[92:95] offset:23552\n\ts_waitcnt lgkmcnt(0)\n\t" \
          "s_waitcnt vmcnt(16)\n\tds_read_b64_tr_b16 a[208:209], %[rt0] offset:24576\n\tds_read_b64_tr_b16 a[210:211], %[rt0] offset:25088\n\tds_read_b64_tr_b16 a[212:213], %[rt1] offset:24576\n\tds_read_b64_tr_b16 a[214:215], %[rt1] offset:25088\n\tds_read_b64_tr_b16 v[232:233], %[rt2] offset:24576\n\t" \
          "ds_read_b64_tr_b16 v[234:235], %[rt2] offset:25088\n\tds_read_b64_tr_b16 v[40:41], %[rt3] offset:24576\n\tds_read_b64_tr_b16 v[42:43], %[rt3] offset:25088\n\tds_read_b64_tr_b16 a[216:217], %[rt0] offset:28672\n\tds_read_b64_tr_b16 a[218:219], %[rt0] offset:29184\n\tds_read_b64_tr_b16 a[220:221], %[rt1] offset:28672\n\t" \
          "ds_read_b64_tr_b16 a[222:223], %[rt1] offset:29184\n\tds_read_b64_tr_b16 v[236:237], %[rt2] offset:28672\n\tds_read_b64_tr_b16 v[238:239], %[rt2] offset:29184\n\tds_read_b64_tr_b16 v[44:45], %[rt3] offset:28672\n\tds_read_b64_tr_b16 v[46:47], %[rt3] offset:29184\n\ts_waitcnt lgkmcnt(0)\n\t" \
          "ds_write_b128 %[own], v[96:99] offset:24576\n\tds_write_b128 %[own], v[100:103] offset:25600\n\tds_write_b128 %[own], v[104:107] offset:26624\n\tds_write_b128 %[own], v[108:111] offset:27648\n\tds_write_b128 %[own], v[112:115] offset:28672\n\tds_write_b128 %[own], v[116:119] offset:29696\n\t" \
          "ds_write_b128 %[own], v[120:123] offset:30720\n\tds_write_b128 %[own], v[124:127] offset:31744\n\ts_waitcnt lgkmcnt(0)\n\ts_waitcnt vmcnt(8)\n\tds_read_b64_tr_b16 a[224:225], %[rt0] offset:32768\n\tds_read_b64_tr_b16 a[226:227], %[rt0] offset:33280\n\t" \
          "ds_read_b64_tr_b16 a[228:229], %[rt1] offset:32768\n\tds_read_b64_tr_b16 a[230:231], %[rt1] offset:33280\n\tds_read_b64_tr_b16 v[240:241], %[rt2] offset:32768\n\tds_read_b64_tr_b16 v[242:243], %[rt2] offset:33280\n\tds_read_b64_tr_b16 v[48:49], %[rt3] offset:32768\n\tds_read_b64_tr_b16 v[50:51], %[rt3] offset:33280\n\t" \
          "ds_read_b64_tr_b16 a[232:233], %[rt0] offset:36864\n\tds_read_b64_tr_b16 a[234:235], %[rt0] offset:37376\n\tds_read_b64_tr_b16 a[236:237], %[rt1] offset:36864\n\tds_read_b64_tr_b16 a[238:239], %[rt1] offset:37376\n\tds_read_b64_tr_b16 v[244:245], %[rt2] offset:36864\n\tds_read_b64_tr_b16 v[246:247], %[rt2] offset:37376\n\t" \
          "ds_read_b64_tr_b16 v[52:53], %[rt3] offset:36864\n\tds_read_b64_tr_b16 v[54:55], %[rt3] offset:37376\n\ts_waitcnt lgkmcnt(0)\n\ts_waitcnt vmcnt(0)\n\tds_read_b64_tr_b16 a[240:241], %[rt0] offset:0\n\tds_read_b64_tr_b16 a[242:243], %[rt0] offset:512\n\t" \
          "ds_read_b64_tr_b16 a[244:245], %[rt1] offset:0\n\tds_read_b64_tr_b16 a[246:247], %[rt1] offset:512\n\tds_read_b64_tr_b16 v[248:249], %[rt2] offset:0\n\tds_read_b64_tr_b16 v[250:251], %[rt2] offset:512\n\tds_read_b64_tr_b16 v[56:57], %[rt3] offset:0\n\tds_read_b64_tr_b16 v[58:59], %[rt3] offset:512\n\t" \
          "ds_read_b64_tr_b16 a[248:249], %[rt0] offset:4096\n\tds_read_b64_tr_b16 a[250:251], %[rt0] offset:4608\n\tds_read_b64_tr_b16 a[252:253], %[rt1] offset:4096\n\tds_read_b64_tr_b16 a[254:255], %[rt1] offset:4608\n\tds_read_b64_tr_b16 v[252:253], %[rt2] offset:4096\n\tds_read_b64_tr_b16 v[254:255], %[rt2] offset:4608\n\t" \
          "ds_read_b64_tr_b16 v[60:61], %[rt3] offset:4096\n\tds_read_b64_tr_b16 v[62:63], %[rt3] offset:4608\n\ts_waitcnt lgkmcnt(0)\n\tds_write_b128 %[own], v[32:35] offset:0\n\tds_write_b128 %[own], v[36:39] offset:1024\n\tds_write_b128 %[own], v[40:43] offset:2048\n\t" \
          "ds_write_b128 %[own], v[44:47] offset:3072\n\tds_write_b128 %[own], v[48:51] offset:4096\n\tds_write_b128 %[own], v[52:55] offset:5120\n\tds_write_b128 %[own], v[56:59] offset:6144\n\tds_write_b128 %[own], v[60:63] offset:7168\n\ts_waitcnt lgkmcnt(0)\n\t" \
          "Lou_wait%=:\n\ts_mov_b32 %[cnt], 0\n\tLou_poll%=:\n\ts_lshr_b32 %[t], %[j], 1\n\ts_cmp_lt_u32 %[t], %[av]\n\ts_cbranch_scc1 Lou_load%=\n\t" \
          "s_sleep 4\n\ts_lshl_b32 %[t], %[av], 7\n\ts_add_u32 %[t], %[t], %[fo]\n\tbuffer_load_dword %[fl], %[vf], %[rf], %[t] offen sc1\n\tbuffer_load_dword v48, off, %[rf], %[tmo] sc1\n\ts_waitcnt vmcnt(0)\n\t" \
          "v_cmp_gt_u32 vcc, 8, %[fl]\n\ts_nop 0\n\ts_ff1_i32_b64 %[t], vcc\n\ts_add_u32 %[av], %[av], %[t]\n\tv_readfirstlane_b32 vcc_lo, v48\n\ts_add_u32 %[cnt], %[cnt], 1\n\t" \
          "s_lshr_b32 vcc_hi, %[cnt], 15\n\ts_or_b32 vcc_lo, vcc_lo, vcc_hi\n\ts_cbranch_scc0 Lou_poll%=\n\tv_mov_b32 v48, 7\n\tbuffer_store_dword v48, off, %[rf], %[tmo] sc1\n\ts_mov_b32 %[av], 64\n\t" \
          "Lou_load%=:\n\tbuffer_load_dwordx4 v[64:67], %[vo0], s[40:43], %[pg0] offen sc1\n\tbuffer_load_dwordx4 v[68:71], %[vo0], s[40:43], %[pg0] offen offset:1024 sc1\n\tbuffer_load_dwordx4 v[72:75], %[vo0], s[40:43], %[pg0] offen offset:2048 sc1\n\tbuffer_load_dwordx4 v[76:79], %[vo0], s[40:43], %[pg0] offen offset:3072 sc1\n\tbuffer_load_dwordx4 v[80:83], %[vo1], s[40:43], %[pg0] offen sc1\n\t" \
          "buffer_load_dwordx4 v[84:87], %[vo1], s[40:43], %[pg0] offen offset:1024 sc1\n\tbuffer_load_dwordx4 v[88:91], %[vo1], s[40:43], %[pg0] offen offset:2048 sc1\n\tbuffer_load_dwordx4 v[92:95], %[vo1], s[40:43], %[pg0] offen offset:3072 sc1\n\tbuffer_load_dwordx4 v[96:99], %[vo0], s[40:43], %[pg1] offen sc1\n\tbuffer_load_dwordx4 v[100:103], %[vo0], s[40:43], %[pg1] offen offset:1024 sc1\n\tbuffer_load_dwordx4 v[104:107], %[vo0], s[40:43], %[pg1] offen offset:2048 sc1\n\t" \
          "buffer_load_dwordx4 v[108:111], %[vo0], s[40:43], %[pg1] offen offset:3072 sc1\n\tbuffer_load_dwordx4 v[112:115], %[vo1], s[40:43], %[pg1] offen sc1\n\tbuffer_load_dwordx4 v[116:119], %[vo1], s[40:43], %[pg1] offen offset:1024 sc1\n\tbuffer_load_dwordx4 v[120:123], %[vo1], s[40:43], %[pg1] offen offset:2048 sc1\n\tbuffer_load_dwordx4 v[124:127], %[vo1], s[40:43], %[pg1] offen offset:3072 sc1\n\tLou_top%=:\n\t" \
          "s_lshl_b32 %[t], %[av], 7\n\ts_add_u32 %[t], %[t], %[fo]\n\tbuffer_load_dword %[fl], %[vf], %[rf], %[t] offen sc1\n\tds_read_b128 v[32:35], %[own] offset:8192\n\tds_read_b128 v[36:39], %[own] offset:9216\n\tds_read_b128 v[40:43], %[own] offset:10240\n\t" \
          "s_waitcnt vmcnt(16) lgkmcnt(2)\n\tv_mfma_f32_16x16x32_bf16 v[48:51], a[0:3], v[64:67], 0\n\tds_read_b128 v[44:47], %[own] offset:11264\n\tv_mfma_f32_16x16x32_bf16 v[52:55], a[4:7], v[64:67], 0\n\tv_mfma_f32_16x16x32_bf16 v[56:59], v[128:131], v[64:67], 0\n\tv_mfma_f32_16x16x32_bf16 v[60:63], v[32:35], v[64:67], 0\n\t" \
          "buffer_load_dwordx4 v[64:67], %[vo0], s[40:43], %[pg2] offen sc1\n\ts_waitcnt vmcnt(16) lgkmcnt(2)\n\tv_mfma_f32_16x16x32_bf16 v[48:51], a[8:11], v[68:71], v[48:51]\n\tds_read_b128 v[32:35], %[own] offset:12288\n\tv_mfma_f32_16x16x32_bf16 v[52:55], a[12:15], v[68:71], v[52:55]\n\tv_mfma_f32_16x16x32_bf16 v[56:59], v[132:135], v[68:71], v[56:59]\n\t" \
          "v_mfma_f32_16x16x32_bf16 v[60:63], v[36:39], v[68:71], v[60:63]\n\tbuffer_load_dwordx4 v[68:71], %[vo0], s[40:43], %[pg2] offen offset:1024 sc1\n\ts_waitcnt vmcnt(16) lgkmcnt(2)\n\tv_mfma_f32_16x16x32_bf16 v[48:51], a[16:19], v[72:75], v[48:51]\n\tds_read_b128 v[36:39], %[own] offset:13312\n\tv_mfma_f32_16x16x32_bf16 v[52:55], a[20:23], v[72:75], v[52:55]\n\t" \
          "v_mfma_f32_16x16x32_bf16 v[56:59], v[136:139], v[72:75], v[56:59]\n\tv_mfma_f32_16x16x32_bf16 v[60:63], v[40:43], v[72:75], v[60:63]\n\tbuffer_load_dwordx4 v[72:75], %[vo0], s[40:43], %[pg2] offen offset:2048 sc1\n\ts_waitcnt vmcnt(16) lgkmcnt(2)\n\tv_mfma_f32_16x16x32_bf16 v[48:51], a[24:27], v[76:79], v[48:51]\n\tds_read_b128 v[40:43], %[own] offset:14336\n\t" \
          "v_mfma_f32_16x16x32_bf16 v[52:55], a[28:31], v[76:79], v[52:55]\n\tv_mfma_f32_16x16x32_bf16 v[56:59], v[140:143], v[76:79], v[56:59]\n\tv_mfma_f32_16x16x32_bf16 v[60:63], v[44:47], v[76:79], v[60:63]\n\tbuffer_load_dwordx4 v[76:79], %[vo0], s[40:43], %[pg2] offen offset:3072 sc1\n\ts_waitcnt vmcnt(16) lgkmcnt(2)\n\tv_mfma_f32_16x16x32_bf16 v[48:51], a[32:35], v[80:83], v[48:51]\n\t" \
          "ds_read_b128 v[44:47], %[own] offset:15360\n\tv_mfma_f32_16x16x32_bf16 v[52:55], a[36:39], v[80:83], v[52:55]\n\tv_mfma_f32_16x16x32_bf16 v[56:59], v[144:147], v[80:83], v[56:59]\n\tv_mfma_f32_16x16x32_bf16 v[60:63], v[32:35], v[80:83], v[60:63]\n\tbuffer_load_dwordx4 v[80:83], %[vo1], s[40:43], %[pg2] offen sc1\n\ts_waitcnt vmcnt(16) lgkmcnt(2)\n\t" \
          "v_mfma_f32_16x16x32_bf16 v[48:51], a[40:43], v[84:87], v[48:51]\n\tds_read_b128 v[32:35], %[own] offset:16384\n\tv_mfma_f32_16x16x32_bf16 v[52:55], a[44:47], v[84:87], v[52:55]\n\tv_mfma_f32_16x16x32_bf16 v[56:59], v[148:151], v[84:87], v[56:59]\n\tv_mfma_f32_16x16x32_bf16 v[60:63], v[36:39], v[84:87], v[60:63]\n\tbuffer_load_dwordx4 v[84:87], %[vo1], s[40:43], %[pg2] offen offset:1024 sc1\n\t" \
          "s_waitcnt vmcnt(16) lgkmcnt(2)\n\tv_mfma_f32_16x16x32_bf16 v[48:51], a[48:51], v[88:91], v[48:51]\n\tds_read_b128 v[36:39], %[own] offset:17408\n\tv_mfma_f32_16x16x32_bf16 v[52:55], a[52:55], v[88:91], v[52:55]\n\tv_mfma_f32_16x16x32_bf16 v[56:59], v[152:155], v[88:91], v[56:59]\n\tv_mfma_f32_16x16x32_bf16 v[60:63], v[40:43], v[88:91], v[60:63]\n\t" \
          "buffer_load_dwordx4 v[88:91], %[vo1], s[40:43], %[pg2] offen offset:2048 sc1\n\ts_waitcnt vmcnt(16) lgkmcnt(2)\n\tv_mfma_f32_16x16x32_bf16 v[48:51], a[56:59], v[92:95], v[48:51]\n\tds_read_b128 v[40:43], %[own] offset:18432\n\tv_mfma_f32_16x16x32_bf16 v[52:55], a[60:63], v[92:95], v[52:55]\n\tv_mfma_f32_16x16x32_bf16 v[56:59], v[156:159], v[92:95], v[56:59]\n\t" \
          "v_mfma_f32_16x16x32_bf16 v[60:63], v[44:47], v[92:95], v[60:63]\n\tbuffer_load_dwordx4 v[92:95], %[vo1], s[40:43], %[pg2] offen offset:3072 sc1\n\ts_waitcnt vmcnt(16) lgkmcnt(2)\n\tv_mfma_f32_16x16x32_bf16 v[48:51], a[64:67], v[96:99], v[48:51]\n\tds_read_b128 v[44:47], %[own] offset:19456\n\tv_mfma_f32_16x16x32_bf16 v[52:55], a[68:71], v[96:99], v[52:55]\n\t" \
          "v_mfma_f32_16x16x32_bf16 v[56:59], v[160:163], v[96:99], v[56:59]\n\tv_mfma_f32_16x16x32_bf16 v[60:63], v[32:35], v[96:99], v[60:63]\n\tbuffer_load_dwordx4 v[96:99], %[vo0], s[40:43], %[pg3] offen sc1\n\ts_waitcnt vmcnt(16) lgkmcnt(2)\n\tv_mfma_f32_16x16x32_bf16 v[48:51], a[72:75], v[100:103], v[48:51]\n\tds_read_b128 v[32:35], %[own] offset:20480\n\t" \
          "v_mfma_f32_16x16x32_bf16 v[52:55], a[76:79], v[100:103], v[52:55]\n\tv_mfma_f32_16x16x32_bf16 v[56:59], v[164:167], v[100:103], v[56:59]\n\tv_mfma_f32_16x16x32_bf16 v[60:63], v[36:39], v[100:103], v[60:63]\n\tbuffer_load_dwordx4 v[100:103], %[vo0], s[40:43], %[pg3] offen offset:1024 sc1\n\ts_waitcnt vmcnt(16) lgkmcnt(2)\n\tv_mfma_f32_16x16x32_bf16 v[48:51], a[80:83], v[104:107], v[48:51]\n\t" \
          "ds_read_b128 v[36:39], %[own] offset:21504\n\tv_mfma_f32_16x16x32_bf16 v[52:55], a[84:87], v[104:107], v[52:55]\n\tv_mfma_f32_16x16x32_bf16 v[56:59], v[168:171], v[104:107], v[56:59]\n\tv_mfma_f32_16x16x32_bf16 v[60:63], v[40:43], v[104:107], v[60:63]\n\tbuffer_load_dwordx4 v[104:107], %[vo0], s[40:43], %[pg3] offen offset:2048 sc1\n\ts_waitcnt vmcnt(16) lgkmcnt(2)\n\t" \
          "v_mfma_f32_16x16x32_bf16 v[48:51], a[88:91], v[108:111], v[48:51]\n\tds_read_b128 v[40:43], %[own] offset:22528\n\tv_mfma_f32_16x16x32_bf16 v[52:55], a[92:95], v[108:111], v[52:55]\n\tv_mfma_f32_16x16x32_bf16 v[56:59], v[172:175], v[108:111], v[56:59]\n\tv_mfma_f32_16x16x32_bf16 v[60:63], v[44:47], v[108:111], v[60:63]\n\tbuffer_load_dwordx4 v[108:111], %[vo0], s[40:43], %[pg3] offen offset:3072 sc1\n\t" \
          "s_waitcnt vmcnt(16) lgkmcnt(2)\n\tv_mfma_f32_16x16x32_bf16 v[48:51], a[96:99], v[112:115], v[48:51]\n\tds_read_b128 v[44:47], %[own] offset:23552\n\tv_mfma_f32_16x16x32_bf16 v[52:55], a[100:103], v[112:115], v[52:55]\n\tv_mfma_f32_16x16x32_bf16 v[56:59], v[176:179], v[112:115], v[56:59]\n\tv_mfma_f32_16x16x32_bf16 v[60:63], v[32:35], v[112:115], v[60:63]\n\t" \
          "buffer_load_dwordx4 v[112:115], %[vo1], s[40:43], %[pg3] offen sc1\n\ts_waitcnt vmcnt(16) lgkmcnt(2)\n\tv_mfma_f32_16x16x32_bf16 v[48:51], a[104:107], v[116:119], v[48:51]\n\tds_read_b128 v[32:35], %[own] offset:24576\n\tv_mfma_f32_16x16x32_bf16 v[52:55], a[108:111], v[116:119], v[52:55]\n\tv_mfma_f32_16x16x32_bf16 v[56:59], v[180:183], v[116:119], v[56:59]\n\t" \
          "v_mfma_f32_16x16x32_bf16 v[60:63], v[36:39], v[116:119], v[60:63]\n\tbuffer_load_dwordx4 v[116:119], %[vo1], s[40:43], %[pg3] offen offset:1024 sc1\n\ts_waitcnt vmcnt(16) lgkmcnt(2)\n\tv_mfma_f32_16x16x32_bf16 v[48:51], a[112:115], v[120:123], v[48:51]\n\tds_read_b128 v[36:39], %[own] offset:25600\n\tv_mfma_f32_16x16x32_bf16 v[52:55], a[116:119], v[120:123], v[52:55]\n\t" \
          "v_mfma_f32_16x16x32_bf16 v[56:59], v[184:187], v[120:123], v[56:59]\n\tv_mfma_f32_16x16x32_bf16 v[60:63], v[40:43], v[120:123], v[60:63]\n\tbuffer_load_dwordx4 v[120:123], %[vo1], s[40:43], %[pg3] offen offset:2048 sc1\n\ts_waitcnt vmcnt(16) lgkmcnt(2)\n\tv_mfma_f32_16x16x32_bf16 v[48:51], a[120:123], v[124:127], v[48:51]\n\tds_read_b128 v[40:43], %[own] offset:26624\n\t" \
          "v_mfma_f32_16x16x32_bf16 v[52:55], a[124:127], v[124:127], v[52:55]\n\tv_mfma_f32_16x16x32_bf16 v[56:59], v[188:191], v[124:127], v[56:59]\n\tv_mfma_f32_16x16x32_bf16 v[60:63], v[44:47], v[124:127], v[60:63]\n\tbuffer_load_dwordx4 v[124:127], %[vo1], s[40:43], %[pg3] offen offset:3072 sc1\n\ts_waitcnt vmcnt(15) lgkmcnt(2)\n\tv_mfma_f32_16x16x32_bf16 v[48:51], a[128:131], v[64:67], v[48:51]\n\t" \
          "ds_read_b128 v[44:47], %[own] offset:27648\n\tv_mfma_f32_16x16x32_bf16 v[52:55], a[132:135], v[64:67], v[52:55]\n\tv_cmp_gt_u32 vcc, 8, %[fl]\n\ts_nop 0\n\ts_ff1_i32_b64 %[t], vcc\n\ts_add_u32 %[av], %[av], %[t]\n\t" \
          "v_mfma_f32_16x16x32_bf16 v[56:59], v[192:195], v[64:67], v[56:59]\n\ts_lshl_b32 %[oo], %[j], 16\n\ts_cmp_lt_u32 %[j], %[j1]\n\ts_cselect_b32 %[t], 2, 4\n\ts_add_u32 %[j], %[j], %[t]\n\ts_lshl_b32 %[t], %[t], 14\n\t" \
          "s_add_u32 %[pg0], %[pg0], %[t]\n\ts_add_u32 %[pg1], %[pg1], %[t]\n\ts_add_u32 %[pg2], %[pg2], %[t]\n\ts_add_u32 %[pg3], %[pg3], %[t]\n\ts_lshr_b32 %[t], %[j], 1\n\ts_cmp_lt_u32 %[t], %[av]\n\t" \
          "s_cselect_b32 s46, s42, 0\n\tv_mfma_f32_16x16x32_bf16 v[60:63], v[32:35], v[64:67], v[60:63]\n\tbuffer_load_dwordx4 v[64:67], %[vo0], s[44:47], %[pg0] offen sc1\n\ts_waitcnt vmcnt(15) lgkmcnt(2)\n\tv_mfma_f32_16x16x32_bf16 v[48:51], a[136:139], v[68:71], v[48:51]\n\tds_read_b128 v[32:35], %[own] offset:28672\n\t" \
          "v_mfma_f32_16x16x32_bf16 v[52:55], a[140:143], v[68:71], v[52:55]\n\tv_mfma_f32_16x16x32_bf16 v[56:59], v[196:199], v[68:71], v[56:59]\n\tv_mfma_f32_16x16x32_bf16 v[60:63], v[36:39], v[68:71], v[60:63]\n\tbuffer_load_dwordx4 v[68:71], %[vo0], s[44:47], %[pg0] offen offset:1024 sc1\n\ts_waitcnt vmcnt(15) lgkmcnt(2)\n\tv_mfma_f32_16x16x32_bf16 v[48:51], a[144:147], v[72:75], v[48:51]\n\t" \
          "ds_read_b128 v[36:39], %[own] offset:29696\n\tv_mfma_f32_16x16x32_bf16 v[52:55], a[148:151], v[72:75], v[52:55]\n\tv_mfma_f32_16x16x32_bf16 v[56:59], v[200:203], v[72:75], v[56:59]\n\tv_mfma_f32_16x16x32_bf16 v[60:63], v[40:43], v[72:75], v[60:63]\n\tbuffer_load_dwordx4 v[72:75], %[vo0], s[44:47], %[pg0] offen offset:2048 sc1\n\ts_waitcnt vmcnt(15) lgkmcnt(2)\n\t" \
          "v_mfma_f32_16x16x32_bf16 v[48:51], a[152:155], v[76:79], v[48:51]\n\tds_read_b128 v[40:43], %[own] offset:30720\n\tv_mfma_f32_16x16x32_bf16 v[52:55], a[156:159], v[76:79], v[52:55]\n\tv_mfma_f32_16x16x32_bf16 v[56:59], v[204:207], v[76:79], v[56:59]\n\tv_mfma_f32_16x16x32_bf16 v[60:63], v[44:47], v[76:79], v[60:63]\n\tbuffer_load_dwordx4 v[76:79], %[vo0], s[44:47], %[pg0] offen offset:3072 sc1\n\t" \
          "s_waitcnt vmcnt(15) lgkmcnt(2)\n\tv_mfma_f32_16x16x32_bf16 v[48:51], a[160:163], v[80:83], v[48:51]\n\tds_read_b128 v[44:47], %[own] offset:31744\n\tv_mfma_f32_16x16x32_bf16 v[52:55], a[164:167], v[80:83], v[52:55]\n\tv_mfma_f32_16x16x32_bf16 v[56:59], v[208:211], v[80:83], v[56:59]\n\tv_mfma_f32_16x16x32_bf16 v[60:63], v[32:35], v[80:83], v[60:63]\n\t" \
          "buffer_load_dwordx4 v[80:83], %[vo1], s[44:47], %[pg0] offen sc1\n\ts_waitcnt vmcnt(15) lgkmcnt(2)\n\tv_mfma_f32_16x16x32_bf16 v[48:51], a[168:171], v[84:87], v[48:51]\n\tds_read_b128 v[32:35], %[own] offset:0\n\tv_mfma_f32_16x16x32_bf16 v[52:55], a[172:175], v[84:87], v[52:55]\n\tv_mfma_f32_16x16x32_bf16 v[56:59], v[212:215], v[84:87], v[56:59]\n\t" \
          "v_mfma_f32_16x16x32_bf16 v[60:63], v[36:39], v[84:87], v[60:63]\n\tbuffer_load_dwordx4 v[84:87], %[vo1], s[44:47], %[pg0] offen offset:1024 sc1\n\ts_waitcnt vmcnt(15) lgkmcnt(2)\n\tv_mfma_f32_16x16x32_bf16 v[48:51], a[176:179], v[88:91], v[48:51]\n\tds_read_b128 v[36:39], %[own] offset:1024\n\tv_mfma_f32_16x16x32_bf16 v[52:55], a[180:183], v[88:91], v[52:55]\n\t" \
          "v_mfma_f32_16x16x32_bf16 v[56:59], v[216:219], v[88:91], v[56:59]\n\tv_mfma_f32_16x16x32_bf16 v[60:63], v[40:43], v[88:91], v[60:63]\n\tbuffer_load_dwordx4 v[88:91], %[vo1], s[44:47], %[pg0] offen offset:2048 sc1\n\ts_waitcnt vmcnt(15) lgkmcnt(2)\n\tv_mfma_f32_16x16x32_bf16 v[48:51], a[184:187], v[92:95], v[48:51]\n\tds_read_b128 v[40:43], %[own] offset:2048\n\t" \
          "v_mfma_f32_16x16x32_bf16 v[52:55], a[188:191], v[92:95], v[52:55]\n\tv_mfma_f32_16x16x32_bf16 v[56:59], v[220:223], v[92:95], v[56:59]\n\tv_mfma_f32_16x16x32_bf16 v[60:63], v[44:47], v[92:95], v[60:63]\n\tbuffer_load_dwordx4 v[92:95], %[vo1], s[44:47], %[pg0] offen offset:3072 sc1\n\ts_waitcnt vmcnt(15) lgkmcnt(2)\n\tv_mfma_f32_16x16x32_bf16 v[48:51], a[192:195], v[96:99], v[48:51]\n\t" \
          "ds_read_b128 v[44:47], %[own] offset:3072\n\tv_mfma_f32_16x16x32_bf16 v[52:55], a[196:199], v[96:99], v[52:55]\n\tv_mfma_f32_16x16x32_bf16 v[56:59], v[224:227], v[96:99], v[56:59]\n\tv_mfma_f32_16x16x32_bf16 v[60:63], v[32:35], v[96:99], v[60:63]\n\tbuffer_load_dwordx4 v[96:99], %[vo0], s[44:47], %[pg1] offen sc1\n\ts_waitcnt vmcnt(15) lgkmcnt(2)\n\t" \
          "v_mfma_f32_16x16x32_bf16 v[48:51], a[200:203], v[100:103], v[48:51]\n\tds_read_b128 v[32:35], %[own] offset:4096\n\tv_mfma_f32_16x16x32_bf16 v[52:55], a[204:207], v[100:103], v[52:55]\n\tv_mfma_f32_16x16x32_bf16 v[56:59], v[228:231], v[100:103], v[56:59]\n\tv_mfma_f32_16x16x32_bf16 v[60:63], v[36:39], v[100:103], v[60:63]\n\tbuffer_load_dwordx4 v[100:103], %[vo0], s[44:47], %[pg1] offen offset:1024 sc1\n\t" \
          "s_waitcnt vmcnt(15) lgkmcnt(2)\n\tv_mfma_f32_16x16x32_bf16 v[48:51], a[208:211], v[104:107], v[48:51]\n\tds_read_b128 v[36:39], %[own] offset:5120\n\tv_mfma_f32_16x16x32_bf16 v[52:55], a[212:215], v[104:107], v[52:55]\n\tv_mfma_f32_16x16x32_bf16 v[56:59], v[232:235], v[104:107], v[56:59]\n\tv_mfma_f32_16x16x32_bf16 v[60:63], v[40:43], v[104:107], v[60:63]\n\t" \
          "buffer_load_dwordx4 v[104:107], %[vo0], s[44:47], %[pg1] offen offset:2048 sc1\n\ts_waitcnt vmcnt(15) lgkmcnt(2)\n\tv_mfma_f32_16x16x32_bf16 v[48:51], a[216:219], v[108:111], v[48:51]\n\tds_read_b128 v[40:43], %[own] offset:6144\n\tv_mfma_f32_16x16x32_bf16 v[52:55], a[220:223], v[108:111], v[52:55]\n\tv_mfma_f32_16x16x32_bf16 v[56:59], v[236:239], v[108:111], v[56:59]\n\t" \
          "v_mfma_f32_16x16x32_bf16 v[60:63], v[44:47], v[108:111], v[60:63]\n\tbuffer_load_dwordx4 v[108:111], %[vo0], s[44:47], %[pg1] offen offset:3072 sc1\n\ts_waitcnt vmcnt(15) lgkmcnt(2)\n\tv_mfma_f32_16x16x32_bf16 v[48:51], a[224:227], v[112:115], v[48:51]\n\tds_read_b128 v[44:47], %[own] offset:7168\n\tv_mfma_f32_16x16x32_bf16 v[52:55], a[228:231], v[112:115], v[52:55]\n\t" \
          "v_mfma_f32_16x16x32_bf16 v[56:59], v[240:243], v[112:115], v[56:59]\n\tv_mfma_f32_16x16x32_bf16 v[60:63], v[32:35], v[112:115], v[60:63]\n\tbuffer_load_dwordx4 v[112:115], %[vo1], s[44:47], %[pg1] offen sc1\n\ts_waitcnt vmcnt(15) lgkmcnt(2)\n\tv_mfma_f32_16x16x32_bf16 v[48:51], a[232:235], v[116:119], v[48:51]\n\tv_mfma_f32_16x16x32_bf16 v[52:55], a[236:239], v[116:119], v[52:55]\n\t" \
          "v_mfma_f32_16x16x32_bf16 v[56:59], v[244:247], v[116:119], v[56:59]\n\tv_mfma_f32_16x16x32_bf16 v[60:63], v[36:39], v[116:119], v[60:63]\n\tbuffer_load_dwordx4 v[116:119], %[vo1], s[44:47], %[pg1] offen offset:1024 sc1\n\ts_waitcnt vmcnt(15) lgkmcnt(1)\n\tv_mfma_f32_16x16x32_bf16 v[48:51], a[240:243], v[120:123], v[48:51]\n\tv_mfma_f32_16x16x32_bf16 v[52:55], a[244:247], v[120:123], v[52:55]\n\t" \
          "v_mfma_f32_16x16x32_bf16 v[56:59], v[248:251], v[120:123], v[56:59]\n\tv_mfma_f32_16x16x32_bf16 v[60:63], v[40:43], v[120:123], v[60:63]\n\tbuffer_load_dwordx4 v[120:123], %[vo1], s[44:47], %[pg1] offen offset:2048 sc1\n\ts_waitcnt vmcnt(15) lgkmcnt(0)\n\tv_mfma_f32_16x16x32_bf16 v[48:51], a[248:251], v[124:127], v[48:51]\n\tv_mfma_f32_16x16x32_bf16 v[52:55], a[252:255], v[124:127], v[52:55]\n\t" \
          "v_mfma_f32_16x16x32_bf16 v[56:59], v[252:255], v[124:127], v[56:59]\n\tv_mfma_f32_16x16x32_bf16 v[60:63], v[44:47], v[124:127], v[60:63]\n\tbuffer_load_dwordx4 v[124:127], %[vo1], s[44:47], %[pg1] offen offset:3072 sc1\n\ts_nop 6\n\tds_write_b128 %[rw_], v[48:51] offset:0\n\tds_write_b128 %[rw_], v[52:55] offset:1024\n\t" \
          "ds_write_b128 %[rw_], v[56:59] offset:2048\n\tds_write_b128 %[rw_], v[60:63] offset:3072\n\ts_waitcnt lgkmcnt(0)\n\ts_barrier\n\tds_read_b128 v[32:35], %[rr0] offset:0\n\tds_read_b128 v[36:39], %[rr0] offset:40960\n\t" \
          "ds_read_b128 v[40:43], %[rr1] offset:0\n\tds_read_b128 v[44:47], %[rr1] offset:40960\n\tv_xor_b32 %[rw_], 0x1000, %[rw_]\n\tv_xor_b32 %[rr0], 0x1000, %[rr0]\n\tv_xor_b32 %[rr1], 0x1000, %[rr1]\n\ts_waitcnt lgkmcnt(0)\n\t" \
          "v_pk_add_f32 v[32:33], v[32:33], v[36:37]\n\tv_pk_add_f32 v[34:35], v[34:35], v[38:39]\n\tv_pk_add_f32 v[40:41], v[40:41], v[44:45]\n\tv_pk_add_f32 v[42:43], v[42:43], v[46:47]\n\tv_pk_add_f32 v[32:33], v[32:33], v[40:41]\n\tv_pk_add_f32 v[34:35], v[34:35], v[42:43]\n\t" \
          "s_nop 0\n\tv_cvt_pk_bf16_f32 v44, v32, v33\n\tv_cvt_pk_bf16_f32 v45, v34, v35\n\tbuffer_store_dwordx2 v[44:45], %[so], %[rc], %[oo] offen\n\ts_cmp_lg_u32 s46, 0\n\ts_cbranch_scc1 Lou_top%=\n\t" \
          "s_cmp_lt_u32 %[j], 128\n\ts_cbranch_scc1 Lou_wait%=\n\ts_waitcnt vmcnt(0)\n\t"
#define OU_CLOB "v32", "v33", "v34", "v35", "v36", "v37", "v38", "v39", "v40", "v41", "v42", "v43", "v44", "v45", "v46", "v47", "v48", "v49", "v50", "v51", "v52", "v53", "v54", "v55", "v56", "v57", "v58", "v59", "v60", "v61", "v62", "v63", "v64", "v65", "v66", "v67", "v68", "v69", "v70", "v71", "v72", "v73", "v74", "v75", "v76", "v77", "v78", "v79", "v80", "v81", "v82", "v83", "v84", "v85", "v86", "v87", "v88", "v89", "v90", "v91", "v92", "v93", "v94", "v95", "v96", "v97", "v98", "v99", "v100", "v101", "v102", "v103", "v104", "v105", "v106", "v107", "v108", "v109", "v110", "v111", "v112", "v113", "v114", "v115", "v116", "v117", "v118", "v119", "v120", "v121", "v122", "v123", "v124", "v125", "v126", "v127", "v128", "v129", "v130", "v131", "v132", "v133", "v134", "v135", "v136", "v137", "v138", "v139", "v140", "v141", "v142", "v143", "v144", "v145", "v146", "v147", "v148", "v149", "v150", "v151", "v152", "v153", "v154", "v155", "v156", "v157", "v158", "v159", "v160", "v161", "v162", "v163", "v164", "v165", "v166", "v167", "v168", "v169", "v170", "v171", "v172", "v173", "v174", "v175", "v176", "v177", "v178", "v179", "v180", "v181", "v182", "v183", "v184", "v185", "v186", "v187", "v188", "v189", "v190", "v191", "v192", "v193", "v194", "v195", "v196", "v197", "v198", "v199", "v200", "v201", "v202", "v203", "v204", "v205", "v206", "v207", "v208", "v209", "v210", "v211", "v212", "v213", "v214", "v215", "v216", "v217", "v218", "v219", "v220", "v221", "v222", "v223", "v224", "v225", "v226", "v227", "v228", "v229", "v230", "v231", "v232", "v233", "v234", "v235", "v236", "v237", "v238", "v239", "v240", "v241", "v242", "v243", "v244", "v245", "v246", "v247", "v248", "v249", "v250", "v251", "v252", "v253", "v254", "v255", "a0", "a1", "a2", "a3", "a4", "a5", "a6", "a7", "a8", "a9", "a10", "a11", "a12", "a13", "a14", "a15", "a16", "a17", "a18", "a19", "a20", "a21", "a22", "a23", "a24", "a25", "a26", "a27", "a28", "a29", "a30", "a31", "a32", "a33", "a34", "a35", "a36", "a37", "a38", "a39", "a40", "a41", "a42", "a43", "a44", "a45", "a46", "a47", "a48", "a49", "a50", "a51", "a52", "a53", "a54", "a55", "a56", "a57", "a58", "a59", "a60", "a61", "a62", "a63", "a64", "a65", "a66", "a67", "a68", "a69", "a70", "a71", "a72", "a73", "a74", "a75", "a76", "a77", "a78", "a79", "a80", "a81", "a82", "a83", "a84", "a85", "a86", "a87", "a88", "a89", "a90", "a91", "a92", "a93", "a94", "a95", "a96", "a97", "a98", "a99", "a100", "a101", "a102", "a103", "a104", "a105", "a106", "a107", "a108", "a109", "a110", "a111", "a112", "a113", "a114", "a115", "a116", "a117", "a118", "a119", "a120", "a121", "a122", "a123", "a124", "a125", "a126", "a127", "a128", "a129", "a130", "a131", "a132", "a133", "a134", "a135", "a136", "a137", "a138", "a139", "a140", "a141", "a142", "a143", "a144", "a145", "a146", "a147", "a148", "a149", "a150", "a151", "a152", "a153", "a154", "a155", "a156", "a157", "a158", "a159", "a160", "a161", "a162", "a163", "a164", "a165", "a166", "a167", "a168", "a169", "a170", "a171", "a172", "a173", "a174", "a175", "a176", "a177", "a178", "a179", "a180", "a181", "a182", "a183", "a184", "a185", "a186", "a187", "a188", "a189", "a190", "a191", "a192", "a193", "a194", "a195", "a196", "a197", "a198", "a199", "a200", "a201", "a202", "a203", "a204", "a205", "a206", "a207", "a208", "a209", "a210", "a211", "a212", "a213", "a214", "a215", "a216", "a217", "a218", "a219", "a220", "a221", "a222", "a223", "a224", "a225", "a226", "a227", "a228", "a229", "a230", "a231", "a232", "a233", "a234", "a235", "a236", "a237", "a238", "a239", "a240", "a241", "a242", "a243", "a244", "a245", "a246", "a247", "a248", "a249", "a250", "a251", "a252", "a253", "a254", "a255", "s40", "s41", "s42", "s43", "s44", "s45", "s46", "s47"
__device__ __forceinline__ void out_unit(const FParams& F, unsigned long long parq, int set, int un, int w) {
  const int lane = opaque_lane(), l15 = lane & 15, lq = lane >> 4, r = un & 7;
  const __amdgpu_buffer_rsrc_t rw = __builtin_amdgcn_make_buffer_rsrc(wg::uni(F.Wo + 64 * un), 0, HDA * DM * 2 - 128 * un, 0x00020000);
  const __amdgpu_buffer_rsrc_t rc = __builtin_amdgcn_make_buffer_rsrc(wg::uni(F.out + 64 * un), 0, S_ * 2 * DM * 2 - 128 * un, 0x00020000);
  const __amdgpu_buffer_rsrc_t rf = __builtin_amdgcn_make_buffer_rsrc(F.ctl, 0, sizeof(Ctl), 0x00020000);
  const int own = w * 40960 + lane * 16;
  int j = (set < 2 ? 2 * OSEG0 : F.oj1) + set, av = OSEG0, pg[4], t, cnt, oo, fl, rw_ = own + 32768, rr0 = 32768 + (l15 >> 2) * 1024 + ((l15 & 3) * 16 + 4 * w + lq) * 16, rr1 = rr0 + 81920;
#pragma unroll
  for (int i = 0; i < 4; ++i) pg[i] = __builtin_amdgcn_readfirstlane(qoff(parq, 4 * w + (i ^ (r >> 1))) + j * QBLK);
  const int rt = w * 40960 + (8 * lq + (l15 >> 2)) * 128 + (((l15 >> 3) | ((lq & 1) << 1)) << 5) + (l15 & 3) * 8, vw = (lane >> 3) * 2048 + (((lane & 7) ^ ((lq & 1) << 1)) << 4);
  asm volatile(OU_BODY
    : [j] "+s"(j), [av] "+s"(av), [pg0] "+s"(pg[0]), [pg1] "+s"(pg[1]), [pg2] "+s"(pg[2]), [pg3] "+s"(pg[3]), [t] "=&s"(t), [cnt] "=&s"(cnt), [oo] "=&s"(oo), [fl] "=&v"(fl), [rw_] "+v"(rw_), [rr0] "+v"(rr0), [rr1] "+v"(rr1)
    : [vo0] "v"(lane * 16 + (r & 1) * 4096), [vo1] "v"(lane * 16 + (~r & 1) * 4096), [vw0] "v"(vw), [vw1] "v"(vw ^ 64), [rt0] "v"(rt), [rt1] "v"(rt ^ 32), [rt2] "v"(rt ^ 64), [rt3] "v"(rt ^ 96), [own] "v"(own),
      [vf] "v"(lane < 8 ? lane * 128 : 0x7ffffff0), [so] "v"((4 * w + lq) * 4096 + l15 * 8),
      [qa] "s"(F.qkvA), [qs] "s"(QSIZE), [rw] "s"(rw), [rc] "s"(rc), [rf] "s"(rf), [wl] "s"(w * 40960), [wo] "s"((1024 * w + 128 * r) * 2048), [fo] "s"((int)offsetof(Ctl, cntO)), [tmo] "s"((int)offsetof(Ctl, tmo)), [j1] "s"(F.oj1)
    : OU_CLOB, "vcc", "scc", "memory");
}

__device__ __forceinline__ float dot8(v4u a, v4u b) {
  float d = 0.f;
#pragma unroll
  for (int e = 0; e < 4; ++e) { const unsigned x = a[e], y = b[e]; d = __builtin_amdgcn_fdot2_f32_bf16(__builtin_bit_cast(bfx2, x), __builtin_bit_cast(bfx2, y), d, false); }
  return d;
}
__device__ __forceinline__ void attnB_16(const u16* qkv, __amdgpu_buffer_rsrc_t rsO, int pos0  , int w) {
  const int tid = 64 * w + opaque_lane(), cc = tid & 127, p0 = pos0 + (tid >> 7);
  v4u q[8], k[15], v[15];
#pragma unroll
  for (int i = 0; i < 15; ++i) {
    const int kp = p0 - 14 + 2 * i, kc = kp < 0 ? 0 : kp;
    k[i] = *(const v4u*)(qkv + (size_t)kc * NQKVB + DB + cc * 8);
    v[i] = *(const v4u*)(qkv + (size_t)kc * NQKVB + 2 * DB + cc * 8);
  }
#pragma unroll
  for (int i = 0; i < 8; ++i) q[i] = *(const v4u*)(qkv + (size_t)(p0 + 2 * i) * NQKVB + cc * 8);
  const float scale = 0.17677669529663687f;
#pragma unroll
  for (int i = 0; i < 8; ++i) {
    const int pos = p0 + 2 * i;
    float lg[8], mx = -INFINITY;
#pragma unroll
    for (int w = 0; w < 8; ++w) {
      float d = dot8(q[i], k[i + 7 - w]);
      d += __int_as_float(__builtin_amdgcn_update_dpp(0, __float_as_int(d), 0xB1, 0xf, 0xf, false));
      d += __int_as_float(__builtin_amdgcn_update_dpp(0, __float_as_int(d), 0x4E, 0xf, 0xf, false));
      lg[w] = d;
    }
#pragma unroll
    for (int w = 0; w < 8; w += 2) {
      unsigned p = pk2(lg[w], lg[w + 1]);
      p = pk2(__uint_as_float(p << 16) * scale, __uint_as_float(p & 0xffff0000u) * scale);
      lg[w] = __uint_as_float(p << 16); lg[w + 1] = __uint_as_float(p & 0xffff0000u);
    }
#pragma unroll
    for (int w = 0; w < 8; ++w) if (pos - 2 * w >= 0) mx = fmaxf(mx, lg[w]);
    float sum = 0.f;
#pragma unroll
    for (int w = 0; w < 8; ++w) { lg[w] = (pos - 2 * w >= 0) ? __expf(lg[w] - mx) : 0.f; sum += lg[w]; }
    const float inv = __builtin_amdgcn_rcpf(sum);
    float o[8] = {0.f, 0.f, 0.f, 0.f, 0.f, 0.f, 0.f, 0.f};
#pragma unroll
    for (int w = 0; w < 8; w += 2) {
      const unsigned pp = pk2(lg[w] * inv, lg[w + 1] * inv);
#pragma unroll
      for (int z = 0; z < 2; ++z) {
        if (pos - 2 * (w + z) >= 0) {
          const float p = z ? __uint_as_float(pp & 0xffff0000u) : __uint_as_float(pp << 16);
          const v4u vv = v[i + 7 - w - z];
#pragma unroll
          for (int e = 0; e < 4; ++e) { o[2 * e] += p * __uint_as_float(vv[e] << 16); o[2 * e + 1] += p * __uint_as_float(vv[e] & 0xffff0000u); }
        }
      }
    }
    __builtin_amdgcn_raw_buffer_store_b128((v4u){pk2(o[0], o[1]), pk2(o[2], o[3]), pk2(o[4], o[5]), pk2(o[6], o[7])}, rsO, (pos * NQKVB + cc * 8) * 2, 0, 16);
  }
}

constexpr int NTASK = 37;
__constant__ unsigned short TASKS[NTASK] = {
  2056, 2072, 2088, 2104, 2120, 2136, 2152, 2168, 2184, 2200, 2216, 2232, 5120, 5136, 5152, 5168, 5184, 5200, 5216, 3072,
  3088, 3104, 3120, 5232, 5248, 3136, 3152, 3168, 3184, 4096, 4112, 4128, 4144, 4160, 4176, 4192, 4208,
};
__device__ __forceinline__ const FParams& args() {
  typedef const __attribute__((address_space(4))) FParams* KP;
  KP p = (KP)__builtin_amdgcn_kernarg_segment_ptr(); asm volatile("" : "+s"(p)); return *(const FParams*)p;
}
__device__ __forceinline__ void worker(const FParams& F, char* lds, const int w0) {
#define lane opaque_lane()
#define tid (64 * w + opaque_lane())
  const int grp = (blockIdx.x & 7) - 3, slot = blockIdx.x >> 3;
#define PARV __ballot(F.par[128 + lane] != 0u), __ballot(F.par[192 + lane] != 0u)
  if (grp < 3) {
    int w = w0;
    if (grp == 2) { const unsigned t1 = (unsigned)__builtin_amdgcn_s_memrealtime() + (F.stag >> 16); while ((int)(t1 - (unsigned)__builtin_amdgcn_s_memrealtime()) > 0) __builtin_amdgcn_s_sleep(16); }
    panel_task(F, (grp == 2 ? 0 : grp + 1) * 32 + slot, w);
    const FParams& F = args();
    asm volatile("" : "+s"(w));
    if (grp == 2) attn_stream(F, PARV, slot >> 3, slot & 7, lds, w); else out_unit(F, __ballot(F.par[192 + lane] != 0u), 2 * grp + (slot >> 4), slot & 15, w);
    return;
  }
  const int x = (grp - 3) * 4 + (slot >> 3);
  int first = slot & 7;
  Ctl* ctl = F.ctl;
  int* sh = (int*)(lds + wg::LDS_USE);
  int next = -1;
  for (;;) {
    const FParams& F = args();
    int w = w0; asm volatile("" : "+s"(w));
    int t = first < 0 ? 0 : first;
    if (next < 0 && first < 0) {
      if (tid == 0) sh[0] = (int)__hip_atomic_fetch_add(&ctl->qhead[x][0], 1u, RLX_AGENT);
      __syncthreads();
      t = __builtin_amdgcn_readfirstlane(sh[0]);
      __syncthreads();
    }
    first = -1;
    if (t >= NTASK) return;
    if (next < 0 && t < 8 && (F.rot & 1024)) {
      const int id = (x * 8 + t) * 4 + w, tz = opaque_lane();
#pragma unroll
      for (int q = 0; q < 4; ++q) {
        const u16* base = q == 0 ? F.Wk : (q == 1 ? F.Wo : (q == 2 ? F.Wq : F.Wv));
        const __amdgpu_buffer_rsrc_t r1 = __builtin_amdgcn_make_buffer_rsrc((void*)base, 0, DM * HDA * 2, 0x00020000);
        const int o3 = (384 - (int)(size_t)base) & 896, o63 = (8064 - (int)(size_t)base) & 8064;
        __builtin_amdgcn_raw_ptr_buffer_load_lds(r1, (__attribute__((address_space(3))) void*)(lds + wg::LDS_USE + 1024), 4, (q & tz & 1) ? 0x7ffffff0 : ((tz >> 1) * 256 + id) * 1024 + (o3 ^ ((tz & 1) << 7)), 0, 0, 0);
        if (!(q & 1)) __builtin_amdgcn_raw_ptr_buffer_load_lds(r1, (__attribute__((address_space(3))) void*)(lds + wg::LDS_USE + 1024), 4, tz < 8 ? ((tz >> 1) * 256 + id) * 8192 + (o63 ^ ((tz & 1) << 7)) : 0x7ffffff0, 0, 0, 0);
      }
    }
    if (next < 0 && t < 8) {
      const unsigned t1 = (unsigned)__builtin_amdgcn_s_memrealtime() + (F.stag & 0xffff);
      while ((int)(t1 - (unsigned)__builtin_amdgcn_s_memrealtime()) > 0) __builtin_amdgcn_s_sleep(16);
    }
    const int code = __builtin_amdgcn_readfirstlane(next < 0 ? (int)TASKS[t] : next); next = -1;
    const int tk = code >> 10, ta = (code >> 4) & 63, tb = code & 7;
    const bool big = (code & 8) != 0;
    const u16 *A = nullptr, *Bt = nullptr; void* C = nullptr; int lda = 0, ldb = 0, ldc = 0, nk = 32, kind = 0, r = ta, n = 0, sst = 0;
    const unsigned* wp = nullptr; unsigned need = 0; bool dep = false; unsigned* sig = nullptr;
    if (tk == 2) {
      const int u = 12 * x + ta; n = u >> 3; r = 2 * (u & 7);
      A = F.Bx + (size_t)r * 128 * DB; lda = DB; Bt = F.wqkvB + n * 256; ldb = NQKVB;
      C = F.qkvB + (size_t)r * 128 * NQKVB + n * 256; ldc = NQKVB; sig = &ctl->cntB[r][0]; sst = 32;
    } else if (tk == 3) {
      const int u = ta * 8 + x; r = u >> 2; n = u & 3;
      kind = 2; dep = true; need = 12; if (lane < 2 && r - lane >= 0) wp = &ctl->cntB[r - lane][0]; sig = &ctl->cntAB[r][0];
    } else if (tk == 4) {
      r = 2 * x + (ta >> 2); n = ta & 3;
      A = F.qkvB + (size_t)r * 128 * NQKVB; lda = NQKVB; Bt = F.woB + n * 256; ldb = DB;
      C = F.out + (size_t)r * 128 * 2 * DM + DM + n * 256; ldc = 2 * DM; dep = true; need = 4; if (lane <= (int)big) wp = &ctl->cntAB[r + lane][0];
    } else if (tk == 5) { kind = 3; sig = &ctl->cntO[4 * r][0]; sst = 32; if ((x >> 2) == (r & 1) && 4 * r < OSEG0) next = code + 1024; }
    else {
      n = x & 3;
      A = F.qkvA + r * 4 * QBLK; lda = QBLK / 32; Bt = F.Wo + n * 256; ldb = DM; nk = HDA / 32;
      C = F.out + (size_t)r * 128 * 2 * DM + n * 256; ldc = 2 * DM;
      dep = true; need = 8; if (lane == 0) wp = &ctl->cntO[4 * r][0];
    }
    if (kind == 3) {
      { const int lz = opaque_lane(); if (lz < 12) wp = &ctl->prg[(lz >> 2) * 32 + 4 * x + (lz & 3)][0]; }
      wg_wait(wp, 8 * r + 8, ctl, w);
      attnA_block(F, PARV, r, 4 * x + w, (u16*)(lds + w * 16384), opaque_lane(), w, (unsigned*)sh + 4);
      asm volatile("s_waitcnt vmcnt(0)" ::: "memory");
      __syncthreads();
    } else {
      if (dep) wg_wait(wp, need, ctl, w);
      const unsigned* rot = F.par + 192;
      { const int t0 = 0;
        if ((F.rot & 48) && t >= t0 && t < t0 + 8) {
          const int id = __builtin_amdgcn_readfirstlane((x * 8 + t - t0) * 4 + w), tz = opaque_lane();
          const __amdgpu_buffer_rsrc_t r1 = __builtin_amdgcn_make_buffer_rsrc((void*)(F.Ax + 256 * DM), 0, 1792 * DM * 2, 0x00020000), r2 = __builtin_amdgcn_make_buffer_rsrc((void*)F.woB, 0, DB * DB * 2, 0x00020000);
          for (int q = 0; q < 14; ++q) __builtin_amdgcn_raw_ptr_buffer_load_lds(r1, (__attribute__((address_space(3))) void*)(lds + wg::LDS_USE + 1024), 16, tz * 16, (id * 14 + q) * 1024, 0, 0);
          for (int q = 0; q < 8; ++q) __builtin_amdgcn_raw_ptr_buffer_load_lds(r2, (__attribute__((address_space(3))) void*)(lds + wg::LDS_USE + 1024), 16, tz * 16, (id * 8 + q) * 1024, 0, 0);
          if (F.rot & 64) {
            const __amdgpu_buffer_rsrc_t r3 = __builtin_amdgcn_make_buffer_rsrc((void*)F.wqkvB, 0, DB * NQKVB * 2, 0x00020000);
            for (int q = 0; q < 24; ++q) __builtin_amdgcn_raw_ptr_buffer_load_lds(r3, (__attribute__((address_space(3))) void*)(lds + wg::LDS_USE + 1024), 16, tz * 16, (id * 24 + q) * 1024, 0, 0);
          }
          if (F.rot & 512) {
            const __amdgpu_buffer_rsrc_t r4 = __builtin_amdgcn_make_buffer_rsrc((void*)F.Bx, 0, S_ * DB * 2, 0x00020000);
            for (int q = 0; q < 16; ++q) __builtin_amdgcn_raw_ptr_buffer_load_lds(r4, (__attribute__((address_space(3))) void*)(lds + wg::LDS_USE + 1024), 16, tz * 16, (id * 16 + q) * 1024, 0, 0);
          }
        } }
      if (((tk == 0 || tk == 2) && (F.rot & 4))) {
        const int np = tk == 0 ? (big ? 4 : 2) : (tk == 2 ? 11 : 16), p0 = (tk == 0 ? 4 * x + tb : n) * 4 * np + w * np, tz = opaque_lane();
        const __amdgpu_buffer_rsrc_t rp = __builtin_amdgcn_make_buffer_rsrc(wg::uni(A), 0, (big ? 256 : 128) * DM * 2, 0x00020000);
        for (int q = 0; q < np; ++q) __builtin_amdgcn_raw_ptr_buffer_load_lds(rp, (__attribute__((address_space(3))) void*)(lds + wg::LDS_USE + 1024), 16, tz * 16, (p0 + q) * 1024, 0, 0);
      }
      if (kind == 0) { if (big) wg::gemm_tile<256>(A, lda, Bt, ldb, nk, C, ldc, false, false, rot, lds, w, (F.eps >> (tk == 2 ? 16 : 0)) & 255); else wg::gemm_tile<128>(A, lda, Bt, ldb, nk, C, ldc, tk == 6, false, rot, lds, w, tk == 4 ? (F.eps >> 8) & 255 : 0); }
      else {
        { const unsigned t1 = (unsigned)__builtin_amdgcn_s_memrealtime() + (ta * 8 + x) * F.abd;
          while ((int)(t1 - (unsigned)__builtin_amdgcn_s_memrealtime()) > 0) __builtin_amdgcn_s_sleep(8); }
        if (F.rot & 128) {
          const __amdgpu_buffer_rsrc_t r2 = __builtin_amdgcn_make_buffer_rsrc((void*)F.woB, 0, DB * DB * 2, 0x00020000);
          for (int q = 0; q < 8; ++q) __builtin_amdgcn_raw_ptr_buffer_load_lds(r2, (__attribute__((address_space(3))) void*)(lds + wg::LDS_USE + 1024), 16, opaque_lane() * 16, (((ta * 8 + x) * 4 + w) * 8 + q) * 1024, 0, 0);
        }
        if (F.rot & 256) {
          const __amdgpu_buffer_rsrc_t r2 = __builtin_amdgcn_make_buffer_rsrc((void*)(F.Ax + 1536 * DM), 0, 512 * DM * 2, 0x00020000);
          for (int q = 0; q < 4; ++q) __builtin_amdgcn_raw_ptr_buffer_load_lds(r2, (__attribute__((address_space(3))) void*)(lds + wg::LDS_USE + 1024), 16, opaque_lane() * 16, (((ta * 8 + x) * 4 + w) * 4 + q) * 1024, 0, 0);
        }
        const __amdgpu_buffer_rsrc_t rsO = __builtin_amdgcn_make_buffer_rsrc(F.qkvB, 0, S_ * NQKVB * 2, 0x00020000);
#pragma unroll 1
        for (int it = 0; it < 2; ++it) attnB_16(F.qkvB, rsO, r * 128 + n * 32 + it * 16, w);
        asm volatile("s_waitcnt vmcnt(0)" ::: "memory");
        __syncthreads();
      }
    }
    if (sig && tid <= (kind == 3 ? 3 : (int)big)) __hip_atomic_fetch_add(sig + tid * sst, 1u, RLX_AGENT);
  }
}

#undef lane
#undef tid
__device__ __forceinline__ unsigned rtnow() { return (unsigned)__builtin_amdgcn_s_memrealtime(); }
__device__ __forceinline__ void wait_rt(unsigned t) {
  int rem = (int)(t - rtnow());
  if (rem > 20000) return;
  while (rem > 12) { __builtin_amdgcn_s_sleep(2); rem = (int)(t - rtnow()); }
  while (rem > 0) rem = (int)(t - rtnow());
}
#define MBASE(s) ((s) * MREG + (int)((parm >> (s)) & 1) * 8192)
#define VBASE(s) ((s) * OREG + (int)((parv >> (s)) & 1) * 8192)
#define OBASE(s) ((s) * OREG + (int)((paro >> (s)) & 1) * 8192)
constexpr int RECO = 32768;
constexpr int ALM = RECO + 1024;
__device__ __forceinline__ float rowmax16(float f) { f = fmaxf(f, dpp_ror<8>(f)); f = fmaxf(f, dpp_ror<4>(f)); f = fmaxf(f, dpp_ror<2>(f)); return fmaxf(f, dpp_ror<1>(f)); }
__device__ __forceinline__ unsigned al_eval(unsigned v, unsigned& t) {
  const bool set = (v >> 16) == 1u;
  const unsigned any = (unsigned)__ballot(set) & 0xffffu;
  if (__builtin_expect(any == 0, 1)) return 0u;
  const int d = (short)(v - t);
  t += __builtin_amdgcn_readfirstlane((int)rowmax16(set && d > 0 && d < 1000 ? (float)d : 0.f));
  return any;
}
__device__ __forceinline__ void al_set(__amdgpu_buffer_rsrc_t rs, int base, int line, unsigned t) {
  const int lz = opaque_lane();
  __builtin_amdgcn_raw_buffer_store_b32((t & 0xffffu) | 0x10000u, rs, lz < 32 && line >= 0 ? base + line * 128 + lz * 4 : 0x7ffffff0, 0, 16);
}
__device__ __forceinline__ void rec_get(unsigned& recA, unsigned& recD, __amdgpu_buffer_rsrc_t rs, int off, unsigned* tmo, int naps = 1) {
#pragma unroll 1
  for (unsigned fails = 0;; ++fails) {
    const v2u rec = __builtin_amdgcn_raw_buffer_load_b64(rs, off, 0, 16);
    recA = __builtin_amdgcn_readfirstlane(rec.x); recD = __builtin_amdgcn_readfirstlane(rec.y);
    if ((recA != 0xffffffffu && recD != 0xffffffffu) || spin_fail(fails, tmo, 1u)) break;
    if (naps > 1) __builtin_amdgcn_s_sleep(6); else __builtin_amdgcn_s_sleep(2);
  }
}

#define QK_LOADER \
          "s_nop 4\n\t" \
          "s_add_u32 m0, %[lw], 0\n\ts_xor_b32 %[t], %[kb], 0x0\n\tbuffer_load_dwordx4 %[go], %[rk], %[t] offen" QK_NTk " lds\n\ts_add_u32 m0, %[lw], 1024\n\ts_xor_b32 %[t], %[kb], 0x10000\n\tbuffer_load_dwordx4 %[go], %[rk], %[t] offen" QK_NTk " lds\n\ts_add_u32 m0, %[lw], 2048\n\ts_xor_b32 %[t], %[kb], 0x20000\n\tbuffer_load_dwordx4 %[go], %[rk], %[t] offen" QK_NTk " lds\n\ts_add_u32 m0, %[lw], 3072\n\ts_xor_b32 %[t], %[kb], 0x30000\n\tbuffer_load_dwordx4 %[go], %[rk], %[t] offen" QK_NTk " lds\n\ts_add_u32 m0, %[lw], 4096\n\ts_xor_b32 %[t], %[kb], 0x40000\n\tbuffer_load_dwordx4 %[go], %[rk], %[t] offen" QK_NTk " lds\n\ts_add_u32 m0, %[lw], 5120\n\ts_xor_b32 %[t], %[kb], 0x50000\n\tbuffer_load_dwordx4 %[go], %[rk], %[t] offen" QK_NTk " lds\n\ts_add_u32 m0, %[lw], 6144\n\ts_xor_b32 %[t], %[kb], 0x60000\n\tbuffer_load_dwordx4 %[go], %[rk], %[t] offen" QK_NTk " lds\n\ts_add_u32 m0, %[lw], 7168\n\ts_xor_b32 %[t], %[kb], 0x70000\n\tbuffer_load_dwordx4 %[go], %[rk], %[t] offen" QK_NTk " lds\n\t" \
          "s_add_u32 m0, %[lw], 8192\n\ts_xor_b32 %[t], %[kb], 0x80000\n\tbuffer_load_dwordx4 %[go], %[rk], %[t] offen" QK_NTk " lds\n\ts_add_u32 m0, %[lw], 9216\n\ts_xor_b32 %[t], %[kb], 0x90000\n\tbuffer_load_dwordx4 %[go], %[rk], %[t] offen" QK_NTk " lds\n\ts_add_u32 m0, %[lw], 10240\n\ts_xor_b32 %[t], %[kb], 0xa0000\n\tbuffer_load_dwordx4 %[go], %[rk], %[t] offen" QK_NTk " lds\n\ts_add_u32 m0, %[lw], 11264\n\ts_xor_b32 %[t], %[kb], 0xb0000\n\tbuffer_load_dwordx4 %[go], %[rk], %[t] offen" QK_NTk " lds\n\ts_add_u32 m0, %[lw], 12288\n\ts_xor_b32 %[t], %[kb], 0xc0000\n\tbuffer_load_dwordx4 %[go], %[rk], %[t] offen" QK_NTk " lds\n\ts_add_u32 m0, %[lw], 13312\n\ts_xor_b32 %[t], %[kb], 0xd0000\n\tbuffer_load_dwordx4 %[go], %[rk], %[t] offen" QK_NTk " lds\n\ts_add_u32 m0, %[lw], 14336\n\ts_xor_b32 %[t], %[kb], 0xe0000\n\tbuffer_load_dwordx4 %[go], %[rk], %[t] offen" QK_NTk " lds\n\ts_add_u32 m0, %[lw], 15360\n\ts_xor_b32 %[t], %[kb], 0xf0000\n\tbuffer_load_dwordx4 %[go], %[rk], %[t] offen" QK_NTk " lds\n\t" \
          "s_add_u32 m0, %[lw], 16384\n\ts_xor_b32 %[t], %[kb], 0x100000\n\tbuffer_load_dwordx4 %[go], %[rk], %[t] offen" QK_NTk " lds\n\ts_add_u32 m0, %[lw], 17408\n\ts_xor_b32 %[t], %[kb], 0x110000\n\tbuffer_load_dwordx4 %[go], %[rk], %[t] offen" QK_NTk " lds\n\ts_add_u32 m0, %[lw], 18432\n\ts_xor_b32 %[t], %[kb], 0x120000\n\tbuffer_load_dwordx4 %[go], %[rk], %[t] offen" QK_NTk " lds\n\ts_add_u32 m0, %[lw], 19456\n\ts_xor_b32 %[t], %[kb], 0x130000\n\tbuffer_load_dwordx4 %[go], %[rk], %[t] offen" QK_NTk " lds\n\ts_add_u32 m0, %[lw], 20480\n\ts_xor_b32 %[t], %[kb], 0x140000\n\tbuffer_load_dwordx4 %[go], %[rk], %[t] offen" QK_NTk " lds\n\ts_add_u32 m0, %[lw], 21504\n\ts_xor_b32 %[t], %[kb], 0x150000\n\tbuffer_load_dwordx4 %[go], %[rk], %[t] offen" QK_NTk " lds\n\ts_add_u32 m0, %[lw], 22528\n\ts_xor_b32 %[t], %[kb], 0x160000\n\tbuffer_load_dwordx4 %[go], %[rk], %[t] offen" QK_NTk " lds\n\ts_add_u32 m0, %[lw], 23552\n\ts_xor_b32 %[t], %[kb], 0x170000\n\tbuffer_load_dwordx4 %[go], %[rk], %[t] offen" QK_NTk " lds\n\t" \
          "s_add_u32 m0, %[lw], 24576\n\ts_xor_b32 %[t], %[kb], 0x180000\n\tbuffer_load_dwordx4 %[go], %[rk], %[t] offen" QK_NTk " lds\n\ts_add_u32 m0, %[lw], 25600\n\ts_xor_b32 %[t], %[kb], 0x190000\n\tbuffer_load_dwordx4 %[go], %[rk], %[t] offen" QK_NTk " lds\n\ts_add_u32 m0, %[lw], 26624\n\ts_xor_b32 %[t], %[kb], 0x1a0000\n\tbuffer_load_dwordx4 %[go], %[rk], %[t] offen" QK_NTk " lds\n\ts_add_u32 m0, %[lw], 27648\n\ts_xor_b32 %[t], %[kb], 0x1b0000\n\tbuffer_load_dwordx4 %[go], %[rk], %[t] offen" QK_NTk " lds\n\ts_add_u32 m0, %[lw], 28672\n\ts_xor_b32 %[t], %[kb], 0x1c0000\n\tbuffer_load_dwordx4 %[go], %[rk], %[t] offen" QK_NTk " lds\n\ts_add_u32 m0, %[lw], 29696\n\ts_xor_b32 %[t], %[kb], 0x1d0000\n\tbuffer_load_dwordx4 %[go], %[rk], %[t] offen" QK_NTk " lds\n\ts_add_u32 m0, %[lw], 30720\n\ts_xor_b32 %[t], %[kb], 0x1e0000\n\tbuffer_load_dwordx4 %[go], %[rk], %[t] offen" QK_NTk " lds\n\ts_add_u32 m0, %[lw], 31744\n\ts_xor_b32 %[t], %[kb], 0x1f0000\n\tbuffer_load_dwordx4 %[go], %[rk], %[t] offen" QK_NTk " lds\n\t" \
          "s_add_u32 m0, %[lw], 32768\n\ts_xor_b32 %[t], %[kb], 0x200000\n\tbuffer_load_dwordx4 %[go], %[rk], %[t] offen" QK_NTk " lds\n\ts_add_u32 m0, %[lw], 33792\n\ts_xor_b32 %[t], %[kb], 0x210000\n\tbuffer_load_dwordx4 %[go], %[rk], %[t] offen" QK_NTk " lds\n\ts_add_u32 m0, %[lw], 34816\n\ts_xor_b32 %[t], %[kb], 0x220000\n\tbuffer_load_dwordx4 %[go], %[rk], %[t] offen" QK_NTk " lds\n\ts_add_u32 m0, %[lw], 35840\n\ts_xor_b32 %[t], %[kb], 0x230000\n\tbuffer_load_dwordx4 %[go], %[rk], %[t] offen" QK_NTk " lds\n\ts_add_u32 m0, %[lw], 36864\n\ts_xor_b32 %[t], %[kb], 0x240000\n\tbuffer_load_dwordx4 %[go], %[rk], %[t] offen" QK_NTk " lds\n\ts_add_u32 m0, %[lw], 37888\n\ts_xor_b32 %[t], %[kb], 0x250000\n\tbuffer_load_dwordx4 %[go], %[rk], %[t] offen" QK_NTk " lds\n\ts_add_u32 m0, %[lw], 38912\n\ts_xor_b32 %[t], %[kb], 0x260000\n\tbuffer_load_dwordx4 %[go], %[rk], %[t] offen" QK_NTk " lds\n\ts_add_u32 m0, %[lw], 39936\n\ts_xor_b32 %[t], %[kb], 0x270000\n\tbuffer_load_dwordx4 %[go], %[rk], %[t] offen" QK_NTk " lds\n\t" \
          "s_waitcnt vmcnt(32)\n\tds_read_b64_tr_b16 v[136:137], %[rd0] offset:0\n\tds_read_b64_tr_b16 v[138:139], %[rd0] offset:128\n\tds_read_b64_tr_b16 v[140:141], %[rd0] offset:4096\n\tds_read_b64_tr_b16 v[142:143], %[rd0] offset:4224\n\tds_read_b64_tr_b16 a[64:65], %[rd1] offset:0\n\tds_read_b64_tr_b16 a[66:67], %[rd1] offset:128\n\tds_read_b64_tr_b16 a[68:69], %[rd1] offset:4096\n\tds_read_b64_tr_b16 a[70:71], %[rd1] offset:4224\n\tds_read_b64_tr_b16 a[128:129], %[rd2] offset:0\n\tds_read_b64_tr_b16 a[130:131], %[rd2] offset:128\n\tds_read_b64_tr_b16 a[132:133], %[rd2] offset:4096\n\tds_read_b64_tr_b16 a[134:135], %[rd2] offset:4224\n\tds_read_b64_tr_b16 a[136:137], %[rd3] offset:0\n\tds_read_b64_tr_b16 a[138:139], %[rd3] offset:128\n\tds_read_b64_tr_b16 a[140:141], %[rd3] offset:4096\n\tds_read_b64_tr_b16 a[142:143], %[rd3] offset:4224\n\ts_waitcnt lgkmcnt(0)\n\t" \
          "s_add_u32 m0, %[lw], 0\n\ts_xor_b32 %[t], %[kb], 0x280000\n\tbuffer_load_dwordx4 %[go], %[rk], %[t] offen" QK_NTk " lds\n\ts_add_u32 m0, %[lw], 1024\n\ts_xor_b32 %[t], %[kb], 0x290000\n\tbuffer_load_dwordx4 %[go], %[rk], %[t] offen" QK_NTk " lds\n\ts_add_u32 m0, %[lw], 2048\n\ts_xor_b32 %[t], %[kb], 0x2a0000\n\tbuffer_load_dwordx4 %[go], %[rk], %[t] offen" QK_NTk " lds\n\ts_add_u32 m0, %[lw], 3072\n\ts_xor_b32 %[t], %[kb], 0x2b0000\n\tbuffer_load_dwordx4 %[go], %[rk], %[t] offen" QK_NTk " lds\n\ts_add_u32 m0, %[lw], 4096\n\ts_xor_b32 %[t], %[kb], 0x2c0000\n\tbuffer_load_dwordx4 %[go], %[rk], %[t] offen" QK_NTk " lds\n\ts_add_u32 m0, %[lw], 5120\n\ts_xor_b32 %[t], %[kb], 0x2d0000\n\tbuffer_load_dwordx4 %[go], %[rk], %[t] offen" QK_NTk " lds\n\ts_add_u32 m0, %[lw], 6144\n\ts_xor_b32 %[t], %[kb], 0x2e0000\n\tbuffer_load_dwordx4 %[go], %[rk], %[t] offen" QK_NTk " lds\n\ts_add_u32 m0, %[lw], 7168\n\ts_xor_b32 %[t], %[kb], 0x2f0000\n\tbuffer_load_dwordx4 %[go], %[rk], %[t] offen" QK_NTk " lds\n\t" \
          "s_waitcnt vmcnt(32)\n\tds_read_b64_tr_b16 v[40:41], %[rd0] offset:8192\n\tds_read_b64_tr_b16 v[42:43], %[rd0] offset:8320\n\tds_read_b64_tr_b16 v[44:45], %[rd0] offset:12288\n\tds_read_b64_tr_b16 v[46:47], %[rd0] offset:12416\n\tds_read_b64_tr_b16 a[72:73], %[rd1] offset:8192\n\tds_read_b64_tr_b16 a[74:75], %[rd1] offset:8320\n\tds_read_b64_tr_b16 a[76:77], %[rd1] offset:12288\n\tds_read_b64_tr_b16 a[78:79], %[rd1] offset:12416\n\tds_read_b64_tr_b16 a[144:145], %[rd2] offset:8192\n\tds_read_b64_tr_b16 a[146:147], %[rd2] offset:8320\n\tds_read_b64_tr_b16 a[148:149], %[rd2] offset:12288\n\tds_read_b64_tr_b16 a[150:151], %[rd2] offset:12416\n\tds_read_b64_tr_b16 a[152:153], %[rd3] offset:8192\n\tds_read_b64_tr_b16 a[154:155], %[rd3] offset:8320\n\tds_read_b64_tr_b16 a[156:157], %[rd3] offset:12288\n\tds_read_b64_tr_b16 a[158:159], %[rd3] offset:12416\n\ts_waitcnt lgkmcnt(0)\n\t" \
          "s_add_u32 m0, %[lw], 8192\n\ts_xor_b32 %[t], %[kb], 0x300000\n\tbuffer_load_dwordx4 %[go], %[rk], %[t] offen" QK_NTk " lds\n\ts_add_u32 m0, %[lw], 9216\n\ts_xor_b32 %[t], %[kb], 0x310000\n\tbuffer_load_dwordx4 %[go], %[rk], %[t] offen" QK_NTk " lds\n\ts_add_u32 m0, %[lw], 10240\n\ts_xor_b32 %[t], %[kb], 0x320000\n\tbuffer_load_dwordx4 %[go], %[rk], %[t] offen" QK_NTk " lds\n\ts_add_u32 m0, %[lw], 11264\n\ts_xor_b32 %[t], %[kb], 0x330000\n\tbuffer_load_dwordx4 %[go], %[rk], %[t] offen" QK_NTk " lds\n\ts_add_u32 m0, %[lw], 12288\n\ts_xor_b32 %[t], %[kb], 0x340000\n\tbuffer_load_dwordx4 %[go], %[rk], %[t] offen" QK_NTk " lds\n\ts_add_u32 m0, %[lw], 13312\n\ts_xor_b32 %[t], %[kb], 0x350000\n\tbuffer_load_dwordx4 %[go], %[rk], %[t] offen" QK_NTk " lds\n\ts_add_u32 m0, %[lw], 14336\n\ts_xor_b32 %[t], %[kb], 0x360000\n\tbuffer_load_dwordx4 %[go], %[rk], %[t] offen" QK_NTk " lds\n\ts_add_u32 m0, %[lw], 15360\n\ts_xor_b32 %[t], %[kb], 0x370000\n\tbuffer_load_dwordx4 %[go], %[rk], %[t] offen" QK_NTk " lds\n\t" \
          "s_waitcnt vmcnt(32)\n\tds_read_b64_tr_b16 v[144:145], %[rd0] offset:16384\n\tds_read_b64_tr_b16 v[146:147], %[rd0] offset:16512\n\tds_read_b64_tr_b16 v[148:149], %[rd0] offset:20480\n\tds_read_b64_tr_b16 v[150:151], %[rd0] offset:20608\n\tds_read_b64_tr_b16 a[80:81], %[rd1] offset:16384\n\tds_read_b64_tr_b16 a[82:83], %[rd1] offset:16512\n\tds_read_b64_tr_b16 a[84:85], %[rd1] offset:20480\n\tds_read_b64_tr_b16 a[86:87], %[rd1] offset:20608\n\tds_read_b64_tr_b16 a[160:161], %[rd2] offset:16384\n\tds_read_b64_tr_b16 a[162:163], %[rd2] offset:16512\n\tds_read_b64_tr_b16 a[164:165], %[rd2] offset:20480\n\tds_read_b64_tr_b16 a[166:167], %[rd2] offset:20608\n\tds_read_b64_tr_b16 a[168:169], %[rd3] offset:16384\n\tds_read_b64_tr_b16 a[170:171], %[rd3] offset:16512\n\tds_read_b64_tr_b16 a[172:173], %[rd3] offset:20480\n\tds_read_b64_tr_b16 a[174:175], %[rd3] offset:20608\n\ts_waitcnt lgkmcnt(0)\n\t" \
          "s_add_u32 m0, %[lw], 16384\n\ts_xor_b32 %[t], %[kb], 0x380000\n\tbuffer_load_dwordx4 %[go], %[rk], %[t] offen" QK_NTk " lds\n\ts_add_u32 m0, %[lw], 17408\n\ts_xor_b32 %[t], %[kb], 0x390000\n\tbuffer_load_dwordx4 %[go], %[rk], %[t] offen" QK_NTk " lds\n\ts_add_u32 m0, %[lw], 18432\n\ts_xor_b32 %[t], %[kb], 0x3a0000\n\tbuffer_load_dwordx4 %[go], %[rk], %[t] offen" QK_NTk " lds\n\ts_add_u32 m0, %[lw], 19456\n\ts_xor_b32 %[t], %[kb], 0x3b0000\n\tbuffer_load_dwordx4 %[go], %[rk], %[t] offen" QK_NTk " lds\n\ts_add_u32 m0, %[lw], 20480\n\ts_xor_b32 %[t], %[kb], 0x3c0000\n\tbuffer_load_dwordx4 %[go], %[rk], %[t] offen" QK_NTk " lds\n\ts_add_u32 m0, %[lw], 21504\n\ts_xor_b32 %[t], %[kb], 0x3d0000\n\tbuffer_load_dwordx4 %[go], %[rk], %[t] offen" QK_NTk " lds\n\ts_add_u32 m0, %[lw], 22528\n\ts_xor_b32 %[t], %[kb], 0x3e0000\n\tbuffer_load_dwordx4 %[go], %[rk], %[t] offen" QK_NTk " lds\n\ts_add_u32 m0, %[lw], 23552\n\ts_xor_b32 %[t], %[kb], 0x3f0000\n\tbuffer_load_dwordx4 %[go], %[rk], %[t] offen" QK_NTk " lds\n\t" \
          "s_waitcnt vmcnt(32)\n\tds_read_b64_tr_b16 v[80:81], %[rd0] offset:24576\n\tds_read_b64_tr_b16 v[82:83], %[rd0] offset:24704\n\tds_read_b64_tr_b16 v[84:85], %[rd0] offset:28672\n\tds_read_b64_tr_b16 v[86:87], %[rd0] offset:28800\n\tds_read_b64_tr_b16 a[88:89], %[rd1] offset:24576\n\tds_read_b64_tr_b16 a[90:91], %[rd1] offset:24704\n\tds_read_b64_tr_b16 a[92:93], %[rd1] offset:28672\n\tds_read_b64_tr_b16 a[94:95], %[rd1] offset:28800\n\tds_read_b64_tr_b16 a[176:177], %[rd2] offset:24576\n\tds_read_b64_tr_b16 a[178:179], %[rd2] offset:24704\n\tds_read_b64_tr_b16 a[180:181], %[rd2] offset:28672\n\tds_read_b64_tr_b16 a[182:183], %[rd2] offset:28800\n\tds_read_b64_tr_b16 a[184:185], %[rd3] offset:24576\n\tds_read_b64_tr_b16 a[186:187], %[rd3] offset:24704\n\tds_read_b64_tr_b16 a[188:189], %[rd3] offset:28672\n\tds_read_b64_tr_b16 a[190:191], %[rd3] offset:28800\n\ts_waitcnt lgkmcnt(0)\n\t" \
          "s_add_u32 m0, %[lw], 24576\n\ts_xor_b32 %[t], %[kb], 0x380000\n\tbuffer_load_dwordx4 %[go], %[rq], %[t] offen" QK_NTq " lds\n\ts_add_u32 m0, %[lw], 25600\n\ts_xor_b32 %[t], %[kb], 0x390000\n\tbuffer_load_dwordx4 %[go], %[rq], %[t] offen" QK_NTq " lds\n\ts_add_u32 m0, %[lw], 26624\n\ts_xor_b32 %[t], %[kb], 0x3a0000\n\tbuffer_load_dwordx4 %[go], %[rq], %[t] offen" QK_NTq " lds\n\ts_add_u32 m0, %[lw], 27648\n\ts_xor_b32 %[t], %[kb], 0x3b0000\n\tbuffer_load_dwordx4 %[go], %[rq], %[t] offen" QK_NTq " lds\n\ts_add_u32 m0, %[lw], 28672\n\ts_xor_b32 %[t], %[kb], 0x3c0000\n\tbuffer_load_dwordx4 %[go], %[rq], %[t] offen" QK_NTq " lds\n\ts_add_u32 m0, %[lw], 29696\n\ts_xor_b32 %[t], %[kb], 0x3d0000\n\tbuffer_load_dwordx4 %[go], %[rq], %[t] offen" QK_NTq " lds\n\ts_add_u32 m0, %[lw], 30720\n\ts_xor_b32 %[t], %[kb], 0x3e0000\n\tbuffer_load_dwordx4 %[go], %[rq], %[t] offen" QK_NTq " lds\n\ts_add_u32 m0, %[lw], 31744\n\ts_xor_b32 %[t], %[kb], 0x3f0000\n\tbuffer_load_dwordx4 %[go], %[rq], %[t] offen" QK_NTq " lds\n\t" \
          "s_waitcnt vmcnt(32)\n\tds_read_b64_tr_b16 v[152:153], %[rd0] offset:32768\n\tds_read_b64_tr_b16 v[154:155], %[rd0] offset:32896\n\tds_read_b64_tr_b16 v[156:157], %[rd0] offset:36864\n\tds_read_b64_tr_b16 v[158:159], %[rd0] offset:36992\n\tds_read_b64_tr_b16 a[96:97], %[rd1] offset:32768\n\tds_read_b64_tr_b16 a[98:99], %[rd1] offset:32896\n\tds_read_b64_tr_b16 a[100:101], %[rd1] offset:36864\n\tds_read_b64_tr_b16 a[102:103], %[rd1] offset:36992\n\tds_read_b64_tr_b16 a[192:193], %[rd2] offset:32768\n\tds_read_b64_tr_b16 a[194:195], %[rd2] offset:32896\n\tds_read_b64_tr_b16 a[196:197], %[rd2] offset:36864\n\tds_read_b64_tr_b16 a[198:199], %[rd2] offset:36992\n\tds_read_b64_tr_b16 a[200:201], %[rd3] offset:32768\n\tds_read_b64_tr_b16 a[202:203], %[rd3] offset:32896\n\tds_read_b64_tr_b16 a[204:205], %[rd3] offset:36864\n\tds_read_b64_tr_b16 a[206:207], %[rd3] offset:36992\n\ts_waitcnt lgkmcnt(0)\n\t" \
          "s_add_u32 m0, %[lw], 32768\n\ts_xor_b32 %[t], %[kb], 0x300000\n\tbuffer_load_dwordx4 %[go], %[rq], %[t] offen" QK_NTq " lds\n\ts_add_u32 m0, %[lw], 33792\n\ts_xor_b32 %[t], %[kb], 0x310000\n\tbuffer_load_dwordx4 %[go], %[rq], %[t] offen" QK_NTq " lds\n\ts_add_u32 m0, %[lw], 34816\n\ts_xor_b32 %[t], %[kb], 0x320000\n\tbuffer_load_dwordx4 %[go], %[rq], %[t] offen" QK_NTq " lds\n\ts_add_u32 m0, %[lw], 35840\n\ts_xor_b32 %[t], %[kb], 0x330000\n\tbuffer_load_dwordx4 %[go], %[rq], %[t] offen" QK_NTq " lds\n\ts_add_u32 m0, %[lw], 36864\n\ts_xor_b32 %[t], %[kb], 0x340000\n\tbuffer_load_dwordx4 %[go], %[rq], %[t] offen" QK_NTq " lds\n\ts_add_u32 m0, %[lw], 37888\n\ts_xor_b32 %[t], %[kb], 0x350000\n\tbuffer_load_dwordx4 %[go], %[rq], %[t] offen" QK_NTq " lds\n\ts_add_u32 m0, %[lw], 38912\n\ts_xor_b32 %[t], %[kb], 0x360000\n\tbuffer_load_dwordx4 %[go], %[rq], %[t] offen" QK_NTq " lds\n\ts_add_u32 m0, %[lw], 39936\n\ts_xor_b32 %[t], %[kb], 0x370000\n\tbuffer_load_dwordx4 %[go], %[rq], %[t] offen" QK_NTq " lds\n\t" \
          "s_waitcnt vmcnt(32)\n\tds_read_b64_tr_b16 v[120:121], %[rd0] offset:0\n\tds_read_b64_tr_b16 v[122:123], %[rd0] offset:128\n\tds_read_b64_tr_b16 v[124:125], %[rd0] offset:4096\n\tds_read_b64_tr_b16 v[126:127], %[rd0] offset:4224\n\tds_read_b64_tr_b16 a[104:105], %[rd1] offset:0\n\tds_read_b64_tr_b16 a[106:107], %[rd1] offset:128\n\tds_read_b64_tr_b16 a[108:109], %[rd1] offset:4096\n\tds_read_b64_tr_b16 a[110:111], %[rd1] offset:4224\n\tds_read_b64_tr_b16 a[208:209], %[rd2] offset:0\n\tds_read_b64_tr_b16 a[210:211], %[rd2] offset:128\n\tds_read_b64_tr_b16 a[212:213], %[rd2] offset:4096\n\tds_read_b64_tr_b16 a[214:215], %[rd2] offset:4224\n\tds_read_b64_tr_b16 a[216:217], %[rd3] offset:0\n\tds_read_b64_tr_b16 a[218:219], %[rd3] offset:128\n\tds_read_b64_tr_b16 a[220:221], %[rd3] offset:4096\n\tds_read_b64_tr_b16 a[222:223], %[rd3] offset:4224\n\ts_waitcnt lgkmcnt(0)\n\t" \
          "s_add_u32 m0, %[lw], 0\n\ts_xor_b32 %[t], %[kb], 0x280000\n\tbuffer_load_dwordx4 %[go], %[rq], %[t] offen" QK_NTq " lds\n\ts_add_u32 m0, %[lw], 1024\n\ts_xor_b32 %[t], %[kb], 0x290000\n\tbuffer_load_dwordx4 %[go], %[rq], %[t] offen" QK_NTq " lds\n\ts_add_u32 m0, %[lw], 2048\n\ts_xor_b32 %[t], %[kb], 0x2a0000\n\tbuffer_load_dwordx4 %[go], %[rq], %[t] offen" QK_NTq " lds\n\ts_add_u32 m0, %[lw], 3072\n\ts_xor_b32 %[t], %[kb], 0x2b0000\n\tbuffer_load_dwordx4 %[go], %[rq], %[t] offen" QK_NTq " lds\n\ts_add_u32 m0, %[lw], 4096\n\ts_xor_b32 %[t], %[kb], 0x2c0000\n\tbuffer_load_dwordx4 %[go], %[rq], %[t] offen" QK_NTq " lds\n\ts_add_u32 m0, %[lw], 5120\n\ts_xor_b32 %[t], %[kb], 0x2d0000\n\tbuffer_load_dwordx4 %[go], %[rq], %[t] offen" QK_NTq " lds\n\ts_add_u32 m0, %[lw], 6144\n\ts_xor_b32 %[t], %[kb], 0x2e0000\n\tbuffer_load_dwordx4 %[go], %[rq], %[t] offen" QK_NTq " lds\n\ts_add_u32 m0, %[lw], 7168\n\ts_xor_b32 %[t], %[kb], 0x2f0000\n\tbuffer_load_dwordx4 %[go], %[rq], %[t] offen" QK_NTq " lds\n\t" \
          "s_waitcnt vmcnt(32)\n\tds_read_b64_tr_b16 a[16:17], %[rd0] offset:8192\n\tds_read_b64_tr_b16 a[18:19], %[rd0] offset:8320\n\tds_read_b64_tr_b16 a[20:21], %[rd0] offset:12288\n\tds_read_b64_tr_b16 a[22:23], %[rd0] offset:12416\n\tds_read_b64_tr_b16 a[112:113], %[rd1] offset:8192\n\tds_read_b64_tr_b16 a[114:115], %[rd1] offset:8320\n\tds_read_b64_tr_b16 a[116:117], %[rd1] offset:12288\n\tds_read_b64_tr_b16 a[118:119], %[rd1] offset:12416\n\tds_read_b64_tr_b16 a[224:225], %[rd2] offset:8192\n\tds_read_b64_tr_b16 a[226:227], %[rd2] offset:8320\n\tds_read_b64_tr_b16 a[228:229], %[rd2] offset:12288\n\tds_read_b64_tr_b16 a[230:231], %[rd2] offset:12416\n\tds_read_b64_tr_b16 a[232:233], %[rd3] offset:8192\n\tds_read_b64_tr_b16 a[234:235], %[rd3] offset:8320\n\tds_read_b64_tr_b16 a[236:237], %[rd3] offset:12288\n\tds_read_b64_tr_b16 a[238:239], %[rd3] offset:12416\n\ts_waitcnt lgkmcnt(0)\n\t" \
          "s_add_u32 m0, %[lw], 8192\n\ts_xor_b32 %[t], %[kb], 0x200000\n\tbuffer_load_dwordx4 %[go], %[rq], %[t] offen" QK_NTq " lds\n\ts_add_u32 m0, %[lw], 9216\n\ts_xor_b32 %[t], %[kb], 0x210000\n\tbuffer_load_dwordx4 %[go], %[rq], %[t] offen" QK_NTq " lds\n\ts_add_u32 m0, %[lw], 10240\n\ts_xor_b32 %[t], %[kb], 0x220000\n\tbuffer_load_dwordx4 %[go], %[rq], %[t] offen" QK_NTq " lds\n\ts_add_u32 m0, %[lw], 11264\n\ts_xor_b32 %[t], %[kb], 0x230000\n\tbuffer_load_dwordx4 %[go], %[rq], %[t] offen" QK_NTq " lds\n\ts_add_u32 m0, %[lw], 12288\n\ts_xor_b32 %[t], %[kb], 0x240000\n\tbuffer_load_dwordx4 %[go], %[rq], %[t] offen" QK_NTq " lds\n\ts_add_u32 m0, %[lw], 13312\n\ts_xor_b32 %[t], %[kb], 0x250000\n\tbuffer_load_dwordx4 %[go], %[rq], %[t] offen" QK_NTq " lds\n\ts_add_u32 m0, %[lw], 14336\n\ts_xor_b32 %[t], %[kb], 0x260000\n\tbuffer_load_dwordx4 %[go], %[rq], %[t] offen" QK_NTq " lds\n\ts_add_u32 m0, %[lw], 15360\n\ts_xor_b32 %[t], %[kb], 0x270000\n\tbuffer_load_dwordx4 %[go], %[rq], %[t] offen" QK_NTq " lds\n\t" \
          "s_waitcnt vmcnt(32)\n\tds_read_b64_tr_b16 a[56:57], %[rd0] offset:16384\n\tds_read_b64_tr_b16 a[58:59], %[rd0] offset:16512\n\tds_read_b64_tr_b16 a[60:61], %[rd0] offset:20480\n\tds_read_b64_tr_b16 a[62:63], %[rd0] offset:20608\n\tds_read_b64_tr_b16 a[120:121], %[rd1] offset:16384\n\tds_read_b64_tr_b16 a[122:123], %[rd1] offset:16512\n\tds_read_b64_tr_b16 a[124:125], %[rd1] offset:20480\n\tds_read_b64_tr_b16 a[126:127], %[rd1] offset:20608\n\tds_read_b64_tr_b16 a[240:241], %[rd2] offset:16384\n\tds_read_b64_tr_b16 a[242:243], %[rd2] offset:16512\n\tds_read_b64_tr_b16 a[244:245], %[rd2] offset:20480\n\tds_read_b64_tr_b16 a[246:247], %[rd2] offset:20608\n\tds_read_b64_tr_b16 a[248:249], %[rd3] offset:16384\n\tds_read_b64_tr_b16 a[250:251], %[rd3] offset:16512\n\tds_read_b64_tr_b16 a[252:253], %[rd3] offset:20480\n\tds_read_b64_tr_b16 a[254:255], %[rd3] offset:20608\n\ts_waitcnt lgkmcnt(0)\n\t" \
          "s_add_u32 m0, %[lw], 16384\n\ts_xor_b32 %[t], %[kb], 0x180000\n\tbuffer_load_dwordx4 %[go], %[rq], %[t] offen" QK_NTq " lds\n\ts_add_u32 m0, %[lw], 17408\n\ts_xor_b32 %[t], %[kb], 0x190000\n\tbuffer_load_dwordx4 %[go], %[rq], %[t] offen" QK_NTq " lds\n\ts_add_u32 m0, %[lw], 18432\n\ts_xor_b32 %[t], %[kb], 0x1a0000\n\tbuffer_load_dwordx4 %[go], %[rq], %[t] offen" QK_NTq " lds\n\ts_add_u32 m0, %[lw], 19456\n\ts_xor_b32 %[t], %[kb], 0x1b0000\n\tbuffer_load_dwordx4 %[go], %[rq], %[t] offen" QK_NTq " lds\n\ts_add_u32 m0, %[lw], 20480\n\ts_xor_b32 %[t], %[kb], 0x1c0000\n\tbuffer_load_dwordx4 %[go], %[rq], %[t] offen" QK_NTq " lds\n\ts_add_u32 m0, %[lw], 21504\n\ts_xor_b32 %[t], %[kb], 0x1d0000\n\tbuffer_load_dwordx4 %[go], %[rq], %[t] offen" QK_NTq " lds\n\ts_add_u32 m0, %[lw], 22528\n\ts_xor_b32 %[t], %[kb], 0x1e0000\n\tbuffer_load_dwordx4 %[go], %[rq], %[t] offen" QK_NTq " lds\n\ts_add_u32 m0, %[lw], 23552\n\ts_xor_b32 %[t], %[kb], 0x1f0000\n\tbuffer_load_dwordx4 %[go], %[rq], %[t] offen" QK_NTq " lds\n\t" \
          "s_waitcnt vmcnt(32)\n\tds_read_b64_tr_b16 a[24:25], %[rd0] offset:24576\n\tds_read_b64_tr_b16 a[26:27], %[rd0] offset:24704\n\tds_read_b64_tr_b16 a[28:29], %[rd0] offset:28672\n\tds_read_b64_tr_b16 a[30:31], %[rd0] offset:28800\n\tds_read_b64_tr_b16 a[32:33], %[rd1] offset:24576\n\tds_read_b64_tr_b16 a[34:35], %[rd1] offset:24704\n\tds_read_b64_tr_b16 a[36:37], %[rd1] offset:28672\n\tds_read_b64_tr_b16 a[38:39], %[rd1] offset:28800\n\tds_read_b64_tr_b16 a[40:41], %[rd2] offset:24576\n\tds_read_b64_tr_b16 a[42:43], %[rd2] offset:24704\n\tds_read_b64_tr_b16 a[44:45], %[rd2] offset:28672\n\tds_read_b64_tr_b16 a[46:47], %[rd2] offset:28800\n\tds_read_b64_tr_b16 a[48:49], %[rd3] offset:24576\n\tds_read_b64_tr_b16 a[50:51], %[rd3] offset:24704\n\tds_read_b64_tr_b16 a[52:53], %[rd3] offset:28672\n\tds_read_b64_tr_b16 a[54:55], %[rd3] offset:28800\n\ts_waitcnt lgkmcnt(0)\n\t" \
          "s_add_u32 m0, %[lw], 24576\n\ts_xor_b32 %[t], %[kb], 0x100000\n\tbuffer_load_dwordx4 %[go], %[rq], %[t] offen" QK_NTq " lds\n\ts_add_u32 m0, %[lw], 25600\n\ts_xor_b32 %[t], %[kb], 0x110000\n\tbuffer_load_dwordx4 %[go], %[rq], %[t] offen" QK_NTq " lds\n\ts_add_u32 m0, %[lw], 26624\n\ts_xor_b32 %[t], %[kb], 0x120000\n\tbuffer_load_dwordx4 %[go], %[rq], %[t] offen" QK_NTq " lds\n\ts_add_u32 m0, %[lw], 27648\n\ts_xor_b32 %[t], %[kb], 0x130000\n\tbuffer_load_dwordx4 %[go], %[rq], %[t] offen" QK_NTq " lds\n\ts_add_u32 m0, %[lw], 28672\n\ts_xor_b32 %[t], %[kb], 0x140000\n\tbuffer_load_dwordx4 %[go], %[rq], %[t] offen" QK_NTq " lds\n\ts_add_u32 m0, %[lw], 29696\n\ts_xor_b32 %[t], %[kb], 0x150000\n\tbuffer_load_dwordx4 %[go], %[rq], %[t] offen" QK_NTq " lds\n\ts_add_u32 m0, %[lw], 30720\n\ts_xor_b32 %[t], %[kb], 0x160000\n\tbuffer_load_dwordx4 %[go], %[rq], %[t] offen" QK_NTq " lds\n\ts_add_u32 m0, %[lw], 31744\n\ts_xor_b32 %[t], %[kb], 0x170000\n\tbuffer_load_dwordx4 %[go], %[rq], %[t] offen" QK_NTq " lds\n\t" \
          "s_waitcnt vmcnt(32)\n\tds_read_b64_tr_b16 v[160:161], %[rd0] offset:32768\n\tds_read_b64_tr_b16 v[162:163], %[rd0] offset:32896\n\tds_read_b64_tr_b16 v[164:165], %[rd0] offset:36864\n\tds_read_b64_tr_b16 v[166:167], %[rd0] offset:36992\n\tds_read_b64_tr_b16 v[128:129], %[rd1] offset:32768\n\tds_read_b64_tr_b16 v[130:131], %[rd1] offset:32896\n\tds_read_b64_tr_b16 v[132:133], %[rd1] offset:36864\n\tds_read_b64_tr_b16 v[134:135], %[rd1] offset:36992\n\tds_read_b64_tr_b16 a[0:1], %[rd2] offset:32768\n\tds_read_b64_tr_b16 a[2:3], %[rd2] offset:32896\n\tds_read_b64_tr_b16 a[4:5], %[rd2] offset:36864\n\tds_read_b64_tr_b16 a[6:7], %[rd2] offset:36992\n\tds_read_b64_tr_b16 a[8:9], %[rd3] offset:32768\n\tds_read_b64_tr_b16 a[10:11], %[rd3] offset:32896\n\tds_read_b64_tr_b16 a[12:13], %[rd3] offset:36864\n\tds_read_b64_tr_b16 a[14:15], %[rd3] offset:36992\n\ts_waitcnt lgkmcnt(0)\n\t" \
          "s_add_u32 m0, %[lw], 32768\n\ts_xor_b32 %[t], %[kb], 0x80000\n\tbuffer_load_dwordx4 %[go], %[rq], %[t] offen" QK_NTq " lds\n\ts_add_u32 m0, %[lw], 33792\n\ts_xor_b32 %[t], %[kb], 0x90000\n\tbuffer_load_dwordx4 %[go], %[rq], %[t] offen" QK_NTq " lds\n\ts_add_u32 m0, %[lw], 34816\n\ts_xor_b32 %[t], %[kb], 0xa0000\n\tbuffer_load_dwordx4 %[go], %[rq], %[t] offen" QK_NTq " lds\n\ts_add_u32 m0, %[lw], 35840\n\ts_xor_b32 %[t], %[kb], 0xb0000\n\tbuffer_load_dwordx4 %[go], %[rq], %[t] offen" QK_NTq " lds\n\ts_add_u32 m0, %[lw], 36864\n\ts_xor_b32 %[t], %[kb], 0xc0000\n\tbuffer_load_dwordx4 %[go], %[rq], %[t] offen" QK_NTq " lds\n\ts_add_u32 m0, %[lw], 37888\n\ts_xor_b32 %[t], %[kb], 0xd0000\n\tbuffer_load_dwordx4 %[go], %[rq], %[t] offen" QK_NTq " lds\n\ts_add_u32 m0, %[lw], 38912\n\ts_xor_b32 %[t], %[kb], 0xe0000\n\tbuffer_load_dwordx4 %[go], %[rq], %[t] offen" QK_NTq " lds\n\ts_add_u32 m0, %[lw], 39936\n\ts_xor_b32 %[t], %[kb], 0xf0000\n\tbuffer_load_dwordx4 %[go], %[rq], %[t] offen" QK_NTq " lds\n\t" \
          "s_waitcnt vmcnt(32)\n\tds_read_b64_tr_b16 v[104:105], %[rd0] offset:0\n\tds_read_b64_tr_b16 v[106:107], %[rd0] offset:128\n\tds_read_b64_tr_b16 v[108:109], %[rd0] offset:4096\n\tds_read_b64_tr_b16 v[110:111], %[rd0] offset:4224\n\tds_read_b64_tr_b16 v[168:169], %[rd1] offset:0\n\tds_read_b64_tr_b16 v[170:171], %[rd1] offset:128\n\tds_read_b64_tr_b16 v[172:173], %[rd1] offset:4096\n\tds_read_b64_tr_b16 v[174:175], %[rd1] offset:4224\n\tds_read_b64_tr_b16 v[112:113], %[rd2] offset:0\n\tds_read_b64_tr_b16 v[114:115], %[rd2] offset:128\n\tds_read_b64_tr_b16 v[116:117], %[rd2] offset:4096\n\tds_read_b64_tr_b16 v[118:119], %[rd2] offset:4224\n\tds_read_b64_tr_b16 v[176:177], %[rd3] offset:0\n\tds_read_b64_tr_b16 v[178:179], %[rd3] offset:128\n\tds_read_b64_tr_b16 v[180:181], %[rd3] offset:4096\n\tds_read_b64_tr_b16 v[182:183], %[rd3] offset:4224\n\ts_waitcnt lgkmcnt(0)\n\t" \
          "s_add_u32 m0, %[lw], 0\n\ts_xor_b32 %[t], %[kb], 0x0\n\tbuffer_load_dwordx4 %[go], %[rq], %[t] offen" QK_NTq " lds\n\ts_add_u32 m0, %[lw], 1024\n\ts_xor_b32 %[t], %[kb], 0x10000\n\tbuffer_load_dwordx4 %[go], %[rq], %[t] offen" QK_NTq " lds\n\ts_add_u32 m0, %[lw], 2048\n\ts_xor_b32 %[t], %[kb], 0x20000\n\tbuffer_load_dwordx4 %[go], %[rq], %[t] offen" QK_NTq " lds\n\ts_add_u32 m0, %[lw], 3072\n\ts_xor_b32 %[t], %[kb], 0x30000\n\tbuffer_load_dwordx4 %[go], %[rq], %[t] offen" QK_NTq " lds\n\ts_add_u32 m0, %[lw], 4096\n\ts_xor_b32 %[t], %[kb], 0x40000\n\tbuffer_load_dwordx4 %[go], %[rq], %[t] offen" QK_NTq " lds\n\ts_add_u32 m0, %[lw], 5120\n\ts_xor_b32 %[t], %[kb], 0x50000\n\tbuffer_load_dwordx4 %[go], %[rq], %[t] offen" QK_NTq " lds\n\ts_add_u32 m0, %[lw], 6144\n\ts_xor_b32 %[t], %[kb], 0x60000\n\tbuffer_load_dwordx4 %[go], %[rq], %[t] offen" QK_NTq " lds\n\ts_add_u32 m0, %[lw], 7168\n\ts_xor_b32 %[t], %[kb], 0x70000\n\tbuffer_load_dwordx4 %[go], %[rq], %[t] offen" QK_NTq " lds\n\t" \
          "s_waitcnt vmcnt(32)\n\tds_read_b64_tr_b16 v[184:185], %[rd0] offset:8192\n\tds_read_b64_tr_b16 v[186:187], %[rd0] offset:8320\n\tds_read_b64_tr_b16 v[188:189], %[rd0] offset:12288\n\tds_read_b64_tr_b16 v[190:191], %[rd0] offset:12416\n\tds_read_b64_tr_b16 v[88:89], %[rd1] offset:8192\n\tds_read_b64_tr_b16 v[90:91], %[rd1] offset:8320\n\tds_read_b64_tr_b16 v[92:93], %[rd1] offset:12288\n\tds_read_b64_tr_b16 v[94:95], %[rd1] offset:12416\n\tds_read_b64_tr_b16 v[192:193], %[rd2] offset:8192\n\tds_read_b64_tr_b16 v[194:195], %[rd2] offset:8320\n\tds_read_b64_tr_b16 v[196:197], %[rd2] offset:12288\n\tds_read_b64_tr_b16 v[198:199], %[rd2] offset:12416\n\tds_read_b64_tr_b16 v[96:97], %[rd3] offset:8192\n\tds_read_b64_tr_b16 v[98:99], %[rd3] offset:8320\n\tds_read_b64_tr_b16 v[100:101], %[rd3] offset:12288\n\tds_read_b64_tr_b16 v[102:103], %[rd3] offset:12416\n\ts_waitcnt lgkmcnt(0)\n\tds_write_b128 %[own], v[136:139] offset:8192\n\tds_write_b128 %[own], v[140:143] offset:9216\n\tds_write_b128 %[own], v[144:147] offset:10240\n\tds_write_b128 %[own], v[148:151] offset:11264\n\tds_write_b128 %[own], v[152:155] offset:12288\n\tds_write_b128 %[own], v[156:159] offset:13312\n\tds_write_b128 %[own], v[160:163] offset:14336\n\tds_write_b128 %[own], v[164:167] offset:15360\n\ts_waitcnt lgkmcnt(0)\n\t" \
          "s_waitcnt vmcnt(24)\n\tds_read_b64_tr_b16 v[64:65], %[rd0] offset:16384\n\tds_read_b64_tr_b16 v[66:67], %[rd0] offset:16512\n\tds_read_b64_tr_b16 v[68:69], %[rd0] offset:20480\n\tds_read_b64_tr_b16 v[70:71], %[rd0] offset:20608\n\tds_read_b64_tr_b16 v[200:201], %[rd1] offset:16384\n\tds_read_b64_tr_b16 v[202:203], %[rd1] offset:16512\n\tds_read_b64_tr_b16 v[204:205], %[rd1] offset:20480\n\tds_read_b64_tr_b16 v[206:207], %[rd1] offset:20608\n\tds_read_b64_tr_b16 v[72:73], %[rd2] offset:16384\n\tds_read_b64_tr_b16 v[74:75], %[rd2] offset:16512\n\tds_read_b64_tr_b16 v[76:77], %[rd2] offset:20480\n\tds_read_b64_tr_b16 v[78:79], %[rd2] offset:20608\n\tds_read_b64_tr_b16 v[208:209], %[rd3] offset:16384\n\tds_read_b64_tr_b16 v[210:211], %[rd3] offset:16512\n\tds_read_b64_tr_b16 v[212:213], %[rd3] offset:20480\n\tds_read_b64_tr_b16 v[214:215], %[rd3] offset:20608\n\ts_waitcnt lgkmcnt(0)\n\tds_write_b128 %[own], v[168:171] offset:16384\n\tds_write_b128 %[own], v[172:175] offset:17408\n\tds_write_b128 %[own], v[176:179] offset:18432\n\tds_write_b128 %[own], v[180:183] offset:19456\n\tds_write_b128 %[own], v[184:187] offset:20480\n\tds_write_b128 %[own], v[188:191] offset:21504\n\tds_write_b128 %[own], v[192:195] offset:22528\n\tds_write_b128 %[own], v[196:199] offset:23552\n\ts_waitcnt lgkmcnt(0)\n\t" \
          "s_waitcnt vmcnt(16)\n\tds_read_b64_tr_b16 v[216:217], %[rd0] offset:24576\n\tds_read_b64_tr_b16 v[218:219], %[rd0] offset:24704\n\tds_read_b64_tr_b16 v[220:221], %[rd0] offset:28672\n\tds_read_b64_tr_b16 v[222:223], %[rd0] offset:28800\n\tds_read_b64_tr_b16 v[48:49], %[rd1] offset:24576\n\tds_read_b64_tr_b16 v[50:51], %[rd1] offset:24704\n\tds_read_b64_tr_b16 v[52:53], %[rd1] offset:28672\n\tds_read_b64_tr_b16 v[54:55], %[rd1] offset:28800\n\tds_read_b64_tr_b16 v[224:225], %[rd2] offset:24576\n\tds_read_b64_tr_b16 v[226:227], %[rd2] offset:24704\n\tds_read_b64_tr_b16 v[228:229], %[rd2] offset:28672\n\tds_read_b64_tr_b16 v[230:231], %[rd2] offset:28800\n\tds_read_b64_tr_b16 v[56:57], %[rd3] offset:24576\n\tds_read_b64_tr_b16 v[58:59], %[rd3] offset:24704\n\tds_read_b64_tr_b16 v[60:61], %[rd3] offset:28672\n\tds_read_b64_tr_b16 v[62:63], %[rd3] offset:28800\n\ts_waitcnt lgkmcnt(0)\n\tds_write_b128 %[own], v[200:203] offset:24576\n\tds_write_b128 %[own], v[204:207] offset:25600\n\tds_write_b128 %[own], v[208:211] offset:26624\n\tds_write_b128 %[own], v[212:215] offset:27648\n\tds_write_b128 %[own], v[216:219] offset:28672\n\tds_write_b128 %[own], v[220:223] offset:29696\n\tds_write_b128 %[own], v[224:227] offset:30720\n\tds_write_b128 %[own], v[228:231] offset:31744\n\ts_waitcnt lgkmcnt(0)\n\t" \
          "s_waitcnt vmcnt(8)\n\tds_read_b64_tr_b16 v[24:25], %[rd0] offset:32768\n\tds_read_b64_tr_b16 v[26:27], %[rd0] offset:32896\n\tds_read_b64_tr_b16 v[28:29], %[rd0] offset:36864\n\tds_read_b64_tr_b16 v[30:31], %[rd0] offset:36992\n\tds_read_b64_tr_b16 v[136:137], %[rd1] offset:32768\n\tds_read_b64_tr_b16 v[138:139], %[rd1] offset:32896\n\tds_read_b64_tr_b16 v[140:141], %[rd1] offset:36864\n\tds_read_b64_tr_b16 v[142:143], %[rd1] offset:36992\n\tds_read_b64_tr_b16 v[32:33], %[rd2] offset:32768\n\tds_read_b64_tr_b16 v[34:35], %[rd2] offset:32896\n\tds_read_b64_tr_b16 v[36:37], %[rd2] offset:36864\n\tds_read_b64_tr_b16 v[38:39], %[rd2] offset:36992\n\tds_read_b64_tr_b16 v[144:145], %[rd3] offset:32768\n\tds_read_b64_tr_b16 v[146:147], %[rd3] offset:32896\n\tds_read_b64_tr_b16 v[148:149], %[rd3] offset:36864\n\tds_read_b64_tr_b16 v[150:151], %[rd3] offset:36992\n\ts_waitcnt lgkmcnt(0)\n\t" \
          "s_waitcnt vmcnt(0)\n\tds_read_b64_tr_b16 v[152:153], %[rd0] offset:0\n\tds_read_b64_tr_b16 v[154:155], %[rd0] offset:128\n\tds_read_b64_tr_b16 v[156:157], %[rd0] offset:4096\n\tds_read_b64_tr_b16 v[158:159], %[rd0] offset:4224\n\tds_read_b64_tr_b16 v[8:9], %[rd1] offset:0\n\tds_read_b64_tr_b16 v[10:11], %[rd1] offset:128\n\tds_read_b64_tr_b16 v[12:13], %[rd1] offset:4096\n\tds_read_b64_tr_b16 v[14:15], %[rd1] offset:4224\n\tds_read_b64_tr_b16 v[160:161], %[rd2] offset:0\n\tds_read_b64_tr_b16 v[162:163], %[rd2] offset:128\n\tds_read_b64_tr_b16 v[164:165], %[rd2] offset:4096\n\tds_read_b64_tr_b16 v[166:167], %[rd2] offset:4224\n\tds_read_b64_tr_b16 v[16:17], %[rd3] offset:0\n\tds_read_b64_tr_b16 v[18:19], %[rd3] offset:128\n\tds_read_b64_tr_b16 v[20:21], %[rd3] offset:4096\n\tds_read_b64_tr_b16 v[22:23], %[rd3] offset:4224\n\ts_waitcnt lgkmcnt(0)\n\tds_write_b128 %[own], v[136:139] offset:0\n\tds_write_b128 %[own], v[140:143] offset:1024\n\tds_write_b128 %[own], v[144:147] offset:2048\n\tds_write_b128 %[own], v[148:151] offset:3072\n\tds_write_b128 %[own], v[152:155] offset:4096\n\tds_write_b128 %[own], v[156:159] offset:5120\n\tds_write_b128 %[own], v[160:163] offset:6144\n\tds_write_b128 %[own], v[164:167] offset:7168\n\ts_waitcnt lgkmcnt(0)\n\t"
#define QK_DEFW "=&{a[0:31]}"(wa[0]), "=&{a[32:63]}"(wa[1]), "=&{a[64:95]}"(wa[2]), "=&{a[96:127]}"(wa[3]), "=&{a[128:159]}"(wa[4]), "=&{a[160:191]}"(wa[5]), "=&{a[192:223]}"(wa[6]), "=&{a[224:255]}"(wa[7]), "=&{v[8:39]}"(wv_[0]), "=&{v[40:71]}"(wv_[1]), "=&{v[72:103]}"(wv_[2]), "=&{v[104:135]}"(wv_[3]), "=&{v[136:167]}"(wv_[4]), "=&{v[168:199]}"(wv_[5]), "=&{v[200:231]}"(wv_[6])
#define QK_STEP \
          "ds_read_b128 v[200:203], %[own] offset:4096\n\t" \
          "ds_read_b128 v[204:207], %[own] offset:5120\n\t" \
          "ds_read_b128 v[208:211], %[own] offset:6144\n\t" \
          "ds_read_b128 v[212:215], %[own] offset:7168\n\t" \
          "ds_read_b128 v[216:219], %[own] offset:8192\n\t" \
          "ds_read_b128 v[220:223], %[own] offset:9216\n\t" \
          "ds_read_b128 v[224:227], %[own] offset:0\n\t" \
          "ds_read_b128 v[228:231], %[own] offset:1024\n\t" \
          "v_mov_b64 v[168:169], 0\n\t" \
          "v_mov_b64 v[170:171], 0\n\t" \
          "v_mov_b64 v[172:173], 0\n\t" \
          "v_mov_b64 v[174:175], 0\n\t" \
          "v_mov_b64 v[176:177], 0\n\t" \
          "v_mov_b64 v[178:179], 0\n\t" \
          "v_mov_b64 v[180:181], 0\n\t" \
          "v_mov_b64 v[182:183], 0\n\t" \
          "v_mov_b64 v[184:185], 0\n\t" \
          "v_mov_b64 v[186:187], 0\n\t" \
          "v_mov_b64 v[188:189], 0\n\t" \
          "v_mov_b64 v[190:191], 0\n\t" \
          "v_mov_b64 v[192:193], 0\n\t" \
          "v_mov_b64 v[194:195], 0\n\t" \
          "v_mov_b64 v[196:197], 0\n\t" \
          "v_mov_b64 v[198:199], 0\n\t" \
          "s_branch Lqk_c%=\n\t" \
          "Lqk_fail%=:\n\t" \
          "s_waitcnt vmcnt(0) lgkmcnt(0)\n\t" \
          "s_nop 7\n\t" \
          "ds_read_b128 v[200:203], %[own] offset:4096\n\t" \
          "ds_read_b128 v[204:207], %[own] offset:5120\n\t" \
          "ds_read_b128 v[208:211], %[own] offset:6144\n\t" \
          "ds_read_b128 v[212:215], %[own] offset:7168\n\t" \
          "ds_read_b128 v[216:219], %[own] offset:8192\n\t" \
          "ds_read_b128 v[220:223], %[own] offset:9216\n\t" \
          "ds_read_b128 v[224:227], %[own] offset:0\n\t" \
          "ds_read_b128 v[228:231], %[own] offset:1024\n\t" \
          "v_mov_b64 v[168:169], 0\n\t" \
          "v_mov_b64 v[170:171], 0\n\t" \
          "v_mov_b64 v[172:173], 0\n\t" \
          "v_mov_b64 v[174:175], 0\n\t" \
          "v_mov_b64 v[176:177], 0\n\t" \
          "v_mov_b64 v[178:179], 0\n\t" \
          "v_mov_b64 v[180:181], 0\n\t" \
          "v_mov_b64 v[182:183], 0\n\t" \
          "v_mov_b64 v[184:185], 0\n\t" \
          "v_mov_b64 v[186:187], 0\n\t" \
          "v_mov_b64 v[188:189], 0\n\t" \
          "v_mov_b64 v[190:191], 0\n\t" \
          "v_mov_b64 v[192:193], 0\n\t" \
          "v_mov_b64 v[194:195], 0\n\t" \
          "v_mov_b64 v[196:197], 0\n\t" \
          "v_mov_b64 v[198:199], 0\n\t" \
          "s_mov_b32 %[cnt], 0\n\t" \
          "Lqk_g%=:\n\t" \
          "buffer_load_dwordx4 v[136:139], %[mlo], %[rsm], %[mb0] offen offset:0 sc1\n\t" \
          "buffer_load_dwordx4 v[140:143], %[mlo], %[rsm], %[mb0] offen offset:1024 sc1\n\t" \
          "buffer_load_dwordx4 v[144:147], %[mlo], %[rsm], %[mb0] offen offset:2048 sc1\n\t" \
          "buffer_load_dwordx4 v[148:151], %[mlo], %[rsm], %[mb0] offen offset:3072 sc1\n\t" \
          "buffer_load_dwordx4 v[152:155], %[mlo], %[rsm], %[mb1] offen offset:0 sc1\n\t" \
          "buffer_load_dwordx4 v[156:159], %[mlo], %[rsm], %[mb1] offen offset:1024 sc1\n\t" \
          "buffer_load_dwordx4 v[160:163], %[mlo], %[rsm], %[mb1] offen offset:2048 sc1\n\t" \
          "buffer_load_dwordx4 v[164:167], %[mlo], %[rsm], %[mb1] offen offset:3072 sc1\n\t" \
          "buffer_load_dword %[al], off, %[rsc], %[tmo] sc1\n\t" \
          "s_waitcnt vmcnt(0) lgkmcnt(0)\n\t" \
          "v_max_u32 %[m], v136, v137\n\t" \
          "v_max3_u32 %[m], v138, v139, %[m]\n\t" \
          "v_max3_u32 %[m], v140, v141, %[m]\n\t" \
          "v_max3_u32 %[m], v142, v143, %[m]\n\t" \
          "v_max3_u32 %[m], v144, v145, %[m]\n\t" \
          "v_max3_u32 %[m], v146, v147, %[m]\n\t" \
          "v_max3_u32 %[m], v148, v149, %[m]\n\t" \
          "v_max3_u32 %[m], v150, v151, %[m]\n\t" \
          "v_max3_u32 %[m], v152, v153, %[m]\n\t" \
          "v_max3_u32 %[m], v154, v155, %[m]\n\t" \
          "v_max3_u32 %[m], v156, v157, %[m]\n\t" \
          "v_max3_u32 %[m], v158, v159, %[m]\n\t" \
          "v_max3_u32 %[m], v160, v161, %[m]\n\t" \
          "v_max3_u32 %[m], v162, v163, %[m]\n\t" \
          "v_max3_u32 %[m], v164, v165, %[m]\n\t" \
          "v_max3_u32 %[m], v166, v167, %[m]\n\t" \
          "v_cmp_eq_u32 %[bad1], -1, %[m]\n\t" \
          "v_readfirstlane_b32 vcc_lo, %[al]\n\t" \
          "s_cmp_eq_u64 %[bad1], 0\n\t" \
          "s_cbranch_scc1 Lqk_ok%=\n\t" \
          "s_add_u32 %[cnt], %[cnt], 1\n\t" \
          "s_sleep 1\n\t" \
          "s_lshr_b32 vcc_hi, %[cnt], 15\n\t" \
          "s_or_b32 vcc_lo, vcc_lo, vcc_hi\n\t" \
          "s_cbranch_scc0 Lqk_g%=\n\t" \
          "v_mov_b32 %[al], 1\n\t" \
          "buffer_store_dword %[al], off, %[rsc], %[tmo] sc1\n\t" \
          "s_mov_b32 %[mb0], %[mbz]\n\t" \
          "s_mov_b32 %[mb1], %[mbz]\n\t" \
          "s_branch Lqk_g%=\n\t" \
          "Lqk_ok%=:\n\t" \
          "s_memrealtime vcc\n\t" \
          "v_mbcnt_lo_u32_b32 %[al], -1, 0\n\t" \
          "v_lshlrev_b32 %[al], 2, %[al]\n\t" \
          "s_waitcnt lgkmcnt(0)\n\t" \
          "s_add_u32 vcc_lo, vcc_lo, %[ald]\n\t" \
          "s_and_b32 vcc_lo, vcc_lo, 0xffff\n\t" \
          "s_bitset1_b32 vcc_lo, 16\n\t" \
          "v_mov_b32 %[m], vcc_lo\n\t" \
          "s_mov_b64 exec, %[alx]\n\t" \
          "buffer_store_dword %[m], %[al], %[rsm], %[als] offen sc1\n\t" \
          "s_mov_b64 exec, -1\n\t" \
          "s_branch Lqk_go%=\n\t" \
          "Lqk_s%=:\n\t" \
          "s_sleep 2\n\t" \
          "Lqk_c%=:\n\t" \
          "s_memrealtime vcc\n\t" \
          "s_waitcnt lgkmcnt(0)\n\t" \
          "s_sub_i32 vcc_lo, %[ta], vcc_lo\n\t" \
          "s_cmp_gt_i32 vcc_lo, 12\n\t" \
          "s_cbranch_scc1 Lqk_s%=\n\t" \
          "Lqk_w%=:\n\t" \
          "s_memrealtime vcc\n\t" \
          "s_waitcnt lgkmcnt(0)\n\t" \
          "s_sub_i32 vcc_lo, %[ta], vcc_lo\n\t" \
          "s_cmp_gt_i32 vcc_lo, 0\n\t" \
          "s_cbranch_scc1 Lqk_w%=\n\t" \
          "buffer_load_dwordx4 v[136:139], %[mlo], %[rsm], %[mb0] offen sc1\n\t" \
          "s_nop 5\n\t" \
          "buffer_load_dwordx4 v[140:143], %[mlo], %[rsm], %[mb0] offen offset:1024 sc1\n\t" \
          "s_nop 5\n\t" \
          "buffer_load_dwordx4 v[144:147], %[mlo], %[rsm], %[mb0] offen offset:2048 sc1\n\t" \
          "s_nop 5\n\t" \
          "buffer_load_dwordx4 v[148:151], %[mlo], %[rsm], %[mb0] offen offset:3072 sc1\n\t" \
          "s_nop 5\n\t" \
          "buffer_load_dwordx4 v[152:155], %[mlo], %[rsm], %[mb1] offen sc1\n\t" \
          "s_nop 5\n\t" \
          "buffer_load_dwordx4 v[156:159], %[mlo], %[rsm], %[mb1] offen offset:1024 sc1\n\t" \
          "s_nop 5\n\t" \
          "buffer_load_dwordx4 v[160:163], %[mlo], %[rsm], %[mb1] offen offset:2048 sc1\n\t" \
          "s_nop 5\n\t" \
          "buffer_load_dwordx4 v[164:167], %[mlo], %[rsm], %[mb1] offen offset:3072 sc1\n\t" \
          "s_waitcnt vmcnt(7)\n\t" \
          "Lqk_go%=:\n\t" \
          "v_smfmac_f32_16x16x64_bf16 v[168:171], v[136:139], v[200:207], %[idx]\n\t" \
          "ds_read_b128 v[200:203], %[own] offset:2048\n\t" \
          "v_smfmac_f32_16x16x64_bf16 v[172:175], v[136:139], v[8:15], %[idx]\n\t" \
          "ds_read_b128 v[204:207], %[own] offset:3072\n\t" \
          "v_smfmac_f32_16x16x64_bf16 v[176:179], v[136:139], v[208:215], %[idx]\n\t" \
          "ds_read_b128 v[208:211], %[own] offset:28672\n\t" \
          "v_smfmac_f32_16x16x64_bf16 v[180:183], v[136:139], v[16:23], %[idx]\n\t" \
          "ds_read_b128 v[212:215], %[own] offset:29696\n\t" \
          "s_waitcnt vmcnt(6)\n\t" \
          "v_smfmac_f32_16x16x64_bf16 v[184:187], v[136:139], v[216:223], %[idx]\n\t" \
          "ds_read_b128 v[216:219], %[own] offset:30720\n\t" \
          "v_smfmac_f32_16x16x64_bf16 v[168:171], v[140:143], v[24:31], %[idx]\n\t" \
          "ds_read_b128 v[220:223], %[own] offset:31744\n\t" \
          "v_smfmac_f32_16x16x64_bf16 v[172:175], v[140:143], v[224:231], %[idx]\n\t" \
          "ds_read_b128 v[224:227], %[own] offset:10240\n\t" \
          "v_smfmac_f32_16x16x64_bf16 v[176:179], v[140:143], v[32:39], %[idx]\n\t" \
          "ds_read_b128 v[228:231], %[own] offset:11264\n\t" \
          "s_waitcnt lgkmcnt(6)\n\t" \
          "v_smfmac_f32_16x16x64_bf16 v[180:183], v[140:143], v[200:207], %[idx]\n\t" \
          "ds_read_b128 v[200:203], %[own] offset:24576\n\t" \
          "v_max3_u32 %[m], v136, v137, v138\n\t" \
          "v_smfmac_f32_16x16x64_bf16 v[184:187], v[140:143], v[40:47], %[idx]\n\t" \
          "ds_read_b128 v[204:207], %[own] offset:25600\n\t" \
          "s_waitcnt vmcnt(5) lgkmcnt(6)\n\t" \
          "v_smfmac_f32_16x16x64_bf16 v[168:171], v[144:147], v[208:215], %[idx]\n\t" \
          "ds_read_b128 v[208:211], %[own] offset:26624\n\t" \
          "v_max3_u32 %[m], v138, v139, %[m]\n\t" \
          "v_smfmac_f32_16x16x64_bf16 v[172:175], v[144:147], v[48:55], %[idx]\n\t" \
          "ds_read_b128 v[212:215], %[own] offset:27648\n\t" \
          "s_waitcnt lgkmcnt(6)\n\t" \
          "v_smfmac_f32_16x16x64_bf16 v[176:179], v[144:147], v[216:223], %[idx]\n\t" \
          "ds_read_b128 v[216:219], %[own] offset:20480\n\t" \
          "v_cmp_eq_u32 %[bad1], -1, %[m]\n\t" \
          "v_smfmac_f32_16x16x64_bf16 v[180:183], v[144:147], v[56:63], %[idx]\n\t" \
          "ds_read_b128 v[220:223], %[own] offset:21504\n\t" \
          "s_waitcnt vmcnt(4) lgkmcnt(6)\n\t" \
          "v_smfmac_f32_16x16x64_bf16 v[184:187], v[144:147], v[224:231], %[idx]\n\t" \
          "ds_read_b128 v[224:227], %[own] offset:22528\n\t" \
          "s_cmp_eq_u64 %[bad1], 0\n\t" \
          "v_smfmac_f32_16x16x64_bf16 v[168:171], v[148:151], v[64:71], %[idx]\n\t" \
          "ds_read_b128 v[228:231], %[own] offset:23552\n\t" \
          "s_waitcnt lgkmcnt(6)\n\t" \
          "v_smfmac_f32_16x16x64_bf16 v[172:175], v[148:151], v[200:207], %[idx]\n\t" \
          "ds_read_b128 v[200:203], %[own] offset:12288\n\t" \
          "s_cbranch_scc0 Lqk_fail%=\n\t" \
          "v_smfmac_f32_16x16x64_bf16 v[176:179], v[148:151], v[72:79], %[idx]\n\t" \
          "ds_read_b128 v[204:207], %[own] offset:13312\n\t" \
          "s_waitcnt lgkmcnt(6)\n\t" \
          "v_smfmac_f32_16x16x64_bf16 v[180:183], v[148:151], v[208:215], %[idx]\n\t" \
          "ds_read_b128 v[208:211], %[own] offset:16384\n\t" \
          "v_max3_u32 %[m], v140, v141, %[m]\n\t" \
          "v_smfmac_f32_16x16x64_bf16 v[184:187], v[148:151], v[80:87], %[idx]\n\t" \
          "ds_read_b128 v[212:215], %[own] offset:17408\n\t" \
          "s_waitcnt vmcnt(3) lgkmcnt(6)\n\t" \
          "v_smfmac_f32_16x16x64_bf16 v[168:171], v[152:155], v[216:223], %[idx]\n\t" \
          "ds_read_b128 v[216:219], %[own] offset:18432\n\t" \
          "v_max3_u32 %[m], v142, v143, %[m]\n\t" \
          "v_smfmac_f32_16x16x64_bf16 v[172:175], v[152:155], v[88:95], %[idx]\n\t" \
          "ds_read_b128 v[220:223], %[own] offset:19456\n\t" \
          "s_waitcnt lgkmcnt(6)\n\t" \
          "v_smfmac_f32_16x16x64_bf16 v[176:179], v[152:155], v[224:231], %[idx]\n\t" \
          "ds_read_b128 v[224:227], %[own] offset:14336\n\t" \
          "v_max3_u32 %[m], v144, v145, %[m]\n\t" \
          "v_smfmac_f32_16x16x64_bf16 v[180:183], v[152:155], v[96:103], %[idx]\n\t" \
          "ds_read_b128 v[228:231], %[own] offset:15360\n\t" \
          "s_waitcnt vmcnt(2) lgkmcnt(6)\n\t" \
          "v_smfmac_f32_16x16x64_bf16 v[184:187], v[152:155], v[200:207], %[idx]\n\t" \
          "v_max3_u32 %[m], v146, v147, %[m]\n\t" \
          "v_smfmac_f32_16x16x64_bf16 v[168:171], v[156:159], v[104:111], %[idx]\n\t" \
          "s_waitcnt lgkmcnt(4)\n\t" \
          "v_max3_u32 %[m], v148, v149, %[m]\n\t" \
          "v_smfmac_f32_16x16x64_bf16 v[172:175], v[156:159], v[208:215], %[idx]\n\t" \
          "v_max3_u32 %[m], v150, v151, %[m]\n\t" \
          "v_smfmac_f32_16x16x64_bf16 v[176:179], v[156:159], v[112:119], %[idx]\n\t" \
          "s_waitcnt lgkmcnt(2)\n\t" \
          "v_max3_u32 %[m], v152, v153, %[m]\n\t" \
          "v_smfmac_f32_16x16x64_bf16 v[180:183], v[156:159], v[216:223], %[idx]\n\t" \
          "ds_read_b128 v[216:219], %[own] offset:32768\n\t" \
          "v_max3_u32 %[m], v154, v155, %[m]\n\t" \
          "v_smfmac_f32_16x16x64_bf16 v[184:187], v[156:159], v[120:127], %[idx]\n\t" \
          "ds_read_b128 v[220:223], %[own] offset:33792\n\t" \
          "s_waitcnt vmcnt(1) lgkmcnt(2)\n\t" \
          "v_smfmac_f32_16x16x64_bf16 v[168:171], v[160:163], v[224:231], %[idx]\n\t" \
          "ds_read_b64_tr_b16 v[224:225], %[vxp]\n\t" \
          "v_max3_u32 %[m], v156, v157, %[m]\n\t" \
          "v_smfmac_f32_16x16x64_bf16 v[172:175], v[160:163], v[128:135], %[idx]\n\t" \
          "ds_read_b64_tr_b16 v[226:227], %[vxp] offset:1024\n\t" \
          "v_max3_u32 %[m], v158, v159, %[m]\n\t" \
          "v_smfmac_f32_16x16x64_bf16 v[176:179], v[160:163], a[0:7], %[idx]\n\t" \
          "ds_read_b64_tr_b16 v[228:229], %[vxp] offset:8\n\t" \
          "v_max3_u32 %[m], v160, v161, %[m]\n\t" \
          "v_smfmac_f32_16x16x64_bf16 v[180:183], v[160:163], a[8:15], %[idx]\n\t" \
          "ds_read_b64_tr_b16 v[230:231], %[vxp] offset:1032\n\t" \
          "v_smfmac_f32_16x16x64_bf16 v[184:187], v[160:163], a[16:23], %[idx]\n\t" \
          "s_waitcnt vmcnt(0)\n\t" \
          "v_max3_u32 %[m], v162, v163, %[m]\n\t" \
          "v_smfmac_f32_16x16x64_bf16 v[168:171], v[164:167], a[24:31], %[idx]\n\t" \
          "v_max3_u32 %[m], v164, v165, %[m]\n\t" \
          "v_smfmac_f32_16x16x64_bf16 v[172:175], v[164:167], a[32:39], %[idx]\n\t" \
          "v_max3_u32 %[m], v166, v167, %[m]\n\t" \
          "v_smfmac_f32_16x16x64_bf16 v[176:179], v[164:167], a[40:47], %[idx]\n\t" \
          "v_cvt_pk_bf16_f32 v200, v168, v169\n\t" \
          "v_cvt_pk_bf16_f32 v201, v170, v171\n\t" \
          "v_smfmac_f32_16x16x64_bf16 v[180:183], v[164:167], a[48:55], %[idx]\n\t" \
          "ds_write_b64 %[own], v[200:201] offset:36864\n\t" \
          "v_cvt_pk_bf16_f32 v202, v172, v173\n\t" \
          "v_cvt_pk_bf16_f32 v203, v174, v175\n\t" \
          "v_smfmac_f32_16x16x64_bf16 v[184:187], v[164:167], a[56:63], %[idx]\n\t" \
          "ds_write_b64 %[own], v[202:203] offset:36872\n\t" \
          "v_smfmac_f32_16x16x64_bf16 v[188:191], v[136:139], a[64:71], %[idx]\n\t" \
          "v_cmp_eq_u32 %[bad1], -1, %[m]\n\t" \
          "v_smfmac_f32_16x16x64_bf16 v[188:191], v[140:143], a[72:79], %[idx]\n\t" \
          "v_cvt_pk_bf16_f32 v208, v176, v177\n\t" \
          "v_smfmac_f32_16x16x64_bf16 v[188:191], v[144:147], a[80:87], %[idx]\n\t" \
          "s_cmp_eq_u64 %[bad1], 0\n\t" \
          "s_cbranch_scc0 Lqk_fail%=\n\t" \
          "s_waitcnt lgkmcnt(0)\n\t" \
          "s_barrier\n\t" \
          "v_cvt_pk_bf16_f32 v209, v178, v179\n\t" \
          "v_smfmac_f32_16x16x64_bf16 v[188:191], v[148:151], a[88:95], %[idx]\n\t" \
          "ds_read_b128 v[210:213], %[p1] offset:36864\n\t" \
          "v_cvt_pk_bf16_f32 v214, v180, v181\n\t" \
          "v_smfmac_f32_16x16x64_bf16 v[188:191], v[152:155], a[96:103], %[idx]\n\t" \
          "v_cvt_pk_bf16_f32 v215, v182, v183\n\t" \
          "v_smfmac_f32_16x16x64_bf16 v[188:191], v[156:159], a[104:111], %[idx]\n\t" \
          "v_cvt_pk_bf16_f32 v204, v184, v185\n\t" \
          "v_smfmac_f32_16x16x64_bf16 v[188:191], v[160:163], a[112:119], %[idx]\n\t" \
          "v_cvt_pk_bf16_f32 v205, v186, v187\n\t" \
          "v_smfmac_f32_16x16x64_bf16 v[188:191], v[164:167], a[120:127], %[idx]\n\t" \
          "s_waitcnt lgkmcnt(0)\n\t" \
          "v_mfma_f32_16x16x32_bf16 v[168:171], v[208:211], %[jk], 0\n\t" \
          "v_smfmac_f32_16x16x64_bf16 v[192:195], v[136:139], a[128:135], %[idx]\n\t" \
          "v_mfma_f32_16x16x32_bf16 v[172:175], v[212:215], %[jk], 0\n\t" \
          "v_cvt_pk_bf16_f32 v176, v168, v169\n\t" \
          "v_cvt_pk_bf16_f32 v177, v170, v171\n\t" \
          "v_smfmac_f32_16x16x64_bf16 v[196:199], v[136:139], a[136:143], %[idx]\n\t" \
          "ds_write_b64 %[own], v[204:205] offset:37888\n\t" \
          "v_cvt_pk_bf16_f32 v206, v188, v189\n\t" \
          "v_smfmac_f32_16x16x64_bf16 v[192:195], v[140:143], a[144:151], %[idx]\n\t" \
          "v_cvt_pk_bf16_f32 v178, v172, v173\n\t" \
          "v_cvt_pk_bf16_f32 v179, v174, v175\n\t" \
          "v_smfmac_f32_16x16x64_bf16 v[196:199], v[140:143], a[152:159], %[idx]\n\t" \
          "v_cvt_pk_bf16_f32 v207, v190, v191\n\t" \
          "v_mfma_f32_16x16x32_bf16 v[208:211], v[216:219], v[176:179], 0\n\t" \
          "v_mov_b32 v168, %[one]\n\t" \
          "v_mov_b32 v169, %[one]\n\t" \
          "v_mfma_f32_16x16x32_bf16 v[212:215], v[220:223], v[176:179], 0\n\t" \
          "v_mov_b32 v170, %[one]\n\t" \
          "v_mov_b32 v171, %[one]\n\t" \
          "v_smfmac_f32_16x16x64_bf16 v[192:195], v[144:147], a[160:167], %[idx]\n\t" \
          "ds_write_b64 %[own], v[206:207] offset:37896\n\t" \
          "v_smfmac_f32_16x16x64_bf16 v[196:199], v[144:147], a[168:175], %[idx]\n\t" \
          "v_cndmask_b32 v208, v208, v212, %[twin]\n\t" \
          "v_cndmask_b32 v209, v209, v213, %[twin]\n\t" \
          "v_smfmac_f32_16x16x64_bf16 v[192:195], v[148:151], a[176:183], %[idx]\n\t" \
          "ds_write_b64 %[own], v[208:209] offset:38912\n\t" \
          "v_cndmask_b32 v210, v210, v214, %[twin]\n\t" \
          "v_cndmask_b32 v211, v211, v215, %[twin]\n\t" \
          "v_smfmac_f32_16x16x64_bf16 v[196:199], v[148:151], a[184:191], %[idx]\n\t" \
          "ds_write_b64 %[own], v[210:211] offset:38920\n\t" \
          "v_smfmac_f32_16x16x64_bf16 v[192:195], v[152:155], a[192:199], %[idx]\n\t" \
          "v_mul_f32 v212, %[qs], v208\n\t" \
          "v_smfmac_f32_16x16x64_bf16 v[196:199], v[152:155], a[200:207], %[idx]\n\t" \
          "buffer_load_dword v232, %[ho], %[rsv], %[vb] offen sc1\n\t" \
          "v_mul_f32 v213, %[qs], v209\n\t" \
          "v_smfmac_f32_16x16x64_bf16 v[192:195], v[156:159], a[208:215], %[idx]\n\t" \
          "buffer_load_dword v234, %[ho], %[rsv], %[vb] offen offset:256 sc1\n\t" \
          "v_mul_f32 v214, %[qs], v210\n\t" \
          "v_smfmac_f32_16x16x64_bf16 v[196:199], v[156:159], a[216:223], %[idx]\n\t" \
          "s_waitcnt lgkmcnt(0)\n\t" \
          "s_barrier\n\t" \
          "v_mul_f32 v215, %[qs], v211\n\t" \
          "v_smfmac_f32_16x16x64_bf16 v[192:195], v[160:163], a[224:231], %[idx]\n\t" \
          "ds_read_b128 v[138:141], %[p1] offset:37888\n\t" \
          "v_smfmac_f32_16x16x64_bf16 v[196:199], v[160:163], a[232:239], %[idx]\n\t" \
          "ds_read_b128 v[216:219], %[p1] offset:38912\n\t" \
          "v_smfmac_f32_16x16x64_bf16 v[192:195], v[164:167], a[240:247], %[idx]\n\t" \
          "ds_read_b128 v[220:223], %[p2] offset:38912\n\t" \
          "v_smfmac_f32_16x16x64_bf16 v[196:199], v[164:167], a[248:255], %[idx]\n\t" \
          "ds_read_b128 v[200:203], %[p3] offset:38912\n\t" \
          "s_nop 0\n\t" \
          "s_waitcnt lgkmcnt(2)\n\t" \
          "v_cvt_pk_bf16_f32 v136, v192, v193\n\t" \
          "v_cvt_pk_bf16_f32 v137, v194, v195\n\t" \
          "v_fma_f32 v212, v216, %[qs], v212\n\t" \
          "v_fma_f32 v213, v217, %[qs], v213\n\t" \
          "v_cvt_pk_bf16_f32 v142, v196, v197\n\t" \
          "v_cvt_pk_bf16_f32 v143, v198, v199\n\t" \
          "v_mfma_f32_16x16x32_bf16 v[144:147], v[136:139], %[jk], 0\n\t" \
          "v_fma_f32 v214, v218, %[qs], v214\n\t" \
          "v_mfma_f32_16x16x32_bf16 v[148:151], v[140:143], %[jk], 0\n\t" \
          "v_fma_f32 v215, v219, %[qs], v215\n\t" \
          "s_waitcnt lgkmcnt(0)\n\t" \
          "v_fma_f32 v212, v220, %[qs], v212\n\t" \
          "v_fma_f32 v213, v221, %[qs], v213\n\t" \
          "v_cvt_pk_bf16_f32 v152, v144, v145\n\t" \
          "v_cvt_pk_bf16_f32 v153, v146, v147\n\t" \
          "v_fma_f32 v214, v222, %[qs], v214\n\t" \
          "v_fma_f32 v215, v223, %[qs], v215\n\t" \
          "v_cvt_pk_bf16_f32 v154, v148, v149\n\t" \
          "v_cvt_pk_bf16_f32 v155, v150, v151\n\t" \
          "v_pk_fma_f32 v[212:213], v[200:201], %[qs2], v[212:213] op_sel_hi:[1,0,1]\n\t" \
          "v_pk_fma_f32 v[214:215], v[202:203], %[qs2], v[214:215] op_sel_hi:[1,0,1]\n\t" \
          "v_mfma_f32_16x16x32_bf16 v[156:159], v[152:155], v[176:179], 0\n\t" \
          "v_exp_f32 v180, v212\n\t" \
          "v_mfma_f32_16x16x32_bf16 v[196:199], v[168:171], v[168:171], 0\n\t" \
          "v_exp_f32 v181, v213\n\t" \
          "v_mfma_f32_16x16x32_bf16 v[196:199], v[168:171], v[168:171], 0\n\t" \
          "ds_write_b64 %[own], v[156:157] offset:39936\n\t" \
          "v_exp_f32 v182, v214\n\t" \
          "v_mov_b32 v137, 0\n\t" \
          "v_exp_f32 v183, v215\n\t" \
          "v_cvt_pk_bf16_f32 v172, v180, v181\n\t" \
          "v_cvt_pk_bf16_f32 v173, v182, v183\n\t" \
          "v_pk_mul_f32 v[160:161], v[156:157], %[qs2] op_sel_hi:[1,0]\n\t" \
          "v_mov_b32_dpp v174, v172 quad_perm:[2,3,0,1] row_mask:0xf bank_mask:0xf\n\t" \
          "v_mov_b32_dpp v175, v173 quad_perm:[2,3,0,1] row_mask:0xf bank_mask:0xf\n\t" \
          "s_nop 0\n\t" \
          "s_waitcnt lgkmcnt(0)\n\t" \
          "v_mfma_f32_16x16x32_bf16 v[184:187], v[172:175], v[168:171], 0\n\t" \
          "s_barrier\n\t" \
          "ds_read_b64 v[144:145], %[p1] offset:39936\n\t" \
          "ds_read_b64 v[146:147], %[p2] offset:39936\n\t" \
          "v_mfma_f32_16x16x32_bf16 v[188:191], v[172:175], v[224:227], 0\n\t" \
          "ds_read_b64 v[148:149], %[p3] offset:39936\n\t" \
          "v_mfma_f32_16x16x32_bf16 v[192:195], v[172:175], v[228:231], 0\n\t" \
          "buffer_load_dword %[al], %[alo], %[rsm], %[mb] offen sc1\n\t" \
          "s_waitcnt lgkmcnt(2)\n\t" \
          "v_fma_f32 v160, v144, %[qs], v160\n\t" \
          "v_fma_f32 v161, v145, %[qs], v161\n\t" \
          "s_waitcnt lgkmcnt(1)\n\t" \
          "v_fma_f32 v160, v146, %[qs], v160\n\t" \
          "v_fma_f32 v161, v147, %[qs], v161\n\t" \
          "s_waitcnt lgkmcnt(0)\n\t" \
          "v_fma_f32 v160, v148, %[qs], v160\n\t" \
          "v_fma_f32 v161, v149, %[qs], v161\n\t" \
          "v_exp_f32 v162, v160\n\t" \
          "v_exp_f32 v163, v161\n\t" \
          "v_max_f32 v182, v160, v161\n\t" \
          "v_cvt_pk_bf16_f32 v136, v162, v163\n\t" \
          "v_cmp_nge_f32 vcc, %[th], v182\n\t" \
          "s_waitcnt vmcnt(1)\n\t" \
          "v_mfma_f32_16x16x16_bf16 v[164:167], v[136:137], v[168:169], v[184:187]\n\t" \
          "v_max_f32 v180, v184, v185\n\t" \
          "v_min_f32 v181, v184, v185\n\t" \
          "v_mfma_f32_16x16x16_bf16 v[188:191], v[136:137], v[232:233], v[188:191]\n\t" \
          "v_cmp_nge_f32 %[b2], %[hi], v180\n\t" \
          "v_cmp_nle_f32 %[b3], %[lo], v181\n\t" \
          "v_mfma_f32_16x16x16_bf16 v[192:195], v[136:137], v[234:235], v[192:195]\n\t" \
          "v_max_u32 v183, v232, v234\n\t" \
          "v_cmp_eq_u32 %[bad], -1, v183\n\t" \
          "v_rcp_f32 v162, v164\n\t" \
          "s_or_b64 %[b2], %[b2], %[b3]\n\t" \
          "v_rcp_f32 v163, v165\n\t" \
          "s_or_b64 %[b2], %[b2], vcc\n\t" \
          "v_pk_mul_f32 v[156:157], v[188:189], v[162:163]\n\t" \
          "s_or_b64 %[bad], %[bad], %[b2]\n\t" \
          "s_cselect_b64 exec, 0, -1\n\t" \
          "v_pk_mul_f32 v[158:159], v[192:193], v[162:163]\n\t" \
          "s_nop 0\n\t" \
          "v_cvt_pk_bf16_f32 v156, v156, v158\n\t" \
          "v_cvt_pk_bf16_f32 v157, v157, v159\n\t" \
          "v_min_u32 v156, -2, v156\n\t" \
          "v_min_u32 v157, -2, v157\n\t" \
          "buffer_store_dword v156, %[ho], %[rso], %[ob] offen sc1\n\t" \
          "buffer_store_dword v157, %[ho], %[rso], %[ob] offen offset:256 sc1\n\t" \
          "s_memrealtime %[po]\n\t" \
          "s_mov_b64 exec, -1\n\t" \
          "s_waitcnt lgkmcnt(0)\n\t"
constexpr int QK_SX = 12, QK_SM = 24, QK_KM = 16, QK_VX = 24, QK_KV = 32768, QK_EX = 36864;
__global__ void __launch_bounds__(256, 1) k_fused(FParams P) {
  __shared__ __attribute__((aligned(16))) char Lraw[sizeof(ChainLds)];
  unsigned long long pc0;
  asm volatile("s_getpc_b64 %0\nLfused_pc0:" : "=s"(pc0));
  const int w = __builtin_amdgcn_readfirstlane(threadIdx.x >> 6);
  if ((blockIdx.x & 7) >= 3) {
    asm volatile("Lfused_worker:");
    worker(P, Lraw, w);
    return;
  }
  ChainLds& L = *(ChainLds*)Lraw;
  u16 *vbuf = P.vbuf, *obuf = P.obuf, *membuf = P.membuf; unsigned* tmo = &P.ctl->tmo[0]; unsigned long long* prof = P.prof; (void)prof;
  const int xq = blockIdx.x & 7, slot = blockIdx.x >> 3, nq = xq < 2 ? 12 : 8;
  const int b = slot < nq ? 12 * xq + slot : NQK + 20 * xq + (slot - nq);
  const int alw = b < NQK ? (b % (NBL == 16 ? 9 : 18) == 0 ? (b / (NBL == 16 ? 9 : 18)) * 4 + w : -1) : (b == 33 ? 16 + w : (b == 95 ? 20 + w : -1));
  const __amdgpu_buffer_rsrc_t rs_o = __builtin_amdgcn_make_buffer_rsrc(obuf, 0, NSEG * OREG, 0x00020000);
  const __amdgpu_buffer_rsrc_t rs_m = __builtin_amdgcn_make_buffer_rsrc(membuf, 0, NSEG * MREG, 0x00020000);
#define PAR_BITS const unsigned* parp = P.par + opaque_lane(); const unsigned long long paro = __ballot(parp[0] != 0u), parm = __ballot(parp[64] != 0u), parv = __ballot(parp[128] != 0u)
  const __amdgpu_buffer_rsrc_t rs_v = __builtin_amdgcn_make_buffer_rsrc(vbuf, 0, NSEG * OREG, 0x00020000);
#define LANE_CONSTS const int lane = opaque_lane(), tid = 64 * w + lane, l15 = lane & 15, lq = lane >> 4, idx = (lane & 1) ? 0xEEEE : 0x4444; \
  const int mem_lane_off = (((lq >> 1) * 2 + (l15 >> 3)) * 16 + (lq & 1) * 8 + (l15 & 7)) * 16 + dbl(w * 4096); \
  const int wrow = 8 * (lq >> 1) + 2 * (lq & 1) + 4 * (l15 >> 3) + 16 * ((l15 >> 2) & 1), wsw = (wrow >> 1) & 3
#define CODE_PREFETCH do { int clen; asm volatile("s_mov_b32 %0, Lfused_worker-Lfused_pc0" : "=s"(clen)); \
      unsigned acc_ = 0; for (int o_ = tid * 64; o_ + 4 <= clen; o_ += 256 * 64) acc_ ^= *(const unsigned*)((const char*)pc0 + o_); asm volatile("" :: "v"(acc_)); } while (0)
  unsigned tA = 0;
  if (b < NQK) {
    const int h = b;
    LANE_CONSTS;
    v32u wa[8], wv_[7];
    const unsigned lb0 = (unsigned)(size_t)Lraw;
    const unsigned own = lb0 + w * 40960 + lane * 16;
    {
      const __amdgpu_buffer_rsrc_t rq = __builtin_amdgcn_make_buffer_rsrc((void*)(P.Wq + h * DHA + 64 * (w >> 1)), 0, 0x7fffffff, 0x00020000), rk = __builtin_amdgcn_make_buffer_rsrc((void*)(P.Wk + h * DHA + 64 * (w >> 1)), 0, 0x7fffffff, 0x00020000);
      const int go = (lane >> 3) * HDA * 2 + (((((lane & 7) >> 1) ^ ((lane >> 4) & 3)) << 1) | (lane & 1)) * 16;
      unsigned rd[4];
#pragma unroll
      for (int t = 0; t < 4; ++t) rd[t] = lb0 + w * 40960 + wrow * 128 + (l15 & 3) * 8 + ((t ^ (2 * (~w & 1)) ^ wsw) << 5);
#define QK_NTq ""
#define QK_NTk ""
      int t_;
      asm volatile(QK_LOADER : QK_DEFW, [t] "=&s"(t_)
                   : [rd0] "v"(rd[0]), [rd1] "v"(rd[1]), [rd2] "v"(rd[2]), [rd3] "v"(rd[3]), [own] "v"(own), [go] "v"(go), [rq] "s"(rq), [rk] "s"(rk), [lw] "s"(lb0 + w * 40960), [kb] "s"((8 * (w & 1) + QROT * 2 * (w & 2)) * 0x80000) : "memory", "scc");
    }
    PAR_BITS;
    const __amdgpu_buffer_rsrc_t rs_x = __builtin_amdgcn_make_buffer_rsrc(P.qkvA, 0, QSIZE, 0x00020000);
    const unsigned long long parq = __ballot(parp[192] != 0u);
    const int xk = qoff(parq, 16 + (h >> 1)) + (h & 1) * 4096, xv = qoff(parq, 32 + (h >> 1)) + (h & 1) * 4096;
    const __amdgpu_buffer_rsrc_t rs_k = __builtin_amdgcn_make_buffer_rsrc(P.kmem, 0, NSEG * MEM * HDA * 2, 0x00020000);
#define PF_ADDR const int tz = opaque_lane()
#define KV_SEGS(v) (min(__builtin_amdgcn_readlane(v, 0), __builtin_amdgcn_readlane(v, 1)) >> 1)
#define WAIT_KV(sg) do { for (unsigned spins = 0;; ++spins) { const unsigned v_ = ld_agent(&P.ctl->prg[32 + (tz & 1) * 32 + h][0]); kvok = KV_SEGS(v_); if (kvok > (sg)) break; \
        if (spin_fail(spins, tmo, 3u)) break; __builtin_amdgcn_s_sleep(8); } } while (0)
    int kvok = 0;
#define LOAD_KV(sg) do { const int so_ = (sg) * 2 * QBLK + w * 1024; \
        _Pragma("unroll") for (int q = 0; q < 4; ++q) \
          asm volatile("s_mov_b32 m0, %0\n\ts_nop 4\n\tbuffer_load_dwordx4 %1, %2, %3 offen sc1 lds" :: "s"(lb0 + w * 40960 + QK_KV + q * 1024), "v"((q < 2 ? tz : tz ^ ((tz >> 1) & 8)) * 16), "s"(rs_x), \
                       "s"(so_ + (q & 1) * QBLK + ((q >> 1) ? xv : xk)) : "memory"); } while (0)
    unsigned pz = 0;
#define LD_ASYNC(dst, voff, rs, soff) asm volatile("s_nop 4\n\tbuffer_load_dword %0, %1, %2, %3 offen sc1" : "=v"(dst) : "v"(voff), "s"(rs), "s"(soff) : "memory")
    {
      PF_ADDR;
      CODE_PREFETCH;
      WAIT_KV(0);
      LOAD_KV(0);
      LD_ASYNC(pz, kvok > 1 && tz < 32 ? tz * 128 : 0x7ffffff0, rs_x, (2 + (w & 1)) * QBLK + ((w >> 1) ? xv : xk));
    }
    const float QS = 0.08838834764831845f * 1.4426950408889634f;
    const unsigned j0 = lq == (l15 >> 2) && !(l15 & 1) ? 0x3f803f80u : 0u, j1 = lq == (l15 >> 2) && (l15 & 1) ? 0x3f803f80u : 0u;
    const v4u JK = {j0, j1, j0, j1};
    const unsigned vxp = lb0 + w * 40960 + QK_KV + 2048 + ((l15 & 3) * 16 + ((4 * lq + (l15 >> 2)) ^ ((l15 & 1) << 3))) * 16;
    const unsigned p1 = lb0 + (w ^ 1) * 40960 + lane * 16, p2 = lb0 + (w ^ 2) * 40960 + lane * 16, p3 = lb0 + (w ^ 3) * 40960 + lane * 16;
    unsigned soloacc = 0; (void)soloacc;
    unsigned pot = 0, kfl = 0, d1p = 140;
    const int mlo = mem_lane_off - dbl(w * 4096) + (w & 1) * 16384, hoff = dbl(h * 2048) + w * 512 + lane * 4;
    v4u vm = {0u, 0u, 0u, 0u};
    const int alo = lane < NBL ? ALM + lane * 128 : (lane >= 62 ? RECO + (lane - 62) * 4 : 0x7ffffff0);
    const __amdgpu_buffer_rsrc_t rs_c = __builtin_amdgcn_make_buffer_rsrc(P.ctl, 0, sizeof(Ctl), 0x00020000);
    for (int s = 0; s < NSEG; ++s) {
      int mb = MBASE(s), vb = VBASE(s), ob = OBASE(s), mb0 = mb + QROT * (w & 2) * 2048, mb1 = mb0 ^ 4096;
      unsigned al; unsigned long long bad, bad1, b2, b3, po_;
      if (__builtin_expect(s == 1, 0)) {
        unsigned recD; unsigned* bw = (unsigned*)(Lraw + QK_EX + 1024);
        if (w == 0) { rec_get(tA, recD, rs_m, mb + RECO, tmo, 8); *bw = tA - (recD & 0xffffu) - (recD >> 16); }
        lds_barrier();
        tA = __builtin_amdgcn_readfirstlane(*bw);
      }
      { int rem = (int)(tA - rtnow());
        if (s == 0 || rem > 20000) tA -= rem; }
      tA = __builtin_amdgcn_readfirstlane(tA); d1p = __builtin_amdgcn_readfirstlane(d1p);
#define QK_STO
      { unsigned cnt;
        asm volatile(QK_STEP
          : "=&{v[136:167]}"(wv_[4]), "=&{v[168:199]}"(wv_[5]), "=&{v[200:231]}"(wv_[6]), "+{v[232:235]}"(vm), [m] "+v"(pz), "+v"(kfl), [al] "=&v"(al),
            [bad] "=&s"(bad), [bad1] "=&s"(bad1), [b2] "=&s"(b2), [b3] "=&s"(b3), [po] "=&s"(po_), [cnt] "=&s"(cnt), [mb0] "+s"(mb0), [mb1] "+s"(mb1) QK_STO
          : "{a[0:31]}"(wa[0]), "{a[32:63]}"(wa[1]), "{a[64:95]}"(wa[2]), "{a[96:127]}"(wa[3]), "{a[128:159]}"(wa[4]), "{a[160:191]}"(wa[5]), "{a[192:223]}"(wa[6]), "{a[224:255]}"(wa[7]),
            "{v[8:39]}"(wv_[0]), "{v[40:71]}"(wv_[1]), "{v[72:103]}"(wv_[2]), "{v[104:135]}"(wv_[3]),
            [own] "v"(own), [p1] "v"(p1), [p2] "v"(p2), [p3] "v"(p3), [vxp] "v"(vxp), [mlo] "v"(mlo), [jk] "v"(JK), [ho] "v"(hoff), [idx] "v"(idx), [alo] "v"(alo),
            [rsm] "s"(rs_m), [rsv] "s"(rs_v), [rso] "s"(rs_o), [rsc] "s"(rs_c), [mb] "s"(mb), [vb] "s"(vb), [ob] "s"(ob), [twin] "s"(0xccccccccccccccccull),
            [one] "s"(0x3f803f80u), [qs] "s"(QS), [qs2] "s"((unsigned long long)__float_as_uint(QS)), [hi] "s"(0x1p40f), [lo] "s"(0x1p-40f), [th] "s"(40.f), [ta] "s"(tA),
            [tmo] "s"((int)offsetof(Ctl, tmo)), [mbz] "s"(MBASE(0)), [ald] "s"(d1p - P.sh1), [als] "s"(mb + ALM + (alw & 15) * 128), [alx] "s"((unsigned long long)(unsigned)__builtin_amdgcn_readfirstlane(s > 0 && alw >= 0 ? -1 : 0))
          : "vcc", "scc", "memory"); }
      {
        const v4u Km = {wv_[4][QK_KM], wv_[4][QK_KM + 1], wv_[4][QK_KM + 2], wv_[4][QK_KM + 3]};
        if (__builtin_expect(bad != 0, 0)) {
#pragma unroll 1
          for (unsigned fails = 0; !__all((vm.x > vm.z ? vm.x : vm.z) != 0xffffffffu); ++fails) {
            __builtin_amdgcn_s_sleep(2);
            vm.x = __builtin_amdgcn_raw_buffer_load_b32(rs_v, hoff, vb, 16); vm.z = __builtin_amdgcn_raw_buffer_load_b32(rs_v, hoff, vb + 256, 16);
            if (spin_fail(fails, tmo, 1u)) break;
          }
          float sx[4], sm[2];
#pragma unroll
          for (int e = 0; e < 4; ++e) sx[e] = __uint_as_float(wv_[6][QK_SX + e]);
          sm[0] = __uint_as_float(wv_[4][QK_SM]); sm[1] = __uint_as_float(wv_[4][QK_SM + 1]);
          float mx = fmaxf(fmaxf(sx[0], sx[1]), fmaxf(sx[2], sx[3]));
          mx = fmaxf(mx, __int_as_float(__builtin_amdgcn_update_dpp(0, __float_as_int(mx), 0x4E, 0xf, 0xf, false)));
          mx = xmax32(xmax16(fmaxf(mx, fmaxf(sm[0], sm[1]))));
          if (b2 == 0) mx = 0.f;
          v4u px; px.x = pk2(__builtin_amdgcn_exp2f(sx[0] - mx), __builtin_amdgcn_exp2f(sx[1] - mx)); px.y = pk2(__builtin_amdgcn_exp2f(sx[2] - mx), __builtin_amdgcn_exp2f(sx[3] - mx));
          px.z = __builtin_amdgcn_update_dpp(0, px.x, 0x4E, 0xf, 0xf, false); px.w = __builtin_amdgcn_update_dpp(0, px.y, 0x4E, 0xf, 0xf, false);
          const v2u pm = {pk2(__builtin_amdgcn_exp2f(sm[0] - mx), __builtin_amdgcn_exp2f(sm[1] - mx)), 0u};
          const v4u on = {0x3f803f80u, 0x3f803f80u, 0x3f803f80u, 0x3f803f80u};
          const v4u vx0 = {wv_[6][QK_VX], wv_[6][QK_VX + 1], wv_[6][QK_VX + 2], wv_[6][QK_VX + 3]}, vx1 = {wv_[6][QK_VX + 4], wv_[6][QK_VX + 5], wv_[6][QK_VX + 6], wv_[6][QK_VX + 7]};
          f32x4 c0, c1, ls;
          asm volatile("s_nop 1\n\t"
                       "v_mfma_f32_16x16x32_bf16 %0, %3, %4, 0\n\t"
                       "v_mfma_f32_16x16x32_bf16 %1, %3, %5, 0\n\t"
                       "v_mfma_f32_16x16x32_bf16 %2, %3, %6, 0\n\t"
                       "s_nop 7\n\t"
                       "v_mfma_f32_16x16x16_bf16 %0, %7, v[232:233], %0\n\t"
                       "v_mfma_f32_16x16x16_bf16 %1, %7, v[234:235], %1\n\t"
                       "v_mfma_f32_16x16x16_bf16 %2, %7, v[168:169], %2\n\t"
                       "s_nop 7"
                       : "=&v"(c0), "=&v"(c1), "=&v"(ls) : "v"(px), "v"(vx0), "v"(vx1), "{v[168:171]}"(on), "v"(pm), "{v[232:235]}"(vm));
          const float i0 = __builtin_amdgcn_rcpf(ls[0]), i1 = __builtin_amdgcn_rcpf(ls[1]);
          __builtin_amdgcn_raw_buffer_store_b32(nosent(pk2(c0[0] * i0, c1[0] * i0)), rs_o, hoff, ob, 16);
          __builtin_amdgcn_raw_buffer_store_b32(nosent(pk2(c0[1] * i1, c1[1] * i1)), rs_o, hoff + 256, ob, 16);
          po_ = rtnow();
        }
        SB0;
        const unsigned po = __builtin_amdgcn_readfirstlane((unsigned)po_);
        __builtin_amdgcn_raw_buffer_store_b32(nosent(po), rs_m, (h | w | opaque_lane()) == 0 ? RECO + 128 : 0x7ffffff0, mb, 16);
        asm volatile("s_waitcnt vmcnt(%1)" : "+v"(al) : "i"(ALP < 3 ? 3 : 1) : "memory");
        pot = po;
      if (s < NSEG - 1) {
        unsigned recD = __builtin_amdgcn_readlane(al, 63);
        tA = __builtin_amdgcn_readlane(al, 62);
        if (__builtin_expect(tA == 0xffffffffu || recD == 0xffffffffu, 0)) rec_get(tA, recD, rs_m, mb + RECO, tmo);
        {
          unsigned tB = tA - (recD >> 16);
          (void)al_eval(al, tB);
          tA = tB + (recD >> 16); if (s > 0) d1p = recD & 0xffffu; else tA = pot;
        }
        PF_ADDR;
        kvok = max(kvok, KV_SEGS(kfl));
        if (__builtin_expect(kvok <= s + 1, 0)) WAIT_KV(s + 1);
        LOAD_KV(s + 1);
        LD_ASYNC(kfl, kvok < s + 5 && kvok < NSEG && tz < 2 ? tz * 4096 : 0x7ffffff0, rs_c, (int)offsetof(Ctl, prg) + (32 + h) * 128);
        const int po2 = s + 2 < kvok && !(tz & 32) ? tz * 128 : 0x7ffffff0;
        LD_ASYNC(pz, po2, rs_x, (2 * (s + 2) + (w & 1)) * QBLK + ((w >> 1) ? xv : xk));
      }
      { const int lz = opaque_lane();
        const int ko = (lz & 2) ? 0x7ffffff0 : h * 2048 + w * 512 + (2 * (lz >> 2) + (lz & 1)) * 16;
        __builtin_amdgcn_raw_buffer_store_b128((v4u){nosent(Km.x), nosent(Km.y), nosent(Km.z), nosent(Km.w)}, rs_k, ko, s * (MEM * HDA * 2), 16); }
      }
    }
  } else {
#define SMF_A(ACC, A, W) asm volatile("v_smfmac_f32_16x16x64_bf16 %0, %1, %2, %3" : "+v"(ACC) : "v"(A), "a"(W), "v"(idx)); SB0
#define SMF_V(ACC, A, W) asm volatile("v_smfmac_f32_16x16x64_bf16 %0, %1, %2, %3" : "+v"(ACC) : "v"(A), "v"(W), "v"(idx)); SB0
#define PIN(x) asm volatile("" : "+v"(x))
    LANE_CONSTS;
    const unsigned c00 = (unsigned)now(), t00 = rtnow();
    const int c = b - NQK, rank = RANKAL == 2 ? 0 : RANKAL == 3 ? (c & 2) : RANKAL ? (c >> 2) & 7 : ((c >> 2) & 1) | ((c & 3) << 1), cg = c >> 1, rh = c & 1;
    v8u wv[16], wo[32];
#define OHEAD(i) (16 * (((i) >> 2) ^ (rank & 1)) + ((4 * ((i) & 3) + w + 4 * (rank >> 1)) & 15))
    {
      { const int r = lane >> 3, h2 = (r >> 1) & 3;
        const int ch = (((((lane & 7) >> 1) ^ h2) << 1) | (lane & 1));
#pragma unroll 4
        for (int q = 0; q < 32; ++q) {
          const int pc = 32 * w + q;
          __builtin_amdgcn_global_load_lds((const unsigned*)(P.Wv + (size_t)(8 * pc + r) * HDA + 64 * c + ch * 8), (unsigned*)(Lraw + pc * 1024), 16, 0, NTV);
        } }
      {
        char* stg = Lraw + w * 32768;
        const u16* src = P.Wo + (size_t)(lane >> 3) * DM + 64 * (cg >> 1) + (lane & 7) * 8;
#define LDO(i) _Pragma("unroll 4") for (int q = 0; q < 16; ++q) \
          __builtin_amdgcn_global_load_lds((const unsigned*)(src + (size_t)(OHEAD(i) * 128 + 8 * q) * DM), (unsigned*)(stg + ((i) & 1) * 16384 + q * 1024), 16, 0, NTO)
        unsigned vb[4];
#pragma unroll
        for (int t = 0; t < 4; ++t) vb[t] = (unsigned)(size_t)stg + wrow * 128 + (l15 & 3) * 8 + ((t ^ w ^ 3 ^ wsw) << 5);
        SFOR(hf, 0, 2)
          asm volatile("s_waitcnt vmcnt(16)" ::: "memory");
          v2u e[8][4];
#pragma unroll
          for (int i = 0; i < 8; ++i)
            asm volatile("ds_read_b64_tr_b16 %0, %4 offset:%5\n\tds_read_b64_tr_b16 %1, %4 offset:%6\n\tds_read_b64_tr_b16 %2, %4 offset:%7\n\tds_read_b64_tr_b16 %3, %4 offset:%8\n\ts_waitcnt lgkmcnt(0)"
                         : "=&v"(e[i][0]), "=&v"(e[i][1]), "=&v"(e[i][2]), "=&v"(e[i][3])
                         : "v"(vb[i & 3]), "i"((2 * hf + (i >> 2)) * 8192), "i"((2 * hf + (i >> 2)) * 8192 + 128), "i"((2 * hf + (i >> 2)) * 8192 + 4096), "i"((2 * hf + (i >> 2)) * 8192 + 4224) : "memory");
          LDO(hf);
#pragma unroll
          for (int i = 0; i < 8; ++i) wv[8 * hf + i] = (v8u){e[i][0].x, e[i][0].y, e[i][1].x, e[i][1].y, e[i][2].x, e[i][2].y, e[i][3].x, e[i][3].y};
        SEND
        const unsigned base = (unsigned)(size_t)stg + (32 * (lq >> 1) + 2 * (lq & 1) + 4 * (l15 >> 3) + 16 * ((l15 >> 2) & 1)) * 128 + (cg & 1) * 64 + (l15 & 3) * 8;
        SFOR(i, 0, 8)
          if constexpr (i < 7) asm volatile("s_waitcnt vmcnt(16)" ::: "memory"); else asm volatile("s_waitcnt vmcnt(0)" ::: "memory");
          v2u e[4][4];
#pragma unroll
          for (int t = 0; t < 4; ++t)
            asm volatile("ds_read_b64_tr_b16 %0, %4 offset:%5\n\tds_read_b64_tr_b16 %1, %4 offset:%6\n\tds_read_b64_tr_b16 %2, %4 offset:%7\n\tds_read_b64_tr_b16 %3, %4 offset:%8"
                         : "=&v"(e[t][0]), "=&v"(e[t][1]), "=&v"(e[t][2]), "=&v"(e[t][3])
                         : "v"(base), "i"((i & 1) * 16384 + (t >> 1) * 1024 + (t & 1) * 32), "i"((i & 1) * 16384 + (t >> 1) * 1024 + (t & 1) * 32 + 128),
                           "i"((i & 1) * 16384 + (t >> 1) * 1024 + (t & 1) * 32 + 64 * 128), "i"((i & 1) * 16384 + (t >> 1) * 1024 + (t & 1) * 32 + 65 * 128) : "memory");
          asm volatile("s_waitcnt lgkmcnt(0)" : "+v"(e[0][0]), "+v"(e[0][1]), "+v"(e[0][2]), "+v"(e[0][3]), "+v"(e[1][0]), "+v"(e[1][1]), "+v"(e[1][2]), "+v"(e[1][3]),
                                                "+v"(e[2][0]), "+v"(e[2][1]), "+v"(e[2][2]), "+v"(e[2][3]), "+v"(e[3][0]), "+v"(e[3][1]), "+v"(e[3][2]), "+v"(e[3][3]) :: "memory");
          if constexpr (i + 2 < 8) { LDO(i + 2); }
#pragma unroll
          for (int t = 0; t < 4; ++t) { wo[4 * i + t] = (v8u){e[t][0].x, e[t][0].y, e[t][1].x, e[t][1].y, e[t][2].x, e[t][2].y, e[t][3].x, e[t][3].y}; asm volatile("" : "+a"(wo[4 * i + t])); }
        SEND
#undef LDO
        lds_barrier();
      }
    }
    PAR_BITS;
    constexpr int RST = 258;
    int redo = lq * 24 + l15 + w * 96; asm volatile("" : "+v"(redo));
    f32x2* red = (f32x2*)&L.sW[0][0][0][0] + redo;
    f32x2* vrw[3];
#pragma unroll
    for (int t = 0; t < 3; ++t) vrw[t] = (f32x2*)L.sA + t * RST + (t ^ w ^ 3) * 64 + lane;
    const f32x2* vrr = (const f32x2*)L.sA + w * 64 + lane;
    const int o_lane_off = ((lq * 2 + rh) * 16 + l15) * 16;
    const unsigned redr = (unsigned)(size_t)((const f32x2*)&L.sW[0][0][0][0] + ((lane >> 3) & 3) * 24 + 8 * (lane >> 5) + (lane & 7));
    const bool keeper = (c == 0 && w == 3);
    int d1 = P.dmv * 16, d2 = P.dov * 16, m1 = (P.dq2 + P.im) * 16, m2 = (P.dq + P.im) * 16;
    int LV = 70;
    bool prev_clean = false;
    unsigned recD = 140;
    const float tpc = 16.f * (float)(rtnow() - t00) / (float)((unsigned)now() - c00);
    int e1 = __builtin_amdgcn_readfirstlane((int)(P.dmv * tpc)), e2 = __builtin_amdgcn_readfirstlane((int)(P.dov * tpc));
    d1 = e1 + m1; d2 = e2 + m2;
    int kx2 = 0, kfl = 0; unsigned kto = 0, kA0 = 0, kpm = 0; (void)kpm;
    int* failw = (int*)L.xs;
    if (tid < 2) failw[tid] = 0;
    CODE_PREFETCH;
    int ga, vo; f32x4 va[4];
#define V_PREP(st) do { ga = MBASE(st) + mem_lane_off; vo = VBASE(st) + dbl(c * 1024) + tid * 4; \
      _Pragma("unroll") for (int t = 0; t < 4; ++t) va[t] = zero4(); \
      asm volatile("" : "+v"(ga), "+v"(vo), "+v"(va[0]), "+v"(va[1]), "+v"(va[2]), "+v"(va[3])); } while (0)
    V_PREP(0);
    f32x16 dd = {};
    for (int s = 0; s < NSEG; ++s) {
      if (s > 0) wait_rt(tA + P.dv);
      f32x2 fv;
#define WARMD SB0
#define V_PASS(ok) do { v4u a[4]; unsigned m; \
        _Pragma("unroll") for (int k = 0; k < 4; ++k) { a[k] = __builtin_amdgcn_raw_buffer_load_b128(rs_m, ga + k * 1024, 0, 16); SB0; if (PACEV && k < 3) { asm volatile("s_nop %0" :: "i"(PACEV / 4 - 2)); SB0; } } \
        _Pragma("unroll") for (int z = 0; z < NWV; ++z) { WARMD; } \
        _Pragma("unroll") for (int u = 0; u < 16; ++u) { \
          const int k = u < 8 ? u >> 2 : 2 + (u & 1), t = u < 8 ? u & 3 : (u - 8) >> 1; \
          SMF_V(va[t], a[k], wv[4 * k + t]); \
          if (u == 0) m = umax3(a[0].x, a[0].y, a[0].z); \
          if (u == 1) m = m > a[0].w ? m : a[0].w; \
          if (u >= 4 && u < 10) m = (u & 1) ? umax3(m, a[(u - 2) >> 1].z, a[(u - 2) >> 1].w) : umax3(m, a[(u - 2) >> 1].x, a[(u - 2) >> 1].y); \
          if (u < 10) { PIN(m); SB0; } \
          if (u == 10) { ok = __ballot(m == 0xffffffffu); asm volatile("" : "+s"(ok)); SB0; } \
          if (u == 14) { *vrw[0] = fv; SB0; } \
          if (u >= 12) { fv[u & 1] = va[(u - 12) >> 1][(u & 1) * 2] + va[(u - 12) >> 1][(u & 1) * 2 + 1]; PIN(fv[u & 1]); SB0; } \
        } } while (0)
      unsigned long long vbad;
      V_PASS(vbad);
      if (__builtin_expect(vbad != 0, 0)) {
#pragma unroll 1
        for (unsigned fails = 0;; ++fails) {
          if (fails > 8) __builtin_amdgcn_s_sleep(4);
          asm volatile("s_nop 7" : "+v"(va[0]), "+v"(va[1]), "+v"(va[2]), "+v"(va[3]));
#pragma unroll
          for (int t = 0; t < 4; ++t) va[t] = zero4();
          asm volatile("s_nop 1" : "+v"(va[0]), "+v"(va[1]), "+v"(va[2]), "+v"(va[3]));
          V_PASS(vbad);
          if (vbad == 0 || spin_fail(fails, tmo, 1u)) break;
        }
      }
      {
        f32x2 f2; f2[0] = va[2][0] + va[2][1]; PIN(f2[0]); f2[1] = va[2][2] + va[2][3]; PIN(f2[1]); SB0;
        WARMD;
        *vrw[1] = fv; SB0; *vrw[2] = f2; SB0;
        lds_barrier();
        const f32x2 v0 = vrr[0], v1 = vrr[RST], v2 = vrr[2 * RST]; SB0;
        WARMD;
        fv[0] = va[3][0] + va[3][1]; PIN(fv[0]); fv[1] = va[3][2] + va[3][3]; PIN(fv[1]); SB0;
        const f32x2 r = (fv + v0) + (v1 + v2);
        unsigned vv = nosent(pk2(r[0], r[1])); PIN(vv); SB0;
        __builtin_amdgcn_raw_buffer_store_b32(vv, rs_v, vo, 0, 16); SB0;
      }
      const unsigned tv = rtnow();
      SB0;
      if (keeper && s > 0) {
        asm volatile("" ::: "memory");
        if (lane == 0) __hip_atomic_store(&P.ctl->prog[0], (unsigned)(s - 1), RLX_AGENT);
        const int kx1 = kto == 0xffffffffu ? e1 >> 4 : (int)(kto - kA0);
        const bool okA = kfl & 1, okB = kfl & 2;
        if (s > 1) {
          const int gain = s < 5 ? P.gain0 : P.gain;
          if (okA) {
            int x = kx1 * 16 - e1; x = x > 320 ? 320 : x; e1 += (x * gain) >> 4;
            if (!okB) m1 = m1 < 60 * 16 ? m1 + P.inc : m1; else if (m1 > P.dq2 * 16) m1 -= P.dec;
            if (m2 > P.dq * 16) m2 -= P.dec;
          } else if (prev_clean) m2 = m2 < 60 * 16 ? m2 + P.inc : m2;
          if (kfl & 4) { int x = kx2 * 16 - e2; x = x > 320 ? 320 : x; e2 += (x * gain) >> 4; }
          d1 = e1 + m1; d2 = e2 + m2;
        }
        prev_clean = okB;
      }
      if (s == NSEG - 1) break;
      const unsigned tA0 = tA;
      LV = (int)(tv - tA0);
      V_PREP(s + 1);
      int oo[8], mo = MBASE(s + 1) + dbl(c * 256) + lane * 4, alo = lane < (keeper ? 8 : NAL) ? MBASE(s) + ALM + 2048 + lane * 128 : (keeper && lane == 63 ? MBASE(s) + RECO + 128 : 0x7ffffff0);
#pragma unroll
      for (int i = 0; i < 8; ++i) oo[i] = OBASE(s) + dbl(OHEAD(i) * 2048) + o_lane_off;
      f32x4 oa[4] = {zero4(), zero4(), zero4(), zero4()};
      asm volatile("" : "+v"(oo[0]), "+v"(oo[1]), "+v"(oo[2]), "+v"(oo[3]), "+v"(oo[4]), "+v"(oo[5]), "+v"(oo[6]), "+v"(oo[7]), "+v"(mo), "+v"(alo),
                        "+v"(oa[0]), "+v"(oa[1]), "+v"(oa[2]), "+v"(oa[3]));
      unsigned flB = 0, tB = tv;
      if (s > 0) {
        wait_rt(tA0 + (recD & 0xffffu) - 50);
        const unsigned al = __builtin_amdgcn_raw_buffer_load_b32(rs_m, lane < NBL ? ALM + lane * 128 : (lane >= 62 ? RECO + (lane - 62) * 4 : 0x7ffffff0), MBASE(s), 16);
        tA = __builtin_amdgcn_readlane(al, 62); recD = __builtin_amdgcn_readlane(al, 63);
        if (__builtin_expect(tA == 0xffffffffu || recD == 0xffffffffu, 0)) rec_get(tA, recD, rs_m, MBASE(s) + RECO, tmo);
        tB = tA - (recD >> 16);
        flB = al_eval(al, tB);
        tA = tB + (recD >> 16);
      }
      flB = __builtin_amdgcn_readfirstlane(flB); tA = __builtin_amdgcn_readfirstlane(tA); tB = __builtin_amdgcn_readfirstlane(tB);
      asm volatile("" : "+s"(flB), "+s"(tA), "+s"(tB));
      wait_rt(tB);
#define O_PART(i0, i1) _Pragma("unroll") for (int i = i0; i < i1; ++i) { \
          _Pragma("unroll") for (int t = 0; t < 4; ++t) { SMF_A(oa[t], f[i], wo[4 * i + t]); \
            if (t < 2) { m = t ? umax3(m, f[i].z, f[i].w) : umax3(m, f[i].x, f[i].y); asm volatile("" : "+v"(m)); SB0; } \
            if (i == i1 - 1 && t == 2) { obad = __ballot(m == 0xffffffffu); asm volatile("" : "+s"(obad)); SB0; } } }
#define O_PASS(first) do { v4u f[8]; \
        _Pragma("unroll") for (int i = 0; i < 8; ++i) { f[i] = __builtin_amdgcn_raw_buffer_load_b128(rs_o, oo[i], 0, 16); SB0; if (first && i < 7) PACE_O; } \
        if (!first) { al_set(rs_m, MBASE(s) + ALM, alnow ? alw : -1, tA + P.sh2); SB0; } \
        unsigned m = 0; \
        O_PART(0, EARLY) \
        if (__builtin_expect(obad == 0, 1)) { O_PART(EARLY, 8) } \
        } while (0)
      unsigned long long obad; bool alnow = s > 0;
      O_PASS(true);
      if (__builtin_expect(obad != 0, 0)) {
        asm volatile("s_nop 7"); SB0;
        failw[1] = s + 1;
#pragma unroll 1
        for (unsigned fails = 0;; ++fails) {
          bool dead = false;
          if (s == 0 || fails > 1) for (unsigned sp = 0;; ++sp) {
            __builtin_amdgcn_s_sleep(4);
            v4u z = __builtin_amdgcn_raw_buffer_load_b128(rs_o, oo[7], 0, 16);
            if (__all(chk4(0u, z) != 0xffffffffu)) break;
            if (spin_fail(sp, tmo, 1u)) { dead = true; break; }
          }
          asm volatile("s_nop 7"); SB0;
#pragma unroll
          for (int t = 0; t < 4; ++t) oa[t] = zero4();
          asm volatile("s_nop 1" : "+v"(oa[0]), "+v"(oa[1]), "+v"(oa[2]), "+v"(oa[3]));
          O_PASS(false);
          alnow = false;
          if (obad == 0) break;
          asm volatile("s_nop 7"); SB0;
          if (dead || spin_fail(fails, tmo, 1u)) break;
        }
      }
      {
        f32x2 r;
        asm volatile("v_add_f32 %0, %2, %3\n\t"
                     "v_add_f32 %1, %4, %5\n\t"
                     "s_nop 0\n\t"
                     "v_add_f32 %0, %0, %6\n\t"
                     "v_add_f32 %0, %0, %7\n\t"
                     "s_nop 1\n\t"
                     "v_add_f32 %1, %1, %8\n\t"
                     "v_add_f32 %1, %1, %9"
                     : "=&v"(r[0]), "=&v"(r[1]) : "v"(oa[0][0]), "v"(oa[0][1]), "v"(oa[1][0]), "v"(oa[1][1]), "v"(oa[2][2]), "v"(oa[2][3]), "v"(oa[3][2]), "v"(oa[3][3]), "v"(oa[0]), "v"(oa[1]), "v"(oa[2]), "v"(oa[3]));
        *red = r; SB0;
      }
      const unsigned alr = __builtin_amdgcn_raw_buffer_load_b32(rs_m, alo, 0, 16); SB0;
      { f32x2 p0, p1, p2, p3; unsigned pk;
        asm volatile("s_cmp_lg_u32 %[w], 0\n\t"
                     "s_waitcnt lgkmcnt(0)\n\t"
                     "s_barrier\n\t"
                     "s_cbranch_scc1 1f\n\t"
                     "ds_read_b64 %0, %[ra]\n\t"
                     "ds_read_b64 %1, %[ra] offset:768\n\t"
                     "ds_read_b64 %2, %[ra] offset:1536\n\t"
                     "ds_read_b64 v[252:253], %[ra] offset:2304\n\t"
                     "s_waitcnt lgkmcnt(2)\n\t"
                     "v_pk_add_f32 %0, %0, %1\n\t"
                     "s_waitcnt lgkmcnt(0)\n\t"
                     "v_pk_add_f32 %2, %2, v[252:253]\n\t"
                     "s_nop 0\n\t"
                     "v_pk_add_f32 v[252:253], %0, %2\n\t"
                     "s_nop 0\n\t"
                     "v_cvt_pk_bf16_f32 %4, v252, v253\n\t"
                     "v_min_u32 %4, -2, %4\n\t"
                     "buffer_store_dword %4, %[mo], %[rs], 0 offen sc1\n\t"
                     "s_branch 2f\n\t"
                     "1:\n\ts_sleep 1\n\t"
                     "2:"
                     : "=&v"(p0), "=&v"(p1), "=&v"(p2), "=&{v[252:253]}"(p3), "=&v"(pk) : [w] "s"(w), [ra] "v"(redr), [mo] "v"(mo), [rs] "s"(rs_m) : "scc", "memory"); }
      SB0;
      const int fw1 = failw[1];
      const unsigned pm = rtnow();
      const bool cleanB = __builtin_amdgcn_readfirstlane(fw1) != s + 1;
      unsigned flA = 0;
      if (s == 0) tA = pm + (c == 0 ? P.pad & 255 : P.pad >> 8);
      else { if (!cleanB) tA += P.sh2; flA = al_eval(alr, tA) | (cleanB ? 0u : 1u); if ((int)(pm + 10 - tA) > 0) tA = pm + 10; }
      if (keeper) {
        const unsigned r1 = (d1 >> 4) + (s < 2 ? (P.boot2 >> (8 * s)) & 255 : 0), r2 = (d2 >> 4) + (s < 2 ? (P.boot3 >> (8 * s)) & 255 : 0);
        __builtin_amdgcn_raw_buffer_store_b64((v2u){nosent(tA + r1 + r2), r1 | (r2 << 16)}, rs_m, lane == 0 ? MBASE(s + 1) + RECO : 0x7ffffff0, 0, 16);
        kto = __builtin_amdgcn_readlane(alr, 63); kx2 = (int)(pm - tB); kA0 = tA0; asm volatile("" : "+s"(flB)); kfl = (flB == 0 ? 1 : 0) | (flA == 0 ? 2 : 0) | (cleanB ? 4 : 0); kpm = pm;
      }
    }
  }
}

__global__ void __launch_bounds__(512) k_prep(PrepParams P) {
  __shared__ unsigned lat[2];
  unsigned long long pc0;
  asm volatile("s_getpc_b64 %0\nLprep_pc0:" : "=s"(pc0));
  const int b = blockIdx.x, tid = threadIdx.x;
  const v4u ff = {0xffffffffu, 0xffffffffu, 0xffffffffu, 0xffffffffu};
  if (b < 3 * NSEG + 48) {
    const int reg = b;
    const bool iso = (reg >> 6) != 1; const int s = reg & (NSEG - 1);
    char* base = reg >= 3 * NSEG ? P.qkv + (size_t)s * QPAGE : (iso ? (reg < NSEG ? P.obuf : P.vbuf) + (size_t)s * OREG : P.membuf + (size_t)s * MREG);
    if (tid < 64) {
      const __amdgpu_buffer_rsrc_t rs = __builtin_amdgcn_make_buffer_rsrc(base, 0, 16384, 0x00020000);
      unsigned best0 = ~0u, best1 = ~0u;
#pragma unroll 1
      for (int t = 0; t < PROBES; ++t) {
        const unsigned long long t0 = __builtin_amdgcn_s_memtime();
        __builtin_amdgcn_raw_buffer_store_b128(ff, rs, tid < (t ? 8 : 16) ? ((t & 1) + (tid >> 3)) * 8192 + (tid & 7) * 16 : 0x7ffffff0, 0, 16);
        asm volatile("s_waitcnt vmcnt(0)" ::: "memory");
        const unsigned dt = (unsigned)(__builtin_amdgcn_s_memtime() - t0);
        if (t & 1) best1 = dt < best1 ? dt : best1; else if (t) best0 = dt < best0 ? dt : best0;
      }
      if (tid == 0) { lat[0] = best0; lat[1] = best1; }
    }
    __syncthreads();
    unsigned xcc; asm volatile("s_getreg_b32 %0, hwreg(HW_REG_XCC_ID)" : "=s"(xcc));
    const int sp = (lat[1] < lat[0]) ^ (((xcc ^ (xcc - b + 1)) >> 2) & 1);
    if (tid == 0) P.par[reg] = reg >= 3 * NSEG ? sp ^ (int)P.far : sp;
    v4u* d = (v4u*)(base + sp * 8192);
    const int nblk = reg >= 3 * NSEG ? 0 : (iso ? 8 : 3);
    for (int i = tid; i < nblk * 512; i += 512) {
      v4u v = ff;
      if (!iso && s == 0) {
        if (i < 1024) {
          const u16* src = P.mem0 + (((i >> 4) & 1) * 4 + ((i >> 1) & 3)) * DM + 32 * (i >> 5) + 8 * ((i >> 3) & 1) + 4 * (i & 1);
#pragma unroll
          for (int p = 0; p < 4; ++p) { unsigned x = src[p] | ((unsigned)src[p + 16] << 16); v[p] = x < 0xfffffffeu ? x : 0xfffffffeu; }
        } else if (i == 1024) v = (v4u){0u, 0u, 0u, 0u};
      }
      __builtin_amdgcn_raw_buffer_store_b128(v, __builtin_amdgcn_make_buffer_rsrc(d, 0, OREG, 0x00020000), ((i >> 9) * 1024 + (i & 511)) * 16, 0, 16);
    }
    if (reg < 3 * NSEG) return;
  } else if (tid == 0) P.par[b] = 0;
  const int fb = b - 3 * NSEG;
  {
    int clen; asm volatile("s_mov_b32 %0, Lprep_pc0-Lfused_pc0" : "=s"(clen));
    unsigned acc_ = 0;
    for (int o_ = 64 + tid * 64; o_ <= clen; o_ += 512 * 64) acc_ ^= *(const unsigned*)((const char*)pc0 - o_);
    asm volatile("" :: "v"(acc_)); }
  v4u pa = {0u, 0u, 0u, 0u};
  if (P.pfr) {
    pa = P.ax[fb * 512 + tid];
#pragma unroll 4
    for (unsigned i = fb * 512 + tid; i < (unsigned)P.pfr * 512; i += 64 * 512) pa ^= P.wk[i] ^ P.wv[i];
  }
  for (unsigned long long i = (unsigned long long)fb * 512 + tid; i < P.sent_vec; i += (unsigned long long)(PSENT - 3 * NSEG) * 512)
    __builtin_amdgcn_raw_buffer_store_b128(ff, __builtin_amdgcn_make_buffer_rsrc(P.sent, 0, (int)P.sent_vec * 16, 0x00020000), (int)i * 16, 0, 16);
  if (KF0) __builtin_amdgcn_raw_buffer_store_b128(ff, __builtin_amdgcn_make_buffer_rsrc((char*)P.sent - TSEG0 * MEM * HDA * 2, 0, KF0 * MEM * HDA * 2, 0x00020000), (fb * 512 + tid) * 16, 0, 16);
  asm volatile("" :: "v"(pa));
  if (fb == 5) for (unsigned i = tid; i < (unsigned)P.ctl_vec; i += 512) P.ctl[i] = (v4u){i < 64 ? 8u : 0u, 0u, 0u, 0u};
}

extern "C" void kernel_launch(void* const* d_in, const int* in_sizes, int n_in,
                              void* d_out, int out_size, void* d_ws, size_t ws_size,
                              hipStream_t stream) {
  const u16* A_x = (const u16*)d_in[0];
  const u16* A_mem0 = (const u16*)d_in[1];
  const u16* A_Wq = (const u16*)d_in[2];
  const u16* A_Wk = (const u16*)d_in[3];
  const u16* A_Wv = (const u16*)d_in[4];
  const u16* A_Wo = (const u16*)d_in[5];
  const u16* B_x = (const u16*)d_in[7];
  const u16* B_wqkv = (const u16*)d_in[8];
  const u16* B_wo = (const u16*)d_in[9];
  u16* out = (u16*)d_out;

  char* ws = (char*)d_ws;
  size_t off = 0;
  auto alloc = [&](size_t bytes) { char* p = ws + off; off += (bytes + 255) & ~(size_t)255; return p; };
  off = (size_t)(-(long long)(size_t)ws) & (OREG - 1);
  char* obuf = alloc((size_t)NSEG * OREG);
  char* vmem = alloc((size_t)NSEG * OREG);
  char* membuf = alloc((size_t)NSEG * MREG);
  u16* kmem = (u16*)alloc((size_t)NSEG * MEM * HDA * 2);
#define KFILL (NSEG - TSEG0)
  const size_t sent_beg = off - (size_t)KFILL * MEM * HDA * 2;
  const size_t sent_end = off;
  unsigned* par = (unsigned*)alloc(4 * NSEG * 4);
  unsigned long long* prof = (unsigned long long*)alloc(96 * 4 * 72 * 8 + 8 * 256 * 16 + 64 * 384 * 8);
  Ctl* ctl = (Ctl*)alloc(sizeof(Ctl));
  off += (size_t)(-(long long)(size_t)(ws + off)) & (QPAGE - 1);
  u16* qkvA = (u16*)alloc(QSIZE);
  u16* qkvB = (u16*)alloc((size_t)S_ * NQKVB * 2);
  float* part = (float*)alloc((size_t)(NSEG - TSEG0) * 8 * SEG * DM * 4);

  PrepParams pp;
  pp.sent = (v4u*)(ws + sent_beg); pp.sent_vec = (sent_end - sent_beg) / 16; pp.ctl = (v4u*)ctl; pp.mem0 = A_mem0; pp.obuf = obuf; pp.membuf = membuf; pp.vbuf = vmem; pp.par = par; pp.ctl_vec = sizeof(Ctl) / 16;
  pp.wk = (const v4u*)A_Wk; pp.wv = (const v4u*)A_Wv; pp.ax = (const v4u*)A_x; pp.pfr = PFR; pp.far = 1; pp.qkv = (char*)qkvA;
  k_prep<<<PSENT, 512, 0, stream>>>(pp);
  FParams cp;
  cp.Ax = A_x; cp.Bx = B_x; cp.Wq = A_Wq; cp.Wk = A_Wk; cp.Wv = A_Wv; cp.Wo = A_Wo; cp.wqkvB = B_wqkv; cp.woB = B_wo;
  cp.qkvA = qkvA; cp.qkvB = qkvB; cp.kmem = kmem; cp.vbuf = (u16*)vmem; cp.obuf = (u16*)obuf; cp.membuf = (u16*)membuf; cp.par = par; cp.out = out;
  cp.part = part; cp.ctl = ctl; cp.prof = prof;
  cp.dq = DQ; cp.dq2 = DQ2; cp.dmv = DMV; cp.dov = DOV; cp.gain = GAIN; cp.inc = MINC; cp.dec = MDEC; cp.rec = RECOV; cp.stag = STAG; cp.sh1 = SH1; cp.sh2 = SH2; cp.sh3 = SH3; cp.dv = DVV; cp.pad = BOOT; cp.rot = ROT; cp.eps = EPSV; cp.abd = ABD; cp.boot2 = BOOT2; cp.boot3 = BOOT3; cp.im = IMARG; cp.gain0 = GAIN0; cp.oj1 = OJ1;
  k_fused<<<256, 256, 0, stream>>>(cp);
}
```

```cpp
#include <hip/hip_runtime.h>
#include <hip/hip_bf16.h>
#include <stdint.h>
#include <stddef.h>

#define DQ 10
#define DQ2 13
#define DMV 2400
#define DOV 1400
#define GAIN 4
#define GAIN0 8
#define MINC 32
#define MDEC 6
#define STAG 0
#define SH1 14
#define SH2 75
#define SH3 6
#define DVV 0
#define BOOT 0x2850
#define PXV (600 * 65536 + 128)
#define PBV (1500 * 65536)
#define EPSV 0x020003
#define ABD 3
#define BOOT2 0x0505
#define BOOT3 0x0a14
#define IMARG 3
#define PFR 0
#define OJ1 48
#define LK1 34
#define LK4 38
#define SH0 10
#define SH4 24
#define VSLOT 60
typedef unsigned short u16;
typedef __attribute__((ext_vector_type(4))) float f32x4;
typedef unsigned v4u __attribute__((ext_vector_type(4)));

constexpr int S_ = 2048, DM = 1024, HA = 32, DHA = 128, HDA = 4096, MEM = 8, SEG = 32, NSEG = 64;
constexpr int QPAGE = 2 << 20, QBLK = 16384, QSIZE = 48 * QPAGE;
__device__ __forceinline__ int qoff(unsigned long long parq, int n) { return n * QPAGE + (int)((parq >> n) & 1) * 8192; }
constexpr int DB = 1024, HB = 32, DHB = 32, NQKVB = 3 * DB;

struct PrepParams {
  v4u* sent; unsigned long long sent_vec;
  v4u* ctl; const u16* mem0; char* obuf; char* membuf; char* vbuf; unsigned* par; unsigned long long ctl_vec;
  const v4u *wk, *wv, *ax; unsigned long long pfr, far; char* qkv;
};
#define PROBES 3
constexpr int PSENT = 256, OREG = 131072, MREG = 65536;
typedef __attribute__((ext_vector_type(8))) __bf16 bfx8;
typedef __attribute__((ext_vector_type(2))) __bf16 bfx2;
typedef __attribute__((ext_vector_type(2))) float f32x2;
typedef unsigned v2u __attribute__((ext_vector_type(2)));
typedef unsigned v8u __attribute__((ext_vector_type(8)));
typedef unsigned v32u __attribute__((ext_vector_type(32)));
constexpr int NQK = 32, NVO = 64;
constexpr int NLW = 15, VXLD = 144;
struct ChainLds {
  v4u sA[16 * 64];
  f32x4 xs[4][64];
  f32x4 ms[4][64];
  v4u kv[4][4][64];
  v4u sW[4][NLW][2][64];
};
constexpr int seq_i(int u) { return u < 16 ? u >> 2 : 4 + ((u - 16) % 24 >> 1); }
constexpr int seq_t(int u) { return u < 16 ? (u & 3) : (u < 40 ? (u & 1) : 2 + (u & 1)); }
constexpr bool seq_l(int u) { return u < 10 ? (u & 1) : (u == 15 || (u >= 17 && u <= 37 && (u & 3) == 1) || u == 40 || u == 42 || u == 44); }
constexpr int rd_a(int u) { return u >= 11 && u <= 33 && (u & 1) ? 4 + (u - 11) / 2 : 0; }
constexpr int rd_w(int u) { if (u < 8 || u > 39 || rd_a(u)) return -1; int n = 0; for (int v = 8; v < u; ++v) n += !rd_a(v); return n; }
constexpr int seq_c(int u) { if (seq_l(u)) return 2; int n = 0; for (int v = 0; v < u; ++v) n += !seq_l(v); return n < 32 - NLW ? 1 : 0; }
constexpr int seq_n(int u) { int n = 0; for (int v = 0; v < u; ++v) n += seq_c(v) == seq_c(u); return n; }
constexpr int seq_u(int i, int t) { for (int u = 0; u < 64; ++u) if (seq_i(u) == i && seq_t(u) == t) return u; return -1; }
template <int I> struct IC { static constexpr int v = I; };
template <int I, int N, class F> __device__ __forceinline__ void sfor(F&& f) { if constexpr (I < N) { f(IC<I>{}); sfor<I + 1, N>(f); } }
#define SFOR(u, a, b) sfor<a, b>([&](auto u##_) __attribute__((always_inline)) { constexpr int u = decltype(u##_)::v;
#define SEND });
constexpr int NPRE = 5;

__device__ __forceinline__ void lds_barrier() { asm volatile("s_waitcnt lgkmcnt(0)" ::: "memory"); __builtin_amdgcn_s_barrier(); asm volatile("" ::: "memory"); }
template <int N> __device__ __forceinline__ float dpp_ror(float v) {
  return __int_as_float(__builtin_amdgcn_update_dpp(0, __float_as_int(v), 0x120 + N, 0xf, 0xf, false));
}
__device__ __forceinline__ unsigned pk2(float a, float b) { f32x2 t = {a, b}; return __builtin_bit_cast(unsigned, __builtin_convertvector(t, bfx2)); }
__device__ __forceinline__ unsigned nosent(unsigned x) { return x < 0xfffffffeu ? x : 0xfffffffeu; }
__device__ __forceinline__ unsigned umax3(unsigned a, unsigned b, unsigned c) { unsigned m = a > b ? a : b; return m > c ? m : c; }
__device__ __forceinline__ unsigned chk4(unsigned m, v4u v) { return umax3(umax3(m, v.x, v.y), v.z, v.w); }
__device__ __forceinline__ unsigned long long now() { return __builtin_amdgcn_s_memtime(); }
__device__ __forceinline__ f32x4 mf(v4u a, v4u b, f32x4 c) {
  return __builtin_amdgcn_mfma_f32_16x16x32_bf16(__builtin_bit_cast(bfx8, a), __builtin_bit_cast(bfx8, b), c, 0, 0, 0);
}
__device__ __forceinline__ v2u trrd(const u16* p) {
  typedef __attribute__((ext_vector_type(4))) short v4i16_t;
  return __builtin_bit_cast(v2u, __builtin_amdgcn_ds_read_tr16_b64_v4i16((__attribute__((address_space(3))) v4i16_t*)p));
}
__device__ __forceinline__ v8u cat8(v4u a, v4u b) { return __builtin_shufflevector(a, b, 0, 1, 2, 3, 4, 5, 6, 7); }
__device__ __forceinline__ float xmax16(float x) { auto r = __builtin_amdgcn_permlane16_swap(__float_as_uint(x), __float_as_uint(x), false, false); return fmaxf(__uint_as_float(r[0]), __uint_as_float(r[1])); }
__device__ __forceinline__ float xmax32(float x) { auto r = __builtin_amdgcn_permlane32_swap(__float_as_uint(x), __float_as_uint(x), false, false); return fmaxf(__uint_as_float(r[0]), __uint_as_float(r[1])); }
__device__ __forceinline__ float xsum16(float x) { auto r = __builtin_amdgcn_permlane16_swap(__float_as_uint(x), __float_as_uint(x), false, false); return __uint_as_float(r[0]) + __uint_as_float(r[1]); }
__device__ __forceinline__ float xsum32(float x) { auto r = __builtin_amdgcn_permlane32_swap(__float_as_uint(x), __float_as_uint(x), false, false); return __uint_as_float(r[0]) + __uint_as_float(r[1]); }
__device__ __forceinline__ int dbl(int off) { return ((off >> 13) << 14) | (off & 8191); }
__device__ __forceinline__ bool spin_fail(unsigned spins, unsigned* tmo, unsigned code) {
  if ((spins & 31u) != 31u) return false;
  if (spins > (1u << 15)) { __hip_atomic_store(tmo, code, __ATOMIC_RELAXED, __HIP_MEMORY_SCOPE_AGENT); return true; }
  return __builtin_amdgcn_readfirstlane(__hip_atomic_load(tmo, __ATOMIC_RELAXED, __HIP_MEMORY_SCOPE_AGENT)) != 0u;
}
#define SB0 __builtin_amdgcn_sched_barrier(0)
typedef __attribute__((ext_vector_type(16))) float f32x16;
#define WARM32(d) asm volatile("v_mfma_f32_16x16x32_bf16 %0, v[8:11], v[8:11], 0\n\tv_mfma_f32_16x16x32_bf16 %0, v[8:11], v[8:11], 0" : "=v"(d) : "{v[8:15]}"(wb[0])); SB0
#define NTQ 0
#define NTV 0
#define NTO 0
#define NTA 4
#define NTB 4
#define TBAUX 0
#define VROT 1
#define NHX 1
#define WOT 1
#define QROT 2
#define RANKAL 4
#define WDK 0
#define PACEO 32
#define PACE_O { asm volatile("s_nop %0" :: "i"(PACEO / 4 - 2)); SB0; }
#define PACEV 0
#define PRET 32
#define EPS 20
#define XB 56
#define ALP 2
#define KF0 8
#define PF1 1
#define PSL 48
#define TSL 6
#define FSL 4
#define OUTAUX 16
#define LA0 4
#define LAX 12
#define NBL 16
#define NAL 8
#define HROT 16
#define EARLY 3
#define NWQ 4
#define PACEQ 1
#define NWV 0
#define NWO 0
__device__ __forceinline__ f32x4 zero4() { f32x2 a, b; asm volatile("v_mov_b64 %0, 0\n\tv_mov_b64 %1, 0" : "=v"(a), "=v"(b)); return (f32x4){a[0], a[1], b[0], b[1]}; }
__device__ __forceinline__ int opaque_lane() { int l; asm volatile("v_mbcnt_lo_u32_b32 %0, -1, 0\n\tv_mbcnt_hi_u32_b32 %0, -1, %0" : "=v"(l)); return l & 63; }

namespace wg {
constexpr int LDS_USE = 131072;
__device__ __forceinline__ void* uni(const void* p) { const unsigned long long x = (unsigned long long)p; return (void*)(((unsigned long long)(unsigned)__builtin_amdgcn_readfirstlane(x >> 32) << 32) | (unsigned)__builtin_amdgcn_readfirstlane(x)); }
template <int MT>
__device__ __forceinline__ void gemm_tile(const u16* A, int lda, const u16* B, int ldb, int nst  , void* C, int ldc, bool apack, bool cpack, const unsigned* qpar  , char* lds, int w, int eps, int pf = -1  ) {
  constexpr int MI = MT / 32, PA = MT / 64, NP = PA + 4;
  constexpr int A_B = MT * 64, ST = A_B + 16384;
  const int lane = opaque_lane();
  const int l15 = lane & 15, lq = lane >> 4, wm = w >> 1, wn = w & 1;
  lda = __builtin_amdgcn_readfirstlane(lda); ldb = __builtin_amdgcn_readfirstlane(ldb); ldc = __builtin_amdgcn_readfirstlane(ldc); nst = __builtin_amdgcn_readfirstlane(nst);
  f32x4 acc[8][MI];
#pragma unroll
  for (int i = 0; i < 8; ++i)
#pragma unroll
    for (int j = 0; j < MI; ++j) acc[i][j] = (f32x4){0.f, 0.f, 0.f, 0.f};
  const unsigned lb = (unsigned)(size_t)lds;
  auto fsw = [](int x) { return ((x ^ (x >> 1)) & 1) << 1 | (x >> 1); };
  unsigned ao[2], bo[2][8];
  ao[0] = lb + wm * (MT / 2) * 64 + (apack ? lane * 16 : l15 * 64 + ((lq ^ fsw(l15 >> 2)) << 4)); ao[1] = ao[0] + 2 * ST;
  { const int hh = (l15 >> 2) | ((lq & 1) << 2);
#pragma unroll
    for (int i = 0; i < 8; ++i) { bo[0][i] = lb + A_B + (8 * lq + (l15 >> 2)) * 512 + ((16 * wn + ((2 * i) ^ (2 * hh)) + ((l15 >> 1) & 1)) << 4) + (l15 & 1) * 8; bo[1][i] = bo[0][i] + 2 * ST; } }
  __amdgpu_buffer_rsrc_t ra = __builtin_amdgcn_make_buffer_rsrc(uni(A), 0, 0x7fffffff, 0x00020000), rb = __builtin_amdgcn_make_buffer_rsrc(uni(B), 0, 0x7fffffff, 0x00020000);
  const int va = (16 * PA * w * lda + (apack ? lane * 8 : (lane >> 2) * lda + (((lane & 3) ^ fsw(lane >> 4)) << 3))) * 2, ska = __builtin_amdgcn_readfirstlane(apack ? 1024 : 64);
  const int vb0 = ((8 * w + (lane >> 5)) * ldb + (((lane & 31) ^ ((lane >> 5) << 1) ^ ((w & 1) << 3)) << 3)) * 2, vb1 = vb0 ^ 64;
  const int sa = 32 * lda, sb = 4 * ldb, skb = 64 * ldb;
  const unsigned ma = lb + PA * w * 1024, mb = lb + A_B + w * 4096;
#define PIECE1(NT, base, imm, vo, rs, so) do { if constexpr (NT) asm volatile("s_add_u32 m0, %0, %1\n\ts_nop 0\n\tbuffer_load_dwordx4 %2, %3, %4 offen nt lds" :: "s"(base), "i"(imm), "v"(vo), "s"(rs), "s"(so) : "memory", "scc"); \
    else asm volatile("s_add_u32 m0, %0, %1\n\ts_nop 0\n\tbuffer_load_dwordx4 %2, %3, %4 offen lds" :: "s"(base), "i"(imm), "v"(vo), "s"(rs), "s"(so) : "memory", "scc"); } while (0)
#define PIECE(q, st) do { \
    if ((q) < PA) PIECE1((NTA & (MT / 64)) != 0, ma, (st) * ST + (q) * 1024, va, ra, ka + (q) * sa); \
    else PIECE1((NTB & (MT / 64)) != 0, mb, (st) * ST + ((q) - PA) * 1024, (((q) - PA) & 1) ? vb1 : vb0, rb, kb + ((q) - PA) * sb); } while (0)
#define RDA(f, j, st) asm volatile("ds_read_b128 %0, %1 offset:%2" : "=v"(fa[f][j]) : "v"(ao[(st) >> 1]), "i"(((st) & 1) * ST + (j) * 1024) : "memory")
#define RDB(f, i, h, st) asm volatile("ds_read_b64_tr_b16 %0, %1 offset:%2" : "=v"(fb[f][i][h]) : "v"(bo[(st) >> 1][i]), "i"(((st) & 1) * ST + (h) * 2048) : "memory")
#define TIE(f) do { _Pragma("unroll") for (int j = 0; j < MI; ++j) asm volatile("" : "+v"(fa[f][j])); \
    _Pragma("unroll") for (int i = 0; i < 8; ++i) asm volatile("" : "+v"(fb[f][i][0]), "+v"(fb[f][i][1])); } while (0)
  v4u fa[2][MI]; v2u fb[2][8][2];
  const unsigned qw = *qpar;
  const unsigned long long parq = apack ? __ballot(qpar[lane] != 0u) : 0ull;
  int ka = qoff(parq, 0), kb = 0;
  constexpr int AH = 12;
  const int pv = w < 2 ? ((64 * (pf >> 3) + lane) * lda + 64 * w) * 2 : ((16 * (pf & 7) + (lane >> 2)) * ldb + 64 * (lane & 3)) * 2;
  const __amdgpu_buffer_rsrc_t rp = __builtin_amdgcn_make_buffer_rsrc(uni(w < 2 ? A : B), 0, 0x7fffffff, 0x00020000);
  const int ps = __builtin_amdgcn_readfirstlane(w < 2 ? 64 : skb);
#define TOUCH(k) do { if (MT == 256 && pf >= 0 && w < 3 && (k) < nst) __builtin_amdgcn_raw_ptr_buffer_load_lds(rp, (__attribute__((address_space(3))) void*)(lds + LDS_USE + 1024), 4, pv, (k) * ps, 0, 2), __builtin_amdgcn_raw_ptr_buffer_load_lds(rp, (__attribute__((address_space(3))) void*)(lds + LDS_USE + 1024), 4, pv + 64, (k) * ps, 0, 0); } while (0)
  if (MT == 256 && pf >= 0) { TOUCH(0); TOUCH(4); TOUCH(8); asm volatile("s_waitcnt vmcnt(0)" ::: "memory"); __builtin_amdgcn_s_sleep(8); }
  asm volatile("s_nop 4" :: "s"(ra), "s"(rb), "s"(sa), "s"(sb), "s"(skb), "s"(ska));
#pragma unroll
  for (int st = 0; st < 4; ++st) {
#pragma unroll
    for (int q = 0; q < NP; ++q) PIECE(q, st);
    ka += ska; kb += skb;
  }
  asm volatile("s_waitcnt vmcnt(%0)\n\ts_barrier" :: "i"(3 * NP) : "memory");
#pragma unroll
  for (int i = 0; i < 8; ++i) { RDB(0, i, 0, 0); RDB(0, i, 1, 0); }
#pragma unroll
  for (int j = 0; j < MI; ++j) RDA(0, j, 0);
  int k4 = 0;
  do {
    if (apack && (k4 & 4)) ka = qoff(parq, (k4 + 4) >> 3);
    TOUCH(k4 + AH);
    if (k4 + 4 >= nst) { ra = __builtin_amdgcn_make_buffer_rsrc(uni(C), 0, 0, 0x00020000); rb = ra; }
#pragma unroll
    for (int st = 0; st < 4; ++st) {
      constexpr int NM = 8 * MI, NR = 16 + MI;
      const int f = st & 1, sn = (st + 1) & 3;
      asm volatile("s_waitcnt vmcnt(%0) lgkmcnt(0)\n\ts_barrier" :: "i"(2 * NP) : "memory");
      TIE(f);
      SB0;
#pragma unroll
      for (int u = 0; u < NM; ++u) {
        const int j = u >> 3, i = u & 7;
        asm volatile("v_mfma_f32_16x16x32_bf16 %0, %1, %2, %0" : "+a"(acc[i][j]) : "v"((v4u){fb[f][i][0].x, fb[f][i][0].y, fb[f][i][1].x, fb[f][i][1].y}), "v"(fa[f][j]));
        SB0;
        constexpr int RS = MT == 256 ? 2 : 1;
        if (u % RS == 0 && u / RS < NR) {
          const int r = u / RS;
          if (r < 16) RDB(f ^ 1, r >> 1, r & 1, sn); else RDA(f ^ 1, r - 16, sn);
          if (MT == 64 && (u == 0 || u == 2)) RDA(f ^ 1, u >> 1, sn);
          SB0;
        }
        constexpr int PS = NM / 8;
        if (u % PS == PS - 1 && u / PS < NP) { PIECE(u / PS, st); SB0; }
      }
      ka += ska; kb += skb;
    }
    k4 += 4;
  } while (k4 < nst);
#undef PIECE
#undef PIECE1
#undef RDA
#undef RDB
  asm volatile("s_nop 7\n\ts_waitcnt lgkmcnt(0)" ::: "memory");
  SB0;
  TIE(0);
#undef TIE
  char* st = lds + LDS_USE + 2048 + w * 4096;
  const __amdgpu_buffer_rsrc_t rs = __builtin_amdgcn_make_buffer_rsrc((char*)uni(C) + __builtin_amdgcn_readfirstlane(cpack && qw ? 8192 : 0), 0, MT * ldc * 2 - (cpack ? 8192 : 0), 0x00020000);
  const int so = wm * (MT / 2) * ldc * 2 + (cpack ? wn * 4096 + lane * 16 : (lq * ldc + wn * 128 + l15 * 8) * 2), sq = cpack ? 1024 : 8 * ldc;
#pragma unroll
  for (int j = 0; j < MI; ++j) {
    v2u p[8]; v4u v[4];
#pragma unroll
    for (int i = 0; i < 8; ++i) { asm volatile("" : "+a"(acc[i][j])); p[i] = (v2u){pk2(acc[i][j][0], acc[i][j][1]), pk2(acc[i][j][2], acc[i][j][3])}; }
#pragma unroll
    for (int q = 0; q < 4; ++q) { v[q] = (v4u){p[2 * q].x, p[2 * q].y, p[2 * q + 1].x, p[2 * q + 1].y}; asm volatile("" : "+v"(v[q])); }
    if (!cpack) {
#pragma unroll
      for (int i = 0; i < 8; ++i) *(v2u*)(st + l15 * 256 + (((2 * i + (lq >> 1)) ^ l15) << 4) + (lq & 1) * 8) = (i & 1) ? (v2u){v[i >> 1].z, v[i >> 1].w} : (v2u){v[i >> 1].x, v[i >> 1].y};
      asm volatile("" ::: "memory");
#pragma unroll
      for (int q = 0; q < 4; ++q) v[q] = *(const v4u*)(st + (4 * q + lq) * 256 + ((l15 ^ (4 * q + lq)) << 4));
      asm volatile("" ::: "memory");
    }
#pragma unroll
    for (int q = 0; q < 4; ++q) {
      if (MT == 128 && OUTAUX != 16 && ldc == 2 * DM) __builtin_amdgcn_raw_buffer_store_b128(v[q], rs, so, 32 * j * ldc + q * sq, OUTAUX);
      else __builtin_amdgcn_raw_buffer_store_b128(v[q], rs, so, 32 * j * ldc + q * sq, 16);
    }
    for (int z = 0; z < eps; ++z) __builtin_amdgcn_s_sleep(4);
    SB0;
  }
  asm volatile("s_waitcnt vmcnt(0)" ::: "memory");
  __builtin_amdgcn_s_barrier();
}
}

struct Ctl {
  unsigned qhead[32];
  unsigned prg[128][32];
  unsigned cntB[16][32];
  unsigned cntAB[16][32];
  unsigned cntO[72][32];
  unsigned prog[32];
  unsigned tmo[32];
};
struct FParams {
  const u16 *Ax, *Bx, *Wq, *Wk, *Wv, *Wo, *wqkvB, *woB;
  u16 *qkvA, *qkvB, *kmem, *vbuf, *obuf, *membuf, *out;
  float* part; Ctl* ctl; unsigned long long* prof; const unsigned* par;
  int dq, dq2, dmv, dov, gain, inc, dec, pb, stag, sh1, sh2, sh3, dv, pad, px, eps, abd, boot2, boot3, im, gain0, oj1, lk1, lk4, sh0, sh4;
};
#define RLX_AGENT __ATOMIC_RELAXED, __HIP_MEMORY_SCOPE_AGENT
__device__ __forceinline__ unsigned ld_agent(const unsigned* p) { return __hip_atomic_load(p, RLX_AGENT); }
__device__ __forceinline__ void wg_wait(const unsigned* p, unsigned need, Ctl* ctl, int w) {
  if (w == 0) {
    for (unsigned spins = 0;; ++spins) {
      const bool ok = (p == nullptr) || ld_agent(p) >= need;
      if (__all(ok)) break;
      if (spin_fail(spins, &ctl->tmo[0], 2u)) break;
      __builtin_amdgcn_s_sleep(16);
    }
    __builtin_amdgcn_fence(__ATOMIC_ACQUIRE, "agent");
    asm volatile("s_waitcnt vmcnt(0)" ::: "memory");
  }
  __syncthreads();
}

#define PN_BODY \
          "s_mov_b64 s[40:41], %[xa]\n\ts_mov_b32 s42, 0x400000\n\ts_mov_b32 s43, 0x20000\n\ts_nop 4\n\tbuffer_load_dword v43, %[vp], s[40:43], %[po] offen nt\n\tbuffer_load_dword v43, %[vp], s[40:43], %[po] offen offset:64\n\t" \
          "s_add_u32 %[po], %[po], 0x10000\n\tbuffer_load_dword v43, %[vp], s[40:43], %[po] offen nt\n\tbuffer_load_dword v43, %[vp], s[40:43], %[po] offen offset:64\n\ts_add_u32 %[po], %[po], 0x10000\n\tbuffer_load_dword v43, %[vp], s[40:43], %[po] offen nt\n\tbuffer_load_dword v43, %[vp], s[40:43], %[po] offen offset:64\n\t" \
          "s_add_u32 %[po], %[po], 0x10000\n\tbuffer_load_dword v43, %[vp], s[40:43], %[po] offen nt\n\tbuffer_load_dword v43, %[vp], s[40:43], %[po] offen offset:64\n\ts_add_u32 %[po], %[po], 0x10000\n\tbuffer_load_dword v43, %[vp], s[40:43], %[po] offen nt\n\tbuffer_load_dword v43, %[vp], s[40:43], %[po] offen offset:64\n\t" \
          "s_add_u32 %[po], %[po], 0x10000\n\ts_add_u32 m0, %[wl], 65536\n\ts_add_u32 %[t], %[wo], 0x0\n\tbuffer_load_dwordx4 %[vw0], %[rw], %[t] offen" PN_NT " lds\n\ts_add_u32 m0, %[wl], 66560\n\ts_add_u32 %[t], %[wo], 0x8000\n\t" \
          "buffer_load_dwordx4 %[vw0], %[rw], %[t] offen" PN_NT " lds\n\ts_add_u32 m0, %[wl], 67584\n\ts_add_u32 %[t], %[wo], 0x10000\n\tbuffer_load_dwordx4 %[vw1], %[rw], %[t] offen" PN_NT " lds\n\ts_add_u32 m0, %[wl], 68608\n\ts_add_u32 %[t], %[wo], 0x18000\n\t" \
          "buffer_load_dwordx4 %[vw1], %[rw], %[t] offen" PN_NT " lds\n\ts_add_u32 m0, %[wl], 81920\n\ts_add_u32 %[t], %[wo], 0x80000\n\tbuffer_load_dwordx4 %[vw0], %[rw], %[t] offen" PN_NT " lds\n\ts_add_u32 m0, %[wl], 82944\n\ts_add_u32 %[t], %[wo], 0x88000\n\t" \
          "buffer_load_dwordx4 %[vw0], %[rw], %[t] offen" PN_NT " lds\n\ts_add_u32 m0, %[wl], 83968\n\ts_add_u32 %[t], %[wo], 0x90000\n\tbuffer_load_dwordx4 %[vw1], %[rw], %[t] offen" PN_NT " lds\n\ts_add_u32 m0, %[wl], 84992\n\ts_add_u32 %[t], %[wo], 0x98000\n\t" \
          "buffer_load_dwordx4 %[vw1], %[rw], %[t] offen" PN_NT " lds\n\ts_add_u32 m0, %[sl], 0\n\ts_nop 0\n\tbuffer_load_dwordx4 %[vd0], s[40:43], %[xo] offen lds\n\tbuffer_load_dwordx4 %[vd0], s[40:43], %[xo] offen offset:1024 lds\n\tbuffer_load_dwordx4 %[vd1], s[40:43], %[xo] offen offset:2048 lds\n\t" \
          "buffer_load_dwordx4 %[vd1], s[40:43], %[xo] offen offset:3072 lds\n\ts_add_u32 m0, %[sl], 4096\n\ts_nop 0\n\tbuffer_load_dwordx4 %[vd2], s[40:43], %[xo] offen lds\n\tbuffer_load_dwordx4 %[vd2], s[40:43], %[xo] offen offset:1024 lds\n\tbuffer_load_dwordx4 %[vd3], s[40:43], %[xo] offen offset:2048 lds\n\t" \
          "buffer_load_dwordx4 %[vd3], s[40:43], %[xo] offen offset:3072 lds\n\ts_add_u32 %[xo], %[xo], 0x8000\n\ts_add_u32 %[sl], %[sl], 0x8000\n\ts_bitset0_b32 %[sl], 17\n\ts_add_u32 m0, %[sl], 0\n\ts_nop 0\n\t" \
          "buffer_load_dwordx4 %[vd0], s[40:43], %[xo] offen lds\n\tbuffer_load_dwordx4 %[vd0], s[40:43], %[xo] offen offset:1024 lds\n\tbuffer_load_dwordx4 %[vd1], s[40:43], %[xo] offen offset:2048 lds\n\tbuffer_load_dwordx4 %[vd1], s[40:43], %[xo] offen offset:3072 lds\n\ts_add_u32 m0, %[sl], 4096\n\ts_nop 0\n\t" \
          "buffer_load_dwordx4 %[vd2], s[40:43], %[xo] offen lds\n\tbuffer_load_dwordx4 %[vd2], s[40:43], %[xo] offen offset:1024 lds\n\tbuffer_load_dwordx4 %[vd3], s[40:43], %[xo] offen offset:2048 lds\n\tbuffer_load_dwordx4 %[vd3], s[40:43], %[xo] offen offset:3072 lds\n\ts_add_u32 %[xo], %[xo], 0x8000\n\ts_add_u32 %[sl], %[sl], 0x8000\n\t" \
          "s_bitset0_b32 %[sl], 17\n\ts_add_u32 m0, %[wl], 98304\n\ts_add_u32 %[t], %[wo], 0x100000\n\tbuffer_load_dwordx4 %[vw0], %[rw], %[t] offen" PN_NT " lds\n\ts_add_u32 m0, %[wl], 99328\n\ts_add_u32 %[t], %[wo], 0x108000\n\t" \
          "buffer_load_dwordx4 %[vw0], %[rw], %[t] offen" PN_NT " lds\n\ts_add_u32 m0, %[wl], 100352\n\ts_add_u32 %[t], %[wo], 0x110000\n\tbuffer_load_dwordx4 %[vw1], %[rw], %[t] offen" PN_NT " lds\n\ts_add_u32 m0, %[wl], 101376\n\ts_add_u32 %[t], %[wo], 0x118000\n\t" \
          "buffer_load_dwordx4 %[vw1], %[rw], %[t] offen" PN_NT " lds\n\ts_add_u32 m0, %[wl], 114688\n\ts_add_u32 %[t], %[wo], 0x180000\n\tbuffer_load_dwordx4 %[vw0], %[rw], %[t] offen" PN_NT " lds\n\ts_add_u32 m0, %[wl], 115712\n\ts_add_u32 %[t], %[wo], 0x188000\n\t" \
          "buffer_load_dwordx4 %[vw0], %[rw], %[t] offen" PN_NT " lds\n\ts_add_u32 m0, %[wl], 116736\n\ts_add_u32 %[t], %[wo], 0x190000\n\tbuffer_load_dwordx4 %[vw1], %[rw], %[t] offen" PN_NT " lds\n\ts_add_u32 m0, %[wl], 117760\n\ts_add_u32 %[t], %[wo], 0x198000\n\t" \
          "buffer_load_dwordx4 %[vw1], %[rw], %[t] offen" PN_NT " lds\n\ts_add_u32 m0, %[wl], 131072\n\ts_add_u32 %[t], %[wo], 0x200000\n\tbuffer_load_dwordx4 %[vw0], %[rw], %[t] offen" PN_NT " lds\n\ts_add_u32 m0, %[wl], 132096\n\ts_add_u32 %[t], %[wo], 0x208000\n\t" \
          "buffer_load_dwordx4 %[vw0], %[rw], %[t] offen" PN_NT " lds\n\ts_add_u32 m0, %[wl], 133120\n\ts_add_u32 %[t], %[wo], 0x210000\n\tbuffer_load_dwordx4 %[vw1], %[rw], %[t] offen" PN_NT " lds\n\ts_add_u32 m0, %[wl], 134144\n\ts_add_u32 %[t], %[wo], 0x218000\n\t" \
          "buffer_load_dwordx4 %[vw1], %[rw], %[t] offen" PN_NT " lds\n\ts_waitcnt vmcnt(32) lgkmcnt(0)\n\ts_barrier\n\ts_add_u32 m0, %[wl], 147456\n\ts_add_u32 %[t], %[wo], 0x280000\n\tbuffer_load_dwordx4 %[vw0], %[rw], %[t] offen" PN_NT " lds\n\t" \
          "s_add_u32 m0, %[wl], 148480\n\ts_add_u32 %[t], %[wo], 0x288000\n\tbuffer_load_dwordx4 %[vw0], %[rw], %[t] offen" PN_NT " lds\n\ts_add_u32 m0, %[wl], 149504\n\ts_add_u32 %[t], %[wo], 0x290000\n\tbuffer_load_dwordx4 %[vw1], %[rw], %[t] offen" PN_NT " lds\n\t" \
          "s_add_u32 m0, %[wl], 150528\n\ts_add_u32 %[t], %[wo], 0x298000\n\tbuffer_load_dwordx4 %[vw1], %[rw], %[t] offen" PN_NT " lds\n\tds_read_b64_tr_b16 a[0:1], %[rt0] offset:0\n\tds_read_b64_tr_b16 a[2:3], %[rt0] offset:1024\n\tds_read_b64_tr_b16 a[4:5], %[rt1] offset:0\n\t" \
          "ds_read_b64_tr_b16 a[6:7], %[rt1] offset:1024\n\tds_read_b64_tr_b16 a[8:9], %[rt0] offset:8192\n\tds_read_b64_tr_b16 a[10:11], %[rt0] offset:9216\n\tds_read_b64_tr_b16 a[12:13], %[rt1] offset:8192\n\tds_read_b64_tr_b16 a[14:15], %[rt1] offset:9216\n\ts_waitcnt vmcnt(32) lgkmcnt(0)\n\t" \
          "s_barrier\n\ts_add_u32 m0, %[wl], 65536\n\ts_add_u32 %[t], %[wo], 0x300000\n\tbuffer_load_dwordx4 %[vw0], %[rw], %[t] offen" PN_NT " lds\n\ts_add_u32 m0, %[wl], 66560\n\ts_add_u32 %[t], %[wo], 0x308000\n\t" \
          "buffer_load_dwordx4 %[vw0], %[rw], %[t] offen" PN_NT " lds\n\ts_add_u32 m0, %[wl], 67584\n\ts_add_u32 %[t], %[wo], 0x310000\n\tbuffer_load_dwordx4 %[vw1], %[rw], %[t] offen" PN_NT " lds\n\ts_add_u32 m0, %[wl], 68608\n\ts_add_u32 %[t], %[wo], 0x318000\n\t" \
          "buffer_load_dwordx4 %[vw1], %[rw], %[t] offen" PN_NT " lds\n\tds_read_b64_tr_b16 a[16:17], %[rt0] offset:16384\n\tds_read_b64_tr_b16 a[18:19], %[rt0] offset:17408\n\tds_read_b64_tr_b16 a[20:21], %[rt1] offset:16384\n\tds_read_b64_tr_b16 a[22:23], %[rt1] offset:17408\n\tds_read_b64_tr_b16 a[24:25], %[rt0] offset:24576\n\t" \
          "ds_read_b64_tr_b16 a[26:27], %[rt0] offset:25600\n\tds_read_b64_tr_b16 a[28:29], %[rt1] offset:24576\n\tds_read_b64_tr_b16 a[30:31], %[rt1] offset:25600\n\ts_waitcnt vmcnt(16) lgkmcnt(0)\n\ts_barrier\n\ts_add_u32 m0, %[wl], 81920\n\t" \
          "s_add_u32 %[t], %[wo], 0x380000\n\tbuffer_load_dwordx4 %[vw0], %[rw], %[t] offen" PN_NT " lds\n\ts_add_u32 m0, %[wl], 82944\n\ts_add_u32 %[t], %[wo], 0x388000\n\tbuffer_load_dwordx4 %[vw0], %[rw], %[t] offen" PN_NT " lds\n\ts_add_u32 m0, %[wl], 83968\n\t" \
          "s_add_u32 %[t], %[wo], 0x390000\n\tbuffer_load_dwordx4 %[vw1], %[rw], %[t] offen" PN_NT " lds\n\ts_add_u32 m0, %[wl], 84992\n\ts_add_u32 %[t], %[wo], 0x398000\n\tbuffer_load_dwordx4 %[vw1], %[rw], %[t] offen" PN_NT " lds\n\tds_read_b64_tr_b16 a[32:33], %[rt0] offset:32768\n\t" \
          "ds_read_b64_tr_b16 a[34:35], %[rt0] offset:33792\n\tds_read_b64_tr_b16 a[36:37], %[rt1] offset:32768\n\tds_read_b64_tr_b16 a[38:39], %[rt1] offset:33792\n\tds_read_b64_tr_b16 a[40:41], %[rt0] offset:40960\n\tds_read_b64_tr_b16 a[42:43], %[rt0] offset:41984\n\tds_read_b64_tr_b16 a[44:45], %[rt1] offset:40960\n\t" \
          "ds_read_b64_tr_b16 a[46:47], %[rt1] offset:41984\n\ts_waitcnt vmcnt(16) lgkmcnt(0)\n\ts_barrier\n\ts_add_u32 m0, %[wl], 98304\n\ts_add_u32 %[t], %[wo], 0x400000\n\tbuffer_load_dwordx4 %[vw0], %[rw], %[t] offen" PN_NT " lds\n\t" \
          "s_add_u32 m0, %[wl], 99328\n\ts_add_u32 %[t], %[wo], 0x408000\n\tbuffer_load_dwordx4 %[vw0], %[rw], %[t] offen" PN_NT " lds\n\ts_add_u32 m0, %[wl], 100352\n\ts_add_u32 %[t], %[wo], 0x410000\n\tbuffer_load_dwordx4 %[vw1], %[rw], %[t] offen" PN_NT " lds\n\t" \
          "s_add_u32 m0, %[wl], 101376\n\ts_add_u32 %[t], %[wo], 0x418000\n\tbuffer_load_dwordx4 %[vw1], %[rw], %[t] offen" PN_NT " lds\n\tds_read_b64_tr_b16 a[48:49], %[rt2] offset:0\n\tds_read_b64_tr_b16 a[50:51], %[rt2] offset:1024\n\tds_read_b64_tr_b16 a[52:53], %[rt3] offset:0\n\t" \
          "ds_read_b64_tr_b16 a[54:55], %[rt3] offset:1024\n\tds_read_b64_tr_b16 a[56:57], %[rt2] offset:8192\n\tds_read_b64_tr_b16 a[58:59], %[rt2] offset:9216\n\tds_read_b64_tr_b16 a[60:61], %[rt3] offset:8192\n\tds_read_b64_tr_b16 a[62:63], %[rt3] offset:9216\n\ts_waitcnt vmcnt(16) lgkmcnt(0)\n\t" \
          "s_barrier\n\ts_add_u32 m0, %[wl], 114688\n\ts_add_u32 %[t], %[wo], 0x480000\n\tbuffer_load_dwordx4 %[vw0], %[rw], %[t] offen" PN_NT " lds\n\ts_add_u32 m0, %[wl], 115712\n\ts_add_u32 %[t], %[wo], 0x488000\n\t" \
          "buffer_load_dwordx4 %[vw0], %[rw], %[t] offen" PN_NT " lds\n\ts_add_u32 m0, %[wl], 116736\n\ts_add_u32 %[t], %[wo], 0x490000\n\tbuffer_load_dwordx4 %[vw1], %[rw], %[t] offen" PN_NT " lds\n\ts_add_u32 m0, %[wl], 117760\n\ts_add_u32 %[t], %[wo], 0x498000\n\t" \
          "buffer_load_dwordx4 %[vw1], %[rw], %[t] offen" PN_NT " lds\n\tds_read_b64_tr_b16 a[64:65], %[rt2] offset:16384\n\tds_read_b64_tr_b16 a[66:67], %[rt2] offset:17408\n\tds_read_b64_tr_b16 a[68:69], %[rt3] offset:16384\n\tds_read_b64_tr_b16 a[70:71], %[rt3] offset:17408\n\tds_read_b64_tr_b16 a[72:73], %[rt2] offset:24576\n\t" \
          "ds_read_b64_tr_b16 a[74:75], %[rt2] offset:25600\n\tds_read_b64_tr_b16 a[76:77], %[rt3] offset:24576\n\tds_read_b64_tr_b16 a[78:79], %[rt3] offset:25600\n\ts_waitcnt vmcnt(16) lgkmcnt(0)\n\ts_barrier\n\ts_add_u32 m0, %[wl], 131072\n\t" \
          "s_add_u32 %[t], %[wo], 0x500000\n\tbuffer_load_dwordx4 %[vw0], %[rw], %[t] offen" PN_NT " lds\n\ts_add_u32 m0, %[wl], 132096\n\ts_add_u32 %[t], %[wo], 0x508000\n\tbuffer_load_dwordx4 %[vw0], %[rw], %[t] offen" PN_NT " lds\n\ts_add_u32 m0, %[wl], 133120\n\t" \
          "s_add_u32 %[t], %[wo], 0x510000\n\tbuffer_load_dwordx4 %[vw1], %[rw], %[t] offen" PN_NT " lds\n\ts_add_u32 m0, %[wl], 134144\n\ts_add_u32 %[t], %[wo], 0x518000\n\tbuffer_load_dwordx4 %[vw1], %[rw], %[t] offen" PN_NT " lds\n\tds_read_b64_tr_b16 a[80:81], %[rt2] offset:32768\n\t" \
          "ds_read_b64_tr_b16 a[82:83], %[rt2] offset:33792\n\tds_read_b64_tr_b16 a[84:85], %[rt3] offset:32768\n\tds_read_b64_tr_b16 a[86:87], %[rt3] offset:33792\n\tds_read_b64_tr_b16 a[88:89], %[rt2] offset:40960\n\tds_read_b64_tr_b16 a[90:91], %[rt2] offset:41984\n\tds_read_b64_tr_b16 a[92:93], %[rt3] offset:40960\n\t" \
          "ds_read_b64_tr_b16 a[94:95], %[rt3] offset:41984\n\ts_waitcnt vmcnt(16) lgkmcnt(0)\n\ts_barrier\n\ts_add_u32 m0, %[wl], 147456\n\ts_add_u32 %[t], %[wo], 0x580000\n\tbuffer_load_dwordx4 %[vw0], %[rw], %[t] offen" PN_NT " lds\n\t" \
          "s_add_u32 m0, %[wl], 148480\n\ts_add_u32 %[t], %[wo], 0x588000\n\tbuffer_load_dwordx4 %[vw0], %[rw], %[t] offen" PN_NT " lds\n\ts_add_u32 m0, %[wl], 149504\n\ts_add_u32 %[t], %[wo], 0x590000\n\tbuffer_load_dwordx4 %[vw1], %[rw], %[t] offen" PN_NT " lds\n\t" \
          "s_add_u32 m0, %[wl], 150528\n\ts_add_u32 %[t], %[wo], 0x598000\n\tbuffer_load_dwordx4 %[vw1], %[rw], %[t] offen" PN_NT " lds\n\tds_read_b64_tr_b16 a[96:97], %[rt0] offset:0\n\tds_read_b64_tr_b16 a[98:99], %[rt0] offset:1024\n\tds_read_b64_tr_b16 a[100:101], %[rt1] offset:0\n\t" \
          "ds_read_b64_tr_b16 a[102:103], %[rt1] offset:1024\n\tds_read_b64_tr_b16 a[104:105], %[rt0] offset:8192\n\tds_read_b64_tr_b16 a[106:107], %[rt0] offset:9216\n\tds_read_b64_tr_b16 a[108:109], %[rt1] offset:8192\n\tds_read_b64_tr_b16 a[110:111], %[rt1] offset:9216\n\ts_waitcnt vmcnt(16) lgkmcnt(0)\n\t" \
          "s_barrier\n\ts_add_u32 m0, %[wl], 65536\n\ts_add_u32 %[t], %[wo], 0x600000\n\tbuffer_load_dwordx4 %[vw0], %[rw], %[t] offen" PN_NT " lds\n\ts_add_u32 m0, %[wl], 66560\n\ts_add_u32 %[t], %[wo], 0x608000\n\t" \
          "buffer_load_dwordx4 %[vw0], %[rw], %[t] offen" PN_NT " lds\n\ts_add_u32 m0, %[wl], 67584\n\ts_add_u32 %[t], %[wo], 0x610000\n\tbuffer_load_dwordx4 %[vw1], %[rw], %[t] offen" PN_NT " lds\n\ts_add_u32 m0, %[wl], 68608\n\ts_add_u32 %[t], %[wo], 0x618000\n\t" \
          "buffer_load_dwordx4 %[vw1], %[rw], %[t] offen" PN_NT " lds\n\tds_read_b64_tr_b16 a[112:113], %[rt0] offset:16384\n\tds_read_b64_tr_b16 a[114:115], %[rt0] offset:17408\n\tds_read_b64_tr_b16 a[116:117], %[rt1] offset:16384\n\tds_read_b64_tr_b16 a[118:119], %[rt1] offset:17408\n\tds_read_b64_tr_b16 a[120:121], %[rt0] offset:24576\n\t" \
          "ds_read_b64_tr_b16 a[122:123], %[rt0] offset:25600\n\tds_read_b64_tr_b16 a[124:125], %[rt1] offset:24576\n\tds_read_b64_tr_b16 a[126:127], %[rt1] offset:25600\n\ts_waitcnt vmcnt(16) lgkmcnt(0)\n\ts_barrier\n\ts_add_u32 m0, %[wl], 81920\n\t" \
          "s_add_u32 %[t], %[wo], 0x680000\n\tbuffer_load_dwordx4 %[vw0], %[rw], %[t] offen" PN_NT " lds\n\ts_add_u32 m0, %[wl], 82944\n\ts_add_u32 %[t], %[wo], 0x688000\n\tbuffer_load_dwordx4 %[vw0], %[rw], %[t] offen" PN_NT " lds\n\ts_add_u32 m0, %[wl], 83968\n\t" \
          "s_add_u32 %[t], %[wo], 0x690000\n\tbuffer_load_dwordx4 %[vw1], %[rw], %[t] offen" PN_NT " lds\n\ts_add_u32 m0, %[wl], 84992\n\ts_add_u32 %[t], %[wo], 0x698000\n\tbuffer_load_dwordx4 %[vw1], %[rw], %[t] offen" PN_NT " lds\n\tds_read_b64_tr_b16 a[128:129], %[rt0] offset:32768\n\t" \
          "ds_read_b64_tr_b16 a[130:131], %[rt0] offset:33792\n\tds_read_b64_tr_b16 a[132:133], %[rt1] offset:32768\n\tds_read_b64_tr_b16 a[134:135], %[rt1] offset:33792\n\tds_read_b64_tr_b16 a[136:137], %[rt0] offset:40960\n\tds_read_b64_tr_b16 a[138:139], %[rt0] offset:41984\n\tds_read_b64_tr_b16 a[140:141], %[rt1] offset:40960\n\t" \
          "ds_read_b64_tr_b16 a[142:143], %[rt1] offset:41984\n\ts_waitcnt vmcnt(16) lgkmcnt(0)\n\ts_barrier\n\ts_add_u32 m0, %[wl], 98304\n\ts_add_u32 %[t], %[wo], 0x700000\n\tbuffer_load_dwordx4 %[vw0], %[rw], %[t] offen" PN_NT " lds\n\t" \
          "s_add_u32 m0, %[wl], 99328\n\ts_add_u32 %[t], %[wo], 0x708000\n\tbuffer_load_dwordx4 %[vw0], %[rw], %[t] offen" PN_NT " lds\n\ts_add_u32 m0, %[wl], 100352\n\ts_add_u32 %[t], %[wo], 0x710000\n\tbuffer_load_dwordx4 %[vw1], %[rw], %[t] offen" PN_NT " lds\n\t" \
          "s_add_u32 m0, %[wl], 101376\n\ts_add_u32 %[t], %[wo], 0x718000\n\tbuffer_load_dwordx4 %[vw1], %[rw], %[t] offen" PN_NT " lds\n\tds_read_b64_tr_b16 a[144:145], %[rt2] offset:0\n\tds_read_b64_tr_b16 a[146:147], %[rt2] offset:1024\n\tds_read_b64_tr_b16 a[148:149], %[rt3] offset:0\n\t" \
          "ds_read_b64_tr_b16 a[150:151], %[rt3] offset:1024\n\tds_read_b64_tr_b16 a[152:153], %[rt2] offset:8192\n\tds_read_b64_tr_b16 a[154:155], %[rt2] offset:9216\n\tds_read_b64_tr_b16 a[156:157], %[rt3] offset:8192\n\tds_read_b64_tr_b16 a[158:159], %[rt3] offset:9216\n\ts_waitcnt vmcnt(16) lgkmcnt(0)\n\t" \
          "s_barrier\n\ts_add_u32 m0, %[wl], 114688\n\ts_add_u32 %[t], %[wo], 0x780000\n\tbuffer_load_dwordx4 %[vw0], %[rw], %[t] offen" PN_NT " lds\n\ts_add_u32 m0, %[wl], 115712\n\ts_add_u32 %[t], %[wo], 0x788000\n\t" \
          "buffer_load_dwordx4 %[vw0], %[rw], %[t] offen" PN_NT " lds\n\ts_add_u32 m0, %[wl], 116736\n\ts_add_u32 %[t], %[wo], 0x790000\n\tbuffer_load_dwordx4 %[vw1], %[rw], %[t] offen" PN_NT " lds\n\ts_add_u32 m0, %[wl], 117760\n\ts_add_u32 %[t], %[wo], 0x798000\n\t" \
          "buffer_load_dwordx4 %[vw1], %[rw], %[t] offen" PN_NT " lds\n\tds_read_b64_tr_b16 a[160:161], %[rt2] offset:16384\n\tds_read_b64_tr_b16 a[162:163], %[rt2] offset:17408\n\tds_read_b64_tr_b16 a[164:165], %[rt3] offset:16384\n\tds_read_b64_tr_b16 a[166:167], %[rt3] offset:17408\n\tds_read_b64_tr_b16 a[168:169], %[rt2] offset:24576\n\t" \
          "ds_read_b64_tr_b16 a[170:171], %[rt2] offset:25600\n\tds_read_b64_tr_b16 a[172:173], %[rt3] offset:24576\n\tds_read_b64_tr_b16 a[174:175], %[rt3] offset:25600\n\ts_waitcnt vmcnt(16) lgkmcnt(0)\n\ts_barrier\n\tds_read_b64_tr_b16 a[176:177], %[rt2] offset:32768\n\t" \
          "ds_read_b64_tr_b16 a[178:179], %[rt2] offset:33792\n\tds_read_b64_tr_b16 a[180:181], %[rt3] offset:32768\n\tds_read_b64_tr_b16 a[182:183], %[rt3] offset:33792\n\tds_read_b64_tr_b16 a[184:185], %[rt2] offset:40960\n\tds_read_b64_tr_b16 a[186:187], %[rt2] offset:41984\n\tds_read_b64_tr_b16 a[188:189], %[rt3] offset:40960\n\t" \
          "ds_read_b64_tr_b16 a[190:191], %[rt3] offset:41984\n\ts_waitcnt vmcnt(12) lgkmcnt(0)\n\ts_barrier\n\tds_read_b64_tr_b16 a[192:193], %[rt0] offset:0\n\tds_read_b64_tr_b16 a[194:195], %[rt0] offset:1024\n\tds_read_b64_tr_b16 a[196:197], %[rt1] offset:0\n\t" \
          "ds_read_b64_tr_b16 a[198:199], %[rt1] offset:1024\n\tds_read_b64_tr_b16 a[200:201], %[rt0] offset:8192\n\tds_read_b64_tr_b16 a[202:203], %[rt0] offset:9216\n\tds_read_b64_tr_b16 a[204:205], %[rt1] offset:8192\n\tds_read_b64_tr_b16 a[206:207], %[rt1] offset:9216\n\ts_waitcnt vmcnt(8) lgkmcnt(0)\n\t" \
          "s_barrier\n\tds_read_b64_tr_b16 a[208:209], %[rt0] offset:16384\n\tds_read_b64_tr_b16 a[210:211], %[rt0] offset:17408\n\tds_read_b64_tr_b16 a[212:213], %[rt1] offset:16384\n\tds_read_b64_tr_b16 a[214:215], %[rt1] offset:17408\n\tds_read_b64_tr_b16 a[216:217], %[rt0] offset:24576\n\t" \
          "ds_read_b64_tr_b16 a[218:219], %[rt0] offset:25600\n\tds_read_b64_tr_b16 a[220:221], %[rt1] offset:24576\n\tds_read_b64_tr_b16 a[222:223], %[rt1] offset:25600\n\ts_waitcnt vmcnt(4) lgkmcnt(0)\n\ts_barrier\n\tds_read_b64_tr_b16 a[224:225], %[rt0] offset:32768\n\t" \
          "ds_read_b64_tr_b16 a[226:227], %[rt0] offset:33792\n\tds_read_b64_tr_b16 a[228:229], %[rt1] offset:32768\n\tds_read_b64_tr_b16 a[230:231], %[rt1] offset:33792\n\tds_read_b64_tr_b16 a[232:233], %[rt0] offset:40960\n\tds_read_b64_tr_b16 a[234:235], %[rt0] offset:41984\n\tds_read_b64_tr_b16 a[236:237], %[rt1] offset:40960\n\t" \
          "ds_read_b64_tr_b16 a[238:239], %[rt1] offset:41984\n\ts_waitcnt vmcnt(0) lgkmcnt(0)\n\ts_barrier\n\tds_read_b64_tr_b16 a[240:241], %[rt2] offset:0\n\tds_read_b64_tr_b16 a[242:243], %[rt2] offset:1024\n\tds_read_b64_tr_b16 a[244:245], %[rt3] offset:0\n\t" \
          "ds_read_b64_tr_b16 a[246:247], %[rt3] offset:1024\n\tds_read_b64_tr_b16 a[248:249], %[rt2] offset:8192\n\tds_read_b64_tr_b16 a[250:251], %[rt2] offset:9216\n\tds_read_b64_tr_b16 a[252:253], %[rt3] offset:8192\n\tds_read_b64_tr_b16 a[254:255], %[rt3] offset:9216\n\ts_waitcnt lgkmcnt(0)\n\t" \
          "s_barrier\n\ts_add_u32 m0, %[sl], 0\n\ts_nop 0\n\tbuffer_load_dwordx4 %[vd0], s[40:43], %[xo] offen lds\n\tbuffer_load_dwordx4 %[vd0], s[40:43], %[xo] offen offset:1024 lds\n\tbuffer_load_dwordx4 %[vd1], s[40:43], %[xo] offen offset:2048 lds\n\t" \
          "buffer_load_dwordx4 %[vd1], s[40:43], %[xo] offen offset:3072 lds\n\ts_add_u32 m0, %[sl], 4096\n\ts_nop 0\n\tbuffer_load_dwordx4 %[vd2], s[40:43], %[xo] offen lds\n\tbuffer_load_dwordx4 %[vd2], s[40:43], %[xo] offen offset:1024 lds\n\tbuffer_load_dwordx4 %[vd3], s[40:43], %[xo] offen offset:2048 lds\n\t" \
          "buffer_load_dwordx4 %[vd3], s[40:43], %[xo] offen offset:3072 lds\n\ts_add_u32 %[xo], %[xo], 0x8000\n\ts_add_u32 %[sl], %[sl], 0x8000\n\ts_bitset0_b32 %[sl], 17\n\tds_read_b128 v[64:67], %[ra0] offset:0\n\tds_read_b128 v[68:71], %[ra1] offset:0\n\t" \
          "ds_read_b128 v[72:75], %[ra2] offset:0\n\tds_read_b128 v[76:79], %[ra3] offset:0\n\tds_read_b128 v[80:83], %[ra0] offset:256\n\tds_read_b128 v[84:87], %[ra1] offset:256\n\tds_read_b128 v[88:91], %[ra2] offset:256\n\tds_read_b128 v[92:95], %[ra3] offset:256\n\t" \
          "s_mov_b64 %[sx], 0\n\tLpn_loop%=:\n\ts_waitcnt lgkmcnt(6)\n\tv_mfma_f32_16x16x32_bf16 v[48:51], a[0:3], v[64:67], 0\n\tds_read_b128 v[96:99], %[ra0] offset:512\n\tv_mfma_f32_16x16x32_bf16 v[52:55], a[4:7], v[64:67], 0\n\t" \
          "s_add_u32 %[t], %[j], 3\n\ts_cmp_lt_u32 %[t], %[je]\n\ts_cselect_b32 s42, s42, 0\n\tv_mfma_f32_16x16x32_bf16 v[48:51], a[8:11], v[68:71], v[48:51]\n\tds_read_b128 v[100:103], %[ra1] offset:512\n\tv_mfma_f32_16x16x32_bf16 v[52:55], a[12:15], v[68:71], v[52:55]\n\t" \
          "v_cvt_pk_bf16_f32 v44, v56, v57\n\tv_cvt_pk_bf16_f32 v45, v58, v59\n\ts_waitcnt lgkmcnt(6)\n\tv_mfma_f32_16x16x32_bf16 v[48:51], a[16:19], v[72:75], v[48:51]\n\tds_read_b128 v[104:107], %[ra2] offset:512\n\tv_mfma_f32_16x16x32_bf16 v[52:55], a[20:23], v[72:75], v[52:55]\n\t" \
          "v_cvt_pk_bf16_f32 v46, v60, v61\n\tv_cvt_pk_bf16_f32 v47, v62, v63\n\tv_mfma_f32_16x16x32_bf16 v[48:51], a[24:27], v[76:79], v[48:51]\n\tds_read_b128 v[108:111], %[ra3] offset:512\n\tv_mfma_f32_16x16x32_bf16 v[52:55], a[28:31], v[76:79], v[52:55]\n\ts_mov_b64 exec, %[sx]\n\t" \
          "buffer_store_dwordx4 v[44:47], %[so], %[rc], %[co] offen sc1\n\ts_mov_b64 exec, -1\n\ts_waitcnt lgkmcnt(6)\n\tv_mfma_f32_16x16x32_bf16 v[48:51], a[32:35], v[80:83], v[48:51]\n\tds_read_b128 v[112:115], %[ra0] offset:768\n\tv_mfma_f32_16x16x32_bf16 v[52:55], a[36:39], v[80:83], v[52:55]\n\t" \
          "s_mov_b64 exec, %[fx]\n\tbuffer_store_dword %[fv], off, %[rf], %[fo] sc1\n\ts_mov_b64 exec, -1\n\tv_mfma_f32_16x16x32_bf16 v[48:51], a[40:43], v[84:87], v[48:51]\n\tds_read_b128 v[116:119], %[ra1] offset:768\n\tbuffer_load_dword v43, %[vp], s[40:43], %[po] offen nt\n\t" \
          "v_mfma_f32_16x16x32_bf16 v[52:55], a[44:47], v[84:87], v[52:55]\n\ts_add_u32 %[co], %[co], %[cs]\n\ts_mov_b32 %[cs], 0x4000\n\ts_mov_b64 %[sx], -1\n\tbuffer_load_dword v43, %[vp], s[40:43], %[po] offen offset:64\n\ts_waitcnt lgkmcnt(6)\n\t" \
          "v_mfma_f32_16x16x32_bf16 v[48:51], a[48:51], v[88:91], v[48:51]\n\tds_read_b128 v[120:123], %[ra2] offset:768\n\ts_add_u32 m0, %[sl], 0\n\tv_mfma_f32_16x16x32_bf16 v[52:55], a[52:55], v[88:91], v[52:55]\n\tbuffer_load_dwordx4 %[vd0], s[40:43], %[xo] offen lds\n\tv_mfma_f32_16x16x32_bf16 v[48:51], a[56:59], v[92:95], v[48:51]\n\t" \
          "ds_read_b128 v[124:127], %[ra3] offset:768\n\tv_mfma_f32_16x16x32_bf16 v[52:55], a[60:63], v[92:95], v[52:55]\n\ts_add_u32 %[po], %[po], 0x10000\n\ts_min_u32 %[po], %[po], 0x3f0000\n\ts_waitcnt lgkmcnt(6)\n\tv_mfma_f32_16x16x32_bf16 v[48:51], a[64:67], v[96:99], v[48:51]\n\t" \
          "ds_read_b128 v[64:67], %[ra0] offset:1024\n\tv_mfma_f32_16x16x32_bf16 v[52:55], a[68:71], v[96:99], v[52:55]\n\tbuffer_load_dwordx4 %[vd0], s[40:43], %[xo] offen offset:1024 lds\n\tv_mfma_f32_16x16x32_bf16 v[48:51], a[72:75], v[100:103], v[48:51]\n\tds_read_b128 v[68:71], %[ra1] offset:1024\n\tv_mfma_f32_16x16x32_bf16 v[52:55], a[76:79], v[100:103], v[52:55]\n\t" \
          "s_waitcnt lgkmcnt(6)\n\tv_mfma_f32_16x16x32_bf16 v[48:51], a[80:83], v[104:107], v[48:51]\n\tds_read_b128 v[72:75], %[ra2] offset:1024\n\tv_mfma_f32_16x16x32_bf16 v[52:55], a[84:87], v[104:107], v[52:55]\n\tbuffer_load_dwordx4 %[vd1], s[40:43], %[xo] offen offset:2048 lds\n\tv_mfma_f32_16x16x32_bf16 v[48:51], a[88:91], v[108:111], v[48:51]\n\t" \
          "ds_read_b128 v[76:79], %[ra3] offset:1024\n\tv_mfma_f32_16x16x32_bf16 v[52:55], a[92:95], v[108:111], v[52:55]\n\ts_waitcnt lgkmcnt(6)\n\tv_mfma_f32_16x16x32_bf16 v[48:51], a[96:99], v[112:115], v[48:51]\n\tds_read_b128 v[80:83], %[ra0] offset:1280\n\tv_mfma_f32_16x16x32_bf16 v[52:55], a[100:103], v[112:115], v[52:55]\n\t" \
          "buffer_load_dwordx4 %[vd1], s[40:43], %[xo] offen offset:3072 lds\n\tv_mfma_f32_16x16x32_bf16 v[48:51], a[104:107], v[116:119], v[48:51]\n\tds_read_b128 v[84:87], %[ra1] offset:1280\n\tv_mfma_f32_16x16x32_bf16 v[52:55], a[108:111], v[116:119], v[52:55]\n\ts_waitcnt lgkmcnt(6)\n\tv_mfma_f32_16x16x32_bf16 v[48:51], a[112:115], v[120:123], v[48:51]\n\t" \
          "ds_read_b128 v[88:91], %[ra2] offset:1280\n\ts_add_u32 m0, %[sl], 4096\n\tv_mfma_f32_16x16x32_bf16 v[52:55], a[116:119], v[120:123], v[52:55]\n\tbuffer_load_dwordx4 %[vd2], s[40:43], %[xo] offen lds\n\tv_mfma_f32_16x16x32_bf16 v[48:51], a[120:123], v[124:127], v[48:51]\n\tds_read_b128 v[92:95], %[ra3] offset:1280\n\t" \
          "v_mfma_f32_16x16x32_bf16 v[52:55], a[124:127], v[124:127], v[52:55]\n\ts_waitcnt lgkmcnt(6)\n\tv_mfma_f32_16x16x32_bf16 v[48:51], a[128:131], v[64:67], v[48:51]\n\tds_read_b128 v[96:99], %[ra0] offset:1536\n\tv_mfma_f32_16x16x32_bf16 v[52:55], a[132:135], v[64:67], v[52:55]\n\tbuffer_load_dwordx4 %[vd2], s[40:43], %[xo] offen offset:1024 lds\n\t" \
          "v_mfma_f32_16x16x32_bf16 v[48:51], a[136:139], v[68:71], v[48:51]\n\tds_read_b128 v[100:103], %[ra1] offset:1536\n\tv_mfma_f32_16x16x32_bf16 v[52:55], a[140:143], v[68:71], v[52:55]\n\ts_waitcnt lgkmcnt(6)\n\tv_mfma_f32_16x16x32_bf16 v[48:51], a[144:147], v[72:75], v[48:51]\n\tds_read_b128 v[104:107], %[ra2] offset:1536\n\t" \
          "v_mfma_f32_16x16x32_bf16 v[52:55], a[148:151], v[72:75], v[52:55]\n\tbuffer_load_dwordx4 %[vd3], s[40:43], %[xo] offen offset:2048 lds\n\tv_mfma_f32_16x16x32_bf16 v[48:51], a[152:155], v[76:79], v[48:51]\n\tds_read_b128 v[108:111], %[ra3] offset:1536\n\tv_mfma_f32_16x16x32_bf16 v[52:55], a[156:159], v[76:79], v[52:55]\n\ts_waitcnt lgkmcnt(6)\n\t" \
          "v_mfma_f32_16x16x32_bf16 v[48:51], a[160:163], v[80:83], v[48:51]\n\tds_read_b128 v[112:115], %[ra0] offset:1792\n\tv_mfma_f32_16x16x32_bf16 v[52:55], a[164:167], v[80:83], v[52:55]\n\tbuffer_load_dwordx4 %[vd3], s[40:43], %[xo] offen offset:3072 lds\n\tv_mfma_f32_16x16x32_bf16 v[48:51], a[168:171], v[84:87], v[48:51]\n\tds_read_b128 v[116:119], %[ra1] offset:1792\n\t" \
          "v_mfma_f32_16x16x32_bf16 v[52:55], a[172:175], v[84:87], v[52:55]\n\ts_waitcnt lgkmcnt(6)\n\tv_mfma_f32_16x16x32_bf16 v[48:51], a[176:179], v[88:91], v[48:51]\n\tds_read_b128 v[120:123], %[ra2] offset:1792\n\tv_mfma_f32_16x16x32_bf16 v[52:55], a[180:183], v[88:91], v[52:55]\n\tv_mfma_f32_16x16x32_bf16 v[48:51], a[184:187], v[92:95], v[48:51]\n\t" \
          "ds_read_b128 v[124:127], %[ra3] offset:1792\n\tv_mfma_f32_16x16x32_bf16 v[52:55], a[188:191], v[92:95], v[52:55]\n\ts_add_u32 %[xo], %[xo], 0x8000\n\ts_add_u32 %[sl], %[sl], 0x8000\n\ts_bitset0_b32 %[sl], 17\n\ts_waitcnt lgkmcnt(6)\n\t" \
          "v_mfma_f32_16x16x32_bf16 v[48:51], a[192:195], v[96:99], v[48:51]\n\tv_mfma_f32_16x16x32_bf16 v[52:55], a[196:199], v[96:99], v[52:55]\n\tv_mfma_f32_16x16x32_bf16 v[48:51], a[200:203], v[100:103], v[48:51]\n\tv_mfma_f32_16x16x32_bf16 v[52:55], a[204:207], v[100:103], v[52:55]\n\ts_waitcnt lgkmcnt(4)\n\tv_mfma_f32_16x16x32_bf16 v[48:51], a[208:211], v[104:107], v[48:51]\n\t" \
          "v_mfma_f32_16x16x32_bf16 v[52:55], a[212:215], v[104:107], v[52:55]\n\tv_mfma_f32_16x16x32_bf16 v[48:51], a[216:219], v[108:111], v[48:51]\n\tv_mfma_f32_16x16x32_bf16 v[52:55], a[220:223], v[108:111], v[52:55]\n\ts_waitcnt vmcnt(20) lgkmcnt(0)\n\ts_barrier\n\tv_mfma_f32_16x16x32_bf16 v[48:51], a[224:227], v[112:115], v[48:51]\n\t" \
          "ds_read_b128 v[64:67], %[ra0] offset:32768\n\tv_mfma_f32_16x16x32_bf16 v[52:55], a[228:231], v[112:115], v[52:55]\n\tds_read_b128 v[68:71], %[ra1] offset:32768\n\tv_mfma_f32_16x16x32_bf16 v[48:51], a[232:235], v[116:119], v[48:51]\n\tds_read_b128 v[72:75], %[ra2] offset:32768\n\tv_mfma_f32_16x16x32_bf16 v[52:55], a[236:239], v[116:119], v[52:55]\n\t" \
          "ds_read_b128 v[76:79], %[ra3] offset:32768\n\tv_mfma_f32_16x16x32_bf16 v[48:51], a[240:243], v[120:123], v[48:51]\n\tds_read_b128 v[80:83], %[ra0] offset:33024\n\tv_mfma_f32_16x16x32_bf16 v[52:55], a[244:247], v[120:123], v[52:55]\n\tds_read_b128 v[84:87], %[ra1] offset:33024\n\tv_mfma_f32_16x16x32_bf16 v[48:51], a[248:251], v[124:127], v[48:51]\n\t" \
          "ds_read_b128 v[88:91], %[ra2] offset:33024\n\tv_mfma_f32_16x16x32_bf16 v[52:55], a[252:255], v[124:127], v[52:55]\n\tds_read_b128 v[92:95], %[ra3] offset:33024\n\ts_waitcnt lgkmcnt(6)\n\tv_mfma_f32_16x16x32_bf16 v[56:59], a[0:3], v[64:67], 0\n\tds_read_b128 v[96:99], %[ra0] offset:33280\n\t" \
          "v_mfma_f32_16x16x32_bf16 v[60:63], a[4:7], v[64:67], 0\n\ts_add_u32 %[t], %[j], 4\n\ts_cmp_lt_u32 %[t], %[je]\n\ts_cselect_b32 s42, s42, 0\n\tv_mfma_f32_16x16x32_bf16 v[56:59], a[8:11], v[68:71], v[56:59]\n\tds_read_b128 v[100:103], %[ra1] offset:33280\n\t" \
          "v_mfma_f32_16x16x32_bf16 v[60:63], a[12:15], v[68:71], v[60:63]\n\tv_cvt_pk_bf16_f32 v44, v48, v49\n\tv_cvt_pk_bf16_f32 v45, v50, v51\n\ts_waitcnt lgkmcnt(6)\n\tv_mfma_f32_16x16x32_bf16 v[56:59], a[16:19], v[72:75], v[56:59]\n\tds_read_b128 v[104:107], %[ra2] offset:33280\n\t" \
          "v_mfma_f32_16x16x32_bf16 v[60:63], a[20:23], v[72:75], v[60:63]\n\tv_cvt_pk_bf16_f32 v46, v52, v53\n\tv_cvt_pk_bf16_f32 v47, v54, v55\n\tv_mfma_f32_16x16x32_bf16 v[56:59], a[24:27], v[76:79], v[56:59]\n\tds_read_b128 v[108:111], %[ra3] offset:33280\n\tv_mfma_f32_16x16x32_bf16 v[60:63], a[28:31], v[76:79], v[60:63]\n\t" \
          "buffer_store_dwordx4 v[44:47], %[so], %[rc], %[co] offen sc1\n\ts_waitcnt lgkmcnt(6)\n\tv_mfma_f32_16x16x32_bf16 v[56:59], a[32:35], v[80:83], v[56:59]\n\tds_read_b128 v[112:115], %[ra0] offset:33536\n\tv_mfma_f32_16x16x32_bf16 v[60:63], a[36:39], v[80:83], v[60:63]\n\tv_mov_b32 %[fv], %[j]\n\t" \
          "v_mfma_f32_16x16x32_bf16 v[56:59], a[40:43], v[84:87], v[56:59]\n\tds_read_b128 v[116:119], %[ra1] offset:33536\n\tv_mfma_f32_16x16x32_bf16 v[60:63], a[44:47], v[84:87], v[60:63]\n\ts_add_u32 %[co], %[co], 0x4000\n\ts_waitcnt lgkmcnt(6)\n\tv_mfma_f32_16x16x32_bf16 v[56:59], a[48:51], v[88:91], v[56:59]\n\t" \
          "ds_read_b128 v[120:123], %[ra2] offset:33536\n\ts_add_u32 m0, %[sl], 0\n\tv_mfma_f32_16x16x32_bf16 v[60:63], a[52:55], v[88:91], v[60:63]\n\tbuffer_load_dwordx4 %[vd0], s[40:43], %[xo] offen lds\n\tv_mfma_f32_16x16x32_bf16 v[56:59], a[56:59], v[92:95], v[56:59]\n\tds_read_b128 v[124:127], %[ra3] offset:33536\n\t" \
          "v_mfma_f32_16x16x32_bf16 v[60:63], a[60:63], v[92:95], v[60:63]\n\ts_waitcnt lgkmcnt(6)\n\tv_mfma_f32_16x16x32_bf16 v[56:59], a[64:67], v[96:99], v[56:59]\n\tds_read_b128 v[64:67], %[ra0] offset:33792\n\tv_mfma_f32_16x16x32_bf16 v[60:63], a[68:71], v[96:99], v[60:63]\n\tbuffer_load_dwordx4 %[vd0], s[40:43], %[xo] offen offset:1024 lds\n\t" \
          "v_mfma_f32_16x16x32_bf16 v[56:59], a[72:75], v[100:103], v[56:59]\n\tds_read_b128 v[68:71], %[ra1] offset:33792\n\tv_mfma_f32_16x16x32_bf16 v[60:63], a[76:79], v[100:103], v[60:63]\n\ts_waitcnt lgkmcnt(6)\n\tv_mfma_f32_16x16x32_bf16 v[56:59], a[80:83], v[104:107], v[56:59]\n\tds_read_b128 v[72:75], %[ra2] offset:33792\n\t" \
          "v_mfma_f32_16x16x32_bf16 v[60:63], a[84:87], v[104:107], v[60:63]\n\tbuffer_load_dwordx4 %[vd1], s[40:43], %[xo] offen offset:2048 lds\n\tv_mfma_f32_16x16x32_bf16 v[56:59], a[88:91], v[108:111], v[56:59]\n\tds_read_b128 v[76:79], %[ra3] offset:33792\n\tv_mfma_f32_16x16x32_bf16 v[60:63], a[92:95], v[108:111], v[60:63]\n\ts_waitcnt lgkmcnt(6)\n\t" \
          "v_mfma_f32_16x16x32_bf16 v[56:59], a[96:99], v[112:115], v[56:59]\n\tds_read_b128 v[80:83], %[ra0] offset:34048\n\tv_mfma_f32_16x16x32_bf16 v[60:63], a[100:103], v[112:115], v[60:63]\n\tbuffer_load_dwordx4 %[vd1], s[40:43], %[xo] offen offset:3072 lds\n\tv_mfma_f32_16x16x32_bf16 v[56:59], a[104:107], v[116:119], v[56:59]\n\tds_read_b128 v[84:87], %[ra1] offset:34048\n\t" \
          "v_mfma_f32_16x16x32_bf16 v[60:63], a[108:111], v[116:119], v[60:63]\n\ts_waitcnt lgkmcnt(6)\n\tv_mfma_f32_16x16x32_bf16 v[56:59], a[112:115], v[120:123], v[56:59]\n\tds_read_b128 v[88:91], %[ra2] offset:34048\n\ts_add_u32 m0, %[sl], 4096\n\tv_mfma_f32_16x16x32_bf16 v[60:63], a[116:119], v[120:123], v[60:63]\n\t" \
          "buffer_load_dwordx4 %[vd2], s[40:43], %[xo] offen lds\n\tv_mfma_f32_16x16x32_bf16 v[56:59], a[120:123], v[124:127], v[56:59]\n\tds_read_b128 v[92:95], %[ra3] offset:34048\n\tv_mfma_f32_16x16x32_bf16 v[60:63], a[124:127], v[124:127], v[60:63]\n\ts_waitcnt lgkmcnt(6)\n\tv_mfma_f32_16x16x32_bf16 v[56:59], a[128:131], v[64:67], v[56:59]\n\t" \
          "ds_read_b128 v[96:99], %[ra0] offset:34304\n\tv_mfma_f32_16x16x32_bf16 v[60:63], a[132:135], v[64:67], v[60:63]\n\tbuffer_load_dwordx4 %[vd2], s[40:43], %[xo] offen offset:1024 lds\n\tv_mfma_f32_16x16x32_bf16 v[56:59], a[136:139], v[68:71], v[56:59]\n\tds_read_b128 v[100:103], %[ra1] offset:34304\n\tv_mfma_f32_16x16x32_bf16 v[60:63], a[140:143], v[68:71], v[60:63]\n\t" \
          "s_waitcnt lgkmcnt(6)\n\tv_mfma_f32_16x16x32_bf16 v[56:59], a[144:147], v[72:75], v[56:59]\n\tds_read_b128 v[104:107], %[ra2] offset:34304\n\tv_mfma_f32_16x16x32_bf16 v[60:63], a[148:151], v[72:75], v[60:63]\n\tbuffer_load_dwordx4 %[vd3], s[40:43], %[xo] offen offset:2048 lds\n\tv_mfma_f32_16x16x32_bf16 v[56:59], a[152:155], v[76:79], v[56:59]\n\t" \
          "ds_read_b128 v[108:111], %[ra3] offset:34304\n\tv_mfma_f32_16x16x32_bf16 v[60:63], a[156:159], v[76:79], v[60:63]\n\ts_waitcnt lgkmcnt(6)\n\tv_mfma_f32_16x16x32_bf16 v[56:59], a[160:163], v[80:83], v[56:59]\n\tds_read_b128 v[112:115], %[ra0] offset:34560\n\tv_mfma_f32_16x16x32_bf16 v[60:63], a[164:167], v[80:83], v[60:63]\n\t" \
          "buffer_load_dwordx4 %[vd3], s[40:43], %[xo] offen offset:3072 lds\n\tv_mfma_f32_16x16x32_bf16 v[56:59], a[168:171], v[84:87], v[56:59]\n\tds_read_b128 v[116:119], %[ra1] offset:34560\n\tv_mfma_f32_16x16x32_bf16 v[60:63], a[172:175], v[84:87], v[60:63]\n\ts_waitcnt lgkmcnt(6)\n\tv_mfma_f32_16x16x32_bf16 v[56:59], a[176:179], v[88:91], v[56:59]\n\t" \
          "ds_read_b128 v[120:123], %[ra2] offset:34560\n\tv_mfma_f32_16x16x32_bf16 v[60:63], a[180:183], v[88:91], v[60:63]\n\tv_mfma_f32_16x16x32_bf16 v[56:59], a[184:187], v[92:95], v[56:59]\n\tds_read_b128 v[124:127], %[ra3] offset:34560\n\tv_mfma_f32_16x16x32_bf16 v[60:63], a[188:191], v[92:95], v[60:63]\n\ts_add_u32 %[xo], %[xo], 0x8000\n\t" \
          "s_add_u32 %[sl], %[sl], 0x8000\n\ts_bitset0_b32 %[sl], 17\n\ts_waitcnt lgkmcnt(6)\n\tv_mfma_f32_16x16x32_bf16 v[56:59], a[192:195], v[96:99], v[56:59]\n\tv_mfma_f32_16x16x32_bf16 v[60:63], a[196:199], v[96:99], v[60:63]\n\tv_xor_b32 %[ra0], 0x10000, %[ra0]\n\t" \
          "v_mfma_f32_16x16x32_bf16 v[56:59], a[200:203], v[100:103], v[56:59]\n\tv_mfma_f32_16x16x32_bf16 v[60:63], a[204:207], v[100:103], v[60:63]\n\tv_xor_b32 %[ra1], 0x10000, %[ra1]\n\ts_waitcnt lgkmcnt(4)\n\tv_mfma_f32_16x16x32_bf16 v[56:59], a[208:211], v[104:107], v[56:59]\n\tv_mfma_f32_16x16x32_bf16 v[60:63], a[212:215], v[104:107], v[60:63]\n\t" \
          "v_xor_b32 %[ra2], 0x10000, %[ra2]\n\tv_mfma_f32_16x16x32_bf16 v[56:59], a[216:219], v[108:111], v[56:59]\n\ts_add_u32 %[j], %[j], 2\n\ts_cmp_lt_u32 %[j], %[je]\n\tv_mfma_f32_16x16x32_bf16 v[60:63], a[220:223], v[108:111], v[60:63]\n\tv_xor_b32 %[ra3], 0x10000, %[ra3]\n\t" \
          "s_waitcnt vmcnt(19) lgkmcnt(0)\n\ts_barrier\n\tv_mfma_f32_16x16x32_bf16 v[56:59], a[224:227], v[112:115], v[56:59]\n\tds_read_b128 v[64:67], %[ra0] offset:0\n\tv_mfma_f32_16x16x32_bf16 v[60:63], a[228:231], v[112:115], v[60:63]\n\tds_read_b128 v[68:71], %[ra1] offset:0\n\t" \
          "v_mfma_f32_16x16x32_bf16 v[56:59], a[232:235], v[116:119], v[56:59]\n\tds_read_b128 v[72:75], %[ra2] offset:0\n\tv_mfma_f32_16x16x32_bf16 v[60:63], a[236:239], v[116:119], v[60:63]\n\tds_read_b128 v[76:79], %[ra3] offset:0\n\tv_mfma_f32_16x16x32_bf16 v[56:59], a[240:243], v[120:123], v[56:59]\n\tds_read_b128 v[80:83], %[ra0] offset:256\n\t" \
          "v_mfma_f32_16x16x32_bf16 v[60:63], a[244:247], v[120:123], v[60:63]\n\tds_read_b128 v[84:87], %[ra1] offset:256\n\tv_mfma_f32_16x16x32_bf16 v[56:59], a[248:251], v[124:127], v[56:59]\n\tds_read_b128 v[88:91], %[ra2] offset:256\n\tv_mfma_f32_16x16x32_bf16 v[60:63], a[252:255], v[124:127], v[60:63]\n\tds_read_b128 v[92:95], %[ra3] offset:256\n\t" \
          "s_cbranch_scc1 Lpn_loop%=\n\ts_nop 7\n\tv_mov_b32 %[fv], %[je]\n\tv_cvt_pk_bf16_f32 v44, v56, v57\n\tv_cvt_pk_bf16_f32 v45, v58, v59\n\tv_cvt_pk_bf16_f32 v46, v60, v61\n\t" \
          "v_cvt_pk_bf16_f32 v47, v62, v63\n\tbuffer_store_dwordx4 v[44:47], %[so], %[rc], %[co] offen sc1\n\ts_waitcnt vmcnt(0) lgkmcnt(0)\n\ts_barrier\n\ts_mov_b64 exec, %[fx]\n\tbuffer_store_dword %[fv], off, %[rf], %[fo] sc1\n\t" \
          "s_mov_b64 exec, -1\n\t"
#define PN_CLOB "v43", "v44", "v45", "v46", "v47", "v48", "v49", "v50", "v51", "v52", "v53", "v54", "v55", "v56", "v57", "v58", "v59", "v60", "v61", "v62", "v63", "v64", "v65", "v66", "v67", "v68", "v69", "v70", "v71", "v72", "v73", "v74", "v75", "v76", "v77", "v78", "v79", "v80", "v81", "v82", "v83", "v84", "v85", "v86", "v87", "v88", "v89", "v90", "v91", "v92", "v93", "v94", "v95", "v96", "v97", "v98", "v99", "v100", "v101", "v102", "v103", "v104", "v105", "v106", "v107", "v108", "v109", "v110", "v111", "v112", "v113", "v114", "v115", "v116", "v117", "v118", "v119", "v120", "v121", "v122", "v123", "v124", "v125", "v126", "v127", "a0", "a1", "a2", "a3", "a4", "a5", "a6", "a7", "a8", "a9", "a10", "a11", "a12", "a13", "a14", "a15", "a16", "a17", "a18", "a19", "a20", "a21", "a22", "a23", "a24", "a25", "a26", "a27", "a28", "a29", "a30", "a31", "a32", "a33", "a34", "a35", "a36", "a37", "a38", "a39", "a40", "a41", "a42", "a43", "a44", "a45", "a46", "a47", "a48", "a49", "a50", "a51", "a52", "a53", "a54", "a55", "a56", "a57", "a58", "a59", "a60", "a61", "a62", "a63", "a64", "a65", "a66", "a67", "a68", "a69", "a70", "a71", "a72", "a73", "a74", "a75", "a76", "a77", "a78", "a79", "a80", "a81", "a82", "a83", "a84", "a85", "a86", "a87", "a88", "a89", "a90", "a91", "a92", "a93", "a94", "a95", "a96", "a97", "a98", "a99", "a100", "a101", "a102", "a103", "a104", "a105", "a106", "a107", "a108", "a109", "a110", "a111", "a112", "a113", "a114", "a115", "a116", "a117", "a118", "a119", "a120", "a121", "a122", "a123", "a124", "a125", "a126", "a127", "a128", "a129", "a130", "a131", "a132", "a133", "a134", "a135", "a136", "a137", "a138", "a139", "a140", "a141", "a142", "a143", "a144", "a145", "a146", "a147", "a148", "a149", "a150", "a151", "a152", "a153", "a154", "a155", "a156", "a157", "a158", "a159", "a160", "a161", "a162", "a163", "a164", "a165", "a166", "a167", "a168", "a169", "a170", "a171", "a172", "a173", "a174", "a175", "a176", "a177", "a178", "a179", "a180", "a181", "a182", "a183", "a184", "a185", "a186", "a187", "a188", "a189", "a190", "a191", "a192", "a193", "a194", "a195", "a196", "a197", "a198", "a199", "a200", "a201", "a202", "a203", "a204", "a205", "a206", "a207", "a208", "a209", "a210", "a211", "a212", "a213", "a214", "a215", "a216", "a217", "a218", "a219", "a220", "a221", "a222", "a223", "a224", "a225", "a226", "a227", "a228", "a229", "a230", "a231", "a232", "a233", "a234", "a235", "a236", "a237", "a238", "a239", "a240", "a241", "a242", "a243", "a244", "a245", "a246", "a247", "a248", "a249", "a250", "a251", "a252", "a253", "a254", "a255", "s40", "s41", "s42", "s43"
#define NTP 0
#define PN_NT ""
__device__ __forceinline__ void panel_task(const FParams& F, int p, int fi, int j, int je, int w) {
  const int lane = opaque_lane(), l15 = lane & 15, lq = lane >> 4, m = p >> 5, h = p & 31, n = 16 * m + (h >> 1);
  const __amdgpu_buffer_rsrc_t rw = __builtin_amdgcn_make_buffer_rsrc(wg::uni((m == 0 ? F.Wq : (m == 1 ? F.Wk : F.Wv)) + h * DHA), 0, 0x7fffffff, 0x00020000);
  const __amdgpu_buffer_rsrc_t rc = __builtin_amdgcn_make_buffer_rsrc((char*)F.qkvA + __builtin_amdgcn_readfirstlane(n * QPAGE + (F.par[192 + n] ? 8192 : 0) + (h & 1) * 4096 + w * 1024), 0, QPAGE, 0x00020000);
  const __amdgpu_buffer_rsrc_t rf = __builtin_amdgcn_make_buffer_rsrc(F.ctl, 0, sizeof(Ctl), 0x00020000);
  int ra[4], vd[4], rt[4], vw[2], fv = 0, xo = w * 8192 + j * 32768, sl = w * 8192, co = j * QBLK, cs = 0, po = j * 32768, t; unsigned long long sx;
#pragma unroll
  for (int i = 0; i < 4; ++i) {
    ra[i] = l15 * 2048 + (((4 * i + lq) ^ l15) << 4); vd[i] = ((lane ^ (4 * w + i)) << 4) + (i >> 1) * 4096;
    rt[i] = 65536 + (i >> 1) * 49152 + (8 * lq + (l15 >> 2)) * 256 + (((2 * w + (i & 1)) ^ ((l15 >> 2) | ((lq & 1) << 2))) << 5) + (l15 & 3) * 8;
  }
  vw[0] = lq * 8192 + ((l15 ^ (lq << 1)) << 4); vw[1] = vw[0] ^ 128;
  asm volatile(PN_BODY
    : [ra0] "+v"(ra[0]), [ra1] "+v"(ra[1]), [ra2] "+v"(ra[2]), [ra3] "+v"(ra[3]), [fv] "+v"(fv), [xo] "+s"(xo), [sl] "+s"(sl), [co] "+s"(co), [cs] "+s"(cs), [po] "+s"(po), [t] "=&s"(t), [j] "+s"(j), [sx] "=&s"(sx)
    : [vd0] "v"(vd[0]), [vd1] "v"(vd[1]), [vd2] "v"(vd[2]), [vd3] "v"(vd[3]), [rt0] "v"(rt[0]), [rt1] "v"(rt[1]), [rt2] "v"(rt[2]), [rt3] "v"(rt[3]), [vw0] "v"(vw[0]), [vw1] "v"(vw[1]), [so] "v"(lane * 16), [vp] "v"(lane < 4 && (F.eps & 8) ? (h * 4 + w) * 512 + lane * 128 : 0x7ffffff0),
      [xa] "s"(F.Ax), [rw] "s"(rw), [rc] "s"(rc), [rf] "s"(rf), [wl] "s"(w * 4096), [wo] "s"(w * 16 * 8192), [fo] "s"((int)offsetof(Ctl, prg) + fi * 128), [je] "s"(je), [fx] "s"((unsigned long long)(unsigned)__builtin_amdgcn_readfirstlane(w == 0))
    : PN_CLOB, "scc", "memory");
}

struct AttnX { v4u q[2][4], kx[2][4], vr[8]; };
template <int AUX = 0>
__device__ __forceinline__ void attnA_load_x(const FParams& F, unsigned long long parq, int seg, int h, int lane, AttnX& X) {
  const __amdgpu_buffer_rsrc_t rs = __builtin_amdgcn_make_buffer_rsrc(F.qkvA, 0, QSIZE, 0x00020000);
  const int so = seg * 2 * QBLK + (h & 1) * 4096, oq = so + qoff(parq, h >> 1), ok = so + qoff(parq, 16 + (h >> 1)), ov = so + qoff(parq, 32 + (h >> 1));
#pragma unroll
  for (int i = 0; i < 8; ++i) X.vr[i] = __builtin_amdgcn_raw_buffer_load_b128(rs, lane * 16 + (i & 3) * 1024, ov + (i >> 2) * QBLK, AUX);
#pragma unroll
  for (int t = 0; t < 2; ++t)
#pragma unroll
    for (int j = 0; j < 4; ++j) {
      X.q[t][j] = __builtin_amdgcn_raw_buffer_load_b128(rs, lane * 16 + j * 1024, oq + t * QBLK, AUX);
      X.kx[t][j] = __builtin_amdgcn_raw_buffer_load_b128(rs, lane * 16 + j * 1024, ok + t * QBLK, AUX);
    }
}
__device__ __forceinline__ void attnA_stage_v(const AttnX& X, u16* vs  , int lane) {
#pragma unroll
  for (int i = 0; i < 8; ++i) *(v4u*)&vs[(16 * (i >> 2) + (lane & 15)) * VXLD + 32 * (i & 3) + 8 * (lane >> 4)] = X.vr[i];
}
template <int WHAT = 3>
__device__ __forceinline__ unsigned attnA_load_m(const FParams& F, unsigned long long parv, int seg, int h, int lane, v4u (&km)[4], unsigned (&vm)[8]) {
  const int l15 = lane & 15, lq = lane >> 4;
  const __amdgpu_buffer_rsrc_t rk = __builtin_amdgcn_make_buffer_rsrc(F.kmem + (size_t)seg * MEM * HDA, 0, MEM * HDA * 2, 0x00020000);
  const __amdgpu_buffer_rsrc_t rv = __builtin_amdgcn_make_buffer_rsrc((char*)F.vbuf + (size_t)seg * OREG + (int)((parv >> seg) & 1) * 8192 + dbl(h * 2048), 0, 2048, 0x00020000);
#pragma unroll
  for (int j = 0; j < 4; ++j)
    if (WHAT & 1) km[j] = __builtin_amdgcn_raw_buffer_load_b128(rk, h * 2048 + (2 * (lane >> 2) + (lane & 1)) * 16 + 512 * j, 0, 16);
#pragma unroll
  for (int t = 0; t < 8; ++t)
    if (WHAT & 2) vm[t] = __builtin_amdgcn_raw_buffer_load_b32(rv, (((l15 >> 3) * 4 + lq) * 16 + 8 * ((l15 >> 2) & 1) + (l15 & 3)) * 4 + 512 * (t >> 1) + 16 * (t & 1), 0, 16);
  unsigned m = 0;
#pragma unroll
  for (int j = 0; j < 4; ++j) if (WHAT & 1) m = chk4(m, km[j]);
#pragma unroll
  for (int t = 0; t < 8; t += 2) if (WHAT & 2) m = umax3(m, vm[t], vm[t + 1]);
  return m;
}
__device__ __forceinline__ void attnA_compute(const AttnX& X, const v4u (&km)[4], const unsigned (&vm)[8], const u16* vs, int lane, v4u (&ov)[2][4]) {
  const int l15 = lane & 15, lq = lane >> 4;
  const float scale = 0.08838834764831845f, L2E = 1.4426950408889634f;
  v4u px[2], pm[2];
#pragma unroll
  for (int t = 0; t < 2; ++t) {
    f32x4 s0 = {0.f,0.f,0.f,0.f}, s1 = s0, s2 = s0;
#pragma unroll
    for (int j = 0; j < 4; ++j) { s0 = mf(km[j], X.q[t][j], s0); s1 = mf(X.kx[0][j], X.q[t][j], s1); if (t == 1) s2 = mf(X.kx[1][j], X.q[t][j], s2); }
    float sv[12];
#pragma unroll
    for (int e = 0; e < 4; ++e) { sv[e] = s0[e]; sv[4 + e] = s1[e]; sv[8 + e] = s2[e]; }
#pragma unroll
    for (int e = 0; e < 12; e += 2) {
      unsigned p = pk2(sv[e], sv[e + 1]);
      p = pk2(__uint_as_float(p << 16) * scale, __uint_as_float(p & 0xffff0000u) * scale);
      sv[e] = __uint_as_float(p << 16); sv[e + 1] = __uint_as_float(p & 0xffff0000u);
    }
    bool ok[12];
#pragma unroll
    for (int e = 0; e < 4; ++e) { ok[e] = e < 2; ok[4 + e] = (t == 1) || (4 * lq + e <= l15); ok[8 + e] = (t == 1) && (4 * lq + e <= l15); }
    float mx = -INFINITY;
#pragma unroll
    for (int e = 0; e < 12; ++e) mx = ok[e] ? fmaxf(mx, sv[e]) : mx;
    mx = xmax32(xmax16(mx));
    float sum = 0.f;
#pragma unroll
    for (int e = 0; e < 12; ++e) { sv[e] = ok[e] ? __builtin_amdgcn_exp2f((sv[e] - mx) * L2E) : 0.f; sum += sv[e]; }
    sum = xsum32(xsum16(sum));
    const float inv = __builtin_amdgcn_rcpf(sum);
#pragma unroll
    for (int e = 0; e < 12; ++e) sv[e] = ok[e] ? sv[e] * inv : 0.f;
    px[t] = (v4u){pk2(sv[4], sv[5]), pk2(sv[6], sv[7]), pk2(sv[8], sv[9]), pk2(sv[10], sv[11])};
    pm[t] = (v4u){pk2(sv[0], sv[1]), 0u, 0u, 0u};
  }
  const u16* vp = &vs[(4 * lq + (l15 >> 2)) * VXLD + 16 * (l15 & 1) + 2 * (l15 & 2)];
#pragma unroll
  for (int u = 0; u < 4; ++u) {
    f32x4 o[2][2];
#pragma unroll
    for (int ab = 0; ab < 2; ++ab) {
      v2u lo = trrd(vp + 32 * u + 8 * ab), hi = trrd(vp + 16 * VXLD + 32 * u + 8 * ab);
      const v4u va = {lo.x, lo.y, hi.x, hi.y}, vb = {vm[2 * u + ab], 0u, 0u, 0u};
#pragma unroll
      for (int t = 0; t < 2; ++t) { o[t][ab] = mf(va, px[t], (f32x4){0.f,0.f,0.f,0.f}); o[t][ab] = mf(vb, pm[t], o[t][ab]); }
    }
#pragma unroll
    for (int t = 0; t < 2; ++t)
      ov[t][u] = (v4u){pk2(o[t][0][0], o[t][0][1]), pk2(o[t][0][2], o[t][0][3]), pk2(o[t][1][0], o[t][1][1]), pk2(o[t][1][2], o[t][1][3])};
  }
}
__device__ __forceinline__ void attnA_block(const FParams& F, unsigned long long parv, unsigned long long parq, int r, int h, u16* vs, int lane, int w, unsigned* shw) {
  AttnX X[2]; v4u km[2][4]; unsigned vm[2][8];
  unsigned seen = 0;
  attnA_load_x(F, parq, 4 * r, h, lane, X[0]);
  bool have = false;
#pragma unroll
  for (int i = 0; i < 4; ++i) {
    const int seg = 4 * r + i, c = i & 1;
    v4u ov[2][4];
    if (!have) {
      if (seen < (unsigned)seg + 1) {
        if (w == 0) {
          for (unsigned spins = 0;; ++spins) {
            seen = __builtin_amdgcn_readfirstlane(ld_agent(&F.ctl->prog[0]));
            if (seen >= (unsigned)seg + 1 || spin_fail(spins, &F.ctl->tmo[0], 6u)) break;
            for (unsigned z = seen; z < (unsigned)seg + 1; ++z) __builtin_amdgcn_s_sleep(PSL);
          }
          *shw = seen;
        }
        __syncthreads();
        seen = __builtin_amdgcn_readfirstlane(*shw);
        __syncthreads();
      }
      asm volatile("" ::: "memory");
      (void)attnA_load_m(F, parv, seg, h, lane, km[c], vm[c]);
    }
    if (i < 3) {
      attnA_load_x(F, parq, seg + 1, h, lane, X[c ^ 1]);
      have = seen >= (unsigned)seg + 2;
      (void)attnA_load_m(F, parv, have ? seg + 1 : seg, h, lane, km[c ^ 1], vm[c ^ 1]);
    }
    SB0;
    attnA_stage_v(X[c], vs, lane);
    attnA_compute(X[c], km[c], vm[c], vs, lane, ov);
    const __amdgpu_buffer_rsrc_t rs = __builtin_amdgcn_make_buffer_rsrc(F.qkvA, 0, QSIZE, 0x00020000);
#pragma unroll
    for (int u = 0; u < 4; ++u)
#pragma unroll
      for (int t = 0; t < 2; ++t)
        __builtin_amdgcn_raw_buffer_store_b128(ov[t][u], rs, lane * 16 + u * 1024, qoff(parq, h >> 1) + (seg * 2 + t) * QBLK + (h & 1) * 4096, 16);
    SB0;
  }
}

constexpr int TSEG0 = 32, OSEG0 = 16, NSET = 4;
__device__ __forceinline__ void attn_stream(const FParams& F, unsigned long long parv, unsigned long long parq, int set, int hg, char* lds, int w) {
  const int lane = opaque_lane(), h = 4 * hg + w;
  Ctl* ctl = F.ctl;
  for (unsigned spins = 0;; ++spins) {
    if (__all(ld_agent(&ctl->prg[(lane < 2 ? lane : 3) * 32 + h][0]) >= 128u) || spin_fail(spins, &ctl->tmo[0], 2u)) break;
    __builtin_amdgcn_s_sleep(16);
  }
  asm volatile("" ::: "memory");
  u16* vs = (u16*)(lds + w * 16384);
  AttnX X;
  attnA_load_x<16>(F, parq, TSEG0 + set, h, lane, X);
#pragma unroll 1
  for (int seg = TSEG0 + set; seg < NSEG; seg += NSET) {
    v4u km[4], ov[2][4]; unsigned vm[8];
    attnA_stage_v(X, vs, lane);
    for (unsigned spins = 0;; ++spins) {
      if (__all(attnA_load_m<1>(F, parv, seg, h, lane, km, vm) != 0xffffffffu) || spin_fail(spins, &ctl->tmo[0], 4u)) break;
      __builtin_amdgcn_s_sleep(TSL);
    }
    asm volatile("" ::: "memory");
    (void)attnA_load_m<2>(F, parv, seg, h, lane, km, vm);
    attnA_compute(X, km, vm, vs, lane, ov);
    const __amdgpu_buffer_rsrc_t rs = __builtin_amdgcn_make_buffer_rsrc(F.qkvA, 0, QSIZE, 0x00020000);
#pragma unroll
    for (int u = 0; u < 4; ++u)
#pragma unroll
      for (int t = 0; t < 2; ++t)
        __builtin_amdgcn_raw_buffer_store_b128(ov[t][u], rs, lane * 16 + u * 1024, qoff(parq, h >> 1) + (seg * 2 + t) * QBLK + (h & 1) * 4096, 16);
    SB0;
    attnA_load_x<16>(F, parq, seg + NSET < NSEG ? seg + NSET : TSEG0 - 1, h, lane, X);
    SB0;
    asm volatile("s_waitcnt vmcnt(24)\n\ts_barrier" ::: "memory");
    if (w == 0 && lane == 0) __hip_atomic_fetch_add(&ctl->cntO[seg][0], 1u, RLX_AGENT);
  }
}
#define OU_BODY \
          "s_mov_b64 s[40:41], %[qa]\n\ts_mov_b32 s42, %[qs]\n\ts_mov_b32 s43, 0x20000\n\ts_mov_b64 s[44:45], %[qa]\n\ts_mov_b32 s47, 0x20000\n\ts_nop 4\n\t" \
          "s_add_u32 m0, %[wl], 0\n\ts_xor_b32 %[t], %[wo], 0x0\n\tbuffer_load_dwordx4 %[vw0], %[rw], %[t] offen lds\n\ts_add_u32 m0, %[wl], 1024\n\ts_xor_b32 %[t], %[wo], 0x4000\n\tbuffer_load_dwordx4 %[vw1], %[rw], %[t] offen lds\n\t" \
          "s_add_u32 m0, %[wl], 2048\n\ts_xor_b32 %[t], %[wo], 0x8000\n\tbuffer_load_dwordx4 %[vw0], %[rw], %[t] offen lds\n\ts_add_u32 m0, %[wl], 3072\n\ts_xor_b32 %[t], %[wo], 0xc000\n\tbuffer_load_dwordx4 %[vw1], %[rw], %[t] offen lds\n\t" \
          "s_add_u32 m0, %[wl], 4096\n\ts_xor_b32 %[t], %[wo], 0x10000\n\tbuffer_load_dwordx4 %[vw0], %[rw], %[t] offen lds\n\ts_add_u32 m0, %[wl], 5120\n\ts_xor_b32 %[t], %[wo], 0x14000\n\tbuffer_load_dwordx4 %[vw1], %[rw], %[t] offen lds\n\t" \
          "s_add_u32 m0, %[wl], 6144\n\ts_xor_b32 %[t], %[wo], 0x18000\n\tbuffer_load_dwordx4 %[vw0], %[rw], %[t] offen lds\n\ts_add_u32 m0, %[wl], 7168\n\ts_xor_b32 %[t], %[wo], 0x1c000\n\tbuffer_load_dwordx4 %[vw1], %[rw], %[t] offen lds\n\t" \
          "s_add_u32 m0, %[wl], 8192\n\ts_xor_b32 %[t], %[wo], 0x20000\n\tbuffer_load_dwordx4 %[vw0], %[rw], %[t] offen lds\n\ts_add_u32 m0, %[wl], 9216\n\ts_xor_b32 %[t], %[wo], 0x24000\n\tbuffer_load_dwordx4 %[vw1], %[rw], %[t] offen lds\n\t" \
          "s_add_u32 m0, %[wl], 10240\n\ts_xor_b32 %[t], %[wo], 0x28000\n\tbuffer_load_dwordx4 %[vw0], %[rw], %[t] offen lds\n\ts_add_u32 m0, %[wl], 11264\n\ts_xor_b32 %[t], %[wo], 0x2c000\n\tbuffer_load_dwordx4 %[vw1], %[rw], %[t] offen lds\n\t" \
          "s_add_u32 m0, %[wl], 12288\n\ts_xor_b32 %[t], %[wo], 0x30000\n\tbuffer_load_dwordx4 %[vw0], %[rw], %[t] offen lds\n\ts_add_u32 m0, %[wl], 13312\n\ts_xor_b32 %[t], %[wo], 0x34000\n\tbuffer_load_dwordx4 %[vw1], %[rw], %[t] offen lds\n\t" \
          "s_add_u32 m0, %[wl], 14336\n\ts_xor_b32 %[t], %[wo], 0x38000\n\tbuffer_load_dwordx4 %[vw0], %[rw], %[t] offen lds\n\ts_add_u32 m0, %[wl], 15360\n\ts_xor_b32 %[t], %[wo], 0x3c000\n\tbuffer_load_dwordx4 %[vw1], %[rw], %[t] offen lds\n\t" \
          "s_add_u32 m0, %[wl], 16384\n\ts_xor_b32 %[t], %[wo], 0x40000\n\tbuffer_load_dwordx4 %[vw0], %[rw], %[t] offen lds\n\ts_add_u32 m0, %[wl], 17408\n\ts_xor_b32 %[t], %[wo], 0x44000\n\tbuffer_load_dwordx4 %[vw1], %[rw], %[t] offen lds\n\t" \
          "s_add_u32 m0, %[wl], 18432\n\ts_xor_b32 %[t], %[wo], 0x48000\n\tbuffer_load_dwordx4 %[vw0], %[rw], %[t] offen lds\n\ts_add_u32 m0, %[wl], 19456\n\ts_xor_b32 %[t], %[wo], 0x4c000\n\tbuffer_load_dwordx4 %[vw1], %[rw], %[t] offen lds\n\t" \
          "s_add_u32 m0, %[wl], 20480\n\ts_xor_b32 %[t], %[wo], 0x50000\n\tbuffer_load_dwordx4 %[vw0], %[rw], %[t] offen lds\n\ts_add_u32 m0, %[wl], 21504\n\ts_xor_b32 %[t], %[wo], 0x54000\n\tbuffer_load_dwordx4 %[vw1], %[rw], %[t] offen lds\n\t" \
          "s_add_u32 m0, %[wl], 22528\n\ts_xor_b32 %[t], %[wo], 0x58000\n\tbuffer_load_dwordx4 %[vw0], %[rw], %[t] offen lds\n\ts_add_u32 m0, %[wl], 23552\n\ts_xor_b32 %[t], %[wo], 0x5c000\n\tbuffer_load_dwordx4 %[vw1], %[rw], %[t] offen lds\n\t" \
          "s_add_u32 m0, %[wl], 24576\n\ts_xor_b32 %[t], %[wo], 0x60000\n\tbuffer_load_dwordx4 %[vw0], %[rw], %[t] offen lds\n\ts_add_u32 m0, %[wl], 25600\n\ts_xor_b32 %[t], %[wo], 0x64000\n\tbuffer_load_dwordx4 %[vw1], %[rw], %[t] offen lds\n\t" \
          "s_add_u32 m0, %[wl], 26624\n\ts_xor_b32 %[t], %[wo], 0x68000\n\tbuffer_load_dwordx4 %[vw0], %[rw], %[t] offen lds\n\ts_add_u32 m0, %[wl], 27648\n\ts_xor_b32 %[t], %[wo], 0x6c000\n\tbuffer_load_dwordx4 %[vw1], %[rw], %[t] offen lds\n\t" \
          "s_add_u32 m0, %[wl], 28672\n\ts_xor_b32 %[t], %[wo], 0x70000\n\tbuffer_load_dwordx4 %[vw0], %[rw], %[t] offen lds\n\ts_add_u32 m0, %[wl], 29696\n\ts_xor_b32 %[t], %[wo], 0x74000\n\tbuffer_load_dwordx4 %[vw1], %[rw], %[t] offen lds\n\t" \
          "s_add_u32 m0, %[wl], 30720\n\ts_xor_b32 %[t], %[wo], 0x78000\n\tbuffer_load_dwordx4 %[vw0], %[rw], %[t] offen lds\n\ts_add_u32 m0, %[wl], 31744\n\ts_xor_b32 %[t], %[wo], 0x7c000\n\tbuffer_load_dwordx4 %[vw1], %[rw], %[t] offen lds\n\t" \
          "s_add_u32 m0, %[wl], 32768\n\ts_xor_b32 %[t], %[wo], 0x80000\n\tbuffer_load_dwordx4 %[vw0], %[rw], %[t] offen lds\n\ts_add_u32 m0, %[wl], 33792\n\ts_xor_b32 %[t], %[wo], 0x84000\n\tbuffer_load_dwordx4 %[vw1], %[rw], %[t] offen lds\n\t" \
          "s_add_u32 m0, %[wl], 34816\n\ts_xor_b32 %[t], %[wo], 0x88000\n\tbuffer_load_dwordx4 %[vw0], %[rw], %[t] offen lds\n\ts_add_u32 m0, %[wl], 35840\n\ts_xor_b32 %[t], %[wo], 0x8c000\n\tbuffer_load_dwordx4 %[vw1], %[rw], %[t] offen lds\n\t" \
          "s_add_u32 m0, %[wl], 36864\n\ts_xor_b32 %[t], %[wo], 0x90000\n\tbuffer_load_dwordx4 %[vw0], %[rw], %[t] offen lds\n\ts_add_u32 m0, %[wl], 37888\n\ts_xor_b32 %[t], %[wo], 0x94000\n\tbuffer_load_dwordx4 %[vw1], %[rw], %[t] offen lds\n\t" \
          "s_add_u32 m0, %[wl], 38912\n\ts_xor_b32 %[t], %[wo], 0x98000\n\tbuffer_load_dwordx4 %[vw0], %[rw], %[t] offen lds\n\ts_add_u32 m0, %[wl], 39936\n\ts_xor_b32 %[t], %[wo], 0x9c000\n\tbuffer_load_dwordx4 %[vw1], %[rw], %[t] offen lds\n\t" \
          "s_waitcnt vmcnt(32)\n\tds_read_b64_tr_b16 a[0:1], %[rt0] offset:0\n\tds_read_b64_tr_b16 a[2:3], %[rt0] offset:512\n\tds_read_b64_tr_b16 a[4:5], %[rt1] offset:0\n\tds_read_b64_tr_b16 a[6:7], %[rt1] offset:512\n\tds_read_b64_tr_b16 v[128:129], %[rt2] offset:0\n\t" \
          "ds_read_b64_tr_b16 v[130:131], %[rt2] offset:512\n\tds_read_b64_tr_b16 v[32:33], %[rt3] offset:0\n\tds_read_b64_tr_b16 v[34:35], %[rt3] offset:512\n\tds_read_b64_tr_b16 a[8:9], %[rt0] offset:4096\n\tds_read_b64_tr_b16 a[10:11], %[rt0] offset:4608\n\tds_read_b64_tr_b16 a[12:13], %[rt1] offset:4096\n\t" \
          "ds_read_b64_tr_b16 a[14:15], %[rt1] offset:4608\n\tds_read_b64_tr_b16 v[132:133], %[rt2] offset:4096\n\tds_read_b64_tr_b16 v[134:135], %[rt2] offset:4608\n\tds_read_b64_tr_b16 v[36:37], %[rt3] offset:4096\n\tds_read_b64_tr_b16 v[38:39], %[rt3] offset:4608\n\ts_waitcnt lgkmcnt(0)\n\t" \
          "s_add_u32 m0, %[wl], 0\n\ts_xor_b32 %[t], %[wo], 0xa0000\n\tbuffer_load_dwordx4 %[vw0], %[rw], %[t] offen lds\n\ts_add_u32 m0, %[wl], 1024\n\ts_xor_b32 %[t], %[wo], 0xa4000\n\tbuffer_load_dwordx4 %[vw1], %[rw], %[t] offen lds\n\t" \
          "s_add_u32 m0, %[wl], 2048\n\ts_xor_b32 %[t], %[wo], 0xa8000\n\tbuffer_load_dwordx4 %[vw0], %[rw], %[t] offen lds\n\ts_add_u32 m0, %[wl], 3072\n\ts_xor_b32 %[t], %[wo], 0xac000\n\tbuffer_load_dwordx4 %[vw1], %[rw], %[t] offen lds\n\t" \
          "s_add_u32 m0, %[wl], 4096\n\ts_xor_b32 %[t], %[wo], 0xb0000\n\tbuffer_load_dwordx4 %[vw0], %[rw], %[t] offen lds\n\ts_add_u32 m0, %[wl], 5120\n\ts_xor_b32 %[t], %[wo], 0xb4000\n\tbuffer_load_dwordx4 %[vw1], %[rw], %[t] offen lds\n\t" \
          "s_add_u32 m0, %[wl], 6144\n\ts_xor_b32 %[t], %[wo], 0xb8000\n\tbuffer_load_dwordx4 %[vw0], %[rw], %[t] offen lds\n\ts_add_u32 m0, %[wl], 7168\n\ts_xor_b32 %[t], %[wo], 0xbc000\n\tbuffer_load_dwordx4 %[vw1], %[rw], %[t] offen lds\n\t" \
          "s_waitcnt vmcnt(32)\n\tds_read_b64_tr_b16 a[16:17], %[rt0] offset:8192\n\tds_read_b64_tr_b16 a[18:19], %[rt0] offset:8704\n\tds_read_b64_tr_b16 a[20:21], %[rt1] offset:8192\n\tds_read_b64_tr_b16 a[22:23], %[rt1] offset:8704\n\tds_read_b64_tr_b16 v[136:137], %[rt2] offset:8192\n\t" \
          "ds_read_b64_tr_b16 v[138:139], %[rt2] offset:8704\n\tds_read_b64_tr_b16 v[40:41], %[rt3] offset:8192\n\tds_read_b64_tr_b16 v[42:43], %[rt3] offset:8704\n\tds_read_b64_tr_b16 a[24:25], %[rt0] offset:12288\n\tds_read_b64_tr_b16 a[26:27], %[rt0] offset:12800\n\tds_read_b64_tr_b16 a[28:29], %[rt1] offset:12288\n\t" \
          "ds_read_b64_tr_b16 a[30:31], %[rt1] offset:12800\n\tds_read_b64_tr_b16 v[140:141], %[rt2] offset:12288\n\tds_read_b64_tr_b16 v[142:143], %[rt2] offset:12800\n\tds_read_b64_tr_b16 v[44:45], %[rt3] offset:12288\n\tds_read_b64_tr_b16 v[46:47], %[rt3] offset:12800\n\ts_waitcnt lgkmcnt(0)\n\t" \
          "s_add_u32 m0, %[wl], 8192\n\ts_xor_b32 %[t], %[wo], 0xc0000\n\tbuffer_load_dwordx4 %[vw0], %[rw], %[t] offen lds\n\ts_add_u32 m0, %[wl], 9216\n\ts_xor_b32 %[t], %[wo], 0xc4000\n\tbuffer_load_dwordx4 %[vw1], %[rw], %[t] offen lds\n\t" \
          "s_add_u32 m0, %[wl], 10240\n\ts_xor_b32 %[t], %[wo], 0xc8000\n\tbuffer_load_dwordx4 %[vw0], %[rw], %[t] offen lds\n\ts_add_u32 m0, %[wl], 11264\n\ts_xor_b32 %[t], %[wo], 0xcc000\n\tbuffer_load_dwordx4 %[vw1], %[rw], %[t] offen lds\n\t" \
          "s_add_u32 m0, %[wl], 12288\n\ts_xor_b32 %[t], %[wo], 0xd0000\n\tbuffer_load_dwordx4 %[vw0], %[rw], %[t] offen lds\n\ts_add_u32 m0, %[wl], 13312\n\ts_xor_b32 %[t], %[wo], 0xd4000\n\tbuffer_load_dwordx4 %[vw1], %[rw], %[t] offen lds\n\t" \
          "s_add_u32 m0, %[wl], 14336\n\ts_xor_b32 %[t], %[wo], 0xd8000\n\tbuffer_load_dwordx4 %[vw0], %[rw], %[t] offen lds\n\ts_add_u32 m0, %[wl], 15360\n\ts_xor_b32 %[t], %[wo], 0xdc000\n\tbuffer_load_dwordx4 %[vw1], %[rw], %[t] offen lds\n\t" \
          "s_waitcnt vmcnt(32)\n\tds_read_b64_tr_b16 a[32:33], %[rt0] offset:16384\n\tds_read_b64_tr_b16 a[34:35], %[rt0] offset:16896\n\tds_read_b64_tr_b16 a[36:37], %[rt1] offset:16384\n\tds_read_b64_tr_b16 a[38:39], %[rt1] offset:16896\n\tds_read_b64_tr_b16 v[144:145], %[rt2] offset:16384\n\t" \
          "ds_read_b64_tr_b16 v[146:147], %[rt2] offset:16896\n\tds_read_b64_tr_b16 v[48:49], %[rt3] offset:16384\n\tds_read_b64_tr_b16 v[50:51], %[rt3] offset:16896\n\tds_read_b64_tr_b16 a[40:41], %[rt0] offset:20480\n\tds_read_b64_tr_b16 a[42:43], %[rt0] offset:20992\n\tds_read_b64_tr_b16 a[44:45], %[rt1] offset:20480\n\t" \
          "ds_read_b64_tr_b16 a[46:47], %[rt1] offset:20992\n\tds_read_b64_tr_b16 v[148:149], %[rt2] offset:20480\n\tds_read_b64_tr_b16 v[150:151], %[rt2] offset:20992\n\tds_read_b64_tr_b16 v[52:53], %[rt3] offset:20480\n\tds_read_b64_tr_b16 v[54:55], %[rt3] offset:20992\n\ts_waitcnt lgkmcnt(0)\n\t" \
          "s_add_u32 m0, %[wl], 16384\n\ts_xor_b32 %[t], %[wo], 0xe0000\n\tbuffer_load_dwordx4 %[vw0], %[rw], %[t] offen lds\n\ts_add_u32 m0, %[wl], 17408\n\ts_xor_b32 %[t], %[wo], 0xe4000\n\tbuffer_load_dwordx4 %[vw1], %[rw], %[t] offen lds\n\t" \
          "s_add_u32 m0, %[wl], 18432\n\ts_xor_b32 %[t], %[wo], 0xe8000\n\tbuffer_load_dwordx4 %[vw0], %[rw], %[t] offen lds\n\ts_add_u32 m0, %[wl], 19456\n\ts_xor_b32 %[t], %[wo], 0xec000\n\tbuffer_load_dwordx4 %[vw1], %[rw], %[t] offen lds\n\t" \
          "s_add_u32 m0, %[wl], 20480\n\ts_xor_b32 %[t], %[wo], 0xf0000\n\tbuffer_load_dwordx4 %[vw0], %[rw], %[t] offen lds\n\ts_add_u32 m0, %[wl], 21504\n\ts_xor_b32 %[t], %[wo], 0xf4000\n\tbuffer_load_dwordx4 %[vw1], %[rw], %[t] offen lds\n\t" \
          "s_add_u32 m0, %[wl], 22528\n\ts_xor_b32 %[t], %[wo], 0xf8000\n\tbuffer_load_dwordx4 %[vw0], %[rw], %[t] offen lds\n\ts_add_u32 m0, %[wl], 23552\n\ts_xor_b32 %[t], %[wo], 0xfc000\n\tbuffer_load_dwordx4 %[vw1], %[rw], %[t] offen lds\n\t" \
          "s_waitcnt vmcnt(32)\n\tds_read_b64_tr_b16 a[48:49], %[rt0] offset:24576\n\tds_read_b64_tr_b16 a[50:51], %[rt0] offset:25088\n\tds_read_b64_tr_b16 a[52:53], %[rt1] offset:24576\n\tds_read_b64_tr_b16 a[54:55], %[rt1] offset:25088\n\tds_read_b64_tr_b16 v[152:153], %[rt2] offset:24576\n\t" \
          "ds_read_b64_tr_b16 v[154:155], %[rt2] offset:25088\n\tds_read_b64_tr_b16 v[56:57], %[rt3] offset:24576\n\tds_read_b64_tr_b16 v[58:59], %[rt3] offset:25088\n\tds_read_b64_tr_b16 a[56:57], %[rt0] offset:28672\n\tds_read_b64_tr_b16 a[58:59], %[rt0] offset:29184\n\tds_read_b64_tr_b16 a[60:61], %[rt1] offset:28672\n\t" \
          "ds_read_b64_tr_b16 a[62:63], %[rt1] offset:29184\n\tds_read_b64_tr_b16 v[156:157], %[rt2] offset:28672\n\tds_read_b64_tr_b16 v[158:159], %[rt2] offset:29184\n\tds_read_b64_tr_b16 v[60:61], %[rt3] offset:28672\n\tds_read_b64_tr_b16 v[62:63], %[rt3] offset:29184\n\ts_waitcnt lgkmcnt(0)\n\t" \
          "s_add_u32 m0, %[wl], 24576\n\ts_xor_b32 %[t], %[wo], 0x100000\n\tbuffer_load_dwordx4 %[vw0], %[rw], %[t] offen lds\n\ts_add_u32 m0, %[wl], 25600\n\ts_xor_b32 %[t], %[wo], 0x104000\n\tbuffer_load_dwordx4 %[vw1], %[rw], %[t] offen lds\n\t" \
          "s_add_u32 m0, %[wl], 26624\n\ts_xor_b32 %[t], %[wo], 0x108000\n\tbuffer_load_dwordx4 %[vw0], %[rw], %[t] offen lds\n\ts_add_u32 m0, %[wl], 27648\n\ts_xor_b32 %[t], %[wo], 0x10c000\n\tbuffer_load_dwordx4 %[vw1], %[rw], %[t] offen lds\n\t" \
          "s_add_u32 m0, %[wl], 28672\n\ts_xor_b32 %[t], %[wo], 0x110000\n\tbuffer_load_dwordx4 %[vw0], %[rw], %[t] offen lds\n\ts_add_u32 m0, %[wl], 29696\n\ts_xor_b32 %[t], %[wo], 0x114000\n\tbuffer_load_dwordx4 %[vw1], %[rw], %[t] offen lds\n\t" \
          "s_add_u32 m0, %[wl], 30720\n\ts_xor_b32 %[t], %[wo], 0x118000\n\tbuffer_load_dwordx4 %[vw0], %[rw], %[t] offen lds\n\ts_add_u32 m0, %[wl], 31744\n\ts_xor_b32 %[t], %[wo], 0x11c000\n\tbuffer_load_dwordx4 %[vw1], %[rw], %[t] offen lds\n\t" \
          "s_waitcnt vmcnt(32)\n\tds_read_b64_tr_b16 a[64:65], %[rt0] offset:32768\n\tds_read_b64_tr_b16 a[66:67], %[rt0] offset:33280\n\tds_read_b64_tr_b16 a[68:69], %[rt1] offset:32768\n\tds_read_b64_tr_b16 a[70:71], %[rt1] offset:33280\n\tds_read_b64_tr_b16 v[160:161], %[rt2] offset:32768\n\t" \
          "ds_read_b64_tr_b16 v[162:163], %[rt2] offset:33280\n\tds_read_b64_tr_b16 v[64:65], %[rt3] offset:32768\n\tds_read_b64_tr_b16 v[66:67], %[rt3] offset:33280\n\tds_read_b64_tr_b16 a[72:73], %[rt0] offset:36864\n\tds_read_b64_tr_b16 a[74:75], %[rt0] offset:37376\n\tds_read_b64_tr_b16 a[76:77], %[rt1] offset:36864\n\t" \
          "ds_read_b64_tr_b16 a[78:79], %[rt1] offset:37376\n\tds_read_b64_tr_b16 v[164:165], %[rt2] offset:36864\n\tds_read_b64_tr_b16 v[166:167], %[rt2] offset:37376\n\tds_read_b64_tr_b16 v[68:69], %[rt3] offset:36864\n\tds_read_b64_tr_b16 v[70:71], %[rt3] offset:37376\n\ts_waitcnt lgkmcnt(0)\n\t" \
          "s_add_u32 m0, %[wl], 32768\n\ts_xor_b32 %[t], %[wo], 0x120000\n\tbuffer_load_dwordx4 %[vw0], %[rw], %[t] offen lds\n\ts_add_u32 m0, %[wl], 33792\n\ts_xor_b32 %[t], %[wo], 0x124000\n\tbuffer_load_dwordx4 %[vw1], %[rw], %[t] offen lds\n\t" \
          "s_add_u32 m0, %[wl], 34816\n\ts_xor_b32 %[t], %[wo], 0x128000\n\tbuffer_load_dwordx4 %[vw0], %[rw], %[t] offen lds\n\ts_add_u32 m0, %[wl], 35840\n\ts_xor_b32 %[t], %[wo], 0x12c000\n\tbuffer_load_dwordx4 %[vw1], %[rw], %[t] offen lds\n\t" \
          "s_add_u32 m0, %[wl], 36864\n\ts_xor_b32 %[t], %[wo], 0x130000\n\tbuffer_load_dwordx4 %[vw0], %[rw], %[t] offen lds\n\ts_add_u32 m0, %[wl], 37888\n\ts_xor_b32 %[t], %[wo], 0x134000\n\tbuffer_load_dwordx4 %[vw1], %[rw], %[t] offen lds\n\t" \
          "s_add_u32 m0, %[wl], 38912\n\ts_xor_b32 %[t], %[wo], 0x138000\n\tbuffer_load_dwordx4 %[vw0], %[rw], %[t] offen lds\n\ts_add_u32 m0, %[wl], 39936\n\ts_xor_b32 %[t], %[wo], 0x13c000\n\tbuffer_load_dwordx4 %[vw1], %[rw], %[t] offen lds\n\t" \
          "s_waitcnt vmcnt(32)\n\tds_read_b64_tr_b16 a[80:81], %[rt0] offset:0\n\tds_read_b64_tr_b16 a[82:83], %[rt0] offset:512\n\tds_read_b64_tr_b16 a[84:85], %[rt1] offset:0\n\tds_read_b64_tr_b16 a[86:87], %[rt1] offset:512\n\tds_read_b64_tr_b16 v[168:169], %[rt2] offset:0\n\t" \
          "ds_read_b64_tr_b16 v[170:171], %[rt2] offset:512\n\tds_read_b64_tr_b16 v[72:73], %[rt3] offset:0\n\tds_read_b64_tr_b16 v[74:75], %[rt3] offset:512\n\tds_read_b64_tr_b16 a[88:89], %[rt0] offset:4096\n\tds_read_b64_tr_b16 a[90:91], %[rt0] offset:4608\n\tds_read_b64_tr_b16 a[92:93], %[rt1] offset:4096\n\t" \
          "ds_read_b64_tr_b16 a[94:95], %[rt1] offset:4608\n\tds_read_b64_tr_b16 v[172:173], %[rt2] offset:4096\n\tds_read_b64_tr_b16 v[174:175], %[rt2] offset:4608\n\tds_read_b64_tr_b16 v[76:77], %[rt3] offset:4096\n\tds_read_b64_tr_b16 v[78:79], %[rt3] offset:4608\n\ts_waitcnt lgkmcnt(0)\n\t" \
          "s_add_u32 m0, %[wl], 0\n\ts_xor_b32 %[t], %[wo], 0x140000\n\tbuffer_load_dwordx4 %[vw0], %[rw], %[t] offen lds\n\ts_add_u32 m0, %[wl], 1024\n\ts_xor_b32 %[t], %[wo], 0x144000\n\tbuffer_load_dwordx4 %[vw1], %[rw], %[t] offen lds\n\t" \
          "s_add_u32 m0, %[wl], 2048\n\ts_xor_b32 %[t], %[wo], 0x148000\n\tbuffer_load_dwordx4 %[vw0], %[rw], %[t] offen lds\n\ts_add_u32 m0, %[wl], 3072\n\ts_xor_b32 %[t], %[wo], 0x14c000\n\tbuffer_load_dwordx4 %[vw1], %[rw], %[t] offen lds\n\t" \
          "s_add_u32 m0, %[wl], 4096\n\ts_xor_b32 %[t], %[wo], 0x150000\n\tbuffer_load_dwordx4 %[vw0], %[rw], %[t] offen lds\n\ts_add_u32 m0, %[wl], 5120\n\ts_xor_b32 %[t], %[wo], 0x154000\n\tbuffer_load_dwordx4 %[vw1], %[rw], %[t] offen lds\n\t" \
          "s_add_u32 m0, %[wl], 6144\n\ts_xor_b32 %[t], %[wo], 0x158000\n\tbuffer_load_dwordx4 %[vw0], %[rw], %[t] offen lds\n\ts_add_u32 m0, %[wl], 7168\n\ts_xor_b32 %[t], %[wo], 0x15c000\n\tbuffer_load_dwordx4 %[vw1], %[rw], %[t] offen lds\n\t" \
          "s_waitcnt vmcnt(32)\n\tds_read_b64_tr_b16 a[96:97], %[rt0] offset:8192\n\tds_read_b64_tr_b16 a[98:99], %[rt0] offset:8704\n\tds_read_b64_tr_b16 a[100:101], %[rt1] offset:8192\n\tds_read_b64_tr_b16 a[102:103], %[rt1] offset:8704\n\tds_read_b64_tr_b16 v[176:177], %[rt2] offset:8192\n\t" \
          "ds_read_b64_tr_b16 v[178:179], %[rt2] offset:8704\n\tds_read_b64_tr_b16 v[80:81], %[rt3] offset:8192\n\tds_read_b64_tr_b16 v[82:83], %[rt3] offset:8704\n\tds_read_b64_tr_b16 a[104:105], %[rt0] offset:12288\n\tds_read_b64_tr_b16 a[106:107], %[rt0] offset:12800\n\tds_read_b64_tr_b16 a[108:109], %[rt1] offset:12288\n\t" \
          "ds_read_b64_tr_b16 a[110:111], %[rt1] offset:12800\n\tds_read_b64_tr_b16 v[180:181], %[rt2] offset:12288\n\tds_read_b64_tr_b16 v[182:183], %[rt2] offset:12800\n\tds_read_b64_tr_b16 v[84:85], %[rt3] offset:12288\n\tds_read_b64_tr_b16 v[86:87], %[rt3] offset:12800\n\ts_waitcnt lgkmcnt(0)\n\t" \
          "s_add_u32 m0, %[wl], 8192\n\ts_xor_b32 %[t], %[wo], 0x160000\n\tbuffer_load_dwordx4 %[vw0], %[rw], %[t] offen lds\n\ts_add_u32 m0, %[wl], 9216\n\ts_xor_b32 %[t], %[wo], 0x164000\n\tbuffer_load_dwordx4 %[vw1], %[rw], %[t] offen lds\n\t" \
          "s_add_u32 m0, %[wl], 10240\n\ts_xor_b32 %[t], %[wo], 0x168000\n\tbuffer_load_dwordx4 %[vw0], %[rw], %[t] offen lds\n\ts_add_u32 m0, %[wl], 11264\n\ts_xor_b32 %[t], %[wo], 0x16c000\n\tbuffer_load_dwordx4 %[vw1], %[rw], %[t] offen lds\n\t" \
          "s_add_u32 m0, %[wl], 12288\n\ts_xor_b32 %[t], %[wo], 0x170000\n\tbuffer_load_dwordx4 %[vw0], %[rw], %[t] offen lds\n\ts_add_u32 m0, %[wl], 13312\n\ts_xor_b32 %[t], %[wo], 0x174000\n\tbuffer_load_dwordx4 %[vw1], %[rw], %[t] offen lds\n\t" \
          "s_add_u32 m0, %[wl], 14336\n\ts_xor_b32 %[t], %[wo], 0x178000\n\tbuffer_load_dwordx4 %[vw0], %[rw], %[t] offen lds\n\ts_add_u32 m0, %[wl], 15360\n\ts_xor_b32 %[t], %[wo], 0x17c000\n\tbuffer_load_dwordx4 %[vw1], %[rw], %[t] offen lds\n\t" \
          "s_waitcnt vmcnt(32)\n\tds_read_b64_tr_b16 a[112:113], %[rt0] offset:16384\n\tds_read_b64_tr_b16 a[114:115], %[rt0] offset:16896\n\tds_read_b64_tr_b16 a[116:117], %[rt1] offset:16384\n\tds_read_b64_tr_b16 a[118:119], %[rt1] offset:16896\n\tds_read_b64_tr_b16 v[184:185], %[rt2] offset:16384\n\t" \
          "ds_read_b64_tr_b16 v[186:187], %[rt2] offset:16896\n\tds_read_b64_tr_b16 v[88:89], %[rt3] offset:16384\n\tds_read_b64_tr_b16 v[90:91], %[rt3] offset:16896\n\tds_read_b64_tr_b16 a[120:121], %[rt0] offset:20480\n\tds_read_b64_tr_b16 a[122:123], %[rt0] offset:20992\n\tds_read_b64_tr_b16 a[124:125], %[rt1] offset:20480\n\t" \
          "ds_read_b64_tr_b16 a[126:127], %[rt1] offset:20992\n\tds_read_b64_tr_b16 v[188:189], %[rt2] offset:20480\n\tds_read_b64_tr_b16 v[190:191], %[rt2] offset:20992\n\tds_read_b64_tr_b16 v[92:93], %[rt3] offset:20480\n\tds_read_b64_tr_b16 v[94:95], %[rt3] offset:20992\n\ts_waitcnt lgkmcnt(0)\n\t" \
          "s_add_u32 m0, %[wl], 16384\n\ts_xor_b32 %[t], %[wo], 0x180000\n\tbuffer_load_dwordx4 %[vw0], %[rw], %[t] offen lds\n\ts_add_u32 m0, %[wl], 17408\n\ts_xor_b32 %[t], %[wo], 0x184000\n\tbuffer_load_dwordx4 %[vw1], %[rw], %[t] offen lds\n\t" \
          "s_add_u32 m0, %[wl], 18432\n\ts_xor_b32 %[t], %[wo], 0x188000\n\tbuffer_load_dwordx4 %[vw0], %[rw], %[t] offen lds\n\ts_add_u32 m0, %[wl], 19456\n\ts_xor_b32 %[t], %[wo], 0x18c000\n\tbuffer_load_dwordx4 %[vw1], %[rw], %[t] offen lds\n\t" \
          "s_add_u32 m0, %[wl], 20480\n\ts_xor_b32 %[t], %[wo], 0x190000\n\tbuffer_load_dwordx4 %[vw0], %[rw], %[t] offen lds\n\ts_add_u32 m0, %[wl], 21504\n\ts_xor_b32 %[t], %[wo], 0x194000\n\tbuffer_load_dwordx4 %[vw1], %[rw], %[t] offen lds\n\t" \
          "s_add_u32 m0, %[wl], 22528\n\ts_xor_b32 %[t], %[wo], 0x198000\n\tbuffer_load_dwordx4 %[vw0], %[rw], %[t] offen lds\n\ts_add_u32 m0, %[wl], 23552\n\ts_xor_b32 %[t], %[wo], 0x19c000\n\tbuffer_load_dwordx4 %[vw1], %[rw], %[t] offen lds\n\t" \
          "s_waitcnt vmcnt(32)\n\tds_read_b64_tr_b16 a[128:129], %[rt0] offset:24576\n\tds_read_b64_tr_b16 a[130:131], %[rt0] offset:25088\n\tds_read_b64_tr_b16 a[132:133], %[rt1] offset:24576\n\tds_read_b64_tr_b16 a[134:135], %[rt1] offset:25088\n\tds_read_b64_tr_b16 v[192:193], %[rt2] offset:24576\n\t" \
          "ds_read_b64_tr_b16 v[194:195], %[rt2] offset:25088\n\tds_read_b64_tr_b16 v[96:97], %[rt3] offset:24576\n\tds_read_b64_tr_b16 v[98:99], %[rt3] offset:25088\n\tds_read_b64_tr_b16 a[136:137], %[rt0] offset:28672\n\tds_read_b64_tr_b16 a[138:139], %[rt0] offset:29184\n\tds_read_b64_tr_b16 a[140:141], %[rt1] offset:28672\n\t" \
          "ds_read_b64_tr_b16 a[142:143], %[rt1] offset:29184\n\tds_read_b64_tr_b16 v[196:197], %[rt2] offset:28672\n\tds_read_b64_tr_b16 v[198:199], %[rt2] offset:29184\n\tds_read_b64_tr_b16 v[100:101], %[rt3] offset:28672\n\tds_read_b64_tr_b16 v[102:103], %[rt3] offset:29184\n\ts_waitcnt lgkmcnt(0)\n\t" \
          "s_add_u32 m0, %[wl], 24576\n\ts_xor_b32 %[t], %[wo], 0x1a0000\n\tbuffer_load_dwordx4 %[vw0], %[rw], %[t] offen lds\n\ts_add_u32 m0, %[wl], 25600\n\ts_xor_b32 %[t], %[wo], 0x1a4000\n\tbuffer_load_dwordx4 %[vw1], %[rw], %[t] offen lds\n\t" \
          "s_add_u32 m0, %[wl], 26624\n\ts_xor_b32 %[t], %[wo], 0x1a8000\n\tbuffer_load_dwordx4 %[vw0], %[rw], %[t] offen lds\n\ts_add_u32 m0, %[wl], 27648\n\ts_xor_b32 %[t], %[wo], 0x1ac000\n\tbuffer_load_dwordx4 %[vw1], %[rw], %[t] offen lds\n\t" \
          "s_add_u32 m0, %[wl], 28672\n\ts_xor_b32 %[t], %[wo], 0x1b0000\n\tbuffer_load_dwordx4 %[vw0], %[rw], %[t] offen lds\n\ts_add_u32 m0, %[wl], 29696\n\ts_xor_b32 %[t], %[wo], 0x1b4000\n\tbuffer_load_dwordx4 %[vw1], %[rw], %[t] offen lds\n\t" \
          "s_add_u32 m0, %[wl], 30720\n\ts_xor_b32 %[t], %[wo], 0x1b8000\n\tbuffer_load_dwordx4 %[vw0], %[rw], %[t] offen lds\n\ts_add_u32 m0, %[wl], 31744\n\ts_xor_b32 %[t], %[wo], 0x1bc000\n\tbuffer_load_dwordx4 %[vw1], %[rw], %[t] offen lds\n\t" \
          "s_waitcnt vmcnt(32)\n\tds_read_b64_tr_b16 a[144:145], %[rt0] offset:32768\n\tds_read_b64_tr_b16 a[146:147], %[rt0] offset:33280\n\tds_read_b64_tr_b16 a[148:149], %[rt1] offset:32768\n\tds_read_b64_tr_b16 a[150:151], %[rt1] offset:33280\n\tds_read_b64_tr_b16 v[200:201], %[rt2] offset:32768\n\t" \
          "ds_read_b64_tr_b16 v[202:203], %[rt2] offset:33280\n\tds_read_b64_tr_b16 v[104:105], %[rt3] offset:32768\n\tds_read_b64_tr_b16 v[106:107], %[rt3] offset:33280\n\tds_read_b64_tr_b16 a[152:153], %[rt0] offset:36864\n\tds_read_b64_tr_b16 a[154:155], %[rt0] offset:37376\n\tds_read_b64_tr_b16 a[156:157], %[rt1] offset:36864\n\t" \
          "ds_read_b64_tr_b16 a[158:159], %[rt1] offset:37376\n\tds_read_b64_tr_b16 v[204:205], %[rt2] offset:36864\n\tds_read_b64_tr_b16 v[206:207], %[rt2] offset:37376\n\tds_read_b64_tr_b16 v[108:109], %[rt3] offset:36864\n\tds_read_b64_tr_b16 v[110:111], %[rt3] offset:37376\n\ts_waitcnt lgkmcnt(0)\n\t" \
          "s_add_u32 m0, %[wl], 32768\n\ts_xor_b32 %[t], %[wo], 0x1c0000\n\tbuffer_load_dwordx4 %[vw0], %[rw], %[t] offen lds\n\ts_add_u32 m0, %[wl], 33792\n\ts_xor_b32 %[t], %[wo], 0x1c4000\n\tbuffer_load_dwordx4 %[vw1], %[rw], %[t] offen lds\n\t" \
          "s_add_u32 m0, %[wl], 34816\n\ts_xor_b32 %[t], %[wo], 0x1c8000\n\tbuffer_load_dwordx4 %[vw0], %[rw], %[t] offen lds\n\ts_add_u32 m0, %[wl], 35840\n\ts_xor_b32 %[t], %[wo], 0x1cc000\n\tbuffer_load_dwordx4 %[vw1], %[rw], %[t] offen lds\n\t" \
          "s_add_u32 m0, %[wl], 36864\n\ts_xor_b32 %[t], %[wo], 0x1d0000\n\tbuffer_load_dwordx4 %[vw0], %[rw], %[t] offen lds\n\ts_add_u32 m0, %[wl], 37888\n\ts_xor_b32 %[t], %[wo], 0x1d4000\n\tbuffer_load_dwordx4 %[vw1], %[rw], %[t] offen lds\n\t" \
          "s_add_u32 m0, %[wl], 38912\n\ts_xor_b32 %[t], %[wo], 0x1d8000\n\tbuffer_load_dwordx4 %[vw0], %[rw], %[t] offen lds\n\ts_add_u32 m0, %[wl], 39936\n\ts_xor_b32 %[t], %[wo], 0x1dc000\n\tbuffer_load_dwordx4 %[vw1], %[rw], %[t] offen lds\n\t" \
          "s_waitcnt vmcnt(32)\n\tds_read_b64_tr_b16 a[160:161], %[rt0] offset:0\n\tds_read_b64_tr_b16 a[162:163], %[rt0] offset:512\n\tds_read_b64_tr_b16 a[164:165], %[rt1] offset:0\n\tds_read_b64_tr_b16 a[166:167], %[rt1] offset:512\n\tds_read_b64_tr_b16 v[208:209], %[rt2] offset:0\n\t" \
          "ds_read_b64_tr_b16 v[210:211], %[rt2] offset:512\n\tds_read_b64_tr_b16 v[112:113], %[rt3] offset:0\n\tds_read_b64_tr_b16 v[114:115], %[rt3] offset:512\n\tds_read_b64_tr_b16 a[168:169], %[rt0] offset:4096\n\tds_read_b64_tr_b16 a[170:171], %[rt0] offset:4608\n\tds_read_b64_tr_b16 a[172:173], %[rt1] offset:4096\n\t" \
          "ds_read_b64_tr_b16 a[174:175], %[rt1] offset:4608\n\tds_read_b64_tr_b16 v[212:213], %[rt2] offset:4096\n\tds_read_b64_tr_b16 v[214:215], %[rt2] offset:4608\n\tds_read_b64_tr_b16 v[116:117], %[rt3] offset:4096\n\tds_read_b64_tr_b16 v[118:119], %[rt3] offset:4608\n\ts_waitcnt lgkmcnt(0)\n\t" \
          "s_add_u32 m0, %[wl], 0\n\ts_xor_b32 %[t], %[wo], 0x1e0000\n\tbuffer_load_dwordx4 %[vw0], %[rw], %[t] offen lds\n\ts_add_u32 m0, %[wl], 1024\n\ts_xor_b32 %[t], %[wo], 0x1e4000\n\tbuffer_load_dwordx4 %[vw1], %[rw], %[t] offen lds\n\t" \
          "s_add_u32 m0, %[wl], 2048\n\ts_xor_b32 %[t], %[wo], 0x1e8000\n\tbuffer_load_dwordx4 %[vw0], %[rw], %[t] offen lds\n\ts_add_u32 m0, %[wl], 3072\n\ts_xor_b32 %[t], %[wo], 0x1ec000\n\tbuffer_load_dwordx4 %[vw1], %[rw], %[t] offen lds\n\t" \
          "s_add_u32 m0, %[wl], 4096\n\ts_xor_b32 %[t], %[wo], 0x1f0000\n\tbuffer_load_dwordx4 %[vw0], %[rw], %[t] offen lds\n\ts_add_u32 m0, %[wl], 5120\n\ts_xor_b32 %[t], %[wo], 0x1f4000\n\tbuffer_load_dwordx4 %[vw1], %[rw], %[t] offen lds\n\t" \
          "s_add_u32 m0, %[wl], 6144\n\ts_xor_b32 %[t], %[wo], 0x1f8000\n\tbuffer_load_dwordx4 %[vw0], %[rw], %[t] offen lds\n\ts_add_u32 m0, %[wl], 7168\n\ts_xor_b32 %[t], %[wo], 0x1fc000\n\tbuffer_load_dwordx4 %[vw1], %[rw], %[t] offen lds\n\t" \
          "s_waitcnt vmcnt(32)\n\tds_read_b64_tr_b16 a[176:177], %[rt0] offset:8192\n\tds_read_b64_tr_b16 a[178:179], %[rt0] offset:8704\n\tds_read_b64_tr_b16 a[180:181], %[rt1] offset:8192\n\tds_read_b64_tr_b16 a[182:183], %[rt1] offset:8704\n\tds_read_b64_tr_b16 v[216:217], %[rt2] offset:8192\n\t" \
          "ds_read_b64_tr_b16 v[218:219], %[rt2] offset:8704\n\tds_read_b64_tr_b16 v[120:121], %[rt3] offset:8192\n\tds_read_b64_tr_b16 v[122:123], %[rt3] offset:8704\n\tds_read_b64_tr_b16 a[184:185], %[rt0] offset:12288\n\tds_read_b64_tr_b16 a[186:187], %[rt0] offset:12800\n\tds_read_b64_tr_b16 a[188:189], %[rt1] offset:12288\n\t" \
          "ds_read_b64_tr_b16 a[190:191], %[rt1] offset:12800\n\tds_read_b64_tr_b16 v[220:221], %[rt2] offset:12288\n\tds_read_b64_tr_b16 v[222:223], %[rt2] offset:12800\n\tds_read_b64_tr_b16 v[124:125], %[rt3] offset:12288\n\tds_read_b64_tr_b16 v[126:127], %[rt3] offset:12800\n\ts_waitcnt lgkmcnt(0)\n\t" \
          "ds_write_b128 %[own], v[32:35] offset:8192\n\tds_write_b128 %[own], v[36:39] offset:9216\n\tds_write_b128 %[own], v[40:43] offset:10240\n\tds_write_b128 %[own], v[44:47] offset:11264\n\tds_write_b128 %[own], v[48:51] offset:12288\n\tds_write_b128 %[own], v[52:55] offset:13312\n\t" \
          "ds_write_b128 %[own], v[56:59] offset:14336\n\tds_write_b128 %[own], v[60:63] offset:15360\n\ts_waitcnt lgkmcnt(0)\n\ts_waitcnt vmcnt(24)\n\tds_read_b64_tr_b16 a[192:193], %[rt0] offset:16384\n\tds_read_b64_tr_b16 a[194:195], %[rt0] offset:16896\n\t" \
          "ds_read_b64_tr_b16 a[196:197], %[rt1] offset:16384\n\tds_read_b64_tr_b16 a[198:199], %[rt1] offset:16896\n\tds_read_b64_tr_b16 v[224:225], %[rt2] offset:16384\n\tds_read_b64_tr_b16 v[226:227], %[rt2] offset:16896\n\tds_read_b64_tr_b16 v[32:33], %[rt3] offset:16384\n\tds_read_b64_tr_b16 v[34:35], %[rt3] offset:16896\n\t" \
          "ds_read_b64_tr_b16 a[200:201], %[rt0] offset:20480\n\tds_read_b64_tr_b16 a[202:203], %[rt0] offset:20992\n\tds_read_b64_tr_b16 a[204:205], %[rt1] offset:20480\n\tds_read_b64_tr_b16 a[206:207], %[rt1] offset:20992\n\tds_read_b64_tr_b16 v[228:229], %[rt2] offset:20480\n\tds_read_b64_tr_b16 v[230:231], %[rt2] offset:20992\n\t" \
          "ds_read_b64_tr_b16 v[36:37], %[rt3] offset:20480\n\tds_read_b64_tr_b16 v[38:39], %[rt3] offset:20992\n\ts_waitcnt lgkmcnt(0)\n\tds_write_b128 %[own], v[64:67] offset:16384\n\tds_write_b128 %[own], v[68:71] offset:17408\n\tds_write_b128 %[own], v[72:75] offset:18432\n\t" \
          "ds_write_b128 %[own], v[76:79] offset:19456\n\tds_write_b128 %[own], v[80:83] offset:20480\n\tds_write_b128 %[own], v[84:87] offset:21504\n\tds_write_b128 %[own], v[88:91] offset:22528\n\tds_write_b128 %[own], v[92:95] offset:23552\n\ts_waitcnt lgkmcnt(0)\n\t" \
          "s_waitcnt vmcnt(16)\n\tds_read_b64_tr_b16 a[208:209], %[rt0] offset:24576\n\tds_read_b64_tr_b16 a[210:211], %[rt0] offset:25088\n\tds_read_b64_tr_b16 a[212:213], %[rt1] offset:24576\n\tds_read_b64_tr_b16 a[214:215], %[rt1] offset:25088\n\tds_read_b64_tr_b16 v[232:233], %[rt2] offset:24576\n\t" \
          "ds_read_b64_tr_b16 v[234:235], %[rt2] offset:25088\n\tds_read_b64_tr_b16 v[40:41], %[rt3] offset:24576\n\tds_read_b64_tr_b16 v[42:43], %[rt3] offset:25088\n\tds_read_b64_tr_b16 a[216:217], %[rt0] offset:28672\n\tds_read_b64_tr_b16 a[218:219], %[rt0] offset:29184\n\tds_read_b64_tr_b16 a[220:221], %[rt1] offset:28672\n\t" \
          "ds_read_b64_tr_b16 a[222:223], %[rt1] offset:29184\n\tds_read_b64_tr_b16 v[236:237], %[rt2] offset:28672\n\tds_read_b64_tr_b16 v[238:239], %[rt2] offset:29184\n\tds_read_b64_tr_b16 v[44:45], %[rt3] offset:28672\n\tds_read_b64_tr_b16 v[46:47], %[rt3] offset:29184\n\ts_waitcnt lgkmcnt(0)\n\t" \
          "ds_write_b128 %[own], v[96:99] offset:24576\n\tds_write_b128 %[own], v[100:103] offset:25600\n\tds_write_b128 %[own], v[104:107] offset:26624\n\tds_write_b128 %[own], v[108:111] offset:27648\n\tds_write_b128 %[own], v[112:115] offset:28672\n\tds_write_b128 %[own], v[116:119] offset:29696\n\t" \
          "ds_write_b128 %[own], v[120:123] offset:30720\n\tds_write_b128 %[own], v[124:127] offset:31744\n\ts_waitcnt lgkmcnt(0)\n\ts_waitcnt vmcnt(8)\n\tds_read_b64_tr_b16 a[224:225], %[rt0] offset:32768\n\tds_read_b64_tr_b16 a[226:227], %[rt0] offset:33280\n\t" \
          "ds_read_b64_tr_b16 a[228:229], %[rt1] offset:32768\n\tds_read_b64_tr_b16 a[230:231], %[rt1] offset:33280\n\tds_read_b64_tr_b16 v[240:241], %[rt2] offset:32768\n\tds_read_b64_tr_b16 v[242:243], %[rt2] offset:33280\n\tds_read_b64_tr_b16 v[48:49], %[rt3] offset:32768\n\tds_read_b64_tr_b16 v[50:51], %[rt3] offset:33280\n\t" \
          "ds_read_b64_tr_b16 a[232:233], %[rt0] offset:36864\n\tds_read_b64_tr_b16 a[234:235], %[rt0] offset:37376\n\tds_read_b64_tr_b16 a[236:237], %[rt1] offset:36864\n\tds_read_b64_tr_b16 a[238:239], %[rt1] offset:37376\n\tds_read_b64_tr_b16 v[244:245], %[rt2] offset:36864\n\tds_read_b64_tr_b16 v[246:247], %[rt2] offset:37376\n\t" \
          "ds_read_b64_tr_b16 v[52:53], %[rt3] offset:36864\n\tds_read_b64_tr_b16 v[54:55], %[rt3] offset:37376\n\ts_waitcnt lgkmcnt(0)\n\ts_waitcnt vmcnt(0)\n\tds_read_b64_tr_b16 a[240:241], %[rt0] offset:0\n\tds_read_b64_tr_b16 a[242:243], %[rt0] offset:512\n\t" \
          "ds_read_b64_tr_b16 a[244:245], %[rt1] offset:0\n\tds_read_b64_tr_b16 a[246:247], %[rt1] offset:512\n\tds_read_b64_tr_b16 v[248:249], %[rt2] offset:0\n\tds_read_b64_tr_b16 v[250:251], %[rt2] offset:512\n\tds_read_b64_tr_b16 v[56:57], %[rt3] offset:0\n\tds_read_b64_tr_b16 v[58:59], %[rt3] offset:512\n\t" \
          "ds_read_b64_tr_b16 a[248:249], %[rt0] offset:4096\n\tds_read_b64_tr_b16 a[250:251], %[rt0] offset:4608\n\tds_read_b64_tr_b16 a[252:253], %[rt1] offset:4096\n\tds_read_b64_tr_b16 a[254:255], %[rt1] offset:4608\n\tds_read_b64_tr_b16 v[252:253], %[rt2] offset:4096\n\tds_read_b64_tr_b16 v[254:255], %[rt2] offset:4608\n\t" \
          "ds_read_b64_tr_b16 v[60:61], %[rt3] offset:4096\n\tds_read_b64_tr_b16 v[62:63], %[rt3] offset:4608\n\ts_waitcnt lgkmcnt(0)\n\tds_write_b128 %[own], v[32:35] offset:0\n\tds_write_b128 %[own], v[36:39] offset:1024\n\tds_write_b128 %[own], v[40:43] offset:2048\n\t" \
          "ds_write_b128 %[own], v[44:47] offset:3072\n\tds_write_b128 %[own], v[48:51] offset:4096\n\tds_write_b128 %[own], v[52:55] offset:5120\n\tds_write_b128 %[own], v[56:59] offset:6144\n\tds_write_b128 %[own], v[60:63] offset:7168\n\ts_waitcnt lgkmcnt(0)\n\t" \
          "Lou_wait%=:\n\ts_mov_b32 %[cnt], 0\n\tLou_poll%=:\n\ts_lshr_b32 %[t], %[j], 1\n\ts_cmp_lt_u32 %[t], %[av]\n\ts_cbranch_scc1 Lou_load%=\n\t" \
          "s_sleep 4\n\ts_lshl_b32 %[t], %[av], 7\n\ts_add_u32 %[t], %[t], %[fo]\n\tbuffer_load_dword %[fl], %[vf], %[rf], %[t] offen sc1\n\tbuffer_load_dword v48, off, %[rf], %[tmo] sc1\n\ts_waitcnt vmcnt(0)\n\t" \
          "v_cmp_gt_u32 vcc, 8, %[fl]\n\ts_nop 0\n\ts_ff1_i32_b64 %[t], vcc\n\ts_add_u32 %[av], %[av], %[t]\n\tv_readfirstlane_b32 vcc_lo, v48\n\ts_add_u32 %[cnt], %[cnt], 1\n\t" \
          "s_lshr_b32 vcc_hi, %[cnt], 15\n\ts_or_b32 vcc_lo, vcc_lo, vcc_hi\n\ts_cbranch_scc0 Lou_poll%=\n\tv_mov_b32 v48, 7\n\tbuffer_store_dword v48, off, %[rf], %[tmo] sc1\n\ts_mov_b32 %[av], 64\n\t" \
          "Lou_load%=:\n\tbuffer_load_dwordx4 v[64:67], %[vo0], s[40:43], %[pg0] offen sc1\n\tbuffer_load_dwordx4 v[68:71], %[vo0], s[40:43], %[pg0] offen offset:1024 sc1\n\tbuffer_load_dwordx4 v[72:75], %[vo0], s[40:43], %[pg0] offen offset:2048 sc1\n\tbuffer_load_dwordx4 v[76:79], %[vo0], s[40:43], %[pg0] offen offset:3072 sc1\n\tbuffer_load_dwordx4 v[80:83], %[vo1], s[40:43], %[pg0] offen sc1\n\t" \
          "buffer_load_dwordx4 v[84:87], %[vo1], s[40:43], %[pg0] offen offset:1024 sc1\n\tbuffer_load_dwordx4 v[88:91], %[vo1], s[40:43], %[pg0] offen offset:2048 sc1\n\tbuffer_load_dwordx4 v[92:95], %[vo1], s[40:43], %[pg0] offen offset:3072 sc1\n\tbuffer_load_dwordx4 v[96:99], %[vo0], s[40:43], %[pg1] offen sc1\n\tbuffer_load_dwordx4 v[100:103], %[vo0], s[40:43], %[pg1] offen offset:1024 sc1\n\tbuffer_load_dwordx4 v[104:107], %[vo0], s[40:43], %[pg1] offen offset:2048 sc1\n\t" \
          "buffer_load_dwordx4 v[108:111], %[vo0], s[40:43], %[pg1] offen offset:3072 sc1\n\tbuffer_load_dwordx4 v[112:115], %[vo1], s[40:43], %[pg1] offen sc1\n\tbuffer_load_dwordx4 v[116:119], %[vo1], s[40:43], %[pg1] offen offset:1024 sc1\n\tbuffer_load_dwordx4 v[120:123], %[vo1], s[40:43], %[pg1] offen offset:2048 sc1\n\tbuffer_load_dwordx4 v[124:127], %[vo1], s[40:43], %[pg1] offen offset:3072 sc1\n\tLou_top%=:\n\t" \
          "s_lshl_b32 %[t], %[av], 7\n\ts_add_u32 %[t], %[t], %[fo]\n\tbuffer_load_dword %[fl], %[vf], %[rf], %[t] offen sc1\n\tds_read_b128 v[32:35], %[own] offset:8192\n\tds_read_b128 v[36:39], %[own] offset:9216\n\tds_read_b128 v[40:43], %[own] offset:10240\n\t" \
          "s_waitcnt vmcnt(16) lgkmcnt(2)\n\tv_mfma_f32_16x16x32_bf16 v[48:51], a[0:3], v[64:67], 0\n\tds_read_b128 v[44:47], %[own] offset:11264\n\tv_mfma_f32_16x16x32_bf16 v[52:55], a[4:7], v[64:67], 0\n\tv_mfma_f32_16x16x32_bf16 v[56:59], v[128:131], v[64:67], 0\n\tv_mfma_f32_16x16x32_bf16 v[60:63], v[32:35], v[64:67], 0\n\t" \
          "buffer_load_dwordx4 v[64:67], %[vo0], s[40:43], %[pg2] offen sc1\n\ts_waitcnt vmcnt(16) lgkmcnt(2)\n\tv_mfma_f32_16x16x32_bf16 v[48:51], a[8:11], v[68:71], v[48:51]\n\tds_read_b128 v[32:35], %[own] offset:12288\n\tv_mfma_f32_16x16x32_bf16 v[52:55], a[12:15], v[68:71], v[52:55]\n\tv_mfma_f32_16x16x32_bf16 v[56:59], v[132:135], v[68:71], v[56:59]\n\t" \
          "v_mfma_f32_16x16x32_bf16 v[60:63], v[36:39], v[68:71], v[60:63]\n\tbuffer_load_dwordx4 v[68:71], %[vo0], s[40:43], %[pg2] offen offset:1024 sc1\n\ts_waitcnt vmcnt(16) lgkmcnt(2)\n\tv_mfma_f32_16x16x32_bf16 v[48:51], a[16:19], v[72:75], v[48:51]\n\tds_read_b128 v[36:39], %[own] offset:13312\n\tv_mfma_f32_16x16x32_bf16 v[52:55], a[20:23], v[72:75], v[52:55]\n\t" \
          "v_mfma_f32_16x16x32_bf16 v[56:59], v[136:139], v[72:75], v[56:59]\n\tv_mfma_f32_16x16x32_bf16 v[60:63], v[40:43], v[72:75], v[60:63]\n\tbuffer_load_dwordx4 v[72:75], %[vo0], s[40:43], %[pg2] offen offset:2048 sc1\n\ts_waitcnt vmcnt(16) lgkmcnt(2)\n\tv_mfma_f32_16x16x32_bf16 v[48:51], a[24:27], v[76:79], v[48:51]\n\tds_read_b128 v[40:43], %[own] offset:14336\n\t" \
          "v_mfma_f32_16x16x32_bf16 v[52:55], a[28:31], v[76:79], v[52:55]\n\tv_mfma_f32_16x16x32_bf16 v[56:59], v[140:143], v[76:79], v[56:59]\n\tv_mfma_f32_16x16x32_bf16 v[60:63], v[44:47], v[76:79], v[60:63]\n\tbuffer_load_dwordx4 v[76:79], %[vo0], s[40:43], %[pg2] offen offset:3072 sc1\n\ts_waitcnt vmcnt(16) lgkmcnt(2)\n\tv_mfma_f32_16x16x32_bf16 v[48:51], a[32:35], v[80:83], v[48:51]\n\t" \
          "ds_read_b128 v[44:47], %[own] offset:15360\n\tv_mfma_f32_16x16x32_bf16 v[52:55], a[36:39], v[80:83], v[52:55]\n\tv_mfma_f32_16x16x32_bf16 v[56:59], v[144:147], v[80:83], v[56:59]\n\tv_mfma_f32_16x16x32_bf16 v[60:63], v[32:35], v[80:83], v[60:63]\n\tbuffer_load_dwordx4 v[80:83], %[vo1], s[40:43], %[pg2] offen sc1\n\ts_waitcnt vmcnt(16) lgkmcnt(2)\n\t" \
          "v_mfma_f32_16x16x32_bf16 v[48:51], a[40:43], v[84:87], v[48:51]\n\tds_read_b128 v[32:35], %[own] offset:16384\n\tv_mfma_f32_16x16x32_bf16 v[52:55], a[44:47], v[84:87], v[52:55]\n\tv_mfma_f32_16x16x32_bf16 v[56:59], v[148:151], v[84:87], v[56:59]\n\tv_mfma_f32_16x16x32_bf16 v[60:63], v[36:39], v[84:87], v[60:63]\n\tbuffer_load_dwordx4 v[84:87], %[vo1], s[40:43], %[pg2] offen offset:1024 sc1\n\t" \
          "s_waitcnt vmcnt(16) lgkmcnt(2)\n\tv_mfma_f32_16x16x32_bf16 v[48:51], a[48:51], v[88:91], v[48:51]\n\tds_read_b128 v[36:39], %[own] offset:17408\n\tv_mfma_f32_16x16x32_bf16 v[52:55], a[52:55], v[88:91], v[52:55]\n\tv_mfma_f32_16x16x32_bf16 v[56:59], v[152:155], v[88:91], v[56:59]\n\tv_mfma_f32_16x16x32_bf16 v[60:63], v[40:43], v[88:91], v[60:63]\n\t" \
          "buffer_load_dwordx4 v[88:91], %[vo1], s[40:43], %[pg2] offen offset:2048 sc1\n\ts_waitcnt vmcnt(16) lgkmcnt(2)\n\tv_mfma_f32_16x16x32_bf16 v[48:51], a[56:59], v[92:95], v[48:51]\n\tds_read_b128 v[40:43], %[own] offset:18432\n\tv_mfma_f32_16x16x32_bf16 v[52:55], a[60:63], v[92:95], v[52:55]\n\tv_mfma_f32_16x16x32_bf16 v[56:59], v[156:159], v[92:95], v[56:59]\n\t" \
          "v_mfma_f32_16x16x32_bf16 v[60:63], v[44:47], v[92:95], v[60:63]\n\tbuffer_load_dwordx4 v[92:95], %[vo1], s[40:43], %[pg2] offen offset:3072 sc1\n\ts_waitcnt vmcnt(16) lgkmcnt(2)\n\tv_mfma_f32_16x16x32_bf16 v[48:51], a[64:67], v[96:99], v[48:51]\n\tds_read_b128 v[44:47], %[own] offset:19456\n\tv_mfma_f32_16x16x32_bf16 v[52:55], a[68:71], v[96:99], v[52:55]\n\t" \
          "v_mfma_f32_16x16x32_bf16 v[56:59], v[160:163], v[96:99], v[56:59]\n\tv_mfma_f32_16x16x32_bf16 v[60:63], v[32:35], v[96:99], v[60:63]\n\tbuffer_load_dwordx4 v[96:99], %[vo0], s[40:43], %[pg3] offen sc1\n\ts_waitcnt vmcnt(16) lgkmcnt(2)\n\tv_mfma_f32_16x16x32_bf16 v[48:51], a[72:75], v[100:103], v[48:51]\n\tds_read_b128 v[32:35], %[own] offset:20480\n\t" \
          "v_mfma_f32_16x16x32_bf16 v[52:55], a[76:79], v[100:103], v[52:55]\n\tv_mfma_f32_16x16x32_bf16 v[56:59], v[164:167], v[100:103], v[56:59]\n\tv_mfma_f32_16x16x32_bf16 v[60:63], v[36:39], v[100:103], v[60:63]\n\tbuffer_load_dwordx4 v[100:103], %[vo0], s[40:43], %[pg3] offen offset:1024 sc1\n\ts_waitcnt vmcnt(16) lgkmcnt(2)\n\tv_mfma_f32_16x16x32_bf16 v[48:51], a[80:83], v[104:107], v[48:51]\n\t" \
          "ds_read_b128 v[36:39], %[own] offset:21504\n\tv_mfma_f32_16x16x32_bf16 v[52:55], a[84:87], v[104:107], v[52:55]\n\tv_mfma_f32_16x16x32_bf16 v[56:59], v[168:171], v[104:107], v[56:59]\n\tv_mfma_f32_16x16x32_bf16 v[60:63], v[40:43], v[104:107], v[60:63]\n\tbuffer_load_dwordx4 v[104:107], %[vo0], s[40:43], %[pg3] offen offset:2048 sc1\n\ts_waitcnt vmcnt(16) lgkmcnt(2)\n\t" \
          "v_mfma_f32_16x16x32_bf16 v[48:51], a[88:91], v[108:111], v[48:51]\n\tds_read_b128 v[40:43], %[own] offset:22528\n\tv_mfma_f32_16x16x32_bf16 v[52:55], a[92:95], v[108:111], v[52:55]\n\tv_mfma_f32_16x16x32_bf16 v[56:59], v[172:175], v[108:111], v[56:59]\n\tv_mfma_f32_16x16x32_bf16 v[60:63], v[44:47], v[108:111], v[60:63]\n\tbuffer_load_dwordx4 v[108:111], %[vo0], s[40:43], %[pg3] offen offset:3072 sc1\n\t" \
          "s_waitcnt vmcnt(16) lgkmcnt(2)\n\tv_mfma_f32_16x16x32_bf16 v[48:51], a[96:99], v[112:115], v[48:51]\n\tds_read_b128 v[44:47], %[own] offset:23552\n\tv_mfma_f32_16x16x32_bf16 v[52:55], a[100:103], v[112:115], v[52:55]\n\tv_mfma_f32_16x16x32_bf16 v[56:59], v[176:179], v[112:115], v[56:59]\n\tv_mfma_f32_16x16x32_bf16 v[60:63], v[32:35], v[112:115], v[60:63]\n\t" \
          "buffer_load_dwordx4 v[112:115], %[vo1], s[40:43], %[pg3] offen sc1\n\ts_waitcnt vmcnt(16) lgkmcnt(2)\n\tv_mfma_f32_16x16x32_bf16 v[48:51], a[104:107], v[116:119], v[48:51]\n\tds_read_b128 v[32:35], %[own] offset:24576\n\tv_mfma_f32_16x16x32_bf16 v[52:55], a[108:111], v[116:119], v[52:55]\n\tv_mfma_f32_16x16x32_bf16 v[56:59], v[180:183], v[116:119], v[56:59]\n\t" \
          "v_mfma_f32_16x16x32_bf16 v[60:63], v[36:39], v[116:119], v[60:63]\n\tbuffer_load_dwordx4 v[116:119], %[vo1], s[40:43], %[pg3] offen offset:1024 sc1\n\ts_waitcnt vmcnt(16) lgkmcnt(2)\n\tv_mfma_f32_16x16x32_bf16 v[48:51], a[112:115], v[120:123], v[48:51]\n\tds_read_b128 v[36:39], %[own] offset:25600\n\tv_mfma_f32_16x16x32_bf16 v[52:55], a[116:119], v[120:123], v[52:55]\n\t" \
          "v_mfma_f32_16x16x32_bf16 v[56:59], v[184:187], v[120:123], v[56:59]\n\tv_mfma_f32_16x16x32_bf16 v[60:63], v[40:43], v[120:123], v[60:63]\n\tbuffer_load_dwordx4 v[120:123], %[vo1], s[40:43], %[pg3] offen offset:2048 sc1\n\ts_waitcnt vmcnt(16) lgkmcnt(2)\n\tv_mfma_f32_16x16x32_bf16 v[48:51], a[120:123], v[124:127], v[48:51]\n\tds_read_b128 v[40:43], %[own] offset:26624\n\t" \
          "v_mfma_f32_16x16x32_bf16 v[52:55], a[124:127], v[124:127], v[52:55]\n\tv_mfma_f32_16x16x32_bf16 v[56:59], v[188:191], v[124:127], v[56:59]\n\tv_mfma_f32_16x16x32_bf16 v[60:63], v[44:47], v[124:127], v[60:63]\n\tbuffer_load_dwordx4 v[124:127], %[vo1], s[40:43], %[pg3] offen offset:3072 sc1\n\ts_waitcnt vmcnt(15) lgkmcnt(2)\n\tv_mfma_f32_16x16x32_bf16 v[48:51], a[128:131], v[64:67], v[48:51]\n\t" \
          "ds_read_b128 v[44:47], %[own] offset:27648\n\tv_mfma_f32_16x16x32_bf16 v[52:55], a[132:135], v[64:67], v[52:55]\n\tv_cmp_gt_u32 vcc, 8, %[fl]\n\ts_nop 0\n\ts_ff1_i32_b64 %[t], vcc\n\ts_add_u32 %[av], %[av], %[t]\n\t" \
          "v_mfma_f32_16x16x32_bf16 v[56:59], v[192:195], v[64:67], v[56:59]\n\ts_lshl_b32 %[oo], %[j], 16\n\ts_cmp_lt_u32 %[j], %[j1]\n\ts_cselect_b32 %[t], 2, 4\n\ts_add_u32 %[j], %[j], %[t]\n\ts_lshl_b32 %[t], %[t], 14\n\t" \
          "s_add_u32 %[pg0], %[pg0], %[t]\n\ts_add_u32 %[pg1], %[pg1], %[t]\n\ts_add_u32 %[pg2], %[pg2], %[t]\n\ts_add_u32 %[pg3], %[pg3], %[t]\n\ts_lshr_b32 %[t], %[j], 1\n\ts_cmp_lt_u32 %[t], %[av]\n\t" \
          "s_cselect_b32 s46, s42, 0\n\tv_mfma_f32_16x16x32_bf16 v[60:63], v[32:35], v[64:67], v[60:63]\n\tbuffer_load_dwordx4 v[64:67], %[vo0], s[44:47], %[pg0] offen sc1\n\ts_waitcnt vmcnt(15) lgkmcnt(2)\n\tv_mfma_f32_16x16x32_bf16 v[48:51], a[136:139], v[68:71], v[48:51]\n\tds_read_b128 v[32:35], %[own] offset:28672\n\t" \
          "v_mfma_f32_16x16x32_bf16 v[52:55], a[140:143], v[68:71], v[52:55]\n\tv_mfma_f32_16x16x32_bf16 v[56:59], v[196:199], v[68:71], v[56:59]\n\tv_mfma_f32_16x16x32_bf16 v[60:63], v[36:39], v[68:71], v[60:63]\n\tbuffer_load_dwordx4 v[68:71], %[vo0], s[44:47], %[pg0] offen offset:1024 sc1\n\ts_waitcnt vmcnt(15) lgkmcnt(2)\n\tv_mfma_f32_16x16x32_bf16 v[48:51], a[144:147], v[72:75], v[48:51]\n\t" \
          "ds_read_b128 v[36:39], %[own] offset:29696\n\tv_mfma_f32_16x16x32_bf16 v[52:55], a[148:151], v[72:75], v[52:55]\n\tv_mfma_f32_16x16x32_bf16 v[56:59], v[200:203], v[72:75], v[56:59]\n\tv_mfma_f32_16x16x32_bf16 v[60:63], v[40:43], v[72:75], v[60:63]\n\tbuffer_load_dwordx4 v[72:75], %[vo0], s[44:47], %[pg0] offen offset:2048 sc1\n\ts_waitcnt vmcnt(15) lgkmcnt(2)\n\t" \
          "v_mfma_f32_16x16x32_bf16 v[48:51], a[152:155], v[76:79], v[48:51]\n\tds_read_b128 v[40:43], %[own] offset:30720\n\tv_mfma_f32_16x16x32_bf16 v[52:55], a[156:159], v[76:79], v[52:55]\n\tv_mfma_f32_16x16x32_bf16 v[56:59], v[204:207], v[76:79], v[56:59]\n\tv_mfma_f32_16x16x32_bf16 v[60:63], v[44:47], v[76:79], v[60:63]\n\tbuffer_load_dwordx4 v[76:79], %[vo0], s[44:47], %[pg0] offen offset:3072 sc1\n\t" \
          "s_waitcnt vmcnt(15) lgkmcnt(2)\n\tv_mfma_f32_16x16x32_bf16 v[48:51], a[160:163], v[80:83], v[48:51]\n\tds_read_b128 v[44:47], %[own] offset:31744\n\tv_mfma_f32_16x16x32_bf16 v[52:55], a[164:167], v[80:83], v[52:55]\n\tv_mfma_f32_16x16x32_bf16 v[56:59], v[208:211], v[80:83], v[56:59]\n\tv_mfma_f32_16x16x32_bf16 v[60:63], v[32:35], v[80:83], v[60:63]\n\t" \
          "buffer_load_dwordx4 v[80:83], %[vo1], s[44:47], %[pg0] offen sc1\n\ts_waitcnt vmcnt(15) lgkmcnt(2)\n\tv_mfma_f32_16x16x32_bf16 v[48:51], a[168:171], v[84:87], v[48:51]\n\tds_read_b128 v[32:35], %[own] offset:0\n\tv_mfma_f32_16x16x32_bf16 v[52:55], a[172:175], v[84:87], v[52:55]\n\tv_mfma_f32_16x16x32_bf16 v[56:59], v[212:215], v[84:87], v[56:59]\n\t" \
          "v_mfma_f32_16x16x32_bf16 v[60:63], v[36:39], v[84:87], v[60:63]\n\tbuffer_load_dwordx4 v[84:87], %[vo1], s[44:47], %[pg0] offen offset:1024 sc1\n\ts_waitcnt vmcnt(15) lgkmcnt(2)\n\tv_mfma_f32_16x16x32_bf16 v[48:51], a[176:179], v[88:91], v[48:51]\n\tds_read_b128 v[36:39], %[own] offset:1024\n\tv_mfma_f32_16x16x32_bf16 v[52:55], a[180:183], v[88:91], v[52:55]\n\t" \
          "v_mfma_f32_16x16x32_bf16 v[56:59], v[216:219], v[88:91], v[56:59]\n\tv_mfma_f32_16x16x32_bf16 v[60:63], v[40:43], v[88:91], v[60:63]\n\tbuffer_load_dwordx4 v[88:91], %[vo1], s[44:47], %[pg0] offen offset:2048 sc1\n\ts_waitcnt vmcnt(15) lgkmcnt(2)\n\tv_mfma_f32_16x16x32_bf16 v[48:51], a[184:187], v[92:95], v[48:51]\n\tds_read_b128 v[40:43], %[own] offset:2048\n\t" \
          "v_mfma_f32_16x16x32_bf16 v[52:55], a[188:191], v[92:95], v[52:55]\n\tv_mfma_f32_16x16x32_bf16 v[56:59], v[220:223], v[92:95], v[56:59]\n\tv_mfma_f32_16x16x32_bf16 v[60:63], v[44:47], v[92:95], v[60:63]\n\tbuffer_load_dwordx4 v[92:95], %[vo1], s[44:47], %[pg0] offen offset:3072 sc1\n\ts_waitcnt vmcnt(15) lgkmcnt(2)\n\tv_mfma_f32_16x16x32_bf16 v[48:51], a[192:195], v[96:99], v[48:51]\n\t" \
          "ds_read_b128 v[44:47], %[own] offset:3072\n\tv_mfma_f32_16x16x32_bf16 v[52:55], a[196:199], v[96:99], v[52:55]\n\tv_mfma_f32_16x16x32_bf16 v[56:59], v[224:227], v[96:99], v[56:59]\n\tv_mfma_f32_16x16x32_bf16 v[60:63], v[32:35], v[96:99], v[60:63]\n\tbuffer_load_dwordx4 v[96:99], %[vo0], s[44:47], %[pg1] offen sc1\n\ts_waitcnt vmcnt(15) lgkmcnt(2)\n\t" \
          "v_mfma_f32_16x16x32_bf16 v[48:51], a[200:203], v[100:103], v[48:51]\n\tds_read_b128 v[32:35], %[own] offset:4096\n\tv_mfma_f32_16x16x32_bf16 v[52:55], a[204:207], v[100:103], v[52:55]\n\tv_mfma_f32_16x16x32_bf16 v[56:59], v[228:231], v[100:103], v[56:59]\n\tv_mfma_f32_16x16x32_bf16 v[60:63], v[36:39], v[100:103], v[60:63]\n\tbuffer_load_dwordx4 v[100:103], %[vo0], s[44:47], %[pg1] offen offset:1024 sc1\n\t" \
          "s_waitcnt vmcnt(15) lgkmcnt(2)\n\tv_mfma_f32_16x16x32_bf16 v[48:51], a[208:211], v[104:107], v[48:51]\n\tds_read_b128 v[36:39], %[own] offset:5120\n\tv_mfma_f32_16x16x32_bf16 v[52:55], a[212:215], v[104:107], v[52:55]\n\tv_mfma_f32_16x16x32_bf16 v[56:59], v[232:235], v[104:107], v[56:59]\n\tv_mfma_f32_16x16x32_bf16 v[60:63], v[40:43], v[104:107], v[60:63]\n\t" \
          "buffer_load_dwordx4 v[104:107], %[vo0], s[44:47], %[pg1] offen offset:2048 sc1\n\ts_waitcnt vmcnt(15) lgkmcnt(2)\n\tv_mfma_f32_16x16x32_bf16 v[48:51], a[216:219], v[108:111], v[48:51]\n\tds_read_b128 v[40:43], %[own] offset:6144\n\tv_mfma_f32_16x16x32_bf16 v[52:55], a[220:223], v[108:111], v[52:55]\n\tv_mfma_f32_16x16x32_bf16 v[56:59], v[236:239], v[108:111], v[56:59]\n\t" \
          "v_mfma_f32_16x16x32_bf16 v[60:63], v[44:47], v[108:111], v[60:63]\n\tbuffer_load_dwordx4 v[108:111], %[vo0], s[44:47], %[pg1] offen offset:3072 sc1\n\ts_waitcnt vmcnt(15) lgkmcnt(2)\n\tv_mfma_f32_16x16x32_bf16 v[48:51], a[224:227], v[112:115], v[48:51]\n\tds_read_b128 v[44:47], %[own] offset:7168\n\tv_mfma_f32_16x16x32_bf16 v[52:55], a[228:231], v[112:115], v[52:55]\n\t" \
          "v_mfma_f32_16x16x32_bf16 v[56:59], v[240:243], v[112:115], v[56:59]\n\tv_mfma_f32_16x16x32_bf16 v[60:63], v[32:35], v[112:115], v[60:63]\n\tbuffer_load_dwordx4 v[112:115], %[vo1], s[44:47], %[pg1] offen sc1\n\ts_waitcnt vmcnt(15) lgkmcnt(2)\n\tv_mfma_f32_16x16x32_bf16 v[48:51], a[232:235], v[116:119], v[48:51]\n\tv_mfma_f32_16x16x32_bf16 v[52:55], a[236:239], v[116:119], v[52:55]\n\t" \
          "v_mfma_f32_16x16x32_bf16 v[56:59], v[244:247], v[116:119], v[56:59]\n\tv_mfma_f32_16x16x32_bf16 v[60:63], v[36:39], v[116:119], v[60:63]\n\tbuffer_load_dwordx4 v[116:119], %[vo1], s[44:47], %[pg1] offen offset:1024 sc1\n\ts_waitcnt vmcnt(15) lgkmcnt(1)\n\tv_mfma_f32_16x16x32_bf16 v[48:51], a[240:243], v[120:123], v[48:51]\n\tv_mfma_f32_16x16x32_bf16 v[52:55], a[244:247], v[120:123], v[52:55]\n\t" \
          "v_mfma_f32_16x16x32_bf16 v[56:59], v[248:251], v[120:123], v[56:59]\n\tv_mfma_f32_16x16x32_bf16 v[60:63], v[40:43], v[120:123], v[60:63]\n\tbuffer_load_dwordx4 v[120:123], %[vo1], s[44:47], %[pg1] offen offset:2048 sc1\n\ts_waitcnt vmcnt(15) lgkmcnt(0)\n\tv_mfma_f32_16x16x32_bf16 v[48:51], a[248:251], v[124:127], v[48:51]\n\tv_mfma_f32_16x16x32_bf16 v[52:55], a[252:255], v[124:127], v[52:55]\n\t" \
          "v_mfma_f32_16x16x32_bf16 v[56:59], v[252:255], v[124:127], v[56:59]\n\tv_mfma_f32_16x16x32_bf16 v[60:63], v[44:47], v[124:127], v[60:63]\n\tbuffer_load_dwordx4 v[124:127], %[vo1], s[44:47], %[pg1] offen offset:3072 sc1\n\ts_nop 6\n\tds_write_b128 %[rw_], v[48:51] offset:0\n\tds_write_b128 %[rw_], v[52:55] offset:1024\n\t" \
          "ds_write_b128 %[rw_], v[56:59] offset:2048\n\tds_write_b128 %[rw_], v[60:63] offset:3072\n\ts_waitcnt lgkmcnt(0)\n\ts_barrier\n\tds_read_b128 v[32:35], %[rr0] offset:0\n\tds_read_b128 v[36:39], %[rr0] offset:40960\n\t" \
          "ds_read_b128 v[40:43], %[rr1] offset:0\n\tds_read_b128 v[44:47], %[rr1] offset:40960\n\tv_xor_b32 %[rw_], 0x1000, %[rw_]\n\tv_xor_b32 %[rr0], 0x1000, %[rr0]\n\tv_xor_b32 %[rr1], 0x1000, %[rr1]\n\ts_waitcnt lgkmcnt(0)\n\t" \
          "v_pk_add_f32 v[32:33], v[32:33], v[36:37]\n\tv_pk_add_f32 v[34:35], v[34:35], v[38:39]\n\tv_pk_add_f32 v[40:41], v[40:41], v[44:45]\n\tv_pk_add_f32 v[42:43], v[42:43], v[46:47]\n\tv_pk_add_f32 v[32:33], v[32:33], v[40:41]\n\tv_pk_add_f32 v[34:35], v[34:35], v[42:43]\n\t" \
          "s_nop 0\n\tv_cvt_pk_bf16_f32 v44, v32, v33\n\tv_cvt_pk_bf16_f32 v45, v34, v35\n\tbuffer_store_dwordx2 v[44:45], %[so], %[rc], %[oo] offen\n\ts_cmp_lg_u32 s46, 0\n\ts_cbranch_scc1 Lou_top%=\n\t" \
          "s_cmp_lt_u32 %[j], 128\n\ts_cbranch_scc1 Lou_wait%=\n\ts_waitcnt vmcnt(0)\n\t"
#define OU_CLOB "v32", "v33", "v34", "v35", "v36", "v37", "v38", "v39", "v40", "v41", "v42", "v43", "v44", "v45", "v46", "v47", "v48", "v49", "v50", "v51", "v52", "v53", "v54", "v55", "v56", "v57", "v58", "v59", "v60", "v61", "v62", "v63", "v64", "v65", "v66", "v67", "v68", "v69", "v70", "v71", "v72", "v73", "v74", "v75", "v76", "v77", "v78", "v79", "v80", "v81", "v82", "v83", "v84", "v85", "v86", "v87", "v88", "v89", "v90", "v91", "v92", "v93", "v94", "v95", "v96", "v97", "v98", "v99", "v100", "v101", "v102", "v103", "v104", "v105", "v106", "v107", "v108", "v109", "v110", "v111", "v112", "v113", "v114", "v115", "v116", "v117", "v118", "v119", "v120", "v121", "v122", "v123", "v124", "v125", "v126", "v127", "v128", "v129", "v130", "v131", "v132", "v133", "v134", "v135", "v136", "v137", "v138", "v139", "v140", "v141", "v142", "v143", "v144", "v145", "v146", "v147", "v148", "v149", "v150", "v151", "v152", "v153", "v154", "v155", "v156", "v157", "v158", "v159", "v160", "v161", "v162", "v163", "v164", "v165", "v166", "v167", "v168", "v169", "v170", "v171", "v172", "v173", "v174", "v175", "v176", "v177", "v178", "v179", "v180", "v181", "v182", "v183", "v184", "v185", "v186", "v187", "v188", "v189", "v190", "v191", "v192", "v193", "v194", "v195", "v196", "v197", "v198", "v199", "v200", "v201", "v202", "v203", "v204", "v205", "v206", "v207", "v208", "v209", "v210", "v211", "v212", "v213", "v214", "v215", "v216", "v217", "v218", "v219", "v220", "v221", "v222", "v223", "v224", "v225", "v226", "v227", "v228", "v229", "v230", "v231", "v232", "v233", "v234", "v235", "v236", "v237", "v238", "v239", "v240", "v241", "v242", "v243", "v244", "v245", "v246", "v247", "v248", "v249", "v250", "v251", "v252", "v253", "v254", "v255", "a0", "a1", "a2", "a3", "a4", "a5", "a6", "a7", "a8", "a9", "a10", "a11", "a12", "a13", "a14", "a15", "a16", "a17", "a18", "a19", "a20", "a21", "a22", "a23", "a24", "a25", "a26", "a27", "a28", "a29", "a30", "a31", "a32", "a33", "a34", "a35", "a36", "a37", "a38", "a39", "a40", "a41", "a42", "a43", "a44", "a45", "a46", "a47", "a48", "a49", "a50", "a51", "a52", "a53", "a54", "a55", "a56", "a57", "a58", "a59", "a60", "a61", "a62", "a63", "a64", "a65", "a66", "a67", "a68", "a69", "a70", "a71", "a72", "a73", "a74", "a75", "a76", "a77", "a78", "a79", "a80", "a81", "a82", "a83", "a84", "a85", "a86", "a87", "a88", "a89", "a90", "a91", "a92", "a93", "a94", "a95", "a96", "a97", "a98", "a99", "a100", "a101", "a102", "a103", "a104", "a105", "a106", "a107", "a108", "a109", "a110", "a111", "a112", "a113", "a114", "a115", "a116", "a117", "a118", "a119", "a120", "a121", "a122", "a123", "a124", "a125", "a126", "a127", "a128", "a129", "a130", "a131", "a132", "a133", "a134", "a135", "a136", "a137", "a138", "a139", "a140", "a141", "a142", "a143", "a144", "a145", "a146", "a147", "a148", "a149", "a150", "a151", "a152", "a153", "a154", "a155", "a156", "a157", "a158", "a159", "a160", "a161", "a162", "a163", "a164", "a165", "a166", "a167", "a168", "a169", "a170", "a171", "a172", "a173", "a174", "a175", "a176", "a177", "a178", "a179", "a180", "a181", "a182", "a183", "a184", "a185", "a186", "a187", "a188", "a189", "a190", "a191", "a192", "a193", "a194", "a195", "a196", "a197", "a198", "a199", "a200", "a201", "a202", "a203", "a204", "a205", "a206", "a207", "a208", "a209", "a210", "a211", "a212", "a213", "a214", "a215", "a216", "a217", "a218", "a219", "a220", "a221", "a222", "a223", "a224", "a225", "a226", "a227", "a228", "a229", "a230", "a231", "a232", "a233", "a234", "a235", "a236", "a237", "a238", "a239", "a240", "a241", "a242", "a243", "a244", "a245", "a246", "a247", "a248", "a249", "a250", "a251", "a252", "a253", "a254", "a255", "s40", "s41", "s42", "s43", "s44", "s45", "s46", "s47"
__device__ __forceinline__ void out_unit(const FParams& F, unsigned long long parq, int set, int un, int w) {
  const int lane = opaque_lane(), l15 = lane & 15, lq = lane >> 4, r = un & 7;
  const __amdgpu_buffer_rsrc_t rw = __builtin_amdgcn_make_buffer_rsrc(wg::uni(F.Wo + 64 * un), 0, HDA * DM * 2 - 128 * un, 0x00020000);
  const __amdgpu_buffer_rsrc_t rc = __builtin_amdgcn_make_buffer_rsrc(wg::uni(F.out + 64 * un), 0, S_ * 2 * DM * 2 - 128 * un, 0x00020000);
  const __amdgpu_buffer_rsrc_t rf = __builtin_amdgcn_make_buffer_rsrc(F.ctl, 0, sizeof(Ctl), 0x00020000);
  const int own = w * 40960 + lane * 16;
  int j = (set < 2 ? 2 * OSEG0 : F.oj1) + set, av = OSEG0, pg[4], t, cnt, oo, fl, rw_ = own + 32768, rr0 = 32768 + (l15 >> 2) * 1024 + ((l15 & 3) * 16 + 4 * w + lq) * 16, rr1 = rr0 + 81920;
#pragma unroll
  for (int i = 0; i < 4; ++i) pg[i] = __builtin_amdgcn_readfirstlane(qoff(parq, 4 * w + (i ^ (r >> 1))) + j * QBLK);
  const int rt = w * 40960 + (8 * lq + (l15 >> 2)) * 128 + (((l15 >> 3) | ((lq & 1) << 1)) << 5) + (l15 & 3) * 8, vw = (lane >> 3) * 2048 + (((lane & 7) ^ ((lq & 1) << 1)) << 4);
  asm volatile(OU_BODY
    : [j] "+s"(j), [av] "+s"(av), [pg0] "+s"(pg[0]), [pg1] "+s"(pg[1]), [pg2] "+s"(pg[2]), [pg3] "+s"(pg[3]), [t] "=&s"(t), [cnt] "=&s"(cnt), [oo] "=&s"(oo), [fl] "=&v"(fl), [rw_] "+v"(rw_), [rr0] "+v"(rr0), [rr1] "+v"(rr1)
    : [vo0] "v"(lane * 16 + (r & 1) * 4096), [vo1] "v"(lane * 16 + (~r & 1) * 4096), [vw0] "v"(vw), [vw1] "v"(vw ^ 64), [rt0] "v"(rt), [rt1] "v"(rt ^ 32), [rt2] "v"(rt ^ 64), [rt3] "v"(rt ^ 96), [own] "v"(own),
      [vf] "v"(lane < 8 ? lane * 128 : 0x7ffffff0), [so] "v"((4 * w + lq) * 4096 + l15 * 8),
      [qa] "s"(F.qkvA), [qs] "s"(QSIZE), [rw] "s"(rw), [rc] "s"(rc), [rf] "s"(rf), [wl] "s"(w * 40960), [wo] "s"((1024 * w + 128 * r) * 2048), [fo] "s"((int)offsetof(Ctl, cntO)), [tmo] "s"((int)offsetof(Ctl, tmo)), [j1] "s"(F.oj1)
    : OU_CLOB, "vcc", "scc", "memory");
}

__device__ __forceinline__ float dot8(v4u a, v4u b) {
  float d = 0.f;
#pragma unroll
  for (int e = 0; e < 4; ++e) { const unsigned x = a[e], y = b[e]; d = __builtin_amdgcn_fdot2_f32_bf16(__builtin_bit_cast(bfx2, x), __builtin_bit_cast(bfx2, y), d, false); }
  return d;
}
__device__ __forceinline__ void attnB_16(const u16* qkv, __amdgpu_buffer_rsrc_t rsO, int pos0  , int w) {
  const int tid = 64 * w + opaque_lane(), cc = tid & 127, p0 = pos0 + (tid >> 7);
  v4u q[8], k[15], v[15];
#pragma unroll
  for (int i = 0; i < 15; ++i) {
    const int kp = p0 - 14 + 2 * i, kc = kp < 0 ? 0 : kp;
    k[i] = *(const v4u*)(qkv + (size_t)kc * NQKVB + DB + cc * 8);
    v[i] = *(const v4u*)(qkv + (size_t)kc * NQKVB + 2 * DB + cc * 8);
  }
#pragma unroll
  for (int i = 0; i < 8; ++i) q[i] = *(const v4u*)(qkv + (size_t)(p0 + 2 * i) * NQKVB + cc * 8);
  const float scale = 0.17677669529663687f;
#pragma unroll
  for (int i = 0; i < 8; ++i) {
    const int pos = p0 + 2 * i;
    float lg[8], mx = -INFINITY;
#pragma unroll
    for (int w = 0; w < 8; ++w) {
      float d = dot8(q[i], k[i + 7 - w]);
      d += __int_as_float(__builtin_amdgcn_update_dpp(0, __float_as_int(d), 0xB1, 0xf, 0xf, false));
      d += __int_as_float(__builtin_amdgcn_update_dpp(0, __float_as_int(d), 0x4E, 0xf, 0xf, false));
      lg[w] = d;
    }
#pragma unroll
    for (int w = 0; w < 8; w += 2) {
      unsigned p = pk2(lg[w], lg[w + 1]);
      p = pk2(__uint_as_float(p << 16) * scale, __uint_as_float(p & 0xffff0000u) * scale);
      lg[w] = __uint_as_float(p << 16); lg[w + 1] = __uint_as_float(p & 0xffff0000u);
    }
#pragma unroll
    for (int w = 0; w < 8; ++w) if (pos - 2 * w >= 0) mx = fmaxf(mx, lg[w]);
    float sum = 0.f;
#pragma unroll
    for (int w = 0; w < 8; ++w) { lg[w] = (pos - 2 * w >= 0) ? __expf(lg[w] - mx) : 0.f; sum += lg[w]; }
    const float inv = __builtin_amdgcn_rcpf(sum);
    float o[8] = {0.f, 0.f, 0.f, 0.f, 0.f, 0.f, 0.f, 0.f};
#pragma unroll
    for (int w = 0; w < 8; w += 2) {
      const unsigned pp = pk2(lg[w] * inv, lg[w + 1] * inv);
#pragma unroll
      for (int z = 0; z < 2; ++z) {
        if (pos - 2 * (w + z) >= 0) {
          const float p = z ? __uint_as_float(pp & 0xffff0000u) : __uint_as_float(pp << 16);
          const v4u vv = v[i + 7 - w - z];
#pragma unroll
          for (int e = 0; e < 4; ++e) { o[2 * e] += p * __uint_as_float(vv[e] << 16); o[2 * e + 1] += p * __uint_as_float(vv[e] & 0xffff0000u); }
        }
      }
    }
    __builtin_amdgcn_raw_buffer_store_b128((v4u){pk2(o[0], o[1]), pk2(o[2], o[3]), pk2(o[4], o[5]), pk2(o[6], o[7])}, rsO, (pos * NQKVB + cc * 8) * 2, 0, 16);
  }
}

constexpr int NTASK = 240;
__constant__ unsigned short TASKS[NTASK] = {
  5120, 5121, 5122, 5123, 5124, 5125, 5126, 5127, 5128, 5129, 5130, 5131, 5132, 5133, 5134, 5135, 5136, 5137, 5138, 5139, 5140, 5141, 5142, 5143,
  5144, 5145, 5146, 5147, 5148, 5149, 5150, 5151, 6144, 6145, 6146, 6147, 6148, 6149, 6150, 6151, 6152, 6153, 6154, 6155, 6156, 6157, 6158, 6159,
  5152, 5153, 5154, 5155, 5156, 5157, 5158, 5159, 5160, 5161, 5162, 5163, 5164, 5165, 5166, 5167, 5168, 5169, 5170, 5171, 5172, 5173, 5174, 5175,
  5176, 5177, 5178, 5179, 5180, 5181, 5182, 5183, 7168, 7169, 7170, 7171, 7172, 7173, 7174, 7175, 7176, 7177, 7178, 7179, 7180, 7181, 7182, 7183,
  3072, 3073, 3074, 3075, 3076, 3077, 3078, 3079, 3080, 3081, 3082, 3083, 3084, 3085, 3086, 3087, 3088, 3089, 3090, 3091, 3092, 3093, 3094, 3095,
  3096, 3097, 3098, 3099, 3100, 3101, 3102, 3103, 3104, 3105, 3106, 3107, 3108, 3109, 3110, 3111, 3112, 3113, 3114, 3115, 3116, 3117, 3118, 3119,
  3120, 3121, 3122, 3123, 3124, 3125, 3126, 3127, 3128, 3129, 3130, 3131, 3132, 3133, 3134, 3135, 7184, 7185, 7186, 7187, 7188, 7189, 7190, 7191,
  7192, 7193, 7194, 7195, 7196, 7197, 7198, 7199, 4096, 4097, 4098, 4099, 4100, 4101, 4102, 4103, 4104, 4105, 4106, 4107, 4108, 4109, 4110, 4111,
  4112, 4113, 4114, 4115, 4116, 4117, 4118, 4119, 4120, 4121, 4122, 4123, 4124, 4125, 4126, 4127, 4128, 4129, 4130, 4131, 4132, 4133, 4134, 4135,
  4136, 4137, 4138, 4139, 4140, 4141, 4142, 4143, 4144, 4145, 4146, 4147, 4148, 4149, 4150, 4151, 4152, 4153, 4154, 4155, 4156, 4157, 4158, 4159,
};
__device__ __forceinline__ const FParams& args() {
  typedef const __attribute__((address_space(4))) FParams* KP;
  KP p = (KP)__builtin_amdgcn_kernarg_segment_ptr(); asm volatile("" : "+s"(p)); return *(const FParams*)p;
}
__device__ __forceinline__ void wait_until(unsigned t) { while ((int)(t - (unsigned)__builtin_amdgcn_s_memrealtime()) > 0) __builtin_amdgcn_s_sleep(8); }
template <int AUX = 0>
__device__ __forceinline__ void touch(const void* base, int bytes, int voff, int soff, char* lds) {
  __builtin_amdgcn_raw_ptr_buffer_load_lds(__builtin_amdgcn_make_buffer_rsrc((void*)base, 0, bytes, 0x00020000), (__attribute__((address_space(3))) void*)(lds + wg::LDS_USE + 1024), 4, voff, soff, 0, AUX);
}
__device__ __forceinline__ void worker(const FParams& F, char* lds, const int w0) {
#define lane opaque_lane()
#define tid (64 * w + opaque_lane())
  const int grp = (blockIdx.x & 7) - 3, slot = blockIdx.x >> 3;
#define PARV __ballot(F.par[128 + lane] != 0u), __ballot(F.par[192 + lane] != 0u)
#define TLOG(t, e)
  { int w = w0;
    const unsigned t0 = (unsigned)__builtin_amdgcn_s_memrealtime();
    TLOG(1024 + grp * 32 + slot, 0);
    if (grp != 2) {
      if (grp > 2) wait_until(t0 + F.stag);
      panel_task(F, (grp < 2 ? grp + 1 : 0) * 32 + slot, (grp - (grp > 2)) * 32 + slot, grp == 4 ? 64 : 0, grp == 3 ? 64 : 128, w);
    } else {
      const int id = slot * 4 + w, tz = opaque_lane();
#pragma unroll
      for (int q = 0; q < 4; ++q) {
        const u16* base = q == 0 ? F.Wk : (q == 1 ? F.Wo : (q == 2 ? F.Wq : F.Wv));
        const int o3 = (384 - (int)(size_t)base) & 896, o63 = (8064 - (int)(size_t)base) & 8064;
        touch(base, DM * HDA * 2, (tz * 128 + id) * 1024 + o3, 0, lds);
        if (!(q & 1)) { touch(base, DM * HDA * 2, (tz * 128 + id) * 1024 + (o3 ^ 128), 0, lds); touch(base, DM * HDA * 2, tz < 16 ? ((tz >> 1) * 128 + id) * 8192 + (o63 ^ ((tz & 1) << 7)) : 0x7ffffff0, 0, lds); }
      }
      if (F.eps & 4) for (int q = 0; q < 8; ++q) touch(F.Wo, DM * HDA * 2, tz * 128, (q * 128 + id) * 8192, lds);
#pragma unroll 1
      for (int q = (F.eps & 8) ? 4 : 0; q < 4; ++q) { wait_until(t0 + (F.px >> 16) + (((q * 128 + id) * (F.px & 0xffff)) >> 6)); touch(F.Ax, S_ * DM * 2, tz * 128, ((id & 1) * 256 + q * 64 + (id >> 1)) * 8192, lds); }
#pragma unroll 1
      for (int q = (F.eps & 2) ? 10 : 0; q < 12; ++q) {
        wait_until(t0 + (F.pb >> 16) + (((q * 128 + id) * (F.pb & 0xffff)) >> 6));
        if (q < 4) touch<TBAUX>(F.Bx, S_ * DB * 2, tz * 128, (q * 128 + id) * 8192, lds);
        else if (q < 10) touch<TBAUX>(F.wqkvB, DB * NQKVB * 2, (tz >> 4) * NQKVB * 2 + (tz & 15) * 128, (((q & 1) * 128 + id) * 4 * NQKVB + ((q - 4) >> 1) * 1024) * 2, lds);
        else touch<TBAUX>(F.woB, DB * DB * 2, tz * 128, ((q - 10) * 128 + id) * 8192, lds);
        if (q == 5 && !(F.eps & 1)) break;
      }
    }
    TLOG(1024 + grp * 32 + slot, 1);
    if (grp < 2) {
      const FParams& F = args();
      asm volatile("" : "+s"(w));
      out_unit(F, __ballot(F.par[192 + lane] != 0u), 2 * grp + (slot >> 4), slot & 15, w);
      TLOG(1280 + grp * 32 + slot, 1);
      return;
    } }
  Ctl* ctl = F.ctl;
  int* sh = (int*)(lds + wg::LDS_USE);
  int next = 2 * 1024 + (grp - 2) * 32 + slot;
  for (;;) {
    const FParams& F = args();
    int w = w0; asm volatile("" : "+s"(w));
    if (next < 0) {
      if (tid == 0) sh[0] = (int)__hip_atomic_fetch_add(&ctl->qhead[0], 1u, RLX_AGENT);
      __syncthreads();
      const int t = __builtin_amdgcn_readfirstlane(sh[0]);
      __syncthreads();
      { if (t >= NTASK) return; next = TASKS[t]; }
    }
    const int code = __builtin_amdgcn_readfirstlane(next); next = -1;
    const int tk = code >> 10, u = code & 1023;
    const u16 *A = nullptr, *Bt = nullptr; void* C = nullptr; int lda = 0, ldb = 0, ldc = 0, nk = 32, r = u >> 2, n = u & 3, nsig = 1;
    const unsigned* wp = nullptr; unsigned need = 0; unsigned* sig = nullptr;
    if (tk == 2) {
      n = u >> 3; r = 2 * (u & 7);
      A = F.Bx + (size_t)r * 128 * DB; lda = DB; Bt = F.wqkvB + n * 256; ldb = NQKVB;
      C = F.qkvB + (size_t)r * 128 * NQKVB + n * 256; ldc = NQKVB; sig = &ctl->cntB[r][0]; nsig = 2;
      if (u < 32 && !(F.eps & 1)) {
        const int id = u * 4 + w, tz = opaque_lane();
#pragma unroll
        for (int q = 6; q < 10; ++q) touch(F.wqkvB, DB * NQKVB * 2, (tz >> 4) * NQKVB * 2 + (tz & 15) * 128, (((q & 1) * 128 + id) * 4 * NQKVB + ((q - 4) >> 1) * 1024) * 2, lds);
        touch(F.woB, DB * DB * 2, tz * 128, id * 8192, lds); touch(F.woB, DB * DB * 2, tz * 128, (128 + id) * 8192, lds);
      }
    } else if (tk == 3) {
      need = 12; if (lane < 2 && r - lane >= 0) wp = &ctl->cntB[r - lane][0]; sig = &ctl->cntAB[r][0];
    } else if (tk == 4) {
      A = F.qkvB + (size_t)r * 128 * NQKVB; lda = NQKVB; Bt = F.woB + n * 256; ldb = DB;
      C = F.out + (size_t)r * 128 * 2 * DM + DM + n * 256; ldc = 2 * DM; need = 4; if (lane == 0) wp = &ctl->cntAB[r][0];
    } else if (tk == 5) {
      r = u >> 3; n = u & 7; sig = &ctl->cntO[4 * r][0]; nsig = 4; need = 8 * r + 8;
      const int lz = opaque_lane(); if (lz < 12) wp = &ctl->prg[((lz >> 2) + (lz >= 8 && r >= 8)) * 32 + 4 * n + (lz & 3)][0];
    } else if (tk == 6) {
      A = F.qkvA + r * 4 * QBLK; lda = QBLK / 32; Bt = F.Wo + n * 256; ldb = DM; nk = HDA / 32;
      C = F.out + (size_t)r * 128 * 2 * DM + n * 256; ldc = 2 * DM; need = 8; if (lane == 0) wp = &ctl->cntO[4 * r][0];
    } else {
      attn_stream(F, PARV, u >> 3, u & 7, lds, w);
      TLOG(lastt, 1);
      return;
    }
    if (need) wg_wait(wp, need, ctl, w);
    if (tk == 5) {
      attnA_block(F, PARV, r, 4 * n + w, (u16*)(lds + w * 16384), opaque_lane(), w, (unsigned*)sh + 4);
      asm volatile("s_waitcnt vmcnt(0)" ::: "memory");
      __syncthreads();
    } else if (tk == 3) {
      wait_until((unsigned)__builtin_amdgcn_s_memrealtime() + u * F.abd);
      const __amdgpu_buffer_rsrc_t rsO = __builtin_amdgcn_make_buffer_rsrc(F.qkvB, 0, S_ * NQKVB * 2, 0x00020000);
#pragma unroll 1
      for (int it = 0; it < 2; ++it) attnB_16(F.qkvB, rsO, r * 128 + n * 32 + it * 16, w);
      asm volatile("s_waitcnt vmcnt(0)" ::: "memory");
      __syncthreads();
    } else if (tk == 2) wg::gemm_tile<256>(A, lda, Bt, ldb, nk, C, ldc, false, false, F.par + 192, lds, w, (F.eps >> 16) & 255, (F.eps & 16) ? u & 31 : -1);
    else wg::gemm_tile<128>(A, lda, Bt, ldb, nk, C, ldc, tk == 6, false, F.par + 192, lds, w, tk == 4 ? (F.eps >> 8) & 255 : 0);
    if (sig && tid < nsig) __hip_atomic_fetch_add(sig + tid * 32, 1u, RLX_AGENT);
  }
}

#undef lane
#undef tid
__device__ __forceinline__ unsigned rtnow() { return (unsigned)__builtin_amdgcn_s_memrealtime(); }
__device__ __forceinline__ void wait_rt(unsigned t) {
  int rem = (int)(t - rtnow());
  if (rem > 20000) return;
  while (rem > 12) { __builtin_amdgcn_s_sleep(2); rem = (int)(t - rtnow()); }
  while (rem > 0) rem = (int)(t - rtnow());
}
#define MBASE(s) ((s) * MREG + (int)((parm >> (s)) & 1) * 8192)
#define VBASE(s) ((s) * OREG + (int)((parv >> (s)) & 1) * 8192)
#define OBASE(s) ((s) * OREG + (int)((paro >> (s)) & 1) * 8192)
constexpr int RECO = 32768;
constexpr int ALM = RECO + 1024;
__device__ __forceinline__ float rowmax16(float f) { f = fmaxf(f, dpp_ror<8>(f)); f = fmaxf(f, dpp_ror<4>(f)); f = fmaxf(f, dpp_ror<2>(f)); return fmaxf(f, dpp_ror<1>(f)); }
__device__ __forceinline__ void al_eval(unsigned v, unsigned& t) {
  const bool set = (v >> 16) == 1u;
  if (__builtin_expect(((unsigned)__ballot(set) & 0xffffu) == 0, 1)) return;
  const int d = (short)(v - t);
  t += __builtin_amdgcn_readfirstlane((int)rowmax16(set && d > 0 && d < 1000 ? (float)d : 0.f));
}
__device__ __forceinline__ void rec_get(unsigned& recA, unsigned& recD, __amdgpu_buffer_rsrc_t rs, int off, unsigned* tmo, int naps = 1) {
#pragma unroll 1
  for (unsigned fails = 0;; ++fails) {
    const v2u rec = __builtin_amdgcn_raw_buffer_load_b64(rs, off, 0, 16);
    recA = __builtin_amdgcn_readfirstlane(rec.x); recD = __builtin_amdgcn_readfirstlane(rec.y);
    if ((recA != 0xffffffffu && recD != 0xffffffffu) || spin_fail(fails, tmo, 1u)) break;
    if (naps > 1) __builtin_amdgcn_s_sleep(6); else __builtin_amdgcn_s_sleep(2);
  }
}

#define QK_LOADER \
          "s_nop 4\n\t" \
          "s_add_u32 m0, %[lw], 0\n\ts_xor_b32 %[t], %[kb], 0x0\n\tbuffer_load_dwordx4 %[go], %[rk], %[t] offen" QK_NTk " lds\n\ts_add_u32 m0, %[lw], 1024\n\ts_xor_b32 %[t], %[kb], 0x10000\n\tbuffer_load_dwordx4 %[go], %[rk], %[t] offen" QK_NTk " lds\n\ts_add_u32 m0, %[lw], 2048\n\ts_xor_b32 %[t], %[kb], 0x20000\n\tbuffer_load_dwordx4 %[go], %[rk], %[t] offen" QK_NTk " lds\n\ts_add_u32 m0, %[lw], 3072\n\ts_xor_b32 %[t], %[kb], 0x30000\n\tbuffer_load_dwordx4 %[go], %[rk], %[t] offen" QK_NTk " lds\n\ts_add_u32 m0, %[lw], 4096\n\ts_xor_b32 %[t], %[kb], 0x40000\n\tbuffer_load_dwordx4 %[go], %[rk], %[t] offen" QK_NTk " lds\n\ts_add_u32 m0, %[lw], 5120\n\ts_xor_b32 %[t], %[kb], 0x50000\n\tbuffer_load_dwordx4 %[go], %[rk], %[t] offen" QK_NTk " lds\n\ts_add_u32 m0, %[lw], 6144\n\ts_xor_b32 %[t], %[kb], 0x60000\n\tbuffer_load_dwordx4 %[go], %[rk], %[t] offen" QK_NTk " lds\n\ts_add_u32 m0, %[lw], 7168\n\ts_xor_b32 %[t], %[kb], 0x70000\n\tbuffer_load_dwordx4 %[go], %[rk], %[t] offen" QK_NTk " lds\n\t" \
          "s_add_u32 m0, %[lw], 8192\n\ts_xor_b32 %[t], %[kb], 0x80000\n\tbuffer_load_dwordx4 %[go], %[rk], %[t] offen" QK_NTk " lds\n\ts_add_u32 m0, %[lw], 9216\n\ts_xor_b32 %[t], %[kb], 0x90000\n\tbuffer_load_dwordx4 %[go], %[rk], %[t] offen" QK_NTk " lds\n\ts_add_u32 m0, %[lw], 10240\n\ts_xor_b32 %[t], %[kb], 0xa0000\n\tbuffer_load_dwordx4 %[go], %[rk], %[t] offen" QK_NTk " lds\n\ts_add_u32 m0, %[lw], 11264\n\ts_xor_b32 %[t], %[kb], 0xb0000\n\tbuffer_load_dwordx4 %[go], %[rk], %[t] offen" QK_NTk " lds\n\ts_add_u32 m0, %[lw], 12288\n\ts_xor_b32 %[t], %[kb], 0xc0000\n\tbuffer_load_dwordx4 %[go], %[rk], %[t] offen" QK_NTk " lds\n\ts_add_u32 m0, %[lw], 13312\n\ts_xor_b32 %[t], %[kb], 0xd0000\n\tbuffer_load_dwordx4 %[go], %[rk], %[t] offen" QK_NTk " lds\n\ts_add_u32 m0, %[lw], 14336\n\ts_xor_b32 %[t], %[kb], 0xe0000\n\tbuffer_load_dwordx4 %[go], %[rk], %[t] offen" QK_NTk " lds\n\ts_add_u32 m0, %[lw], 15360\n\ts_xor_b32 %[t], %[kb], 0xf0000\n\tbuffer_load_dwordx4 %[go], %[rk], %[t] offen" QK_NTk " lds\n\t" \
          "s_add_u32 m0, %[lw], 16384\n\ts_xor_b32 %[t], %[kb], 0x100000\n\tbuffer_load_dwordx4 %[go], %[rk], %[t] offen" QK_NTk " lds\n\ts_add_u32 m0, %[lw], 17408\n\ts_xor_b32 %[t], %[kb], 0x110000\n\tbuffer_load_dwordx4 %[go], %[rk], %[t] offen" QK_NTk " lds\n\ts_add_u32 m0, %[lw], 18432\n\ts_xor_b32 %[t], %[kb], 0x120000\n\tbuffer_load_dwordx4 %[go], %[rk], %[t] offen" QK_NTk " lds\n\ts_add_u32 m0, %[lw], 19456\n\ts_xor_b32 %[t], %[kb], 0x130000\n\tbuffer_load_dwordx4 %[go], %[rk], %[t] offen" QK_NTk " lds\n\ts_add_u32 m0, %[lw], 20480\n\ts_xor_b32 %[t], %[kb], 0x140000\n\tbuffer_load_dwordx4 %[go], %[rk], %[t] offen" QK_NTk " lds\n\ts_add_u32 m0, %[lw], 21504\n\ts_xor_b32 %[t], %[kb], 0x150000\n\tbuffer_load_dwordx4 %[go], %[rk], %[t] offen" QK_NTk " lds\n\ts_add_u32 m0, %[lw], 22528\n\ts_xor_b32 %[t], %[kb], 0x160000\n\tbuffer_load_dwordx4 %[go], %[rk], %[t] offen" QK_NTk " lds\n\ts_add_u32 m0, %[lw], 23552\n\ts_xor_b32 %[t], %[kb], 0x170000\n\tbuffer_load_dwordx4 %[go], %[rk], %[t] offen" QK_NTk " lds\n\t" \
          "s_add_u32 m0, %[lw], 24576\n\ts_xor_b32 %[t], %[kb], 0x180000\n\tbuffer_load_dwordx4 %[go], %[rk], %[t] offen" QK_NTk " lds\n\ts_add_u32 m0, %[lw], 25600\n\ts_xor_b32 %[t], %[kb], 0x190000\n\tbuffer_load_dwordx4 %[go], %[rk], %[t] offen" QK_NTk " lds\n\ts_add_u32 m0, %[lw], 26624\n\ts_xor_b32 %[t], %[kb], 0x1a0000\n\tbuffer_load_dwordx4 %[go], %[rk], %[t] offen" QK_NTk " lds\n\ts_add_u32 m0, %[lw], 27648\n\ts_xor_b32 %[t], %[kb], 0x1b0000\n\tbuffer_load_dwordx4 %[go], %[rk], %[t] offen" QK_NTk " lds\n\ts_add_u32 m0, %[lw], 28672\n\ts_xor_b32 %[t], %[kb], 0x1c0000\n\tbuffer_load_dwordx4 %[go], %[rk], %[t] offen" QK_NTk " lds\n\ts_add_u32 m0, %[lw], 29696\n\ts_xor_b32 %[t], %[kb], 0x1d0000\n\tbuffer_load_dwordx4 %[go], %[rk], %[t] offen" QK_NTk " lds\n\ts_add_u32 m0, %[lw], 30720\n\ts_xor_b32 %[t], %[kb], 0x1e0000\n\tbuffer_load_dwordx4 %[go], %[rk], %[t] offen" QK_NTk " lds\n\ts_add_u32 m0, %[lw], 31744\n\ts_xor_b32 %[t], %[kb], 0x1f0000\n\tbuffer_load_dwordx4 %[go], %[rk], %[t] offen" QK_NTk " lds\n\t" \
          "s_add_u32 m0, %[lw], 32768\n\ts_xor_b32 %[t], %[kb], 0x200000\n\tbuffer_load_dwordx4 %[go], %[rk], %[t] offen" QK_NTk " lds\n\ts_add_u32 m0, %[lw], 33792\n\ts_xor_b32 %[t], %[kb], 0x210000\n\tbuffer_load_dwordx4 %[go], %[rk], %[t] offen" QK_NTk " lds\n\ts_add_u32 m0, %[lw], 34816\n\ts_xor_b32 %[t], %[kb], 0x220000\n\tbuffer_load_dwordx4 %[go], %[rk], %[t] offen" QK_NTk " lds\n\ts_add_u32 m0, %[lw], 35840\n\ts_xor_b32 %[t], %[kb], 0x230000\n\tbuffer_load_dwordx4 %[go], %[rk], %[t] offen" QK_NTk " lds\n\ts_add_u32 m0, %[lw], 36864\n\ts_xor_b32 %[t], %[kb], 0x240000\n\tbuffer_load_dwordx4 %[go], %[rk], %[t] offen" QK_NTk " lds\n\ts_add_u32 m0, %[lw], 37888\n\ts_xor_b32 %[t], %[kb], 0x250000\n\tbuffer_load_dwordx4 %[go], %[rk], %[t] offen" QK_NTk " lds\n\ts_add_u32 m0, %[lw], 38912\n\ts_xor_b32 %[t], %[kb], 0x260000\n\tbuffer_load_dwordx4 %[go], %[rk], %[t] offen" QK_NTk " lds\n\ts_add_u32 m0, %[lw], 39936\n\ts_xor_b32 %[t], %[kb], 0x270000\n\tbuffer_load_dwordx4 %[go], %[rk], %[t] offen" QK_NTk " lds\n\t" \
          "s_waitcnt vmcnt(32)\n\tds_read_b64_tr_b16 v[136:137], %[rd0] offset:0\n\tds_read_b64_tr_b16 v[138:139], %[rd0] offset:128\n\tds_read_b64_tr_b16 v[140:141], %[rd0] offset:4096\n\tds_read_b64_tr_b16 v[142:143], %[rd0] offset:4224\n\tds_read_b64_tr_b16 a[64:65], %[rd1] offset:0\n\tds_read_b64_tr_b16 a[66:67], %[rd1] offset:128\n\tds_read_b64_tr_b16 a[68:69], %[rd1] offset:4096\n\tds_read_b64_tr_b16 a[70:71], %[rd1] offset:4224\n\tds_read_b64_tr_b16 a[128:129], %[rd2] offset:0\n\tds_read_b64_tr_b16 a[130:131], %[rd2] offset:128\n\tds_read_b64_tr_b16 a[132:133], %[rd2] offset:4096\n\tds_read_b64_tr_b16 a[134:135], %[rd2] offset:4224\n\tds_read_b64_tr_b16 a[136:137], %[rd3] offset:0\n\tds_read_b64_tr_b16 a[138:139], %[rd3] offset:128\n\tds_read_b64_tr_b16 a[140:141], %[rd3] offset:4096\n\tds_read_b64_tr_b16 a[142:143], %[rd3] offset:4224\n\ts_waitcnt lgkmcnt(0)\n\t" \
          "s_add_u32 m0, %[lw], 0\n\ts_xor_b32 %[t], %[kb], 0x280000\n\tbuffer_load_dwordx4 %[go], %[rk], %[t] offen" QK_NTk " lds\n\ts_add_u32 m0, %[lw], 1024\n\ts_xor_b32 %[t], %[kb], 0x290000\n\tbuffer_load_dwordx4 %[go], %[rk], %[t] offen" QK_NTk " lds\n\ts_add_u32 m0, %[lw], 2048\n\ts_xor_b32 %[t], %[kb], 0x2a0000\n\tbuffer_load_dwordx4 %[go], %[rk], %[t] offen" QK_NTk " lds\n\ts_add_u32 m0, %[lw], 3072\n\ts_xor_b32 %[t], %[kb], 0x2b0000\n\tbuffer_load_dwordx4 %[go], %[rk], %[t] offen" QK_NTk " lds\n\ts_add_u32 m0, %[lw], 4096\n\ts_xor_b32 %[t], %[kb], 0x2c0000\n\tbuffer_load_dwordx4 %[go], %[rk], %[t] offen" QK_NTk " lds\n\ts_add_u32 m0, %[lw], 5120\n\ts_xor_b32 %[t], %[kb], 0x2d0000\n\tbuffer_load_dwordx4 %[go], %[rk], %[t] offen" QK_NTk " lds\n\ts_add_u32 m0, %[lw], 6144\n\ts_xor_b32 %[t], %[kb], 0x2e0000\n\tbuffer_load_dwordx4 %[go], %[rk], %[t] offen" QK_NTk " lds\n\ts_add_u32 m0, %[lw], 7168\n\ts_xor_b32 %[t], %[kb], 0x2f0000\n\tbuffer_load_dwordx4 %[go], %[rk], %[t] offen" QK_NTk " lds\n\t" \
          "s_waitcnt vmcnt(32)\n\tds_read_b64_tr_b16 v[40:41], %[rd0] offset:8192\n\tds_read_b64_tr_b16 v[42:43], %[rd0] offset:8320\n\tds_read_b64_tr_b16 v[44:45], %[rd0] offset:12288\n\tds_read_b64_tr_b16 v[46:47], %[rd0] offset:12416\n\tds_read_b64_tr_b16 a[72:73], %[rd1] offset:8192\n\tds_read_b64_tr_b16 a[74:75], %[rd1] offset:8320\n\tds_read_b64_tr_b16 a[76:77], %[rd1] offset:12288\n\tds_read_b64_tr_b16 a[78:79], %[rd1] offset:12416\n\tds_read_b64_tr_b16 a[144:145], %[rd2] offset:8192\n\tds_read_b64_tr_b16 a[146:147], %[rd2] offset:8320\n\tds_read_b64_tr_b16 a[148:149], %[rd2] offset:12288\n\tds_read_b64_tr_b16 a[150:151], %[rd2] offset:12416\n\tds_read_b64_tr_b16 a[152:153], %[rd3] offset:8192\n\tds_read_b64_tr_b16 a[154:155], %[rd3] offset:8320\n\tds_read_b64_tr_b16 a[156:157], %[rd3] offset:12288\n\tds_read_b64_tr_b16 a[158:159], %[rd3] offset:12416\n\ts_waitcnt lgkmcnt(0)\n\t" \
          "s_add_u32 m0, %[lw], 8192\n\ts_xor_b32 %[t], %[kb], 0x300000\n\tbuffer_load_dwordx4 %[go], %[rk], %[t] offen" QK_NTk " lds\n\ts_add_u32 m0, %[lw], 9216\n\ts_xor_b32 %[t], %[kb], 0x310000\n\tbuffer_load_dwordx4 %[go], %[rk], %[t] offen" QK_NTk " lds\n\ts_add_u32 m0, %[lw], 10240\n\ts_xor_b32 %[t], %[kb], 0x320000\n\tbuffer_load_dwordx4 %[go], %[rk], %[t] offen" QK_NTk " lds\n\ts_add_u32 m0, %[lw], 11264\n\ts_xor_b32 %[t], %[kb], 0x330000\n\tbuffer_load_dwordx4 %[go], %[rk], %[t] offen" QK_NTk " lds\n\ts_add_u32 m0, %[lw], 12288\n\ts_xor_b32 %[t], %[kb], 0x340000\n\tbuffer_load_dwordx4 %[go], %[rk], %[t] offen" QK_NTk " lds\n\ts_add_u32 m0, %[lw], 13312\n\ts_xor_b32 %[t], %[kb], 0x350000\n\tbuffer_load_dwordx4 %[go], %[rk], %[t] offen" QK_NTk " lds\n\ts_add_u32 m0, %[lw], 14336\n\ts_xor_b32 %[t], %[kb], 0x360000\n\tbuffer_load_dwordx4 %[go], %[rk], %[t] offen" QK_NTk " lds\n\ts_add_u32 m0, %[lw], 15360\n\ts_xor_b32 %[t], %[kb], 0x370000\n\tbuffer_load_dwordx4 %[go], %[rk], %[t] offen" QK_NTk " lds\n\t" \
          "s_waitcnt vmcnt(32)\n\tds_read_b64_tr_b16 v[144:145], %[rd0] offset:16384\n\tds_read_b64_tr_b16 v[146:147], %[rd0] offset:16512\n\tds_read_b64_tr_b16 v[148:149], %[rd0] offset:20480\n\tds_read_b64_tr_b16 v[150:151], %[rd0] offset:20608\n\tds_read_b64_tr_b16 a[80:81], %[rd1] offset:16384\n\tds_read_b64_tr_b16 a[82:83], %[rd1] offset:16512\n\tds_read_b64_tr_b16 a[84:85], %[rd1] offset:20480\n\tds_read_b64_tr_b16 a[86:87], %[rd1] offset:20608\n\tds_read_b64_tr_b16 a[160:161], %[rd2] offset:16384\n\tds_read_b64_tr_b16 a[162:163], %[rd2] offset:16512\n\tds_read_b64_tr_b16 a[164:165], %[rd2] offset:20480\n\tds_read_b64_tr_b16 a[166:167], %[rd2] offset:20608\n\tds_read_b64_tr_b16 a[168:169], %[rd3] offset:16384\n\tds_read_b64_tr_b16 a[170:171], %[rd3] offset:16512\n\tds_read_b64_tr_b16 a[172:173], %[rd3] offset:20480\n\tds_read_b64_tr_b16 a[174:175], %[rd3] offset:20608\n\ts_waitcnt lgkmcnt(0)\n\t" \
          "s_add_u32 m0, %[lw], 16384\n\ts_xor_b32 %[t], %[kb], 0x380000\n\tbuffer_load_dwordx4 %[go], %[rk], %[t] offen" QK_NTk " lds\n\ts_add_u32 m0, %[lw], 17408\n\ts_xor_b32 %[t], %[kb], 0x390000\n\tbuffer_load_dwordx4 %[go], %[rk], %[t] offen" QK_NTk " lds\n\ts_add_u32 m0, %[lw], 18432\n\ts_xor_b32 %[t], %[kb], 0x3a0000\n\tbuffer_load_dwordx4 %[go], %[rk], %[t] offen" QK_NTk " lds\n\ts_add_u32 m0, %[lw], 19456\n\ts_xor_b32 %[t], %[kb], 0x3b0000\n\tbuffer_load_dwordx4 %[go], %[rk], %[t] offen" QK_NTk " lds\n\ts_add_u32 m0, %[lw], 20480\n\ts_xor_b32 %[t], %[kb], 0x3c0000\n\tbuffer_load_dwordx4 %[go], %[rk], %[t] offen" QK_NTk " lds\n\ts_add_u32 m0, %[lw], 21504\n\ts_xor_b32 %[t], %[kb], 0x3d0000\n\tbuffer_load_dwordx4 %[go], %[rk], %[t] offen" QK_NTk " lds\n\ts_add_u32 m0, %[lw], 22528\n\ts_xor_b32 %[t], %[kb], 0x3e0000\n\tbuffer_load_dwordx4 %[go], %[rk], %[t] offen" QK_NTk " lds\n\ts_add_u32 m0, %[lw], 23552\n\ts_xor_b32 %[t], %[kb], 0x3f0000\n\tbuffer_load_dwordx4 %[go], %[rk], %[t] offen" QK_NTk " lds\n\t" \
          "s_waitcnt vmcnt(32)\n\tds_read_b64_tr_b16 v[80:81], %[rd0] offset:24576\n\tds_read_b64_tr_b16 v[82:83], %[rd0] offset:24704\n\tds_read_b64_tr_b16 v[84:85], %[rd0] offset:28672\n\tds_read_b64_tr_b16 v[86:87], %[rd0] offset:28800\n\tds_read_b64_tr_b16 a[88:89], %[rd1] offset:24576\n\tds_read_b64_tr_b16 a[90:91], %[rd1] offset:24704\n\tds_read_b64_tr_b16 a[92:93], %[rd1] offset:28672\n\tds_read_b64_tr_b16 a[94:95], %[rd1] offset:28800\n\tds_read_b64_tr_b16 a[176:177], %[rd2] offset:24576\n\tds_read_b64_tr_b16 a[178:179], %[rd2] offset:24704\n\tds_read_b64_tr_b16 a[180:181], %[rd2] offset:28672\n\tds_read_b64_tr_b16 a[182:183], %[rd2] offset:28800\n\tds_read_b64_tr_b16 a[184:185], %[rd3] offset:24576\n\tds_read_b64_tr_b16 a[186:187], %[rd3] offset:24704\n\tds_read_b64_tr_b16 a[188:189], %[rd3] offset:28672\n\tds_read_b64_tr_b16 a[190:191], %[rd3] offset:28800\n\ts_waitcnt lgkmcnt(0)\n\t" \
          "s_add_u32 m0, %[lw], 24576\n\ts_xor_b32 %[t], %[kb], 0x380000\n\tbuffer_load_dwordx4 %[go], %[rq], %[t] offen" QK_NTq " lds\n\ts_add_u32 m0, %[lw], 25600\n\ts_xor_b32 %[t], %[kb], 0x390000\n\tbuffer_load_dwordx4 %[go], %[rq], %[t] offen" QK_NTq " lds\n\ts_add_u32 m0, %[lw], 26624\n\ts_xor_b32 %[t], %[kb], 0x3a0000\n\tbuffer_load_dwordx4 %[go], %[rq], %[t] offen" QK_NTq " lds\n\ts_add_u32 m0, %[lw], 27648\n\ts_xor_b32 %[t], %[kb], 0x3b0000\n\tbuffer_load_dwordx4 %[go], %[rq], %[t] offen" QK_NTq " lds\n\ts_add_u32 m0, %[lw], 28672\n\ts_xor_b32 %[t], %[kb], 0x3c0000\n\tbuffer_load_dwordx4 %[go], %[rq], %[t] offen" QK_NTq " lds\n\ts_add_u32 m0, %[lw], 29696\n\ts_xor_b32 %[t], %[kb], 0x3d0000\n\tbuffer_load_dwordx4 %[go], %[rq], %[t] offen" QK_NTq " lds\n\ts_add_u32 m0, %[lw], 30720\n\ts_xor_b32 %[t], %[kb], 0x3e0000\n\tbuffer_load_dwordx4 %[go], %[rq], %[t] offen" QK_NTq " lds\n\ts_add_u32 m0, %[lw], 31744\n\ts_xor_b32 %[t], %[kb], 0x3f0000\n\tbuffer_load_dwordx4 %[go], %[rq], %[t] offen" QK_NTq " lds\n\t" \
          "s_waitcnt vmcnt(32)\n\tds_read_b64_tr_b16 v[152:153], %[rd0] offset:32768\n\tds_read_b64_tr_b16 v[154:155], %[rd0] offset:32896\n\tds_read_b64_tr_b16 v[156:157], %[rd0] offset:36864\n\tds_read_b64_tr_b16 v[158:159], %[rd0] offset:36992\n\tds_read_b64_tr_b16 a[96:97], %[rd1] offset:32768\n\tds_read_b64_tr_b16 a[98:99], %[rd1] offset:32896\n\tds_read_b64_tr_b16 a[100:101], %[rd1] offset:36864\n\tds_read_b64_tr_b16 a[102:103], %[rd1] offset:36992\n\tds_read_b64_tr_b16 a[192:193], %[rd2] offset:32768\n\tds_read_b64_tr_b16 a[194:195], %[rd2] offset:32896\n\tds_read_b64_tr_b16 a[196:197], %[rd2] offset:36864\n\tds_read_b64_tr_b16 a[198:199], %[rd2] offset:36992\n\tds_read_b64_tr_b16 a[200:201], %[rd3] offset:32768\n\tds_read_b64_tr_b16 a[202:203], %[rd3] offset:32896\n\tds_read_b64_tr_b16 a[204:205], %[rd3] offset:36864\n\tds_read_b64_tr_b16 a[206:207], %[rd3] offset:36992\n\ts_waitcnt lgkmcnt(0)\n\t" \
          "s_add_u32 m0, %[lw], 32768\n\ts_xor_b32 %[t], %[kb], 0x300000\n\tbuffer_load_dwordx4 %[go], %[rq], %[t] offen" QK_NTq " lds\n\ts_add_u32 m0, %[lw], 33792\n\ts_xor_b32 %[t], %[kb], 0x310000\n\tbuffer_load_dwordx4 %[go], %[rq], %[t] offen" QK_NTq " lds\n\ts_add_u32 m0, %[lw], 34816\n\ts_xor_b32 %[t], %[kb], 0x320000\n\tbuffer_load_dwordx4 %[go], %[rq], %[t] offen" QK_NTq " lds\n\ts_add_u32 m0, %[lw], 35840\n\ts_xor_b32 %[t], %[kb], 0x330000\n\tbuffer_load_dwordx4 %[go], %[rq], %[t] offen" QK_NTq " lds\n\ts_add_u32 m0, %[lw], 36864\n\ts_xor_b32 %[t], %[kb], 0x340000\n\tbuffer_load_dwordx4 %[go], %[rq], %[t] offen" QK_NTq " lds\n\ts_add_u32 m0, %[lw], 37888\n\ts_xor_b32 %[t], %[kb], 0x350000\n\tbuffer_load_dwordx4 %[go], %[rq], %[t] offen" QK_NTq " lds\n\ts_add_u32 m0, %[lw], 38912\n\ts_xor_b32 %[t], %[kb], 0x360000\n\tbuffer_load_dwordx4 %[go], %[rq], %[t] offen" QK_NTq " lds\n\ts_add_u32 m0, %[lw], 39936\n\ts_xor_b32 %[t], %[kb], 0x370000\n\tbuffer_load_dwordx4 %[go], %[rq], %[t] offen" QK_NTq " lds\n\t" \
          "s_waitcnt vmcnt(32)\n\tds_read_b64_tr_b16 v[120:121], %[rd0] offset:0\n\tds_read_b64_tr_b16 v[122:123], %[rd0] offset:128\n\tds_read_b64_tr_b16 v[124:125], %[rd0] offset:4096\n\tds_read_b64_tr_b16 v[126:127], %[rd0] offset:4224\n\tds_read_b64_tr_b16 a[104:105], %[rd1] offset:0\n\tds_read_b64_tr_b16 a[106:107], %[rd1] offset:128\n\tds_read_b64_tr_b16 a[108:109], %[rd1] offset:4096\n\tds_read_b64_tr_b16 a[110:111], %[rd1] offset:4224\n\tds_read_b64_tr_b16 a[208:209], %[rd2] offset:0\n\tds_read_b64_tr_b16 a[210:211], %[rd2] offset:128\n\tds_read_b64_tr_b16 a[212:213], %[rd2] offset:4096\n\tds_read_b64_tr_b16 a[214:215], %[rd2] offset:4224\n\tds_read_b64_tr_b16 a[216:217], %[rd3] offset:0\n\tds_read_b64_tr_b16 a[218:219], %[rd3] offset:128\n\tds_read_b64_tr_b16 a[220:221], %[rd3] offset:4096\n\tds_read_b64_tr_b16 a[222:223], %[rd3] offset:4224\n\ts_waitcnt lgkmcnt(0)\n\t" \
          "s_add_u32 m0, %[lw], 0\n\ts_xor_b32 %[t], %[kb], 0x280000\n\tbuffer_load_dwordx4 %[go], %[rq], %[t] offen" QK_NTq " lds\n\ts_add_u32 m0, %[lw], 1024\n\ts_xor_b32 %[t], %[kb], 0x290000\n\tbuffer_load_dwordx4 %[go], %[rq], %[t] offen" QK_NTq " lds\n\ts_add_u32 m0, %[lw], 2048\n\ts_xor_b32 %[t], %[kb], 0x2a0000\n\tbuffer_load_dwordx4 %[go], %[rq], %[t] offen" QK_NTq " lds\n\ts_add_u32 m0, %[lw], 3072\n\ts_xor_b32 %[t], %[kb], 0x2b0000\n\tbuffer_load_dwordx4 %[go], %[rq], %[t] offen" QK_NTq " lds\n\ts_add_u32 m0, %[lw], 4096\n\ts_xor_b32 %[t], %[kb], 0x2c0000\n\tbuffer_load_dwordx4 %[go], %[rq], %[t] offen" QK_NTq " lds\n\ts_add_u32 m0, %[lw], 5120\n\ts_xor_b32 %[t], %[kb], 0x2d0000\n\tbuffer_load_dwordx4 %[go], %[rq], %[t] offen" QK_NTq " lds\n\ts_add_u32 m0, %[lw], 6144\n\ts_xor_b32 %[t], %[kb], 0x2e0000\n\tbuffer_load_dwordx4 %[go], %[rq], %[t] offen" QK_NTq " lds\n\ts_add_u32 m0, %[lw], 7168\n\ts_xor_b32 %[t], %[kb], 0x2f0000\n\tbuffer_load_dwordx4 %[go], %[rq], %[t] offen" QK_NTq " lds\n\t" \
          "s_waitcnt vmcnt(32)\n\tds_read_b64_tr_b16 a[16:17], %[rd0] offset:8192\n\tds_read_b64_tr_b16 a[18:19], %[rd0] offset:8320\n\tds_read_b64_tr_b16 a[20:21], %[rd0] offset:12288\n\tds_read_b64_tr_b16 a[22:23], %[rd0] offset:12416\n\tds_read_b64_tr_b16 a[112:113], %[rd1] offset:8192\n\tds_read_b64_tr_b16 a[114:115], %[rd1] offset:8320\n\tds_read_b64_tr_b16 a[116:117], %[rd1] offset:12288\n\tds_read_b64_tr_b16 a[118:119], %[rd1] offset:12416\n\tds_read_b64_tr_b16 a[224:225], %[rd2] offset:8192\n\tds_read_b64_tr_b16 a[226:227], %[rd2] offset:8320\n\tds_read_b64_tr_b16 a[228:229], %[rd2] offset:12288\n\tds_read_b64_tr_b16 a[230:231], %[rd2] offset:12416\n\tds_read_b64_tr_b16 a[232:233], %[rd3] offset:8192\n\tds_read_b64_tr_b16 a[234:235], %[rd3] offset:8320\n\tds_read_b64_tr_b16 a[236:237], %[rd3] offset:12288\n\tds_read_b64_tr_b16 a[238:239], %[rd3] offset:12416\n\ts_waitcnt lgkmcnt(0)\n\t" \
          "s_add_u32 m0, %[lw], 8192\n\ts_xor_b32 %[t], %[kb], 0x200000\n\tbuffer_load_dwordx4 %[go], %[rq], %[t] offen" QK_NTq " lds\n\ts_add_u32 m0, %[lw], 9216\n\ts_xor_b32 %[t], %[kb], 0x210000\n\tbuffer_load_dwordx4 %[go], %[rq], %[t] offen" QK_NTq " lds\n\ts_add_u32 m0, %[lw], 10240\n\ts_xor_b32 %[t], %[kb], 0x220000\n\tbuffer_load_dwordx4 %[go], %[rq], %[t] offen" QK_NTq " lds\n\ts_add_u32 m0, %[lw], 11264\n\ts_xor_b32 %[t], %[kb], 0x230000\n\tbuffer_load_dwordx4 %[go], %[rq], %[t] offen" QK_NTq " lds\n\ts_add_u32 m0, %[lw], 12288\n\ts_xor_b32 %[t], %[kb], 0x240000\n\tbuffer_load_dwordx4 %[go], %[rq], %[t] offen" QK_NTq " lds\n\ts_add_u32 m0, %[lw], 13312\n\ts_xor_b32 %[t], %[kb], 0x250000\n\tbuffer_load_dwordx4 %[go], %[rq], %[t] offen" QK_NTq " lds\n\ts_add_u32 m0, %[lw], 14336\n\ts_xor_b32 %[t], %[kb], 0x260000\n\tbuffer_load_dwordx4 %[go], %[rq], %[t] offen" QK_NTq " lds\n\ts_add_u32 m0, %[lw], 15360\n\ts_xor_b32 %[t], %[kb], 0x270000\n\tbuffer_load_dwordx4 %[go], %[rq], %[t] offen" QK_NTq " lds\n\t" \
          "s_waitcnt vmcnt(32)\n\tds_read_b64_tr_b16 a[56:57], %[rd0] offset:16384\n\tds_read_b64_tr_b16 a[58:59], %[rd0] offset:16512\n\tds_read_b64_tr_b16 a[60:61], %[rd0] offset:20480\n\tds_read_b64_tr_b16 a[62:63], %[rd0] offset:20608\n\tds_read_b64_tr_b16 a[120:121], %[rd1] offset:16384\n\tds_read_b64_tr_b16 a[122:123], %[rd1] offset:16512\n\tds_read_b64_tr_b16 a[124:125], %[rd1] offset:20480\n\tds_read_b64_tr_b16 a[126:127], %[rd1] offset:20608\n\tds_read_b64_tr_b16 a[240:241], %[rd2] offset:16384\n\tds_read_b64_tr_b16 a[242:243], %[rd2] offset:16512\n\tds_read_b64_tr_b16 a[244:245], %[rd2] offset:20480\n\tds_read_b64_tr_b16 a[246:247], %[rd2] offset:20608\n\tds_read_b64_tr_b16 a[248:249], %[rd3] offset:16384\n\tds_read_b64_tr_b16 a[250:251], %[rd3] offset:16512\n\tds_read_b64_tr_b16 a[252:253], %[rd3] offset:20480\n\tds_read_b64_tr_b16 a[254:255], %[rd3] offset:20608\n\ts_waitcnt lgkmcnt(0)\n\t" \
          "s_add_u32 m0, %[lw], 16384\n\ts_xor_b32 %[t], %[kb], 0x180000\n\tbuffer_load_dwordx4 %[go], %[rq], %[t] offen" QK_NTq " lds\n\ts_add_u32 m0, %[lw], 17408\n\ts_xor_b32 %[t], %[kb], 0x190000\n\tbuffer_load_dwordx4 %[go], %[rq], %[t] offen" QK_NTq " lds\n\ts_add_u32 m0, %[lw], 18432\n\ts_xor_b32 %[t], %[kb], 0x1a0000\n\tbuffer_load_dwordx4 %[go], %[rq], %[t] offen" QK_NTq " lds\n\ts_add_u32 m0, %[lw], 19456\n\ts_xor_b32 %[t], %[kb], 0x1b0000\n\tbuffer_load_dwordx4 %[go], %[rq], %[t] offen" QK_NTq " lds\n\ts_add_u32 m0, %[lw], 20480\n\ts_xor_b32 %[t], %[kb], 0x1c0000\n\tbuffer_load_dwordx4 %[go], %[rq], %[t] offen" QK_NTq " lds\n\ts_add_u32 m0, %[lw], 21504\n\ts_xor_b32 %[t], %[kb], 0x1d0000\n\tbuffer_load_dwordx4 %[go], %[rq], %[t] offen" QK_NTq " lds\n\ts_add_u32 m0, %[lw], 22528\n\ts_xor_b32 %[t], %[kb], 0x1e0000\n\tbuffer_load_dwordx4 %[go], %[rq], %[t] offen" QK_NTq " lds\n\ts_add_u32 m0, %[lw], 23552\n\ts_xor_b32 %[t], %[kb], 0x1f0000\n\tbuffer_load_dwordx4 %[go], %[rq], %[t] offen" QK_NTq " lds\n\t" \
          "s_waitcnt vmcnt(32)\n\tds_read_b64_tr_b16 a[24:25], %[rd0] offset:24576\n\tds_read_b64_tr_b16 a[26:27], %[rd0] offset:24704\n\tds_read_b64_tr_b16 a[28:29], %[rd0] offset:28672\n\tds_read_b64_tr_b16 a[30:31], %[rd0] offset:28800\n\tds_read_b64_tr_b16 a[32:33], %[rd1] offset:24576\n\tds_read_b64_tr_b16 a[34:35], %[rd1] offset:24704\n\tds_read_b64_tr_b16 a[36:37], %[rd1] offset:28672\n\tds_read_b64_tr_b16 a[38:39], %[rd1] offset:28800\n\tds_read_b64_tr_b16 a[40:41], %[rd2] offset:24576\n\tds_read_b64_tr_b16 a[42:43], %[rd2] offset:24704\n\tds_read_b64_tr_b16 a[44:45], %[rd2] offset:28672\n\tds_read_b64_tr_b16 a[46:47], %[rd2] offset:28800\n\tds_read_b64_tr_b16 a[48:49], %[rd3] offset:24576\n\tds_read_b64_tr_b16 a[50:51], %[rd3] offset:24704\n\tds_read_b64_tr_b16 a[52:53], %[rd3] offset:28672\n\tds_read_b64_tr_b16 a[54:55], %[rd3] offset:28800\n\ts_waitcnt lgkmcnt(0)\n\t" \
          "s_add_u32 m0, %[lw], 24576\n\ts_xor_b32 %[t], %[kb], 0x100000\n\tbuffer_load_dwordx4 %[go], %[rq], %[t] offen" QK_NTq " lds\n\ts_add_u32 m0, %[lw], 25600\n\ts_xor_b32 %[t], %[kb], 0x110000\n\tbuffer_load_dwordx4 %[go], %[rq], %[t] offen" QK_NTq " lds\n\ts_add_u32 m0, %[lw], 26624\n\ts_xor_b32 %[t], %[kb], 0x120000\n\tbuffer_load_dwordx4 %[go], %[rq], %[t] offen" QK_NTq " lds\n\ts_add_u32 m0, %[lw], 27648\n\ts_xor_b32 %[t], %[kb], 0x130000\n\tbuffer_load_dwordx4 %[go], %[rq], %[t] offen" QK_NTq " lds\n\ts_add_u32 m0, %[lw], 28672\n\ts_xor_b32 %[t], %[kb], 0x140000\n\tbuffer_load_dwordx4 %[go], %[rq], %[t] offen" QK_NTq " lds\n\ts_add_u32 m0, %[lw], 29696\n\ts_xor_b32 %[t], %[kb], 0x150000\n\tbuffer_load_dwordx4 %[go], %[rq], %[t] offen" QK_NTq " lds\n\ts_add_u32 m0, %[lw], 30720\n\ts_xor_b32 %[t], %[kb], 0x160000\n\tbuffer_load_dwordx4 %[go], %[rq], %[t] offen" QK_NTq " lds\n\ts_add_u32 m0, %[lw], 31744\n\ts_xor_b32 %[t], %[kb], 0x170000\n\tbuffer_load_dwordx4 %[go], %[rq], %[t] offen" QK_NTq " lds\n\t" \
          "s_waitcnt vmcnt(32)\n\tds_read_b64_tr_b16 v[160:161], %[rd0] offset:32768\n\tds_read_b64_tr_b16 v[162:163], %[rd0] offset:32896\n\tds_read_b64_tr_b16 v[164:165], %[rd0] offset:36864\n\tds_read_b64_tr_b16 v[166:167], %[rd0] offset:36992\n\tds_read_b64_tr_b16 v[128:129], %[rd1] offset:32768\n\tds_read_b64_tr_b16 v[130:131], %[rd1] offset:32896\n\tds_read_b64_tr_b16 v[132:133], %[rd1] offset:36864\n\tds_read_b64_tr_b16 v[134:135], %[rd1] offset:36992\n\tds_read_b64_tr_b16 a[0:1], %[rd2] offset:32768\n\tds_read_b64_tr_b16 a[2:3], %[rd2] offset:32896\n\tds_read_b64_tr_b16 a[4:5], %[rd2] offset:36864\n\tds_read_b64_tr_b16 a[6:7], %[rd2] offset:36992\n\tds_read_b64_tr_b16 a[8:9], %[rd3] offset:32768\n\tds_read_b64_tr_b16 a[10:11], %[rd3] offset:32896\n\tds_read_b64_tr_b16 a[12:13], %[rd3] offset:36864\n\tds_read_b64_tr_b16 a[14:15], %[rd3] offset:36992\n\ts_waitcnt lgkmcnt(0)\n\t" \
          "s_add_u32 m0, %[lw], 32768\n\ts_xor_b32 %[t], %[kb], 0x80000\n\tbuffer_load_dwordx4 %[go], %[rq], %[t] offen" QK_NTq " lds\n\ts_add_u32 m0, %[lw], 33792\n\ts_xor_b32 %[t], %[kb], 0x90000\n\tbuffer_load_dwordx4 %[go], %[rq], %[t] offen" QK_NTq " lds\n\ts_add_u32 m0, %[lw], 34816\n\ts_xor_b32 %[t], %[kb], 0xa0000\n\tbuffer_load_dwordx4 %[go], %[rq], %[t] offen" QK_NTq " lds\n\ts_add_u32 m0, %[lw], 35840\n\ts_xor_b32 %[t], %[kb], 0xb0000\n\tbuffer_load_dwordx4 %[go], %[rq], %[t] offen" QK_NTq " lds\n\ts_add_u32 m0, %[lw], 36864\n\ts_xor_b32 %[t], %[kb], 0xc0000\n\tbuffer_load_dwordx4 %[go], %[rq], %[t] offen" QK_NTq " lds\n\ts_add_u32 m0, %[lw], 37888\n\ts_xor_b32 %[t], %[kb], 0xd0000\n\tbuffer_load_dwordx4 %[go], %[rq], %[t] offen" QK_NTq " lds\n\ts_add_u32 m0, %[lw], 38912\n\ts_xor_b32 %[t], %[kb], 0xe0000\n\tbuffer_load_dwordx4 %[go], %[rq], %[t] offen" QK_NTq " lds\n\ts_add_u32 m0, %[lw], 39936\n\ts_xor_b32 %[t], %[kb], 0xf0000\n\tbuffer_load_dwordx4 %[go], %[rq], %[t] offen" QK_NTq " lds\n\t" \
          "s_waitcnt vmcnt(32)\n\tds_read_b64_tr_b16 v[104:105], %[rd0] offset:0\n\tds_read_b64_tr_b16 v[106:107], %[rd0] offset:128\n\tds_read_b64_tr_b16 v[108:109], %[rd0] offset:4096\n\tds_read_b64_tr_b16 v[110:111], %[rd0] offset:4224\n\tds_read_b64_tr_b16 v[168:169], %[rd1] offset:0\n\tds_read_b64_tr_b16 v[170:171], %[rd1] offset:128\n\tds_read_b64_tr_b16 v[172:173], %[rd1] offset:4096\n\tds_read_b64_tr_b16 v[174:175], %[rd1] offset:4224\n\tds_read_b64_tr_b16 v[112:113], %[rd2] offset:0\n\tds_read_b64_tr_b16 v[114:115], %[rd2] offset:128\n\tds_read_b64_tr_b16 v[116:117], %[rd2] offset:4096\n\tds_read_b64_tr_b16 v[118:119], %[rd2] offset:4224\n\tds_read_b64_tr_b16 v[176:177], %[rd3] offset:0\n\tds_read_b64_tr_b16 v[178:179], %[rd3] offset:128\n\tds_read_b64_tr_b16 v[180:181], %[rd3] offset:4096\n\tds_read_b64_tr_b16 v[182:183], %[rd3] offset:4224\n\ts_waitcnt lgkmcnt(0)\n\t" \
          "s_add_u32 m0, %[lw], 0\n\ts_xor_b32 %[t], %[kb], 0x0\n\tbuffer_load_dwordx4 %[go], %[rq], %[t] offen" QK_NTq " lds\n\ts_add_u32 m0, %[lw], 1024\n\ts_xor_b32 %[t], %[kb], 0x10000\n\tbuffer_load_dwordx4 %[go], %[rq], %[t] offen" QK_NTq " lds\n\ts_add_u32 m0, %[lw], 2048\n\ts_xor_b32 %[t], %[kb], 0x20000\n\tbuffer_load_dwordx4 %[go], %[rq], %[t] offen" QK_NTq " lds\n\ts_add_u32 m0, %[lw], 3072\n\ts_xor_b32 %[t], %[kb], 0x30000\n\tbuffer_load_dwordx4 %[go], %[rq], %[t] offen" QK_NTq " lds\n\ts_add_u32 m0, %[lw], 4096\n\ts_xor_b32 %[t], %[kb], 0x40000\n\tbuffer_load_dwordx4 %[go], %[rq], %[t] offen" QK_NTq " lds\n\ts_add_u32 m0, %[lw], 5120\n\ts_xor_b32 %[t], %[kb], 0x50000\n\tbuffer_load_dwordx4 %[go], %[rq], %[t] offen" QK_NTq " lds\n\ts_add_u32 m0, %[lw], 6144\n\ts_xor_b32 %[t], %[kb], 0x60000\n\tbuffer_load_dwordx4 %[go], %[rq], %[t] offen" QK_NTq " lds\n\ts_add_u32 m0, %[lw], 7168\n\ts_xor_b32 %[t], %[kb], 0x70000\n\tbuffer_load_dwordx4 %[go], %[rq], %[t] offen" QK_NTq " lds\n\t" \
          "s_waitcnt vmcnt(32)\n\tds_read_b64_tr_b16 v[184:185], %[rd0] offset:8192\n\tds_read_b64_tr_b16 v[186:187], %[rd0] offset:8320\n\tds_read_b64_tr_b16 v[188:189], %[rd0] offset:12288\n\tds_read_b64_tr_b16 v[190:191], %[rd0] offset:12416\n\tds_read_b64_tr_b16 v[88:89], %[rd1] offset:8192\n\tds_read_b64_tr_b16 v[90:91], %[rd1] offset:8320\n\tds_read_b64_tr_b16 v[92:93], %[rd1] offset:12288\n\tds_read_b64_tr_b16 v[94:95], %[rd1] offset:12416\n\tds_read_b64_tr_b16 v[192:193], %[rd2] offset:8192\n\tds_read_b64_tr_b16 v[194:195], %[rd2] offset:8320\n\tds_read_b64_tr_b16 v[196:197], %[rd2] offset:12288\n\tds_read_b64_tr_b16 v[198:199], %[rd2] offset:12416\n\tds_read_b64_tr_b16 v[96:97], %[rd3] offset:8192\n\tds_read_b64_tr_b16 v[98:99], %[rd3] offset:8320\n\tds_read_b64_tr_b16 v[100:101], %[rd3] offset:12288\n\tds_read_b64_tr_b16 v[102:103], %[rd3] offset:12416\n\ts_waitcnt lgkmcnt(0)\n\tds_write_b128 %[own], v[136:139] offset:8192\n\tds_write_b128 %[own], v[140:143] offset:9216\n\tds_write_b128 %[own], v[144:147] offset:10240\n\tds_write_b128 %[own], v[148:151] offset:11264\n\tds_write_b128 %[own], v[152:155] offset:12288\n\tds_write_b128 %[own], v[156:159] offset:13312\n\tds_write_b128 %[own], v[160:163] offset:14336\n\tds_write_b128 %[own], v[164:167] offset:15360\n\ts_waitcnt lgkmcnt(0)\n\t" \
          "s_waitcnt vmcnt(24)\n\tds_read_b64_tr_b16 v[64:65], %[rd0] offset:16384\n\tds_read_b64_tr_b16 v[66:67], %[rd0] offset:16512\n\tds_read_b64_tr_b16 v[68:69], %[rd0] offset:20480\n\tds_read_b64_tr_b16 v[70:71], %[rd0] offset:20608\n\tds_read_b64_tr_b16 v[200:201], %[rd1] offset:16384\n\tds_read_b64_tr_b16 v[202:203], %[rd1] offset:16512\n\tds_read_b64_tr_b16 v[204:205], %[rd1] offset:20480\n\tds_read_b64_tr_b16 v[206:207], %[rd1] offset:20608\n\tds_read_b64_tr_b16 v[72:73], %[rd2] offset:16384\n\tds_read_b64_tr_b16 v[74:75], %[rd2] offset:16512\n\tds_read_b64_tr_b16 v[76:77], %[rd2] offset:20480\n\tds_read_b64_tr_b16 v[78:79], %[rd2] offset:20608\n\tds_read_b64_tr_b16 v[208:209], %[rd3] offset:16384\n\tds_read_b64_tr_b16 v[210:211], %[rd3] offset:16512\n\tds_read_b64_tr_b16 v[212:213], %[rd3] offset:20480\n\tds_read_b64_tr_b16 v[214:215], %[rd3] offset:20608\n\ts_waitcnt lgkmcnt(0)\n\tds_write_b128 %[own], v[168:171] offset:16384\n\tds_write_b128 %[own], v[172:175] offset:17408\n\tds_write_b128 %[own], v[176:179] offset:18432\n\tds_write_b128 %[own], v[180:183] offset:19456\n\tds_write_b128 %[own], v[184:187] offset:20480\n\tds_write_b128 %[own], v[188:191] offset:21504\n\tds_write_b128 %[own], v[192:195] offset:22528\n\tds_write_b128 %[own], v[196:199] offset:23552\n\ts_waitcnt lgkmcnt(0)\n\t" \
          "s_waitcnt vmcnt(16)\n\tds_read_b64_tr_b16 v[216:217], %[rd0] offset:24576\n\tds_read_b64_tr_b16 v[218:219], %[rd0] offset:24704\n\tds_read_b64_tr_b16 v[220:221], %[rd0] offset:28672\n\tds_read_b64_tr_b16 v[222:223], %[rd0] offset:28800\n\tds_read_b64_tr_b16 v[48:49], %[rd1] offset:24576\n\tds_read_b64_tr_b16 v[50:51], %[rd1] offset:24704\n\tds_read_b64_tr_b16 v[52:53], %[rd1] offset:28672\n\tds_read_b64_tr_b16 v[54:55], %[rd1] offset:28800\n\tds_read_b64_tr_b16 v[224:225], %[rd2] offset:24576\n\tds_read_b64_tr_b16 v[226:227], %[rd2] offset:24704\n\tds_read_b64_tr_b16 v[228:229], %[rd2] offset:28672\n\tds_read_b64_tr_b16 v[230:231], %[rd2] offset:28800\n\tds_read_b64_tr_b16 v[56:57], %[rd3] offset:24576\n\tds_read_b64_tr_b16 v[58:59], %[rd3] offset:24704\n\tds_read_b64_tr_b16 v[60:61], %[rd3] offset:28672\n\tds_read_b64_tr_b16 v[62:63], %[rd3] offset:28800\n\ts_waitcnt lgkmcnt(0)\n\tds_write_b128 %[own], v[200:203] offset:24576\n\tds_write_b128 %[own], v[204:207] offset:25600\n\tds_write_b128 %[own], v[208:211] offset:26624\n\tds_write_b128 %[own], v[212:215] offset:27648\n\tds_write_b128 %[own], v[216:219] offset:28672\n\tds_write_b128 %[own], v[220:223] offset:29696\n\tds_write_b128 %[own], v[224:227] offset:30720\n\tds_write_b128 %[own], v[228:231] offset:31744\n\ts_waitcnt lgkmcnt(0)\n\t" \
          "s_waitcnt vmcnt(8)\n\tds_read_b64_tr_b16 v[24:25], %[rd0] offset:32768\n\tds_read_b64_tr_b16 v[26:27], %[rd0] offset:32896\n\tds_read_b64_tr_b16 v[28:29], %[rd0] offset:36864\n\tds_read_b64_tr_b16 v[30:31], %[rd0] offset:36992\n\tds_read_b64_tr_b16 v[136:137], %[rd1] offset:32768\n\tds_read_b64_tr_b16 v[138:139], %[rd1] offset:32896\n\tds_read_b64_tr_b16 v[140:141], %[rd1] offset:36864\n\tds_read_b64_tr_b16 v[142:143], %[rd1] offset:36992\n\tds_read_b64_tr_b16 v[32:33], %[rd2] offset:32768\n\tds_read_b64_tr_b16 v[34:35], %[rd2] offset:32896\n\tds_read_b64_tr_b16 v[36:37], %[rd2] offset:36864\n\tds_read_b64_tr_b16 v[38:39], %[rd2] offset:36992\n\tds_read_b64_tr_b16 v[144:145], %[rd3] offset:32768\n\tds_read_b64_tr_b16 v[146:147], %[rd3] offset:32896\n\tds_read_b64_tr_b16 v[148:149], %[rd3] offset:36864\n\tds_read_b64_tr_b16 v[150:151], %[rd3] offset:36992\n\ts_waitcnt lgkmcnt(0)\n\t" \
          "s_waitcnt vmcnt(0)\n\tds_read_b64_tr_b16 v[152:153], %[rd0] offset:0\n\tds_read_b64_tr_b16 v[154:155], %[rd0] offset:128\n\tds_read_b64_tr_b16 v[156:157], %[rd0] offset:4096\n\tds_read_b64_tr_b16 v[158:159], %[rd0] offset:4224\n\tds_read_b64_tr_b16 v[8:9], %[rd1] offset:0\n\tds_read_b64_tr_b16 v[10:11], %[rd1] offset:128\n\tds_read_b64_tr_b16 v[12:13], %[rd1] offset:4096\n\tds_read_b64_tr_b16 v[14:15], %[rd1] offset:4224\n\tds_read_b64_tr_b16 v[160:161], %[rd2] offset:0\n\tds_read_b64_tr_b16 v[162:163], %[rd2] offset:128\n\tds_read_b64_tr_b16 v[164:165], %[rd2] offset:4096\n\tds_read_b64_tr_b16 v[166:167], %[rd2] offset:4224\n\tds_read_b64_tr_b16 v[16:17], %[rd3] offset:0\n\tds_read_b64_tr_b16 v[18:19], %[rd3] offset:128\n\tds_read_b64_tr_b16 v[20:21], %[rd3] offset:4096\n\tds_read_b64_tr_b16 v[22:23], %[rd3] offset:4224\n\ts_waitcnt lgkmcnt(0)\n\tds_write_b128 %[own], v[136:139] offset:0\n\tds_write_b128 %[own], v[140:143] offset:1024\n\tds_write_b128 %[own], v[144:147] offset:2048\n\tds_write_b128 %[own], v[148:151] offset:3072\n\tds_write_b128 %[own], v[152:155] offset:4096\n\tds_write_b128 %[own], v[156:159] offset:5120\n\tds_write_b128 %[own], v[160:163] offset:6144\n\tds_write_b128 %[own], v[164:167] offset:7168\n\ts_waitcnt lgkmcnt(0)\n\t"
#define QK_DEFW "=&{a[0:31]}"(wa[0]), "=&{a[32:63]}"(wa[1]), "=&{a[64:95]}"(wa[2]), "=&{a[96:127]}"(wa[3]), "=&{a[128:159]}"(wa[4]), "=&{a[160:191]}"(wa[5]), "=&{a[192:223]}"(wa[6]), "=&{a[224:255]}"(wa[7]), "=&{v[8:39]}"(wv_[0]), "=&{v[40:71]}"(wv_[1]), "=&{v[72:103]}"(wv_[2]), "=&{v[104:135]}"(wv_[3]), "=&{v[136:167]}"(wv_[4]), "=&{v[168:199]}"(wv_[5]), "=&{v[200:231]}"(wv_[6])
#define QK_STEP \
          "ds_read_b128 v[200:203], %[own] offset:4096\n\t" \
          "ds_read_b128 v[204:207], %[own] offset:5120\n\t" \
          "ds_read_b128 v[208:211], %[own] offset:6144\n\t" \
          "ds_read_b128 v[212:215], %[own] offset:7168\n\t" \
          "ds_read_b128 v[216:219], %[own] offset:8192\n\t" \
          "ds_read_b128 v[220:223], %[own] offset:9216\n\t" \
          "ds_read_b128 v[224:227], %[own] offset:0\n\t" \
          "ds_read_b128 v[228:231], %[own] offset:1024\n\t" \
          "v_mov_b64 v[168:169], 0\n\t" \
          "v_mov_b64 v[170:171], 0\n\t" \
          "v_mov_b64 v[172:173], 0\n\t" \
          "v_mov_b64 v[174:175], 0\n\t" \
          "v_mov_b64 v[176:177], 0\n\t" \
          "v_mov_b64 v[178:179], 0\n\t" \
          "v_mov_b64 v[180:181], 0\n\t" \
          "v_mov_b64 v[182:183], 0\n\t" \
          "v_mov_b64 v[184:185], 0\n\t" \
          "v_mov_b64 v[186:187], 0\n\t" \
          "v_mov_b64 v[188:189], 0\n\t" \
          "v_mov_b64 v[190:191], 0\n\t" \
          "v_mov_b64 v[192:193], 0\n\t" \
          "v_mov_b64 v[194:195], 0\n\t" \
          "v_mov_b64 v[196:197], 0\n\t" \
          "v_mov_b64 v[198:199], 0\n\t" \
          "s_mov_b32 %[ann], %[t]\n\t" \
          "s_mov_b32 %[fl], 0\n\t" \
          "s_mov_b64 %[en], -1\n\t" \
          "s_waitcnt lgkmcnt(0)\n\t" \
          "s_cmp_eq_u32 %[w], 3\n\t" \
          "s_cbranch_scc0 Lqk_z%=\n\t" \
          "s_branch Lqk_c%=\n\t" \
          "Lqk_fail%=:\n\t" \
          "s_memrealtime vcc\n\t" \
          "s_waitcnt lgkmcnt(0)\n\t" \
          "s_add_u32 %[ann], vcc_lo, %[ald0]\n\t" \
          "v_mbcnt_lo_u32_b32 %[al], -1, 0\n\t" \
          "v_lshlrev_b32 %[al], 2, %[al]\n\t" \
          "s_and_b32 vcc_lo, %[ann], 0xffff\n\t" \
          "s_or_b32 vcc_lo, vcc_lo, 0x10000\n\t" \
          "v_mov_b32 %[m], vcc_lo\n\t" \
          "s_mov_b64 exec, %[alx]\n\t" \
          "buffer_store_dword %[m], %[al], %[rsm], %[als] offen sc1\n\t" \
          "s_mov_b64 exec, -1\n\t" \
          "buffer_load_dwordx4 v[136:139], %[ml0], %[rsm], %[mb0] offen sc1\n\t" \
          "buffer_load_dwordx4 v[140:143], %[ml1], %[rsm], %[mb0] offen sc1\n\t" \
          "buffer_load_dwordx4 v[144:147], %[ml0], %[rsm], %[mb1] offen sc1\n\t" \
          "buffer_load_dwordx4 v[148:151], %[ml1], %[rsm], %[mb1] offen sc1\n\t" \
          "buffer_load_dwordx4 v[152:155], %[ml0], %[rsm], %[mb2] offen sc1\n\t" \
          "buffer_load_dwordx4 v[156:159], %[ml1], %[rsm], %[mb2] offen sc1\n\t" \
          "buffer_load_dwordx4 v[160:163], %[ml0], %[rsm], %[mb3] offen sc1\n\t" \
          "buffer_load_dwordx4 v[164:167], %[ml1], %[rsm], %[mb3] offen sc1\n\t" \
          "ds_read_b128 v[200:203], %[own] offset:4096\n\t" \
          "ds_read_b128 v[204:207], %[own] offset:5120\n\t" \
          "ds_read_b128 v[208:211], %[own] offset:6144\n\t" \
          "ds_read_b128 v[212:215], %[own] offset:7168\n\t" \
          "ds_read_b128 v[216:219], %[own] offset:8192\n\t" \
          "ds_read_b128 v[220:223], %[own] offset:9216\n\t" \
          "ds_read_b128 v[224:227], %[own] offset:0\n\t" \
          "ds_read_b128 v[228:231], %[own] offset:1024\n\t" \
          "v_mov_b64 v[168:169], 0\n\t" \
          "v_mov_b64 v[170:171], 0\n\t" \
          "v_mov_b64 v[172:173], 0\n\t" \
          "v_mov_b64 v[174:175], 0\n\t" \
          "v_mov_b64 v[176:177], 0\n\t" \
          "v_mov_b64 v[178:179], 0\n\t" \
          "v_mov_b64 v[180:181], 0\n\t" \
          "v_mov_b64 v[182:183], 0\n\t" \
          "v_mov_b64 v[184:185], 0\n\t" \
          "v_mov_b64 v[186:187], 0\n\t" \
          "v_mov_b64 v[188:189], 0\n\t" \
          "v_mov_b64 v[190:191], 0\n\t" \
          "v_mov_b64 v[192:193], 0\n\t" \
          "v_mov_b64 v[194:195], 0\n\t" \
          "v_mov_b64 v[196:197], 0\n\t" \
          "v_mov_b64 v[198:199], 0\n\t" \
          "s_mov_b32 %[fl], 1\n\t" \
          "s_mov_b64 %[en], 0\n\t" \
          "s_mov_b32 %[cnt], 0\n\t" \
          "s_waitcnt vmcnt(7)\n\t" \
          "v_max3_u32 %[m], v136, v137, v138\n\t" \
          "v_max_u32 %[m], %[m], v139\n\t" \
          "v_cmp_eq_u32 vcc, -1, %[m]\n\t" \
          "s_cbranch_vccnz Lqk_p0_%=\n\t" \
          "Lqk_k0_%=:\n\t" \
          "s_waitcnt vmcnt(6)\n\t" \
          "v_max3_u32 %[m], v140, v141, v142\n\t" \
          "v_max_u32 %[m], %[m], v143\n\t" \
          "v_cmp_eq_u32 vcc, -1, %[m]\n\t" \
          "s_cbranch_vccnz Lqk_p1_%=\n\t" \
          "Lqk_k1_%=:\n\t" \
          "s_waitcnt vmcnt(5)\n\t" \
          "v_max3_u32 %[m], v144, v145, v146\n\t" \
          "v_max_u32 %[m], %[m], v147\n\t" \
          "v_cmp_eq_u32 vcc, -1, %[m]\n\t" \
          "s_cbranch_vccnz Lqk_p2_%=\n\t" \
          "Lqk_k2_%=:\n\t" \
          "s_waitcnt vmcnt(4)\n\t" \
          "v_max3_u32 %[m], v148, v149, v150\n\t" \
          "v_max_u32 %[m], %[m], v151\n\t" \
          "v_cmp_eq_u32 vcc, -1, %[m]\n\t" \
          "s_cbranch_vccnz Lqk_p3_%=\n\t" \
          "Lqk_k3_%=:\n\t" \
          "s_waitcnt vmcnt(3)\n\t" \
          "v_max3_u32 %[m], v152, v153, v154\n\t" \
          "v_max_u32 %[m], %[m], v155\n\t" \
          "v_cmp_eq_u32 vcc, -1, %[m]\n\t" \
          "s_cbranch_vccnz Lqk_p4_%=\n\t" \
          "Lqk_k4_%=:\n\t" \
          "s_waitcnt vmcnt(2)\n\t" \
          "v_max3_u32 %[m], v156, v157, v158\n\t" \
          "v_max_u32 %[m], %[m], v159\n\t" \
          "v_cmp_eq_u32 vcc, -1, %[m]\n\t" \
          "s_cbranch_vccnz Lqk_p5_%=\n\t" \
          "Lqk_k5_%=:\n\t" \
          "s_waitcnt vmcnt(1)\n\t" \
          "v_max3_u32 %[m], v160, v161, v162\n\t" \
          "v_max_u32 %[m], %[m], v163\n\t" \
          "v_cmp_eq_u32 vcc, -1, %[m]\n\t" \
          "s_cbranch_vccnz Lqk_p6_%=\n\t" \
          "Lqk_k6_%=:\n\t" \
          "s_waitcnt vmcnt(0)\n\t" \
          "v_max3_u32 %[m], v164, v165, v166\n\t" \
          "v_max_u32 %[m], %[m], v167\n\t" \
          "v_cmp_eq_u32 vcc, -1, %[m]\n\t" \
          "s_cbranch_vccnz Lqk_p7_%=\n\t" \
          "Lqk_k7_%=:\n\t" \
          "s_memrealtime vcc\n\t" \
          "s_waitcnt lgkmcnt(0)\n\t" \
          "s_add_u32 vcc_lo, vcc_lo, %[ald]\n\t" \
          "s_sub_i32 vcc_hi, vcc_lo, %[ann]\n\t" \
          "s_cmp_gt_i32 vcc_hi, 0\n\t" \
          "s_cbranch_scc0 Lqk_go%=\n\t" \
          "s_mov_b32 %[ann], vcc_lo\n\t" \
          "v_mbcnt_lo_u32_b32 %[al], -1, 0\n\t" \
          "v_lshlrev_b32 %[al], 2, %[al]\n\t" \
          "s_and_b32 vcc_lo, %[ann], 0xffff\n\t" \
          "s_or_b32 vcc_lo, vcc_lo, 0x10000\n\t" \
          "v_mov_b32 %[m], vcc_lo\n\t" \
          "s_mov_b64 exec, %[alx]\n\t" \
          "buffer_store_dword %[m], %[al], %[rsm], %[als] offen sc1\n\t" \
          "s_mov_b64 exec, -1\n\t" \
          "s_branch Lqk_go%=\n\t" \
          "Lqk_p0_%=:\n\t" \
          "s_cmp_lt_i32 %[cnt], 0\n\t" \
          "s_cbranch_scc1 Lqk_k0_%=\n\t" \
          "s_add_u32 %[cnt], %[cnt], 1\n\t" \
          "s_and_b32 vcc_lo, %[cnt], 3\n\t" \
          "s_cbranch_scc1 Lqk_q0_%=\n\t" \
          "s_getpc_b64 s[48:49]\n\t" \
          "s_branch Lqk_spin%=\n\t" \
          "Lqk_q0_%=:\n\t" \
          "buffer_load_dwordx4 v[136:139], %[ml0], %[rsm], %[mb0] offen sc1\n\t" \
          "s_waitcnt vmcnt(0)\n\t" \
          "v_max3_u32 %[m], v136, v137, v138\n\t" \
          "v_max_u32 %[m], %[m], v139\n\t" \
          "v_cmp_eq_u32 vcc, -1, %[m]\n\t" \
          "s_cbranch_vccnz Lqk_p0_%=\n\t" \
          "buffer_load_dwordx4 v[140:143], %[ml1], %[rsm], %[mb0] offen sc1\n\t" \
          "buffer_load_dwordx4 v[144:147], %[ml0], %[rsm], %[mb1] offen sc1\n\t" \
          "buffer_load_dwordx4 v[148:151], %[ml1], %[rsm], %[mb1] offen sc1\n\t" \
          "buffer_load_dwordx4 v[152:155], %[ml0], %[rsm], %[mb2] offen sc1\n\t" \
          "buffer_load_dwordx4 v[156:159], %[ml1], %[rsm], %[mb2] offen sc1\n\t" \
          "buffer_load_dwordx4 v[160:163], %[ml0], %[rsm], %[mb3] offen sc1\n\t" \
          "buffer_load_dwordx4 v[164:167], %[ml1], %[rsm], %[mb3] offen sc1\n\t" \
          "s_branch Lqk_k0_%=\n\t" \
          "Lqk_p1_%=:\n\t" \
          "s_cmp_lt_i32 %[cnt], 0\n\t" \
          "s_cbranch_scc1 Lqk_k1_%=\n\t" \
          "s_add_u32 %[cnt], %[cnt], 1\n\t" \
          "s_and_b32 vcc_lo, %[cnt], 3\n\t" \
          "s_cbranch_scc1 Lqk_q1_%=\n\t" \
          "s_getpc_b64 s[48:49]\n\t" \
          "s_branch Lqk_spin%=\n\t" \
          "Lqk_q1_%=:\n\t" \
          "buffer_load_dwordx4 v[140:143], %[ml1], %[rsm], %[mb0] offen sc1\n\t" \
          "s_waitcnt vmcnt(0)\n\t" \
          "v_max3_u32 %[m], v140, v141, v142\n\t" \
          "v_max_u32 %[m], %[m], v143\n\t" \
          "v_cmp_eq_u32 vcc, -1, %[m]\n\t" \
          "s_cbranch_vccnz Lqk_p1_%=\n\t" \
          "buffer_load_dwordx4 v[144:147], %[ml0], %[rsm], %[mb1] offen sc1\n\t" \
          "buffer_load_dwordx4 v[148:151], %[ml1], %[rsm], %[mb1] offen sc1\n\t" \
          "buffer_load_dwordx4 v[152:155], %[ml0], %[rsm], %[mb2] offen sc1\n\t" \
          "buffer_load_dwordx4 v[156:159], %[ml1], %[rsm], %[mb2] offen sc1\n\t" \
          "buffer_load_dwordx4 v[160:163], %[ml0], %[rsm], %[mb3] offen sc1\n\t" \
          "buffer_load_dwordx4 v[164:167], %[ml1], %[rsm], %[mb3] offen sc1\n\t" \
          "s_branch Lqk_k1_%=\n\t" \
          "Lqk_p2_%=:\n\t" \
          "s_cmp_lt_i32 %[cnt], 0\n\t" \
          "s_cbranch_scc1 Lqk_k2_%=\n\t" \
          "s_add_u32 %[cnt], %[cnt], 1\n\t" \
          "s_and_b32 vcc_lo, %[cnt], 3\n\t" \
          "s_cbranch_scc1 Lqk_q2_%=\n\t" \
          "s_getpc_b64 s[48:49]\n\t" \
          "s_branch Lqk_spin%=\n\t" \
          "Lqk_q2_%=:\n\t" \
          "buffer_load_dwordx4 v[144:147], %[ml0], %[rsm], %[mb1] offen sc1\n\t" \
          "s_waitcnt vmcnt(0)\n\t" \
          "v_max3_u32 %[m], v144, v145, v146\n\t" \
          "v_max_u32 %[m], %[m], v147\n\t" \
          "v_cmp_eq_u32 vcc, -1, %[m]\n\t" \
          "s_cbranch_vccnz Lqk_p2_%=\n\t" \
          "buffer_load_dwordx4 v[148:151], %[ml1], %[rsm], %[mb1] offen sc1\n\t" \
          "buffer_load_dwordx4 v[152:155], %[ml0], %[rsm], %[mb2] offen sc1\n\t" \
          "buffer_load_dwordx4 v[156:159], %[ml1], %[rsm], %[mb2] offen sc1\n\t" \
          "buffer_load_dwordx4 v[160:163], %[ml0], %[rsm], %[mb3] offen sc1\n\t" \
          "buffer_load_dwordx4 v[164:167], %[ml1], %[rsm], %[mb3] offen sc1\n\t" \
          "s_branch Lqk_k2_%=\n\t" \
          "Lqk_p3_%=:\n\t" \
          "s_cmp_lt_i32 %[cnt], 0\n\t" \
          "s_cbranch_scc1 Lqk_k3_%=\n\t" \
          "s_add_u32 %[cnt], %[cnt], 1\n\t" \
          "s_and_b32 vcc_lo, %[cnt], 3\n\t" \
          "s_cbranch_scc1 Lqk_q3_%=\n\t" \
          "s_getpc_b64 s[48:49]\n\t" \
          "s_branch Lqk_spin%=\n\t" \
          "Lqk_q3_%=:\n\t" \
          "buffer_load_dwordx4 v[148:151], %[ml1], %[rsm], %[mb1] offen sc1\n\t" \
          "s_waitcnt vmcnt(0)\n\t" \
          "v_max3_u32 %[m], v148, v149, v150\n\t" \
          "v_max_u32 %[m], %[m], v151\n\t" \
          "v_cmp_eq_u32 vcc, -1, %[m]\n\t" \
          "s_cbranch_vccnz Lqk_p3_%=\n\t" \
          "buffer_load_dwordx4 v[152:155], %[ml0], %[rsm], %[mb2] offen sc1\n\t" \
          "buffer_load_dwordx4 v[156:159], %[ml1], %[rsm], %[mb2] offen sc1\n\t" \
          "buffer_load_dwordx4 v[160:163], %[ml0], %[rsm], %[mb3] offen sc1\n\t" \
          "buffer_load_dwordx4 v[164:167], %[ml1], %[rsm], %[mb3] offen sc1\n\t" \
          "s_branch Lqk_k3_%=\n\t" \
          "Lqk_p4_%=:\n\t" \
          "s_cmp_lt_i32 %[cnt], 0\n\t" \
          "s_cbranch_scc1 Lqk_k4_%=\n\t" \
          "s_add_u32 %[cnt], %[cnt], 1\n\t" \
          "s_and_b32 vcc_lo, %[cnt], 3\n\t" \
          "s_cbranch_scc1 Lqk_q4_%=\n\t" \
          "s_getpc_b64 s[48:49]\n\t" \
          "s_branch Lqk_spin%=\n\t" \
          "Lqk_q4_%=:\n\t" \
          "buffer_load_dwordx4 v[152:155], %[ml0], %[rsm], %[mb2] offen sc1\n\t" \
          "s_waitcnt vmcnt(0)\n\t" \
          "v_max3_u32 %[m], v152, v153, v154\n\t" \
          "v_max_u32 %[m], %[m], v155\n\t" \
          "v_cmp_eq_u32 vcc, -1, %[m]\n\t" \
          "s_cbranch_vccnz Lqk_p4_%=\n\t" \
          "buffer_load_dwordx4 v[156:159], %[ml1], %[rsm], %[mb2] offen sc1\n\t" \
          "buffer_load_dwordx4 v[160:163], %[ml0], %[rsm], %[mb3] offen sc1\n\t" \
          "buffer_load_dwordx4 v[164:167], %[ml1], %[rsm], %[mb3] offen sc1\n\t" \
          "s_branch Lqk_k4_%=\n\t" \
          "Lqk_p5_%=:\n\t" \
          "s_cmp_lt_i32 %[cnt], 0\n\t" \
          "s_cbranch_scc1 Lqk_k5_%=\n\t" \
          "s_add_u32 %[cnt], %[cnt], 1\n\t" \
          "s_and_b32 vcc_lo, %[cnt], 3\n\t" \
          "s_cbranch_scc1 Lqk_q5_%=\n\t" \
          "s_getpc_b64 s[48:49]\n\t" \
          "s_branch Lqk_spin%=\n\t" \
          "Lqk_q5_%=:\n\t" \
          "buffer_load_dwordx4 v[156:159], %[ml1], %[rsm], %[mb2] offen sc1\n\t" \
          "s_waitcnt vmcnt(0)\n\t" \
          "v_max3_u32 %[m], v156, v157, v158\n\t" \
          "v_max_u32 %[m], %[m], v159\n\t" \
          "v_cmp_eq_u32 vcc, -1, %[m]\n\t" \
          "s_cbranch_vccnz Lqk_p5_%=\n\t" \
          "buffer_load_dwordx4 v[160:163], %[ml0], %[rsm], %[mb3] offen sc1\n\t" \
          "buffer_load_dwordx4 v[164:167], %[ml1], %[rsm], %[mb3] offen sc1\n\t" \
          "s_branch Lqk_k5_%=\n\t" \
          "Lqk_p6_%=:\n\t" \
          "s_cmp_lt_i32 %[cnt], 0\n\t" \
          "s_cbranch_scc1 Lqk_k6_%=\n\t" \
          "s_add_u32 %[cnt], %[cnt], 1\n\t" \
          "s_and_b32 vcc_lo, %[cnt], 3\n\t" \
          "s_cbranch_scc1 Lqk_q6_%=\n\t" \
          "s_getpc_b64 s[48:49]\n\t" \
          "s_branch Lqk_spin%=\n\t" \
          "Lqk_q6_%=:\n\t" \
          "buffer_load_dwordx4 v[160:163], %[ml0], %[rsm], %[mb3] offen sc1\n\t" \
          "s_waitcnt vmcnt(0)\n\t" \
          "v_max3_u32 %[m], v160, v161, v162\n\t" \
          "v_max_u32 %[m], %[m], v163\n\t" \
          "v_cmp_eq_u32 vcc, -1, %[m]\n\t" \
          "s_cbranch_vccnz Lqk_p6_%=\n\t" \
          "buffer_load_dwordx4 v[164:167], %[ml1], %[rsm], %[mb3] offen sc1\n\t" \
          "s_branch Lqk_k6_%=\n\t" \
          "Lqk_p7_%=:\n\t" \
          "s_cmp_lt_i32 %[cnt], 0\n\t" \
          "s_cbranch_scc1 Lqk_k7_%=\n\t" \
          "s_add_u32 %[cnt], %[cnt], 1\n\t" \
          "s_and_b32 vcc_lo, %[cnt], 3\n\t" \
          "s_cbranch_scc1 Lqk_q7_%=\n\t" \
          "s_getpc_b64 s[48:49]\n\t" \
          "s_branch Lqk_spin%=\n\t" \
          "Lqk_q7_%=:\n\t" \
          "buffer_load_dwordx4 v[164:167], %[ml1], %[rsm], %[mb3] offen sc1\n\t" \
          "s_waitcnt vmcnt(0)\n\t" \
          "v_max3_u32 %[m], v164, v165, v166\n\t" \
          "v_max_u32 %[m], %[m], v167\n\t" \
          "v_cmp_eq_u32 vcc, -1, %[m]\n\t" \
          "s_cbranch_vccnz Lqk_p7_%=\n\t" \
          "s_branch Lqk_k7_%=\n\t" \
          "Lqk_spin%=:\n\t" \
          "s_memrealtime vcc\n\t" \
          "s_waitcnt lgkmcnt(0)\n\t" \
          "s_add_u32 %[ann], vcc_lo, %[ald0]\n\t" \
          "v_mbcnt_lo_u32_b32 %[al], -1, 0\n\t" \
          "v_lshlrev_b32 %[al], 2, %[al]\n\t" \
          "s_and_b32 vcc_lo, %[ann], 0xffff\n\t" \
          "s_or_b32 vcc_lo, vcc_lo, 0x10000\n\t" \
          "v_mov_b32 %[m], vcc_lo\n\t" \
          "s_mov_b64 exec, %[alx]\n\t" \
          "buffer_store_dword %[m], %[al], %[rsm], %[als] offen sc1\n\t" \
          "s_mov_b64 exec, -1\n\t" \
          "buffer_load_dword %[al], off, %[rsc], %[tmo] sc1\n\t" \
          "s_add_u32 s48, s48, 4\n\t" \
          "s_addc_u32 s49, s49, 0\n\t" \
          "s_waitcnt vmcnt(0)\n\t" \
          "v_readfirstlane_b32 vcc_lo, %[al]\n\t" \
          "s_lshr_b32 vcc_hi, %[cnt], 16\n\t" \
          "s_or_b32 vcc_lo, vcc_lo, vcc_hi\n\t" \
          "s_cbranch_scc0 Lqk_back%=\n\t" \
          "v_mov_b32 %[al], 1\n\t" \
          "buffer_store_dword %[al], off, %[rsc], %[tmo] sc1\n\t" \
          "s_mov_b32 %[cnt], -1\n\t" \
          "Lqk_back%=:\n\t" \
          "s_setpc_b64 s[48:49]\n\t" \
          "Lqk_s%=:\n\t" \
          "s_sleep 2\n\t" \
          "Lqk_c%=:\n\t" \
          "s_memrealtime vcc\n\t" \
          "s_waitcnt lgkmcnt(0)\n\t" \
          "s_sub_i32 vcc_lo, %[t], vcc_lo\n\t" \
          "s_cmp_gt_i32 vcc_lo, 16\n\t" \
          "s_cbranch_scc1 Lqk_s%=\n\t" \
          "Lqk_w%=:\n\t" \
          "s_memrealtime s[50:51]\n\t" \
          "s_getpc_b64 s[48:49]\n\t" \
          "Lqk_y%=:\n\t" \
          "s_add_u32 s48, s48, Lqk_z%=-Lqk_y%=\n\t" \
          "s_addc_u32 s49, s49, 0\n\t" \
          "Lqk_v%=:\n\t" \
          "s_memrealtime s[52:53]\n\t" \
          "s_waitcnt lgkmcnt(1)\n\t" \
          "s_sub_i32 vcc_lo, %[t], s50\n\t" \
          "s_cmp_gt_i32 vcc_lo, 3\n\t" \
          "s_cbranch_scc0 Lqk_x%=\n\t" \
          "s_memrealtime s[50:51]\n\t" \
          "s_waitcnt lgkmcnt(1)\n\t" \
          "s_sub_i32 vcc_lo, %[t], s52\n\t" \
          "s_cmp_gt_i32 vcc_lo, 3\n\t" \
          "s_cbranch_scc0 Lqk_x%=\n\t" \
          "s_memrealtime s[52:53]\n\t" \
          "s_waitcnt lgkmcnt(1)\n\t" \
          "s_sub_i32 vcc_lo, %[t], s50\n\t" \
          "s_cmp_gt_i32 vcc_lo, 3\n\t" \
          "s_cbranch_scc0 Lqk_x%=\n\t" \
          "s_memrealtime s[50:51]\n\t" \
          "s_waitcnt lgkmcnt(1)\n\t" \
          "s_sub_i32 vcc_lo, %[t], s52\n\t" \
          "s_cmp_gt_i32 vcc_lo, 3\n\t" \
          "s_cbranch_scc0 Lqk_x%=\n\t" \
          "s_memrealtime s[52:53]\n\t" \
          "s_waitcnt lgkmcnt(1)\n\t" \
          "s_sub_i32 vcc_lo, %[t], s50\n\t" \
          "s_cmp_gt_i32 vcc_lo, 3\n\t" \
          "s_cbranch_scc0 Lqk_x%=\n\t" \
          "s_memrealtime s[50:51]\n\t" \
          "s_waitcnt lgkmcnt(1)\n\t" \
          "s_sub_i32 vcc_lo, %[t], s52\n\t" \
          "s_cmp_gt_i32 vcc_lo, 3\n\t" \
          "s_cbranch_scc0 Lqk_x%=\n\t" \
          "s_memrealtime s[52:53]\n\t" \
          "s_waitcnt lgkmcnt(1)\n\t" \
          "s_sub_i32 vcc_lo, %[t], s50\n\t" \
          "s_cmp_gt_i32 vcc_lo, 3\n\t" \
          "s_cbranch_scc0 Lqk_x%=\n\t" \
          "s_memrealtime s[50:51]\n\t" \
          "s_waitcnt lgkmcnt(1)\n\t" \
          "s_sub_i32 vcc_lo, %[t], s52\n\t" \
          "s_cmp_gt_i32 vcc_lo, 3\n\t" \
          "s_cbranch_scc0 Lqk_x%=\n\t" \
          "s_branch Lqk_v%=\n\t" \
          "Lqk_x%=:\n\t" \
          "s_max_i32 vcc_lo, vcc_lo, 0\n\t" \
          "s_lshl_b32 vcc_lo, vcc_lo, 2\n\t" \
          "s_sub_u32 s48, s48, vcc_lo\n\t" \
          "s_subb_u32 s49, s49, 0\n\t" \
          "s_setpc_b64 s[48:49]\n\t" \
          "s_nop 5\n\t" \
          "s_nop 5\n\t" \
          "s_nop 5\n\t" \
          "Lqk_z%=:\n\t" \
          "s_barrier\n\t" \
          "buffer_load_dwordx4 v[136:139], %[ml0], %[rsm], %[mb0] offen sc1\n\t" \
          "s_nop 9\n\t" \
          "buffer_load_dwordx4 v[140:143], %[ml1], %[rsm], %[mb0] offen sc1\n\t" \
          "s_nop 9\n\t" \
          "buffer_load_dwordx4 v[144:147], %[ml0], %[rsm], %[mb1] offen sc1\n\t" \
          "s_nop 9\n\t" \
          "buffer_load_dwordx4 v[148:151], %[ml1], %[rsm], %[mb1] offen sc1\n\t" \
          "s_nop 9\n\t" \
          "buffer_load_dwordx4 v[152:155], %[ml0], %[rsm], %[mb2] offen sc1\n\t" \
          "s_nop 9\n\t" \
          "buffer_load_dwordx4 v[156:159], %[ml1], %[rsm], %[mb2] offen sc1\n\t" \
          "s_nop 9\n\t" \
          "buffer_load_dwordx4 v[160:163], %[ml0], %[rsm], %[mb3] offen sc1\n\t" \
          "s_nop 9\n\t" \
          "buffer_load_dwordx4 v[164:167], %[ml1], %[rsm], %[mb3] offen sc1\n\t" \
          "s_waitcnt vmcnt(7)\n\t" \
          "Lqk_go%=:\n\t" \
          "v_smfmac_f32_16x16x64_bf16 v[168:171], v[136:139], v[200:207], %[idx]\n\t" \
          "ds_read_b128 v[200:203], %[own] offset:2048\n\t" \
          "v_smfmac_f32_16x16x64_bf16 v[172:175], v[136:139], v[8:15], %[idx]\n\t" \
          "ds_read_b128 v[204:207], %[own] offset:3072\n\t" \
          "v_smfmac_f32_16x16x64_bf16 v[176:179], v[136:139], v[208:215], %[idx]\n\t" \
          "ds_read_b128 v[208:211], %[own] offset:28672\n\t" \
          "v_smfmac_f32_16x16x64_bf16 v[180:183], v[136:139], v[16:23], %[idx]\n\t" \
          "ds_read_b128 v[212:215], %[own] offset:29696\n\t" \
          "s_waitcnt vmcnt(6)\n\t" \
          "v_smfmac_f32_16x16x64_bf16 v[184:187], v[136:139], v[216:223], %[idx]\n\t" \
          "ds_read_b128 v[216:219], %[own] offset:30720\n\t" \
          "v_smfmac_f32_16x16x64_bf16 v[168:171], v[140:143], v[24:31], %[idx]\n\t" \
          "ds_read_b128 v[220:223], %[own] offset:31744\n\t" \
          "v_smfmac_f32_16x16x64_bf16 v[172:175], v[140:143], v[224:231], %[idx]\n\t" \
          "ds_read_b128 v[224:227], %[own] offset:10240\n\t" \
          "v_smfmac_f32_16x16x64_bf16 v[176:179], v[140:143], v[32:39], %[idx]\n\t" \
          "ds_read_b128 v[228:231], %[own] offset:11264\n\t" \
          "s_waitcnt lgkmcnt(6)\n\t" \
          "v_smfmac_f32_16x16x64_bf16 v[180:183], v[140:143], v[200:207], %[idx]\n\t" \
          "ds_read_b128 v[200:203], %[own] offset:24576\n\t" \
          "v_max3_u32 %[m], v136, v137, v138\n\t" \
          "v_smfmac_f32_16x16x64_bf16 v[184:187], v[140:143], v[40:47], %[idx]\n\t" \
          "ds_read_b128 v[204:207], %[own] offset:25600\n\t" \
          "s_waitcnt vmcnt(5) lgkmcnt(6)\n\t" \
          "v_smfmac_f32_16x16x64_bf16 v[168:171], v[144:147], v[208:215], %[idx]\n\t" \
          "ds_read_b128 v[208:211], %[own] offset:26624\n\t" \
          "v_max3_u32 %[m], v138, v139, %[m]\n\t" \
          "v_smfmac_f32_16x16x64_bf16 v[172:175], v[144:147], v[48:55], %[idx]\n\t" \
          "ds_read_b128 v[212:215], %[own] offset:27648\n\t" \
          "s_waitcnt lgkmcnt(6)\n\t" \
          "v_smfmac_f32_16x16x64_bf16 v[176:179], v[144:147], v[216:223], %[idx]\n\t" \
          "ds_read_b128 v[216:219], %[own] offset:20480\n\t" \
          "v_cmp_eq_u32 %[bad1], -1, %[m]\n\t" \
          "v_smfmac_f32_16x16x64_bf16 v[180:183], v[144:147], v[56:63], %[idx]\n\t" \
          "ds_read_b128 v[220:223], %[own] offset:21504\n\t" \
          "s_waitcnt vmcnt(4) lgkmcnt(6)\n\t" \
          "v_smfmac_f32_16x16x64_bf16 v[184:187], v[144:147], v[224:231], %[idx]\n\t" \
          "ds_read_b128 v[224:227], %[own] offset:22528\n\t" \
          "s_and_b64 %[bad1], %[bad1], %[en]\n\t" \
          "v_smfmac_f32_16x16x64_bf16 v[168:171], v[148:151], v[64:71], %[idx]\n\t" \
          "ds_read_b128 v[228:231], %[own] offset:23552\n\t" \
          "s_waitcnt lgkmcnt(6)\n\t" \
          "v_smfmac_f32_16x16x64_bf16 v[172:175], v[148:151], v[200:207], %[idx]\n\t" \
          "ds_read_b128 v[200:203], %[own] offset:12288\n\t" \
          "s_cbranch_scc1 Lqk_fail%=\n\t" \
          "v_smfmac_f32_16x16x64_bf16 v[176:179], v[148:151], v[72:79], %[idx]\n\t" \
          "ds_read_b128 v[204:207], %[own] offset:13312\n\t" \
          "s_waitcnt lgkmcnt(6)\n\t" \
          "v_smfmac_f32_16x16x64_bf16 v[180:183], v[148:151], v[208:215], %[idx]\n\t" \
          "ds_read_b128 v[208:211], %[own] offset:16384\n\t" \
          "v_max3_u32 %[m], v140, v141, %[m]\n\t" \
          "v_smfmac_f32_16x16x64_bf16 v[184:187], v[148:151], v[80:87], %[idx]\n\t" \
          "ds_read_b128 v[212:215], %[own] offset:17408\n\t" \
          "s_waitcnt vmcnt(3) lgkmcnt(6)\n\t" \
          "v_smfmac_f32_16x16x64_bf16 v[168:171], v[152:155], v[216:223], %[idx]\n\t" \
          "ds_read_b128 v[216:219], %[own] offset:18432\n\t" \
          "v_max3_u32 %[m], v142, v143, %[m]\n\t" \
          "v_smfmac_f32_16x16x64_bf16 v[172:175], v[152:155], v[88:95], %[idx]\n\t" \
          "ds_read_b128 v[220:223], %[own] offset:19456\n\t" \
          "s_waitcnt lgkmcnt(6)\n\t" \
          "v_smfmac_f32_16x16x64_bf16 v[176:179], v[152:155], v[224:231], %[idx]\n\t" \
          "ds_read_b128 v[224:227], %[own] offset:14336\n\t" \
          "v_max3_u32 %[m], v144, v145, %[m]\n\t" \
          "v_smfmac_f32_16x16x64_bf16 v[180:183], v[152:155], v[96:103], %[idx]\n\t" \
          "ds_read_b128 v[228:231], %[own] offset:15360\n\t" \
          "s_waitcnt vmcnt(2) lgkmcnt(6)\n\t" \
          "v_smfmac_f32_16x16x64_bf16 v[184:187], v[152:155], v[200:207], %[idx]\n\t" \
          "v_max3_u32 %[m], v146, v147, %[m]\n\t" \
          "v_smfmac_f32_16x16x64_bf16 v[168:171], v[156:159], v[104:111], %[idx]\n\t" \
          "s_waitcnt lgkmcnt(4)\n\t" \
          "v_max3_u32 %[m], v148, v149, %[m]\n\t" \
          "v_smfmac_f32_16x16x64_bf16 v[172:175], v[156:159], v[208:215], %[idx]\n\t" \
          "v_max3_u32 %[m], v150, v151, %[m]\n\t" \
          "v_smfmac_f32_16x16x64_bf16 v[176:179], v[156:159], v[112:119], %[idx]\n\t" \
          "s_waitcnt lgkmcnt(2)\n\t" \
          "v_max3_u32 %[m], v152, v153, %[m]\n\t" \
          "v_smfmac_f32_16x16x64_bf16 v[180:183], v[156:159], v[216:223], %[idx]\n\t" \
          "ds_read_b128 v[216:219], %[own] offset:32768\n\t" \
          "v_max3_u32 %[m], v154, v155, %[m]\n\t" \
          "v_smfmac_f32_16x16x64_bf16 v[184:187], v[156:159], v[120:127], %[idx]\n\t" \
          "ds_read_b128 v[220:223], %[own] offset:33792\n\t" \
          "s_waitcnt vmcnt(1) lgkmcnt(2)\n\t" \
          "v_smfmac_f32_16x16x64_bf16 v[168:171], v[160:163], v[224:231], %[idx]\n\t" \
          "ds_read_b64_tr_b16 v[224:225], %[vxp]\n\t" \
          "v_max3_u32 %[m], v156, v157, %[m]\n\t" \
          "v_smfmac_f32_16x16x64_bf16 v[172:175], v[160:163], v[128:135], %[idx]\n\t" \
          "ds_read_b64_tr_b16 v[226:227], %[vxp] offset:1024\n\t" \
          "v_max3_u32 %[m], v158, v159, %[m]\n\t" \
          "v_smfmac_f32_16x16x64_bf16 v[176:179], v[160:163], a[0:7], %[idx]\n\t" \
          "ds_read_b64_tr_b16 v[228:229], %[vxp] offset:8\n\t" \
          "v_max3_u32 %[m], v160, v161, %[m]\n\t" \
          "v_smfmac_f32_16x16x64_bf16 v[180:183], v[160:163], a[8:15], %[idx]\n\t" \
          "ds_read_b64_tr_b16 v[230:231], %[vxp] offset:1032\n\t" \
          "v_smfmac_f32_16x16x64_bf16 v[184:187], v[160:163], a[16:23], %[idx]\n\t" \
          "s_waitcnt vmcnt(0)\n\t" \
          "v_max3_u32 %[m], v162, v163, %[m]\n\t" \
          "v_smfmac_f32_16x16x64_bf16 v[168:171], v[164:167], a[24:31], %[idx]\n\t" \
          "v_max3_u32 %[m], v164, v165, %[m]\n\t" \
          "v_smfmac_f32_16x16x64_bf16 v[172:175], v[164:167], a[32:39], %[idx]\n\t" \
          "v_max3_u32 %[m], v166, v167, %[m]\n\t" \
          "v_smfmac_f32_16x16x64_bf16 v[176:179], v[164:167], a[40:47], %[idx]\n\t" \
          "v_cvt_pk_bf16_f32 v200, v168, v169\n\t" \
          "v_cvt_pk_bf16_f32 v201, v170, v171\n\t" \
          "v_smfmac_f32_16x16x64_bf16 v[180:183], v[164:167], a[48:55], %[idx]\n\t" \
          "ds_write_b64 %[own], v[200:201] offset:36864\n\t" \
          "v_cvt_pk_bf16_f32 v202, v172, v173\n\t" \
          "v_cvt_pk_bf16_f32 v203, v174, v175\n\t" \
          "v_smfmac_f32_16x16x64_bf16 v[184:187], v[164:167], a[56:63], %[idx]\n\t" \
          "ds_write_b64 %[own], v[202:203] offset:36872\n\t" \
          "v_smfmac_f32_16x16x64_bf16 v[188:191], v[136:139], a[64:71], %[idx]\n\t" \
          "v_cmp_eq_u32 %[bad1], -1, %[m]\n\t" \
          "v_smfmac_f32_16x16x64_bf16 v[188:191], v[140:143], a[72:79], %[idx]\n\t" \
          "v_cvt_pk_bf16_f32 v208, v176, v177\n\t" \
          "v_smfmac_f32_16x16x64_bf16 v[188:191], v[144:147], a[80:87], %[idx]\n\t" \
          "s_and_b64 %[bad1], %[bad1], %[en]\n\t" \
          "s_cbranch_scc1 Lqk_fail%=\n\t" \
          "s_waitcnt lgkmcnt(0)\n\t" \
          "s_barrier\n\t" \
          "v_cvt_pk_bf16_f32 v209, v178, v179\n\t" \
          "v_smfmac_f32_16x16x64_bf16 v[188:191], v[148:151], a[88:95], %[idx]\n\t" \
          "ds_read_b128 v[210:213], %[p1] offset:36864\n\t" \
          "v_cvt_pk_bf16_f32 v214, v180, v181\n\t" \
          "v_smfmac_f32_16x16x64_bf16 v[188:191], v[152:155], a[96:103], %[idx]\n\t" \
          "v_cvt_pk_bf16_f32 v215, v182, v183\n\t" \
          "v_smfmac_f32_16x16x64_bf16 v[188:191], v[156:159], a[104:111], %[idx]\n\t" \
          "v_cvt_pk_bf16_f32 v204, v184, v185\n\t" \
          "v_smfmac_f32_16x16x64_bf16 v[188:191], v[160:163], a[112:119], %[idx]\n\t" \
          "v_cvt_pk_bf16_f32 v205, v186, v187\n\t" \
          "v_smfmac_f32_16x16x64_bf16 v[188:191], v[164:167], a[120:127], %[idx]\n\t" \
          "s_waitcnt lgkmcnt(0)\n\t" \
          "v_mfma_f32_16x16x32_bf16 v[168:171], v[208:211], %[jk], 0\n\t" \
          "v_smfmac_f32_16x16x64_bf16 v[192:195], v[136:139], a[128:135], %[idx]\n\t" \
          "v_mfma_f32_16x16x32_bf16 v[172:175], v[212:215], %[jk], 0\n\t" \
          "v_cvt_pk_bf16_f32 v176, v168, v169\n\t" \
          "v_cvt_pk_bf16_f32 v177, v170, v171\n\t" \
          "v_smfmac_f32_16x16x64_bf16 v[196:199], v[136:139], a[136:143], %[idx]\n\t" \
          "ds_write_b64 %[own], v[204:205] offset:37888\n\t" \
          "v_cvt_pk_bf16_f32 v206, v188, v189\n\t" \
          "v_smfmac_f32_16x16x64_bf16 v[192:195], v[140:143], a[144:151], %[idx]\n\t" \
          "v_cvt_pk_bf16_f32 v178, v172, v173\n\t" \
          "v_cvt_pk_bf16_f32 v179, v174, v175\n\t" \
          "v_smfmac_f32_16x16x64_bf16 v[196:199], v[140:143], a[152:159], %[idx]\n\t" \
          "v_cvt_pk_bf16_f32 v207, v190, v191\n\t" \
          "v_mfma_f32_16x16x32_bf16 v[208:211], v[216:219], v[176:179], 0\n\t" \
          "v_mov_b32 v168, %[one]\n\t" \
          "v_mov_b32 v169, %[one]\n\t" \
          "v_mfma_f32_16x16x32_bf16 v[212:215], v[220:223], v[176:179], 0\n\t" \
          "v_mov_b32 v170, %[one]\n\t" \
          "v_mov_b32 v171, %[one]\n\t" \
          "v_smfmac_f32_16x16x64_bf16 v[192:195], v[144:147], a[160:167], %[idx]\n\t" \
          "ds_write_b64 %[own], v[206:207] offset:37896\n\t" \
          "v_smfmac_f32_16x16x64_bf16 v[196:199], v[144:147], a[168:175], %[idx]\n\t" \
          "v_cndmask_b32 v208, v208, v212, %[twin]\n\t" \
          "v_cndmask_b32 v209, v209, v213, %[twin]\n\t" \
          "v_smfmac_f32_16x16x64_bf16 v[192:195], v[148:151], a[176:183], %[idx]\n\t" \
          "ds_write_b64 %[own], v[208:209] offset:38912\n\t" \
          "v_cndmask_b32 v210, v210, v214, %[twin]\n\t" \
          "v_cndmask_b32 v211, v211, v215, %[twin]\n\t" \
          "v_smfmac_f32_16x16x64_bf16 v[196:199], v[148:151], a[184:191], %[idx]\n\t" \
          "ds_write_b64 %[own], v[210:211] offset:38920\n\t" \
          "v_smfmac_f32_16x16x64_bf16 v[192:195], v[152:155], a[192:199], %[idx]\n\t" \
          "v_mul_f32 v212, %[qs], v208\n\t" \
          "v_smfmac_f32_16x16x64_bf16 v[196:199], v[152:155], a[200:207], %[idx]\n\t" \
          "buffer_load_dword v232, %[ho], %[rsv], %[vb] offen sc1\n\t" \
          "v_mul_f32 v213, %[qs], v209\n\t" \
          "v_smfmac_f32_16x16x64_bf16 v[192:195], v[156:159], a[208:215], %[idx]\n\t" \
          "buffer_load_dword v234, %[ho], %[rsv], %[vb] offen offset:256 sc1\n\t" \
          "v_mul_f32 v214, %[qs], v210\n\t" \
          "v_smfmac_f32_16x16x64_bf16 v[196:199], v[156:159], a[216:223], %[idx]\n\t" \
          "s_waitcnt lgkmcnt(0)\n\t" \
          "s_barrier\n\t" \
          "v_mul_f32 v215, %[qs], v211\n\t" \
          "v_smfmac_f32_16x16x64_bf16 v[192:195], v[160:163], a[224:231], %[idx]\n\t" \
          "ds_read_b128 v[138:141], %[p1] offset:37888\n\t" \
          "v_smfmac_f32_16x16x64_bf16 v[196:199], v[160:163], a[232:239], %[idx]\n\t" \
          "ds_read_b128 v[216:219], %[p1] offset:38912\n\t" \
          "v_smfmac_f32_16x16x64_bf16 v[192:195], v[164:167], a[240:247], %[idx]\n\t" \
          "ds_read_b128 v[220:223], %[p2] offset:38912\n\t" \
          "v_smfmac_f32_16x16x64_bf16 v[196:199], v[164:167], a[248:255], %[idx]\n\t" \
          "ds_read_b128 v[200:203], %[p3] offset:38912\n\t" \
          "s_nop 0\n\t" \
          "s_waitcnt lgkmcnt(2)\n\t" \
          "v_cvt_pk_bf16_f32 v136, v192, v193\n\t" \
          "v_cvt_pk_bf16_f32 v137, v194, v195\n\t" \
          "v_fma_f32 v212, v216, %[qs], v212\n\t" \
          "v_fma_f32 v213, v217, %[qs], v213\n\t" \
          "v_cvt_pk_bf16_f32 v142, v196, v197\n\t" \
          "v_cvt_pk_bf16_f32 v143, v198, v199\n\t" \
          "v_mfma_f32_16x16x32_bf16 v[144:147], v[136:139], %[jk], 0\n\t" \
          "v_fma_f32 v214, v218, %[qs], v214\n\t" \
          "v_mfma_f32_16x16x32_bf16 v[148:151], v[140:143], %[jk], 0\n\t" \
          "v_fma_f32 v215, v219, %[qs], v215\n\t" \
          "s_waitcnt lgkmcnt(0)\n\t" \
          "v_fma_f32 v212, v220, %[qs], v212\n\t" \
          "v_fma_f32 v213, v221, %[qs], v213\n\t" \
          "v_cvt_pk_bf16_f32 v152, v144, v145\n\t" \
          "v_cvt_pk_bf16_f32 v153, v146, v147\n\t" \
          "v_fma_f32 v214, v222, %[qs], v214\n\t" \
          "v_fma_f32 v215, v223, %[qs], v215\n\t" \
          "v_cvt_pk_bf16_f32 v154, v148, v149\n\t" \
          "v_cvt_pk_bf16_f32 v155, v150, v151\n\t" \
          "v_pk_fma_f32 v[212:213], v[200:201], %[qs2], v[212:213] op_sel_hi:[1,0,1]\n\t" \
          "v_pk_fma_f32 v[214:215], v[202:203], %[qs2], v[214:215] op_sel_hi:[1,0,1]\n\t" \
          "v_mfma_f32_16x16x32_bf16 v[156:159], v[152:155], v[176:179], 0\n\t" \
          "v_exp_f32 v180, v212\n\t" \
          "v_mfma_f32_16x16x32_bf16 v[196:199], v[168:171], v[168:171], 0\n\t" \
          "v_exp_f32 v181, v213\n\t" \
          "v_mfma_f32_16x16x32_bf16 v[196:199], v[168:171], v[168:171], 0\n\t" \
          "ds_write_b64 %[own], v[156:157] offset:39936\n\t" \
          "v_exp_f32 v182, v214\n\t" \
          "v_mov_b32 v137, 0\n\t" \
          "v_exp_f32 v183, v215\n\t" \
          "v_cvt_pk_bf16_f32 v172, v180, v181\n\t" \
          "v_cvt_pk_bf16_f32 v173, v182, v183\n\t" \
          "v_pk_mul_f32 v[160:161], v[156:157], %[qs2] op_sel_hi:[1,0]\n\t" \
          "v_mov_b32_dpp v174, v172 quad_perm:[2,3,0,1] row_mask:0xf bank_mask:0xf\n\t" \
          "v_mov_b32_dpp v175, v173 quad_perm:[2,3,0,1] row_mask:0xf bank_mask:0xf\n\t" \
          "s_nop 0\n\t" \
          "s_waitcnt lgkmcnt(0)\n\t" \
          "v_mfma_f32_16x16x32_bf16 v[184:187], v[172:175], v[168:171], 0\n\t" \
          "s_barrier\n\t" \
          "ds_read_b64 v[144:145], %[p1] offset:39936\n\t" \
          "ds_read_b64 v[146:147], %[p2] offset:39936\n\t" \
          "v_mfma_f32_16x16x32_bf16 v[188:191], v[172:175], v[224:227], 0\n\t" \
          "ds_read_b64 v[148:149], %[p3] offset:39936\n\t" \
          "v_mfma_f32_16x16x32_bf16 v[192:195], v[172:175], v[228:231], 0\n\t" \
          "buffer_load_dword %[al], %[alo], %[rsm], %[mb] offen sc1\n\t" \
          "s_waitcnt lgkmcnt(2)\n\t" \
          "v_fma_f32 v160, v144, %[qs], v160\n\t" \
          "v_fma_f32 v161, v145, %[qs], v161\n\t" \
          "s_waitcnt lgkmcnt(1)\n\t" \
          "v_fma_f32 v160, v146, %[qs], v160\n\t" \
          "v_fma_f32 v161, v147, %[qs], v161\n\t" \
          "s_waitcnt lgkmcnt(0)\n\t" \
          "v_fma_f32 v160, v148, %[qs], v160\n\t" \
          "v_fma_f32 v161, v149, %[qs], v161\n\t" \
          "v_exp_f32 v162, v160\n\t" \
          "v_exp_f32 v163, v161\n\t" \
          "v_max_f32 v182, v160, v161\n\t" \
          "v_cvt_pk_bf16_f32 v136, v162, v163\n\t" \
          "v_cmp_nge_f32 vcc, %[th], v182\n\t" \
          "s_waitcnt vmcnt(1)\n\t" \
          "v_mfma_f32_16x16x16_bf16 v[164:167], v[136:137], v[168:169], v[184:187]\n\t" \
          "v_max_f32 v180, v184, v185\n\t" \
          "v_min_f32 v181, v184, v185\n\t" \
          "v_mfma_f32_16x16x16_bf16 v[188:191], v[136:137], v[232:233], v[188:191]\n\t" \
          "v_cmp_nge_f32 %[b2], %[hi], v180\n\t" \
          "v_cmp_nle_f32 %[b3], %[lo], v181\n\t" \
          "v_mfma_f32_16x16x16_bf16 v[192:195], v[136:137], v[234:235], v[192:195]\n\t" \
          "v_max_u32 v183, v232, v234\n\t" \
          "v_cmp_eq_u32 %[bad], -1, v183\n\t" \
          "v_rcp_f32 v162, v164\n\t" \
          "s_or_b64 %[b2], %[b2], %[b3]\n\t" \
          "v_rcp_f32 v163, v165\n\t" \
          "s_or_b64 %[b2], %[b2], vcc\n\t" \
          "v_pk_mul_f32 v[156:157], v[188:189], v[162:163]\n\t" \
          "s_or_b64 %[bad], %[bad], %[b2]\n\t" \
          "s_cselect_b64 exec, 0, -1\n\t" \
          "v_pk_mul_f32 v[158:159], v[192:193], v[162:163]\n\t" \
          "s_nop 0\n\t" \
          "v_cvt_pk_bf16_f32 v156, v156, v158\n\t" \
          "v_cvt_pk_bf16_f32 v157, v157, v159\n\t" \
          "v_min_u32 v156, -2, v156\n\t" \
          "v_min_u32 v157, -2, v157\n\t" \
          "buffer_store_dword v156, %[ho], %[rso], %[ob] offen sc1\n\t" \
          "buffer_store_dword v157, %[ho], %[rso], %[ob] offen offset:256 sc1\n\t" \
          "s_memrealtime %[po]\n\t" \
          "s_mov_b64 exec, -1\n\t" \
          "s_waitcnt lgkmcnt(0)\n\t"
constexpr int QK_SX = 12, QK_SM = 24, QK_KM = 16, QK_VX = 24, QK_KV = 32768, QK_EX = 36864;
#define VO_BODY \
          "v_mov_b64 v[224:225], 0\n\t" \
          "v_mov_b64 v[226:227], 0\n\t" \
          "v_mov_b64 v[228:229], 0\n\t" \
          "v_mov_b64 v[230:231], 0\n\t" \
          "v_mov_b64 v[232:233], 0\n\t" \
          "v_mov_b64 v[234:235], 0\n\t" \
          "v_mov_b64 v[236:237], 0\n\t" \
          "v_mov_b64 v[238:239], 0\n\t" \
          "v_mov_b64 v[176:177], 0\n\t" \
          "v_mov_b64 v[178:179], 0\n\t" \
          "v_mov_b64 v[180:181], 0\n\t" \
          "v_mov_b64 v[182:183], 0\n\t" \
          "v_mov_b64 v[184:185], 0\n\t" \
          "v_mov_b64 v[186:187], 0\n\t" \
          "v_mov_b64 v[188:189], 0\n\t" \
          "v_mov_b64 v[190:191], 0\n\t" \
          "s_mov_b64 %[en], -1\n\t" \
          "s_cmp_eq_u32 %[s1], 0\n\t" \
          "s_cbranch_scc1 Lv_n%=\n\t" \
          "s_cmp_eq_u32 %[w], 3\n\t" \
          "s_cbranch_scc0 Lo_z%=\n\t" \
          "s_branch Lo_c%=\n\t" \
          "Lo_f0_%=:\n\t" \
          "buffer_load_dwordx4 v[192:195], %[oo0], %[rso], %[ob] offen sc1\n\t" \
          "Lo_f1_%=:\n\t" \
          "buffer_load_dwordx4 v[196:199], %[oo1], %[rso], %[ob] offen sc1\n\t" \
          "Lo_f2_%=:\n\t" \
          "buffer_load_dwordx4 v[200:203], %[oo2], %[rso], %[ob] offen sc1\n\t" \
          "Lo_f3_%=:\n\t" \
          "buffer_load_dwordx4 v[204:207], %[oo3], %[rso], %[ob] offen sc1\n\t" \
          "Lo_f4_%=:\n\t" \
          "buffer_load_dwordx4 v[208:211], %[oo4], %[rso], %[ob] offen sc1\n\t" \
          "Lo_f5_%=:\n\t" \
          "buffer_load_dwordx4 v[212:215], %[oo5], %[rso], %[ob] offen sc1\n\t" \
          "Lo_f6_%=:\n\t" \
          "buffer_load_dwordx4 v[216:219], %[oo6], %[rso], %[ob] offen sc1\n\t" \
          "Lo_f7_%=:\n\t" \
          "buffer_load_dwordx4 v[220:223], %[oo7], %[rso], %[ob] offen sc1\n\t" \
          "v_mov_b32 v242, 0x10000\n\t" \
          "s_mov_b64 exec, %[alx]\n\t" \
          "buffer_store_dword v242, %[l4], %[rsm], %[als] offen sc1\n\t" \
          "s_mov_b64 exec, -1\n\t" \
          "v_mov_b32 v243, %[s1]\n\t" \
          "ds_write_b32 %[fa], v243\n\t" \
          "v_mov_b64 v[224:225], 0\n\t" \
          "v_mov_b64 v[226:227], 0\n\t" \
          "v_mov_b64 v[228:229], 0\n\t" \
          "v_mov_b64 v[230:231], 0\n\t" \
          "v_mov_b64 v[232:233], 0\n\t" \
          "v_mov_b64 v[234:235], 0\n\t" \
          "v_mov_b64 v[236:237], 0\n\t" \
          "v_mov_b64 v[238:239], 0\n\t" \
          "s_nop 1\n\t" \
          "s_mov_b32 %[cnt], 0\n\t" \
          "s_waitcnt vmcnt(7)\n\t" \
          "v_max3_u32 v240, v192, v193, v194\n\t" \
          "v_max_u32 v240, v240, v195\n\t" \
          "v_cmp_eq_u32 vcc, -1, v240\n\t" \
          "s_cbranch_vccnz Lo_p0_%=\n\t" \
          "Lo_k0_%=:\n\t" \
          "v_smfmac_f32_16x16x64_bf16 v[224:227], v[192:195], a[0:7], %[idx]\n\t" \
          "v_smfmac_f32_16x16x64_bf16 v[228:231], v[192:195], a[8:15], %[idx]\n\t" \
          "v_smfmac_f32_16x16x64_bf16 v[232:235], v[192:195], a[16:23], %[idx]\n\t" \
          "v_smfmac_f32_16x16x64_bf16 v[236:239], v[192:195], a[24:31], %[idx]\n\t" \
          "s_waitcnt vmcnt(6)\n\t" \
          "v_max3_u32 v240, v196, v197, v198\n\t" \
          "v_max_u32 v240, v240, v199\n\t" \
          "v_cmp_eq_u32 vcc, -1, v240\n\t" \
          "s_cbranch_vccnz Lo_p1_%=\n\t" \
          "Lo_k1_%=:\n\t" \
          "v_smfmac_f32_16x16x64_bf16 v[224:227], v[196:199], a[32:39], %[idx]\n\t" \
          "v_smfmac_f32_16x16x64_bf16 v[228:231], v[196:199], a[40:47], %[idx]\n\t" \
          "v_smfmac_f32_16x16x64_bf16 v[232:235], v[196:199], a[48:55], %[idx]\n\t" \
          "v_smfmac_f32_16x16x64_bf16 v[236:239], v[196:199], a[56:63], %[idx]\n\t" \
          "s_waitcnt vmcnt(5)\n\t" \
          "v_max3_u32 v240, v200, v201, v202\n\t" \
          "v_max_u32 v240, v240, v203\n\t" \
          "v_cmp_eq_u32 vcc, -1, v240\n\t" \
          "s_cbranch_vccnz Lo_p2_%=\n\t" \
          "Lo_k2_%=:\n\t" \
          "v_smfmac_f32_16x16x64_bf16 v[224:227], v[200:203], a[64:71], %[idx]\n\t" \
          "v_smfmac_f32_16x16x64_bf16 v[228:231], v[200:203], a[72:79], %[idx]\n\t" \
          "v_smfmac_f32_16x16x64_bf16 v[232:235], v[200:203], a[80:87], %[idx]\n\t" \
          "v_smfmac_f32_16x16x64_bf16 v[236:239], v[200:203], a[88:95], %[idx]\n\t" \
          "s_waitcnt vmcnt(4)\n\t" \
          "v_max3_u32 v240, v204, v205, v206\n\t" \
          "v_max_u32 v240, v240, v207\n\t" \
          "v_cmp_eq_u32 vcc, -1, v240\n\t" \
          "s_cbranch_vccnz Lo_p3_%=\n\t" \
          "Lo_k3_%=:\n\t" \
          "v_smfmac_f32_16x16x64_bf16 v[224:227], v[204:207], a[96:103], %[idx]\n\t" \
          "v_smfmac_f32_16x16x64_bf16 v[228:231], v[204:207], a[104:111], %[idx]\n\t" \
          "v_smfmac_f32_16x16x64_bf16 v[232:235], v[204:207], a[112:119], %[idx]\n\t" \
          "v_smfmac_f32_16x16x64_bf16 v[236:239], v[204:207], a[120:127], %[idx]\n\t" \
          "s_waitcnt vmcnt(3)\n\t" \
          "v_max3_u32 v240, v208, v209, v210\n\t" \
          "v_max_u32 v240, v240, v211\n\t" \
          "v_cmp_eq_u32 vcc, -1, v240\n\t" \
          "s_cbranch_vccnz Lo_p4_%=\n\t" \
          "Lo_k4_%=:\n\t" \
          "v_smfmac_f32_16x16x64_bf16 v[224:227], v[208:211], a[128:135], %[idx]\n\t" \
          "v_smfmac_f32_16x16x64_bf16 v[228:231], v[208:211], a[136:143], %[idx]\n\t" \
          "v_smfmac_f32_16x16x64_bf16 v[232:235], v[208:211], a[144:151], %[idx]\n\t" \
          "v_smfmac_f32_16x16x64_bf16 v[236:239], v[208:211], a[152:159], %[idx]\n\t" \
          "s_waitcnt vmcnt(2)\n\t" \
          "v_max3_u32 v240, v212, v213, v214\n\t" \
          "v_max_u32 v240, v240, v215\n\t" \
          "v_cmp_eq_u32 vcc, -1, v240\n\t" \
          "s_cbranch_vccnz Lo_p5_%=\n\t" \
          "Lo_k5_%=:\n\t" \
          "v_smfmac_f32_16x16x64_bf16 v[224:227], v[212:215], a[160:167], %[idx]\n\t" \
          "v_smfmac_f32_16x16x64_bf16 v[228:231], v[212:215], a[168:175], %[idx]\n\t" \
          "v_smfmac_f32_16x16x64_bf16 v[232:235], v[212:215], a[176:183], %[idx]\n\t" \
          "v_smfmac_f32_16x16x64_bf16 v[236:239], v[212:215], a[184:191], %[idx]\n\t" \
          "s_waitcnt vmcnt(1)\n\t" \
          "v_max3_u32 v240, v216, v217, v218\n\t" \
          "v_max_u32 v240, v240, v219\n\t" \
          "v_cmp_eq_u32 vcc, -1, v240\n\t" \
          "s_cbranch_vccnz Lo_p6_%=\n\t" \
          "Lo_k6_%=:\n\t" \
          "v_smfmac_f32_16x16x64_bf16 v[224:227], v[216:219], a[192:199], %[idx]\n\t" \
          "v_smfmac_f32_16x16x64_bf16 v[228:231], v[216:219], a[200:207], %[idx]\n\t" \
          "v_smfmac_f32_16x16x64_bf16 v[232:235], v[216:219], a[208:215], %[idx]\n\t" \
          "v_smfmac_f32_16x16x64_bf16 v[236:239], v[216:219], a[216:223], %[idx]\n\t" \
          "s_waitcnt vmcnt(0)\n\t" \
          "v_max3_u32 v240, v220, v221, v222\n\t" \
          "v_max_u32 v240, v240, v223\n\t" \
          "v_cmp_eq_u32 vcc, -1, v240\n\t" \
          "s_cbranch_vccnz Lo_p7_%=\n\t" \
          "Lo_k7_%=:\n\t" \
          "v_smfmac_f32_16x16x64_bf16 v[224:227], v[220:223], a[224:231], %[idx]\n\t" \
          "v_smfmac_f32_16x16x64_bf16 v[228:231], v[220:223], a[232:239], %[idx]\n\t" \
          "v_smfmac_f32_16x16x64_bf16 v[232:235], v[220:223], a[240:247], %[idx]\n\t" \
          "v_smfmac_f32_16x16x64_bf16 v[236:239], v[220:223], a[248:255], %[idx]\n\t" \
          "s_nop 7\n\t" \
          "v_add_f32 v242, v224, v225\n\t" \
          "v_add_f32 v244, v234, v235\n\t" \
          "s_cmp_lg_u32 %[w], 0\n\t" \
          "s_waitcnt lgkmcnt(0)\n\t" \
          "s_branch Lo_tail%=\n\t" \
          "Lo_p0_%=:\n\t" \
          "s_cmp_lt_i32 %[cnt], 0\n\t" \
          "s_cbranch_scc1 Lo_k0_%=\n\t" \
          "s_add_u32 %[cnt], %[cnt], 1\n\t" \
          "s_and_b32 vcc_lo, %[cnt], 3\n\t" \
          "s_cbranch_scc1 Lo_q0_%=\n\t" \
          "s_getpc_b64 s[48:49]\n\t" \
          "s_branch Lo_spin%=\n\t" \
          "Lo_q0_%=:\n\t" \
          "buffer_load_dwordx4 v[192:195], %[oo0], %[rso], %[ob] offen sc1\n\t" \
          "s_waitcnt vmcnt(0)\n\t" \
          "v_max3_u32 v240, v192, v193, v194\n\t" \
          "v_max_u32 v240, v240, v195\n\t" \
          "v_cmp_eq_u32 vcc, -1, v240\n\t" \
          "s_cbranch_vccnz Lo_p0_%=\n\t" \
          "buffer_load_dwordx4 v[196:199], %[oo1], %[rso], %[ob] offen sc1\n\t" \
          "buffer_load_dwordx4 v[200:203], %[oo2], %[rso], %[ob] offen sc1\n\t" \
          "buffer_load_dwordx4 v[204:207], %[oo3], %[rso], %[ob] offen sc1\n\t" \
          "buffer_load_dwordx4 v[208:211], %[oo4], %[rso], %[ob] offen sc1\n\t" \
          "buffer_load_dwordx4 v[212:215], %[oo5], %[rso], %[ob] offen sc1\n\t" \
          "buffer_load_dwordx4 v[216:219], %[oo6], %[rso], %[ob] offen sc1\n\t" \
          "buffer_load_dwordx4 v[220:223], %[oo7], %[rso], %[ob] offen sc1\n\t" \
          "s_branch Lo_k0_%=\n\t" \
          "Lo_p1_%=:\n\t" \
          "s_cmp_lt_i32 %[cnt], 0\n\t" \
          "s_cbranch_scc1 Lo_k1_%=\n\t" \
          "s_add_u32 %[cnt], %[cnt], 1\n\t" \
          "s_and_b32 vcc_lo, %[cnt], 3\n\t" \
          "s_cbranch_scc1 Lo_q1_%=\n\t" \
          "s_getpc_b64 s[48:49]\n\t" \
          "s_branch Lo_spin%=\n\t" \
          "Lo_q1_%=:\n\t" \
          "buffer_load_dwordx4 v[196:199], %[oo1], %[rso], %[ob] offen sc1\n\t" \
          "s_waitcnt vmcnt(0)\n\t" \
          "v_max3_u32 v240, v196, v197, v198\n\t" \
          "v_max_u32 v240, v240, v199\n\t" \
          "v_cmp_eq_u32 vcc, -1, v240\n\t" \
          "s_cbranch_vccnz Lo_p1_%=\n\t" \
          "buffer_load_dwordx4 v[200:203], %[oo2], %[rso], %[ob] offen sc1\n\t" \
          "buffer_load_dwordx4 v[204:207], %[oo3], %[rso], %[ob] offen sc1\n\t" \
          "buffer_load_dwordx4 v[208:211], %[oo4], %[rso], %[ob] offen sc1\n\t" \
          "buffer_load_dwordx4 v[212:215], %[oo5], %[rso], %[ob] offen sc1\n\t" \
          "buffer_load_dwordx4 v[216:219], %[oo6], %[rso], %[ob] offen sc1\n\t" \
          "buffer_load_dwordx4 v[220:223], %[oo7], %[rso], %[ob] offen sc1\n\t" \
          "s_branch Lo_k1_%=\n\t" \
          "Lo_p2_%=:\n\t" \
          "s_cmp_lt_i32 %[cnt], 0\n\t" \
          "s_cbranch_scc1 Lo_k2_%=\n\t" \
          "s_add_u32 %[cnt], %[cnt], 1\n\t" \
          "s_and_b32 vcc_lo, %[cnt], 3\n\t" \
          "s_cbranch_scc1 Lo_q2_%=\n\t" \
          "s_getpc_b64 s[48:49]\n\t" \
          "s_branch Lo_spin%=\n\t" \
          "Lo_q2_%=:\n\t" \
          "buffer_load_dwordx4 v[200:203], %[oo2], %[rso], %[ob] offen sc1\n\t" \
          "s_waitcnt vmcnt(0)\n\t" \
          "v_max3_u32 v240, v200, v201, v202\n\t" \
          "v_max_u32 v240, v240, v203\n\t" \
          "v_cmp_eq_u32 vcc, -1, v240\n\t" \
          "s_cbranch_vccnz Lo_p2_%=\n\t" \
          "buffer_load_dwordx4 v[204:207], %[oo3], %[rso], %[ob] offen sc1\n\t" \
          "buffer_load_dwordx4 v[208:211], %[oo4], %[rso], %[ob] offen sc1\n\t" \
          "buffer_load_dwordx4 v[212:215], %[oo5], %[rso], %[ob] offen sc1\n\t" \
          "buffer_load_dwordx4 v[216:219], %[oo6], %[rso], %[ob] offen sc1\n\t" \
          "buffer_load_dwordx4 v[220:223], %[oo7], %[rso], %[ob] offen sc1\n\t" \
          "s_branch Lo_k2_%=\n\t" \
          "Lo_p3_%=:\n\t" \
          "s_cmp_lt_i32 %[cnt], 0\n\t" \
          "s_cbranch_scc1 Lo_k3_%=\n\t" \
          "s_add_u32 %[cnt], %[cnt], 1\n\t" \
          "s_and_b32 vcc_lo, %[cnt], 3\n\t" \
          "s_cbranch_scc1 Lo_q3_%=\n\t" \
          "s_getpc_b64 s[48:49]\n\t" \
          "s_branch Lo_spin%=\n\t" \
          "Lo_q3_%=:\n\t" \
          "buffer_load_dwordx4 v[204:207], %[oo3], %[rso], %[ob] offen sc1\n\t" \
          "s_waitcnt vmcnt(0)\n\t" \
          "v_max3_u32 v240, v204, v205, v206\n\t" \
          "v_max_u32 v240, v240, v207\n\t" \
          "v_cmp_eq_u32 vcc, -1, v240\n\t" \
          "s_cbranch_vccnz Lo_p3_%=\n\t" \
          "buffer_load_dwordx4 v[208:211], %[oo4], %[rso], %[ob] offen sc1\n\t" \
          "buffer_load_dwordx4 v[212:215], %[oo5], %[rso], %[ob] offen sc1\n\t" \
          "buffer_load_dwordx4 v[216:219], %[oo6], %[rso], %[ob] offen sc1\n\t" \
          "buffer_load_dwordx4 v[220:223], %[oo7], %[rso], %[ob] offen sc1\n\t" \
          "s_branch Lo_k3_%=\n\t" \
          "Lo_p4_%=:\n\t" \
          "s_cmp_lt_i32 %[cnt], 0\n\t" \
          "s_cbranch_scc1 Lo_k4_%=\n\t" \
          "s_add_u32 %[cnt], %[cnt], 1\n\t" \
          "s_and_b32 vcc_lo, %[cnt], 3\n\t" \
          "s_cbranch_scc1 Lo_q4_%=\n\t" \
          "s_getpc_b64 s[48:49]\n\t" \
          "s_branch Lo_spin%=\n\t" \
          "Lo_q4_%=:\n\t" \
          "buffer_load_dwordx4 v[208:211], %[oo4], %[rso], %[ob] offen sc1\n\t" \
          "s_waitcnt vmcnt(0)\n\t" \
          "v_max3_u32 v240, v208, v209, v210\n\t" \
          "v_max_u32 v240, v240, v211\n\t" \
          "v_cmp_eq_u32 vcc, -1, v240\n\t" \
          "s_cbranch_vccnz Lo_p4_%=\n\t" \
          "buffer_load_dwordx4 v[212:215], %[oo5], %[rso], %[ob] offen sc1\n\t" \
          "buffer_load_dwordx4 v[216:219], %[oo6], %[rso], %[ob] offen sc1\n\t" \
          "buffer_load_dwordx4 v[220:223], %[oo7], %[rso], %[ob] offen sc1\n\t" \
          "s_branch Lo_k4_%=\n\t" \
          "Lo_p5_%=:\n\t" \
          "s_cmp_lt_i32 %[cnt], 0\n\t" \
          "s_cbranch_scc1 Lo_k5_%=\n\t" \
          "s_add_u32 %[cnt], %[cnt], 1\n\t" \
          "s_and_b32 vcc_lo, %[cnt], 3\n\t" \
          "s_cbranch_scc1 Lo_q5_%=\n\t" \
          "s_getpc_b64 s[48:49]\n\t" \
          "s_branch Lo_spin%=\n\t" \
          "Lo_q5_%=:\n\t" \
          "buffer_load_dwordx4 v[212:215], %[oo5], %[rso], %[ob] offen sc1\n\t" \
          "s_waitcnt vmcnt(0)\n\t" \
          "v_max3_u32 v240, v212, v213, v214\n\t" \
          "v_max_u32 v240, v240, v215\n\t" \
          "v_cmp_eq_u32 vcc, -1, v240\n\t" \
          "s_cbranch_vccnz Lo_p5_%=\n\t" \
          "buffer_load_dwordx4 v[216:219], %[oo6], %[rso], %[ob] offen sc1\n\t" \
          "buffer_load_dwordx4 v[220:223], %[oo7], %[rso], %[ob] offen sc1\n\t" \
          "s_branch Lo_k5_%=\n\t" \
          "Lo_p6_%=:\n\t" \
          "s_cmp_lt_i32 %[cnt], 0\n\t" \
          "s_cbranch_scc1 Lo_k6_%=\n\t" \
          "s_add_u32 %[cnt], %[cnt], 1\n\t" \
          "s_and_b32 vcc_lo, %[cnt], 3\n\t" \
          "s_cbranch_scc1 Lo_q6_%=\n\t" \
          "s_getpc_b64 s[48:49]\n\t" \
          "s_branch Lo_spin%=\n\t" \
          "Lo_q6_%=:\n\t" \
          "buffer_load_dwordx4 v[216:219], %[oo6], %[rso], %[ob] offen sc1\n\t" \
          "s_waitcnt vmcnt(0)\n\t" \
          "v_max3_u32 v240, v216, v217, v218\n\t" \
          "v_max_u32 v240, v240, v219\n\t" \
          "v_cmp_eq_u32 vcc, -1, v240\n\t" \
          "s_cbranch_vccnz Lo_p6_%=\n\t" \
          "buffer_load_dwordx4 v[220:223], %[oo7], %[rso], %[ob] offen sc1\n\t" \
          "s_branch Lo_k6_%=\n\t" \
          "Lo_p7_%=:\n\t" \
          "s_cmp_lt_i32 %[cnt], 0\n\t" \
          "s_cbranch_scc1 Lo_k7_%=\n\t" \
          "s_add_u32 %[cnt], %[cnt], 1\n\t" \
          "s_and_b32 vcc_lo, %[cnt], 3\n\t" \
          "s_cbranch_scc1 Lo_q7_%=\n\t" \
          "s_getpc_b64 s[48:49]\n\t" \
          "s_branch Lo_spin%=\n\t" \
          "Lo_q7_%=:\n\t" \
          "buffer_load_dwordx4 v[220:223], %[oo7], %[rso], %[ob] offen sc1\n\t" \
          "s_waitcnt vmcnt(0)\n\t" \
          "v_max3_u32 v240, v220, v221, v222\n\t" \
          "v_max_u32 v240, v240, v223\n\t" \
          "v_cmp_eq_u32 vcc, -1, v240\n\t" \
          "s_cbranch_vccnz Lo_p7_%=\n\t" \
          "s_branch Lo_k7_%=\n\t" \
          "Lo_spin%=:\n\t" \
          "buffer_load_dword v241, off, %[rsc], %[tmo] sc1\n\t" \
          "s_add_u32 s48, s48, 4\n\t" \
          "s_addc_u32 s49, s49, 0\n\t" \
          "s_waitcnt vmcnt(0)\n\t" \
          "v_readfirstlane_b32 vcc_lo, v241\n\t" \
          "s_lshr_b32 vcc_hi, %[cnt], 16\n\t" \
          "s_or_b32 vcc_lo, vcc_lo, vcc_hi\n\t" \
          "s_cbranch_scc0 Lo_back%=\n\t" \
          "v_mov_b32 v241, 1\n\t" \
          "buffer_store_dword v241, off, %[rsc], %[tmo] sc1\n\t" \
          "s_mov_b32 %[cnt], -1\n\t" \
          "Lo_back%=:\n\t" \
          "s_setpc_b64 s[48:49]\n\t" \
          "Lo_fa%=:\n\t" \
          "s_add_u32 %[ta], %[pm], %[shf]\n\t" \
          "s_branch Lo_fb%=\n\t" \
          "Lo_ka%=:\n\t" \
          "s_add_u32 vcc_lo, %[ta], %[r12]\n\t" \
          "s_min_u32 vcc_lo, vcc_lo, -2\n\t" \
          "v_mov_b32 v252, vcc_lo\n\t" \
          "v_mov_b32 v253, %[rd]\n\t" \
          "s_mov_b64 exec, 1\n\t" \
          "buffer_store_dwordx2 v[252:253], off, %[rsm], %[rco] sc1\n\t" \
          "s_mov_b64 exec, -1\n\t" \
          "s_branch Lo_kb%=\n\t" \
          "Lv_fail%=:\n\t" \
          "buffer_load_dwordx4 v[192:195], %[ga0], %[rsm], %[mbn] offen sc1\n\t" \
          "buffer_load_dwordx4 v[196:199], %[ga1], %[rsm], %[mbn] offen sc1\n\t" \
          "buffer_load_dwordx4 v[200:203], %[ga2], %[rsm], %[mbn] offen sc1\n\t" \
          "buffer_load_dwordx4 v[204:207], %[ga3], %[rsm], %[mbn] offen sc1\n\t" \
          "s_nop 3\n\t" \
          "v_mov_b64 v[176:177], 0\n\t" \
          "v_mov_b64 v[178:179], 0\n\t" \
          "v_mov_b64 v[180:181], 0\n\t" \
          "v_mov_b64 v[182:183], 0\n\t" \
          "v_mov_b64 v[184:185], 0\n\t" \
          "v_mov_b64 v[186:187], 0\n\t" \
          "v_mov_b64 v[188:189], 0\n\t" \
          "v_mov_b64 v[190:191], 0\n\t" \
          "s_mov_b64 %[en], 0\n\t" \
          "s_mov_b32 %[cnt], 0\n\t" \
          "s_waitcnt vmcnt(3)\n\t" \
          "v_max3_u32 v240, v192, v193, v194\n\t" \
          "v_max_u32 v240, v240, v195\n\t" \
          "v_cmp_eq_u32 vcc, -1, v240\n\t" \
          "s_cbranch_vccnz Lv_p0_%=\n\t" \
          "Lv_k0_%=:\n\t" \
          "s_waitcnt vmcnt(2)\n\t" \
          "v_max3_u32 v240, v196, v197, v198\n\t" \
          "v_max_u32 v240, v240, v199\n\t" \
          "v_cmp_eq_u32 vcc, -1, v240\n\t" \
          "s_cbranch_vccnz Lv_p1_%=\n\t" \
          "Lv_k1_%=:\n\t" \
          "s_waitcnt vmcnt(1)\n\t" \
          "v_max3_u32 v240, v200, v201, v202\n\t" \
          "v_max_u32 v240, v240, v203\n\t" \
          "v_cmp_eq_u32 vcc, -1, v240\n\t" \
          "s_cbranch_vccnz Lv_p2_%=\n\t" \
          "Lv_k2_%=:\n\t" \
          "s_waitcnt vmcnt(0)\n\t" \
          "v_max3_u32 v240, v204, v205, v206\n\t" \
          "v_max_u32 v240, v240, v207\n\t" \
          "v_cmp_eq_u32 vcc, -1, v240\n\t" \
          "s_cbranch_vccnz Lv_p3_%=\n\t" \
          "Lv_k3_%=:\n\t" \
          "s_branch Lv_go%=\n\t" \
          "Lv_p0_%=:\n\t" \
          "s_cmp_lt_i32 %[cnt], 0\n\t" \
          "s_cbranch_scc1 Lv_k0_%=\n\t" \
          "s_add_u32 %[cnt], %[cnt], 1\n\t" \
          "s_and_b32 vcc_lo, %[cnt], 3\n\t" \
          "s_cbranch_scc1 Lv_q0_%=\n\t" \
          "s_getpc_b64 s[48:49]\n\t" \
          "s_branch Lv_spin%=\n\t" \
          "Lv_q0_%=:\n\t" \
          "buffer_load_dwordx4 v[192:195], %[ga0], %[rsm], %[mbn] offen sc1\n\t" \
          "s_waitcnt vmcnt(0)\n\t" \
          "v_max3_u32 v240, v192, v193, v194\n\t" \
          "v_max_u32 v240, v240, v195\n\t" \
          "v_cmp_eq_u32 vcc, -1, v240\n\t" \
          "s_cbranch_vccnz Lv_p0_%=\n\t" \
          "buffer_load_dwordx4 v[196:199], %[ga1], %[rsm], %[mbn] offen sc1\n\t" \
          "buffer_load_dwordx4 v[200:203], %[ga2], %[rsm], %[mbn] offen sc1\n\t" \
          "buffer_load_dwordx4 v[204:207], %[ga3], %[rsm], %[mbn] offen sc1\n\t" \
          "s_branch Lv_k0_%=\n\t" \
          "Lv_p1_%=:\n\t" \
          "s_cmp_lt_i32 %[cnt], 0\n\t" \
          "s_cbranch_scc1 Lv_k1_%=\n\t" \
          "s_add_u32 %[cnt], %[cnt], 1\n\t" \
          "s_and_b32 vcc_lo, %[cnt], 3\n\t" \
          "s_cbranch_scc1 Lv_q1_%=\n\t" \
          "s_getpc_b64 s[48:49]\n\t" \
          "s_branch Lv_spin%=\n\t" \
          "Lv_q1_%=:\n\t" \
          "buffer_load_dwordx4 v[196:199], %[ga1], %[rsm], %[mbn] offen sc1\n\t" \
          "s_waitcnt vmcnt(0)\n\t" \
          "v_max3_u32 v240, v196, v197, v198\n\t" \
          "v_max_u32 v240, v240, v199\n\t" \
          "v_cmp_eq_u32 vcc, -1, v240\n\t" \
          "s_cbranch_vccnz Lv_p1_%=\n\t" \
          "buffer_load_dwordx4 v[200:203], %[ga2], %[rsm], %[mbn] offen sc1\n\t" \
          "buffer_load_dwordx4 v[204:207], %[ga3], %[rsm], %[mbn] offen sc1\n\t" \
          "s_branch Lv_k1_%=\n\t" \
          "Lv_p2_%=:\n\t" \
          "s_cmp_lt_i32 %[cnt], 0\n\t" \
          "s_cbranch_scc1 Lv_k2_%=\n\t" \
          "s_add_u32 %[cnt], %[cnt], 1\n\t" \
          "s_and_b32 vcc_lo, %[cnt], 3\n\t" \
          "s_cbranch_scc1 Lv_q2_%=\n\t" \
          "s_getpc_b64 s[48:49]\n\t" \
          "s_branch Lv_spin%=\n\t" \
          "Lv_q2_%=:\n\t" \
          "buffer_load_dwordx4 v[200:203], %[ga2], %[rsm], %[mbn] offen sc1\n\t" \
          "s_waitcnt vmcnt(0)\n\t" \
          "v_max3_u32 v240, v200, v201, v202\n\t" \
          "v_max_u32 v240, v240, v203\n\t" \
          "v_cmp_eq_u32 vcc, -1, v240\n\t" \
          "s_cbranch_vccnz Lv_p2_%=\n\t" \
          "buffer_load_dwordx4 v[204:207], %[ga3], %[rsm], %[mbn] offen sc1\n\t" \
          "s_branch Lv_k2_%=\n\t" \
          "Lv_p3_%=:\n\t" \
          "s_cmp_lt_i32 %[cnt], 0\n\t" \
          "s_cbranch_scc1 Lv_k3_%=\n\t" \
          "s_add_u32 %[cnt], %[cnt], 1\n\t" \
          "s_and_b32 vcc_lo, %[cnt], 3\n\t" \
          "s_cbranch_scc1 Lv_q3_%=\n\t" \
          "s_getpc_b64 s[48:49]\n\t" \
          "s_branch Lv_spin%=\n\t" \
          "Lv_q3_%=:\n\t" \
          "buffer_load_dwordx4 v[204:207], %[ga3], %[rsm], %[mbn] offen sc1\n\t" \
          "s_waitcnt vmcnt(0)\n\t" \
          "v_max3_u32 v240, v204, v205, v206\n\t" \
          "v_max_u32 v240, v240, v207\n\t" \
          "v_cmp_eq_u32 vcc, -1, v240\n\t" \
          "s_cbranch_vccnz Lv_p3_%=\n\t" \
          "s_branch Lv_k3_%=\n\t" \
          "Lv_spin%=:\n\t" \
          "buffer_load_dword v241, off, %[rsc], %[tmo] sc1\n\t" \
          "s_add_u32 s48, s48, 4\n\t" \
          "s_addc_u32 s49, s49, 0\n\t" \
          "s_waitcnt vmcnt(0)\n\t" \
          "v_readfirstlane_b32 vcc_lo, v241\n\t" \
          "s_lshr_b32 vcc_hi, %[cnt], 16\n\t" \
          "s_or_b32 vcc_lo, vcc_lo, vcc_hi\n\t" \
          "s_cbranch_scc0 Lv_back%=\n\t" \
          "v_mov_b32 v241, 1\n\t" \
          "buffer_store_dword v241, off, %[rsc], %[tmo] sc1\n\t" \
          "s_mov_b32 %[cnt], -1\n\t" \
          "Lv_back%=:\n\t" \
          "s_setpc_b64 s[48:49]\n\t" \
          "Lo_al%=:\n\t" \
          "v_subrev_u32 v252, %[t], v255\n\t" \
          "v_bfe_i32 v252, v252, 0, 16\n\t" \
          "v_cndmask_b32 v252, 0, v252, vcc\n\t" \
          "v_cmp_gt_u32 vcc, 1000, v252\n\t" \
          "s_nop 1\n\t" \
          "v_cndmask_b32 v252, 0, v252, vcc\n\t" \
          "s_nop 1\n\t" \
          "v_max_i32_dpp v252, v252, v252 row_ror:8 row_mask:0xf bank_mask:0xf\n\t" \
          "s_nop 1\n\t" \
          "v_max_i32_dpp v252, v252, v252 row_ror:4 row_mask:0xf bank_mask:0xf\n\t" \
          "s_nop 1\n\t" \
          "v_max_i32_dpp v252, v252, v252 row_ror:2 row_mask:0xf bank_mask:0xf\n\t" \
          "s_nop 1\n\t" \
          "v_max_i32_dpp v252, v252, v252 row_ror:1 row_mask:0xf bank_mask:0xf\n\t" \
          "s_nop 0\n\t" \
          "v_readfirstlane_b32 vcc_lo, v252\n\t" \
          "s_add_u32 %[t], %[t], vcc_lo\n\t" \
          "s_memrealtime vcc\n\t" \
          "s_waitcnt lgkmcnt(0)\n\t" \
          "s_sub_i32 vcc_lo, %[t], vcc_lo\n\t" \
          "s_cmp_gt_i32 vcc_lo, %[lk]\n\t" \
          "s_cbranch_scc0 Lo_w%=\n\t" \
          "Lo_s%=:\n\t" \
          "s_sleep 1\n\t" \
          "Lo_c%=:\n\t" \
          "s_memrealtime vcc\n\t" \
          "s_waitcnt lgkmcnt(0)\n\t" \
          "s_sub_i32 vcc_lo, %[t], vcc_lo\n\t" \
          "s_cmp_gt_i32 vcc_lo, %[lk]\n\t" \
          "s_cbranch_scc1 Lo_s%=\n\t" \
          "buffer_load_dword v255, %[al1], %[rsm], %[mb] offen sc1\n\t" \
          "s_waitcnt vmcnt(0)\n\t" \
          "v_lshrrev_b32 v252, 16, v255\n\t" \
          "v_cmp_eq_u32 vcc, 1, v252\n\t" \
          "s_cbranch_vccnz Lo_al%=\n\t" \
          "Lo_w%=:\n\t" \
          "s_memrealtime s[50:51]\n\t" \
          "s_getpc_b64 s[48:49]\n\t" \
          "Lo_y%=:\n\t" \
          "s_add_u32 s48, s48, Lo_z%=-Lo_y%=\n\t" \
          "s_addc_u32 s49, s49, 0\n\t" \
          "Lo_v%=:\n\t" \
          "s_memrealtime s[52:53]\n\t" \
          "s_waitcnt lgkmcnt(1)\n\t" \
          "s_sub_i32 vcc_lo, %[t], s50\n\t" \
          "s_cmp_gt_i32 vcc_lo, 3\n\t" \
          "s_cbranch_scc0 Lo_x%=\n\t" \
          "s_memrealtime s[50:51]\n\t" \
          "s_waitcnt lgkmcnt(1)\n\t" \
          "s_sub_i32 vcc_lo, %[t], s52\n\t" \
          "s_cmp_gt_i32 vcc_lo, 3\n\t" \
          "s_cbranch_scc0 Lo_x%=\n\t" \
          "s_memrealtime s[52:53]\n\t" \
          "s_waitcnt lgkmcnt(1)\n\t" \
          "s_sub_i32 vcc_lo, %[t], s50\n\t" \
          "s_cmp_gt_i32 vcc_lo, 3\n\t" \
          "s_cbranch_scc0 Lo_x%=\n\t" \
          "s_memrealtime s[50:51]\n\t" \
          "s_waitcnt lgkmcnt(1)\n\t" \
          "s_sub_i32 vcc_lo, %[t], s52\n\t" \
          "s_cmp_gt_i32 vcc_lo, 3\n\t" \
          "s_cbranch_scc0 Lo_x%=\n\t" \
          "s_memrealtime s[52:53]\n\t" \
          "s_waitcnt lgkmcnt(1)\n\t" \
          "s_sub_i32 vcc_lo, %[t], s50\n\t" \
          "s_cmp_gt_i32 vcc_lo, 3\n\t" \
          "s_cbranch_scc0 Lo_x%=\n\t" \
          "s_memrealtime s[50:51]\n\t" \
          "s_waitcnt lgkmcnt(1)\n\t" \
          "s_sub_i32 vcc_lo, %[t], s52\n\t" \
          "s_cmp_gt_i32 vcc_lo, 3\n\t" \
          "s_cbranch_scc0 Lo_x%=\n\t" \
          "s_memrealtime s[52:53]\n\t" \
          "s_waitcnt lgkmcnt(1)\n\t" \
          "s_sub_i32 vcc_lo, %[t], s50\n\t" \
          "s_cmp_gt_i32 vcc_lo, 3\n\t" \
          "s_cbranch_scc0 Lo_x%=\n\t" \
          "s_memrealtime s[50:51]\n\t" \
          "s_waitcnt lgkmcnt(1)\n\t" \
          "s_sub_i32 vcc_lo, %[t], s52\n\t" \
          "s_cmp_gt_i32 vcc_lo, 3\n\t" \
          "s_cbranch_scc0 Lo_x%=\n\t" \
          "s_branch Lo_v%=\n\t" \
          "Lo_x%=:\n\t" \
          "s_max_i32 vcc_lo, vcc_lo, 0\n\t" \
          "s_lshl_b32 vcc_lo, vcc_lo, 2\n\t" \
          "s_sub_u32 s48, s48, vcc_lo\n\t" \
          "s_subb_u32 s49, s49, 0\n\t" \
          "s_setpc_b64 s[48:49]\n\t" \
          "s_nop 5\n\t" \
          "s_nop 5\n\t" \
          "s_nop 5\n\t" \
          "Lo_z%=:\n\t" \
          "s_barrier\n\t" \
          VO_ST0 \
          "buffer_load_dwordx4 v[192:195], %[oo0], %[rso], %[ob] offen sc1\n\t" \
          "s_nop 9\n\t" \
          "buffer_load_dwordx4 v[196:199], %[oo1], %[rso], %[ob] offen sc1\n\t" \
          "s_nop 9\n\t" \
          "buffer_load_dwordx4 v[200:203], %[oo2], %[rso], %[ob] offen sc1\n\t" \
          "s_nop 9\n\t" \
          "buffer_load_dwordx4 v[204:207], %[oo3], %[rso], %[ob] offen sc1\n\t" \
          "s_nop 9\n\t" \
          "buffer_load_dwordx4 v[208:211], %[oo4], %[rso], %[ob] offen sc1\n\t" \
          "s_nop 9\n\t" \
          "buffer_load_dwordx4 v[212:215], %[oo5], %[rso], %[ob] offen sc1\n\t" \
          "s_nop 9\n\t" \
          "buffer_load_dwordx4 v[216:219], %[oo6], %[rso], %[ob] offen sc1\n\t" \
          "s_nop 9\n\t" \
          "buffer_load_dwordx4 v[220:223], %[oo7], %[rso], %[ob] offen sc1\n\t" \
          "s_waitcnt vmcnt(7)\n\t" \
          "v_smfmac_f32_16x16x64_bf16 v[224:227], v[192:195], a[0:7], %[idx]\n\t" \
          "v_max3_u32 v240, v192, v193, v194\n\t" \
          "v_smfmac_f32_16x16x64_bf16 v[228:231], v[192:195], a[8:15], %[idx]\n\t" \
          "v_max3_u32 v240, v194, v195, v240\n\t" \
          "v_smfmac_f32_16x16x64_bf16 v[232:235], v[192:195], a[16:23], %[idx]\n\t" \
          "v_cmp_eq_u32 vcc, -1, v240\n\t" \
          "v_smfmac_f32_16x16x64_bf16 v[236:239], v[192:195], a[24:31], %[idx]\n\t" \
          "s_waitcnt vmcnt(6)\n\t" \
          "v_smfmac_f32_16x16x64_bf16 v[224:227], v[196:199], a[32:39], %[idx]\n\t" \
          "s_cbranch_vccnz Lo_f0_%=\n\t" \
          "v_max3_u32 v240, v196, v197, v198\n\t" \
          "v_smfmac_f32_16x16x64_bf16 v[228:231], v[196:199], a[40:47], %[idx]\n\t" \
          "v_max3_u32 v240, v198, v199, v240\n\t" \
          "v_smfmac_f32_16x16x64_bf16 v[232:235], v[196:199], a[48:55], %[idx]\n\t" \
          "v_cmp_eq_u32 vcc, -1, v240\n\t" \
          "v_smfmac_f32_16x16x64_bf16 v[236:239], v[196:199], a[56:63], %[idx]\n\t" \
          "s_waitcnt vmcnt(5)\n\t" \
          "v_smfmac_f32_16x16x64_bf16 v[224:227], v[200:203], a[64:71], %[idx]\n\t" \
          "s_cbranch_vccnz Lo_f1_%=\n\t" \
          "v_max3_u32 v240, v200, v201, v202\n\t" \
          "v_smfmac_f32_16x16x64_bf16 v[228:231], v[200:203], a[72:79], %[idx]\n\t" \
          "v_max3_u32 v240, v202, v203, v240\n\t" \
          "v_smfmac_f32_16x16x64_bf16 v[232:235], v[200:203], a[80:87], %[idx]\n\t" \
          "v_cmp_eq_u32 vcc, -1, v240\n\t" \
          "v_smfmac_f32_16x16x64_bf16 v[236:239], v[200:203], a[88:95], %[idx]\n\t" \
          "s_waitcnt vmcnt(4)\n\t" \
          "v_smfmac_f32_16x16x64_bf16 v[224:227], v[204:207], a[96:103], %[idx]\n\t" \
          "s_cbranch_vccnz Lo_f2_%=\n\t" \
          "v_max3_u32 v240, v204, v205, v206\n\t" \
          "v_smfmac_f32_16x16x64_bf16 v[228:231], v[204:207], a[104:111], %[idx]\n\t" \
          "v_max3_u32 v240, v206, v207, v240\n\t" \
          "v_smfmac_f32_16x16x64_bf16 v[232:235], v[204:207], a[112:119], %[idx]\n\t" \
          "v_cmp_eq_u32 vcc, -1, v240\n\t" \
          "v_smfmac_f32_16x16x64_bf16 v[236:239], v[204:207], a[120:127], %[idx]\n\t" \
          "s_waitcnt vmcnt(3)\n\t" \
          "v_smfmac_f32_16x16x64_bf16 v[224:227], v[208:211], a[128:135], %[idx]\n\t" \
          "s_cbranch_vccnz Lo_f3_%=\n\t" \
          "v_max3_u32 v240, v208, v209, v210\n\t" \
          "v_smfmac_f32_16x16x64_bf16 v[228:231], v[208:211], a[136:143], %[idx]\n\t" \
          "v_max3_u32 v240, v210, v211, v240\n\t" \
          "v_smfmac_f32_16x16x64_bf16 v[232:235], v[208:211], a[144:151], %[idx]\n\t" \
          "v_cmp_eq_u32 vcc, -1, v240\n\t" \
          "v_smfmac_f32_16x16x64_bf16 v[236:239], v[208:211], a[152:159], %[idx]\n\t" \
          "s_waitcnt vmcnt(2)\n\t" \
          "v_smfmac_f32_16x16x64_bf16 v[224:227], v[212:215], a[160:167], %[idx]\n\t" \
          "s_cbranch_vccnz Lo_f4_%=\n\t" \
          "v_max3_u32 v240, v212, v213, v214\n\t" \
          "v_smfmac_f32_16x16x64_bf16 v[228:231], v[212:215], a[168:175], %[idx]\n\t" \
          "v_max3_u32 v240, v214, v215, v240\n\t" \
          "v_smfmac_f32_16x16x64_bf16 v[232:235], v[212:215], a[176:183], %[idx]\n\t" \
          "v_cmp_eq_u32 vcc, -1, v240\n\t" \
          "v_smfmac_f32_16x16x64_bf16 v[236:239], v[212:215], a[184:191], %[idx]\n\t" \
          "s_waitcnt vmcnt(1)\n\t" \
          "v_smfmac_f32_16x16x64_bf16 v[224:227], v[216:219], a[192:199], %[idx]\n\t" \
          "s_cbranch_vccnz Lo_f5_%=\n\t" \
          "v_max3_u32 v240, v216, v217, v218\n\t" \
          "v_smfmac_f32_16x16x64_bf16 v[228:231], v[216:219], a[200:207], %[idx]\n\t" \
          "v_max3_u32 v240, v218, v219, v240\n\t" \
          "v_smfmac_f32_16x16x64_bf16 v[232:235], v[216:219], a[208:215], %[idx]\n\t" \
          "v_cmp_eq_u32 vcc, -1, v240\n\t" \
          "v_smfmac_f32_16x16x64_bf16 v[236:239], v[216:219], a[216:223], %[idx]\n\t" \
          "s_waitcnt vmcnt(0)\n\t" \
          "v_smfmac_f32_16x16x64_bf16 v[224:227], v[220:223], a[224:231], %[idx]\n\t" \
          "s_cbranch_vccnz Lo_f6_%=\n\t" \
          "v_max3_u32 v240, v220, v221, v222\n\t" \
          "v_smfmac_f32_16x16x64_bf16 v[232:235], v[220:223], a[240:247], %[idx]\n\t" \
          "v_max3_u32 v240, v222, v223, v240\n\t" \
          "v_smfmac_f32_16x16x64_bf16 v[228:231], v[220:223], a[232:239], %[idx]\n\t" \
          "v_cmp_eq_u32 vcc, -1, v240\n\t" \
          "v_smfmac_f32_16x16x64_bf16 v[236:239], v[220:223], a[248:255], %[idx]\n\t" \
          VO_ST1 \
          "s_cmp_lg_u32 %[w], 0\n\t" \
          "v_add_f32 v242, v224, v225\n\t" \
          "v_add_f32 v244, v234, v235\n\t" \
          "s_cbranch_vccnz Lo_f7_%=\n\t" \
          "Lo_tail%=:\n\t" \
          "v_add_f32 v243, v228, v229\n\t" \
          "v_add_f32 v242, v242, v244\n\t" \
          "s_nop 0\n\t" \
          "v_add_f32 v243, v243, v238\n\t" \
          "s_cbranch_scc0 Lo_w0%=\n\t" \
          "v_add_f32 v243, v243, v239\n\t" \
          "ds_write_b64 %[rw], v[242:243]\n\t" \
          "s_waitcnt lgkmcnt(0)\n\t" \
          "s_barrier\n\t" \
          "s_cmp_eq_u32 %[w], 3\n\t" \
          "s_cbranch_scc0 Lv_z%=\n\t" \
          "s_sleep 1\n\t" \
          "s_branch Lo_end%=\n\t" \
          "Lo_w0%=:\n\t" \
          "v_add_f32 v243, v243, v239\n\t" \
          "s_barrier\n\t" \
          "ds_read_b64 v[244:245], %[rw] offset:512\n\t" \
          "ds_read_b64 v[246:247], %[rw] offset:1024\n\t" \
          "ds_read_b64 v[248:249], %[rw] offset:1536\n\t" \
          "s_waitcnt lgkmcnt(2)\n\t" \
          "v_pk_add_f32 v[242:243], v[242:243], v[244:245]\n\t" \
          "s_waitcnt lgkmcnt(1)\n\t" \
          "v_pk_add_f32 v[242:243], v[242:243], v[246:247]\n\t" \
          "s_waitcnt lgkmcnt(0)\n\t" \
          "v_pk_add_f32 v[242:243], v[242:243], v[248:249]\n\t" \
          "s_nop 0\n\t" \
          "v_cvt_pk_bf16_f32 v242, v242, v243\n\t" \
          "v_min_u32 v242, -2, v242\n\t" \
          "buffer_store_dword v242, %[mo], %[rsm], %[mbn] offen sc1\n\t" \
          VO_ST3 \
          "s_branch Lv_z%=\n\t" \
          "Lo_end%=:\n\t" \
          VO_ST3 \
          "s_memrealtime vcc\n\t" \
          "ds_read_b32 v241, %[fa]\n\t" \
          "s_add_u32 %[ta], %[t], %[d2]\n\t" \
          "s_waitcnt lgkmcnt(0)\n\t" \
          "s_mov_b32 %[pm], vcc_lo\n\t" \
          "v_readfirstlane_b32 %[fw], v241\n\t" \
          "s_add_u32 vcc_lo, vcc_lo, %[mn]\n\t" \
          "s_sub_i32 vcc_hi, vcc_lo, %[ta]\n\t" \
          "s_cmp_gt_i32 vcc_hi, 0\n\t" \
          "s_cselect_b32 %[ta], vcc_lo, %[ta]\n\t" \
          "s_cmp_eq_u32 %[fw], %[s1]\n\t" \
          "s_cbranch_scc1 Lo_fa%=\n\t" \
          "Lo_fb%=:\n\t" \
          "s_cmp_eq_u32 %[rd], 0\n\t" \
          "s_cbranch_scc0 Lo_ka%=\n\t" \
          "Lo_kb%=:\n\t" \
          "s_add_u32 %[cnt], %[ta], %[dv]\n\t" \
          "Lv_w%=:\n\t" \
          "s_memrealtime s[50:51]\n\t" \
          "s_getpc_b64 s[48:49]\n\t" \
          "Lv_y%=:\n\t" \
          "s_add_u32 s48, s48, Lv_z%=-Lv_y%=\n\t" \
          "s_addc_u32 s49, s49, 0\n\t" \
          "Lv_v%=:\n\t" \
          "s_memrealtime s[52:53]\n\t" \
          "s_waitcnt lgkmcnt(1)\n\t" \
          "s_sub_i32 vcc_lo, %[cnt], s50\n\t" \
          "s_cmp_gt_i32 vcc_lo, 3\n\t" \
          "s_cbranch_scc0 Lv_x%=\n\t" \
          "s_memrealtime s[50:51]\n\t" \
          "s_waitcnt lgkmcnt(1)\n\t" \
          "s_sub_i32 vcc_lo, %[cnt], s52\n\t" \
          "s_cmp_gt_i32 vcc_lo, 3\n\t" \
          "s_cbranch_scc0 Lv_x%=\n\t" \
          "s_memrealtime s[52:53]\n\t" \
          "s_waitcnt lgkmcnt(1)\n\t" \
          "s_sub_i32 vcc_lo, %[cnt], s50\n\t" \
          "s_cmp_gt_i32 vcc_lo, 3\n\t" \
          "s_cbranch_scc0 Lv_x%=\n\t" \
          "s_memrealtime s[50:51]\n\t" \
          "s_waitcnt lgkmcnt(1)\n\t" \
          "s_sub_i32 vcc_lo, %[cnt], s52\n\t" \
          "s_cmp_gt_i32 vcc_lo, 3\n\t" \
          "s_cbranch_scc0 Lv_x%=\n\t" \
          "s_memrealtime s[52:53]\n\t" \
          "s_waitcnt lgkmcnt(1)\n\t" \
          "s_sub_i32 vcc_lo, %[cnt], s50\n\t" \
          "s_cmp_gt_i32 vcc_lo, 3\n\t" \
          "s_cbranch_scc0 Lv_x%=\n\t" \
          "s_memrealtime s[50:51]\n\t" \
          "s_waitcnt lgkmcnt(1)\n\t" \
          "s_sub_i32 vcc_lo, %[cnt], s52\n\t" \
          "s_cmp_gt_i32 vcc_lo, 3\n\t" \
          "s_cbranch_scc0 Lv_x%=\n\t" \
          "s_memrealtime s[52:53]\n\t" \
          "s_waitcnt lgkmcnt(1)\n\t" \
          "s_sub_i32 vcc_lo, %[cnt], s50\n\t" \
          "s_cmp_gt_i32 vcc_lo, 3\n\t" \
          "s_cbranch_scc0 Lv_x%=\n\t" \
          "s_memrealtime s[50:51]\n\t" \
          "s_waitcnt lgkmcnt(1)\n\t" \
          "s_sub_i32 vcc_lo, %[cnt], s52\n\t" \
          "s_cmp_gt_i32 vcc_lo, 3\n\t" \
          "s_cbranch_scc0 Lv_x%=\n\t" \
          "s_branch Lv_v%=\n\t" \
          "Lv_x%=:\n\t" \
          "s_max_i32 vcc_lo, vcc_lo, 0\n\t" \
          "s_lshl_b32 vcc_lo, vcc_lo, 2\n\t" \
          "s_sub_u32 s48, s48, vcc_lo\n\t" \
          "s_subb_u32 s49, s49, 0\n\t" \
          "s_setpc_b64 s[48:49]\n\t" \
          "s_nop 5\n\t" \
          "s_nop 5\n\t" \
          "s_nop 5\n\t" \
          "Lv_z%=:\n\t" \
          "s_barrier\n\t" \
          "Lv_n%=:\n\t" \
          "buffer_load_dwordx4 v[192:195], %[ga0], %[rsm], %[mbn] offen sc1\n\t" \
          "s_nop 15\n\t" \
          "s_nop 5\n\t" \
          "buffer_load_dwordx4 v[196:199], %[ga1], %[rsm], %[mbn] offen sc1\n\t" \
          "s_nop 15\n\t" \
          "s_nop 5\n\t" \
          "buffer_load_dwordx4 v[200:203], %[ga2], %[rsm], %[mbn] offen sc1\n\t" \
          "s_nop 15\n\t" \
          "s_nop 5\n\t" \
          "buffer_load_dwordx4 v[204:207], %[ga3], %[rsm], %[mbn] offen sc1\n\t" \
          "buffer_load_dword %[al], %[alo], %[rsm], %[mb] offen sc1\n\t" \
          "Lv_go%=:\n\t" \
          "s_waitcnt vmcnt(4)\n\t" \
          "v_smfmac_f32_16x16x64_bf16 v[176:179], v[192:195], v[8:15], %[idx]\n\t" \
          "v_max3_u32 v240, v192, v193, v194\n\t" \
          "v_smfmac_f32_16x16x64_bf16 v[180:183], v[192:195], v[16:23], %[idx]\n\t" \
          "v_max3_u32 v240, v194, v195, v240\n\t" \
          "v_smfmac_f32_16x16x64_bf16 v[184:187], v[192:195], v[24:31], %[idx]\n\t" \
          "buffer_load_dwordx2 %[rec], off, %[rsm], %[rco] sc1\n\t" \
          "v_smfmac_f32_16x16x64_bf16 v[188:191], v[192:195], v[32:39], %[idx]\n\t" \
          "s_waitcnt vmcnt(4)\n\t" \
          "v_smfmac_f32_16x16x64_bf16 v[176:179], v[196:199], v[40:47], %[idx]\n\t" \
          "v_max3_u32 v240, v196, v197, v240\n\t" \
          "v_smfmac_f32_16x16x64_bf16 v[180:183], v[196:199], v[48:55], %[idx]\n\t" \
          "v_max3_u32 v240, v198, v199, v240\n\t" \
          "v_smfmac_f32_16x16x64_bf16 v[184:187], v[196:199], v[56:63], %[idx]\n\t" \
          "v_smfmac_f32_16x16x64_bf16 v[188:191], v[196:199], v[64:71], %[idx]\n\t" \
          "s_waitcnt vmcnt(3)\n\t" \
          "v_smfmac_f32_16x16x64_bf16 v[176:179], v[200:203], v[72:79], %[idx]\n\t" \
          "v_max3_u32 v240, v200, v201, v240\n\t" \
          "v_smfmac_f32_16x16x64_bf16 v[180:183], v[200:203], v[80:87], %[idx]\n\t" \
          "v_max3_u32 v240, v202, v203, v240\n\t" \
          "v_smfmac_f32_16x16x64_bf16 v[184:187], v[200:203], v[88:95], %[idx]\n\t" \
          "v_smfmac_f32_16x16x64_bf16 v[188:191], v[200:203], v[96:103], %[idx]\n\t" \
          "s_waitcnt vmcnt(2)\n\t" \
          "v_smfmac_f32_16x16x64_bf16 v[176:179], v[204:207], v[104:111], %[idx]\n\t" \
          "v_max3_u32 v240, v204, v205, v240\n\t" \
          "v_smfmac_f32_16x16x64_bf16 v[180:183], v[204:207], v[112:119], %[idx]\n\t" \
          "v_max3_u32 v240, v206, v207, v240\n\t" \
          "v_smfmac_f32_16x16x64_bf16 v[184:187], v[204:207], v[120:127], %[idx]\n\t" \
          "v_cmp_eq_u32 vcc, -1, v240\n\t" \
          "v_smfmac_f32_16x16x64_bf16 v[188:191], v[204:207], v[128:135], %[idx]\n\t" \
          "s_and_b64 vcc, vcc, %[en]\n\t" \
          "v_add_f32 v242, v176, v177\n\t" \
          "v_add_f32 v243, v178, v179\n\t" \
          "v_add_f32 v244, v180, v181\n\t" \
          "v_add_f32 v245, v182, v183\n\t" \
          "s_cbranch_scc1 Lv_fail%=\n\t" \
          "v_add_f32 v246, v184, v185\n\t" \
          "v_add_f32 v247, v186, v187\n\t" \
          "ds_write_b64 %[vw0], v[242:243]\n\t" \
          "ds_write_b64 %[vw1], v[244:245]\n\t" \
          "ds_write_b64 %[vw2], v[246:247]\n\t" \
          "v_add_f32 v248, v188, v189\n\t" \
          "v_add_f32 v249, v190, v191\n\t" \
          "v_mfma_f32_16x16x32_bf16 v[208:211], v[212:215], v[212:215], 0\n\t" \
          "v_mfma_f32_16x16x32_bf16 v[208:211], v[212:215], v[212:215], 0\n\t" \
          "s_waitcnt lgkmcnt(0)\n\t" \
          "s_barrier\n\t" \
          "ds_read_b64 v[242:243], %[vr] offset:0\n\t" \
          "v_mfma_f32_16x16x32_bf16 v[208:211], v[212:215], v[212:215], 0\n\t" \
          "ds_read_b64 v[244:245], %[vr] offset:2064\n\t" \
          "ds_read_b64 v[246:247], %[vr] offset:4128\n\t" \
          "v_mfma_f32_16x16x32_bf16 v[208:211], v[212:215], v[212:215], 0\n\t" \
          "s_waitcnt lgkmcnt(2)\n\t" \
          "v_add_f32 v248, v248, v242\n\t" \
          "v_add_f32 v249, v249, v243\n\t" \
          "s_waitcnt lgkmcnt(1)\n\t" \
          "v_add_f32 v248, v248, v244\n\t" \
          "v_add_f32 v249, v249, v245\n\t" \
          "s_waitcnt lgkmcnt(0)\n\t" \
          "v_add_f32 v248, v248, v246\n\t" \
          "v_add_f32 v249, v249, v247\n\t" \
          "v_cvt_pk_bf16_f32 v242, v248, v249\n\t" \
          "v_min_u32 v242, -2, v242\n\t" \
          "buffer_store_dword v242, %[vo], %[rsv], %[vb] offen sc1\n\t" \
          "s_memrealtime %[te]\n\t" \
          "s_waitcnt vmcnt(1) lgkmcnt(0)\n\t"
#define VO_W "{a[0:31]}"(wa[0]), "{a[32:63]}"(wa[1]), "{a[64:95]}"(wa[2]), "{a[96:127]}"(wa[3]), "{a[128:159]}"(wa[4]), "{a[160:191]}"(wa[5]), "{a[192:223]}"(wa[6]), "{a[224:255]}"(wa[7]), "{v[8:39]}"(wv_[0]), "{v[40:71]}"(wv_[1]), "{v[72:103]}"(wv_[2]), "{v[104:135]}"(wv_[3])
#define VO_DEFW "=&{a[0:31]}"(wa[0]), "=&{a[32:63]}"(wa[1]), "=&{a[64:95]}"(wa[2]), "=&{a[96:127]}"(wa[3]), "=&{a[128:159]}"(wa[4]), "=&{a[160:191]}"(wa[5]), "=&{a[192:223]}"(wa[6]), "=&{a[224:255]}"(wa[7]), "=&{v[8:39]}"(wv_[0]), "=&{v[40:71]}"(wv_[1]), "=&{v[72:103]}"(wv_[2]), "=&{v[104:135]}"(wv_[3])
#define VO_LOADER \
          "s_mov_b64 s[48:49], %[wo]\n\ts_mov_b32 s50, %[wsz]\n\ts_mov_b32 s51, 0x20000\n\ts_nop 4\n\ts_add_u32 m0, %[stg], 0\n\ts_xor_b32 %[t], %[vk], 0x0\n\t" \
          "buffer_load_dwordx4 %[sv], %[rv], %[t] offen lds\n\ts_add_u32 m0, %[stg], 1024\n\ts_xor_b32 %[t], %[vk], 0x10000\n\tbuffer_load_dwordx4 %[sv], %[rv], %[t] offen lds\n\ts_add_u32 m0, %[stg], 2048\n\ts_xor_b32 %[t], %[vk], 0x20000\n\t" \
          "buffer_load_dwordx4 %[sv], %[rv], %[t] offen lds\n\ts_add_u32 m0, %[stg], 3072\n\ts_xor_b32 %[t], %[vk], 0x30000\n\tbuffer_load_dwordx4 %[sv], %[rv], %[t] offen lds\n\ts_add_u32 m0, %[stg], 4096\n\ts_xor_b32 %[t], %[vk], 0x40000\n\t" \
          "buffer_load_dwordx4 %[sv], %[rv], %[t] offen lds\n\ts_add_u32 m0, %[stg], 5120\n\ts_xor_b32 %[t], %[vk], 0x50000\n\tbuffer_load_dwordx4 %[sv], %[rv], %[t] offen lds\n\ts_add_u32 m0, %[stg], 6144\n\ts_xor_b32 %[t], %[vk], 0x60000\n\t" \
          "buffer_load_dwordx4 %[sv], %[rv], %[t] offen lds\n\ts_add_u32 m0, %[stg], 7168\n\ts_xor_b32 %[t], %[vk], 0x70000\n\tbuffer_load_dwordx4 %[sv], %[rv], %[t] offen lds\n\ts_add_u32 m0, %[stg], 8192\n\ts_xor_b32 %[t], %[vk], 0x100000\n\t" \
          "buffer_load_dwordx4 %[sv], %[rv], %[t] offen lds\n\ts_add_u32 m0, %[stg], 9216\n\ts_xor_b32 %[t], %[vk], 0x110000\n\tbuffer_load_dwordx4 %[sv], %[rv], %[t] offen lds\n\ts_add_u32 m0, %[stg], 10240\n\ts_xor_b32 %[t], %[vk], 0x120000\n\t" \
          "buffer_load_dwordx4 %[sv], %[rv], %[t] offen lds\n\ts_add_u32 m0, %[stg], 11264\n\ts_xor_b32 %[t], %[vk], 0x130000\n\tbuffer_load_dwordx4 %[sv], %[rv], %[t] offen lds\n\ts_add_u32 m0, %[stg], 12288\n\ts_xor_b32 %[t], %[vk], 0x140000\n\t" \
          "buffer_load_dwordx4 %[sv], %[rv], %[t] offen lds\n\ts_add_u32 m0, %[stg], 13312\n\ts_xor_b32 %[t], %[vk], 0x150000\n\tbuffer_load_dwordx4 %[sv], %[rv], %[t] offen lds\n\ts_add_u32 m0, %[stg], 14336\n\ts_xor_b32 %[t], %[vk], 0x160000\n\t" \
          "buffer_load_dwordx4 %[sv], %[rv], %[t] offen lds\n\ts_add_u32 m0, %[stg], 15360\n\ts_xor_b32 %[t], %[vk], 0x170000\n\tbuffer_load_dwordx4 %[sv], %[rv], %[t] offen lds\n\ts_add_u32 m0, %[stg], 16384\n\ts_xor_b32 %[t], %[vk], 0x200000\n\t" \
          "buffer_load_dwordx4 %[sv], %[rv], %[t] offen lds\n\ts_add_u32 m0, %[stg], 17408\n\ts_xor_b32 %[t], %[vk], 0x210000\n\tbuffer_load_dwordx4 %[sv], %[rv], %[t] offen lds\n\ts_add_u32 m0, %[stg], 18432\n\ts_xor_b32 %[t], %[vk], 0x220000\n\t" \
          "buffer_load_dwordx4 %[sv], %[rv], %[t] offen lds\n\ts_add_u32 m0, %[stg], 19456\n\ts_xor_b32 %[t], %[vk], 0x230000\n\tbuffer_load_dwordx4 %[sv], %[rv], %[t] offen lds\n\ts_add_u32 m0, %[stg], 20480\n\ts_xor_b32 %[t], %[vk], 0x240000\n\t" \
          "buffer_load_dwordx4 %[sv], %[rv], %[t] offen lds\n\ts_add_u32 m0, %[stg], 21504\n\ts_xor_b32 %[t], %[vk], 0x250000\n\tbuffer_load_dwordx4 %[sv], %[rv], %[t] offen lds\n\ts_add_u32 m0, %[stg], 22528\n\ts_xor_b32 %[t], %[vk], 0x260000\n\t" \
          "buffer_load_dwordx4 %[sv], %[rv], %[t] offen lds\n\ts_add_u32 m0, %[stg], 23552\n\ts_xor_b32 %[t], %[vk], 0x270000\n\tbuffer_load_dwordx4 %[sv], %[rv], %[t] offen lds\n\ts_add_u32 m0, %[stg], 24576\n\ts_xor_b32 %[t], %[vk], 0x300000\n\t" \
          "buffer_load_dwordx4 %[sv], %[rv], %[t] offen lds\n\ts_add_u32 m0, %[stg], 25600\n\ts_xor_b32 %[t], %[vk], 0x310000\n\tbuffer_load_dwordx4 %[sv], %[rv], %[t] offen lds\n\ts_add_u32 m0, %[stg], 26624\n\ts_xor_b32 %[t], %[vk], 0x320000\n\t" \
          "buffer_load_dwordx4 %[sv], %[rv], %[t] offen lds\n\ts_add_u32 m0, %[stg], 27648\n\ts_xor_b32 %[t], %[vk], 0x330000\n\tbuffer_load_dwordx4 %[sv], %[rv], %[t] offen lds\n\ts_add_u32 m0, %[stg], 28672\n\ts_xor_b32 %[t], %[vk], 0x340000\n\t" \
          "buffer_load_dwordx4 %[sv], %[rv], %[t] offen lds\n\ts_add_u32 m0, %[stg], 29696\n\ts_xor_b32 %[t], %[vk], 0x350000\n\tbuffer_load_dwordx4 %[sv], %[rv], %[t] offen lds\n\ts_add_u32 m0, %[stg], 30720\n\ts_xor_b32 %[t], %[vk], 0x360000\n\t" \
          "buffer_load_dwordx4 %[sv], %[rv], %[t] offen lds\n\ts_add_u32 m0, %[stg], 31744\n\ts_xor_b32 %[t], %[vk], 0x370000\n\tbuffer_load_dwordx4 %[sv], %[rv], %[t] offen lds\n\ts_waitcnt vmcnt(16)\n\tds_read_b64_tr_b16 v[8:9], %[vb0] offset:0\n\t" \
          "ds_read_b64_tr_b16 v[10:11], %[vb0] offset:128\n\tds_read_b64_tr_b16 v[12:13], %[vb0] offset:4096\n\tds_read_b64_tr_b16 v[14:15], %[vb0] offset:4224\n\tds_read_b64_tr_b16 v[16:17], %[vb1] offset:0\n\tds_read_b64_tr_b16 v[18:19], %[vb1] offset:128\n\tds_read_b64_tr_b16 v[20:21], %[vb1] offset:4096\n\t" \
          "ds_read_b64_tr_b16 v[22:23], %[vb1] offset:4224\n\tds_read_b64_tr_b16 v[24:25], %[vb2] offset:0\n\tds_read_b64_tr_b16 v[26:27], %[vb2] offset:128\n\tds_read_b64_tr_b16 v[28:29], %[vb2] offset:4096\n\tds_read_b64_tr_b16 v[30:31], %[vb2] offset:4224\n\tds_read_b64_tr_b16 v[32:33], %[vb3] offset:0\n\t" \
          "ds_read_b64_tr_b16 v[34:35], %[vb3] offset:128\n\tds_read_b64_tr_b16 v[36:37], %[vb3] offset:4096\n\tds_read_b64_tr_b16 v[38:39], %[vb3] offset:4224\n\tds_read_b64_tr_b16 v[40:41], %[vb0] offset:8192\n\tds_read_b64_tr_b16 v[42:43], %[vb0] offset:8320\n\tds_read_b64_tr_b16 v[44:45], %[vb0] offset:12288\n\t" \
          "ds_read_b64_tr_b16 v[46:47], %[vb0] offset:12416\n\tds_read_b64_tr_b16 v[48:49], %[vb1] offset:8192\n\tds_read_b64_tr_b16 v[50:51], %[vb1] offset:8320\n\tds_read_b64_tr_b16 v[52:53], %[vb1] offset:12288\n\tds_read_b64_tr_b16 v[54:55], %[vb1] offset:12416\n\tds_read_b64_tr_b16 v[56:57], %[vb2] offset:8192\n\t" \
          "ds_read_b64_tr_b16 v[58:59], %[vb2] offset:8320\n\tds_read_b64_tr_b16 v[60:61], %[vb2] offset:12288\n\tds_read_b64_tr_b16 v[62:63], %[vb2] offset:12416\n\tds_read_b64_tr_b16 v[64:65], %[vb3] offset:8192\n\tds_read_b64_tr_b16 v[66:67], %[vb3] offset:8320\n\tds_read_b64_tr_b16 v[68:69], %[vb3] offset:12288\n\t" \
          "ds_read_b64_tr_b16 v[70:71], %[vb3] offset:12416\n\ts_waitcnt lgkmcnt(0)\n\ts_add_u32 %[ho], %[k0], 1\n\ts_and_b32 %[ho], %[ho], 2\n\ts_or_b32 %[ho], %[ho], 0\n\ts_lshl_b32 %[ho], %[ho], 20\n\t" \
          "s_xor_b32 %[ho], %[ho], %[hx]\n\ts_add_u32 m0, %[stg], 0\n\ts_add_u32 %[t], %[ho], 0\n\tbuffer_load_dwordx4 %[so], s[48:51], %[t] offen lds\n\ts_add_u32 m0, %[stg], 1024\n\ts_add_u32 %[t], %[ho], 16384\n\t" \
          "buffer_load_dwordx4 %[so], s[48:51], %[t] offen lds\n\ts_add_u32 m0, %[stg], 2048\n\ts_add_u32 %[t], %[ho], 32768\n\tbuffer_load_dwordx4 %[so], s[48:51], %[t] offen lds\n\ts_add_u32 m0, %[stg], 3072\n\ts_add_u32 %[t], %[ho], 49152\n\t" \
          "buffer_load_dwordx4 %[so], s[48:51], %[t] offen lds\n\ts_add_u32 m0, %[stg], 4096\n\ts_add_u32 %[t], %[ho], 65536\n\tbuffer_load_dwordx4 %[so], s[48:51], %[t] offen lds\n\ts_add_u32 m0, %[stg], 5120\n\ts_add_u32 %[t], %[ho], 81920\n\t" \
          "buffer_load_dwordx4 %[so], s[48:51], %[t] offen lds\n\ts_add_u32 m0, %[stg], 6144\n\ts_add_u32 %[t], %[ho], 98304\n\tbuffer_load_dwordx4 %[so], s[48:51], %[t] offen lds\n\ts_add_u32 m0, %[stg], 7168\n\ts_add_u32 %[t], %[ho], 114688\n\t" \
          "buffer_load_dwordx4 %[so], s[48:51], %[t] offen lds\n\ts_add_u32 m0, %[stg], 8192\n\ts_add_u32 %[t], %[ho], 131072\n\tbuffer_load_dwordx4 %[so], s[48:51], %[t] offen lds\n\ts_add_u32 m0, %[stg], 9216\n\ts_add_u32 %[t], %[ho], 147456\n\t" \
          "buffer_load_dwordx4 %[so], s[48:51], %[t] offen lds\n\ts_add_u32 m0, %[stg], 10240\n\ts_add_u32 %[t], %[ho], 163840\n\tbuffer_load_dwordx4 %[so], s[48:51], %[t] offen lds\n\ts_add_u32 m0, %[stg], 11264\n\ts_add_u32 %[t], %[ho], 180224\n\t" \
          "buffer_load_dwordx4 %[so], s[48:51], %[t] offen lds\n\ts_add_u32 m0, %[stg], 12288\n\ts_add_u32 %[t], %[ho], 196608\n\tbuffer_load_dwordx4 %[so], s[48:51], %[t] offen lds\n\ts_add_u32 m0, %[stg], 13312\n\ts_add_u32 %[t], %[ho], 212992\n\t" \
          "buffer_load_dwordx4 %[so], s[48:51], %[t] offen lds\n\ts_add_u32 m0, %[stg], 14336\n\ts_add_u32 %[t], %[ho], 229376\n\tbuffer_load_dwordx4 %[so], s[48:51], %[t] offen lds\n\ts_add_u32 m0, %[stg], 15360\n\ts_add_u32 %[t], %[ho], 245760\n\t" \
          "buffer_load_dwordx4 %[so], s[48:51], %[t] offen lds\n\ts_waitcnt vmcnt(16)\n\tds_read_b64_tr_b16 v[72:73], %[vb0] offset:16384\n\tds_read_b64_tr_b16 v[74:75], %[vb0] offset:16512\n\tds_read_b64_tr_b16 v[76:77], %[vb0] offset:20480\n\tds_read_b64_tr_b16 v[78:79], %[vb0] offset:20608\n\t" \
          "ds_read_b64_tr_b16 v[80:81], %[vb1] offset:16384\n\tds_read_b64_tr_b16 v[82:83], %[vb1] offset:16512\n\tds_read_b64_tr_b16 v[84:85], %[vb1] offset:20480\n\tds_read_b64_tr_b16 v[86:87], %[vb1] offset:20608\n\tds_read_b64_tr_b16 v[88:89], %[vb2] offset:16384\n\tds_read_b64_tr_b16 v[90:91], %[vb2] offset:16512\n\t" \
          "ds_read_b64_tr_b16 v[92:93], %[vb2] offset:20480\n\tds_read_b64_tr_b16 v[94:95], %[vb2] offset:20608\n\tds_read_b64_tr_b16 v[96:97], %[vb3] offset:16384\n\tds_read_b64_tr_b16 v[98:99], %[vb3] offset:16512\n\tds_read_b64_tr_b16 v[100:101], %[vb3] offset:20480\n\tds_read_b64_tr_b16 v[102:103], %[vb3] offset:20608\n\t" \
          "ds_read_b64_tr_b16 v[104:105], %[vb0] offset:24576\n\tds_read_b64_tr_b16 v[106:107], %[vb0] offset:24704\n\tds_read_b64_tr_b16 v[108:109], %[vb0] offset:28672\n\tds_read_b64_tr_b16 v[110:111], %[vb0] offset:28800\n\tds_read_b64_tr_b16 v[112:113], %[vb1] offset:24576\n\tds_read_b64_tr_b16 v[114:115], %[vb1] offset:24704\n\t" \
          "ds_read_b64_tr_b16 v[116:117], %[vb1] offset:28672\n\tds_read_b64_tr_b16 v[118:119], %[vb1] offset:28800\n\tds_read_b64_tr_b16 v[120:121], %[vb2] offset:24576\n\tds_read_b64_tr_b16 v[122:123], %[vb2] offset:24704\n\tds_read_b64_tr_b16 v[124:125], %[vb2] offset:28672\n\tds_read_b64_tr_b16 v[126:127], %[vb2] offset:28800\n\t" \
          "ds_read_b64_tr_b16 v[128:129], %[vb3] offset:24576\n\tds_read_b64_tr_b16 v[130:131], %[vb3] offset:24704\n\tds_read_b64_tr_b16 v[132:133], %[vb3] offset:28672\n\tds_read_b64_tr_b16 v[134:135], %[vb3] offset:28800\n\ts_waitcnt lgkmcnt(0)\n\ts_add_u32 %[ho], %[k0], 0\n\t" \
          "s_and_b32 %[ho], %[ho], 2\n\ts_or_b32 %[ho], %[ho], 1\n\ts_lshl_b32 %[ho], %[ho], 20\n\ts_xor_b32 %[ho], %[ho], %[hx]\n\ts_add_u32 m0, %[stg], 16384\n\ts_add_u32 %[t], %[ho], 0\n\t" \
          "buffer_load_dwordx4 %[so], s[48:51], %[t] offen lds\n\ts_add_u32 m0, %[stg], 17408\n\ts_add_u32 %[t], %[ho], 16384\n\tbuffer_load_dwordx4 %[so], s[48:51], %[t] offen lds\n\ts_add_u32 m0, %[stg], 18432\n\ts_add_u32 %[t], %[ho], 32768\n\t" \
          "buffer_load_dwordx4 %[so], s[48:51], %[t] offen lds\n\ts_add_u32 m0, %[stg], 19456\n\ts_add_u32 %[t], %[ho], 49152\n\tbuffer_load_dwordx4 %[so], s[48:51], %[t] offen lds\n\ts_add_u32 m0, %[stg], 20480\n\ts_add_u32 %[t], %[ho], 65536\n\t" \
          "buffer_load_dwordx4 %[so], s[48:51], %[t] offen lds\n\ts_add_u32 m0, %[stg], 21504\n\ts_add_u32 %[t], %[ho], 81920\n\tbuffer_load_dwordx4 %[so], s[48:51], %[t] offen lds\n\ts_add_u32 m0, %[stg], 22528\n\ts_add_u32 %[t], %[ho], 98304\n\t" \
          "buffer_load_dwordx4 %[so], s[48:51], %[t] offen lds\n\ts_add_u32 m0, %[stg], 23552\n\ts_add_u32 %[t], %[ho], 114688\n\tbuffer_load_dwordx4 %[so], s[48:51], %[t] offen lds\n\ts_add_u32 m0, %[stg], 24576\n\ts_add_u32 %[t], %[ho], 131072\n\t" \
          "buffer_load_dwordx4 %[so], s[48:51], %[t] offen lds\n\ts_add_u32 m0, %[stg], 25600\n\ts_add_u32 %[t], %[ho], 147456\n\tbuffer_load_dwordx4 %[so], s[48:51], %[t] offen lds\n\ts_add_u32 m0, %[stg], 26624\n\ts_add_u32 %[t], %[ho], 163840\n\t" \
          "buffer_load_dwordx4 %[so], s[48:51], %[t] offen lds\n\ts_add_u32 m0, %[stg], 27648\n\ts_add_u32 %[t], %[ho], 180224\n\tbuffer_load_dwordx4 %[so], s[48:51], %[t] offen lds\n\ts_add_u32 m0, %[stg], 28672\n\ts_add_u32 %[t], %[ho], 196608\n\t" \
          "buffer_load_dwordx4 %[so], s[48:51], %[t] offen lds\n\ts_add_u32 m0, %[stg], 29696\n\ts_add_u32 %[t], %[ho], 212992\n\tbuffer_load_dwordx4 %[so], s[48:51], %[t] offen lds\n\ts_add_u32 m0, %[stg], 30720\n\ts_add_u32 %[t], %[ho], 229376\n\t" \
          "buffer_load_dwordx4 %[so], s[48:51], %[t] offen lds\n\ts_add_u32 m0, %[stg], 31744\n\ts_add_u32 %[t], %[ho], 245760\n\tbuffer_load_dwordx4 %[so], s[48:51], %[t] offen lds\n\ts_mov_b32 %[n], 8\n\ts_cmp_eq_u32 %[k0], 2\n\t" \
          "s_cbranch_scc1 Lw_2_%=\n\ts_cmp_eq_u32 %[k0], 0\n\ts_cbranch_scc1 Lw_0_%=\n\ts_waitcnt vmcnt(0)\n\ts_cmp_eq_u32 %[k0], 1\n\ts_cbranch_scc1 Lw_1_%=\n\t" \
          "s_branch Lw_3_%=\n\tLw_0_%=:\n\ts_waitcnt vmcnt(16)\n\tds_read_b64_tr_b16 a[0:1], %[ob] offset:0\n\tds_read_b64_tr_b16 a[2:3], %[ob] offset:128\n\tds_read_b64_tr_b16 a[4:5], %[ob] offset:8192\n\t" \
          "ds_read_b64_tr_b16 a[6:7], %[ob] offset:8320\n\tds_read_b64_tr_b16 a[8:9], %[ob] offset:32\n\tds_read_b64_tr_b16 a[10:11], %[ob] offset:160\n\tds_read_b64_tr_b16 a[12:13], %[ob] offset:8224\n\tds_read_b64_tr_b16 a[14:15], %[ob] offset:8352\n\tds_read_b64_tr_b16 a[16:17], %[ob] offset:1024\n\t" \
          "ds_read_b64_tr_b16 a[18:19], %[ob] offset:1152\n\tds_read_b64_tr_b16 a[20:21], %[ob] offset:9216\n\tds_read_b64_tr_b16 a[22:23], %[ob] offset:9344\n\tds_read_b64_tr_b16 a[24:25], %[ob] offset:1056\n\tds_read_b64_tr_b16 a[26:27], %[ob] offset:1184\n\tds_read_b64_tr_b16 a[28:29], %[ob] offset:9248\n\t" \
          "ds_read_b64_tr_b16 a[30:31], %[ob] offset:9376\n\ts_cmp_gt_u32 %[n], 2\n\ts_cselect_b32 s50, s50, 0\n\ts_cmp_gt_u32 %[n], 6\n\ts_cselect_b32 %[ho], 0x2, 0x6\n\ts_lshl_b32 %[ho], %[ho], 20\n\t" \
          "s_xor_b32 %[ho], %[ho], %[hx]\n\ts_waitcnt lgkmcnt(0)\n\ts_add_u32 m0, %[stg], 0\n\ts_add_u32 %[t], %[ho], 0\n\tbuffer_load_dwordx4 %[so], s[48:51], %[t] offen lds\n\ts_add_u32 m0, %[stg], 1024\n\t" \
          "s_add_u32 %[t], %[ho], 16384\n\tbuffer_load_dwordx4 %[so], s[48:51], %[t] offen lds\n\ts_add_u32 m0, %[stg], 2048\n\ts_add_u32 %[t], %[ho], 32768\n\tbuffer_load_dwordx4 %[so], s[48:51], %[t] offen lds\n\ts_add_u32 m0, %[stg], 3072\n\t" \
          "s_add_u32 %[t], %[ho], 49152\n\tbuffer_load_dwordx4 %[so], s[48:51], %[t] offen lds\n\ts_add_u32 m0, %[stg], 4096\n\ts_add_u32 %[t], %[ho], 65536\n\tbuffer_load_dwordx4 %[so], s[48:51], %[t] offen lds\n\ts_add_u32 m0, %[stg], 5120\n\t" \
          "s_add_u32 %[t], %[ho], 81920\n\tbuffer_load_dwordx4 %[so], s[48:51], %[t] offen lds\n\ts_add_u32 m0, %[stg], 6144\n\ts_add_u32 %[t], %[ho], 98304\n\tbuffer_load_dwordx4 %[so], s[48:51], %[t] offen lds\n\ts_add_u32 m0, %[stg], 7168\n\t" \
          "s_add_u32 %[t], %[ho], 114688\n\tbuffer_load_dwordx4 %[so], s[48:51], %[t] offen lds\n\ts_add_u32 m0, %[stg], 8192\n\ts_add_u32 %[t], %[ho], 131072\n\tbuffer_load_dwordx4 %[so], s[48:51], %[t] offen lds\n\ts_add_u32 m0, %[stg], 9216\n\t" \
          "s_add_u32 %[t], %[ho], 147456\n\tbuffer_load_dwordx4 %[so], s[48:51], %[t] offen lds\n\ts_add_u32 m0, %[stg], 10240\n\ts_add_u32 %[t], %[ho], 163840\n\tbuffer_load_dwordx4 %[so], s[48:51], %[t] offen lds\n\ts_add_u32 m0, %[stg], 11264\n\t" \
          "s_add_u32 %[t], %[ho], 180224\n\tbuffer_load_dwordx4 %[so], s[48:51], %[t] offen lds\n\ts_add_u32 m0, %[stg], 12288\n\ts_add_u32 %[t], %[ho], 196608\n\tbuffer_load_dwordx4 %[so], s[48:51], %[t] offen lds\n\ts_add_u32 m0, %[stg], 13312\n\t" \
          "s_add_u32 %[t], %[ho], 212992\n\tbuffer_load_dwordx4 %[so], s[48:51], %[t] offen lds\n\ts_add_u32 m0, %[stg], 14336\n\ts_add_u32 %[t], %[ho], 229376\n\tbuffer_load_dwordx4 %[so], s[48:51], %[t] offen lds\n\ts_add_u32 m0, %[stg], 15360\n\t" \
          "s_add_u32 %[t], %[ho], 245760\n\tbuffer_load_dwordx4 %[so], s[48:51], %[t] offen lds\n\ts_sub_u32 %[n], %[n], 1\n\ts_cmp_eq_u32 %[n], 4\n\ts_cbranch_scc1 Lw_5_%=\n\tLw_1_%=:\n\t" \
          "s_waitcnt vmcnt(16)\n\tds_read_b64_tr_b16 a[32:33], %[ob] offset:16384\n\tds_read_b64_tr_b16 a[34:35], %[ob] offset:16512\n\tds_read_b64_tr_b16 a[36:37], %[ob] offset:24576\n\tds_read_b64_tr_b16 a[38:39], %[ob] offset:24704\n\tds_read_b64_tr_b16 a[40:41], %[ob] offset:16416\n\t" \
          "ds_read_b64_tr_b16 a[42:43], %[ob] offset:16544\n\tds_read_b64_tr_b16 a[44:45], %[ob] offset:24608\n\tds_read_b64_tr_b16 a[46:47], %[ob] offset:24736\n\tds_read_b64_tr_b16 a[48:49], %[ob] offset:17408\n\tds_read_b64_tr_b16 a[50:51], %[ob] offset:17536\n\tds_read_b64_tr_b16 a[52:53], %[ob] offset:25600\n\t" \
          "ds_read_b64_tr_b16 a[54:55], %[ob] offset:25728\n\tds_read_b64_tr_b16 a[56:57], %[ob] offset:17440\n\tds_read_b64_tr_b16 a[58:59], %[ob] offset:17568\n\tds_read_b64_tr_b16 a[60:61], %[ob] offset:25632\n\tds_read_b64_tr_b16 a[62:63], %[ob] offset:25760\n\ts_cmp_gt_u32 %[n], 2\n\t" \
          "s_cselect_b32 s50, s50, 0\n\ts_cmp_gt_u32 %[n], 6\n\ts_cselect_b32 %[ho], 0x3, 0x7\n\ts_lshl_b32 %[ho], %[ho], 20\n\ts_xor_b32 %[ho], %[ho], %[hx]\n\ts_waitcnt lgkmcnt(0)\n\t" \
          "s_add_u32 m0, %[stg], 16384\n\ts_add_u32 %[t], %[ho], 0\n\tbuffer_load_dwordx4 %[so], s[48:51], %[t] offen lds\n\ts_add_u32 m0, %[stg], 17408\n\ts_add_u32 %[t], %[ho], 16384\n\tbuffer_load_dwordx4 %[so], s[48:51], %[t] offen lds\n\t" \
          "s_add_u32 m0, %[stg], 18432\n\ts_add_u32 %[t], %[ho], 32768\n\tbuffer_load_dwordx4 %[so], s[48:51], %[t] offen lds\n\ts_add_u32 m0, %[stg], 19456\n\ts_add_u32 %[t], %[ho], 49152\n\tbuffer_load_dwordx4 %[so], s[48:51], %[t] offen lds\n\t" \
          "s_add_u32 m0, %[stg], 20480\n\ts_add_u32 %[t], %[ho], 65536\n\tbuffer_load_dwordx4 %[so], s[48:51], %[t] offen lds\n\ts_add_u32 m0, %[stg], 21504\n\ts_add_u32 %[t], %[ho], 81920\n\tbuffer_load_dwordx4 %[so], s[48:51], %[t] offen lds\n\t" \
          "s_add_u32 m0, %[stg], 22528\n\ts_add_u32 %[t], %[ho], 98304\n\tbuffer_load_dwordx4 %[so], s[48:51], %[t] offen lds\n\ts_add_u32 m0, %[stg], 23552\n\ts_add_u32 %[t], %[ho], 114688\n\tbuffer_load_dwordx4 %[so], s[48:51], %[t] offen lds\n\t" \
          "s_add_u32 m0, %[stg], 24576\n\ts_add_u32 %[t], %[ho], 131072\n\tbuffer_load_dwordx4 %[so], s[48:51], %[t] offen lds\n\ts_add_u32 m0, %[stg], 25600\n\ts_add_u32 %[t], %[ho], 147456\n\tbuffer_load_dwordx4 %[so], s[48:51], %[t] offen lds\n\t" \
          "s_add_u32 m0, %[stg], 26624\n\ts_add_u32 %[t], %[ho], 163840\n\tbuffer_load_dwordx4 %[so], s[48:51], %[t] offen lds\n\ts_add_u32 m0, %[stg], 27648\n\ts_add_u32 %[t], %[ho], 180224\n\tbuffer_load_dwordx4 %[so], s[48:51], %[t] offen lds\n\t" \
          "s_add_u32 m0, %[stg], 28672\n\ts_add_u32 %[t], %[ho], 196608\n\tbuffer_load_dwordx4 %[so], s[48:51], %[t] offen lds\n\ts_add_u32 m0, %[stg], 29696\n\ts_add_u32 %[t], %[ho], 212992\n\tbuffer_load_dwordx4 %[so], s[48:51], %[t] offen lds\n\t" \
          "s_add_u32 m0, %[stg], 30720\n\ts_add_u32 %[t], %[ho], 229376\n\tbuffer_load_dwordx4 %[so], s[48:51], %[t] offen lds\n\ts_add_u32 m0, %[stg], 31744\n\ts_add_u32 %[t], %[ho], 245760\n\tbuffer_load_dwordx4 %[so], s[48:51], %[t] offen lds\n\t" \
          "s_sub_u32 %[n], %[n], 1\n\ts_cmp_eq_u32 %[n], 4\n\ts_cbranch_scc1 Lw_6_%=\n\tLw_2_%=:\n\ts_waitcnt vmcnt(16)\n\tds_read_b64_tr_b16 a[64:65], %[ob] offset:0\n\t" \
          "ds_read_b64_tr_b16 a[66:67], %[ob] offset:128\n\tds_read_b64_tr_b16 a[68:69], %[ob] offset:8192\n\tds_read_b64_tr_b16 a[70:71], %[ob] offset:8320\n\tds_read_b64_tr_b16 a[72:73], %[ob] offset:32\n\tds_read_b64_tr_b16 a[74:75], %[ob] offset:160\n\tds_read_b64_tr_b16 a[76:77], %[ob] offset:8224\n\t" \
          "ds_read_b64_tr_b16 a[78:79], %[ob] offset:8352\n\tds_read_b64_tr_b16 a[80:81], %[ob] offset:1024\n\tds_read_b64_tr_b16 a[82:83], %[ob] offset:1152\n\tds_read_b64_tr_b16 a[84:85], %[ob] offset:9216\n\tds_read_b64_tr_b16 a[86:87], %[ob] offset:9344\n\tds_read_b64_tr_b16 a[88:89], %[ob] offset:1056\n\t" \
          "ds_read_b64_tr_b16 a[90:91], %[ob] offset:1184\n\tds_read_b64_tr_b16 a[92:93], %[ob] offset:9248\n\tds_read_b64_tr_b16 a[94:95], %[ob] offset:9376\n\ts_cmp_gt_u32 %[n], 2\n\ts_cselect_b32 s50, s50, 0\n\ts_cmp_gt_u32 %[n], 6\n\t" \
          "s_cselect_b32 %[ho], 0x0, 0x4\n\ts_lshl_b32 %[ho], %[ho], 20\n\ts_xor_b32 %[ho], %[ho], %[hx]\n\ts_waitcnt lgkmcnt(0)\n\ts_add_u32 m0, %[stg], 0\n\ts_add_u32 %[t], %[ho], 0\n\t" \
          "buffer_load_dwordx4 %[so], s[48:51], %[t] offen lds\n\ts_add_u32 m0, %[stg], 1024\n\ts_add_u32 %[t], %[ho], 16384\n\tbuffer_load_dwordx4 %[so], s[48:51], %[t] offen lds\n\ts_add_u32 m0, %[stg], 2048\n\ts_add_u32 %[t], %[ho], 32768\n\t" \
          "buffer_load_dwordx4 %[so], s[48:51], %[t] offen lds\n\ts_add_u32 m0, %[stg], 3072\n\ts_add_u32 %[t], %[ho], 49152\n\tbuffer_load_dwordx4 %[so], s[48:51], %[t] offen lds\n\ts_add_u32 m0, %[stg], 4096\n\ts_add_u32 %[t], %[ho], 65536\n\t" \
          "buffer_load_dwordx4 %[so], s[48:51], %[t] offen lds\n\ts_add_u32 m0, %[stg], 5120\n\ts_add_u32 %[t], %[ho], 81920\n\tbuffer_load_dwordx4 %[so], s[48:51], %[t] offen lds\n\ts_add_u32 m0, %[stg], 6144\n\ts_add_u32 %[t], %[ho], 98304\n\t" \
          "buffer_load_dwordx4 %[so], s[48:51], %[t] offen lds\n\ts_add_u32 m0, %[stg], 7168\n\ts_add_u32 %[t], %[ho], 114688\n\tbuffer_load_dwordx4 %[so], s[48:51], %[t] offen lds\n\ts_add_u32 m0, %[stg], 8192\n\ts_add_u32 %[t], %[ho], 131072\n\t" \
          "buffer_load_dwordx4 %[so], s[48:51], %[t] offen lds\n\ts_add_u32 m0, %[stg], 9216\n\ts_add_u32 %[t], %[ho], 147456\n\tbuffer_load_dwordx4 %[so], s[48:51], %[t] offen lds\n\ts_add_u32 m0, %[stg], 10240\n\ts_add_u32 %[t], %[ho], 163840\n\t" \
          "buffer_load_dwordx4 %[so], s[48:51], %[t] offen lds\n\ts_add_u32 m0, %[stg], 11264\n\ts_add_u32 %[t], %[ho], 180224\n\tbuffer_load_dwordx4 %[so], s[48:51], %[t] offen lds\n\ts_add_u32 m0, %[stg], 12288\n\ts_add_u32 %[t], %[ho], 196608\n\t" \
          "buffer_load_dwordx4 %[so], s[48:51], %[t] offen lds\n\ts_add_u32 m0, %[stg], 13312\n\ts_add_u32 %[t], %[ho], 212992\n\tbuffer_load_dwordx4 %[so], s[48:51], %[t] offen lds\n\ts_add_u32 m0, %[stg], 14336\n\ts_add_u32 %[t], %[ho], 229376\n\t" \
          "buffer_load_dwordx4 %[so], s[48:51], %[t] offen lds\n\ts_add_u32 m0, %[stg], 15360\n\ts_add_u32 %[t], %[ho], 245760\n\tbuffer_load_dwordx4 %[so], s[48:51], %[t] offen lds\n\ts_sub_u32 %[n], %[n], 1\n\ts_cmp_eq_u32 %[n], 4\n\t" \
          "s_cbranch_scc1 Lw_7_%=\n\tLw_3_%=:\n\ts_waitcnt vmcnt(16)\n\tds_read_b64_tr_b16 a[96:97], %[ob] offset:16384\n\tds_read_b64_tr_b16 a[98:99], %[ob] offset:16512\n\tds_read_b64_tr_b16 a[100:101], %[ob] offset:24576\n\t" \
          "ds_read_b64_tr_b16 a[102:103], %[ob] offset:24704\n\tds_read_b64_tr_b16 a[104:105], %[ob] offset:16416\n\tds_read_b64_tr_b16 a[106:107], %[ob] offset:16544\n\tds_read_b64_tr_b16 a[108:109], %[ob] offset:24608\n\tds_read_b64_tr_b16 a[110:111], %[ob] offset:24736\n\tds_read_b64_tr_b16 a[112:113], %[ob] offset:17408\n\t" \
          "ds_read_b64_tr_b16 a[114:115], %[ob] offset:17536\n\tds_read_b64_tr_b16 a[116:117], %[ob] offset:25600\n\tds_read_b64_tr_b16 a[118:119], %[ob] offset:25728\n\tds_read_b64_tr_b16 a[120:121], %[ob] offset:17440\n\tds_read_b64_tr_b16 a[122:123], %[ob] offset:17568\n\tds_read_b64_tr_b16 a[124:125], %[ob] offset:25632\n\t" \
          "ds_read_b64_tr_b16 a[126:127], %[ob] offset:25760\n\ts_cmp_gt_u32 %[n], 2\n\ts_cselect_b32 s50, s50, 0\n\ts_cmp_gt_u32 %[n], 6\n\ts_cselect_b32 %[ho], 0x1, 0x5\n\ts_lshl_b32 %[ho], %[ho], 20\n\t" \
          "s_xor_b32 %[ho], %[ho], %[hx]\n\ts_waitcnt lgkmcnt(0)\n\ts_add_u32 m0, %[stg], 16384\n\ts_add_u32 %[t], %[ho], 0\n\tbuffer_load_dwordx4 %[so], s[48:51], %[t] offen lds\n\ts_add_u32 m0, %[stg], 17408\n\t" \
          "s_add_u32 %[t], %[ho], 16384\n\tbuffer_load_dwordx4 %[so], s[48:51], %[t] offen lds\n\ts_add_u32 m0, %[stg], 18432\n\ts_add_u32 %[t], %[ho], 32768\n\tbuffer_load_dwordx4 %[so], s[48:51], %[t] offen lds\n\ts_add_u32 m0, %[stg], 19456\n\t" \
          "s_add_u32 %[t], %[ho], 49152\n\tbuffer_load_dwordx4 %[so], s[48:51], %[t] offen lds\n\ts_add_u32 m0, %[stg], 20480\n\ts_add_u32 %[t], %[ho], 65536\n\tbuffer_load_dwordx4 %[so], s[48:51], %[t] offen lds\n\ts_add_u32 m0, %[stg], 21504\n\t" \
          "s_add_u32 %[t], %[ho], 81920\n\tbuffer_load_dwordx4 %[so], s[48:51], %[t] offen lds\n\ts_add_u32 m0, %[stg], 22528\n\ts_add_u32 %[t], %[ho], 98304\n\tbuffer_load_dwordx4 %[so], s[48:51], %[t] offen lds\n\ts_add_u32 m0, %[stg], 23552\n\t" \
          "s_add_u32 %[t], %[ho], 114688\n\tbuffer_load_dwordx4 %[so], s[48:51], %[t] offen lds\n\ts_add_u32 m0, %[stg], 24576\n\ts_add_u32 %[t], %[ho], 131072\n\tbuffer_load_dwordx4 %[so], s[48:51], %[t] offen lds\n\ts_add_u32 m0, %[stg], 25600\n\t" \
          "s_add_u32 %[t], %[ho], 147456\n\tbuffer_load_dwordx4 %[so], s[48:51], %[t] offen lds\n\ts_add_u32 m0, %[stg], 26624\n\ts_add_u32 %[t], %[ho], 163840\n\tbuffer_load_dwordx4 %[so], s[48:51], %[t] offen lds\n\ts_add_u32 m0, %[stg], 27648\n\t" \
          "s_add_u32 %[t], %[ho], 180224\n\tbuffer_load_dwordx4 %[so], s[48:51], %[t] offen lds\n\ts_add_u32 m0, %[stg], 28672\n\ts_add_u32 %[t], %[ho], 196608\n\tbuffer_load_dwordx4 %[so], s[48:51], %[t] offen lds\n\ts_add_u32 m0, %[stg], 29696\n\t" \
          "s_add_u32 %[t], %[ho], 212992\n\tbuffer_load_dwordx4 %[so], s[48:51], %[t] offen lds\n\ts_add_u32 m0, %[stg], 30720\n\ts_add_u32 %[t], %[ho], 229376\n\tbuffer_load_dwordx4 %[so], s[48:51], %[t] offen lds\n\ts_add_u32 m0, %[stg], 31744\n\t" \
          "s_add_u32 %[t], %[ho], 245760\n\tbuffer_load_dwordx4 %[so], s[48:51], %[t] offen lds\n\ts_sub_u32 %[n], %[n], 1\n\ts_cmp_eq_u32 %[n], 4\n\ts_cbranch_scc1 Lw_4_%=\n\ts_branch Lw_0_%=\n\t" \
          "Lw_4_%=:\n\ts_waitcnt vmcnt(16)\n\tds_read_b64_tr_b16 a[128:129], %[ob] offset:0\n\tds_read_b64_tr_b16 a[130:131], %[ob] offset:128\n\tds_read_b64_tr_b16 a[132:133], %[ob] offset:8192\n\tds_read_b64_tr_b16 a[134:135], %[ob] offset:8320\n\t" \
          "ds_read_b64_tr_b16 a[136:137], %[ob] offset:32\n\tds_read_b64_tr_b16 a[138:139], %[ob] offset:160\n\tds_read_b64_tr_b16 a[140:141], %[ob] offset:8224\n\tds_read_b64_tr_b16 a[142:143], %[ob] offset:8352\n\tds_read_b64_tr_b16 a[144:145], %[ob] offset:1024\n\tds_read_b64_tr_b16 a[146:147], %[ob] offset:1152\n\t" \
          "ds_read_b64_tr_b16 a[148:149], %[ob] offset:9216\n\tds_read_b64_tr_b16 a[150:151], %[ob] offset:9344\n\tds_read_b64_tr_b16 a[152:153], %[ob] offset:1056\n\tds_read_b64_tr_b16 a[154:155], %[ob] offset:1184\n\tds_read_b64_tr_b16 a[156:157], %[ob] offset:9248\n\tds_read_b64_tr_b16 a[158:159], %[ob] offset:9376\n\t" \
          "s_cmp_gt_u32 %[n], 2\n\ts_cselect_b32 s50, s50, 0\n\ts_cmp_gt_u32 %[n], 6\n\ts_cselect_b32 %[ho], 0x2, 0x6\n\ts_lshl_b32 %[ho], %[ho], 20\n\ts_xor_b32 %[ho], %[ho], %[hx]\n\t" \
          "s_waitcnt lgkmcnt(0)\n\ts_add_u32 m0, %[stg], 0\n\ts_add_u32 %[t], %[ho], 0\n\tbuffer_load_dwordx4 %[so], s[48:51], %[t] offen lds\n\ts_add_u32 m0, %[stg], 1024\n\ts_add_u32 %[t], %[ho], 16384\n\t" \
          "buffer_load_dwordx4 %[so], s[48:51], %[t] offen lds\n\ts_add_u32 m0, %[stg], 2048\n\ts_add_u32 %[t], %[ho], 32768\n\tbuffer_load_dwordx4 %[so], s[48:51], %[t] offen lds\n\ts_add_u32 m0, %[stg], 3072\n\ts_add_u32 %[t], %[ho], 49152\n\t" \
          "buffer_load_dwordx4 %[so], s[48:51], %[t] offen lds\n\ts_add_u32 m0, %[stg], 4096\n\ts_add_u32 %[t], %[ho], 65536\n\tbuffer_load_dwordx4 %[so], s[48:51], %[t] offen lds\n\ts_add_u32 m0, %[stg], 5120\n\ts_add_u32 %[t], %[ho], 81920\n\t" \
          "buffer_load_dwordx4 %[so], s[48:51], %[t] offen lds\n\ts_add_u32 m0, %[stg], 6144\n\ts_add_u32 %[t], %[ho], 98304\n\tbuffer_load_dwordx4 %[so], s[48:51], %[t] offen lds\n\ts_add_u32 m0, %[stg], 7168\n\ts_add_u32 %[t], %[ho], 114688\n\t" \
          "buffer_load_dwordx4 %[so], s[48:51], %[t] offen lds\n\ts_add_u32 m0, %[stg], 8192\n\ts_add_u32 %[t], %[ho], 131072\n\tbuffer_load_dwordx4 %[so], s[48:51], %[t] offen lds\n\ts_add_u32 m0, %[stg], 9216\n\ts_add_u32 %[t], %[ho], 147456\n\t" \
          "buffer_load_dwordx4 %[so], s[48:51], %[t] offen lds\n\ts_add_u32 m0, %[stg], 10240\n\ts_add_u32 %[t], %[ho], 163840\n\tbuffer_load_dwordx4 %[so], s[48:51], %[t] offen lds\n\ts_add_u32 m0, %[stg], 11264\n\ts_add_u32 %[t], %[ho], 180224\n\t" \
          "buffer_load_dwordx4 %[so], s[48:51], %[t] offen lds\n\ts_add_u32 m0, %[stg], 12288\n\ts_add_u32 %[t], %[ho], 196608\n\tbuffer_load_dwordx4 %[so], s[48:51], %[t] offen lds\n\ts_add_u32 m0, %[stg], 13312\n\ts_add_u32 %[t], %[ho], 212992\n\t" \
          "buffer_load_dwordx4 %[so], s[48:51], %[t] offen lds\n\ts_add_u32 m0, %[stg], 14336\n\ts_add_u32 %[t], %[ho], 229376\n\tbuffer_load_dwordx4 %[so], s[48:51], %[t] offen lds\n\ts_add_u32 m0, %[stg], 15360\n\ts_add_u32 %[t], %[ho], 245760\n\t" \
          "buffer_load_dwordx4 %[so], s[48:51], %[t] offen lds\n\ts_sub_u32 %[n], %[n], 1\n\ts_cmp_eq_u32 %[n], 0\n\ts_cbranch_scc1 Lw_end%=\n\tLw_5_%=:\n\ts_waitcnt vmcnt(16)\n\t" \
          "ds_read_b64_tr_b16 a[160:161], %[ob] offset:16384\n\tds_read_b64_tr_b16 a[162:163], %[ob] offset:16512\n\tds_read_b64_tr_b16 a[164:165], %[ob] offset:24576\n\tds_read_b64_tr_b16 a[166:167], %[ob] offset:24704\n\tds_read_b64_tr_b16 a[168:169], %[ob] offset:16416\n\tds_read_b64_tr_b16 a[170:171], %[ob] offset:16544\n\t" \
          "ds_read_b64_tr_b16 a[172:173], %[ob] offset:24608\n\tds_read_b64_tr_b16 a[174:175], %[ob] offset:24736\n\tds_read_b64_tr_b16 a[176:177], %[ob] offset:17408\n\tds_read_b64_tr_b16 a[178:179], %[ob] offset:17536\n\tds_read_b64_tr_b16 a[180:181], %[ob] offset:25600\n\tds_read_b64_tr_b16 a[182:183], %[ob] offset:25728\n\t" \
          "ds_read_b64_tr_b16 a[184:185], %[ob] offset:17440\n\tds_read_b64_tr_b16 a[186:187], %[ob] offset:17568\n\tds_read_b64_tr_b16 a[188:189], %[ob] offset:25632\n\tds_read_b64_tr_b16 a[190:191], %[ob] offset:25760\n\ts_cmp_gt_u32 %[n], 2\n\ts_cselect_b32 s50, s50, 0\n\t" \
          "s_cmp_gt_u32 %[n], 6\n\ts_cselect_b32 %[ho], 0x3, 0x7\n\ts_lshl_b32 %[ho], %[ho], 20\n\ts_xor_b32 %[ho], %[ho], %[hx]\n\ts_waitcnt lgkmcnt(0)\n\ts_add_u32 m0, %[stg], 16384\n\t" \
          "s_add_u32 %[t], %[ho], 0\n\tbuffer_load_dwordx4 %[so], s[48:51], %[t] offen lds\n\ts_add_u32 m0, %[stg], 17408\n\ts_add_u32 %[t], %[ho], 16384\n\tbuffer_load_dwordx4 %[so], s[48:51], %[t] offen lds\n\ts_add_u32 m0, %[stg], 18432\n\t" \
          "s_add_u32 %[t], %[ho], 32768\n\tbuffer_load_dwordx4 %[so], s[48:51], %[t] offen lds\n\ts_add_u32 m0, %[stg], 19456\n\ts_add_u32 %[t], %[ho], 49152\n\tbuffer_load_dwordx4 %[so], s[48:51], %[t] offen lds\n\ts_add_u32 m0, %[stg], 20480\n\t" \
          "s_add_u32 %[t], %[ho], 65536\n\tbuffer_load_dwordx4 %[so], s[48:51], %[t] offen lds\n\ts_add_u32 m0, %[stg], 21504\n\ts_add_u32 %[t], %[ho], 81920\n\tbuffer_load_dwordx4 %[so], s[48:51], %[t] offen lds\n\ts_add_u32 m0, %[stg], 22528\n\t" \
          "s_add_u32 %[t], %[ho], 98304\n\tbuffer_load_dwordx4 %[so], s[48:51], %[t] offen lds\n\ts_add_u32 m0, %[stg], 23552\n\ts_add_u32 %[t], %[ho], 114688\n\tbuffer_load_dwordx4 %[so], s[48:51], %[t] offen lds\n\ts_add_u32 m0, %[stg], 24576\n\t" \
          "s_add_u32 %[t], %[ho], 131072\n\tbuffer_load_dwordx4 %[so], s[48:51], %[t] offen lds\n\ts_add_u32 m0, %[stg], 25600\n\ts_add_u32 %[t], %[ho], 147456\n\tbuffer_load_dwordx4 %[so], s[48:51], %[t] offen lds\n\ts_add_u32 m0, %[stg], 26624\n\t" \
          "s_add_u32 %[t], %[ho], 163840\n\tbuffer_load_dwordx4 %[so], s[48:51], %[t] offen lds\n\ts_add_u32 m0, %[stg], 27648\n\ts_add_u32 %[t], %[ho], 180224\n\tbuffer_load_dwordx4 %[so], s[48:51], %[t] offen lds\n\ts_add_u32 m0, %[stg], 28672\n\t" \
          "s_add_u32 %[t], %[ho], 196608\n\tbuffer_load_dwordx4 %[so], s[48:51], %[t] offen lds\n\ts_add_u32 m0, %[stg], 29696\n\ts_add_u32 %[t], %[ho], 212992\n\tbuffer_load_dwordx4 %[so], s[48:51], %[t] offen lds\n\ts_add_u32 m0, %[stg], 30720\n\t" \
          "s_add_u32 %[t], %[ho], 229376\n\tbuffer_load_dwordx4 %[so], s[48:51], %[t] offen lds\n\ts_add_u32 m0, %[stg], 31744\n\ts_add_u32 %[t], %[ho], 245760\n\tbuffer_load_dwordx4 %[so], s[48:51], %[t] offen lds\n\ts_sub_u32 %[n], %[n], 1\n\t" \
          "s_cmp_eq_u32 %[n], 0\n\ts_cbranch_scc1 Lw_end%=\n\tLw_6_%=:\n\ts_waitcnt vmcnt(16)\n\tds_read_b64_tr_b16 a[192:193], %[ob] offset:0\n\tds_read_b64_tr_b16 a[194:195], %[ob] offset:128\n\t" \
          "ds_read_b64_tr_b16 a[196:197], %[ob] offset:8192\n\tds_read_b64_tr_b16 a[198:199], %[ob] offset:8320\n\tds_read_b64_tr_b16 a[200:201], %[ob] offset:32\n\tds_read_b64_tr_b16 a[202:203], %[ob] offset:160\n\tds_read_b64_tr_b16 a[204:205], %[ob] offset:8224\n\tds_read_b64_tr_b16 a[206:207], %[ob] offset:8352\n\t" \
          "ds_read_b64_tr_b16 a[208:209], %[ob] offset:1024\n\tds_read_b64_tr_b16 a[210:211], %[ob] offset:1152\n\tds_read_b64_tr_b16 a[212:213], %[ob] offset:9216\n\tds_read_b64_tr_b16 a[214:215], %[ob] offset:9344\n\tds_read_b64_tr_b16 a[216:217], %[ob] offset:1056\n\tds_read_b64_tr_b16 a[218:219], %[ob] offset:1184\n\t" \
          "ds_read_b64_tr_b16 a[220:221], %[ob] offset:9248\n\tds_read_b64_tr_b16 a[222:223], %[ob] offset:9376\n\ts_cmp_gt_u32 %[n], 2\n\ts_cselect_b32 s50, s50, 0\n\ts_cmp_gt_u32 %[n], 6\n\ts_cselect_b32 %[ho], 0x0, 0x4\n\t" \
          "s_lshl_b32 %[ho], %[ho], 20\n\ts_xor_b32 %[ho], %[ho], %[hx]\n\ts_waitcnt lgkmcnt(0)\n\ts_add_u32 m0, %[stg], 0\n\ts_add_u32 %[t], %[ho], 0\n\tbuffer_load_dwordx4 %[so], s[48:51], %[t] offen lds\n\t" \
          "s_add_u32 m0, %[stg], 1024\n\ts_add_u32 %[t], %[ho], 16384\n\tbuffer_load_dwordx4 %[so], s[48:51], %[t] offen lds\n\ts_add_u32 m0, %[stg], 2048\n\ts_add_u32 %[t], %[ho], 32768\n\tbuffer_load_dwordx4 %[so], s[48:51], %[t] offen lds\n\t" \
          "s_add_u32 m0, %[stg], 3072\n\ts_add_u32 %[t], %[ho], 49152\n\tbuffer_load_dwordx4 %[so], s[48:51], %[t] offen lds\n\ts_add_u32 m0, %[stg], 4096\n\ts_add_u32 %[t], %[ho], 65536\n\tbuffer_load_dwordx4 %[so], s[48:51], %[t] offen lds\n\t" \
          "s_add_u32 m0, %[stg], 5120\n\ts_add_u32 %[t], %[ho], 81920\n\tbuffer_load_dwordx4 %[so], s[48:51], %[t] offen lds\n\ts_add_u32 m0, %[stg], 6144\n\ts_add_u32 %[t], %[ho], 98304\n\tbuffer_load_dwordx4 %[so], s[48:51], %[t] offen lds\n\t" \
          "s_add_u32 m0, %[stg], 7168\n\ts_add_u32 %[t], %[ho], 114688\n\tbuffer_load_dwordx4 %[so], s[48:51], %[t] offen lds\n\ts_add_u32 m0, %[stg], 8192\n\ts_add_u32 %[t], %[ho], 131072\n\tbuffer_load_dwordx4 %[so], s[48:51], %[t] offen lds\n\t" \
          "s_add_u32 m0, %[stg], 9216\n\ts_add_u32 %[t], %[ho], 147456\n\tbuffer_load_dwordx4 %[so], s[48:51], %[t] offen lds\n\ts_add_u32 m0, %[stg], 10240\n\ts_add_u32 %[t], %[ho], 163840\n\tbuffer_load_dwordx4 %[so], s[48:51], %[t] offen lds\n\t" \
          "s_add_u32 m0, %[stg], 11264\n\ts_add_u32 %[t], %[ho], 180224\n\tbuffer_load_dwordx4 %[so], s[48:51], %[t] offen lds\n\ts_add_u32 m0, %[stg], 12288\n\ts_add_u32 %[t], %[ho], 196608\n\tbuffer_load_dwordx4 %[so], s[48:51], %[t] offen lds\n\t" \
          "s_add_u32 m0, %[stg], 13312\n\ts_add_u32 %[t], %[ho], 212992\n\tbuffer_load_dwordx4 %[so], s[48:51], %[t] offen lds\n\ts_add_u32 m0, %[stg], 14336\n\ts_add_u32 %[t], %[ho], 229376\n\tbuffer_load_dwordx4 %[so], s[48:51], %[t] offen lds\n\t" \
          "s_add_u32 m0, %[stg], 15360\n\ts_add_u32 %[t], %[ho], 245760\n\tbuffer_load_dwordx4 %[so], s[48:51], %[t] offen lds\n\ts_sub_u32 %[n], %[n], 1\n\ts_cmp_eq_u32 %[n], 0\n\ts_cbranch_scc1 Lw_end%=\n\t" \
          "Lw_7_%=:\n\ts_waitcnt vmcnt(16)\n\tds_read_b64_tr_b16 a[224:225], %[ob] offset:16384\n\tds_read_b64_tr_b16 a[226:227], %[ob] offset:16512\n\tds_read_b64_tr_b16 a[228:229], %[ob] offset:24576\n\tds_read_b64_tr_b16 a[230:231], %[ob] offset:24704\n\t" \
          "ds_read_b64_tr_b16 a[232:233], %[ob] offset:16416\n\tds_read_b64_tr_b16 a[234:235], %[ob] offset:16544\n\tds_read_b64_tr_b16 a[236:237], %[ob] offset:24608\n\tds_read_b64_tr_b16 a[238:239], %[ob] offset:24736\n\tds_read_b64_tr_b16 a[240:241], %[ob] offset:17408\n\tds_read_b64_tr_b16 a[242:243], %[ob] offset:17536\n\t" \
          "ds_read_b64_tr_b16 a[244:245], %[ob] offset:25600\n\tds_read_b64_tr_b16 a[246:247], %[ob] offset:25728\n\tds_read_b64_tr_b16 a[248:249], %[ob] offset:17440\n\tds_read_b64_tr_b16 a[250:251], %[ob] offset:17568\n\tds_read_b64_tr_b16 a[252:253], %[ob] offset:25632\n\tds_read_b64_tr_b16 a[254:255], %[ob] offset:25760\n\t" \
          "s_cmp_gt_u32 %[n], 2\n\ts_cselect_b32 s50, s50, 0\n\ts_cmp_gt_u32 %[n], 6\n\ts_cselect_b32 %[ho], 0x1, 0x5\n\ts_lshl_b32 %[ho], %[ho], 20\n\ts_xor_b32 %[ho], %[ho], %[hx]\n\t" \
          "s_waitcnt lgkmcnt(0)\n\ts_add_u32 m0, %[stg], 16384\n\ts_add_u32 %[t], %[ho], 0\n\tbuffer_load_dwordx4 %[so], s[48:51], %[t] offen lds\n\ts_add_u32 m0, %[stg], 17408\n\ts_add_u32 %[t], %[ho], 16384\n\t" \
          "buffer_load_dwordx4 %[so], s[48:51], %[t] offen lds\n\ts_add_u32 m0, %[stg], 18432\n\ts_add_u32 %[t], %[ho], 32768\n\tbuffer_load_dwordx4 %[so], s[48:51], %[t] offen lds\n\ts_add_u32 m0, %[stg], 19456\n\ts_add_u32 %[t], %[ho], 49152\n\t" \
          "buffer_load_dwordx4 %[so], s[48:51], %[t] offen lds\n\ts_add_u32 m0, %[stg], 20480\n\ts_add_u32 %[t], %[ho], 65536\n\tbuffer_load_dwordx4 %[so], s[48:51], %[t] offen lds\n\ts_add_u32 m0, %[stg], 21504\n\ts_add_u32 %[t], %[ho], 81920\n\t" \
          "buffer_load_dwordx4 %[so], s[48:51], %[t] offen lds\n\ts_add_u32 m0, %[stg], 22528\n\ts_add_u32 %[t], %[ho], 98304\n\tbuffer_load_dwordx4 %[so], s[48:51], %[t] offen lds\n\ts_add_u32 m0, %[stg], 23552\n\ts_add_u32 %[t], %[ho], 114688\n\t" \
          "buffer_load_dwordx4 %[so], s[48:51], %[t] offen lds\n\ts_add_u32 m0, %[stg], 24576\n\ts_add_u32 %[t], %[ho], 131072\n\tbuffer_load_dwordx4 %[so], s[48:51], %[t] offen lds\n\ts_add_u32 m0, %[stg], 25600\n\ts_add_u32 %[t], %[ho], 147456\n\t" \
          "buffer_load_dwordx4 %[so], s[48:51], %[t] offen lds\n\ts_add_u32 m0, %[stg], 26624\n\ts_add_u32 %[t], %[ho], 163840\n\tbuffer_load_dwordx4 %[so], s[48:51], %[t] offen lds\n\ts_add_u32 m0, %[stg], 27648\n\ts_add_u32 %[t], %[ho], 180224\n\t" \
          "buffer_load_dwordx4 %[so], s[48:51], %[t] offen lds\n\ts_add_u32 m0, %[stg], 28672\n\ts_add_u32 %[t], %[ho], 196608\n\tbuffer_load_dwordx4 %[so], s[48:51], %[t] offen lds\n\ts_add_u32 m0, %[stg], 29696\n\ts_add_u32 %[t], %[ho], 212992\n\t" \
          "buffer_load_dwordx4 %[so], s[48:51], %[t] offen lds\n\ts_add_u32 m0, %[stg], 30720\n\ts_add_u32 %[t], %[ho], 229376\n\tbuffer_load_dwordx4 %[so], s[48:51], %[t] offen lds\n\ts_add_u32 m0, %[stg], 31744\n\ts_add_u32 %[t], %[ho], 245760\n\t" \
          "buffer_load_dwordx4 %[so], s[48:51], %[t] offen lds\n\ts_sub_u32 %[n], %[n], 1\n\ts_cmp_eq_u32 %[n], 0\n\ts_cbranch_scc1 Lw_end%=\n\ts_branch Lw_4_%=\n\tLw_end%=:\n\t" \
          "s_waitcnt vmcnt(0)\n\ts_barrier\n\t"
#define VO_CLOB "v176", "v177", "v178", "v179", "v180", "v181", "v182", "v183", "v184", "v185", "v186", "v187", "v188", "v189", "v190", "v191", "v192", "v193", "v194", "v195", "v196", "v197", "v198", "v199", "v200", "v201", "v202", "v203", "v204", "v205", "v206", "v207", "v208", "v209", "v210", "v211", "v212", "v213", "v214", "v215", "v216", "v217", "v218", "v219", "v220", "v221", "v222", "v223", "v224", "v225", "v226", "v227", "v228", "v229", "v230", "v231", "v232", "v233", "v234", "v235", "v236", "v237", "v238", "v239", "v240", "v241", "v242", "v243", "v244", "v245", "v246", "v247", "v248", "v249", "v250", "v251", "v252", "v253", "v254", "v255", "s48", "s49", "s50", "s51", "s52", "s53"
__global__ void __launch_bounds__(256, 1) k_fused(FParams P) {
  __shared__ __attribute__((aligned(16))) char Lraw[sizeof(ChainLds)];
  unsigned long long pc0;
  asm volatile("s_getpc_b64 %0\nLfused_pc0:" : "=s"(pc0));
  const int w = __builtin_amdgcn_readfirstlane(threadIdx.x >> 6);
  if ((blockIdx.x & 7) >= 3) {
    asm volatile("Lfused_worker:");
    worker(P, Lraw, w);
    return;
  }
  ChainLds& L = *(ChainLds*)Lraw;
  u16 *vbuf = P.vbuf, *obuf = P.obuf, *membuf = P.membuf; unsigned* tmo = &P.ctl->tmo[0]; unsigned long long* prof = P.prof; (void)prof;
  const int xq = blockIdx.x & 7, slot = blockIdx.x >> 3, nq = xq < 2 ? 12 : 8;
  const int b = slot < nq ? 12 * xq + slot : NQK + 20 * xq + (slot - nq);
  const int alw = b < NQK ? (b % 9 == 0 ? (b / 9) * 4 + w : -1) : (b == 33 ? 16 + w : (b == 95 ? 20 + w : -1));
  const int xr = xq;
  const unsigned long long alx = (unsigned)__builtin_amdgcn_readfirstlane(alw >= 0 ? -1 : 0);
  const __amdgpu_buffer_rsrc_t rs_o = __builtin_amdgcn_make_buffer_rsrc(obuf, 0, NSEG * OREG, 0x00020000);
  const __amdgpu_buffer_rsrc_t rs_m = __builtin_amdgcn_make_buffer_rsrc(membuf, 0, NSEG * MREG, 0x00020000);
#define PAR_BITS const unsigned* parp = P.par + opaque_lane(); const unsigned long long paro = __ballot(parp[0] != 0u), parm = __ballot(parp[64] != 0u), parv = __ballot(parp[128] != 0u)
  const __amdgpu_buffer_rsrc_t rs_v = __builtin_amdgcn_make_buffer_rsrc(vbuf, 0, NSEG * OREG, 0x00020000);
#define LANE_CONSTS const int lane = opaque_lane(), tid = 64 * w + lane, l15 = lane & 15, lq = lane >> 4, idx = (lane & 1) ? 0xEEEE : 0x4444; \
  const int mem_lane_off = (((lq >> 1) * 2 + (l15 >> 3)) * 16 + (lq & 1) * 8 + (l15 & 7)) * 16 + dbl(w * 4096); \
  const int wrow = 8 * (lq >> 1) + 2 * (lq & 1) + 4 * (l15 >> 3) + 16 * ((l15 >> 2) & 1), wsw = (wrow >> 1) & 3
#define CODE_PREFETCH do { int clen; asm volatile("s_mov_b32 %0, Lfused_worker-Lfused_pc0" : "=s"(clen)); \
      unsigned acc_ = 0; for (int o_ = tid * 64; o_ + 4 <= clen; o_ += 256 * 64) acc_ ^= *(const unsigned*)((const char*)pc0 + o_); asm volatile("" :: "v"(acc_)); } while (0)
  unsigned tA = 0;
  if (b < NQK) {
    const int h = b;
    LANE_CONSTS;
    v32u wa[8], wv_[7];
    const unsigned lb0 = (unsigned)(size_t)Lraw;
    const unsigned own = lb0 + w * 40960 + lane * 16;
    {
      const __amdgpu_buffer_rsrc_t rq = __builtin_amdgcn_make_buffer_rsrc((void*)(P.Wq + h * DHA + 64 * (w >> 1)), 0, 0x7fffffff, 0x00020000), rk = __builtin_amdgcn_make_buffer_rsrc((void*)(P.Wk + h * DHA + 64 * (w >> 1)), 0, 0x7fffffff, 0x00020000);
      const int go = (lane >> 3) * HDA * 2 + (((((lane & 7) >> 1) ^ ((lane >> 4) & 3)) << 1) | (lane & 1)) * 16;
      unsigned rd[4];
#pragma unroll
      for (int t = 0; t < 4; ++t) rd[t] = lb0 + w * 40960 + wrow * 128 + (l15 & 3) * 8 + ((t ^ (2 * (~w & 1)) ^ wsw) << 5);
#define QK_NTq ""
#define QK_NTk ""
      int t_;
      asm volatile(QK_LOADER : QK_DEFW, [t] "=&s"(t_)
                   : [rd0] "v"(rd[0]), [rd1] "v"(rd[1]), [rd2] "v"(rd[2]), [rd3] "v"(rd[3]), [own] "v"(own), [go] "v"(go), [rq] "s"(rq), [rk] "s"(rk), [lw] "s"(lb0 + w * 40960), [kb] "s"((8 * (w & 1) + 2 * xr + NHX * (w >> 1)) * 0x80000) : "memory", "scc");
    }
    PAR_BITS;
    const __amdgpu_buffer_rsrc_t rs_x = __builtin_amdgcn_make_buffer_rsrc(P.qkvA, 0, QSIZE, 0x00020000);
    const unsigned long long parq = __ballot(parp[192] != 0u);
    const int xk = qoff(parq, 16 + (h >> 1)) + (h & 1) * 4096, xv = qoff(parq, 32 + (h >> 1)) + (h & 1) * 4096;
    const __amdgpu_buffer_rsrc_t rs_k = __builtin_amdgcn_make_buffer_rsrc(P.kmem, 0, NSEG * MEM * HDA * 2, 0x00020000);
#define PF_ADDR const int tz = opaque_lane()
#define KV_SEGS(v) (min(__builtin_amdgcn_readlane(v, 0), __builtin_amdgcn_readlane(v, 1)) >> 1)
#define WAIT_KV(sg) do { for (unsigned spins = 0;; ++spins) { const unsigned v_ = ld_agent(&P.ctl->prg[(tz & 1) * 32 + h][0]); kvok = KV_SEGS(v_); if (kvok > (sg)) break; \
        if (spin_fail(spins, tmo, 3u)) break; __builtin_amdgcn_s_sleep(8); } } while (0)
    int kvok = 0;
#define LOAD_KV(sg) do { const int so_ = (sg) * 2 * QBLK + w * 1024; \
        _Pragma("unroll") for (int q = 0; q < 4; ++q) \
          asm volatile("s_mov_b32 m0, %0\n\ts_nop 4\n\tbuffer_load_dwordx4 %1, %2, %3 offen sc1 lds" :: "s"(lb0 + w * 40960 + QK_KV + q * 1024), "v"((q < 2 ? tz : tz ^ ((tz >> 1) & 8)) * 16), "s"(rs_x), \
                       "s"(so_ + (q & 1) * QBLK + ((q >> 1) ? xv : xk)) : "memory"); } while (0)
    unsigned pz = 0;
#define LD_ASYNC(dst, voff, rs, soff) asm volatile("s_nop 4\n\tbuffer_load_dword %0, %1, %2, %3 offen sc1" : "=v"(dst) : "v"(voff), "s"(rs), "s"(soff) : "memory")
    {
      PF_ADDR;
      CODE_PREFETCH;
      WAIT_KV(0);
      LOAD_KV(0);
      LD_ASYNC(pz, kvok > 1 && tz < 32 ? tz * 128 : 0x7ffffff0, rs_x, (2 + (w & 1)) * QBLK + ((w >> 1) ? xv : xk));
    }
    const float QS = 0.08838834764831845f * 1.4426950408889634f;
    const unsigned j0 = lq == (l15 >> 2) && !(l15 & 1) ? 0x3f803f80u : 0u, j1 = lq == (l15 >> 2) && (l15 & 1) ? 0x3f803f80u : 0u;
    const v4u JK = {j0, j1, j0, j1};
    const unsigned vxp = lb0 + w * 40960 + QK_KV + 2048 + ((l15 & 3) * 16 + ((4 * lq + (l15 >> 2)) ^ ((l15 & 1) << 3))) * 16;
    const unsigned p1 = lb0 + (w ^ 1) * 40960 + lane * 16, p2 = lb0 + (w ^ 2) * 40960 + lane * 16, p3 = lb0 + (w ^ 3) * 40960 + lane * 16;
    unsigned soloacc = 0; (void)soloacc;
    unsigned pot = 0, kfl = 0, d1p = 140;
    const int mlo = mem_lane_off - dbl(w * 4096) + (w & 1) * 16384 + NHX * (w >> 1) * 1024, hoff = dbl(h * 2048) + w * 512 + lane * 4;
    v4u vm = {0u, 0u, 0u, 0u};
    const int alo = lane < 16 ? ALM + lane * 128 : (lane >= 62 ? RECO + (lane - 62) * 4 : 0x7ffffff0);
    const __amdgpu_buffer_rsrc_t rs_c = __builtin_amdgcn_make_buffer_rsrc(P.ctl, 0, sizeof(Ctl), 0x00020000);
    for (int s = 0; s < NSEG; ++s) {
      int mb = MBASE(s), vb = VBASE(s), ob = OBASE(s), mb0 = mb + xr * 2048;
      unsigned al, ann, fl; unsigned long long bad, bad1, b2, b3, po_;
      if (__builtin_expect(s == 1, 0)) {
        unsigned recD; unsigned* bw = (unsigned*)(Lraw + QK_EX + 1024);
        if (w == 0) { rec_get(tA, recD, rs_m, mb + RECO, tmo, 8); *bw = tA - (recD & 0xffffu) - (recD >> 16); }
        lds_barrier();
        tA = __builtin_amdgcn_readfirstlane(*bw);
      }
      { int rem = (int)(tA - rtnow());
        if (s == 0 || rem > 20000) tA -= rem; }
      tA = __builtin_amdgcn_readfirstlane(tA); d1p = __builtin_amdgcn_readfirstlane(d1p);
#define QK_STO
      { unsigned cnt; unsigned long long en;
        asm volatile(QK_STEP
          : "=&{v[136:167]}"(wv_[4]), "=&{v[168:199]}"(wv_[5]), "=&{v[200:231]}"(wv_[6]), "+{v[232:235]}"(vm), [m] "+v"(pz), "+v"(kfl), [al] "=&v"(al),
            [bad] "=&s"(bad), [bad1] "=&s"(bad1), [b2] "=&s"(b2), [b3] "=&s"(b3), [po] "=&s"(po_), [cnt] "=&s"(cnt), [en] "=&s"(en), [ann] "=&s"(ann), [fl] "=&s"(fl) QK_STO
          : "{a[0:31]}"(wa[0]), "{a[32:63]}"(wa[1]), "{a[64:95]}"(wa[2]), "{a[96:127]}"(wa[3]), "{a[128:159]}"(wa[4]), "{a[160:191]}"(wa[5]), "{a[192:223]}"(wa[6]), "{a[224:255]}"(wa[7]),
            "{v[8:39]}"(wv_[0]), "{v[40:71]}"(wv_[1]), "{v[72:103]}"(wv_[2]), "{v[104:135]}"(wv_[3]),
            [own] "v"(own), [p1] "v"(p1), [p2] "v"(p2), [p3] "v"(p3), [vxp] "v"(vxp), [ml0] "v"(mlo), [ml1] "v"(mlo ^ 1024), [jk] "v"(JK), [ho] "v"(hoff), [idx] "v"(idx), [alo] "v"(alo),
            [rsm] "s"(rs_m), [rsv] "s"(rs_v), [rso] "s"(rs_o), [rsc] "s"(rs_c), [mb] "s"(mb), [vb] "s"(vb), [ob] "s"(ob), [twin] "s"(0xccccccccccccccccull),
            [one] "s"(0x3f803f80u), [qs] "s"(QS), [qs2] "s"((unsigned long long)__float_as_uint(QS)), [hi] "s"(0x1p40f), [lo] "s"(0x1p-40f), [th] "s"(40.f), [t] "s"(tA), [w] "s"(w), [ald0] "s"(d1p + P.sh0),
            [tmo] "s"((int)offsetof(Ctl, tmo)), [mb0] "s"(mb0), [mb1] "s"(mb0 ^ 2048), [mb2] "s"(mb0 ^ 4096), [mb3] "s"(mb0 ^ 6144), [ald] "s"(d1p - P.sh1), [als] "s"(mb + ALM + (alw & 15) * 128), [alx] "s"(alx)
          : "vcc", "scc", "s48", "s49", "s50", "s51", "s52", "s53", "memory"); }
      {
        const v4u Km = {wv_[4][QK_KM], wv_[4][QK_KM + 1], wv_[4][QK_KM + 2], wv_[4][QK_KM + 3]};
        if (__builtin_expect(bad != 0, 0)) {
#pragma unroll 1
          for (unsigned fails = 0; !__all((vm.x > vm.z ? vm.x : vm.z) != 0xffffffffu); ++fails) {
            __builtin_amdgcn_s_sleep(2);
            vm.x = __builtin_amdgcn_raw_buffer_load_b32(rs_v, hoff, vb, 16); vm.z = __builtin_amdgcn_raw_buffer_load_b32(rs_v, hoff, vb + 256, 16);
            if (spin_fail(fails, tmo, 1u)) break;
          }
          float sx[4], sm[2];
#pragma unroll
          for (int e = 0; e < 4; ++e) sx[e] = __uint_as_float(wv_[6][QK_SX + e]);
          sm[0] = __uint_as_float(wv_[4][QK_SM]); sm[1] = __uint_as_float(wv_[4][QK_SM + 1]);
          float mx = fmaxf(fmaxf(sx[0], sx[1]), fmaxf(sx[2], sx[3]));
          mx = fmaxf(mx, __int_as_float(__builtin_amdgcn_update_dpp(0, __float_as_int(mx), 0x4E, 0xf, 0xf, false)));
          mx = xmax32(xmax16(fmaxf(mx, fmaxf(sm[0], sm[1]))));
          if (b2 == 0) mx = 0.f;
          v4u px; px.x = pk2(__builtin_amdgcn_exp2f(sx[0] - mx), __builtin_amdgcn_exp2f(sx[1] - mx)); px.y = pk2(__builtin_amdgcn_exp2f(sx[2] - mx), __builtin_amdgcn_exp2f(sx[3] - mx));
          px.z = __builtin_amdgcn_update_dpp(0, px.x, 0x4E, 0xf, 0xf, false); px.w = __builtin_amdgcn_update_dpp(0, px.y, 0x4E, 0xf, 0xf, false);
          const v2u pm = {pk2(__builtin_amdgcn_exp2f(sm[0] - mx), __builtin_amdgcn_exp2f(sm[1] - mx)), 0u};
          const v4u on = {0x3f803f80u, 0x3f803f80u, 0x3f803f80u, 0x3f803f80u};
          const v4u vx0 = {wv_[6][QK_VX], wv_[6][QK_VX + 1], wv_[6][QK_VX + 2], wv_[6][QK_VX + 3]}, vx1 = {wv_[6][QK_VX + 4], wv_[6][QK_VX + 5], wv_[6][QK_VX + 6], wv_[6][QK_VX + 7]};
          f32x4 c0, c1, ls;
          asm volatile("s_nop 1\n\t"
                       "v_mfma_f32_16x16x32_bf16 %0, %3, %4, 0\n\t"
                       "v_mfma_f32_16x16x32_bf16 %1, %3, %5, 0\n\t"
                       "v_mfma_f32_16x16x32_bf16 %2, %3, %6, 0\n\t"
                       "s_nop 7\n\t"
                       "v_mfma_f32_16x16x16_bf16 %0, %7, v[232:233], %0\n\t"
                       "v_mfma_f32_16x16x16_bf16 %1, %7, v[234:235], %1\n\t"
                       "v_mfma_f32_16x16x16_bf16 %2, %7, v[168:169], %2\n\t"
                       "s_nop 7"
                       : "=&v"(c0), "=&v"(c1), "=&v"(ls) : "v"(px), "v"(vx0), "v"(vx1), "{v[168:171]}"(on), "v"(pm), "{v[232:235]}"(vm));
          const float i0 = __builtin_amdgcn_rcpf(ls[0]), i1 = __builtin_amdgcn_rcpf(ls[1]);
          __builtin_amdgcn_raw_buffer_store_b32(nosent(pk2(c0[0] * i0, c1[0] * i0)), rs_o, hoff, ob, 16);
          __builtin_amdgcn_raw_buffer_store_b32(nosent(pk2(c0[1] * i1, c1[1] * i1)), rs_o, hoff + 256, ob, 16);
          po_ = rtnow();
        }
        SB0;
        const unsigned po = __builtin_amdgcn_readfirstlane((unsigned)po_);
        fl = __builtin_amdgcn_readfirstlane(fl | (bad != 0));
        __builtin_amdgcn_raw_buffer_store_b32(po - tA, rs_m, (h | (w ^ 3) | opaque_lane() | fl) == 0 ? RECO + 128 : 0x7ffffff0, mb, 16);
        asm volatile("s_waitcnt vmcnt(%1)" : "+v"(al) : "i"(ALP < 3 ? 3 : 1) : "memory");
        pot = po;
      if (s < NSEG - 1) {
        unsigned recD = __builtin_amdgcn_readlane(al, 63);
        tA = __builtin_amdgcn_readlane(al, 62);
        if (__builtin_expect(tA == 0xffffffffu || recD == 0xffffffffu, 0)) rec_get(tA, recD, rs_m, mb + RECO, tmo);
        if (s > 0) {
          d1p = recD & 0xffffu;
          unsigned tB = tA - (recD >> 16);
          al_eval(al, tB);
          if (__builtin_expect((int)(pot + P.sh3 - tB) > 0, 0)) tB = pot + P.sh3;
          tA = tB + (recD >> 16);
        } else tA = pot;
        PF_ADDR;
        kvok = max(kvok, KV_SEGS(kfl));
        if (__builtin_expect(kvok <= s + 1, 0)) WAIT_KV(s + 1);
        LOAD_KV(s + 1);
        LD_ASYNC(kfl, kvok < s + 5 && kvok < NSEG && tz < 2 ? tz * 4096 : 0x7ffffff0, rs_c, (int)offsetof(Ctl, prg) + h * 128);
        const int po2 = s + 2 < kvok && !(tz & 32) ? tz * 128 : 0x7ffffff0;
        LD_ASYNC(pz, po2, rs_x, (2 * (s + 2) + (w & 1)) * QBLK + ((w >> 1) ? xv : xk));
      }
      { const int lz = opaque_lane();
        const int ko = (lz & 2) ? 0x7ffffff0 : h * 2048 + w * 512 + (2 * (lz >> 2) + (lz & 1)) * 16;
        __builtin_amdgcn_raw_buffer_store_b128((v4u){nosent(Km.x), nosent(Km.y), nosent(Km.z), nosent(Km.w)}, rs_k, ko, s * (MEM * HDA * 2), 16); }
      }
    }
  } else {
#define SMF_A(ACC, A, W) asm volatile("v_smfmac_f32_16x16x64_bf16 %0, %1, %2, %3" : "+v"(ACC) : "v"(A), "a"(W), "v"(idx)); SB0
#define SMF_V(ACC, A, W) asm volatile("v_smfmac_f32_16x16x64_bf16 %0, %1, %2, %3" : "+v"(ACC) : "v"(A), "v"(W), "v"(idx)); SB0
#define PIN(x) asm volatile("" : "+v"(x))
    LANE_CONSTS;
    const unsigned c00 = (unsigned)now(), t00 = rtnow();
    const int c = b - NQK, cg = c >> 1, rh = c & 1;
    v32u wa[8], wv_[4];
#define VCH(j) (8 * (w & 1) + 2 * ((j) ^ xq) + (w >> 1))
#define OHEAD(i) (4 * ((i) ^ (2 * xq)) + w)
    {
      const unsigned stg = (unsigned)(size_t)Lraw + w * 32768;
      const unsigned vb = stg + wrow * 128 + (l15 & 3) * 8 + ((w ^ 3 ^ wsw) << 5);
      const __amdgpu_buffer_rsrc_t rv = __builtin_amdgcn_make_buffer_rsrc((void*)(P.Wv + 64 * c), 0, DM * HDA * 2 - 128 * c, 0x00020000);
      int t_, ho, n_;
      asm volatile(VO_LOADER : VO_DEFW, [t] "=&s"(t_), [ho] "=&s"(ho), [n] "=&s"(n_)
        : [sv] "v"((lane >> 3) * HDA * 2 + (((((lane & 7) >> 1) ^ ((lane >> 4) & 3)) << 1) | (lane & 1)) * 16), [so] "v"((lane >> 3) * DM * 2 + (lane & 7) * 16), [vb0] "v"(vb), [vb1] "v"(vb ^ 32), [vb2] "v"(vb ^ 64), [vb3] "v"(vb ^ 96),
          [ob] "v"(stg + (32 * (lq >> 1) + 2 * (lq & 1) + 4 * (l15 >> 3) + 16 * ((l15 >> 2) & 1)) * 128 + (cg & 1) * 64 + (l15 & 3) * 8),
          [stg] "s"(stg), [rv] "s"(rv), [vk] "s"(VCH(0) * 0x80000), [wo] "s"(P.Wo + 64 * (cg >> 1)), [wsz] "s"(HDA * DM * 2 - 128 * (cg >> 1)), [hx] "s"(OHEAD(0) * 0x40000), [k0] "s"(c & 3)
        : "s48", "s49", "s50", "s51", "s52", "s53", "scc", "memory");
    }
    PAR_BITS;
    const unsigned lb0 = (unsigned)(size_t)Lraw;
    unsigned vw[3];
#pragma unroll
    for (int t = 0; t < 3; ++t) vw[t] = lb0 + t * 2064 + ((t ^ w ^ 3) * 64 + lane) * 8;
    const unsigned vr = lb0 + tid * 8, rw = lb0 + 8192 + tid * 8, fa = lb0 + 16384;
    int oo[8];
#pragma unroll
    for (int i = 0; i < 8; ++i) oo[i] = dbl(OHEAD(i) * 2048) + ((lq * 2 + rh) * 16 + l15) * 16;
    int ga[4];
#pragma unroll
    for (int j = 0; j < 4; ++j) ga[j] = mem_lane_off - dbl(w * 4096) + dbl(VCH(j) * 1024);
    const int vo = dbl(c * 1024) + tid * 4, mo = dbl(c * 256) + (32 * (l15 >> 3) + 8 * lq + (l15 & 7)) * 4;
    const __amdgpu_buffer_rsrc_t rs_c = __builtin_amdgcn_make_buffer_rsrc(P.ctl, 0, sizeof(Ctl), 0x00020000);
    const bool keeper = (c == 0 && w == 3);
    int d1 = P.dmv * 16, d2 = P.dov * 16, m1 = (P.dq2 + P.im) * 16, m2 = (P.dq + P.im) * 16;
    int LV = 70;
    bool prev_clean = false;
    unsigned recD = 140;
    const float tpc = 16.f * (float)(rtnow() - t00) / (float)((unsigned)now() - c00);
    int e1 = __builtin_amdgcn_readfirstlane((int)(P.dmv * tpc)), e2 = __builtin_amdgcn_readfirstlane((int)(P.dov * tpc));
    d1 = e1 + m1; d2 = e2 + m2;
    if (tid < 32) ((int*)(Lraw + 16384))[tid] = 0;
    CODE_PREFETCH;
    const int alk = !keeper ? 0x7ffffff0 : (lane < 24 ? ALM + lane * 128 : (lane == 63 ? RECO + 128 : 0x7ffffff0));
    const int al1 = lane < 16 ? ALM + lane * 128 : 0x7ffffff0;
    const int boot = c == 0 ? P.pad & 255 : P.pad >> 8;
    unsigned tB = 0; int mb = MBASE(0), ob = 0;
    for (int s = 0; s < NSEG; ++s) {
      unsigned alr, pm, fw; unsigned long long te; v2u rec;
      const int mbn = MBASE(s), d2r = recD >> 16;
      const unsigned r1 = (d1 >> 4) + (s - 1u < 2u ? (P.boot2 >> (8 * s - 8)) & 255 : 0), r2 = (d2 >> 4) + (s - 1u < 2u ? (P.boot3 >> (8 * s - 8)) & 255 : 0);
#define VO_ST0
#define VO_ST1
#define VO_ST3
#define VO_STO
      { unsigned cnt; unsigned long long en;
        asm volatile(VO_BODY : [al] "=&v"(alr), [rec] "=&v"(rec), [te] "=&s"(te), [cnt] "=&s"(cnt), [en] "=&s"(en), [t] "+s"(tB), [ta] "=&s"(tA), [pm] "=&s"(pm), [fw] "=&s"(fw) VO_STO
          : VO_W, [oo0] "v"(oo[0]), [oo1] "v"(oo[1]), [oo2] "v"(oo[2]), [oo3] "v"(oo[3]), [oo4] "v"(oo[4]), [oo5] "v"(oo[5]), [oo6] "v"(oo[6]), [oo7] "v"(oo[7]), [idx] "v"(idx), [rw] "v"(rw), [mo] "v"(mo), [fa] "v"(fa), [l4] "v"(lane * 4), [al1] "v"(al1),
            [ga0] "v"(ga[0]), [ga1] "v"(ga[1]), [ga2] "v"(ga[2]), [ga3] "v"(ga[3]), [alo] "v"(alk), [vw0] "v"(vw[0]), [vw1] "v"(vw[1]), [vw2] "v"(vw[2]), [vr] "v"(vr), [vo] "v"(vo),
            [w] "s"(w), [rsm] "s"(rs_m), [rso] "s"(rs_o), [rsv] "s"(rs_v), [rsc] "s"(rs_c), [ob] "s"(ob), [mb] "s"(mb), [mbn] "s"(mbn), [vb] "s"(VBASE(s)), [rco] "s"(mbn + RECO), [tmo] "s"((int)offsetof(Ctl, tmo)), [s1] "s"(s),
            [lk] "s"(P.lk1), [als] "s"(mb + ALM + (alw & 31) * 128), [alx] "s"(alx), [dv] "s"(P.dv),
            [d2] "s"(d2r), [mn] "s"(s == 1 ? boot : 10), [shf] "s"(s == 1 ? boot : P.sh4), [r12] "s"(r1 + r2), [rd] "s"(keeper ? r1 | (r2 << 16) : 0u)
          : VO_CLOB, "vcc", "scc", "memory"); }
      const unsigned tv = __builtin_amdgcn_readfirstlane((unsigned)te);
      if (c == 2 && tid == 64 && s > 0) __hip_atomic_store(&P.ctl->prog[0], (unsigned)(s - 1), RLX_AGENT);
      if (keeper && s > 0) {
        const unsigned long long f = __ballot((alr >> 16) == 1u);
        const bool kclean = __builtin_amdgcn_readfirstlane(fw) != s, okA = (f & 0xffff) == 0, okB = kclean && ((f >> 16) & 255) == 0;
        const int kx1 = __builtin_amdgcn_readlane(alr, 63), kx2 = __builtin_amdgcn_readfirstlane(pm - tB);
        if (s > 1) {
          const int gain = s < 5 ? P.gain0 : P.gain;
          if (okA) {
            if (kx1 != -1) { int x = kx1 * 16 - e1; x = x > 320 ? 320 : x; e1 += (x * gain) >> 4; }
            if (!okB) m1 = m1 < 60 * 16 ? m1 + P.inc : m1; else if (m1 > P.dq2 * 16) m1 -= P.dec;
            if (m2 > P.dq * 16) m2 -= P.dec;
          } else if (prev_clean) m2 = m2 < 60 * 16 ? m2 + P.inc : m2;
          if (kclean) { int x = kx2 * 16 - e2; x = x > 320 ? 320 : x; e2 += (x * gain) >> 4; }
          d1 = e1 + m1; d2 = e2 + m2;
        }
        prev_clean = okB;
      }
      tB = tv; recD = 0;
      if (s > 0) {
        unsigned recA = __builtin_amdgcn_readfirstlane(rec.x); recD = __builtin_amdgcn_readfirstlane(rec.y);
        if (__builtin_expect(recA == 0xffffffffu || recD == 0xffffffffu, 0)) rec_get(recA, recD, rs_m, mbn + RECO, tmo);
        tB = recA - (recD >> 16);
        if ((int)(tB - tv) > 20000) tB = tv;
      }
      tB = __builtin_amdgcn_readfirstlane(tB); recD = __builtin_amdgcn_readfirstlane(recD);
      mb = mbn; ob = OBASE(s);
    }
  }
}

__global__ void __launch_bounds__(512) k_prep(PrepParams P) {
  __shared__ unsigned lat[2];
  unsigned long long pc0;
  asm volatile("s_getpc_b64 %0\nLprep_pc0:" : "=s"(pc0));
  const int b = blockIdx.x, tid = threadIdx.x;
  const v4u ff = {0xffffffffu, 0xffffffffu, 0xffffffffu, 0xffffffffu};
  if (b < 3 * NSEG + 48) {
    const int reg = b;
    const bool iso = (reg >> 6) != 1; const int s = reg & (NSEG - 1);
    char* base = reg >= 3 * NSEG ? P.qkv + (size_t)s * QPAGE : (iso ? (reg < NSEG ? P.obuf : P.vbuf) + (size_t)s * OREG : P.membuf + (size_t)s * MREG);
    if (tid < 64) {
      const __amdgpu_buffer_rsrc_t rs = __builtin_amdgcn_make_buffer_rsrc(base, 0, 16384, 0x00020000);
      unsigned best0 = ~0u, best1 = ~0u;
#pragma unroll 1
      for (int t = 0; t < PROBES; ++t) {
        const unsigned long long t0 = __builtin_amdgcn_s_memtime();
        __builtin_amdgcn_raw_buffer_store_b128(ff, rs, tid < (t ? 8 : 16) ? ((t & 1) + (tid >> 3)) * 8192 + (tid & 7) * 16 : 0x7ffffff0, 0, 16);
        asm volatile("s_waitcnt vmcnt(0)" ::: "memory");
        const unsigned dt = (unsigned)(__builtin_amdgcn_s_memtime() - t0);
        if (t & 1) best1 = dt < best1 ? dt : best1; else if (t) best0 = dt < best0 ? dt : best0;
      }
      if (tid == 0) { lat[0] = best0; lat[1] = best1; }
    }
    __syncthreads();
    unsigned xcc; asm volatile("s_getreg_b32 %0, hwreg(HW_REG_XCC_ID)" : "=s"(xcc));
    const int sp = (lat[1] < lat[0]) ^ (((xcc ^ (xcc - b + 1)) >> 2) & 1);
    if (tid == 0) P.par[reg] = reg >= 3 * NSEG ? sp ^ (int)P.far : sp;
    v4u* d = (v4u*)(base + sp * 8192);
    const int nblk = reg >= 3 * NSEG ? 0 : (iso ? 8 : 3);
    for (int i = tid; i < nblk * 512; i += 512) {
      v4u v = ff;
      if (!iso && s == 0) {
        if (i < 1024) {
          const u16* src = P.mem0 + (((i >> 4) & 1) * 4 + ((i >> 1) & 3)) * DM + 32 * (i >> 5) + 8 * ((i >> 3) & 1) + 4 * (i & 1);
#pragma unroll
          for (int p = 0; p < 4; ++p) { unsigned x = src[p] | ((unsigned)src[p + 16] << 16); v[p] = x < 0xfffffffeu ? x : 0xfffffffeu; }
        } else if (i == 1024) v = (v4u){0u, 0u, 0u, 0u};
      }
      __builtin_amdgcn_raw_buffer_store_b128(v, __builtin_amdgcn_make_buffer_rsrc(d, 0, OREG, 0x00020000), ((i >> 9) * 1024 + (i & 511)) * 16, 0, 16);
    }
    if (reg < 3 * NSEG) return;
  } else if (tid == 0) P.par[b] = 0;
  const int fb = b - 3 * NSEG;
  {
    int clen; asm volatile("s_mov_b32 %0, Lprep_pc0-Lfused_pc0" : "=s"(clen));
    unsigned acc_ = 0;
    for (int o_ = 64 + tid * 64; o_ <= clen; o_ += 512 * 64) acc_ ^= *(const unsigned*)((const char*)pc0 - o_);
    asm volatile("" :: "v"(acc_)); }
  v4u pa = {0u, 0u, 0u, 0u};
  if (P.pfr) {
    pa = P.ax[fb * 512 + tid];
#pragma unroll 4
    for (unsigned i = fb * 512 + tid; i < (unsigned)P.pfr * 512; i += 64 * 512) pa ^= P.wk[i] ^ P.wv[i];
  }
  for (unsigned long long i = (unsigned long long)fb * 512 + tid; i < P.sent_vec; i += (unsigned long long)(PSENT - 3 * NSEG) * 512)
    __builtin_amdgcn_raw_buffer_store_b128(ff, __builtin_amdgcn_make_buffer_rsrc(P.sent, 0, (int)P.sent_vec * 16, 0x00020000), (int)i * 16, 0, 16);
  if (KF0) __builtin_amdgcn_raw_buffer_store_b128(ff, __builtin_amdgcn_make_buffer_rsrc((char*)P.sent - TSEG0 * MEM * HDA * 2, 0, KF0 * MEM * HDA * 2, 0x00020000), (fb * 512 + tid) * 16, 0, 16);
  asm volatile("" :: "v"(pa));
  if (fb == 5) for (unsigned i = tid; i < (unsigned)P.ctl_vec; i += 512) P.ctl[i] = (v4u){0u, 0u, 0u, 0u};
}

extern "C" void kernel_launch(void* const* d_in, const int* in_sizes, int n_in,
                              void* d_out, int out_size, void* d_ws, size_t ws_size,
                              hipStream_t stream) {
  const u16* A_x = (const u16*)d_in[0];
  const u16* A_mem0 = (const u16*)d_in[1];
  const u16* A_Wq = (const u16*)d_in[2];
  const u16* A_Wk = (const u16*)d_in[3];
  const u16* A_Wv = (const u16*)d_in[4];
  const u16* A_Wo = (const u16*)d_in[5];
  const u16* B_x = (const u16*)d_in[7];
  const u16* B_wqkv = (const u16*)d_in[8];
  const u16* B_wo = (const u16*)d_in[9];
  u16* out = (u16*)d_out;

  char* ws = (char*)d_ws;
  size_t off = 0;
  auto alloc = [&](size_t bytes) { char* p = ws + off; off += (bytes + 255) & ~(size_t)255; return p; };
  off = (size_t)(-(long long)(size_t)ws) & (OREG - 1);
  char* obuf = alloc((size_t)NSEG * OREG);
  char* vmem = alloc((size_t)NSEG * OREG);
  char* membuf = alloc((size_t)NSEG * MREG);
  u16* kmem = (u16*)alloc((size_t)NSEG * MEM * HDA * 2);
#define KFILL (NSEG - TSEG0)
  const size_t sent_beg = off - (size_t)KFILL * MEM * HDA * 2;
  const size_t sent_end = off;
  unsigned* par = (unsigned*)alloc(4 * NSEG * 4);
  unsigned long long* prof = (unsigned long long*)alloc(96 * 4 * 72 * 8 + 8 * 256 * 16 + 64 * 384 * 8);
  Ctl* ctl = (Ctl*)alloc(sizeof(Ctl));
  off += (size_t)(-(long long)(size_t)(ws + off)) & (QPAGE - 1);
  u16* qkvA = (u16*)alloc(QSIZE);
  u16* qkvB = (u16*)alloc((size_t)S_ * NQKVB * 2);
  float* part = (float*)alloc((size_t)(NSEG - TSEG0) * 8 * SEG * DM * 4);

  PrepParams pp;
  pp.sent = (v4u*)(ws + sent_beg); pp.sent_vec = (sent_end - sent_beg) / 16; pp.ctl = (v4u*)ctl; pp.mem0 = A_mem0; pp.obuf = obuf; pp.membuf = membuf; pp.vbuf = vmem; pp.par = par; pp.ctl_vec = sizeof(Ctl) / 16;
  pp.wk = (const v4u*)A_Wk; pp.wv = (const v4u*)A_Wv; pp.ax = (const v4u*)A_x; pp.pfr = PFR; pp.far = 1; pp.qkv = (char*)qkvA;
  k_prep<<<PSENT, 512, 0, stream>>>(pp);
  FParams cp;
  cp.Ax = A_x; cp.Bx = B_x; cp.Wq = A_Wq; cp.Wk = A_Wk; cp.Wv = A_Wv; cp.Wo = A_Wo; cp.wqkvB = B_wqkv; cp.woB = B_wo;
  cp.qkvA = qkvA; cp.qkvB = qkvB; cp.kmem = kmem; cp.vbuf = (u16*)vmem; cp.obuf = (u16*)obuf; cp.membuf = (u16*)membuf; cp.par = par; cp.out = out;
  cp.part = part; cp.ctl = ctl; cp.prof = prof;
  cp.dq = DQ; cp.dq2 = DQ2; cp.dmv = DMV; cp.dov = DOV; cp.gain = GAIN; cp.inc = MINC; cp.dec = MDEC; cp.pb = PBV; cp.stag = STAG; cp.sh1 = SH1; cp.sh2 = SH2; cp.sh3 = SH3; cp.dv = DVV; cp.pad = BOOT; cp.px = PXV; cp.eps = EPSV; cp.abd = ABD; cp.boot2 = BOOT2; cp.boot3 = BOOT3; cp.im = IMARG; cp.gain0 = GAIN0; cp.oj1 = OJ1; cp.lk1 = LK1; cp.lk4 = LK4; cp.sh0 = SH0; cp.sh4 = SH4;
  k_fused<<<256, 256, 0, stream>>>(cp);
}
```

```cpp
#include <hip/hip_runtime.h>
#include <hip/hip_bf16.h>
#include <stdint.h>
#include <stddef.h>

#define DQ 10
#define DQ2 13
#define DMV 2400
#define DOV 1400
#define GAIN 4
#define GAIN0 8
#define MINC 32
#define MDEC 6
#define STAG 1000
#define SH1 14
#define SH2 75
#define SH3 6
#define DVV 0
#define BOOT 0x2850
#define PXV (1200 * 65536 + 32)
#define PBV (1800 * 65536 + 3)
#define PROBES 4
#define JB 80
#define BOOT2 0x0505
#define BOOT3 0x0a14
#define IMARG 3
#define PFR 0
#define OJ1 48
#define LK1 34
#define LK4 38
#define SH0 10
#define SH4 24
#define VSLOT 60
typedef unsigned short u16;
typedef __attribute__((ext_vector_type(4))) float f32x4;
typedef unsigned v4u __attribute__((ext_vector_type(4)));

constexpr int S_ = 2048, DM = 1024, HA = 32, DHA = 128, HDA = 4096, MEM = 8, SEG = 32, NSEG = 64;
constexpr int QPAGE = 2 << 20, QBLK = 16384, NPAGE = 60, QSIZE = NPAGE * QPAGE;
__device__ __forceinline__ int qoff(unsigned long long parq, int n) { return n * QPAGE + (int)((parq >> n) & 1) * 8192; }
constexpr int DB = 1024, HB = 32, DHB = 32, NQKVB = 3 * DB;

constexpr int ROFF_O = 0, ROFF_V = 131072, ROFF_M = 262144, RSLOT = 327680, RSIZE = 4 * RSLOT, KVSEG = 131072, SSEG0 = 60;
typedef __attribute__((ext_vector_type(8))) __bf16 bfx8;
typedef __attribute__((ext_vector_type(2))) __bf16 bfx2;
typedef __attribute__((ext_vector_type(2))) float f32x2;
typedef unsigned v2u __attribute__((ext_vector_type(2)));
typedef unsigned v8u __attribute__((ext_vector_type(8)));
typedef unsigned v32u __attribute__((ext_vector_type(32)));
constexpr int NQK = 32, NVO = 64;
constexpr int VXLD = 144, LDS_ALL = 163840;
template <int I> struct IC { static constexpr int v = I; };
template <int I, int N, class F> __device__ __forceinline__ void sfor(F&& f) { if constexpr (I < N) { f(IC<I>{}); sfor<I + 1, N>(f); } }
#define SFOR(u, a, b) sfor<a, b>([&](auto u##_) __attribute__((always_inline)) { constexpr int u = decltype(u##_)::v;
#define SEND });

__device__ __forceinline__ void lds_barrier() { asm volatile("s_waitcnt lgkmcnt(0)" ::: "memory"); __builtin_amdgcn_s_barrier(); asm volatile("" ::: "memory"); }
template <int N> __device__ __forceinline__ float dpp_ror(float v) {
  return __int_as_float(__builtin_amdgcn_update_dpp(0, __float_as_int(v), 0x120 + N, 0xf, 0xf, false));
}
__device__ __forceinline__ unsigned pk2(float a, float b) { f32x2 t = {a, b}; return __builtin_bit_cast(unsigned, __builtin_convertvector(t, bfx2)); }
__device__ __forceinline__ unsigned nosent(unsigned x) { return x < 0xfffffffeu ? x : 0xfffffffeu; }
__device__ __forceinline__ unsigned umax3(unsigned a, unsigned b, unsigned c) { unsigned m = a > b ? a : b; return m > c ? m : c; }
__device__ __forceinline__ unsigned chk4(unsigned m, v4u v) { return umax3(umax3(m, v.x, v.y), v.z, v.w); }
__device__ __forceinline__ unsigned long long now() { return __builtin_amdgcn_s_memtime(); }
__device__ __forceinline__ f32x4 mf(v4u a, v4u b, f32x4 c) {
  return __builtin_amdgcn_mfma_f32_16x16x32_bf16(__builtin_bit_cast(bfx8, a), __builtin_bit_cast(bfx8, b), c, 0, 0, 0);
}
__device__ __forceinline__ v2u trrd(const u16* p) {
  typedef __attribute__((ext_vector_type(4))) short v4i16_t;
  return __builtin_bit_cast(v2u, __builtin_amdgcn_ds_read_tr16_b64_v4i16((__attribute__((address_space(3))) v4i16_t*)p));
}
__device__ __forceinline__ v8u cat8(v4u a, v4u b) { return __builtin_shufflevector(a, b, 0, 1, 2, 3, 4, 5, 6, 7); }
__device__ __forceinline__ float xmax16(float x) { auto r = __builtin_amdgcn_permlane16_swap(__float_as_uint(x), __float_as_uint(x), false, false); return fmaxf(__uint_as_float(r[0]), __uint_as_float(r[1])); }
__device__ __forceinline__ float xmax32(float x) { auto r = __builtin_amdgcn_permlane32_swap(__float_as_uint(x), __float_as_uint(x), false, false); return fmaxf(__uint_as_float(r[0]), __uint_as_float(r[1])); }
__device__ __forceinline__ float xsum16(float x) { auto r = __builtin_amdgcn_permlane16_swap(__float_as_uint(x), __float_as_uint(x), false, false); return __uint_as_float(r[0]) + __uint_as_float(r[1]); }
__device__ __forceinline__ float xsum32(float x) { auto r = __builtin_amdgcn_permlane32_swap(__float_as_uint(x), __float_as_uint(x), false, false); return __uint_as_float(r[0]) + __uint_as_float(r[1]); }
__device__ __forceinline__ int dbl(int off) { return ((off >> 13) << 14) | (off & 8191); }
__device__ __forceinline__ bool spin_fail(unsigned spins, unsigned* tmo, unsigned code) {
  if ((spins & 31u) != 31u) return false;
  if (spins > (1u << 15)) { __hip_atomic_store(tmo, code, __ATOMIC_RELAXED, __HIP_MEMORY_SCOPE_AGENT); return true; }
  return __builtin_amdgcn_readfirstlane(__hip_atomic_load(tmo, __ATOMIC_RELAXED, __HIP_MEMORY_SCOPE_AGENT)) != 0u;
}
#define SB0 __builtin_amdgcn_sched_barrier(0)
#define NTQ 0
#define PSL 48
#define TSL 6
__device__ __forceinline__ int opaque_lane() { int l; asm volatile("v_mbcnt_lo_u32_b32 %0, -1, 0\n\tv_mbcnt_hi_u32_b32 %0, -1, %0" : "=v"(l)); return l & 63; }

#define OUTAUX 16
namespace wg {
constexpr int LDS_USE = 131072;
__device__ __forceinline__ void* uni(const void* p) { const unsigned long long x = (unsigned long long)p; return (void*)(((unsigned long long)(unsigned)__builtin_amdgcn_readfirstlane(x >> 32) << 32) | (unsigned)__builtin_amdgcn_readfirstlane(x)); }
template <int MT>
__device__ __forceinline__ void gemm_tile(const u16* A, int lda, const u16* B, int ldb, int nst  , void* C, int ldc, unsigned long long parq  , char* lds, int w) {
  constexpr bool apack = true, cpack = false;
  constexpr int MI = MT / 32, PA = MT / 64, NP = PA + 4;
  constexpr int A_B = MT * 64, ST = A_B + 16384;
  const int lane = opaque_lane();
  const int l15 = lane & 15, lq = lane >> 4, wm = w >> 1, wn = w & 1;
  lda = __builtin_amdgcn_readfirstlane(lda); ldb = __builtin_amdgcn_readfirstlane(ldb); ldc = __builtin_amdgcn_readfirstlane(ldc); nst = __builtin_amdgcn_readfirstlane(nst);
  f32x4 acc[8][MI];
#pragma unroll
  for (int i = 0; i < 8; ++i)
#pragma unroll
    for (int j = 0; j < MI; ++j) acc[i][j] = (f32x4){0.f, 0.f, 0.f, 0.f};
  const unsigned lb = (unsigned)(size_t)lds;
  auto fsw = [](int x) { return ((x ^ (x >> 1)) & 1) << 1 | (x >> 1); };
  unsigned ao[2], bo[2][8];
  ao[0] = lb + wm * (MT / 2) * 64 + (apack ? lane * 16 : l15 * 64 + ((lq ^ fsw(l15 >> 2)) << 4)); ao[1] = ao[0] + 2 * ST;
  { const int hh = (l15 >> 2) | ((lq & 1) << 2);
#pragma unroll
    for (int i = 0; i < 8; ++i) { bo[0][i] = lb + A_B + (8 * lq + (l15 >> 2)) * 512 + ((16 * wn + ((2 * i) ^ (2 * hh)) + ((l15 >> 1) & 1)) << 4) + (l15 & 1) * 8; bo[1][i] = bo[0][i] + 2 * ST; } }
  __amdgpu_buffer_rsrc_t ra = __builtin_amdgcn_make_buffer_rsrc(uni(A), 0, 0x7fffffff, 0x00020000), rb = __builtin_amdgcn_make_buffer_rsrc(uni(B), 0, 0x7fffffff, 0x00020000);
  const int va = (16 * PA * w * lda + (apack ? lane * 8 : (lane >> 2) * lda + (((lane & 3) ^ fsw(lane >> 4)) << 3))) * 2, ska = __builtin_amdgcn_readfirstlane(apack ? 1024 : 64);
  const int vb0 = ((8 * w + (lane >> 5)) * ldb + (((lane & 31) ^ ((lane >> 5) << 1) ^ ((w & 1) << 3)) << 3)) * 2, vb1 = vb0 ^ 64;
  const int sa = 32 * lda, sb = 4 * ldb, skb = 64 * ldb;
  const unsigned ma = lb + PA * w * 1024, mb = lb + A_B + w * 4096;
#define PIECE1(NT, base, imm, vo, rs, so) do { if constexpr (NT) asm volatile("s_add_u32 m0, %0, %1\n\ts_nop 0\n\tbuffer_load_dwordx4 %2, %3, %4 offen nt lds" :: "s"(base), "i"(imm), "v"(vo), "s"(rs), "s"(so) : "memory", "scc"); \
    else asm volatile("s_add_u32 m0, %0, %1\n\ts_nop 0\n\tbuffer_load_dwordx4 %2, %3, %4 offen lds" :: "s"(base), "i"(imm), "v"(vo), "s"(rs), "s"(so) : "memory", "scc"); } while (0)
#define PIECE(q, st) do { \
    if ((q) < PA) PIECE1(false, ma, (st) * ST + (q) * 1024, va, ra, ka + (q) * sa); \
    else PIECE1(false, mb, (st) * ST + ((q) - PA) * 1024, (((q) - PA) & 1) ? vb1 : vb0, rb, kb + ((q) - PA) * sb); } while (0)
#define RDA(f, j, st) asm volatile("ds_read_b128 %0, %1 offset:%2" : "=v"(fa[f][j]) : "v"(ao[(st) >> 1]), "i"(((st) & 1) * ST + (j) * 1024) : "memory")
#define RDB(f, i, h, st) asm volatile("ds_read_b64_tr_b16 %0, %1 offset:%2" : "=v"(fb[f][i][h]) : "v"(bo[(st) >> 1][i]), "i"(((st) & 1) * ST + (h) * 2048) : "memory")
#define TIE(f) do { _Pragma("unroll") for (int j = 0; j < MI; ++j) asm volatile("" : "+v"(fa[f][j])); \
    _Pragma("unroll") for (int i = 0; i < 8; ++i) asm volatile("" : "+v"(fb[f][i][0]), "+v"(fb[f][i][1])); } while (0)
  v4u fa[2][MI]; v2u fb[2][8][2];
  int ka = qoff(parq, 0), kb = 0;
  asm volatile("s_nop 4" :: "s"(ra), "s"(rb), "s"(sa), "s"(sb), "s"(skb), "s"(ska));
#pragma unroll
  for (int st = 0; st < 4; ++st) {
#pragma unroll
    for (int q = 0; q < NP; ++q) PIECE(q, st);
    ka += ska; kb += skb;
  }
  asm volatile("s_waitcnt vmcnt(%0)\n\ts_barrier" :: "i"(3 * NP) : "memory");
#pragma unroll
  for (int i = 0; i < 8; ++i) { RDB(0, i, 0, 0); RDB(0, i, 1, 0); }
#pragma unroll
  for (int j = 0; j < MI; ++j) RDA(0, j, 0);
  int k4 = 0;
  do {
    if (apack && (k4 & 4)) ka = qoff(parq, (k4 + 4) >> 3);
    if (k4 + 4 >= nst) { ra = __builtin_amdgcn_make_buffer_rsrc(uni(C), 0, 0, 0x00020000); rb = ra; }
#pragma unroll
    for (int st = 0; st < 4; ++st) {
      constexpr int NM = 8 * MI, NR = 16 + MI;
      const int f = st & 1, sn = (st + 1) & 3;
      asm volatile("s_waitcnt vmcnt(%0) lgkmcnt(0)\n\ts_barrier" :: "i"(2 * NP) : "memory");
      TIE(f);
      SB0;
#pragma unroll
      for (int u = 0; u < NM; ++u) {
        const int j = u >> 3, i = u & 7;
        asm volatile("v_mfma_f32_16x16x32_bf16 %0, %1, %2, %0" : "+a"(acc[i][j]) : "v"((v4u){fb[f][i][0].x, fb[f][i][0].y, fb[f][i][1].x, fb[f][i][1].y}), "v"(fa[f][j]));
        SB0;
        constexpr int RS = MT == 256 ? 2 : 1;
        if (u % RS == 0 && u / RS < NR) {
          const int r = u / RS;
          if (r < 16) RDB(f ^ 1, r >> 1, r & 1, sn); else RDA(f ^ 1, r - 16, sn);
          if (MT == 64 && (u == 0 || u == 2)) RDA(f ^ 1, u >> 1, sn);
          SB0;
        }
        constexpr int PS = NM / 8;
        if (u % PS == PS - 1 && u / PS < NP) { PIECE(u / PS, st); SB0; }
      }
      ka += ska; kb += skb;
    }
    k4 += 4;
  } while (k4 < nst);
#undef PIECE
#undef PIECE1
#undef RDA
#undef RDB
  asm volatile("s_nop 7\n\ts_waitcnt lgkmcnt(0)" ::: "memory");
  SB0;
  TIE(0);
#undef TIE
  char* st = lds + LDS_USE + 2048 + w * 4096;
  const __amdgpu_buffer_rsrc_t rs = __builtin_amdgcn_make_buffer_rsrc(uni(C), 0, MT * ldc * 2 - 1536 * 2, 0x00020000);
  const int so = wm * (MT / 2) * ldc * 2 + (cpack ? wn * 4096 + lane * 16 : (lq * ldc + wn * 128 + l15 * 8) * 2), sq = cpack ? 1024 : 8 * ldc;
#pragma unroll
  for (int j = 0; j < MI; ++j) {
    v2u p[8]; v4u v[4];
#pragma unroll
    for (int i = 0; i < 8; ++i) { asm volatile("" : "+a"(acc[i][j])); p[i] = (v2u){pk2(acc[i][j][0], acc[i][j][1]), pk2(acc[i][j][2], acc[i][j][3])}; }
#pragma unroll
    for (int q = 0; q < 4; ++q) { v[q] = (v4u){p[2 * q].x, p[2 * q].y, p[2 * q + 1].x, p[2 * q + 1].y}; asm volatile("" : "+v"(v[q])); }
    if (!cpack) {
#pragma unroll
      for (int i = 0; i < 8; ++i) *(v2u*)(st + l15 * 256 + (((2 * i + (lq >> 1)) ^ l15) << 4) + (lq & 1) * 8) = (i & 1) ? (v2u){v[i >> 1].z, v[i >> 1].w} : (v2u){v[i >> 1].x, v[i >> 1].y};
      asm volatile("" ::: "memory");
#pragma unroll
      for (int q = 0; q < 4; ++q) v[q] = *(const v4u*)(st + (4 * q + lq) * 256 + ((l15 ^ (4 * q + lq)) << 4));
      asm volatile("" ::: "memory");
    }
#pragma unroll
    for (int q = 0; q < 4; ++q) {
      __builtin_amdgcn_raw_buffer_store_b128(v[q], rs, so, 32 * j * ldc + q * sq, OUTAUX);
    }
    SB0;
  }
  asm volatile("s_waitcnt vmcnt(0)" ::: "memory");
  __builtin_amdgcn_s_barrier();
}
}

struct Ctl {
  unsigned qhead[32];
  unsigned prg[192][32];
  unsigned cntAB[16][32];
  unsigned cntO[72][32];
  unsigned prog[32];
  unsigned tmo[32];
  unsigned chk[32];
};
struct FParams {
  const u16 *Ax, *mem0, *Bx, *Wq, *Wk, *Wv, *Wo, *wqkvB, *woB;
  u16 *qkvA, *out; char *kvm, *ring, *rdy;
  Ctl* ctl; unsigned long long* prof;
  int dq, dq2, dmv, dov, gain, inc, dec, pb, stag, sh1, sh2, sh3, dv, pad, px, probes, jb, boot2, boot3, im, gain0, oj1, lk1, lk4, sh0, sh4;
};
#define RLX_AGENT __ATOMIC_RELAXED, __HIP_MEMORY_SCOPE_AGENT
__device__ __forceinline__ unsigned ld_agent(const unsigned* p) { return __hip_atomic_load(p, RLX_AGENT); }
constexpr int RDY_SIZE = 64 * 128;
__device__ __forceinline__ v4u rdy_load(const FParams& F) { const int lz = opaque_lane(); return __builtin_amdgcn_raw_buffer_load_b128(__builtin_amdgcn_make_buffer_rsrc(F.rdy, 0, RDY_SIZE, 0x00020000), lz * 128, 0, 16); }
__device__ __forceinline__ unsigned long long rdy_tag(v4u r) { return (((unsigned long long)(unsigned)__builtin_amdgcn_readlane(r.y, 63) << 32) | (unsigned)__builtin_amdgcn_readlane(r.x, 63)) + 1; }
__device__ __forceinline__ unsigned long long rdy_wait(const FParams& F, v4u r, unsigned long long& tag) {
  tag = rdy_tag(r);
  for (unsigned spins = 0; (~__ballot(r.x == (unsigned)tag && r.y == (unsigned)(tag >> 32)) << 4) != 0 && spins < (1u << 16); ++spins) { __builtin_amdgcn_s_sleep(8); r = rdy_load(F); }
  asm volatile("" ::: "memory");
  return (__ballot(r.z & 1) & ~(15ull << 60)) | (__builtin_popcountll(__ballot(r.z & 4) << 4) > 30 ? 1ull << 63 : 0ull);
}
__device__ __forceinline__ void check_in(const FParams& F, unsigned long long tag, int w) {
  __syncthreads();
  if (w == 0 && opaque_lane() == 0 && __hip_atomic_fetch_add(&F.ctl->chk[0], 1u, RLX_AGENT) == 255u) __hip_atomic_store((unsigned long long*)(F.rdy + 63 * 128), tag, RLX_AGENT);
}
__device__ __forceinline__ void wg_wait(const unsigned* p, unsigned need, Ctl* ctl, int w) {
  if (w == 0) {
    for (unsigned spins = 0;; ++spins) {
      const bool ok = (p == nullptr) || ld_agent(p) >= need;
      if (__all(ok)) break;
      if (spin_fail(spins, &ctl->tmo[0], 2u)) break;
      __builtin_amdgcn_s_sleep(16);
    }
    __builtin_amdgcn_fence(__ATOMIC_ACQUIRE, "agent");
    asm volatile("s_waitcnt vmcnt(0)" ::: "memory");
  }
  __syncthreads();
}

#define PN_BODY(PN_NT) \
          "s_mov_b64 s[40:41], %[xa]\n\ts_mov_b32 s42, 0x400000\n\ts_mov_b32 s43, 0x20000\n\ts_nop 4\n\ts_add_u32 m0, %[wl], 65536\n\ts_mul_i32 %[t], %[ldb], 0\n\t" \
          "buffer_load_dwordx4 %[vw0], %[rw], %[t] offen" PN_NT " lds\n\ts_add_u32 m0, %[wl], 66560\n\ts_mul_i32 %[t], %[ldb], 4\n\tbuffer_load_dwordx4 %[vw0], %[rw], %[t] offen" PN_NT " lds\n\ts_add_u32 m0, %[wl], 67584\n\ts_mul_i32 %[t], %[ldb], 8\n\t" \
          "buffer_load_dwordx4 %[vw1], %[rw], %[t] offen" PN_NT " lds\n\ts_add_u32 m0, %[wl], 68608\n\ts_mul_i32 %[t], %[ldb], 12\n\tbuffer_load_dwordx4 %[vw1], %[rw], %[t] offen" PN_NT " lds\n\ts_add_u32 m0, %[wl], 81920\n\ts_mul_i32 %[t], %[ldb], 64\n\t" \
          "buffer_load_dwordx4 %[vw0], %[rw], %[t] offen" PN_NT " lds\n\ts_add_u32 m0, %[wl], 82944\n\ts_mul_i32 %[t], %[ldb], 68\n\tbuffer_load_dwordx4 %[vw0], %[rw], %[t] offen" PN_NT " lds\n\ts_add_u32 m0, %[wl], 83968\n\ts_mul_i32 %[t], %[ldb], 72\n\t" \
          "buffer_load_dwordx4 %[vw1], %[rw], %[t] offen" PN_NT " lds\n\ts_add_u32 m0, %[wl], 84992\n\ts_mul_i32 %[t], %[ldb], 76\n\tbuffer_load_dwordx4 %[vw1], %[rw], %[t] offen" PN_NT " lds\n\ts_add_u32 m0, %[sl], 0\n\ts_nop 0\n\t" \
          "buffer_load_dwordx4 %[vd0], s[40:43], %[xo] offen lds\n\tbuffer_load_dwordx4 %[vd0], s[40:43], %[xo] offen offset:1024 lds\n\tbuffer_load_dwordx4 %[vd1], s[40:43], %[xo] offen offset:2048 lds\n\tbuffer_load_dwordx4 %[vd1], s[40:43], %[xo] offen offset:3072 lds\n\ts_add_u32 m0, %[sl], 4096\n\ts_nop 0\n\t" \
          "buffer_load_dwordx4 %[vd2], s[40:43], %[xo] offen lds\n\tbuffer_load_dwordx4 %[vd2], s[40:43], %[xo] offen offset:1024 lds\n\tbuffer_load_dwordx4 %[vd3], s[40:43], %[xo] offen offset:2048 lds\n\tbuffer_load_dwordx4 %[vd3], s[40:43], %[xo] offen offset:3072 lds\n\ts_add_u32 %[xo], %[xo], 0x8000\n\ts_add_u32 %[sl], %[sl], 0x8000\n\t" \
          "s_bitset0_b32 %[sl], 17\n\ts_add_u32 m0, %[sl], 0\n\ts_nop 0\n\tbuffer_load_dwordx4 %[vd0], s[40:43], %[xo] offen lds\n\tbuffer_load_dwordx4 %[vd0], s[40:43], %[xo] offen offset:1024 lds\n\tbuffer_load_dwordx4 %[vd1], s[40:43], %[xo] offen offset:2048 lds\n\t" \
          "buffer_load_dwordx4 %[vd1], s[40:43], %[xo] offen offset:3072 lds\n\ts_add_u32 m0, %[sl], 4096\n\ts_nop 0\n\tbuffer_load_dwordx4 %[vd2], s[40:43], %[xo] offen lds\n\tbuffer_load_dwordx4 %[vd2], s[40:43], %[xo] offen offset:1024 lds\n\tbuffer_load_dwordx4 %[vd3], s[40:43], %[xo] offen offset:2048 lds\n\t" \
          "buffer_load_dwordx4 %[vd3], s[40:43], %[xo] offen offset:3072 lds\n\ts_add_u32 %[xo], %[xo], 0x8000\n\ts_add_u32 %[sl], %[sl], 0x8000\n\ts_bitset0_b32 %[sl], 17\n\ts_add_u32 m0, %[wl], 98304\n\ts_mul_i32 %[t], %[ldb], 128\n\t" \
          "buffer_load_dwordx4 %[vw0], %[rw], %[t] offen" PN_NT " lds\n\ts_add_u32 m0, %[wl], 99328\n\ts_mul_i32 %[t], %[ldb], 132\n\tbuffer_load_dwordx4 %[vw0], %[rw], %[t] offen" PN_NT " lds\n\ts_add_u32 m0, %[wl], 100352\n\ts_mul_i32 %[t], %[ldb], 136\n\t" \
          "buffer_load_dwordx4 %[vw1], %[rw], %[t] offen" PN_NT " lds\n\ts_add_u32 m0, %[wl], 101376\n\ts_mul_i32 %[t], %[ldb], 140\n\tbuffer_load_dwordx4 %[vw1], %[rw], %[t] offen" PN_NT " lds\n\ts_add_u32 m0, %[wl], 114688\n\ts_mul_i32 %[t], %[ldb], 192\n\t" \
          "buffer_load_dwordx4 %[vw0], %[rw], %[t] offen" PN_NT " lds\n\ts_add_u32 m0, %[wl], 115712\n\ts_mul_i32 %[t], %[ldb], 196\n\tbuffer_load_dwordx4 %[vw0], %[rw], %[t] offen" PN_NT " lds\n\ts_add_u32 m0, %[wl], 116736\n\ts_mul_i32 %[t], %[ldb], 200\n\t" \
          "buffer_load_dwordx4 %[vw1], %[rw], %[t] offen" PN_NT " lds\n\ts_add_u32 m0, %[wl], 117760\n\ts_mul_i32 %[t], %[ldb], 204\n\tbuffer_load_dwordx4 %[vw1], %[rw], %[t] offen" PN_NT " lds\n\ts_add_u32 m0, %[wl], 131072\n\ts_mul_i32 %[t], %[ldb], 256\n\t" \
          "buffer_load_dwordx4 %[vw0], %[rw], %[t] offen" PN_NT " lds\n\ts_add_u32 m0, %[wl], 132096\n\ts_mul_i32 %[t], %[ldb], 260\n\tbuffer_load_dwordx4 %[vw0], %[rw], %[t] offen" PN_NT " lds\n\ts_add_u32 m0, %[wl], 133120\n\ts_mul_i32 %[t], %[ldb], 264\n\t" \
          "buffer_load_dwordx4 %[vw1], %[rw], %[t] offen" PN_NT " lds\n\ts_add_u32 m0, %[wl], 134144\n\ts_mul_i32 %[t], %[ldb], 268\n\tbuffer_load_dwordx4 %[vw1], %[rw], %[t] offen" PN_NT " lds\n\ts_waitcnt vmcnt(32) lgkmcnt(0)\n\ts_barrier\n\t" \
          "s_add_u32 m0, %[wl], 147456\n\ts_mul_i32 %[t], %[ldb], 320\n\tbuffer_load_dwordx4 %[vw0], %[rw], %[t] offen" PN_NT " lds\n\ts_add_u32 m0, %[wl], 148480\n\ts_mul_i32 %[t], %[ldb], 324\n\tbuffer_load_dwordx4 %[vw0], %[rw], %[t] offen" PN_NT " lds\n\t" \
          "s_add_u32 m0, %[wl], 149504\n\ts_mul_i32 %[t], %[ldb], 328\n\tbuffer_load_dwordx4 %[vw1], %[rw], %[t] offen" PN_NT " lds\n\ts_add_u32 m0, %[wl], 150528\n\ts_mul_i32 %[t], %[ldb], 332\n\tbuffer_load_dwordx4 %[vw1], %[rw], %[t] offen" PN_NT " lds\n\t" \
          "ds_read_b64_tr_b16 a[0:1], %[rt0] offset:0\n\tds_read_b64_tr_b16 a[2:3], %[rt0] offset:1024\n\tds_read_b64_tr_b16 a[4:5], %[rt1] offset:0\n\tds_read_b64_tr_b16 a[6:7], %[rt1] offset:1024\n\tds_read_b64_tr_b16 a[8:9], %[rt0] offset:8192\n\tds_read_b64_tr_b16 a[10:11], %[rt0] offset:9216\n\t" \
          "ds_read_b64_tr_b16 a[12:13], %[rt1] offset:8192\n\tds_read_b64_tr_b16 a[14:15], %[rt1] offset:9216\n\ts_waitcnt vmcnt(32) lgkmcnt(0)\n\ts_barrier\n\ts_add_u32 m0, %[wl], 65536\n\ts_mul_i32 %[t], %[ldb], 384\n\t" \
          "buffer_load_dwordx4 %[vw0], %[rw], %[t] offen" PN_NT " lds\n\ts_add_u32 m0, %[wl], 66560\n\ts_mul_i32 %[t], %[ldb], 388\n\tbuffer_load_dwordx4 %[vw0], %[rw], %[t] offen" PN_NT " lds\n\ts_add_u32 m0, %[wl], 67584\n\ts_mul_i32 %[t], %[ldb], 392\n\t" \
          "buffer_load_dwordx4 %[vw1], %[rw], %[t] offen" PN_NT " lds\n\ts_add_u32 m0, %[wl], 68608\n\ts_mul_i32 %[t], %[ldb], 396\n\tbuffer_load_dwordx4 %[vw1], %[rw], %[t] offen" PN_NT " lds\n\tds_read_b64_tr_b16 a[16:17], %[rt0] offset:16384\n\tds_read_b64_tr_b16 a[18:19], %[rt0] offset:17408\n\t" \
          "ds_read_b64_tr_b16 a[20:21], %[rt1] offset:16384\n\tds_read_b64_tr_b16 a[22:23], %[rt1] offset:17408\n\tds_read_b64_tr_b16 a[24:25], %[rt0] offset:24576\n\tds_read_b64_tr_b16 a[26:27], %[rt0] offset:25600\n\tds_read_b64_tr_b16 a[28:29], %[rt1] offset:24576\n\tds_read_b64_tr_b16 a[30:31], %[rt1] offset:25600\n\t" \
          "s_waitcnt vmcnt(16) lgkmcnt(0)\n\ts_barrier\n\ts_add_u32 m0, %[wl], 81920\n\ts_mul_i32 %[t], %[ldb], 448\n\tbuffer_load_dwordx4 %[vw0], %[rw], %[t] offen" PN_NT " lds\n\ts_add_u32 m0, %[wl], 82944\n\t" \
          "s_mul_i32 %[t], %[ldb], 452\n\tbuffer_load_dwordx4 %[vw0], %[rw], %[t] offen" PN_NT " lds\n\ts_add_u32 m0, %[wl], 83968\n\ts_mul_i32 %[t], %[ldb], 456\n\tbuffer_load_dwordx4 %[vw1], %[rw], %[t] offen" PN_NT " lds\n\ts_add_u32 m0, %[wl], 84992\n\t" \
          "s_mul_i32 %[t], %[ldb], 460\n\tbuffer_load_dwordx4 %[vw1], %[rw], %[t] offen" PN_NT " lds\n\tds_read_b64_tr_b16 a[32:33], %[rt0] offset:32768\n\tds_read_b64_tr_b16 a[34:35], %[rt0] offset:33792\n\tds_read_b64_tr_b16 a[36:37], %[rt1] offset:32768\n\tds_read_b64_tr_b16 a[38:39], %[rt1] offset:33792\n\t" \
          "ds_read_b64_tr_b16 a[40:41], %[rt0] offset:40960\n\tds_read_b64_tr_b16 a[42:43], %[rt0] offset:41984\n\tds_read_b64_tr_b16 a[44:45], %[rt1] offset:40960\n\tds_read_b64_tr_b16 a[46:47], %[rt1] offset:41984\n\ts_waitcnt vmcnt(16) lgkmcnt(0)\n\ts_barrier\n\t" \
          "s_add_u32 m0, %[wl], 98304\n\ts_mul_i32 %[t], %[ldb], 512\n\tbuffer_load_dwordx4 %[vw0], %[rw], %[t] offen" PN_NT " lds\n\ts_add_u32 m0, %[wl], 99328\n\ts_mul_i32 %[t], %[ldb], 516\n\tbuffer_load_dwordx4 %[vw0], %[rw], %[t] offen" PN_NT " lds\n\t" \
          "s_add_u32 m0, %[wl], 100352\n\ts_mul_i32 %[t], %[ldb], 520\n\tbuffer_load_dwordx4 %[vw1], %[rw], %[t] offen" PN_NT " lds\n\ts_add_u32 m0, %[wl], 101376\n\ts_mul_i32 %[t], %[ldb], 524\n\tbuffer_load_dwordx4 %[vw1], %[rw], %[t] offen" PN_NT " lds\n\t" \
          "ds_read_b64_tr_b16 a[48:49], %[rt2] offset:0\n\tds_read_b64_tr_b16 a[50:51], %[rt2] offset:1024\n\tds_read_b64_tr_b16 a[52:53], %[rt3] offset:0\n\tds_read_b64_tr_b16 a[54:55], %[rt3] offset:1024\n\tds_read_b64_tr_b16 a[56:57], %[rt2] offset:8192\n\tds_read_b64_tr_b16 a[58:59], %[rt2] offset:9216\n\t" \
          "ds_read_b64_tr_b16 a[60:61], %[rt3] offset:8192\n\tds_read_b64_tr_b16 a[62:63], %[rt3] offset:9216\n\ts_waitcnt vmcnt(16) lgkmcnt(0)\n\ts_barrier\n\ts_add_u32 m0, %[wl], 114688\n\ts_mul_i32 %[t], %[ldb], 576\n\t" \
          "buffer_load_dwordx4 %[vw0], %[rw], %[t] offen" PN_NT " lds\n\ts_add_u32 m0, %[wl], 115712\n\ts_mul_i32 %[t], %[ldb], 580\n\tbuffer_load_dwordx4 %[vw0], %[rw], %[t] offen" PN_NT " lds\n\ts_add_u32 m0, %[wl], 116736\n\ts_mul_i32 %[t], %[ldb], 584\n\t" \
          "buffer_load_dwordx4 %[vw1], %[rw], %[t] offen" PN_NT " lds\n\ts_add_u32 m0, %[wl], 117760\n\ts_mul_i32 %[t], %[ldb], 588\n\tbuffer_load_dwordx4 %[vw1], %[rw], %[t] offen" PN_NT " lds\n\tds_read_b64_tr_b16 a[64:65], %[rt2] offset:16384\n\tds_read_b64_tr_b16 a[66:67], %[rt2] offset:17408\n\t" \
          "ds_read_b64_tr_b16 a[68:69], %[rt3] offset:16384\n\tds_read_b64_tr_b16 a[70:71], %[rt3] offset:17408\n\tds_read_b64_tr_b16 a[72:73], %[rt2] offset:24576\n\tds_read_b64_tr_b16 a[74:75], %[rt2] offset:25600\n\tds_read_b64_tr_b16 a[76:77], %[rt3] offset:24576\n\tds_read_b64_tr_b16 a[78:79], %[rt3] offset:25600\n\t" \
          "s_waitcnt vmcnt(16) lgkmcnt(0)\n\ts_barrier\n\ts_add_u32 m0, %[wl], 131072\n\ts_mul_i32 %[t], %[ldb], 640\n\tbuffer_load_dwordx4 %[vw0], %[rw], %[t] offen" PN_NT " lds\n\ts_add_u32 m0, %[wl], 132096\n\t" \
          "s_mul_i32 %[t], %[ldb], 644\n\tbuffer_load_dwordx4 %[vw0], %[rw], %[t] offen" PN_NT " lds\n\ts_add_u32 m0, %[wl], 133120\n\ts_mul_i32 %[t], %[ldb], 648\n\tbuffer_load_dwordx4 %[vw1], %[rw], %[t] offen" PN_NT " lds\n\ts_add_u32 m0, %[wl], 134144\n\t" \
          "s_mul_i32 %[t], %[ldb], 652\n\tbuffer_load_dwordx4 %[vw1], %[rw], %[t] offen" PN_NT " lds\n\tds_read_b64_tr_b16 a[80:81], %[rt2] offset:32768\n\tds_read_b64_tr_b16 a[82:83], %[rt2] offset:33792\n\tds_read_b64_tr_b16 a[84:85], %[rt3] offset:32768\n\tds_read_b64_tr_b16 a[86:87], %[rt3] offset:33792\n\t" \
          "ds_read_b64_tr_b16 a[88:89], %[rt2] offset:40960\n\tds_read_b64_tr_b16 a[90:91], %[rt2] offset:41984\n\tds_read_b64_tr_b16 a[92:93], %[rt3] offset:40960\n\tds_read_b64_tr_b16 a[94:95], %[rt3] offset:41984\n\ts_waitcnt vmcnt(16) lgkmcnt(0)\n\ts_barrier\n\t" \
          "s_add_u32 m0, %[wl], 147456\n\ts_mul_i32 %[t], %[ldb], 704\n\tbuffer_load_dwordx4 %[vw0], %[rw], %[t] offen" PN_NT " lds\n\ts_add_u32 m0, %[wl], 148480\n\ts_mul_i32 %[t], %[ldb], 708\n\tbuffer_load_dwordx4 %[vw0], %[rw], %[t] offen" PN_NT " lds\n\t" \
          "s_add_u32 m0, %[wl], 149504\n\ts_mul_i32 %[t], %[ldb], 712\n\tbuffer_load_dwordx4 %[vw1], %[rw], %[t] offen" PN_NT " lds\n\ts_add_u32 m0, %[wl], 150528\n\ts_mul_i32 %[t], %[ldb], 716\n\tbuffer_load_dwordx4 %[vw1], %[rw], %[t] offen" PN_NT " lds\n\t" \
          "ds_read_b64_tr_b16 a[96:97], %[rt0] offset:0\n\tds_read_b64_tr_b16 a[98:99], %[rt0] offset:1024\n\tds_read_b64_tr_b16 a[100:101], %[rt1] offset:0\n\tds_read_b64_tr_b16 a[102:103], %[rt1] offset:1024\n\tds_read_b64_tr_b16 a[104:105], %[rt0] offset:8192\n\tds_read_b64_tr_b16 a[106:107], %[rt0] offset:9216\n\t" \
          "ds_read_b64_tr_b16 a[108:109], %[rt1] offset:8192\n\tds_read_b64_tr_b16 a[110:111], %[rt1] offset:9216\n\ts_waitcnt vmcnt(16) lgkmcnt(0)\n\ts_barrier\n\ts_add_u32 m0, %[wl], 65536\n\ts_mul_i32 %[t], %[ldb], 768\n\t" \
          "buffer_load_dwordx4 %[vw0], %[rw], %[t] offen" PN_NT " lds\n\ts_add_u32 m0, %[wl], 66560\n\ts_mul_i32 %[t], %[ldb], 772\n\tbuffer_load_dwordx4 %[vw0], %[rw], %[t] offen" PN_NT " lds\n\ts_add_u32 m0, %[wl], 67584\n\ts_mul_i32 %[t], %[ldb], 776\n\t" \
          "buffer_load_dwordx4 %[vw1], %[rw], %[t] offen" PN_NT " lds\n\ts_add_u32 m0, %[wl], 68608\n\ts_mul_i32 %[t], %[ldb], 780\n\tbuffer_load_dwordx4 %[vw1], %[rw], %[t] offen" PN_NT " lds\n\tds_read_b64_tr_b16 a[112:113], %[rt0] offset:16384\n\tds_read_b64_tr_b16 a[114:115], %[rt0] offset:17408\n\t" \
          "ds_read_b64_tr_b16 a[116:117], %[rt1] offset:16384\n\tds_read_b64_tr_b16 a[118:119], %[rt1] offset:17408\n\tds_read_b64_tr_b16 a[120:121], %[rt0] offset:24576\n\tds_read_b64_tr_b16 a[122:123], %[rt0] offset:25600\n\tds_read_b64_tr_b16 a[124:125], %[rt1] offset:24576\n\tds_read_b64_tr_b16 a[126:127], %[rt1] offset:25600\n\t" \
          "s_waitcnt vmcnt(16) lgkmcnt(0)\n\ts_barrier\n\ts_add_u32 m0, %[wl], 81920\n\ts_mul_i32 %[t], %[ldb], 832\n\tbuffer_load_dwordx4 %[vw0], %[rw], %[t] offen" PN_NT " lds\n\ts_add_u32 m0, %[wl], 82944\n\t" \
          "s_mul_i32 %[t], %[ldb], 836\n\tbuffer_load_dwordx4 %[vw0], %[rw], %[t] offen" PN_NT " lds\n\ts_add_u32 m0, %[wl], 83968\n\ts_mul_i32 %[t], %[ldb], 840\n\tbuffer_load_dwordx4 %[vw1], %[rw], %[t] offen" PN_NT " lds\n\ts_add_u32 m0, %[wl], 84992\n\t" \
          "s_mul_i32 %[t], %[ldb], 844\n\tbuffer_load_dwordx4 %[vw1], %[rw], %[t] offen" PN_NT " lds\n\tds_read_b64_tr_b16 a[128:129], %[rt0] offset:32768\n\tds_read_b64_tr_b16 a[130:131], %[rt0] offset:33792\n\tds_read_b64_tr_b16 a[132:133], %[rt1] offset:32768\n\tds_read_b64_tr_b16 a[134:135], %[rt1] offset:33792\n\t" \
          "ds_read_b64_tr_b16 a[136:137], %[rt0] offset:40960\n\tds_read_b64_tr_b16 a[138:139], %[rt0] offset:41984\n\tds_read_b64_tr_b16 a[140:141], %[rt1] offset:40960\n\tds_read_b64_tr_b16 a[142:143], %[rt1] offset:41984\n\ts_waitcnt vmcnt(16) lgkmcnt(0)\n\ts_barrier\n\t" \
          "s_add_u32 m0, %[wl], 98304\n\ts_mul_i32 %[t], %[ldb], 896\n\tbuffer_load_dwordx4 %[vw0], %[rw], %[t] offen" PN_NT " lds\n\ts_add_u32 m0, %[wl], 99328\n\ts_mul_i32 %[t], %[ldb], 900\n\tbuffer_load_dwordx4 %[vw0], %[rw], %[t] offen" PN_NT " lds\n\t" \
          "s_add_u32 m0, %[wl], 100352\n\ts_mul_i32 %[t], %[ldb], 904\n\tbuffer_load_dwordx4 %[vw1], %[rw], %[t] offen" PN_NT " lds\n\ts_add_u32 m0, %[wl], 101376\n\ts_mul_i32 %[t], %[ldb], 908\n\tbuffer_load_dwordx4 %[vw1], %[rw], %[t] offen" PN_NT " lds\n\t" \
          "ds_read_b64_tr_b16 a[144:145], %[rt2] offset:0\n\tds_read_b64_tr_b16 a[146:147], %[rt2] offset:1024\n\tds_read_b64_tr_b16 a[148:149], %[rt3] offset:0\n\tds_read_b64_tr_b16 a[150:151], %[rt3] offset:1024\n\tds_read_b64_tr_b16 a[152:153], %[rt2] offset:8192\n\tds_read_b64_tr_b16 a[154:155], %[rt2] offset:9216\n\t" \
          "ds_read_b64_tr_b16 a[156:157], %[rt3] offset:8192\n\tds_read_b64_tr_b16 a[158:159], %[rt3] offset:9216\n\ts_waitcnt vmcnt(16) lgkmcnt(0)\n\ts_barrier\n\ts_add_u32 m0, %[wl], 114688\n\ts_mul_i32 %[t], %[ldb], 960\n\t" \
          "buffer_load_dwordx4 %[vw0], %[rw], %[t] offen" PN_NT " lds\n\ts_add_u32 m0, %[wl], 115712\n\ts_mul_i32 %[t], %[ldb], 964\n\tbuffer_load_dwordx4 %[vw0], %[rw], %[t] offen" PN_NT " lds\n\ts_add_u32 m0, %[wl], 116736\n\ts_mul_i32 %[t], %[ldb], 968\n\t" \
          "buffer_load_dwordx4 %[vw1], %[rw], %[t] offen" PN_NT " lds\n\ts_add_u32 m0, %[wl], 117760\n\ts_mul_i32 %[t], %[ldb], 972\n\tbuffer_load_dwordx4 %[vw1], %[rw], %[t] offen" PN_NT " lds\n\tds_read_b64_tr_b16 a[160:161], %[rt2] offset:16384\n\tds_read_b64_tr_b16 a[162:163], %[rt2] offset:17408\n\t" \
          "ds_read_b64_tr_b16 a[164:165], %[rt3] offset:16384\n\tds_read_b64_tr_b16 a[166:167], %[rt3] offset:17408\n\tds_read_b64_tr_b16 a[168:169], %[rt2] offset:24576\n\tds_read_b64_tr_b16 a[170:171], %[rt2] offset:25600\n\tds_read_b64_tr_b16 a[172:173], %[rt3] offset:24576\n\tds_read_b64_tr_b16 a[174:175], %[rt3] offset:25600\n\t" \
          "s_waitcnt vmcnt(16) lgkmcnt(0)\n\ts_barrier\n\tbuffer_load_dwordx4 v[40:43], %[ro], %[rr], 0 offen sc1\n\tds_read_b64_tr_b16 a[176:177], %[rt2] offset:32768\n\tds_read_b64_tr_b16 a[178:179], %[rt2] offset:33792\n\tds_read_b64_tr_b16 a[180:181], %[rt3] offset:32768\n\t" \
          "ds_read_b64_tr_b16 a[182:183], %[rt3] offset:33792\n\tds_read_b64_tr_b16 a[184:185], %[rt2] offset:40960\n\tds_read_b64_tr_b16 a[186:187], %[rt2] offset:41984\n\tds_read_b64_tr_b16 a[188:189], %[rt3] offset:40960\n\tds_read_b64_tr_b16 a[190:191], %[rt3] offset:41984\n\ts_waitcnt vmcnt(13) lgkmcnt(0)\n\t" \
          "s_barrier\n\tds_read_b64_tr_b16 a[192:193], %[rt0] offset:0\n\tds_read_b64_tr_b16 a[194:195], %[rt0] offset:1024\n\tds_read_b64_tr_b16 a[196:197], %[rt1] offset:0\n\tds_read_b64_tr_b16 a[198:199], %[rt1] offset:1024\n\tds_read_b64_tr_b16 a[200:201], %[rt0] offset:8192\n\t" \
          "ds_read_b64_tr_b16 a[202:203], %[rt0] offset:9216\n\tds_read_b64_tr_b16 a[204:205], %[rt1] offset:8192\n\tds_read_b64_tr_b16 a[206:207], %[rt1] offset:9216\n\ts_waitcnt vmcnt(9) lgkmcnt(0)\n\ts_barrier\n\tds_read_b64_tr_b16 a[208:209], %[rt0] offset:16384\n\t" \
          "ds_read_b64_tr_b16 a[210:211], %[rt0] offset:17408\n\tds_read_b64_tr_b16 a[212:213], %[rt1] offset:16384\n\tds_read_b64_tr_b16 a[214:215], %[rt1] offset:17408\n\tds_read_b64_tr_b16 a[216:217], %[rt0] offset:24576\n\tds_read_b64_tr_b16 a[218:219], %[rt0] offset:25600\n\tds_read_b64_tr_b16 a[220:221], %[rt1] offset:24576\n\t" \
          "ds_read_b64_tr_b16 a[222:223], %[rt1] offset:25600\n\ts_waitcnt vmcnt(5) lgkmcnt(0)\n\ts_barrier\n\tds_read_b64_tr_b16 a[224:225], %[rt0] offset:32768\n\tds_read_b64_tr_b16 a[226:227], %[rt0] offset:33792\n\tds_read_b64_tr_b16 a[228:229], %[rt1] offset:32768\n\t" \
          "ds_read_b64_tr_b16 a[230:231], %[rt1] offset:33792\n\tds_read_b64_tr_b16 a[232:233], %[rt0] offset:40960\n\tds_read_b64_tr_b16 a[234:235], %[rt0] offset:41984\n\tds_read_b64_tr_b16 a[236:237], %[rt1] offset:40960\n\tds_read_b64_tr_b16 a[238:239], %[rt1] offset:41984\n\ts_waitcnt vmcnt(0) lgkmcnt(0)\n\t" \
          "s_barrier\n\tds_read_b64_tr_b16 a[240:241], %[rt2] offset:0\n\tds_read_b64_tr_b16 a[242:243], %[rt2] offset:1024\n\tds_read_b64_tr_b16 a[244:245], %[rt3] offset:0\n\tds_read_b64_tr_b16 a[246:247], %[rt3] offset:1024\n\tds_read_b64_tr_b16 a[248:249], %[rt2] offset:8192\n\t" \
          "ds_read_b64_tr_b16 a[250:251], %[rt2] offset:9216\n\tds_read_b64_tr_b16 a[252:253], %[rt3] offset:8192\n\tds_read_b64_tr_b16 a[254:255], %[rt3] offset:9216\n\ts_waitcnt lgkmcnt(0)\n\ts_barrier\n\ts_add_u32 m0, %[sl], 0\n\t" \
          "s_nop 0\n\tbuffer_load_dwordx4 %[vd0], s[40:43], %[xo] offen lds\n\tbuffer_load_dwordx4 %[vd0], s[40:43], %[xo] offen offset:1024 lds\n\tbuffer_load_dwordx4 %[vd1], s[40:43], %[xo] offen offset:2048 lds\n\tbuffer_load_dwordx4 %[vd1], s[40:43], %[xo] offen offset:3072 lds\n\ts_add_u32 m0, %[sl], 4096\n\t" \
          "s_nop 0\n\tbuffer_load_dwordx4 %[vd2], s[40:43], %[xo] offen lds\n\tbuffer_load_dwordx4 %[vd2], s[40:43], %[xo] offen offset:1024 lds\n\tbuffer_load_dwordx4 %[vd3], s[40:43], %[xo] offen offset:2048 lds\n\tbuffer_load_dwordx4 %[vd3], s[40:43], %[xo] offen offset:3072 lds\n\ts_add_u32 %[xo], %[xo], 0x8000\n\t" \
          "s_add_u32 %[sl], %[sl], 0x8000\n\ts_bitset0_b32 %[sl], 17\n\ts_mov_b32 %[t], 0\n\tv_readlane_b32 s44, v40, 63\n\tv_readlane_b32 s45, v41, 63\n\ts_add_u32 s44, s44, 1\n\t" \
          "s_addc_u32 s45, s45, 0\n\tLpn_r%=:\n\tv_cmp_eq_u64 vcc, s[44:45], v[40:41]\n\ts_add_u32 %[t], %[t], 0x10000\n\ts_cbranch_scc1 Lpn_g%=\n\ts_or_b32 vcc_hi, vcc_hi, 0xf0000000\n\t" \
          "s_and_b32 vcc_lo, vcc_lo, vcc_hi\n\ts_cmp_eq_u32 vcc_lo, -1\n\ts_cbranch_scc1 Lpn_g%=\n\ts_sleep 8\n\tbuffer_load_dwordx4 v[40:43], %[ro], %[rr], 0 offen sc1\n\ts_waitcnt vmcnt(0)\n\t" \
          "s_branch Lpn_r%=\n\tLpn_g%=:\n\tv_readlane_b32 %[t], v42, %[pl]\n\ts_and_b32 %[t], %[t], 1\n\ts_lshl_b32 %[t], %[t], 13\n\ts_add_u32 %[co], %[co], %[t]\n\t" \
          "ds_read_b128 v[64:67], %[ra0] offset:0\n\tds_read_b128 v[68:71], %[ra1] offset:0\n\tds_read_b128 v[72:75], %[ra2] offset:0\n\tds_read_b128 v[76:79], %[ra3] offset:0\n\tds_read_b128 v[80:83], %[ra0] offset:256\n\tds_read_b128 v[84:87], %[ra1] offset:256\n\t" \
          "ds_read_b128 v[88:91], %[ra2] offset:256\n\tds_read_b128 v[92:95], %[ra3] offset:256\n\ts_mov_b64 %[sx], 0\n\tLpn_loop%=:\n\ts_waitcnt lgkmcnt(6)\n\tv_mfma_f32_16x16x32_bf16 v[48:51], a[0:3], v[64:67], 0\n\t" \
          "ds_read_b128 v[96:99], %[ra0] offset:512\n\tv_mfma_f32_16x16x32_bf16 v[52:55], a[4:7], v[64:67], 0\n\ts_add_u32 %[t], %[j], 3\n\ts_cmp_lt_u32 %[t], %[je]\n\ts_cselect_b32 s42, s42, 0\n\tv_mfma_f32_16x16x32_bf16 v[48:51], a[8:11], v[68:71], v[48:51]\n\t" \
          "ds_read_b128 v[100:103], %[ra1] offset:512\n\tv_mfma_f32_16x16x32_bf16 v[52:55], a[12:15], v[68:71], v[52:55]\n\tv_cvt_pk_bf16_f32 v44, v56, v57\n\tv_cvt_pk_bf16_f32 v45, v58, v59\n\ts_waitcnt lgkmcnt(6)\n\tv_mfma_f32_16x16x32_bf16 v[48:51], a[16:19], v[72:75], v[48:51]\n\t" \
          "ds_read_b128 v[104:107], %[ra2] offset:512\n\tv_mfma_f32_16x16x32_bf16 v[52:55], a[20:23], v[72:75], v[52:55]\n\tv_cvt_pk_bf16_f32 v46, v60, v61\n\tv_cvt_pk_bf16_f32 v47, v62, v63\n\tv_mfma_f32_16x16x32_bf16 v[48:51], a[24:27], v[76:79], v[48:51]\n\tds_read_b128 v[108:111], %[ra3] offset:512\n\t" \
          "v_mfma_f32_16x16x32_bf16 v[52:55], a[28:31], v[76:79], v[52:55]\n\ts_mov_b64 exec, %[sx]\n\tbuffer_store_dwordx4 v[44:47], %[so], %[rc], %[co] offen sc1\n\ts_mov_b64 exec, -1\n\ts_waitcnt lgkmcnt(6)\n\tv_mfma_f32_16x16x32_bf16 v[48:51], a[32:35], v[80:83], v[48:51]\n\t" \
          "ds_read_b128 v[112:115], %[ra0] offset:768\n\tv_mfma_f32_16x16x32_bf16 v[52:55], a[36:39], v[80:83], v[52:55]\n\ts_mov_b64 exec, %[fx]\n\tbuffer_store_dword %[fv], off, %[rf], %[fo] sc1\n\ts_mov_b64 exec, -1\n\tv_mfma_f32_16x16x32_bf16 v[48:51], a[40:43], v[84:87], v[48:51]\n\t" \
          "ds_read_b128 v[116:119], %[ra1] offset:768\n\tv_mfma_f32_16x16x32_bf16 v[52:55], a[44:47], v[84:87], v[52:55]\n\ts_add_u32 %[co], %[co], %[cs]\n\ts_mov_b32 %[cs], 0x4000\n\ts_mov_b64 %[sx], -1\n\ts_waitcnt lgkmcnt(6)\n\t" \
          "v_mfma_f32_16x16x32_bf16 v[48:51], a[48:51], v[88:91], v[48:51]\n\tds_read_b128 v[120:123], %[ra2] offset:768\n\ts_add_u32 m0, %[sl], 0\n\tv_mfma_f32_16x16x32_bf16 v[52:55], a[52:55], v[88:91], v[52:55]\n\tbuffer_load_dwordx4 %[vd0], s[40:43], %[xo] offen lds\n\tv_mfma_f32_16x16x32_bf16 v[48:51], a[56:59], v[92:95], v[48:51]\n\t" \
          "ds_read_b128 v[124:127], %[ra3] offset:768\n\tv_mfma_f32_16x16x32_bf16 v[52:55], a[60:63], v[92:95], v[52:55]\n\ts_waitcnt lgkmcnt(6)\n\tv_mfma_f32_16x16x32_bf16 v[48:51], a[64:67], v[96:99], v[48:51]\n\tds_read_b128 v[64:67], %[ra0] offset:1024\n\tv_mfma_f32_16x16x32_bf16 v[52:55], a[68:71], v[96:99], v[52:55]\n\t" \
          "buffer_load_dwordx4 %[vd0], s[40:43], %[xo] offen offset:1024 lds\n\tv_mfma_f32_16x16x32_bf16 v[48:51], a[72:75], v[100:103], v[48:51]\n\tds_read_b128 v[68:71], %[ra1] offset:1024\n\tv_mfma_f32_16x16x32_bf16 v[52:55], a[76:79], v[100:103], v[52:55]\n\ts_waitcnt lgkmcnt(6)\n\tv_mfma_f32_16x16x32_bf16 v[48:51], a[80:83], v[104:107], v[48:51]\n\t" \
          "ds_read_b128 v[72:75], %[ra2] offset:1024\n\tv_mfma_f32_16x16x32_bf16 v[52:55], a[84:87], v[104:107], v[52:55]\n\tbuffer_load_dwordx4 %[vd1], s[40:43], %[xo] offen offset:2048 lds\n\tv_mfma_f32_16x16x32_bf16 v[48:51], a[88:91], v[108:111], v[48:51]\n\tds_read_b128 v[76:79], %[ra3] offset:1024\n\tv_mfma_f32_16x16x32_bf16 v[52:55], a[92:95], v[108:111], v[52:55]\n\t" \
          "s_waitcnt lgkmcnt(6)\n\tv_mfma_f32_16x16x32_bf16 v[48:51], a[96:99], v[112:115], v[48:51]\n\tds_read_b128 v[80:83], %[ra0] offset:1280\n\tv_mfma_f32_16x16x32_bf16 v[52:55], a[100:103], v[112:115], v[52:55]\n\tbuffer_load_dwordx4 %[vd1], s[40:43], %[xo] offen offset:3072 lds\n\tv_mfma_f32_16x16x32_bf16 v[48:51], a[104:107], v[116:119], v[48:51]\n\t" \
          "ds_read_b128 v[84:87], %[ra1] offset:1280\n\tv_mfma_f32_16x16x32_bf16 v[52:55], a[108:111], v[116:119], v[52:55]\n\ts_waitcnt lgkmcnt(6)\n\tv_mfma_f32_16x16x32_bf16 v[48:51], a[112:115], v[120:123], v[48:51]\n\tds_read_b128 v[88:91], %[ra2] offset:1280\n\ts_add_u32 m0, %[sl], 4096\n\t" \
          "v_mfma_f32_16x16x32_bf16 v[52:55], a[116:119], v[120:123], v[52:55]\n\tbuffer_load_dwordx4 %[vd2], s[40:43], %[xo] offen lds\n\tv_mfma_f32_16x16x32_bf16 v[48:51], a[120:123], v[124:127], v[48:51]\n\tds_read_b128 v[92:95], %[ra3] offset:1280\n\tv_mfma_f32_16x16x32_bf16 v[52:55], a[124:127], v[124:127], v[52:55]\n\ts_waitcnt lgkmcnt(6)\n\t" \
          "v_mfma_f32_16x16x32_bf16 v[48:51], a[128:131], v[64:67], v[48:51]\n\tds_read_b128 v[96:99], %[ra0] offset:1536\n\tv_mfma_f32_16x16x32_bf16 v[52:55], a[132:135], v[64:67], v[52:55]\n\tbuffer_load_dwordx4 %[vd2], s[40:43], %[xo] offen offset:1024 lds\n\tv_mfma_f32_16x16x32_bf16 v[48:51], a[136:139], v[68:71], v[48:51]\n\tds_read_b128 v[100:103], %[ra1] offset:1536\n\t" \
          "v_mfma_f32_16x16x32_bf16 v[52:55], a[140:143], v[68:71], v[52:55]\n\ts_waitcnt lgkmcnt(6)\n\tv_mfma_f32_16x16x32_bf16 v[48:51], a[144:147], v[72:75], v[48:51]\n\tds_read_b128 v[104:107], %[ra2] offset:1536\n\tv_mfma_f32_16x16x32_bf16 v[52:55], a[148:151], v[72:75], v[52:55]\n\tbuffer_load_dwordx4 %[vd3], s[40:43], %[xo] offen offset:2048 lds\n\t" \
          "v_mfma_f32_16x16x32_bf16 v[48:51], a[152:155], v[76:79], v[48:51]\n\tds_read_b128 v[108:111], %[ra3] offset:1536\n\tv_mfma_f32_16x16x32_bf16 v[52:55], a[156:159], v[76:79], v[52:55]\n\ts_waitcnt lgkmcnt(6)\n\tv_mfma_f32_16x16x32_bf16 v[48:51], a[160:163], v[80:83], v[48:51]\n\tds_read_b128 v[112:115], %[ra0] offset:1792\n\t" \
          "v_mfma_f32_16x16x32_bf16 v[52:55], a[164:167], v[80:83], v[52:55]\n\tbuffer_load_dwordx4 %[vd3], s[40:43], %[xo] offen offset:3072 lds\n\tv_mfma_f32_16x16x32_bf16 v[48:51], a[168:171], v[84:87], v[48:51]\n\tds_read_b128 v[116:119], %[ra1] offset:1792\n\tv_mfma_f32_16x16x32_bf16 v[52:55], a[172:175], v[84:87], v[52:55]\n\ts_waitcnt lgkmcnt(6)\n\t" \
          "v_mfma_f32_16x16x32_bf16 v[48:51], a[176:179], v[88:91], v[48:51]\n\tds_read_b128 v[120:123], %[ra2] offset:1792\n\tv_mfma_f32_16x16x32_bf16 v[52:55], a[180:183], v[88:91], v[52:55]\n\tv_mfma_f32_16x16x32_bf16 v[48:51], a[184:187], v[92:95], v[48:51]\n\tds_read_b128 v[124:127], %[ra3] offset:1792\n\tv_mfma_f32_16x16x32_bf16 v[52:55], a[188:191], v[92:95], v[52:55]\n\t" \
          "s_add_u32 %[xo], %[xo], 0x8000\n\ts_add_u32 %[sl], %[sl], 0x8000\n\ts_bitset0_b32 %[sl], 17\n\ts_waitcnt lgkmcnt(6)\n\tv_mfma_f32_16x16x32_bf16 v[48:51], a[192:195], v[96:99], v[48:51]\n\tv_mfma_f32_16x16x32_bf16 v[52:55], a[196:199], v[96:99], v[52:55]\n\t" \
          "v_mfma_f32_16x16x32_bf16 v[48:51], a[200:203], v[100:103], v[48:51]\n\tv_mfma_f32_16x16x32_bf16 v[52:55], a[204:207], v[100:103], v[52:55]\n\ts_waitcnt lgkmcnt(4)\n\tv_mfma_f32_16x16x32_bf16 v[48:51], a[208:211], v[104:107], v[48:51]\n\tv_mfma_f32_16x16x32_bf16 v[52:55], a[212:215], v[104:107], v[52:55]\n\tv_mfma_f32_16x16x32_bf16 v[48:51], a[216:219], v[108:111], v[48:51]\n\t" \
          "v_mfma_f32_16x16x32_bf16 v[52:55], a[220:223], v[108:111], v[52:55]\n\ts_waitcnt vmcnt(18) lgkmcnt(0)\n\ts_barrier\n\tv_mfma_f32_16x16x32_bf16 v[48:51], a[224:227], v[112:115], v[48:51]\n\tds_read_b128 v[64:67], %[ra0] offset:32768\n\tv_mfma_f32_16x16x32_bf16 v[52:55], a[228:231], v[112:115], v[52:55]\n\t" \
          "ds_read_b128 v[68:71], %[ra1] offset:32768\n\tv_mfma_f32_16x16x32_bf16 v[48:51], a[232:235], v[116:119], v[48:51]\n\tds_read_b128 v[72:75], %[ra2] offset:32768\n\tv_mfma_f32_16x16x32_bf16 v[52:55], a[236:239], v[116:119], v[52:55]\n\tds_read_b128 v[76:79], %[ra3] offset:32768\n\tv_mfma_f32_16x16x32_bf16 v[48:51], a[240:243], v[120:123], v[48:51]\n\t" \
          "ds_read_b128 v[80:83], %[ra0] offset:33024\n\tv_mfma_f32_16x16x32_bf16 v[52:55], a[244:247], v[120:123], v[52:55]\n\tds_read_b128 v[84:87], %[ra1] offset:33024\n\tv_mfma_f32_16x16x32_bf16 v[48:51], a[248:251], v[124:127], v[48:51]\n\tds_read_b128 v[88:91], %[ra2] offset:33024\n\tv_mfma_f32_16x16x32_bf16 v[52:55], a[252:255], v[124:127], v[52:55]\n\t" \
          "ds_read_b128 v[92:95], %[ra3] offset:33024\n\ts_waitcnt lgkmcnt(6)\n\tv_mfma_f32_16x16x32_bf16 v[56:59], a[0:3], v[64:67], 0\n\tds_read_b128 v[96:99], %[ra0] offset:33280\n\tv_mfma_f32_16x16x32_bf16 v[60:63], a[4:7], v[64:67], 0\n\ts_add_u32 %[t], %[j], 4\n\t" \
          "s_cmp_lt_u32 %[t], %[je]\n\ts_cselect_b32 s42, s42, 0\n\tv_mfma_f32_16x16x32_bf16 v[56:59], a[8:11], v[68:71], v[56:59]\n\tds_read_b128 v[100:103], %[ra1] offset:33280\n\tv_mfma_f32_16x16x32_bf16 v[60:63], a[12:15], v[68:71], v[60:63]\n\tv_cvt_pk_bf16_f32 v44, v48, v49\n\t" \
          "v_cvt_pk_bf16_f32 v45, v50, v51\n\ts_waitcnt lgkmcnt(6)\n\tv_mfma_f32_16x16x32_bf16 v[56:59], a[16:19], v[72:75], v[56:59]\n\tds_read_b128 v[104:107], %[ra2] offset:33280\n\tv_mfma_f32_16x16x32_bf16 v[60:63], a[20:23], v[72:75], v[60:63]\n\tv_cvt_pk_bf16_f32 v46, v52, v53\n\t" \
          "v_cvt_pk_bf16_f32 v47, v54, v55\n\tv_mfma_f32_16x16x32_bf16 v[56:59], a[24:27], v[76:79], v[56:59]\n\tds_read_b128 v[108:111], %[ra3] offset:33280\n\tv_mfma_f32_16x16x32_bf16 v[60:63], a[28:31], v[76:79], v[60:63]\n\tbuffer_store_dwordx4 v[44:47], %[so], %[rc], %[co] offen sc1\n\ts_waitcnt lgkmcnt(6)\n\t" \
          "v_mfma_f32_16x16x32_bf16 v[56:59], a[32:35], v[80:83], v[56:59]\n\tds_read_b128 v[112:115], %[ra0] offset:33536\n\tv_mfma_f32_16x16x32_bf16 v[60:63], a[36:39], v[80:83], v[60:63]\n\tv_mov_b32 %[fv], %[j]\n\tv_mfma_f32_16x16x32_bf16 v[56:59], a[40:43], v[84:87], v[56:59]\n\tds_read_b128 v[116:119], %[ra1] offset:33536\n\t" \
          "v_mfma_f32_16x16x32_bf16 v[60:63], a[44:47], v[84:87], v[60:63]\n\ts_add_u32 %[co], %[co], 0x4000\n\ts_waitcnt lgkmcnt(6)\n\tv_mfma_f32_16x16x32_bf16 v[56:59], a[48:51], v[88:91], v[56:59]\n\tds_read_b128 v[120:123], %[ra2] offset:33536\n\ts_add_u32 m0, %[sl], 0\n\t" \
          "v_mfma_f32_16x16x32_bf16 v[60:63], a[52:55], v[88:91], v[60:63]\n\tbuffer_load_dwordx4 %[vd0], s[40:43], %[xo] offen lds\n\tv_mfma_f32_16x16x32_bf16 v[56:59], a[56:59], v[92:95], v[56:59]\n\tds_read_b128 v[124:127], %[ra3] offset:33536\n\tv_mfma_f32_16x16x32_bf16 v[60:63], a[60:63], v[92:95], v[60:63]\n\ts_waitcnt lgkmcnt(6)\n\t" \
          "v_mfma_f32_16x16x32_bf16 v[56:59], a[64:67], v[96:99], v[56:59]\n\tds_read_b128 v[64:67], %[ra0] offset:33792\n\tv_mfma_f32_16x16x32_bf16 v[60:63], a[68:71], v[96:99], v[60:63]\n\tbuffer_load_dwordx4 %[vd0], s[40:43], %[xo] offen offset:1024 lds\n\tv_mfma_f32_16x16x32_bf16 v[56:59], a[72:75], v[100:103], v[56:59]\n\tds_read_b128 v[68:71], %[ra1] offset:33792\n\t" \
          "v_mfma_f32_16x16x32_bf16 v[60:63], a[76:79], v[100:103], v[60:63]\n\ts_waitcnt lgkmcnt(6)\n\tv_mfma_f32_16x16x32_bf16 v[56:59], a[80:83], v[104:107], v[56:59]\n\tds_read_b128 v[72:75], %[ra2] offset:33792\n\tv_mfma_f32_16x16x32_bf16 v[60:63], a[84:87], v[104:107], v[60:63]\n\tbuffer_load_dwordx4 %[vd1], s[40:43], %[xo] offen offset:2048 lds\n\t" \
          "v_mfma_f32_16x16x32_bf16 v[56:59], a[88:91], v[108:111], v[56:59]\n\tds_read_b128 v[76:79], %[ra3] offset:33792\n\tv_mfma_f32_16x16x32_bf16 v[60:63], a[92:95], v[108:111], v[60:63]\n\ts_waitcnt lgkmcnt(6)\n\tv_mfma_f32_16x16x32_bf16 v[56:59], a[96:99], v[112:115], v[56:59]\n\tds_read_b128 v[80:83], %[ra0] offset:34048\n\t" \
          "v_mfma_f32_16x16x32_bf16 v[60:63], a[100:103], v[112:115], v[60:63]\n\tbuffer_load_dwordx4 %[vd1], s[40:43], %[xo] offen offset:3072 lds\n\tv_mfma_f32_16x16x32_bf16 v[56:59], a[104:107], v[116:119], v[56:59]\n\tds_read_b128 v[84:87], %[ra1] offset:34048\n\tv_mfma_f32_16x16x32_bf16 v[60:63], a[108:111], v[116:119], v[60:63]\n\ts_waitcnt lgkmcnt(6)\n\t" \
          "v_mfma_f32_16x16x32_bf16 v[56:59], a[112:115], v[120:123], v[56:59]\n\tds_read_b128 v[88:91], %[ra2] offset:34048\n\ts_add_u32 m0, %[sl], 4096\n\tv_mfma_f32_16x16x32_bf16 v[60:63], a[116:119], v[120:123], v[60:63]\n\tbuffer_load_dwordx4 %[vd2], s[40:43], %[xo] offen lds\n\tv_mfma_f32_16x16x32_bf16 v[56:59], a[120:123], v[124:127], v[56:59]\n\t" \
          "ds_read_b128 v[92:95], %[ra3] offset:34048\n\tv_mfma_f32_16x16x32_bf16 v[60:63], a[124:127], v[124:127], v[60:63]\n\ts_waitcnt lgkmcnt(6)\n\tv_mfma_f32_16x16x32_bf16 v[56:59], a[128:131], v[64:67], v[56:59]\n\tds_read_b128 v[96:99], %[ra0] offset:34304\n\tv_mfma_f32_16x16x32_bf16 v[60:63], a[132:135], v[64:67], v[60:63]\n\t" \
          "buffer_load_dwordx4 %[vd2], s[40:43], %[xo] offen offset:1024 lds\n\tv_mfma_f32_16x16x32_bf16 v[56:59], a[136:139], v[68:71], v[56:59]\n\tds_read_b128 v[100:103], %[ra1] offset:34304\n\tv_mfma_f32_16x16x32_bf16 v[60:63], a[140:143], v[68:71], v[60:63]\n\ts_waitcnt lgkmcnt(6)\n\tv_mfma_f32_16x16x32_bf16 v[56:59], a[144:147], v[72:75], v[56:59]\n\t" \
          "ds_read_b128 v[104:107], %[ra2] offset:34304\n\tv_mfma_f32_16x16x32_bf16 v[60:63], a[148:151], v[72:75], v[60:63]\n\tbuffer_load_dwordx4 %[vd3], s[40:43], %[xo] offen offset:2048 lds\n\tv_mfma_f32_16x16x32_bf16 v[56:59], a[152:155], v[76:79], v[56:59]\n\tds_read_b128 v[108:111], %[ra3] offset:34304\n\tv_mfma_f32_16x16x32_bf16 v[60:63], a[156:159], v[76:79], v[60:63]\n\t" \
          "s_waitcnt lgkmcnt(6)\n\tv_mfma_f32_16x16x32_bf16 v[56:59], a[160:163], v[80:83], v[56:59]\n\tds_read_b128 v[112:115], %[ra0] offset:34560\n\tv_mfma_f32_16x16x32_bf16 v[60:63], a[164:167], v[80:83], v[60:63]\n\tbuffer_load_dwordx4 %[vd3], s[40:43], %[xo] offen offset:3072 lds\n\tv_mfma_f32_16x16x32_bf16 v[56:59], a[168:171], v[84:87], v[56:59]\n\t" \
          "ds_read_b128 v[116:119], %[ra1] offset:34560\n\tv_mfma_f32_16x16x32_bf16 v[60:63], a[172:175], v[84:87], v[60:63]\n\ts_waitcnt lgkmcnt(6)\n\tv_mfma_f32_16x16x32_bf16 v[56:59], a[176:179], v[88:91], v[56:59]\n\tds_read_b128 v[120:123], %[ra2] offset:34560\n\tv_mfma_f32_16x16x32_bf16 v[60:63], a[180:183], v[88:91], v[60:63]\n\t" \
          "v_mfma_f32_16x16x32_bf16 v[56:59], a[184:187], v[92:95], v[56:59]\n\tds_read_b128 v[124:127], %[ra3] offset:34560\n\tv_mfma_f32_16x16x32_bf16 v[60:63], a[188:191], v[92:95], v[60:63]\n\ts_add_u32 %[xo], %[xo], 0x8000\n\ts_add_u32 %[sl], %[sl], 0x8000\n\ts_bitset0_b32 %[sl], 17\n\t" \
          "s_waitcnt lgkmcnt(6)\n\tv_mfma_f32_16x16x32_bf16 v[56:59], a[192:195], v[96:99], v[56:59]\n\tv_mfma_f32_16x16x32_bf16 v[60:63], a[196:199], v[96:99], v[60:63]\n\tv_xor_b32 %[ra0], 0x10000, %[ra0]\n\tv_mfma_f32_16x16x32_bf16 v[56:59], a[200:203], v[100:103], v[56:59]\n\tv_mfma_f32_16x16x32_bf16 v[60:63], a[204:207], v[100:103], v[60:63]\n\t" \
          "v_xor_b32 %[ra1], 0x10000, %[ra1]\n\ts_waitcnt lgkmcnt(4)\n\tv_mfma_f32_16x16x32_bf16 v[56:59], a[208:211], v[104:107], v[56:59]\n\tv_mfma_f32_16x16x32_bf16 v[60:63], a[212:215], v[104:107], v[60:63]\n\tv_xor_b32 %[ra2], 0x10000, %[ra2]\n\tv_mfma_f32_16x16x32_bf16 v[56:59], a[216:219], v[108:111], v[56:59]\n\t" \
          "s_add_u32 %[j], %[j], 2\n\ts_cmp_lt_u32 %[j], %[je]\n\tv_mfma_f32_16x16x32_bf16 v[60:63], a[220:223], v[108:111], v[60:63]\n\tv_xor_b32 %[ra3], 0x10000, %[ra3]\n\ts_waitcnt vmcnt(17) lgkmcnt(0)\n\ts_barrier\n\t" \
          "v_mfma_f32_16x16x32_bf16 v[56:59], a[224:227], v[112:115], v[56:59]\n\tds_read_b128 v[64:67], %[ra0] offset:0\n\tv_mfma_f32_16x16x32_bf16 v[60:63], a[228:231], v[112:115], v[60:63]\n\tds_read_b128 v[68:71], %[ra1] offset:0\n\tv_mfma_f32_16x16x32_bf16 v[56:59], a[232:235], v[116:119], v[56:59]\n\tds_read_b128 v[72:75], %[ra2] offset:0\n\t" \
          "v_mfma_f32_16x16x32_bf16 v[60:63], a[236:239], v[116:119], v[60:63]\n\tds_read_b128 v[76:79], %[ra3] offset:0\n\tv_mfma_f32_16x16x32_bf16 v[56:59], a[240:243], v[120:123], v[56:59]\n\tds_read_b128 v[80:83], %[ra0] offset:256\n\tv_mfma_f32_16x16x32_bf16 v[60:63], a[244:247], v[120:123], v[60:63]\n\tds_read_b128 v[84:87], %[ra1] offset:256\n\t" \
          "v_mfma_f32_16x16x32_bf16 v[56:59], a[248:251], v[124:127], v[56:59]\n\tds_read_b128 v[88:91], %[ra2] offset:256\n\tv_mfma_f32_16x16x32_bf16 v[60:63], a[252:255], v[124:127], v[60:63]\n\tds_read_b128 v[92:95], %[ra3] offset:256\n\ts_cbranch_scc1 Lpn_loop%=\n\ts_nop 7\n\t" \
          "v_mov_b32 %[fv], %[je]\n\tv_cvt_pk_bf16_f32 v44, v56, v57\n\tv_cvt_pk_bf16_f32 v45, v58, v59\n\tv_cvt_pk_bf16_f32 v46, v60, v61\n\tv_cvt_pk_bf16_f32 v47, v62, v63\n\tbuffer_store_dwordx4 v[44:47], %[so], %[rc], %[co] offen sc1\n\t" \
          "s_waitcnt vmcnt(0) lgkmcnt(0)\n\ts_barrier\n\ts_mov_b64 exec, %[fx]\n\tbuffer_store_dword %[fv], off, %[rf], %[fo] sc1\n\ts_mov_b64 exec, -1\n\t"
#define PN_CLOB "v44", "v45", "v46", "v47", "v48", "v49", "v50", "v51", "v52", "v53", "v54", "v55", "v56", "v57", "v58", "v59", "v60", "v61", "v62", "v63", "v64", "v65", "v66", "v67", "v68", "v69", "v70", "v71", "v72", "v73", "v74", "v75", "v76", "v77", "v78", "v79", "v80", "v81", "v82", "v83", "v84", "v85", "v86", "v87", "v88", "v89", "v90", "v91", "v92", "v93", "v94", "v95", "v96", "v97", "v98", "v99", "v100", "v101", "v102", "v103", "v104", "v105", "v106", "v107", "v108", "v109", "v110", "v111", "v112", "v113", "v114", "v115", "v116", "v117", "v118", "v119", "v120", "v121", "v122", "v123", "v124", "v125", "v126", "v127", "a0", "a1", "a2", "a3", "a4", "a5", "a6", "a7", "a8", "a9", "a10", "a11", "a12", "a13", "a14", "a15", "a16", "a17", "a18", "a19", "a20", "a21", "a22", "a23", "a24", "a25", "a26", "a27", "a28", "a29", "a30", "a31", "a32", "a33", "a34", "a35", "a36", "a37", "a38", "a39", "a40", "a41", "a42", "a43", "a44", "a45", "a46", "a47", "a48", "a49", "a50", "a51", "a52", "a53", "a54", "a55", "a56", "a57", "a58", "a59", "a60", "a61", "a62", "a63", "a64", "a65", "a66", "a67", "a68", "a69", "a70", "a71", "a72", "a73", "a74", "a75", "a76", "a77", "a78", "a79", "a80", "a81", "a82", "a83", "a84", "a85", "a86", "a87", "a88", "a89", "a90", "a91", "a92", "a93", "a94", "a95", "a96", "a97", "a98", "a99", "a100", "a101", "a102", "a103", "a104", "a105", "a106", "a107", "a108", "a109", "a110", "a111", "a112", "a113", "a114", "a115", "a116", "a117", "a118", "a119", "a120", "a121", "a122", "a123", "a124", "a125", "a126", "a127", "a128", "a129", "a130", "a131", "a132", "a133", "a134", "a135", "a136", "a137", "a138", "a139", "a140", "a141", "a142", "a143", "a144", "a145", "a146", "a147", "a148", "a149", "a150", "a151", "a152", "a153", "a154", "a155", "a156", "a157", "a158", "a159", "a160", "a161", "a162", "a163", "a164", "a165", "a166", "a167", "a168", "a169", "a170", "a171", "a172", "a173", "a174", "a175", "a176", "a177", "a178", "a179", "a180", "a181", "a182", "a183", "a184", "a185", "a186", "a187", "a188", "a189", "a190", "a191", "a192", "a193", "a194", "a195", "a196", "a197", "a198", "a199", "a200", "a201", "a202", "a203", "a204", "a205", "a206", "a207", "a208", "a209", "a210", "a211", "a212", "a213", "a214", "a215", "a216", "a217", "a218", "a219", "a220", "a221", "a222", "a223", "a224", "a225", "a226", "a227", "a228", "a229", "a230", "a231", "a232", "a233", "a234", "a235", "a236", "a237", "a238", "a239", "a240", "a241", "a242", "a243", "a244", "a245", "a246", "a247", "a248", "a249", "a250", "a251", "a252", "a253", "a254", "a255", "s40", "s41", "s42", "s43", "s44", "s45"
#define NTP 0
template <bool NT>
__device__ __forceinline__ v4u panel_task(const FParams& F, int p, int fi, int j, int je, int w) {
  const int lane = opaque_lane(), l15 = lane & 15, lq = lane >> 4, m = p >> 5, ldb = __builtin_amdgcn_readfirstlane(m < 3 ? HDA * 2 : NQKVB * 2);
  const __amdgpu_buffer_rsrc_t rw = __builtin_amdgcn_make_buffer_rsrc(wg::uni((m == 0 ? F.Wq : (m == 1 ? F.Wk : (m == 2 ? F.Wv : F.wqkvB))) + (p & 31) * 128), 0, DM * ldb - (p & 31) * 256, 0x00020000);
  const __amdgpu_buffer_rsrc_t rf = __builtin_amdgcn_make_buffer_rsrc(F.ctl, 0, sizeof(Ctl), 0x00020000), rr = __builtin_amdgcn_make_buffer_rsrc(F.rdy, 0, RDY_SIZE, 0x00020000);
  void* const xa = wg::uni(m < 3 ? F.Ax : F.Bx);
  int ra[4], vd[4], rt[4], vw[2], fv = 0, xo = w * 8192 + j * 32768, sl = w * 8192, co = j * QBLK, cs = 0, t; unsigned long long sx;
  v4u rec;
#pragma unroll
  for (int i = 0; i < 4; ++i) {
    ra[i] = l15 * 2048 + (((4 * i + lq) ^ l15) << 4); vd[i] = ((lane ^ (4 * w + i)) << 4) + (i >> 1) * 4096;
    rt[i] = 65536 + (i >> 1) * 49152 + (8 * lq + (l15 >> 2)) * 256 + (((2 * w + (i & 1)) ^ ((l15 >> 2) | ((lq & 1) << 2))) << 5) + (l15 & 3) * 8;
  }
  vw[0] = (16 * w + lq) * ldb + ((l15 ^ (lq << 1)) << 4); vw[1] = vw[0] ^ 128;
  const __amdgpu_buffer_rsrc_t rc = __builtin_amdgcn_make_buffer_rsrc((char*)F.qkvA + __builtin_amdgcn_readfirstlane((p >> 1) * QPAGE + (p & 1) * 4096 + w * 1024), 0, QPAGE - 8192 + 1024, 0x00020000);
#define PN_OPS : "=&{v[40:43]}"(rec), [ra0] "+v"(ra[0]), [ra1] "+v"(ra[1]), [ra2] "+v"(ra[2]), [ra3] "+v"(ra[3]), [fv] "+v"(fv), [xo] "+s"(xo), [sl] "+s"(sl), [co] "+s"(co), [cs] "+s"(cs), [t] "=&s"(t), [j] "+s"(j), [sx] "=&s"(sx) \
    : [vd0] "v"(vd[0]), [vd1] "v"(vd[1]), [vd2] "v"(vd[2]), [vd3] "v"(vd[3]), [rt0] "v"(rt[0]), [rt1] "v"(rt[1]), [rt2] "v"(rt[2]), [rt3] "v"(rt[3]), [vw0] "v"(vw[0]), [vw1] "v"(vw[1]), [ro] "v"(lane * 128), [so] "v"(lane * 16), \
      [xa] "s"(xa), [rw] "s"(rw), [rr] "s"(rr), [rc] "s"(rc), [rf] "s"(rf), [wl] "s"(w * 4096), [ldb] "s"(ldb), [fo] "s"((int)offsetof(Ctl, prg) + fi * 128), [je] "s"(je), \
      [fx] "s"((unsigned long long)(unsigned)__builtin_amdgcn_readfirstlane(w == 0)), [pl] "s"(p >> 1) : PN_CLOB, "vcc", "scc", "memory"
  if constexpr (NT) asm volatile(PN_BODY(" nt") PN_OPS); else asm volatile(PN_BODY("") PN_OPS);
  return rec;
}

struct AttnX { v4u q[2][4], kx[2][4], vr[8]; };
template <int AUX = 0>
__device__ __forceinline__ void attnA_load_x(const FParams& F, unsigned long long parq, int seg, int h, int lane, AttnX& X) {
  const __amdgpu_buffer_rsrc_t rs = __builtin_amdgcn_make_buffer_rsrc(F.qkvA, 0, QSIZE, 0x00020000);
  const int so = seg * 2 * QBLK + (h & 1) * 4096, oq = so + qoff(parq, h >> 1), ok = so + qoff(parq, 16 + (h >> 1)), ov = so + qoff(parq, 32 + (h >> 1));
#pragma unroll
  for (int i = 0; i < 8; ++i) X.vr[i] = __builtin_amdgcn_raw_buffer_load_b128(rs, lane * 16 + (i & 3) * 1024, ov + (i >> 2) * QBLK, AUX);
#pragma unroll
  for (int t = 0; t < 2; ++t)
#pragma unroll
    for (int j = 0; j < 4; ++j) {
      X.q[t][j] = __builtin_amdgcn_raw_buffer_load_b128(rs, lane * 16 + j * 1024, oq + t * QBLK, AUX);
      X.kx[t][j] = __builtin_amdgcn_raw_buffer_load_b128(rs, lane * 16 + j * 1024, ok + t * QBLK, AUX);
    }
}
__device__ __forceinline__ void attnA_stage_v(const AttnX& X, u16* vs  , int lane) {
#pragma unroll
  for (int i = 0; i < 8; ++i) *(v4u*)&vs[(16 * (i >> 2) + (lane & 15)) * VXLD + 32 * (i & 3) + 8 * (lane >> 4)] = X.vr[i];
}
__device__ __forceinline__ unsigned attnA_load_m(const FParams& F, int seg, int h, int lane, v4u (&km)[4], unsigned (&vm)[8]) {
  const int l15 = lane & 15, lq = lane >> 4;
  const __amdgpu_buffer_rsrc_t rk = __builtin_amdgcn_make_buffer_rsrc(F.kvm + (size_t)seg * KVSEG + h * 4096, 0, 4096, 0x00020000);
#pragma unroll
  for (int j = 0; j < 4; ++j)
    km[j] = __builtin_amdgcn_raw_buffer_load_b128(rk, (2 * (lane >> 2) + (lane & 1)) * 16 + 512 * j, 0, 16);
#pragma unroll
  for (int t = 0; t < 8; ++t)
    vm[t] = __builtin_amdgcn_raw_buffer_load_b32(rk, 2048 + (((l15 >> 3) * 4 + lq) * 16 + 8 * ((l15 >> 2) & 1) + (l15 & 3)) * 4 + 512 * (t >> 1) + 16 * (t & 1), 0, 16);
  unsigned m = 0;
#pragma unroll
  for (int j = 0; j < 4; ++j) m = chk4(m, km[j]);
#pragma unroll
  for (int t = 0; t < 8; t += 2) m = umax3(m, vm[t], vm[t + 1]);
  return m;
}
__device__ __forceinline__ void attnA_compute(const AttnX& X, const v4u (&km)[4], const unsigned (&vm)[8], const u16* vs, int lane, v4u (&ov)[2][4]) {
  const int l15 = lane & 15, lq = lane >> 4;
  const float scale = 0.08838834764831845f, L2E = 1.4426950408889634f;
  v4u px[2], pm[2];
#pragma unroll
  for (int t = 0; t < 2; ++t) {
    f32x4 s0 = {0.f,0.f,0.f,0.f}, s1 = s0, s2 = s0;
#pragma unroll
    for (int j = 0; j < 4; ++j) { s0 = mf(km[j], X.q[t][j], s0); s1 = mf(X.kx[0][j], X.q[t][j], s1); if (t == 1) s2 = mf(X.kx[1][j], X.q[t][j], s2); }
    float sv[12];
#pragma unroll
    for (int e = 0; e < 4; ++e) { sv[e] = s0[e]; sv[4 + e] = s1[e]; sv[8 + e] = s2[e]; }
#pragma unroll
    for (int e = 0; e < 12; e += 2) {
      unsigned p = pk2(sv[e], sv[e + 1]);
      p = pk2(__uint_as_float(p << 16) * scale, __uint_as_float(p & 0xffff0000u) * scale);
      sv[e] = __uint_as_float(p << 16); sv[e + 1] = __uint_as_float(p & 0xffff0000u);
    }
    bool ok[12];
#pragma unroll
    for (int e = 0; e < 4; ++e) { ok[e] = e < 2; ok[4 + e] = (t == 1) || (4 * lq + e <= l15); ok[8 + e] = (t == 1) && (4 * lq + e <= l15); }
    float mx = -INFINITY;
#pragma unroll
    for (int e = 0; e < 12; ++e) mx = ok[e] ? fmaxf(mx, sv[e]) : mx;
    mx = xmax32(xmax16(mx));
    float sum = 0.f;
#pragma unroll
    for (int e = 0; e < 12; ++e) { sv[e] = ok[e] ? __builtin_amdgcn_exp2f((sv[e] - mx) * L2E) : 0.f; sum += sv[e]; }
    sum = xsum32(xsum16(sum));
    const float inv = __builtin_amdgcn_rcpf(sum);
#pragma unroll
    for (int e = 0; e < 12; ++e) sv[e] = ok[e] ? sv[e] * inv : 0.f;
    px[t] = (v4u){pk2(sv[4], sv[5]), pk2(sv[6], sv[7]), pk2(sv[8], sv[9]), pk2(sv[10], sv[11])};
    pm[t] = (v4u){pk2(sv[0], sv[1]), 0u, 0u, 0u};
  }
  const u16* vp = &vs[(4 * lq + (l15 >> 2)) * VXLD + 16 * (l15 & 1) + 2 * (l15 & 2)];
#pragma unroll
  for (int u = 0; u < 4; ++u) {
    f32x4 o[2][2];
#pragma unroll
    for (int ab = 0; ab < 2; ++ab) {
      v2u lo = trrd(vp + 32 * u + 8 * ab), hi = trrd(vp + 16 * VXLD + 32 * u + 8 * ab);
      const v4u va = {lo.x, lo.y, hi.x, hi.y}, vb = {vm[2 * u + ab], 0u, 0u, 0u};
#pragma unroll
      for (int t = 0; t < 2; ++t) { o[t][ab] = mf(va, px[t], (f32x4){0.f,0.f,0.f,0.f}); o[t][ab] = mf(vb, pm[t], o[t][ab]); }
    }
#pragma unroll
    for (int t = 0; t < 2; ++t)
      ov[t][u] = (v4u){pk2(o[t][0][0], o[t][0][1]), pk2(o[t][0][2], o[t][0][3]), pk2(o[t][1][0], o[t][1][1]), pk2(o[t][1][2], o[t][1][3])};
  }
}
__device__ __forceinline__ void attnA_block(const FParams& F, unsigned long long parq, int r, int h, u16* vs, int lane, int w, unsigned* shw) {
  AttnX X[2]; v4u km[2][4]; unsigned vm[2][8];
  unsigned seen = 0;
  attnA_load_x(F, parq, 4 * r, h, lane, X[0]);
  bool have = false;
#pragma unroll
  for (int i = 0; i < 4; ++i) {
    const int seg = 4 * r + i, c = i & 1;
    v4u ov[2][4];
    if (!have) {
      if (seen < (unsigned)seg + 1) {
        if (w == 0) {
          for (unsigned spins = 0;; ++spins) {
            seen = __builtin_amdgcn_readfirstlane(ld_agent(&F.ctl->prog[0]));
            if (seen >= (unsigned)seg + 1 || spin_fail(spins, &F.ctl->tmo[0], 6u)) break;
            for (unsigned z = seen; z < (unsigned)seg + 1; ++z) __builtin_amdgcn_s_sleep(PSL);
          }
          *shw = seen;
        }
        __syncthreads();
        seen = __builtin_amdgcn_readfirstlane(*shw);
        __syncthreads();
      }
      asm volatile("" ::: "memory");
      (void)attnA_load_m(F, seg, h, lane, km[c], vm[c]);
    }
    if (i < 3) {
      attnA_load_x(F, parq, seg + 1, h, lane, X[c ^ 1]);
      have = seen >= (unsigned)seg + 2;
      (void)attnA_load_m(F, have ? seg + 1 : seg, h, lane, km[c ^ 1], vm[c ^ 1]);
    }
    SB0;
    attnA_stage_v(X[c], vs, lane);
    attnA_compute(X[c], km[c], vm[c], vs, lane, ov);
    const __amdgpu_buffer_rsrc_t rs = __builtin_amdgcn_make_buffer_rsrc(F.qkvA, 0, QSIZE, 0x00020000);
#pragma unroll
    for (int u = 0; u < 4; ++u)
#pragma unroll
      for (int t = 0; t < 2; ++t)
        __builtin_amdgcn_raw_buffer_store_b128(ov[t][u], rs, lane * 16 + u * 1024, qoff(parq, h >> 1) + (seg * 2 + t) * QBLK + (h & 1) * 4096, 16);
    SB0;
  }
}

constexpr int TSEG0 = 32, OSEG0 = 16, NSET = 4;
__device__ __forceinline__ void attn_stream(const FParams& F, unsigned long long parq, int set, int hg, char* lds, int w) {
  const int lane = opaque_lane(), h = 4 * hg + w;
  Ctl* ctl = F.ctl;
  for (unsigned spins = 0;; ++spins) {
    if (__all(ld_agent(&ctl->prg[(lane < 2 ? lane : 3) * 32 + h][0]) >= 128u) || spin_fail(spins, &ctl->tmo[0], 2u)) break;
    __builtin_amdgcn_s_sleep(16);
  }
  asm volatile("" ::: "memory");
  u16* vs = (u16*)(lds + w * 16384);
  AttnX X; int seen = 0;
  attnA_load_x<16>(F, parq, TSEG0 + set, h, lane, X);
#pragma unroll 1
  for (int seg = TSEG0 + set; seg < NSEG; seg += NSET) {
    v4u km[4], ov[2][4]; unsigned vm[8];
    attnA_stage_v(X, vs, lane);
    for (unsigned spins = 0; seen <= (seg < SSEG0 ? seg : seg - 3); ++spins) {
      seen = __builtin_amdgcn_readfirstlane(ld_agent(&ctl->prog[0]));
      if (seen > (seg < SSEG0 ? seg : seg - 3) || spin_fail(spins, &ctl->tmo[0], 6u)) break;
      __builtin_amdgcn_s_sleep(PSL);
    }
    asm volatile("" ::: "memory");
    for (unsigned spins = 0;; ++spins) {
      if (__all(attnA_load_m(F, seg, h, lane, km, vm) != 0xffffffffu) || spin_fail(spins, &ctl->tmo[0], 4u)) break;
      __builtin_amdgcn_s_sleep(TSL);
    }
    attnA_compute(X, km, vm, vs, lane, ov);
    const __amdgpu_buffer_rsrc_t rs = __builtin_amdgcn_make_buffer_rsrc(F.qkvA, 0, QSIZE, 0x00020000);
#pragma unroll
    for (int u = 0; u < 4; ++u)
#pragma unroll
      for (int t = 0; t < 2; ++t)
        __builtin_amdgcn_raw_buffer_store_b128(ov[t][u], rs, lane * 16 + u * 1024, qoff(parq, h >> 1) + (seg * 2 + t) * QBLK + (h & 1) * 4096, 16);
    SB0;
    attnA_load_x<16>(F, parq, seg + NSET < NSEG ? seg + NSET : TSEG0 - 1, h, lane, X);
    SB0;
    asm volatile("s_waitcnt vmcnt(24)\n\ts_barrier" ::: "memory");
    if (w == 0 && lane == 0) __hip_atomic_fetch_add(&ctl->cntO[seg][0], 1u, RLX_AGENT);
  }
}
#define OU_BODY \
          "s_mov_b64 s[40:41], %[qa]\n\ts_mov_b32 s42, %[qs]\n\ts_mov_b32 s43, 0x20000\n\ts_mov_b64 s[44:45], %[qa]\n\ts_mov_b32 s47, 0x20000\n\ts_nop 4\n\t" \
          "s_add_u32 m0, %[wl], 0\n\ts_xor_b32 %[t], %[wo], 0x0\n\tbuffer_load_dwordx4 %[vw0], %[rw], %[t] offen lds\n\ts_add_u32 m0, %[wl], 1024\n\ts_xor_b32 %[t], %[wo], 0x4000\n\tbuffer_load_dwordx4 %[vw1], %[rw], %[t] offen lds\n\t" \
          "s_add_u32 m0, %[wl], 2048\n\ts_xor_b32 %[t], %[wo], 0x8000\n\tbuffer_load_dwordx4 %[vw0], %[rw], %[t] offen lds\n\ts_add_u32 m0, %[wl], 3072\n\ts_xor_b32 %[t], %[wo], 0xc000\n\tbuffer_load_dwordx4 %[vw1], %[rw], %[t] offen lds\n\t" \
          "s_add_u32 m0, %[wl], 4096\n\ts_xor_b32 %[t], %[wo], 0x10000\n\tbuffer_load_dwordx4 %[vw0], %[rw], %[t] offen lds\n\ts_add_u32 m0, %[wl], 5120\n\ts_xor_b32 %[t], %[wo], 0x14000\n\tbuffer_load_dwordx4 %[vw1], %[rw], %[t] offen lds\n\t" \
          "s_add_u32 m0, %[wl], 6144\n\ts_xor_b32 %[t], %[wo], 0x18000\n\tbuffer_load_dwordx4 %[vw0], %[rw], %[t] offen lds\n\ts_add_u32 m0, %[wl], 7168\n\ts_xor_b32 %[t], %[wo], 0x1c000\n\tbuffer_load_dwordx4 %[vw1], %[rw], %[t] offen lds\n\t" \
          "s_add_u32 m0, %[wl], 8192\n\ts_xor_b32 %[t], %[wo], 0x20000\n\tbuffer_load_dwordx4 %[vw0], %[rw], %[t] offen lds\n\ts_add_u32 m0, %[wl], 9216\n\ts_xor_b32 %[t], %[wo], 0x24000\n\tbuffer_load_dwordx4 %[vw1], %[rw], %[t] offen lds\n\t" \
          "s_add_u32 m0, %[wl], 10240\n\ts_xor_b32 %[t], %[wo], 0x28000\n\tbuffer_load_dwordx4 %[vw0], %[rw], %[t] offen lds\n\ts_add_u32 m0, %[wl], 11264\n\ts_xor_b32 %[t], %[wo], 0x2c000\n\tbuffer_load_dwordx4 %[vw1], %[rw], %[t] offen lds\n\t" \
          "s_add_u32 m0, %[wl], 12288\n\ts_xor_b32 %[t], %[wo], 0x30000\n\tbuffer_load_dwordx4 %[vw0], %[rw], %[t] offen lds\n\ts_add_u32 m0, %[wl], 13312\n\ts_xor_b32 %[t], %[wo], 0x34000\n\tbuffer_load_dwordx4 %[vw1], %[rw], %[t] offen lds\n\t" \
          "s_add_u32 m0, %[wl], 14336\n\ts_xor_b32 %[t], %[wo], 0x38000\n\tbuffer_load_dwordx4 %[vw0], %[rw], %[t] offen lds\n\ts_add_u32 m0, %[wl], 15360\n\ts_xor_b32 %[t], %[wo], 0x3c000\n\tbuffer_load_dwordx4 %[vw1], %[rw], %[t] offen lds\n\t" \
          "s_add_u32 m0, %[wl], 16384\n\ts_xor_b32 %[t], %[wo], 0x40000\n\tbuffer_load_dwordx4 %[vw0], %[rw], %[t] offen lds\n\ts_add_u32 m0, %[wl], 17408\n\ts_xor_b32 %[t], %[wo], 0x44000\n\tbuffer_load_dwordx4 %[vw1], %[rw], %[t] offen lds\n\t" \
          "s_add_u32 m0, %[wl], 18432\n\ts_xor_b32 %[t], %[wo], 0x48000\n\tbuffer_load_dwordx4 %[vw0], %[rw], %[t] offen lds\n\ts_add_u32 m0, %[wl], 19456\n\ts_xor_b32 %[t], %[wo], 0x4c000\n\tbuffer_load_dwordx4 %[vw1], %[rw], %[t] offen lds\n\t" \
          "s_add_u32 m0, %[wl], 20480\n\ts_xor_b32 %[t], %[wo], 0x50000\n\tbuffer_load_dwordx4 %[vw0], %[rw], %[t] offen lds\n\ts_add_u32 m0, %[wl], 21504\n\ts_xor_b32 %[t], %[wo], 0x54000\n\tbuffer_load_dwordx4 %[vw1], %[rw], %[t] offen lds\n\t" \
          "s_add_u32 m0, %[wl], 22528\n\ts_xor_b32 %[t], %[wo], 0x58000\n\tbuffer_load_dwordx4 %[vw0], %[rw], %[t] offen lds\n\ts_add_u32 m0, %[wl], 23552\n\ts_xor_b32 %[t], %[wo], 0x5c000\n\tbuffer_load_dwordx4 %[vw1], %[rw], %[t] offen lds\n\t" \
          "s_add_u32 m0, %[wl], 24576\n\ts_xor_b32 %[t], %[wo], 0x60000\n\tbuffer_load_dwordx4 %[vw0], %[rw], %[t] offen lds\n\ts_add_u32 m0, %[wl], 25600\n\ts_xor_b32 %[t], %[wo], 0x64000\n\tbuffer_load_dwordx4 %[vw1], %[rw], %[t] offen lds\n\t" \
          "s_add_u32 m0, %[wl], 26624\n\ts_xor_b32 %[t], %[wo], 0x68000\n\tbuffer_load_dwordx4 %[vw0], %[rw], %[t] offen lds\n\ts_add_u32 m0, %[wl], 27648\n\ts_xor_b32 %[t], %[wo], 0x6c000\n\tbuffer_load_dwordx4 %[vw1], %[rw], %[t] offen lds\n\t" \
          "s_add_u32 m0, %[wl], 28672\n\ts_xor_b32 %[t], %[wo], 0x70000\n\tbuffer_load_dwordx4 %[vw0], %[rw], %[t] offen lds\n\ts_add_u32 m0, %[wl], 29696\n\ts_xor_b32 %[t], %[wo], 0x74000\n\tbuffer_load_dwordx4 %[vw1], %[rw], %[t] offen lds\n\t" \
          "s_add_u32 m0, %[wl], 30720\n\ts_xor_b32 %[t], %[wo], 0x78000\n\tbuffer_load_dwordx4 %[vw0], %[rw], %[t] offen lds\n\ts_add_u32 m0, %[wl], 31744\n\ts_xor_b32 %[t], %[wo], 0x7c000\n\tbuffer_load_dwordx4 %[vw1], %[rw], %[t] offen lds\n\t" \
          "s_add_u32 m0, %[wl], 32768\n\ts_xor_b32 %[t], %[wo], 0x80000\n\tbuffer_load_dwordx4 %[vw0], %[rw], %[t] offen lds\n\ts_add_u32 m0, %[wl], 33792\n\ts_xor_b32 %[t], %[wo], 0x84000\n\tbuffer_load_dwordx4 %[vw1], %[rw], %[t] offen lds\n\t" \
          "s_add_u32 m0, %[wl], 34816\n\ts_xor_b32 %[t], %[wo], 0x88000\n\tbuffer_load_dwordx4 %[vw0], %[rw], %[t] offen lds\n\ts_add_u32 m0, %[wl], 35840\n\ts_xor_b32 %[t], %[wo], 0x8c000\n\tbuffer_load_dwordx4 %[vw1], %[rw], %[t] offen lds\n\t" \
          "s_add_u32 m0, %[wl], 36864\n\ts_xor_b32 %[t], %[wo], 0x90000\n\tbuffer_load_dwordx4 %[vw0], %[rw], %[t] offen lds\n\ts_add_u32 m0, %[wl], 37888\n\ts_xor_b32 %[t], %[wo], 0x94000\n\tbuffer_load_dwordx4 %[vw1], %[rw], %[t] offen lds\n\t" \
          "s_add_u32 m0, %[wl], 38912\n\ts_xor_b32 %[t], %[wo], 0x98000\n\tbuffer_load_dwordx4 %[vw0], %[rw], %[t] offen lds\n\ts_add_u32 m0, %[wl], 39936\n\ts_xor_b32 %[t], %[wo], 0x9c000\n\tbuffer_load_dwordx4 %[vw1], %[rw], %[t] offen lds\n\t" \
          "s_waitcnt vmcnt(32)\n\tds_read_b64_tr_b16 a[0:1], %[rt0] offset:0\n\tds_read_b64_tr_b16 a[2:3], %[rt0] offset:512\n\tds_read_b64_tr_b16 a[4:5], %[rt1] offset:0\n\tds_read_b64_tr_b16 a[6:7], %[rt1] offset:512\n\tds_read_b64_tr_b16 v[128:129], %[rt2] offset:0\n\t" \
          "ds_read_b64_tr_b16 v[130:131], %[rt2] offset:512\n\tds_read_b64_tr_b16 v[32:33], %[rt3] offset:0\n\tds_read_b64_tr_b16 v[34:35], %[rt3] offset:512\n\tds_read_b64_tr_b16 a[8:9], %[rt0] offset:4096\n\tds_read_b64_tr_b16 a[10:11], %[rt0] offset:4608\n\tds_read_b64_tr_b16 a[12:13], %[rt1] offset:4096\n\t" \
          "ds_read_b64_tr_b16 a[14:15], %[rt1] offset:4608\n\tds_read_b64_tr_b16 v[132:133], %[rt2] offset:4096\n\tds_read_b64_tr_b16 v[134:135], %[rt2] offset:4608\n\tds_read_b64_tr_b16 v[36:37], %[rt3] offset:4096\n\tds_read_b64_tr_b16 v[38:39], %[rt3] offset:4608\n\ts_waitcnt lgkmcnt(0)\n\t" \
          "s_add_u32 m0, %[wl], 0\n\ts_xor_b32 %[t], %[wo], 0xa0000\n\tbuffer_load_dwordx4 %[vw0], %[rw], %[t] offen lds\n\ts_add_u32 m0, %[wl], 1024\n\ts_xor_b32 %[t], %[wo], 0xa4000\n\tbuffer_load_dwordx4 %[vw1], %[rw], %[t] offen lds\n\t" \
          "s_add_u32 m0, %[wl], 2048\n\ts_xor_b32 %[t], %[wo], 0xa8000\n\tbuffer_load_dwordx4 %[vw0], %[rw], %[t] offen lds\n\ts_add_u32 m0, %[wl], 3072\n\ts_xor_b32 %[t], %[wo], 0xac000\n\tbuffer_load_dwordx4 %[vw1], %[rw], %[t] offen lds\n\t" \
          "s_add_u32 m0, %[wl], 4096\n\ts_xor_b32 %[t], %[wo], 0xb0000\n\tbuffer_load_dwordx4 %[vw0], %[rw], %[t] offen lds\n\ts_add_u32 m0, %[wl], 5120\n\ts_xor_b32 %[t], %[wo], 0xb4000\n\tbuffer_load_dwordx4 %[vw1], %[rw], %[t] offen lds\n\t" \
          "s_add_u32 m0, %[wl], 6144\n\ts_xor_b32 %[t], %[wo], 0xb8000\n\tbuffer_load_dwordx4 %[vw0], %[rw], %[t] offen lds\n\ts_add_u32 m0, %[wl], 7168\n\ts_xor_b32 %[t], %[wo], 0xbc000\n\tbuffer_load_dwordx4 %[vw1], %[rw], %[t] offen lds\n\t" \
          "s_waitcnt vmcnt(32)\n\tds_read_b64_tr_b16 a[16:17], %[rt0] offset:8192\n\tds_read_b64_tr_b16 a[18:19], %[rt0] offset:8704\n\tds_read_b64_tr_b16 a[20:21], %[rt1] offset:8192\n\tds_read_b64_tr_b16 a[22:23], %[rt1] offset:8704\n\tds_read_b64_tr_b16 v[136:137], %[rt2] offset:8192\n\t" \
          "ds_read_b64_tr_b16 v[138:139], %[rt2] offset:8704\n\tds_read_b64_tr_b16 v[40:41], %[rt3] offset:8192\n\tds_read_b64_tr_b16 v[42:43], %[rt3] offset:8704\n\tds_read_b64_tr_b16 a[24:25], %[rt0] offset:12288\n\tds_read_b64_tr_b16 a[26:27], %[rt0] offset:12800\n\tds_read_b64_tr_b16 a[28:29], %[rt1] offset:12288\n\t" \
          "ds_read_b64_tr_b16 a[30:31], %[rt1] offset:12800\n\tds_read_b64_tr_b16 v[140:141], %[rt2] offset:12288\n\tds_read_b64_tr_b16 v[142:143], %[rt2] offset:12800\n\tds_read_b64_tr_b16 v[44:45], %[rt3] offset:12288\n\tds_read_b64_tr_b16 v[46:47], %[rt3] offset:12800\n\ts_waitcnt lgkmcnt(0)\n\t" \
          "s_add_u32 m0, %[wl], 8192\n\ts_xor_b32 %[t], %[wo], 0xc0000\n\tbuffer_load_dwordx4 %[vw0], %[rw], %[t] offen lds\n\ts_add_u32 m0, %[wl], 9216\n\ts_xor_b32 %[t], %[wo], 0xc4000\n\tbuffer_load_dwordx4 %[vw1], %[rw], %[t] offen lds\n\t" \
          "s_add_u32 m0, %[wl], 10240\n\ts_xor_b32 %[t], %[wo], 0xc8000\n\tbuffer_load_dwordx4 %[vw0], %[rw], %[t] offen lds\n\ts_add_u32 m0, %[wl], 11264\n\ts_xor_b32 %[t], %[wo], 0xcc000\n\tbuffer_load_dwordx4 %[vw1], %[rw], %[t] offen lds\n\t" \
          "s_add_u32 m0, %[wl], 12288\n\ts_xor_b32 %[t], %[wo], 0xd0000\n\tbuffer_load_dwordx4 %[vw0], %[rw], %[t] offen lds\n\ts_add_u32 m0, %[wl], 13312\n\ts_xor_b32 %[t], %[wo], 0xd4000\n\tbuffer_load_dwordx4 %[vw1], %[rw], %[t] offen lds\n\t" \
          "s_add_u32 m0, %[wl], 14336\n\ts_xor_b32 %[t], %[wo], 0xd8000\n\tbuffer_load_dwordx4 %[vw0], %[rw], %[t] offen lds\n\ts_add_u32 m0, %[wl], 15360\n\ts_xor_b32 %[t], %[wo], 0xdc000\n\tbuffer_load_dwordx4 %[vw1], %[rw], %[t] offen lds\n\t" \
          "s_waitcnt vmcnt(32)\n\tds_read_b64_tr_b16 a[32:33], %[rt0] offset:16384\n\tds_read_b64_tr_b16 a[34:35], %[rt0] offset:16896\n\tds_read_b64_tr_b16 a[36:37], %[rt1] offset:16384\n\tds_read_b64_tr_b16 a[38:39], %[rt1] offset:16896\n\tds_read_b64_tr_b16 v[144:145], %[rt2] offset:16384\n\t" \
          "ds_read_b64_tr_b16 v[146:147], %[rt2] offset:16896\n\tds_read_b64_tr_b16 v[48:49], %[rt3] offset:16384\n\tds_read_b64_tr_b16 v[50:51], %[rt3] offset:16896\n\tds_read_b64_tr_b16 a[40:41], %[rt0] offset:20480\n\tds_read_b64_tr_b16 a[42:43], %[rt0] offset:20992\n\tds_read_b64_tr_b16 a[44:45], %[rt1] offset:20480\n\t" \
          "ds_read_b64_tr_b16 a[46:47], %[rt1] offset:20992\n\tds_read_b64_tr_b16 v[148:149], %[rt2] offset:20480\n\tds_read_b64_tr_b16 v[150:151], %[rt2] offset:20992\n\tds_read_b64_tr_b16 v[52:53], %[rt3] offset:20480\n\tds_read_b64_tr_b16 v[54:55], %[rt3] offset:20992\n\ts_waitcnt lgkmcnt(0)\n\t" \
          "s_add_u32 m0, %[wl], 16384\n\ts_xor_b32 %[t], %[wo], 0xe0000\n\tbuffer_load_dwordx4 %[vw0], %[rw], %[t] offen lds\n\ts_add_u32 m0, %[wl], 17408\n\ts_xor_b32 %[t], %[wo], 0xe4000\n\tbuffer_load_dwordx4 %[vw1], %[rw], %[t] offen lds\n\t" \
          "s_add_u32 m0, %[wl], 18432\n\ts_xor_b32 %[t], %[wo], 0xe8000\n\tbuffer_load_dwordx4 %[vw0], %[rw], %[t] offen lds\n\ts_add_u32 m0, %[wl], 19456\n\ts_xor_b32 %[t], %[wo], 0xec000\n\tbuffer_load_dwordx4 %[vw1], %[rw], %[t] offen lds\n\t" \
          "s_add_u32 m0, %[wl], 20480\n\ts_xor_b32 %[t], %[wo], 0xf0000\n\tbuffer_load_dwordx4 %[vw0], %[rw], %[t] offen lds\n\ts_add_u32 m0, %[wl], 21504\n\ts_xor_b32 %[t], %[wo], 0xf4000\n\tbuffer_load_dwordx4 %[vw1], %[rw], %[t] offen lds\n\t" \
          "s_add_u32 m0, %[wl], 22528\n\ts_xor_b32 %[t], %[wo], 0xf8000\n\tbuffer_load_dwordx4 %[vw0], %[rw], %[t] offen lds\n\ts_add_u32 m0, %[wl], 23552\n\ts_xor_b32 %[t], %[wo], 0xfc000\n\tbuffer_load_dwordx4 %[vw1], %[rw], %[t] offen lds\n\t" \
          "s_waitcnt vmcnt(32)\n\tds_read_b64_tr_b16 a[48:49], %[rt0] offset:24576\n\tds_read_b64_tr_b16 a[50:51], %[rt0] offset:25088\n\tds_read_b64_tr_b16 a[52:53], %[rt1] offset:24576\n\tds_read_b64_tr_b16 a[54:55], %[rt1] offset:25088\n\tds_read_b64_tr_b16 v[152:153], %[rt2] offset:24576\n\t" \
          "ds_read_b64_tr_b16 v[154:155], %[rt2] offset:25088\n\tds_read_b64_tr_b16 v[56:57], %[rt3] offset:24576\n\tds_read_b64_tr_b16 v[58:59], %[rt3] offset:25088\n\tds_read_b64_tr_b16 a[56:57], %[rt0] offset:28672\n\tds_read_b64_tr_b16 a[58:59], %[rt0] offset:29184\n\tds_read_b64_tr_b16 a[60:61], %[rt1] offset:28672\n\t" \
          "ds_read_b64_tr_b16 a[62:63], %[rt1] offset:29184\n\tds_read_b64_tr_b16 v[156:157], %[rt2] offset:28672\n\tds_read_b64_tr_b16 v[158:159], %[rt2] offset:29184\n\tds_read_b64_tr_b16 v[60:61], %[rt3] offset:28672\n\tds_read_b64_tr_b16 v[62:63], %[rt3] offset:29184\n\ts_waitcnt lgkmcnt(0)\n\t" \
          "s_add_u32 m0, %[wl], 24576\n\ts_xor_b32 %[t], %[wo], 0x100000\n\tbuffer_load_dwordx4 %[vw0], %[rw], %[t] offen lds\n\ts_add_u32 m0, %[wl], 25600\n\ts_xor_b32 %[t], %[wo], 0x104000\n\tbuffer_load_dwordx4 %[vw1], %[rw], %[t] offen lds\n\t" \
          "s_add_u32 m0, %[wl], 26624\n\ts_xor_b32 %[t], %[wo], 0x108000\n\tbuffer_load_dwordx4 %[vw0], %[rw], %[t] offen lds\n\ts_add_u32 m0, %[wl], 27648\n\ts_xor_b32 %[t], %[wo], 0x10c000\n\tbuffer_load_dwordx4 %[vw1], %[rw], %[t] offen lds\n\t" \
          "s_add_u32 m0, %[wl], 28672\n\ts_xor_b32 %[t], %[wo], 0x110000\n\tbuffer_load_dwordx4 %[vw0], %[rw], %[t] offen lds\n\ts_add_u32 m0, %[wl], 29696\n\ts_xor_b32 %[t], %[wo], 0x114000\n\tbuffer_load_dwordx4 %[vw1], %[rw], %[t] offen lds\n\t" \
          "s_add_u32 m0, %[wl], 30720\n\ts_xor_b32 %[t], %[wo], 0x118000\n\tbuffer_load_dwordx4 %[vw0], %[rw], %[t] offen lds\n\ts_add_u32 m0, %[wl], 31744\n\ts_xor_b32 %[t], %[wo], 0x11c000\n\tbuffer_load_dwordx4 %[vw1], %[rw], %[t] offen lds\n\t" \
          "s_waitcnt vmcnt(32)\n\tds_read_b64_tr_b16 a[64:65], %[rt0] offset:32768\n\tds_read_b64_tr_b16 a[66:67], %[rt0] offset:33280\n\tds_read_b64_tr_b16 a[68:69], %[rt1] offset:32768\n\tds_read_b64_tr_b16 a[70:71], %[rt1] offset:33280\n\tds_read_b64_tr_b16 v[160:161], %[rt2] offset:32768\n\t" \
          "ds_read_b64_tr_b16 v[162:163], %[rt2] offset:33280\n\tds_read_b64_tr_b16 v[64:65], %[rt3] offset:32768\n\tds_read_b64_tr_b16 v[66:67], %[rt3] offset:33280\n\tds_read_b64_tr_b16 a[72:73], %[rt0] offset:36864\n\tds_read_b64_tr_b16 a[74:75], %[rt0] offset:37376\n\tds_read_b64_tr_b16 a[76:77], %[rt1] offset:36864\n\t" \
          "ds_read_b64_tr_b16 a[78:79], %[rt1] offset:37376\n\tds_read_b64_tr_b16 v[164:165], %[rt2] offset:36864\n\tds_read_b64_tr_b16 v[166:167], %[rt2] offset:37376\n\tds_read_b64_tr_b16 v[68:69], %[rt3] offset:36864\n\tds_read_b64_tr_b16 v[70:71], %[rt3] offset:37376\n\ts_waitcnt lgkmcnt(0)\n\t" \
          "s_add_u32 m0, %[wl], 32768\n\ts_xor_b32 %[t], %[wo], 0x120000\n\tbuffer_load_dwordx4 %[vw0], %[rw], %[t] offen lds\n\ts_add_u32 m0, %[wl], 33792\n\ts_xor_b32 %[t], %[wo], 0x124000\n\tbuffer_load_dwordx4 %[vw1], %[rw], %[t] offen lds\n\t" \
          "s_add_u32 m0, %[wl], 34816\n\ts_xor_b32 %[t], %[wo], 0x128000\n\tbuffer_load_dwordx4 %[vw0], %[rw], %[t] offen lds\n\ts_add_u32 m0, %[wl], 35840\n\ts_xor_b32 %[t], %[wo], 0x12c000\n\tbuffer_load_dwordx4 %[vw1], %[rw], %[t] offen lds\n\t" \
          "s_add_u32 m0, %[wl], 36864\n\ts_xor_b32 %[t], %[wo], 0x130000\n\tbuffer_load_dwordx4 %[vw0], %[rw], %[t] offen lds\n\ts_add_u32 m0, %[wl], 37888\n\ts_xor_b32 %[t], %[wo], 0x134000\n\tbuffer_load_dwordx4 %[vw1], %[rw], %[t] offen lds\n\t" \
          "s_add_u32 m0, %[wl], 38912\n\ts_xor_b32 %[t], %[wo], 0x138000\n\tbuffer_load_dwordx4 %[vw0], %[rw], %[t] offen lds\n\ts_add_u32 m0, %[wl], 39936\n\ts_xor_b32 %[t], %[wo], 0x13c000\n\tbuffer_load_dwordx4 %[vw1], %[rw], %[t] offen lds\n\t" \
          "s_waitcnt vmcnt(32)\n\tds_read_b64_tr_b16 a[80:81], %[rt0] offset:0\n\tds_read_b64_tr_b16 a[82:83], %[rt0] offset:512\n\tds_read_b64_tr_b16 a[84:85], %[rt1] offset:0\n\tds_read_b64_tr_b16 a[86:87], %[rt1] offset:512\n\tds_read_b64_tr_b16 v[168:169], %[rt2] offset:0\n\t" \
          "ds_read_b64_tr_b16 v[170:171], %[rt2] offset:512\n\tds_read_b64_tr_b16 v[72:73], %[rt3] offset:0\n\tds_read_b64_tr_b16 v[74:75], %[rt3] offset:512\n\tds_read_b64_tr_b16 a[88:89], %[rt0] offset:4096\n\tds_read_b64_tr_b16 a[90:91], %[rt0] offset:4608\n\tds_read_b64_tr_b16 a[92:93], %[rt1] offset:4096\n\t" \
          "ds_read_b64_tr_b16 a[94:95], %[rt1] offset:4608\n\tds_read_b64_tr_b16 v[172:173], %[rt2] offset:4096\n\tds_read_b64_tr_b16 v[174:175], %[rt2] offset:4608\n\tds_read_b64_tr_b16 v[76:77], %[rt3] offset:4096\n\tds_read_b64_tr_b16 v[78:79], %[rt3] offset:4608\n\ts_waitcnt lgkmcnt(0)\n\t" \
          "s_add_u32 m0, %[wl], 0\n\ts_xor_b32 %[t], %[wo], 0x140000\n\tbuffer_load_dwordx4 %[vw0], %[rw], %[t] offen lds\n\ts_add_u32 m0, %[wl], 1024\n\ts_xor_b32 %[t], %[wo], 0x144000\n\tbuffer_load_dwordx4 %[vw1], %[rw], %[t] offen lds\n\t" \
          "s_add_u32 m0, %[wl], 2048\n\ts_xor_b32 %[t], %[wo], 0x148000\n\tbuffer_load_dwordx4 %[vw0], %[rw], %[t] offen lds\n\ts_add_u32 m0, %[wl], 3072\n\ts_xor_b32 %[t], %[wo], 0x14c000\n\tbuffer_load_dwordx4 %[vw1], %[rw], %[t] offen lds\n\t" \
          "s_add_u32 m0, %[wl], 4096\n\ts_xor_b32 %[t], %[wo], 0x150000\n\tbuffer_load_dwordx4 %[vw0], %[rw], %[t] offen lds\n\ts_add_u32 m0, %[wl], 5120\n\ts_xor_b32 %[t], %[wo], 0x154000\n\tbuffer_load_dwordx4 %[vw1], %[rw], %[t] offen lds\n\t" \
          "s_add_u32 m0, %[wl], 6144\n\ts_xor_b32 %[t], %[wo], 0x158000\n\tbuffer_load_dwordx4 %[vw0], %[rw], %[t] offen lds\n\ts_add_u32 m0, %[wl], 7168\n\ts_xor_b32 %[t], %[wo], 0x15c000\n\tbuffer_load_dwordx4 %[vw1], %[rw], %[t] offen lds\n\t" \
          "s_waitcnt vmcnt(32)\n\tds_read_b64_tr_b16 a[96:97], %[rt0] offset:8192\n\tds_read_b64_tr_b16 a[98:99], %[rt0] offset:8704\n\tds_read_b64_tr_b16 a[100:101], %[rt1] offset:8192\n\tds_read_b64_tr_b16 a[102:103], %[rt1] offset:8704\n\tds_read_b64_tr_b16 v[176:177], %[rt2] offset:8192\n\t" \
          "ds_read_b64_tr_b16 v[178:179], %[rt2] offset:8704\n\tds_read_b64_tr_b16 v[80:81], %[rt3] offset:8192\n\tds_read_b64_tr_b16 v[82:83], %[rt3] offset:8704\n\tds_read_b64_tr_b16 a[104:105], %[rt0] offset:12288\n\tds_read_b64_tr_b16 a[106:107], %[rt0] offset:12800\n\tds_read_b64_tr_b16 a[108:109], %[rt1] offset:12288\n\t" \
          "ds_read_b64_tr_b16 a[110:111], %[rt1] offset:12800\n\tds_read_b64_tr_b16 v[180:181], %[rt2] offset:12288\n\tds_read_b64_tr_b16 v[182:183], %[rt2] offset:12800\n\tds_read_b64_tr_b16 v[84:85], %[rt3] offset:12288\n\tds_read_b64_tr_b16 v[86:87], %[rt3] offset:12800\n\ts_waitcnt lgkmcnt(0)\n\t" \
          "s_add_u32 m0, %[wl], 8192\n\ts_xor_b32 %[t], %[wo], 0x160000\n\tbuffer_load_dwordx4 %[vw0], %[rw], %[t] offen lds\n\ts_add_u32 m0, %[wl], 9216\n\ts_xor_b32 %[t], %[wo], 0x164000\n\tbuffer_load_dwordx4 %[vw1], %[rw], %[t] offen lds\n\t" \
          "s_add_u32 m0, %[wl], 10240\n\ts_xor_b32 %[t], %[wo], 0x168000\n\tbuffer_load_dwordx4 %[vw0], %[rw], %[t] offen lds\n\ts_add_u32 m0, %[wl], 11264\n\ts_xor_b32 %[t], %[wo], 0x16c000\n\tbuffer_load_dwordx4 %[vw1], %[rw], %[t] offen lds\n\t" \
          "s_add_u32 m0, %[wl], 12288\n\ts_xor_b32 %[t], %[wo], 0x170000\n\tbuffer_load_dwordx4 %[vw0], %[rw], %[t] offen lds\n\ts_add_u32 m0, %[wl], 13312\n\ts_xor_b32 %[t], %[wo], 0x174000\n\tbuffer_load_dwordx4 %[vw1], %[rw], %[t] offen lds\n\t" \
          "s_add_u32 m0, %[wl], 14336\n\ts_xor_b32 %[t], %[wo], 0x178000\n\tbuffer_load_dwordx4 %[vw0], %[rw], %[t] offen lds\n\ts_add_u32 m0, %[wl], 15360\n\ts_xor_b32 %[t], %[wo], 0x17c000\n\tbuffer_load_dwordx4 %[vw1], %[rw], %[t] offen lds\n\t" \
          "s_waitcnt vmcnt(32)\n\tds_read_b64_tr_b16 a[112:113], %[rt0] offset:16384\n\tds_read_b64_tr_b16 a[114:115], %[rt0] offset:16896\n\tds_read_b64_tr_b16 a[116:117], %[rt1] offset:16384\n\tds_read_b64_tr_b16 a[118:119], %[rt1] offset:16896\n\tds_read_b64_tr_b16 v[184:185], %[rt2] offset:16384\n\t" \
          "ds_read_b64_tr_b16 v[186:187], %[rt2] offset:16896\n\tds_read_b64_tr_b16 v[88:89], %[rt3] offset:16384\n\tds_read_b64_tr_b16 v[90:91], %[rt3] offset:16896\n\tds_read_b64_tr_b16 a[120:121], %[rt0] offset:20480\n\tds_read_b64_tr_b16 a[122:123], %[rt0] offset:20992\n\tds_read_b64_tr_b16 a[124:125], %[rt1] offset:20480\n\t" \
          "ds_read_b64_tr_b16 a[126:127], %[rt1] offset:20992\n\tds_read_b64_tr_b16 v[188:189], %[rt2] offset:20480\n\tds_read_b64_tr_b16 v[190:191], %[rt2] offset:20992\n\tds_read_b64_tr_b16 v[92:93], %[rt3] offset:20480\n\tds_read_b64_tr_b16 v[94:95], %[rt3] offset:20992\n\ts_waitcnt lgkmcnt(0)\n\t" \
          "s_add_u32 m0, %[wl], 16384\n\ts_xor_b32 %[t], %[wo], 0x180000\n\tbuffer_load_dwordx4 %[vw0], %[rw], %[t] offen lds\n\ts_add_u32 m0, %[wl], 17408\n\ts_xor_b32 %[t], %[wo], 0x184000\n\tbuffer_load_dwordx4 %[vw1], %[rw], %[t] offen lds\n\t" \
          "s_add_u32 m0, %[wl], 18432\n\ts_xor_b32 %[t], %[wo], 0x188000\n\tbuffer_load_dwordx4 %[vw0], %[rw], %[t] offen lds\n\ts_add_u32 m0, %[wl], 19456\n\ts_xor_b32 %[t], %[wo], 0x18c000\n\tbuffer_load_dwordx4 %[vw1], %[rw], %[t] offen lds\n\t" \
          "s_add_u32 m0, %[wl], 20480\n\ts_xor_b32 %[t], %[wo], 0x190000\n\tbuffer_load_dwordx4 %[vw0], %[rw], %[t] offen lds\n\ts_add_u32 m0, %[wl], 21504\n\ts_xor_b32 %[t], %[wo], 0x194000\n\tbuffer_load_dwordx4 %[vw1], %[rw], %[t] offen lds\n\t" \
          "s_add_u32 m0, %[wl], 22528\n\ts_xor_b32 %[t], %[wo], 0x198000\n\tbuffer_load_dwordx4 %[vw0], %[rw], %[t] offen lds\n\ts_add_u32 m0, %[wl], 23552\n\ts_xor_b32 %[t], %[wo], 0x19c000\n\tbuffer_load_dwordx4 %[vw1], %[rw], %[t] offen lds\n\t" \
          "s_waitcnt vmcnt(32)\n\tds_read_b64_tr_b16 a[128:129], %[rt0] offset:24576\n\tds_read_b64_tr_b16 a[130:131], %[rt0] offset:25088\n\tds_read_b64_tr_b16 a[132:133], %[rt1] offset:24576\n\tds_read_b64_tr_b16 a[134:135], %[rt1] offset:25088\n\tds_read_b64_tr_b16 v[192:193], %[rt2] offset:24576\n\t" \
          "ds_read_b64_tr_b16 v[194:195], %[rt2] offset:25088\n\tds_read_b64_tr_b16 v[96:97], %[rt3] offset:24576\n\tds_read_b64_tr_b16 v[98:99], %[rt3] offset:25088\n\tds_read_b64_tr_b16 a[136:137], %[rt0] offset:28672\n\tds_read_b64_tr_b16 a[138:139], %[rt0] offset:29184\n\tds_read_b64_tr_b16 a[140:141], %[rt1] offset:28672\n\t" \
          "ds_read_b64_tr_b16 a[142:143], %[rt1] offset:29184\n\tds_read_b64_tr_b16 v[196:197], %[rt2] offset:28672\n\tds_read_b64_tr_b16 v[198:199], %[rt2] offset:29184\n\tds_read_b64_tr_b16 v[100:101], %[rt3] offset:28672\n\tds_read_b64_tr_b16 v[102:103], %[rt3] offset:29184\n\ts_waitcnt lgkmcnt(0)\n\t" \
          "s_add_u32 m0, %[wl], 24576\n\ts_xor_b32 %[t], %[wo], 0x1a0000\n\tbuffer_load_dwordx4 %[vw0], %[rw], %[t] offen lds\n\ts_add_u32 m0, %[wl], 25600\n\ts_xor_b32 %[t], %[wo], 0x1a4000\n\tbuffer_load_dwordx4 %[vw1], %[rw], %[t] offen lds\n\t" \
          "s_add_u32 m0, %[wl], 26624\n\ts_xor_b32 %[t], %[wo], 0x1a8000\n\tbuffer_load_dwordx4 %[vw0], %[rw], %[t] offen lds\n\ts_add_u32 m0, %[wl], 27648\n\ts_xor_b32 %[t], %[wo], 0x1ac000\n\tbuffer_load_dwordx4 %[vw1], %[rw], %[t] offen lds\n\t" \
          "s_add_u32 m0, %[wl], 28672\n\ts_xor_b32 %[t], %[wo], 0x1b0000\n\tbuffer_load_dwordx4 %[vw0], %[rw], %[t] offen lds\n\ts_add_u32 m0, %[wl], 29696\n\ts_xor_b32 %[t], %[wo], 0x1b4000\n\tbuffer_load_dwordx4 %[vw1], %[rw], %[t] offen lds\n\t" \
          "s_add_u32 m0, %[wl], 30720\n\ts_xor_b32 %[t], %[wo], 0x1b8000\n\tbuffer_load_dwordx4 %[vw0], %[rw], %[t] offen lds\n\ts_add_u32 m0, %[wl], 31744\n\ts_xor_b32 %[t], %[wo], 0x1bc000\n\tbuffer_load_dwordx4 %[vw1], %[rw], %[t] offen lds\n\t" \
          "s_waitcnt vmcnt(32)\n\tds_read_b64_tr_b16 a[144:145], %[rt0] offset:32768\n\tds_read_b64_tr_b16 a[146:147], %[rt0] offset:33280\n\tds_read_b64_tr_b16 a[148:149], %[rt1] offset:32768\n\tds_read_b64_tr_b16 a[150:151], %[rt1] offset:33280\n\tds_read_b64_tr_b16 v[200:201], %[rt2] offset:32768\n\t" \
          "ds_read_b64_tr_b16 v[202:203], %[rt2] offset:33280\n\tds_read_b64_tr_b16 v[104:105], %[rt3] offset:32768\n\tds_read_b64_tr_b16 v[106:107], %[rt3] offset:33280\n\tds_read_b64_tr_b16 a[152:153], %[rt0] offset:36864\n\tds_read_b64_tr_b16 a[154:155], %[rt0] offset:37376\n\tds_read_b64_tr_b16 a[156:157], %[rt1] offset:36864\n\t" \
          "ds_read_b64_tr_b16 a[158:159], %[rt1] offset:37376\n\tds_read_b64_tr_b16 v[204:205], %[rt2] offset:36864\n\tds_read_b64_tr_b16 v[206:207], %[rt2] offset:37376\n\tds_read_b64_tr_b16 v[108:109], %[rt3] offset:36864\n\tds_read_b64_tr_b16 v[110:111], %[rt3] offset:37376\n\ts_waitcnt lgkmcnt(0)\n\t" \
          "s_add_u32 m0, %[wl], 32768\n\ts_xor_b32 %[t], %[wo], 0x1c0000\n\tbuffer_load_dwordx4 %[vw0], %[rw], %[t] offen lds\n\ts_add_u32 m0, %[wl], 33792\n\ts_xor_b32 %[t], %[wo], 0x1c4000\n\tbuffer_load_dwordx4 %[vw1], %[rw], %[t] offen lds\n\t" \
          "s_add_u32 m0, %[wl], 34816\n\ts_xor_b32 %[t], %[wo], 0x1c8000\n\tbuffer_load_dwordx4 %[vw0], %[rw], %[t] offen lds\n\ts_add_u32 m0, %[wl], 35840\n\ts_xor_b32 %[t], %[wo], 0x1cc000\n\tbuffer_load_dwordx4 %[vw1], %[rw], %[t] offen lds\n\t" \
          "s_add_u32 m0, %[wl], 36864\n\ts_xor_b32 %[t], %[wo], 0x1d0000\n\tbuffer_load_dwordx4 %[vw0], %[rw], %[t] offen lds\n\ts_add_u32 m0, %[wl], 37888\n\ts_xor_b32 %[t], %[wo], 0x1d4000\n\tbuffer_load_dwordx4 %[vw1], %[rw], %[t] offen lds\n\t" \
          "s_add_u32 m0, %[wl], 38912\n\ts_xor_b32 %[t], %[wo], 0x1d8000\n\tbuffer_load_dwordx4 %[vw0], %[rw], %[t] offen lds\n\ts_add_u32 m0, %[wl], 39936\n\ts_xor_b32 %[t], %[wo], 0x1dc000\n\tbuffer_load_dwordx4 %[vw1], %[rw], %[t] offen lds\n\t" \
          "s_waitcnt vmcnt(32)\n\tds_read_b64_tr_b16 a[160:161], %[rt0] offset:0\n\tds_read_b64_tr_b16 a[162:163], %[rt0] offset:512\n\tds_read_b64_tr_b16 a[164:165], %[rt1] offset:0\n\tds_read_b64_tr_b16 a[166:167], %[rt1] offset:512\n\tds_read_b64_tr_b16 v[208:209], %[rt2] offset:0\n\t" \
          "ds_read_b64_tr_b16 v[210:211], %[rt2] offset:512\n\tds_read_b64_tr_b16 v[112:113], %[rt3] offset:0\n\tds_read_b64_tr_b16 v[114:115], %[rt3] offset:512\n\tds_read_b64_tr_b16 a[168:169], %[rt0] offset:4096\n\tds_read_b64_tr_b16 a[170:171], %[rt0] offset:4608\n\tds_read_b64_tr_b16 a[172:173], %[rt1] offset:4096\n\t" \
          "ds_read_b64_tr_b16 a[174:175], %[rt1] offset:4608\n\tds_read_b64_tr_b16 v[212:213], %[rt2] offset:4096\n\tds_read_b64_tr_b16 v[214:215], %[rt2] offset:4608\n\tds_read_b64_tr_b16 v[116:117], %[rt3] offset:4096\n\tds_read_b64_tr_b16 v[118:119], %[rt3] offset:4608\n\ts_waitcnt lgkmcnt(0)\n\t" \
          "s_add_u32 m0, %[wl], 0\n\ts_xor_b32 %[t], %[wo], 0x1e0000\n\tbuffer_load_dwordx4 %[vw0], %[rw], %[t] offen lds\n\ts_add_u32 m0, %[wl], 1024\n\ts_xor_b32 %[t], %[wo], 0x1e4000\n\tbuffer_load_dwordx4 %[vw1], %[rw], %[t] offen lds\n\t" \
          "s_add_u32 m0, %[wl], 2048\n\ts_xor_b32 %[t], %[wo], 0x1e8000\n\tbuffer_load_dwordx4 %[vw0], %[rw], %[t] offen lds\n\ts_add_u32 m0, %[wl], 3072\n\ts_xor_b32 %[t], %[wo], 0x1ec000\n\tbuffer_load_dwordx4 %[vw1], %[rw], %[t] offen lds\n\t" \
          "s_add_u32 m0, %[wl], 4096\n\ts_xor_b32 %[t], %[wo], 0x1f0000\n\tbuffer_load_dwordx4 %[vw0], %[rw], %[t] offen lds\n\ts_add_u32 m0, %[wl], 5120\n\ts_xor_b32 %[t], %[wo], 0x1f4000\n\tbuffer_load_dwordx4 %[vw1], %[rw], %[t] offen lds\n\t" \
          "s_add_u32 m0, %[wl], 6144\n\ts_xor_b32 %[t], %[wo], 0x1f8000\n\tbuffer_load_dwordx4 %[vw0], %[rw], %[t] offen lds\n\ts_add_u32 m0, %[wl], 7168\n\ts_xor_b32 %[t], %[wo], 0x1fc000\n\tbuffer_load_dwordx4 %[vw1], %[rw], %[t] offen lds\n\t" \
          "s_waitcnt vmcnt(32)\n\tds_read_b64_tr_b16 a[176:177], %[rt0] offset:8192\n\tds_read_b64_tr_b16 a[178:179], %[rt0] offset:8704\n\tds_read_b64_tr_b16 a[180:181], %[rt1] offset:8192\n\tds_read_b64_tr_b16 a[182:183], %[rt1] offset:8704\n\tds_read_b64_tr_b16 v[216:217], %[rt2] offset:8192\n\t" \
          "ds_read_b64_tr_b16 v[218:219], %[rt2] offset:8704\n\tds_read_b64_tr_b16 v[120:121], %[rt3] offset:8192\n\tds_read_b64_tr_b16 v[122:123], %[rt3] offset:8704\n\tds_read_b64_tr_b16 a[184:185], %[rt0] offset:12288\n\tds_read_b64_tr_b16 a[186:187], %[rt0] offset:12800\n\tds_read_b64_tr_b16 a[188:189], %[rt1] offset:12288\n\t" \
          "ds_read_b64_tr_b16 a[190:191], %[rt1] offset:12800\n\tds_read_b64_tr_b16 v[220:221], %[rt2] offset:12288\n\tds_read_b64_tr_b16 v[222:223], %[rt2] offset:12800\n\tds_read_b64_tr_b16 v[124:125], %[rt3] offset:12288\n\tds_read_b64_tr_b16 v[126:127], %[rt3] offset:12800\n\ts_waitcnt lgkmcnt(0)\n\t" \
          "ds_write_b128 %[own], v[32:35] offset:8192\n\tds_write_b128 %[own], v[36:39] offset:9216\n\tds_write_b128 %[own], v[40:43] offset:10240\n\tds_write_b128 %[own], v[44:47] offset:11264\n\tds_write_b128 %[own], v[48:51] offset:12288\n\tds_write_b128 %[own], v[52:55] offset:13312\n\t" \
          "ds_write_b128 %[own], v[56:59] offset:14336\n\tds_write_b128 %[own], v[60:63] offset:15360\n\ts_waitcnt lgkmcnt(0)\n\ts_waitcnt vmcnt(24)\n\tds_read_b64_tr_b16 a[192:193], %[rt0] offset:16384\n\tds_read_b64_tr_b16 a[194:195], %[rt0] offset:16896\n\t" \
          "ds_read_b64_tr_b16 a[196:197], %[rt1] offset:16384\n\tds_read_b64_tr_b16 a[198:199], %[rt1] offset:16896\n\tds_read_b64_tr_b16 v[224:225], %[rt2] offset:16384\n\tds_read_b64_tr_b16 v[226:227], %[rt2] offset:16896\n\tds_read_b64_tr_b16 v[32:33], %[rt3] offset:16384\n\tds_read_b64_tr_b16 v[34:35], %[rt3] offset:16896\n\t" \
          "ds_read_b64_tr_b16 a[200:201], %[rt0] offset:20480\n\tds_read_b64_tr_b16 a[202:203], %[rt0] offset:20992\n\tds_read_b64_tr_b16 a[204:205], %[rt1] offset:20480\n\tds_read_b64_tr_b16 a[206:207], %[rt1] offset:20992\n\tds_read_b64_tr_b16 v[228:229], %[rt2] offset:20480\n\tds_read_b64_tr_b16 v[230:231], %[rt2] offset:20992\n\t" \
          "ds_read_b64_tr_b16 v[36:37], %[rt3] offset:20480\n\tds_read_b64_tr_b16 v[38:39], %[rt3] offset:20992\n\ts_waitcnt lgkmcnt(0)\n\tds_write_b128 %[own], v[64:67] offset:16384\n\tds_write_b128 %[own], v[68:71] offset:17408\n\tds_write_b128 %[own], v[72:75] offset:18432\n\t" \
          "ds_write_b128 %[own], v[76:79] offset:19456\n\tds_write_b128 %[own], v[80:83] offset:20480\n\tds_write_b128 %[own], v[84:87] offset:21504\n\tds_write_b128 %[own], v[88:91] offset:22528\n\tds_write_b128 %[own], v[92:95] offset:23552\n\ts_waitcnt lgkmcnt(0)\n\t" \
          "s_waitcnt vmcnt(16)\n\tds_read_b64_tr_b16 a[208:209], %[rt0] offset:24576\n\tds_read_b64_tr_b16 a[210:211], %[rt0] offset:25088\n\tds_read_b64_tr_b16 a[212:213], %[rt1] offset:24576\n\tds_read_b64_tr_b16 a[214:215], %[rt1] offset:25088\n\tds_read_b64_tr_b16 v[232:233], %[rt2] offset:24576\n\t" \
          "ds_read_b64_tr_b16 v[234:235], %[rt2] offset:25088\n\tds_read_b64_tr_b16 v[40:41], %[rt3] offset:24576\n\tds_read_b64_tr_b16 v[42:43], %[rt3] offset:25088\n\tds_read_b64_tr_b16 a[216:217], %[rt0] offset:28672\n\tds_read_b64_tr_b16 a[218:219], %[rt0] offset:29184\n\tds_read_b64_tr_b16 a[220:221], %[rt1] offset:28672\n\t" \
          "ds_read_b64_tr_b16 a[222:223], %[rt1] offset:29184\n\tds_read_b64_tr_b16 v[236:237], %[rt2] offset:28672\n\tds_read_b64_tr_b16 v[238:239], %[rt2] offset:29184\n\tds_read_b64_tr_b16 v[44:45], %[rt3] offset:28672\n\tds_read_b64_tr_b16 v[46:47], %[rt3] offset:29184\n\ts_waitcnt lgkmcnt(0)\n\t" \
          "ds_write_b128 %[own], v[96:99] offset:24576\n\tds_write_b128 %[own], v[100:103] offset:25600\n\tds_write_b128 %[own], v[104:107] offset:26624\n\tds_write_b128 %[own], v[108:111] offset:27648\n\tds_write_b128 %[own], v[112:115] offset:28672\n\tds_write_b128 %[own], v[116:119] offset:29696\n\t" \
          "ds_write_b128 %[own], v[120:123] offset:30720\n\tds_write_b128 %[own], v[124:127] offset:31744\n\ts_waitcnt lgkmcnt(0)\n\ts_waitcnt vmcnt(8)\n\tds_read_b64_tr_b16 a[224:225], %[rt0] offset:32768\n\tds_read_b64_tr_b16 a[226:227], %[rt0] offset:33280\n\t" \
          "ds_read_b64_tr_b16 a[228:229], %[rt1] offset:32768\n\tds_read_b64_tr_b16 a[230:231], %[rt1] offset:33280\n\tds_read_b64_tr_b16 v[240:241], %[rt2] offset:32768\n\tds_read_b64_tr_b16 v[242:243], %[rt2] offset:33280\n\tds_read_b64_tr_b16 v[48:49], %[rt3] offset:32768\n\tds_read_b64_tr_b16 v[50:51], %[rt3] offset:33280\n\t" \
          "ds_read_b64_tr_b16 a[232:233], %[rt0] offset:36864\n\tds_read_b64_tr_b16 a[234:235], %[rt0] offset:37376\n\tds_read_b64_tr_b16 a[236:237], %[rt1] offset:36864\n\tds_read_b64_tr_b16 a[238:239], %[rt1] offset:37376\n\tds_read_b64_tr_b16 v[244:245], %[rt2] offset:36864\n\tds_read_b64_tr_b16 v[246:247], %[rt2] offset:37376\n\t" \
          "ds_read_b64_tr_b16 v[52:53], %[rt3] offset:36864\n\tds_read_b64_tr_b16 v[54:55], %[rt3] offset:37376\n\ts_waitcnt lgkmcnt(0)\n\ts_waitcnt vmcnt(0)\n\tds_read_b64_tr_b16 a[240:241], %[rt0] offset:0\n\tds_read_b64_tr_b16 a[242:243], %[rt0] offset:512\n\t" \
          "ds_read_b64_tr_b16 a[244:245], %[rt1] offset:0\n\tds_read_b64_tr_b16 a[246:247], %[rt1] offset:512\n\tds_read_b64_tr_b16 v[248:249], %[rt2] offset:0\n\tds_read_b64_tr_b16 v[250:251], %[rt2] offset:512\n\tds_read_b64_tr_b16 v[56:57], %[rt3] offset:0\n\tds_read_b64_tr_b16 v[58:59], %[rt3] offset:512\n\t" \
          "ds_read_b64_tr_b16 a[248:249], %[rt0] offset:4096\n\tds_read_b64_tr_b16 a[250:251], %[rt0] offset:4608\n\tds_read_b64_tr_b16 a[252:253], %[rt1] offset:4096\n\tds_read_b64_tr_b16 a[254:255], %[rt1] offset:4608\n\tds_read_b64_tr_b16 v[252:253], %[rt2] offset:4096\n\tds_read_b64_tr_b16 v[254:255], %[rt2] offset:4608\n\t" \
          "ds_read_b64_tr_b16 v[60:61], %[rt3] offset:4096\n\tds_read_b64_tr_b16 v[62:63], %[rt3] offset:4608\n\ts_waitcnt lgkmcnt(0)\n\tds_write_b128 %[own], v[32:35] offset:0\n\tds_write_b128 %[own], v[36:39] offset:1024\n\tds_write_b128 %[own], v[40:43] offset:2048\n\t" \
          "ds_write_b128 %[own], v[44:47] offset:3072\n\tds_write_b128 %[own], v[48:51] offset:4096\n\tds_write_b128 %[own], v[52:55] offset:5120\n\tds_write_b128 %[own], v[56:59] offset:6144\n\tds_write_b128 %[own], v[60:63] offset:7168\n\ts_waitcnt lgkmcnt(0)\n\t" \
          "Lou_wait%=:\n\ts_mov_b32 %[cnt], 0\n\tLou_poll%=:\n\ts_lshr_b32 %[t], %[j], 1\n\ts_cmp_lt_u32 %[t], %[av]\n\ts_cbranch_scc1 Lou_load%=\n\t" \
          "s_sleep 4\n\ts_lshl_b32 %[t], %[av], 7\n\ts_add_u32 %[t], %[t], %[fo]\n\tbuffer_load_dword %[fl], %[vf], %[rf], %[t] offen sc1\n\tbuffer_load_dword v48, off, %[rf], %[tmo] sc1\n\ts_waitcnt vmcnt(0)\n\t" \
          "v_cmp_gt_u32 vcc, 8, %[fl]\n\ts_nop 0\n\ts_ff1_i32_b64 %[t], vcc\n\ts_add_u32 %[av], %[av], %[t]\n\tv_readfirstlane_b32 vcc_lo, v48\n\ts_add_u32 %[cnt], %[cnt], 1\n\t" \
          "s_lshr_b32 vcc_hi, %[cnt], 15\n\ts_or_b32 vcc_lo, vcc_lo, vcc_hi\n\ts_cbranch_scc0 Lou_poll%=\n\tv_mov_b32 v48, 7\n\tbuffer_store_dword v48, off, %[rf], %[tmo] sc1\n\ts_mov_b32 %[av], 64\n\t" \
          "Lou_load%=:\n\tbuffer_load_dwordx4 v[64:67], %[vo0], s[40:43], %[pg0] offen sc1\n\tbuffer_load_dwordx4 v[68:71], %[vo0], s[40:43], %[pg0] offen offset:1024 sc1\n\tbuffer_load_dwordx4 v[72:75], %[vo0], s[40:43], %[pg0] offen offset:2048 sc1\n\tbuffer_load_dwordx4 v[76:79], %[vo0], s[40:43], %[pg0] offen offset:3072 sc1\n\tbuffer_load_dwordx4 v[80:83], %[vo1], s[40:43], %[pg0] offen sc1\n\t" \
          "buffer_load_dwordx4 v[84:87], %[vo1], s[40:43], %[pg0] offen offset:1024 sc1\n\tbuffer_load_dwordx4 v[88:91], %[vo1], s[40:43], %[pg0] offen offset:2048 sc1\n\tbuffer_load_dwordx4 v[92:95], %[vo1], s[40:43], %[pg0] offen offset:3072 sc1\n\tbuffer_load_dwordx4 v[96:99], %[vo0], s[40:43], %[pg1] offen sc1\n\tbuffer_load_dwordx4 v[100:103], %[vo0], s[40:43], %[pg1] offen offset:1024 sc1\n\tbuffer_load_dwordx4 v[104:107], %[vo0], s[40:43], %[pg1] offen offset:2048 sc1\n\t" \
          "buffer_load_dwordx4 v[108:111], %[vo0], s[40:43], %[pg1] offen offset:3072 sc1\n\tbuffer_load_dwordx4 v[112:115], %[vo1], s[40:43], %[pg1] offen sc1\n\tbuffer_load_dwordx4 v[116:119], %[vo1], s[40:43], %[pg1] offen offset:1024 sc1\n\tbuffer_load_dwordx4 v[120:123], %[vo1], s[40:43], %[pg1] offen offset:2048 sc1\n\tbuffer_load_dwordx4 v[124:127], %[vo1], s[40:43], %[pg1] offen offset:3072 sc1\n\tLou_top%=:\n\t" \
          "s_lshl_b32 %[t], %[av], 7\n\ts_add_u32 %[t], %[t], %[fo]\n\tbuffer_load_dword %[fl], %[vf], %[rf], %[t] offen sc1\n\tds_read_b128 v[32:35], %[own] offset:8192\n\tds_read_b128 v[36:39], %[own] offset:9216\n\tds_read_b128 v[40:43], %[own] offset:10240\n\t" \
          "s_waitcnt vmcnt(16) lgkmcnt(2)\n\tv_mfma_f32_16x16x32_bf16 v[48:51], a[0:3], v[64:67], 0\n\tds_read_b128 v[44:47], %[own] offset:11264\n\tv_mfma_f32_16x16x32_bf16 v[52:55], a[4:7], v[64:67], 0\n\tv_mfma_f32_16x16x32_bf16 v[56:59], v[128:131], v[64:67], 0\n\tv_mfma_f32_16x16x32_bf16 v[60:63], v[32:35], v[64:67], 0\n\t" \
          "buffer_load_dwordx4 v[64:67], %[vo0], s[40:43], %[pg2] offen sc1\n\ts_waitcnt vmcnt(16) lgkmcnt(2)\n\tv_mfma_f32_16x16x32_bf16 v[48:51], a[8:11], v[68:71], v[48:51]\n\tds_read_b128 v[32:35], %[own] offset:12288\n\tv_mfma_f32_16x16x32_bf16 v[52:55], a[12:15], v[68:71], v[52:55]\n\tv_mfma_f32_16x16x32_bf16 v[56:59], v[132:135], v[68:71], v[56:59]\n\t" \
          "v_mfma_f32_16x16x32_bf16 v[60:63], v[36:39], v[68:71], v[60:63]\n\tbuffer_load_dwordx4 v[68:71], %[vo0], s[40:43], %[pg2] offen offset:1024 sc1\n\ts_waitcnt vmcnt(16) lgkmcnt(2)\n\tv_mfma_f32_16x16x32_bf16 v[48:51], a[16:19], v[72:75], v[48:51]\n\tds_read_b128 v[36:39], %[own] offset:13312\n\tv_mfma_f32_16x16x32_bf16 v[52:55], a[20:23], v[72:75], v[52:55]\n\t" \
          "v_mfma_f32_16x16x32_bf16 v[56:59], v[136:139], v[72:75], v[56:59]\n\tv_mfma_f32_16x16x32_bf16 v[60:63], v[40:43], v[72:75], v[60:63]\n\tbuffer_load_dwordx4 v[72:75], %[vo0], s[40:43], %[pg2] offen offset:2048 sc1\n\ts_waitcnt vmcnt(16) lgkmcnt(2)\n\tv_mfma_f32_16x16x32_bf16 v[48:51], a[24:27], v[76:79], v[48:51]\n\tds_read_b128 v[40:43], %[own] offset:14336\n\t" \
          "v_mfma_f32_16x16x32_bf16 v[52:55], a[28:31], v[76:79], v[52:55]\n\tv_mfma_f32_16x16x32_bf16 v[56:59], v[140:143], v[76:79], v[56:59]\n\tv_mfma_f32_16x16x32_bf16 v[60:63], v[44:47], v[76:79], v[60:63]\n\tbuffer_load_dwordx4 v[76:79], %[vo0], s[40:43], %[pg2] offen offset:3072 sc1\n\ts_waitcnt vmcnt(16) lgkmcnt(2)\n\tv_mfma_f32_16x16x32_bf16 v[48:51], a[32:35], v[80:83], v[48:51]\n\t" \
          "ds_read_b128 v[44:47], %[own] offset:15360\n\tv_mfma_f32_16x16x32_bf16 v[52:55], a[36:39], v[80:83], v[52:55]\n\tv_mfma_f32_16x16x32_bf16 v[56:59], v[144:147], v[80:83], v[56:59]\n\tv_mfma_f32_16x16x32_bf16 v[60:63], v[32:35], v[80:83], v[60:63]\n\tbuffer_load_dwordx4 v[80:83], %[vo1], s[40:43], %[pg2] offen sc1\n\ts_waitcnt vmcnt(16) lgkmcnt(2)\n\t" \
          "v_mfma_f32_16x16x32_bf16 v[48:51], a[40:43], v[84:87], v[48:51]\n\tds_read_b128 v[32:35], %[own] offset:16384\n\tv_mfma_f32_16x16x32_bf16 v[52:55], a[44:47], v[84:87], v[52:55]\n\tv_mfma_f32_16x16x32_bf16 v[56:59], v[148:151], v[84:87], v[56:59]\n\tv_mfma_f32_16x16x32_bf16 v[60:63], v[36:39], v[84:87], v[60:63]\n\tbuffer_load_dwordx4 v[84:87], %[vo1], s[40:43], %[pg2] offen offset:1024 sc1\n\t" \
          "s_waitcnt vmcnt(16) lgkmcnt(2)\n\tv_mfma_f32_16x16x32_bf16 v[48:51], a[48:51], v[88:91], v[48:51]\n\tds_read_b128 v[36:39], %[own] offset:17408\n\tv_mfma_f32_16x16x32_bf16 v[52:55], a[52:55], v[88:91], v[52:55]\n\tv_mfma_f32_16x16x32_bf16 v[56:59], v[152:155], v[88:91], v[56:59]\n\tv_mfma_f32_16x16x32_bf16 v[60:63], v[40:43], v[88:91], v[60:63]\n\t" \
          "buffer_load_dwordx4 v[88:91], %[vo1], s[40:43], %[pg2] offen offset:2048 sc1\n\ts_waitcnt vmcnt(16) lgkmcnt(2)\n\tv_mfma_f32_16x16x32_bf16 v[48:51], a[56:59], v[92:95], v[48:51]\n\tds_read_b128 v[40:43], %[own] offset:18432\n\tv_mfma_f32_16x16x32_bf16 v[52:55], a[60:63], v[92:95], v[52:55]\n\tv_mfma_f32_16x16x32_bf16 v[56:59], v[156:159], v[92:95], v[56:59]\n\t" \
          "v_mfma_f32_16x16x32_bf16 v[60:63], v[44:47], v[92:95], v[60:63]\n\tbuffer_load_dwordx4 v[92:95], %[vo1], s[40:43], %[pg2] offen offset:3072 sc1\n\ts_waitcnt vmcnt(16) lgkmcnt(2)\n\tv_mfma_f32_16x16x32_bf16 v[48:51], a[64:67], v[96:99], v[48:51]\n\tds_read_b128 v[44:47], %[own] offset:19456\n\tv_mfma_f32_16x16x32_bf16 v[52:55], a[68:71], v[96:99], v[52:55]\n\t" \
          "v_mfma_f32_16x16x32_bf16 v[56:59], v[160:163], v[96:99], v[56:59]\n\tv_mfma_f32_16x16x32_bf16 v[60:63], v[32:35], v[96:99], v[60:63]\n\tbuffer_load_dwordx4 v[96:99], %[vo0], s[40:43], %[pg3] offen sc1\n\ts_waitcnt vmcnt(16) lgkmcnt(2)\n\tv_mfma_f32_16x16x32_bf16 v[48:51], a[72:75], v[100:103], v[48:51]\n\tds_read_b128 v[32:35], %[own] offset:20480\n\t" \
          "v_mfma_f32_16x16x32_bf16 v[52:55], a[76:79], v[100:103], v[52:55]\n\tv_mfma_f32_16x16x32_bf16 v[56:59], v[164:167], v[100:103], v[56:59]\n\tv_mfma_f32_16x16x32_bf16 v[60:63], v[36:39], v[100:103], v[60:63]\n\tbuffer_load_dwordx4 v[100:103], %[vo0], s[40:43], %[pg3] offen offset:1024 sc1\n\ts_waitcnt vmcnt(16) lgkmcnt(2)\n\tv_mfma_f32_16x16x32_bf16 v[48:51], a[80:83], v[104:107], v[48:51]\n\t" \
          "ds_read_b128 v[36:39], %[own] offset:21504\n\tv_mfma_f32_16x16x32_bf16 v[52:55], a[84:87], v[104:107], v[52:55]\n\tv_mfma_f32_16x16x32_bf16 v[56:59], v[168:171], v[104:107], v[56:59]\n\tv_mfma_f32_16x16x32_bf16 v[60:63], v[40:43], v[104:107], v[60:63]\n\tbuffer_load_dwordx4 v[104:107], %[vo0], s[40:43], %[pg3] offen offset:2048 sc1\n\ts_waitcnt vmcnt(16) lgkmcnt(2)\n\t" \
          "v_mfma_f32_16x16x32_bf16 v[48:51], a[88:91], v[108:111], v[48:51]\n\tds_read_b128 v[40:43], %[own] offset:22528\n\tv_mfma_f32_16x16x32_bf16 v[52:55], a[92:95], v[108:111], v[52:55]\n\tv_mfma_f32_16x16x32_bf16 v[56:59], v[172:175], v[108:111], v[56:59]\n\tv_mfma_f32_16x16x32_bf16 v[60:63], v[44:47], v[108:111], v[60:63]\n\tbuffer_load_dwordx4 v[108:111], %[vo0], s[40:43], %[pg3] offen offset:3072 sc1\n\t" \
          "s_waitcnt vmcnt(16) lgkmcnt(2)\n\tv_mfma_f32_16x16x32_bf16 v[48:51], a[96:99], v[112:115], v[48:51]\n\tds_read_b128 v[44:47], %[own] offset:23552\n\tv_mfma_f32_16x16x32_bf16 v[52:55], a[100:103], v[112:115], v[52:55]\n\tv_mfma_f32_16x16x32_bf16 v[56:59], v[176:179], v[112:115], v[56:59]\n\tv_mfma_f32_16x16x32_bf16 v[60:63], v[32:35], v[112:115], v[60:63]\n\t" \
          "buffer_load_dwordx4 v[112:115], %[vo1], s[40:43], %[pg3] offen sc1\n\ts_waitcnt vmcnt(16) lgkmcnt(2)\n\tv_mfma_f32_16x16x32_bf16 v[48:51], a[104:107], v[116:119], v[48:51]\n\tds_read_b128 v[32:35], %[own] offset:24576\n\tv_mfma_f32_16x16x32_bf16 v[52:55], a[108:111], v[116:119], v[52:55]\n\tv_mfma_f32_16x16x32_bf16 v[56:59], v[180:183], v[116:119], v[56:59]\n\t" \
          "v_mfma_f32_16x16x32_bf16 v[60:63], v[36:39], v[116:119], v[60:63]\n\tbuffer_load_dwordx4 v[116:119], %[vo1], s[40:43], %[pg3] offen offset:1024 sc1\n\ts_waitcnt vmcnt(16) lgkmcnt(2)\n\tv_mfma_f32_16x16x32_bf16 v[48:51], a[112:115], v[120:123], v[48:51]\n\tds_read_b128 v[36:39], %[own] offset:25600\n\tv_mfma_f32_16x16x32_bf16 v[52:55], a[116:119], v[120:123], v[52:55]\n\t" \
          "v_mfma_f32_16x16x32_bf16 v[56:59], v[184:187], v[120:123], v[56:59]\n\tv_mfma_f32_16x16x32_bf16 v[60:63], v[40:43], v[120:123], v[60:63]\n\tbuffer_load_dwordx4 v[120:123], %[vo1], s[40:43], %[pg3] offen offset:2048 sc1\n\ts_waitcnt vmcnt(16) lgkmcnt(2)\n\tv_mfma_f32_16x16x32_bf16 v[48:51], a[120:123], v[124:127], v[48:51]\n\tds_read_b128 v[40:43], %[own] offset:26624\n\t" \
          "v_mfma_f32_16x16x32_bf16 v[52:55], a[124:127], v[124:127], v[52:55]\n\tv_mfma_f32_16x16x32_bf16 v[56:59], v[188:191], v[124:127], v[56:59]\n\tv_mfma_f32_16x16x32_bf16 v[60:63], v[44:47], v[124:127], v[60:63]\n\tbuffer_load_dwordx4 v[124:127], %[vo1], s[40:43], %[pg3] offen offset:3072 sc1\n\ts_waitcnt vmcnt(15) lgkmcnt(2)\n\tv_mfma_f32_16x16x32_bf16 v[48:51], a[128:131], v[64:67], v[48:51]\n\t" \
          "ds_read_b128 v[44:47], %[own] offset:27648\n\tv_mfma_f32_16x16x32_bf16 v[52:55], a[132:135], v[64:67], v[52:55]\n\tv_cmp_gt_u32 vcc, 8, %[fl]\n\ts_nop 0\n\ts_ff1_i32_b64 %[t], vcc\n\ts_add_u32 %[av], %[av], %[t]\n\t" \
          "v_mfma_f32_16x16x32_bf16 v[56:59], v[192:195], v[64:67], v[56:59]\n\ts_lshl_b32 %[oo], %[j], 16\n\ts_cmp_lt_u32 %[j], %[j1]\n\ts_cselect_b32 %[t], 2, 4\n\ts_add_u32 %[j], %[j], %[t]\n\ts_lshl_b32 %[t], %[t], 14\n\t" \
          "s_add_u32 %[pg0], %[pg0], %[t]\n\ts_add_u32 %[pg1], %[pg1], %[t]\n\ts_add_u32 %[pg2], %[pg2], %[t]\n\ts_add_u32 %[pg3], %[pg3], %[t]\n\ts_lshr_b32 %[t], %[j], 1\n\ts_cmp_lt_u32 %[t], %[av]\n\t" \
          "s_cselect_b32 s46, s42, 0\n\tv_mfma_f32_16x16x32_bf16 v[60:63], v[32:35], v[64:67], v[60:63]\n\tbuffer_load_dwordx4 v[64:67], %[vo0], s[44:47], %[pg0] offen sc1\n\ts_waitcnt vmcnt(15) lgkmcnt(2)\n\tv_mfma_f32_16x16x32_bf16 v[48:51], a[136:139], v[68:71], v[48:51]\n\tds_read_b128 v[32:35], %[own] offset:28672\n\t" \
          "v_mfma_f32_16x16x32_bf16 v[52:55], a[140:143], v[68:71], v[52:55]\n\tv_mfma_f32_16x16x32_bf16 v[56:59], v[196:199], v[68:71], v[56:59]\n\tv_mfma_f32_16x16x32_bf16 v[60:63], v[36:39], v[68:71], v[60:63]\n\tbuffer_load_dwordx4 v[68:71], %[vo0], s[44:47], %[pg0] offen offset:1024 sc1\n\ts_waitcnt vmcnt(15) lgkmcnt(2)\n\tv_mfma_f32_16x16x32_bf16 v[48:51], a[144:147], v[72:75], v[48:51]\n\t" \
          "ds_read_b128 v[36:39], %[own] offset:29696\n\tv_mfma_f32_16x16x32_bf16 v[52:55], a[148:151], v[72:75], v[52:55]\n\tv_mfma_f32_16x16x32_bf16 v[56:59], v[200:203], v[72:75], v[56:59]\n\tv_mfma_f32_16x16x32_bf16 v[60:63], v[40:43], v[72:75], v[60:63]\n\tbuffer_load_dwordx4 v[72:75], %[vo0], s[44:47], %[pg0] offen offset:2048 sc1\n\ts_waitcnt vmcnt(15) lgkmcnt(2)\n\t" \
          "v_mfma_f32_16x16x32_bf16 v[48:51], a[152:155], v[76:79], v[48:51]\n\tds_read_b128 v[40:43], %[own] offset:30720\n\tv_mfma_f32_16x16x32_bf16 v[52:55], a[156:159], v[76:79], v[52:55]\n\tv_mfma_f32_16x16x32_bf16 v[56:59], v[204:207], v[76:79], v[56:59]\n\tv_mfma_f32_16x16x32_bf16 v[60:63], v[44:47], v[76:79], v[60:63]\n\tbuffer_load_dwordx4 v[76:79], %[vo0], s[44:47], %[pg0] offen offset:3072 sc1\n\t" \
          "s_waitcnt vmcnt(15) lgkmcnt(2)\n\tv_mfma_f32_16x16x32_bf16 v[48:51], a[160:163], v[80:83], v[48:51]\n\tds_read_b128 v[44:47], %[own] offset:31744\n\tv_mfma_f32_16x16x32_bf16 v[52:55], a[164:167], v[80:83], v[52:55]\n\tv_mfma_f32_16x16x32_bf16 v[56:59], v[208:211], v[80:83], v[56:59]\n\tv_mfma_f32_16x16x32_bf16 v[60:63], v[32:35], v[80:83], v[60:63]\n\t" \
          "buffer_load_dwordx4 v[80:83], %[vo1], s[44:47], %[pg0] offen sc1\n\ts_waitcnt vmcnt(15) lgkmcnt(2)\n\tv_mfma_f32_16x16x32_bf16 v[48:51], a[168:171], v[84:87], v[48:51]\n\tds_read_b128 v[32:35], %[own] offset:0\n\tv_mfma_f32_16x16x32_bf16 v[52:55], a[172:175], v[84:87], v[52:55]\n\tv_mfma_f32_16x16x32_bf16 v[56:59], v[212:215], v[84:87], v[56:59]\n\t" \
          "v_mfma_f32_16x16x32_bf16 v[60:63], v[36:39], v[84:87], v[60:63]\n\tbuffer_load_dwordx4 v[84:87], %[vo1], s[44:47], %[pg0] offen offset:1024 sc1\n\ts_waitcnt vmcnt(15) lgkmcnt(2)\n\tv_mfma_f32_16x16x32_bf16 v[48:51], a[176:179], v[88:91], v[48:51]\n\tds_read_b128 v[36:39], %[own] offset:1024\n\tv_mfma_f32_16x16x32_bf16 v[52:55], a[180:183], v[88:91], v[52:55]\n\t" \
          "v_mfma_f32_16x16x32_bf16 v[56:59], v[216:219], v[88:91], v[56:59]\n\tv_mfma_f32_16x16x32_bf16 v[60:63], v[40:43], v[88:91], v[60:63]\n\tbuffer_load_dwordx4 v[88:91], %[vo1], s[44:47], %[pg0] offen offset:2048 sc1\n\ts_waitcnt vmcnt(15) lgkmcnt(2)\n\tv_mfma_f32_16x16x32_bf16 v[48:51], a[184:187], v[92:95], v[48:51]\n\tds_read_b128 v[40:43], %[own] offset:2048\n\t" \
          "v_mfma_f32_16x16x32_bf16 v[52:55], a[188:191], v[92:95], v[52:55]\n\tv_mfma_f32_16x16x32_bf16 v[56:59], v[220:223], v[92:95], v[56:59]\n\tv_mfma_f32_16x16x32_bf16 v[60:63], v[44:47], v[92:95], v[60:63]\n\tbuffer_load_dwordx4 v[92:95], %[vo1], s[44:47], %[pg0] offen offset:3072 sc1\n\ts_waitcnt vmcnt(15) lgkmcnt(2)\n\tv_mfma_f32_16x16x32_bf16 v[48:51], a[192:195], v[96:99], v[48:51]\n\t" \
          "ds_read_b128 v[44:47], %[own] offset:3072\n\tv_mfma_f32_16x16x32_bf16 v[52:55], a[196:199], v[96:99], v[52:55]\n\tv_mfma_f32_16x16x32_bf16 v[56:59], v[224:227], v[96:99], v[56:59]\n\tv_mfma_f32_16x16x32_bf16 v[60:63], v[32:35], v[96:99], v[60:63]\n\tbuffer_load_dwordx4 v[96:99], %[vo0], s[44:47], %[pg1] offen sc1\n\ts_waitcnt vmcnt(15) lgkmcnt(2)\n\t" \
          "v_mfma_f32_16x16x32_bf16 v[48:51], a[200:203], v[100:103], v[48:51]\n\tds_read_b128 v[32:35], %[own] offset:4096\n\tv_mfma_f32_16x16x32_bf16 v[52:55], a[204:207], v[100:103], v[52:55]\n\tv_mfma_f32_16x16x32_bf16 v[56:59], v[228:231], v[100:103], v[56:59]\n\tv_mfma_f32_16x16x32_bf16 v[60:63], v[36:39], v[100:103], v[60:63]\n\tbuffer_load_dwordx4 v[100:103], %[vo0], s[44:47], %[pg1] offen offset:1024 sc1\n\t" \
          "s_waitcnt vmcnt(15) lgkmcnt(2)\n\tv_mfma_f32_16x16x32_bf16 v[48:51], a[208:211], v[104:107], v[48:51]\n\tds_read_b128 v[36:39], %[own] offset:5120\n\tv_mfma_f32_16x16x32_bf16 v[52:55], a[212:215], v[104:107], v[52:55]\n\tv_mfma_f32_16x16x32_bf16 v[56:59], v[232:235], v[104:107], v[56:59]\n\tv_mfma_f32_16x16x32_bf16 v[60:63], v[40:43], v[104:107], v[60:63]\n\t" \
          "buffer_load_dwordx4 v[104:107], %[vo0], s[44:47], %[pg1] offen offset:2048 sc1\n\ts_waitcnt vmcnt(15) lgkmcnt(2)\n\tv_mfma_f32_16x16x32_bf16 v[48:51], a[216:219], v[108:111], v[48:51]\n\tds_read_b128 v[40:43], %[own] offset:6144\n\tv_mfma_f32_16x16x32_bf16 v[52:55], a[220:223], v[108:111], v[52:55]\n\tv_mfma_f32_16x16x32_bf16 v[56:59], v[236:239], v[108:111], v[56:59]\n\t" \
          "v_mfma_f32_16x16x32_bf16 v[60:63], v[44:47], v[108:111], v[60:63]\n\tbuffer_load_dwordx4 v[108:111], %[vo0], s[44:47], %[pg1] offen offset:3072 sc1\n\ts_waitcnt vmcnt(15) lgkmcnt(2)\n\tv_mfma_f32_16x16x32_bf16 v[48:51], a[224:227], v[112:115], v[48:51]\n\tds_read_b128 v[44:47], %[own] offset:7168\n\tv_mfma_f32_16x16x32_bf16 v[52:55], a[228:231], v[112:115], v[52:55]\n\t" \
          "v_mfma_f32_16x16x32_bf16 v[56:59], v[240:243], v[112:115], v[56:59]\n\tv_mfma_f32_16x16x32_bf16 v[60:63], v[32:35], v[112:115], v[60:63]\n\tbuffer_load_dwordx4 v[112:115], %[vo1], s[44:47], %[pg1] offen sc1\n\ts_waitcnt vmcnt(15) lgkmcnt(2)\n\tv_mfma_f32_16x16x32_bf16 v[48:51], a[232:235], v[116:119], v[48:51]\n\tv_mfma_f32_16x16x32_bf16 v[52:55], a[236:239], v[116:119], v[52:55]\n\t" \
          "v_mfma_f32_16x16x32_bf16 v[56:59], v[244:247], v[116:119], v[56:59]\n\tv_mfma_f32_16x16x32_bf16 v[60:63], v[36:39], v[116:119], v[60:63]\n\tbuffer_load_dwordx4 v[116:119], %[vo1], s[44:47], %[pg1] offen offset:1024 sc1\n\ts_waitcnt vmcnt(15) lgkmcnt(1)\n\tv_mfma_f32_16x16x32_bf16 v[48:51], a[240:243], v[120:123], v[48:51]\n\tv_mfma_f32_16x16x32_bf16 v[52:55], a[244:247], v[120:123], v[52:55]\n\t" \
          "v_mfma_f32_16x16x32_bf16 v[56:59], v[248:251], v[120:123], v[56:59]\n\tv_mfma_f32_16x16x32_bf16 v[60:63], v[40:43], v[120:123], v[60:63]\n\tbuffer_load_dwordx4 v[120:123], %[vo1], s[44:47], %[pg1] offen offset:2048 sc1\n\ts_waitcnt vmcnt(15) lgkmcnt(0)\n\tv_mfma_f32_16x16x32_bf16 v[48:51], a[248:251], v[124:127], v[48:51]\n\tv_mfma_f32_16x16x32_bf16 v[52:55], a[252:255], v[124:127], v[52:55]\n\t" \
          "v_mfma_f32_16x16x32_bf16 v[56:59], v[252:255], v[124:127], v[56:59]\n\tv_mfma_f32_16x16x32_bf16 v[60:63], v[44:47], v[124:127], v[60:63]\n\tbuffer_load_dwordx4 v[124:127], %[vo1], s[44:47], %[pg1] offen offset:3072 sc1\n\ts_nop 6\n\tds_write_b128 %[rw_], v[48:51] offset:0\n\tds_write_b128 %[rw_], v[52:55] offset:1024\n\t" \
          "ds_write_b128 %[rw_], v[56:59] offset:2048\n\tds_write_b128 %[rw_], v[60:63] offset:3072\n\ts_waitcnt lgkmcnt(0)\n\ts_barrier\n\tds_read_b128 v[32:35], %[rr0] offset:0\n\tds_read_b128 v[36:39], %[rr0] offset:40960\n\t" \
          "ds_read_b128 v[40:43], %[rr1] offset:0\n\tds_read_b128 v[44:47], %[rr1] offset:40960\n\tv_xor_b32 %[rw_], 0x1000, %[rw_]\n\tv_xor_b32 %[rr0], 0x1000, %[rr0]\n\tv_xor_b32 %[rr1], 0x1000, %[rr1]\n\ts_waitcnt lgkmcnt(0)\n\t" \
          "v_pk_add_f32 v[32:33], v[32:33], v[36:37]\n\tv_pk_add_f32 v[34:35], v[34:35], v[38:39]\n\tv_pk_add_f32 v[40:41], v[40:41], v[44:45]\n\tv_pk_add_f32 v[42:43], v[42:43], v[46:47]\n\tv_pk_add_f32 v[32:33], v[32:33], v[40:41]\n\tv_pk_add_f32 v[34:35], v[34:35], v[42:43]\n\t" \
          "s_nop 0\n\tv_cvt_pk_bf16_f32 v44, v32, v33\n\tv_cvt_pk_bf16_f32 v45, v34, v35\n\tbuffer_store_dwordx2 v[44:45], %[so], %[rc], %[oo] offen\n\ts_cmp_lg_u32 s46, 0\n\ts_cbranch_scc1 Lou_top%=\n\t" \
          "s_cmp_lt_u32 %[j], 128\n\ts_cbranch_scc1 Lou_wait%=\n\ts_waitcnt vmcnt(0)\n\t"
#define OU_CLOB "v32", "v33", "v34", "v35", "v36", "v37", "v38", "v39", "v40", "v41", "v42", "v43", "v44", "v45", "v46", "v47", "v48", "v49", "v50", "v51", "v52", "v53", "v54", "v55", "v56", "v57", "v58", "v59", "v60", "v61", "v62", "v63", "v64", "v65", "v66", "v67", "v68", "v69", "v70", "v71", "v72", "v73", "v74", "v75", "v76", "v77", "v78", "v79", "v80", "v81", "v82", "v83", "v84", "v85", "v86", "v87", "v88", "v89", "v90", "v91", "v92", "v93", "v94", "v95", "v96", "v97", "v98", "v99", "v100", "v101", "v102", "v103", "v104", "v105", "v106", "v107", "v108", "v109", "v110", "v111", "v112", "v113", "v114", "v115", "v116", "v117", "v118", "v119", "v120", "v121", "v122", "v123", "v124", "v125", "v126", "v127", "v128", "v129", "v130", "v131", "v132", "v133", "v134", "v135", "v136", "v137", "v138", "v139", "v140", "v141", "v142", "v143", "v144", "v145", "v146", "v147", "v148", "v149", "v150", "v151", "v152", "v153", "v154", "v155", "v156", "v157", "v158", "v159", "v160", "v161", "v162", "v163", "v164", "v165", "v166", "v167", "v168", "v169", "v170", "v171", "v172", "v173", "v174", "v175", "v176", "v177", "v178", "v179", "v180", "v181", "v182", "v183", "v184", "v185", "v186", "v187", "v188", "v189", "v190", "v191", "v192", "v193", "v194", "v195", "v196", "v197", "v198", "v199", "v200", "v201", "v202", "v203", "v204", "v205", "v206", "v207", "v208", "v209", "v210", "v211", "v212", "v213", "v214", "v215", "v216", "v217", "v218", "v219", "v220", "v221", "v222", "v223", "v224", "v225", "v226", "v227", "v228", "v229", "v230", "v231", "v232", "v233", "v234", "v235", "v236", "v237", "v238", "v239", "v240", "v241", "v242", "v243", "v244", "v245", "v246", "v247", "v248", "v249", "v250", "v251", "v252", "v253", "v254", "v255", "a0", "a1", "a2", "a3", "a4", "a5", "a6", "a7", "a8", "a9", "a10", "a11", "a12", "a13", "a14", "a15", "a16", "a17", "a18", "a19", "a20", "a21", "a22", "a23", "a24", "a25", "a26", "a27", "a28", "a29", "a30", "a31", "a32", "a33", "a34", "a35", "a36", "a37", "a38", "a39", "a40", "a41", "a42", "a43", "a44", "a45", "a46", "a47", "a48", "a49", "a50", "a51", "a52", "a53", "a54", "a55", "a56", "a57", "a58", "a59", "a60", "a61", "a62", "a63", "a64", "a65", "a66", "a67", "a68", "a69", "a70", "a71", "a72", "a73", "a74", "a75", "a76", "a77", "a78", "a79", "a80", "a81", "a82", "a83", "a84", "a85", "a86", "a87", "a88", "a89", "a90", "a91", "a92", "a93", "a94", "a95", "a96", "a97", "a98", "a99", "a100", "a101", "a102", "a103", "a104", "a105", "a106", "a107", "a108", "a109", "a110", "a111", "a112", "a113", "a114", "a115", "a116", "a117", "a118", "a119", "a120", "a121", "a122", "a123", "a124", "a125", "a126", "a127", "a128", "a129", "a130", "a131", "a132", "a133", "a134", "a135", "a136", "a137", "a138", "a139", "a140", "a141", "a142", "a143", "a144", "a145", "a146", "a147", "a148", "a149", "a150", "a151", "a152", "a153", "a154", "a155", "a156", "a157", "a158", "a159", "a160", "a161", "a162", "a163", "a164", "a165", "a166", "a167", "a168", "a169", "a170", "a171", "a172", "a173", "a174", "a175", "a176", "a177", "a178", "a179", "a180", "a181", "a182", "a183", "a184", "a185", "a186", "a187", "a188", "a189", "a190", "a191", "a192", "a193", "a194", "a195", "a196", "a197", "a198", "a199", "a200", "a201", "a202", "a203", "a204", "a205", "a206", "a207", "a208", "a209", "a210", "a211", "a212", "a213", "a214", "a215", "a216", "a217", "a218", "a219", "a220", "a221", "a222", "a223", "a224", "a225", "a226", "a227", "a228", "a229", "a230", "a231", "a232", "a233", "a234", "a235", "a236", "a237", "a238", "a239", "a240", "a241", "a242", "a243", "a244", "a245", "a246", "a247", "a248", "a249", "a250", "a251", "a252", "a253", "a254", "a255", "s40", "s41", "s42", "s43", "s44", "s45", "s46", "s47"
__device__ __forceinline__ void out_unit(const FParams& F, unsigned long long parq, int set, int un, int w) {
  const int lane = opaque_lane(), l15 = lane & 15, lq = lane >> 4, r = un & 7;
  const __amdgpu_buffer_rsrc_t rw = __builtin_amdgcn_make_buffer_rsrc(wg::uni(F.Wo + 64 * un), 0, HDA * DM * 2 - 128 * un, 0x00020000);
  const __amdgpu_buffer_rsrc_t rc = __builtin_amdgcn_make_buffer_rsrc(wg::uni(F.out + 64 * un), 0, S_ * 2 * DM * 2 - 128 * un, 0x00020000);
  const __amdgpu_buffer_rsrc_t rf = __builtin_amdgcn_make_buffer_rsrc(F.ctl, 0, sizeof(Ctl), 0x00020000);
  const int own = w * 40960 + lane * 16;
  int j = (set < 2 ? 2 * OSEG0 : F.oj1) + set, av = OSEG0, pg[4], t, cnt, oo, fl, rw_ = own + 32768, rr0 = 32768 + (l15 >> 2) * 1024 + ((l15 & 3) * 16 + 4 * w + lq) * 16, rr1 = rr0 + 81920;
#pragma unroll
  for (int i = 0; i < 4; ++i) pg[i] = __builtin_amdgcn_readfirstlane(qoff(parq, 4 * w + (i ^ (r >> 1))) + j * QBLK);
  const int rt = w * 40960 + (8 * lq + (l15 >> 2)) * 128 + (((l15 >> 3) | ((lq & 1) << 1)) << 5) + (l15 & 3) * 8, vw = (lane >> 3) * 2048 + (((lane & 7) ^ ((lq & 1) << 1)) << 4);
  asm volatile(OU_BODY
    : [j] "+s"(j), [av] "+s"(av), [pg0] "+s"(pg[0]), [pg1] "+s"(pg[1]), [pg2] "+s"(pg[2]), [pg3] "+s"(pg[3]), [t] "=&s"(t), [cnt] "=&s"(cnt), [oo] "=&s"(oo), [fl] "=&v"(fl), [rw_] "+v"(rw_), [rr0] "+v"(rr0), [rr1] "+v"(rr1)
    : [vo0] "v"(lane * 16 + (r & 1) * 4096), [vo1] "v"(lane * 16 + (~r & 1) * 4096), [vw0] "v"(vw), [vw1] "v"(vw ^ 64), [rt0] "v"(rt), [rt1] "v"(rt ^ 32), [rt2] "v"(rt ^ 64), [rt3] "v"(rt ^ 96), [own] "v"(own),
      [vf] "v"(lane < 8 ? lane * 128 : 0x7ffffff0), [so] "v"((4 * w + lq) * 4096 + l15 * 8),
      [qa] "s"(F.qkvA), [qs] "s"(QSIZE), [rw] "s"(rw), [rc] "s"(rc), [rf] "s"(rf), [wl] "s"(w * 40960), [wo] "s"((1024 * w + 128 * r) * 2048), [fo] "s"((int)offsetof(Ctl, cntO)), [tmo] "s"((int)offsetof(Ctl, tmo)), [j1] "s"(F.oj1)
    : OU_CLOB, "vcc", "scc", "memory");
}

__device__ __forceinline__ void attnB_task(const FParams& F, unsigned long long parq, int r, int pg, u16* vs  , int w) {
  const int lane = opaque_lane(), l15 = lane & 15, lq = lane >> 4;
  const __amdgpu_buffer_rsrc_t rs = __builtin_amdgcn_make_buffer_rsrc(F.qkvA, 0, QSIZE, 0x00020000);
  const int so = 8 * r * QBLK + w * 2048, oq = so + qoff(parq, 48 + pg), ok = so + qoff(parq, 52 + pg), ov = so + qoff(parq, 56 + pg), p0 = r ? -QBLK : 0;
  v4u q[2][8], k[2][9], v[2][9];
#pragma unroll
  for (int hh = 0; hh < 2; ++hh) {
#pragma unroll
    for (int i = 0; i < 9; ++i) v[hh][i] = __builtin_amdgcn_raw_buffer_load_b128(rs, lane * 16 + hh * 1024, ov + (i ? (i - 1) * QBLK : p0), 0);
#pragma unroll
    for (int i = 0; i < 9; ++i) { k[hh][i] = __builtin_amdgcn_raw_buffer_load_b128(rs, lane * 16 + hh * 1024, ok + (i ? (i - 1) * QBLK : p0), 0); if (i < 8) q[hh][i] = __builtin_amdgcn_raw_buffer_load_b128(rs, lane * 16 + hh * 1024, oq + i * QBLK, 0); }
  }
  const float scale = 0.17677669529663687f, L2E = 1.4426950408889634f;
  const bool par = l15 & 1, ownA = l15 - 4 * lq - par >= 0, ownB = l15 - 4 * lq - par - 2 >= 0;
  const unsigned sa = par ? 0x01000c0cu : 0x0c0c0100u, sb = par ? 0x03020c0cu : 0x0c0c0302u, sz = 0x0c0c0c0cu;
  const unsigned sel[4] = {ownA ? sz : sa, ownB ? sz : sb, ownA ? sa : sz, ownB ? sb : sz};
  const u16* vp = &vs[(4 * lq + (l15 >> 2)) * 40 + 16 * (l15 & 1) + 2 * (l15 & 2)];
#pragma unroll
  for (int hh = 0; hh < 2; ++hh) {
    SB0;
#pragma unroll
    for (int i = 0; i < 9; ++i) *(v4u*)&vs[(16 * i + l15) * 40 + 8 * lq] = v[hh][i];
#pragma unroll
    for (int i = 0; i < 8; ++i) {
      const f32x4 z = {0.f, 0.f, 0.f, 0.f}, s0 = mf(k[hh][i], q[hh][i], z), s1 = mf(k[hh][i + 1], q[hh][i], z);
      float a = ownA ? (par ? s1[1] : s1[0]) : (par ? s0[1] : s0[0]), b = ownB ? (par ? s1[3] : s1[2]) : (par ? s0[3] : s0[2]);
      unsigned p = pk2(a, b);
      p = pk2(__uint_as_float(p << 16) * scale, __uint_as_float(p & 0xffff0000u) * scale);
      a = __uint_as_float(p << 16); b = __uint_as_float(p & 0xffff0000u);
      const bool va = i > 0 || r > 0 || ownA, vb = i > 0 || r > 0 || ownB;
      const float mx = xmax32(xmax16(fmaxf(va ? a : -INFINITY, vb ? b : -INFINITY)));
      a = va ? __builtin_amdgcn_exp2f((a - mx) * L2E) : 0.f; b = vb ? __builtin_amdgcn_exp2f((b - mx) * L2E) : 0.f;
      const float inv = __builtin_amdgcn_rcpf(xsum32(xsum16(a + b)));
      p = pk2(a * inv, b * inv);
      const v4u px = {__builtin_amdgcn_perm(0u, p, sel[0]), __builtin_amdgcn_perm(0u, p, sel[1]), __builtin_amdgcn_perm(0u, p, sel[2]), __builtin_amdgcn_perm(0u, p, sel[3])};
      f32x4 o[2];
#pragma unroll
      for (int ab = 0; ab < 2; ++ab) { const v2u lo = trrd(vp + 16 * i * 40 + 8 * ab), hi = trrd(vp + 16 * (i + 1) * 40 + 8 * ab); o[ab] = mf((v4u){lo.x, lo.y, hi.x, hi.y}, px, z); }
      __builtin_amdgcn_raw_buffer_store_b128((v4u){pk2(o[0][0], o[0][1]), pk2(o[0][2], o[0][3]), pk2(o[1][0], o[1][1]), pk2(o[1][2], o[1][3])}, rs, lane * 16 + hh * 1024, oq + i * QBLK, 16);
    }
  }
}

constexpr int NTASK = 240;
__constant__ unsigned short TASKS[NTASK] = {
  5120, 5121, 5122, 5123, 5124, 5125, 5126, 5127, 5128, 5129, 5130, 5131, 5132, 5133, 5134, 5135, 5136, 5137, 5138, 5139, 5140, 5141, 5142, 5143,
  3072, 3073, 3074, 3075, 3076, 3077, 3078, 3079, 3080, 3081, 3082, 3083, 3084, 3085, 3086, 3087, 3088, 3089, 3090, 3091, 3092, 3093, 3094, 3095,
  6144, 6145, 6146, 6147, 6148, 6149, 6150, 6151, 5144, 5145, 5146, 5147, 5148, 5149, 5150, 5151, 4096, 4097, 4098, 4099, 4100, 4101, 4102, 4103,
  4104, 4105, 4106, 4107, 4108, 4109, 4110, 4111, 4112, 4113, 4114, 4115, 4116, 4117, 4118, 4119, 6152, 6153, 6154, 6155, 6156, 6157, 6158, 6159,
  5152, 5153, 5154, 5155, 5156, 5157, 5158, 5159, 5160, 5161, 5162, 5163, 5164, 5165, 5166, 5167, 3096, 3097, 3098, 3099, 3100, 3101, 3102, 3103,
  3104, 3105, 3106, 3107, 3108, 3109, 3110, 3111, 5168, 5169, 5170, 5171, 5172, 5173, 5174, 5175, 4120, 4121, 4122, 4123, 4124, 4125, 4126, 4127,
  4128, 4129, 4130, 4131, 4132, 4133, 4134, 4135, 5176, 5177, 5178, 5179, 5180, 5181, 5182, 5183, 7168, 7169, 7170, 7171, 7172, 7173, 7174, 7175,
  7176, 7177, 7178, 7179, 7180, 7181, 7182, 7183, 7184, 7185, 7186, 7187, 7188, 7189, 7190, 7191, 7192, 7193, 7194, 7195, 7196, 7197, 7198, 7199,
  3112, 3113, 3114, 3115, 3116, 3117, 3118, 3119, 3120, 3121, 3122, 3123, 3124, 3125, 3126, 3127, 3128, 3129, 3130, 3131, 3132, 3133, 3134, 3135,
  4136, 4137, 4138, 4139, 4140, 4141, 4142, 4143, 4144, 4145, 4146, 4147, 4148, 4149, 4150, 4151, 4152, 4153, 4154, 4155, 4156, 4157, 4158, 4159,
};
__device__ __forceinline__ const FParams& args() {
  typedef const __attribute__((address_space(4))) FParams* KP;
  KP p = (KP)__builtin_amdgcn_kernarg_segment_ptr(); asm volatile("" : "+s"(p)); return *(const FParams*)p;
}
__device__ __forceinline__ void wait_until(unsigned t) { while ((int)(t - (unsigned)__builtin_amdgcn_s_memrealtime()) > 0) __builtin_amdgcn_s_sleep(8); }
template <int AUX = 0>
__device__ __forceinline__ void touch(const void* base, int bytes, int voff, int soff, char* lds) {
  __builtin_amdgcn_raw_ptr_buffer_load_lds(__builtin_amdgcn_make_buffer_rsrc((void*)base, 0, bytes, 0x00020000), (__attribute__((address_space(3))) void*)(lds + wg::LDS_USE + 1024), 4, voff, soff, 0, AUX);
}
constexpr int RECO = 32768;
__device__ __forceinline__ void setup(const FParams& F, int n, int w) {
  const int lane = opaque_lane(), tid = 64 * w + lane;
  const v4u ff = {0xffffffffu, 0xffffffffu, 0xffffffffu, 0xffffffffu}, ep = rdy_load(F);
  const __amdgpu_buffer_rsrc_t rs = __builtin_amdgcn_make_buffer_rsrc(F.ring, 0, QPAGE + QSIZE, 0x00020000);
  const int o = ((lane & 16) ? RSIZE + n * 128 : (1 + n) * QPAGE) + ((lane >> 3) & 1) * 8192 + (lane & 7) * 16;
  __builtin_amdgcn_raw_buffer_store_b128(ff, rs, tid < 32 ? o : 0x7ffffff0, 0, 16);
#pragma unroll 2
  for (int i = n * 256 + tid; i < RSIZE / 16; i += NPAGE * 256) {
    v4u v = ff;
    const int m = i - ROFF_M / 16;
    if ((unsigned)m < 2048u) {
      const int q = (m >> 10) * 512 + (m & 511);
      const u16* src = F.mem0 + (((q >> 4) & 1) * 4 + ((q >> 1) & 3)) * DM + 32 * (q >> 5) + 8 * ((q >> 3) & 1) + 4 * (q & 1);
#pragma unroll
      for (int p = 0; p < 4; ++p) v[p] = nosent(src[p] | ((unsigned)src[p + 16] << 16));
    }
    __builtin_amdgcn_raw_buffer_store_b128(v, rs, i * 16, 0, 16);
  }
  const __amdgpu_buffer_rsrc_t rk = __builtin_amdgcn_make_buffer_rsrc(F.kvm + SSEG0 * KVSEG, 0, (NSEG - SSEG0) * KVSEG, 0x00020000);
#pragma unroll
  for (int q = 0; q < 3; ++q) __builtin_amdgcn_raw_buffer_store_b128(ff, rk, ((q * NPAGE + n) * 256 + tid) * 16, 0, 16);
  __builtin_amdgcn_raw_buffer_store_b128((v4u){0u, 0u, 0u, 0u}, __builtin_amdgcn_make_buffer_rsrc(F.ctl, 0, sizeof(Ctl), 0x00020000), w == 3 ? (n * 64 + lane) * 16 : 0x7ffffff0, 0, 16);
  static_assert(sizeof(Ctl) <= NPAGE * 64 * 16 && (NSEG - SSEG0) * KVSEG <= 3 * NPAGE * 4096, "");
  const unsigned long long tag = rdy_tag(ep);
  asm volatile("s_waitcnt vmcnt(0)" ::: "memory");
  __syncthreads();
  if (w == 0) {
    unsigned best[4] = {~0u, ~0u, ~0u, ~0u};
#pragma unroll 1
    for (int r = 0; r < F.probes; ++r)
#pragma unroll
      for (int t = 0; t < 4; ++t) {
        if (t >= 2 && r) break;
        const unsigned long long t0 = __builtin_amdgcn_s_memtime();
        __builtin_amdgcn_raw_buffer_store_b128(ff, rs, (lane >> 3) == t ? o : 0x7ffffff0, 0, 16);
        asm volatile("s_waitcnt vmcnt(0)" ::: "memory");
        best[t] = min(best[t], (unsigned)(__builtin_amdgcn_s_memtime() - t0));
      }
    unsigned xcc; asm volatile("s_getreg_b32 %0, hwreg(HW_REG_XCC_ID)" : "=s"(xcc));
    const bool flip = ((xcc ^ (xcc - blockIdx.x + 1)) >> 2) & 1;
    const unsigned bits = ((best[1] < best[0]) == flip ? 1 : 0) | ((best[3] < best[2]) != flip ? 4 : 0);
    __builtin_amdgcn_raw_buffer_store_b128((v4u){(unsigned)tag, (unsigned)(tag >> 32), bits, 0u}, __builtin_amdgcn_make_buffer_rsrc(F.rdy, 0, RDY_SIZE, 0x00020000), lane < 8 ? n * 128 + lane * 16 : 0x7ffffff0, 0, 16);
  }
}
__device__ __forceinline__ void worker(const FParams& F, unsigned cz, char* lds, const int w0) {
#define lane opaque_lane()
#define tid (64 * w + opaque_lane())
  const int grp = (blockIdx.x & 7) - 3, slot = blockIdx.x >> 3;
#define TLOG(t, e)
  unsigned long long par, tag;
  { int w = w0; v4u rec;
    const unsigned t0 = (unsigned)__builtin_amdgcn_s_memrealtime();
    const int id = slot * 4 + w, tz = opaque_lane();
    if (grp != 2) {
      if (grp > 2) { if (slot < 30) setup(F, (grp - 3) * 30 + slot, w); TLOG(1024 + grp * 32 + slot, 0); wait_until(t0 + F.stag); }
      rec = panel_task<NTP>(F, (grp < 2 ? grp + 1 : 0) * 32 + slot, (grp - (grp > 2)) * 32 + slot, grp == 4 ? 64 : 0, grp == 3 ? 64 : 128, w);
    } else {
#pragma unroll
      for (int q = 0; q < 4; ++q) {
        const u16* base = q == 0 ? F.Wk : (q == 1 ? F.Wo : (q == 2 ? F.Wq : F.Wv));
        const int o3 = (384 - (int)(size_t)base) & 896, o63 = (8064 - (int)(size_t)base) & 8064;
        touch(base, DM * HDA * 2, (tz * 128 + id) * 1024 + o3, 0, lds);
        if (!(q & 1)) { touch(base, DM * HDA * 2, (tz * 128 + id) * 1024 + (o3 ^ 128), 0, lds); touch(base, DM * HDA * 2, tz < 16 ? ((tz >> 1) * 128 + id) * 8192 + (o63 ^ ((tz & 1) << 7)) : 0x7ffffff0, 0, lds); }
      }
#pragma unroll 1
      for (int q = 0; q < 4; ++q) { wait_until(t0 + (F.px >> 16) + (((q * 128 + id) * (F.px & 0xffff)) >> 6)); touch(F.Ax, S_ * DM * 2, tz * 128, ((id & 1) * 256 + q * 64 + (id >> 1)) * 8192, lds); }
      touch(F.woB, DB * DB * 2, tz * 128, id * 8192, lds); touch(F.woB, DB * DB * 2, tz * 128, (128 + id) * 8192, lds);
      wait_until(t0 + (F.pb >> 16));
      rec = rdy_load(F);
      asm volatile("s_waitcnt vmcnt(0)" ::: "memory");
      __syncthreads();
    }
    asm volatile("" :: "v"(cz));
    par = rdy_wait(F, rec, tag);
    if (grp == 2 || grp == 3) {
      const FParams& F = args();
      asm volatile("" : "+s"(w));
      const int j0 = grp == 2 ? 0 : F.jb, j1 = grp == 2 ? F.jb : 128;
      if (slot < 24) (void)panel_task<true>(F, 96 + slot, 64 + grp * 32 + slot, j0, j1, w);
      else if (F.pb & 1) {
        const int id = (slot - 24) * 4 + w, tz = opaque_lane();
        int seen = j0;
#pragma unroll 1
        for (int j = j0 + (id >> 2); j < j1; j += 8) {
          for (unsigned spins = 0; j > seen + 16; ++spins) {
            seen = max(seen, __builtin_amdgcn_readfirstlane(ld_agent(&F.ctl->prg[64 + grp * 32 + (id & 15)][0])));
            if (spin_fail(spins, &F.ctl->tmo[0], 2u)) break;
            __builtin_amdgcn_s_sleep(32);
          }
          if (F.pb & 2) touch<2>(F.Bx, S_ * DB * 2, tz * 128, (j * 4 + (id & 3)) * 8192, lds);
          touch(F.Bx, S_ * DB * 2, tz * 128 + 64, (j * 4 + (id & 3)) * 8192, lds);
        }
        asm volatile("s_waitcnt vmcnt(0)" ::: "memory");
      }
    }
    check_in(F, tag, w);
    TLOG(1024 + grp * 32 + slot, 1);
    if (grp < 2) {
      const FParams& F = args();
      asm volatile("" : "+s"(w));
      out_unit(F, par, 2 * grp + (slot >> 4), slot & 15, w);
      TLOG(1280 + grp * 32 + slot, 1);
      return;
    } }
  Ctl* ctl = F.ctl;
  int* sh = (int*)(lds + wg::LDS_USE);
  int next = -1;
  for (;;) {
    const FParams& F = args();
    int w = w0; asm volatile("" : "+s"(w));
    if (next < 0) {
      if (tid == 0) sh[0] = (int)__hip_atomic_fetch_add(&ctl->qhead[0], 1u, RLX_AGENT);
      __syncthreads();
      const int t = __builtin_amdgcn_readfirstlane(sh[0]);
      __syncthreads();
      { if (t >= NTASK) return; next = TASKS[t]; }
    }
    const int code = __builtin_amdgcn_readfirstlane(next); next = -1;
    const int tk = code >> 10, u = code & 1023;
    const u16 *A = nullptr, *Bt = nullptr; void* C = nullptr; int lda = 0, ldb = 0, ldc = 0, nk = 32, r = u >> 2, n = u & 3, nsig = 1;
    const unsigned* wp = nullptr; unsigned need = 0; unsigned* sig = nullptr;
    if (tk == 3) {
      const int lz = opaque_lane(); const bool hi = 8 * r >= F.jb;
      need = lz < 8 ? 8 * r + 8 : F.jb; sig = &ctl->cntAB[r][0];
      if ((lz & 7) < 6 && lz < (hi ? 16 : 8)) wp = &ctl->prg[(lz < 8 && hi ? 160 : 128) + ((lz & 7) >> 1) * 8 + 2 * n + (lz & 1)][0];
    } else if (tk == 4) {
      A = F.qkvA + r * 4 * QBLK + 24 * QPAGE; lda = QBLK / 32; Bt = F.woB + n * 256; ldb = DB;
      C = F.out + (size_t)r * 128 * 2 * DM + DM + n * 256; ldc = 2 * DM; need = 4; if (lane == 0) wp = &ctl->cntAB[r][0];
    } else if (tk == 5) {
      r = u >> 3; n = u & 7; sig = &ctl->cntO[4 * r][0]; nsig = 4; need = 8 * r + 8;
      const int lz = opaque_lane(); if (lz < 12) wp = &ctl->prg[((lz >> 2) + (lz >= 8 && r >= 8)) * 32 + 4 * n + (lz & 3)][0];
    } else if (tk == 6) {
      A = F.qkvA + r * 4 * QBLK; lda = QBLK / 32; Bt = F.Wo + n * 256; ldb = DM; nk = HDA / 32;
      C = F.out + (size_t)r * 128 * 2 * DM + n * 256; ldc = 2 * DM; need = 8; if (lane == 0) wp = &ctl->cntO[4 * r][0];
    } else {
      attn_stream(F, par, u >> 3, u & 7, lds, w);
      TLOG(lastt, 1);
      return;
    }
    wg_wait(wp, need, ctl, w);
    if (tk == 5) {
      attnA_block(F, par, r, 4 * n + w, (u16*)(lds + w * 16384), opaque_lane(), w, (unsigned*)sh + 4);
      asm volatile("s_waitcnt vmcnt(0)" ::: "memory");
      __syncthreads();
    } else if (tk == 3) {
      attnB_task(F, par, r, n, (u16*)(lds + w * 16384), w);
      asm volatile("s_waitcnt vmcnt(0)" ::: "memory");
      __syncthreads();
    } else wg::gemm_tile<128>(A, lda, Bt, ldb, nk, C, ldc, par >> (tk == 4 ? 48 : 0), lds, w);
    if (sig && tid < nsig) __hip_atomic_fetch_add(sig + tid * 32, 1u, RLX_AGENT);
  }
}

#undef lane
#undef tid
__device__ __forceinline__ unsigned rtnow() { return (unsigned)__builtin_amdgcn_s_memrealtime(); }
#define RBASE(s) (((s) & 3) * RSLOT + rpl)
constexpr int ALM = RECO + 1024;
__device__ __forceinline__ float rowmax16(float f) { f = fmaxf(f, dpp_ror<8>(f)); f = fmaxf(f, dpp_ror<4>(f)); f = fmaxf(f, dpp_ror<2>(f)); return fmaxf(f, dpp_ror<1>(f)); }
__device__ __forceinline__ void al_eval(unsigned v, unsigned& t) {
  const bool set = (v >> 16) == 1u;
  if (__builtin_expect(((unsigned)__ballot(set) & 0xffffu) == 0, 1)) return;
  const int d = (short)(v - t);
  t += __builtin_amdgcn_readfirstlane((int)rowmax16(set && d > 0 && d < 1000 ? (float)d : 0.f));
}
__device__ __forceinline__ void rec_get(unsigned& recA, unsigned& recD, __amdgpu_buffer_rsrc_t rs, int off, unsigned* tmo, int naps = 1) {
#pragma unroll 1
  for (unsigned fails = 0;; ++fails) {
    const v2u rec = __builtin_amdgcn_raw_buffer_load_b64(rs, off, 0, 16);
    recA = __builtin_amdgcn_readfirstlane(rec.x); recD = __builtin_amdgcn_readfirstlane(rec.y);
    if ((recA != 0xffffffffu && recD != 0xffffffffu) || spin_fail(fails, tmo, 1u)) break;
    if (naps > 1) __builtin_amdgcn_s_sleep(6); else __builtin_amdgcn_s_sleep(2);
  }
}

#define QK_LOADER \
          "s_nop 4\n\t" \
          "s_add_u32 m0, %[lw], 0\n\ts_xor_b32 %[t], %[kb], 0x0\n\tbuffer_load_dwordx4 %[go], %[rk], %[t] offen" QK_NTk " lds\n\ts_add_u32 m0, %[lw], 1024\n\ts_xor_b32 %[t], %[kb], 0x10000\n\tbuffer_load_dwordx4 %[go], %[rk], %[t] offen" QK_NTk " lds\n\ts_add_u32 m0, %[lw], 2048\n\ts_xor_b32 %[t], %[kb], 0x20000\n\tbuffer_load_dwordx4 %[go], %[rk], %[t] offen" QK_NTk " lds\n\ts_add_u32 m0, %[lw], 3072\n\ts_xor_b32 %[t], %[kb], 0x30000\n\tbuffer_load_dwordx4 %[go], %[rk], %[t] offen" QK_NTk " lds\n\ts_add_u32 m0, %[lw], 4096\n\ts_xor_b32 %[t], %[kb], 0x40000\n\tbuffer_load_dwordx4 %[go], %[rk], %[t] offen" QK_NTk " lds\n\ts_add_u32 m0, %[lw], 5120\n\ts_xor_b32 %[t], %[kb], 0x50000\n\tbuffer_load_dwordx4 %[go], %[rk], %[t] offen" QK_NTk " lds\n\ts_add_u32 m0, %[lw], 6144\n\ts_xor_b32 %[t], %[kb], 0x60000\n\tbuffer_load_dwordx4 %[go], %[rk], %[t] offen" QK_NTk " lds\n\ts_add_u32 m0, %[lw], 7168\n\ts_xor_b32 %[t], %[kb], 0x70000\n\tbuffer_load_dwordx4 %[go], %[rk], %[t] offen" QK_NTk " lds\n\t" \
          "s_add_u32 m0, %[lw], 8192\n\ts_xor_b32 %[t], %[kb], 0x80000\n\tbuffer_load_dwordx4 %[go], %[rk], %[t] offen" QK_NTk " lds\n\ts_add_u32 m0, %[lw], 9216\n\ts_xor_b32 %[t], %[kb], 0x90000\n\tbuffer_load_dwordx4 %[go], %[rk], %[t] offen" QK_NTk " lds\n\ts_add_u32 m0, %[lw], 10240\n\ts_xor_b32 %[t], %[kb], 0xa0000\n\tbuffer_load_dwordx4 %[go], %[rk], %[t] offen" QK_NTk " lds\n\ts_add_u32 m0, %[lw], 11264\n\ts_xor_b32 %[t], %[kb], 0xb0000\n\tbuffer_load_dwordx4 %[go], %[rk], %[t] offen" QK_NTk " lds\n\ts_add_u32 m0, %[lw], 12288\n\ts_xor_b32 %[t], %[kb], 0xc0000\n\tbuffer_load_dwordx4 %[go], %[rk], %[t] offen" QK_NTk " lds\n\ts_add_u32 m0, %[lw], 13312\n\ts_xor_b32 %[t], %[kb], 0xd0000\n\tbuffer_load_dwordx4 %[go], %[rk], %[t] offen" QK_NTk " lds\n\ts_add_u32 m0, %[lw], 14336\n\ts_xor_b32 %[t], %[kb], 0xe0000\n\tbuffer_load_dwordx4 %[go], %[rk], %[t] offen" QK_NTk " lds\n\ts_add_u32 m0, %[lw], 15360\n\ts_xor_b32 %[t], %[kb], 0xf0000\n\tbuffer_load_dwordx4 %[go], %[rk], %[t] offen" QK_NTk " lds\n\t" \
          "s_add_u32 m0, %[lw], 16384\n\ts_xor_b32 %[t], %[kb], 0x100000\n\tbuffer_load_dwordx4 %[go], %[rk], %[t] offen" QK_NTk " lds\n\ts_add_u32 m0, %[lw], 17408\n\ts_xor_b32 %[t], %[kb], 0x110000\n\tbuffer_load_dwordx4 %[go], %[rk], %[t] offen" QK_NTk " lds\n\ts_add_u32 m0, %[lw], 18432\n\ts_xor_b32 %[t], %[kb], 0x120000\n\tbuffer_load_dwordx4 %[go], %[rk], %[t] offen" QK_NTk " lds\n\ts_add_u32 m0, %[lw], 19456\n\ts_xor_b32 %[t], %[kb], 0x130000\n\tbuffer_load_dwordx4 %[go], %[rk], %[t] offen" QK_NTk " lds\n\ts_add_u32 m0, %[lw], 20480\n\ts_xor_b32 %[t], %[kb], 0x140000\n\tbuffer_load_dwordx4 %[go], %[rk], %[t] offen" QK_NTk " lds\n\ts_add_u32 m0, %[lw], 21504\n\ts_xor_b32 %[t], %[kb], 0x150000\n\tbuffer_load_dwordx4 %[go], %[rk], %[t] offen" QK_NTk " lds\n\ts_add_u32 m0, %[lw], 22528\n\ts_xor_b32 %[t], %[kb], 0x160000\n\tbuffer_load_dwordx4 %[go], %[rk], %[t] offen" QK_NTk " lds\n\ts_add_u32 m0, %[lw], 23552\n\ts_xor_b32 %[t], %[kb], 0x170000\n\tbuffer_load_dwordx4 %[go], %[rk], %[t] offen" QK_NTk " lds\n\t" \
          "s_add_u32 m0, %[lw], 24576\n\ts_xor_b32 %[t], %[kb], 0x180000\n\tbuffer_load_dwordx4 %[go], %[rk], %[t] offen" QK_NTk " lds\n\ts_add_u32 m0, %[lw], 25600\n\ts_xor_b32 %[t], %[kb], 0x190000\n\tbuffer_load_dwordx4 %[go], %[rk], %[t] offen" QK_NTk " lds\n\ts_add_u32 m0, %[lw], 26624\n\ts_xor_b32 %[t], %[kb], 0x1a0000\n\tbuffer_load_dwordx4 %[go], %[rk], %[t] offen" QK_NTk " lds\n\ts_add_u32 m0, %[lw], 27648\n\ts_xor_b32 %[t], %[kb], 0x1b0000\n\tbuffer_load_dwordx4 %[go], %[rk], %[t] offen" QK_NTk " lds\n\ts_add_u32 m0, %[lw], 28672\n\ts_xor_b32 %[t], %[kb], 0x1c0000\n\tbuffer_load_dwordx4 %[go], %[rk], %[t] offen" QK_NTk " lds\n\ts_add_u32 m0, %[lw], 29696\n\ts_xor_b32 %[t], %[kb], 0x1d0000\n\tbuffer_load_dwordx4 %[go], %[rk], %[t] offen" QK_NTk " lds\n\ts_add_u32 m0, %[lw], 30720\n\ts_xor_b32 %[t], %[kb], 0x1e0000\n\tbuffer_load_dwordx4 %[go], %[rk], %[t] offen" QK_NTk " lds\n\ts_add_u32 m0, %[lw], 31744\n\ts_xor_b32 %[t], %[kb], 0x1f0000\n\tbuffer_load_dwordx4 %[go], %[rk], %[t] offen" QK_NTk " lds\n\t" \
          "s_add_u32 m0, %[lw], 32768\n\ts_xor_b32 %[t], %[kb], 0x200000\n\tbuffer_load_dwordx4 %[go], %[rk], %[t] offen" QK_NTk " lds\n\ts_add_u32 m0, %[lw], 33792\n\ts_xor_b32 %[t], %[kb], 0x210000\n\tbuffer_load_dwordx4 %[go], %[rk], %[t] offen" QK_NTk " lds\n\ts_add_u32 m0, %[lw], 34816\n\ts_xor_b32 %[t], %[kb], 0x220000\n\tbuffer_load_dwordx4 %[go], %[rk], %[t] offen" QK_NTk " lds\n\ts_add_u32 m0, %[lw], 35840\n\ts_xor_b32 %[t], %[kb], 0x230000\n\tbuffer_load_dwordx4 %[go], %[rk], %[t] offen" QK_NTk " lds\n\ts_add_u32 m0, %[lw], 36864\n\ts_xor_b32 %[t], %[kb], 0x240000\n\tbuffer_load_dwordx4 %[go], %[rk], %[t] offen" QK_NTk " lds\n\ts_add_u32 m0, %[lw], 37888\n\ts_xor_b32 %[t], %[kb], 0x250000\n\tbuffer_load_dwordx4 %[go], %[rk], %[t] offen" QK_NTk " lds\n\ts_add_u32 m0, %[lw], 38912\n\ts_xor_b32 %[t], %[kb], 0x260000\n\tbuffer_load_dwordx4 %[go], %[rk], %[t] offen" QK_NTk " lds\n\ts_add_u32 m0, %[lw], 39936\n\ts_xor_b32 %[t], %[kb], 0x270000\n\tbuffer_load_dwordx4 %[go], %[rk], %[t] offen" QK_NTk " lds\n\t" \
          "s_waitcnt vmcnt(32)\n\tds_read_b64_tr_b16 v[136:137], %[rd0] offset:0\n\tds_read_b64_tr_b16 v[138:139], %[rd0] offset:128\n\tds_read_b64_tr_b16 v[140:141], %[rd0] offset:4096\n\tds_read_b64_tr_b16 v[142:143], %[rd0] offset:4224\n\tds_read_b64_tr_b16 a[64:65], %[rd1] offset:0\n\tds_read_b64_tr_b16 a[66:67], %[rd1] offset:128\n\tds_read_b64_tr_b16 a[68:69], %[rd1] offset:4096\n\tds_read_b64_tr_b16 a[70:71], %[rd1] offset:4224\n\tds_read_b64_tr_b16 a[128:129], %[rd2] offset:0\n\tds_read_b64_tr_b16 a[130:131], %[rd2] offset:128\n\tds_read_b64_tr_b16 a[132:133], %[rd2] offset:4096\n\tds_read_b64_tr_b16 a[134:135], %[rd2] offset:4224\n\tds_read_b64_tr_b16 a[136:137], %[rd3] offset:0\n\tds_read_b64_tr_b16 a[138:139], %[rd3] offset:128\n\tds_read_b64_tr_b16 a[140:141], %[rd3] offset:4096\n\tds_read_b64_tr_b16 a[142:143], %[rd3] offset:4224\n\ts_waitcnt lgkmcnt(0)\n\t" \
          "s_add_u32 m0, %[lw], 0\n\ts_xor_b32 %[t], %[kb], 0x280000\n\tbuffer_load_dwordx4 %[go], %[rk], %[t] offen" QK_NTk " lds\n\ts_add_u32 m0, %[lw], 1024\n\ts_xor_b32 %[t], %[kb], 0x290000\n\tbuffer_load_dwordx4 %[go], %[rk], %[t] offen" QK_NTk " lds\n\ts_add_u32 m0, %[lw], 2048\n\ts_xor_b32 %[t], %[kb], 0x2a0000\n\tbuffer_load_dwordx4 %[go], %[rk], %[t] offen" QK_NTk " lds\n\ts_add_u32 m0, %[lw], 3072\n\ts_xor_b32 %[t], %[kb], 0x2b0000\n\tbuffer_load_dwordx4 %[go], %[rk], %[t] offen" QK_NTk " lds\n\ts_add_u32 m0, %[lw], 4096\n\ts_xor_b32 %[t], %[kb], 0x2c0000\n\tbuffer_load_dwordx4 %[go], %[rk], %[t] offen" QK_NTk " lds\n\ts_add_u32 m0, %[lw], 5120\n\ts_xor_b32 %[t], %[kb], 0x2d0000\n\tbuffer_load_dwordx4 %[go], %[rk], %[t] offen" QK_NTk " lds\n\ts_add_u32 m0, %[lw], 6144\n\ts_xor_b32 %[t], %[kb], 0x2e0000\n\tbuffer_load_dwordx4 %[go], %[rk], %[t] offen" QK_NTk " lds\n\ts_add_u32 m0, %[lw], 7168\n\ts_xor_b32 %[t], %[kb], 0x2f0000\n\tbuffer_load_dwordx4 %[go], %[rk], %[t] offen" QK_NTk " lds\n\t" \
          "s_waitcnt vmcnt(32)\n\tds_read_b64_tr_b16 v[40:41], %[rd0] offset:8192\n\tds_read_b64_tr_b16 v[42:43], %[rd0] offset:8320\n\tds_read_b64_tr_b16 v[44:45], %[rd0] offset:12288\n\tds_read_b64_tr_b16 v[46:47], %[rd0] offset:12416\n\tds_read_b64_tr_b16 a[72:73], %[rd1] offset:8192\n\tds_read_b64_tr_b16 a[74:75], %[rd1] offset:8320\n\tds_read_b64_tr_b16 a[76:77], %[rd1] offset:12288\n\tds_read_b64_tr_b16 a[78:79], %[rd1] offset:12416\n\tds_read_b64_tr_b16 a[144:145], %[rd2] offset:8192\n\tds_read_b64_tr_b16 a[146:147], %[rd2] offset:8320\n\tds_read_b64_tr_b16 a[148:149], %[rd2] offset:12288\n\tds_read_b64_tr_b16 a[150:151], %[rd2] offset:12416\n\tds_read_b64_tr_b16 a[152:153], %[rd3] offset:8192\n\tds_read_b64_tr_b16 a[154:155], %[rd3] offset:8320\n\tds_read_b64_tr_b16 a[156:157], %[rd3] offset:12288\n\tds_read_b64_tr_b16 a[158:159], %[rd3] offset:12416\n\ts_waitcnt lgkmcnt(0)\n\t" \
          "s_add_u32 m0, %[lw], 8192\n\ts_xor_b32 %[t], %[kb], 0x300000\n\tbuffer_load_dwordx4 %[go], %[rk], %[t] offen" QK_NTk " lds\n\ts_add_u32 m0, %[lw], 9216\n\ts_xor_b32 %[t], %[kb], 0x310000\n\tbuffer_load_dwordx4 %[go], %[rk], %[t] offen" QK_NTk " lds\n\ts_add_u32 m0, %[lw], 10240\n\ts_xor_b32 %[t], %[kb], 0x320000\n\tbuffer_load_dwordx4 %[go], %[rk], %[t] offen" QK_NTk " lds\n\ts_add_u32 m0, %[lw], 11264\n\ts_xor_b32 %[t], %[kb], 0x330000\n\tbuffer_load_dwordx4 %[go], %[rk], %[t] offen" QK_NTk " lds\n\ts_add_u32 m0, %[lw], 12288\n\ts_xor_b32 %[t], %[kb], 0x340000\n\tbuffer_load_dwordx4 %[go], %[rk], %[t] offen" QK_NTk " lds\n\ts_add_u32 m0, %[lw], 13312\n\ts_xor_b32 %[t], %[kb], 0x350000\n\tbuffer_load_dwordx4 %[go], %[rk], %[t] offen" QK_NTk " lds\n\ts_add_u32 m0, %[lw], 14336\n\ts_xor_b32 %[t], %[kb], 0x360000\n\tbuffer_load_dwordx4 %[go], %[rk], %[t] offen" QK_NTk " lds\n\ts_add_u32 m0, %[lw], 15360\n\ts_xor_b32 %[t], %[kb], 0x370000\n\tbuffer_load_dwordx4 %[go], %[rk], %[t] offen" QK_NTk " lds\n\t" \
          "s_waitcnt vmcnt(32)\n\tds_read_b64_tr_b16 v[144:145], %[rd0] offset:16384\n\tds_read_b64_tr_b16 v[146:147], %[rd0] offset:16512\n\tds_read_b64_tr_b16 v[148:149], %[rd0] offset:20480\n\tds_read_b64_tr_b16 v[150:151], %[rd0] offset:20608\n\tds_read_b64_tr_b16 a[80:81], %[rd1] offset:16384\n\tds_read_b64_tr_b16 a[82:83], %[rd1] offset:16512\n\tds_read_b64_tr_b16 a[84:85], %[rd1] offset:20480\n\tds_read_b64_tr_b16 a[86:87], %[rd1] offset:20608\n\tds_read_b64_tr_b16 a[160:161], %[rd2] offset:16384\n\tds_read_b64_tr_b16 a[162:163], %[rd2] offset:16512\n\tds_read_b64_tr_b16 a[164:165], %[rd2] offset:20480\n\tds_read_b64_tr_b16 a[166:167], %[rd2] offset:20608\n\tds_read_b64_tr_b16 a[168:169], %[rd3] offset:16384\n\tds_read_b64_tr_b16 a[170:171], %[rd3] offset:16512\n\tds_read_b64_tr_b16 a[172:173], %[rd3] offset:20480\n\tds_read_b64_tr_b16 a[174:175], %[rd3] offset:20608\n\ts_waitcnt lgkmcnt(0)\n\t" \
          "s_add_u32 m0, %[lw], 16384\n\ts_xor_b32 %[t], %[kb], 0x380000\n\tbuffer_load_dwordx4 %[go], %[rk], %[t] offen" QK_NTk " lds\n\ts_add_u32 m0, %[lw], 17408\n\ts_xor_b32 %[t], %[kb], 0x390000\n\tbuffer_load_dwordx4 %[go], %[rk], %[t] offen" QK_NTk " lds\n\ts_add_u32 m0, %[lw], 18432\n\ts_xor_b32 %[t], %[kb], 0x3a0000\n\tbuffer_load_dwordx4 %[go], %[rk], %[t] offen" QK_NTk " lds\n\ts_add_u32 m0, %[lw], 19456\n\ts_xor_b32 %[t], %[kb], 0x3b0000\n\tbuffer_load_dwordx4 %[go], %[rk], %[t] offen" QK_NTk " lds\n\ts_add_u32 m0, %[lw], 20480\n\ts_xor_b32 %[t], %[kb], 0x3c0000\n\tbuffer_load_dwordx4 %[go], %[rk], %[t] offen" QK_NTk " lds\n\ts_add_u32 m0, %[lw], 21504\n\ts_xor_b32 %[t], %[kb], 0x3d0000\n\tbuffer_load_dwordx4 %[go], %[rk], %[t] offen" QK_NTk " lds\n\ts_add_u32 m0, %[lw], 22528\n\ts_xor_b32 %[t], %[kb], 0x3e0000\n\tbuffer_load_dwordx4 %[go], %[rk], %[t] offen" QK_NTk " lds\n\ts_add_u32 m0, %[lw], 23552\n\ts_xor_b32 %[t], %[kb], 0x3f0000\n\tbuffer_load_dwordx4 %[go], %[rk], %[t] offen" QK_NTk " lds\n\t" \
          "s_waitcnt vmcnt(32)\n\tds_read_b64_tr_b16 v[80:81], %[rd0] offset:24576\n\tds_read_b64_tr_b16 v[82:83], %[rd0] offset:24704\n\tds_read_b64_tr_b16 v[84:85], %[rd0] offset:28672\n\tds_read_b64_tr_b16 v[86:87], %[rd0] offset:28800\n\tds_read_b64_tr_b16 a[88:89], %[rd1] offset:24576\n\tds_read_b64_tr_b16 a[90:91], %[rd1] offset:24704\n\tds_read_b64_tr_b16 a[92:93], %[rd1] offset:28672\n\tds_read_b64_tr_b16 a[94:95], %[rd1] offset:28800\n\tds_read_b64_tr_b16 a[176:177], %[rd2] offset:24576\n\tds_read_b64_tr_b16 a[178:179], %[rd2] offset:24704\n\tds_read_b64_tr_b16 a[180:181], %[rd2] offset:28672\n\tds_read_b64_tr_b16 a[182:183], %[rd2] offset:28800\n\tds_read_b64_tr_b16 a[184:185], %[rd3] offset:24576\n\tds_read_b64_tr_b16 a[186:187], %[rd3] offset:24704\n\tds_read_b64_tr_b16 a[188:189], %[rd3] offset:28672\n\tds_read_b64_tr_b16 a[190:191], %[rd3] offset:28800\n\ts_waitcnt lgkmcnt(0)\n\t" \
          "s_add_u32 m0, %[lw], 24576\n\ts_xor_b32 %[t], %[kb], 0x380000\n\tbuffer_load_dwordx4 %[go], %[rq], %[t] offen" QK_NTq " lds\n\ts_add_u32 m0, %[lw], 25600\n\ts_xor_b32 %[t], %[kb], 0x390000\n\tbuffer_load_dwordx4 %[go], %[rq], %[t] offen" QK_NTq " lds\n\ts_add_u32 m0, %[lw], 26624\n\ts_xor_b32 %[t], %[kb], 0x3a0000\n\tbuffer_load_dwordx4 %[go], %[rq], %[t] offen" QK_NTq " lds\n\ts_add_u32 m0, %[lw], 27648\n\ts_xor_b32 %[t], %[kb], 0x3b0000\n\tbuffer_load_dwordx4 %[go], %[rq], %[t] offen" QK_NTq " lds\n\ts_add_u32 m0, %[lw], 28672\n\ts_xor_b32 %[t], %[kb], 0x3c0000\n\tbuffer_load_dwordx4 %[go], %[rq], %[t] offen" QK_NTq " lds\n\ts_add_u32 m0, %[lw], 29696\n\ts_xor_b32 %[t], %[kb], 0x3d0000\n\tbuffer_load_dwordx4 %[go], %[rq], %[t] offen" QK_NTq " lds\n\ts_add_u32 m0, %[lw], 30720\n\ts_xor_b32 %[t], %[kb], 0x3e0000\n\tbuffer_load_dwordx4 %[go], %[rq], %[t] offen" QK_NTq " lds\n\ts_add_u32 m0, %[lw], 31744\n\ts_xor_b32 %[t], %[kb], 0x3f0000\n\tbuffer_load_dwordx4 %[go], %[rq], %[t] offen" QK_NTq " lds\n\t" \
          "s_waitcnt vmcnt(32)\n\tds_read_b64_tr_b16 v[152:153], %[rd0] offset:32768\n\tds_read_b64_tr_b16 v[154:155], %[rd0] offset:32896\n\tds_read_b64_tr_b16 v[156:157], %[rd0] offset:36864\n\tds_read_b64_tr_b16 v[158:159], %[rd0] offset:36992\n\tds_read_b64_tr_b16 a[96:97], %[rd1] offset:32768\n\tds_read_b64_tr_b16 a[98:99], %[rd1] offset:32896\n\tds_read_b64_tr_b16 a[100:101], %[rd1] offset:36864\n\tds_read_b64_tr_b16 a[102:103], %[rd1] offset:36992\n\tds_read_b64_tr_b16 a[192:193], %[rd2] offset:32768\n\tds_read_b64_tr_b16 a[194:195], %[rd2] offset:32896\n\tds_read_b64_tr_b16 a[196:197], %[rd2] offset:36864\n\tds_read_b64_tr_b16 a[198:199], %[rd2] offset:36992\n\tds_read_b64_tr_b16 a[200:201], %[rd3] offset:32768\n\tds_read_b64_tr_b16 a[202:203], %[rd3] offset:32896\n\tds_read_b64_tr_b16 a[204:205], %[rd3] offset:36864\n\tds_read_b64_tr_b16 a[206:207], %[rd3] offset:36992\n\ts_waitcnt lgkmcnt(0)\n\t" \
          "s_add_u32 m0, %[lw], 32768\n\ts_xor_b32 %[t], %[kb], 0x300000\n\tbuffer_load_dwordx4 %[go], %[rq], %[t] offen" QK_NTq " lds\n\ts_add_u32 m0, %[lw], 33792\n\ts_xor_b32 %[t], %[kb], 0x310000\n\tbuffer_load_dwordx4 %[go], %[rq], %[t] offen" QK_NTq " lds\n\ts_add_u32 m0, %[lw], 34816\n\ts_xor_b32 %[t], %[kb], 0x320000\n\tbuffer_load_dwordx4 %[go], %[rq], %[t] offen" QK_NTq " lds\n\ts_add_u32 m0, %[lw], 35840\n\ts_xor_b32 %[t], %[kb], 0x330000\n\tbuffer_load_dwordx4 %[go], %[rq], %[t] offen" QK_NTq " lds\n\ts_add_u32 m0, %[lw], 36864\n\ts_xor_b32 %[t], %[kb], 0x340000\n\tbuffer_load_dwordx4 %[go], %[rq], %[t] offen" QK_NTq " lds\n\ts_add_u32 m0, %[lw], 37888\n\ts_xor_b32 %[t], %[kb], 0x350000\n\tbuffer_load_dwordx4 %[go], %[rq], %[t] offen" QK_NTq " lds\n\ts_add_u32 m0, %[lw], 38912\n\ts_xor_b32 %[t], %[kb], 0x360000\n\tbuffer_load_dwordx4 %[go], %[rq], %[t] offen" QK_NTq " lds\n\ts_add_u32 m0, %[lw], 39936\n\ts_xor_b32 %[t], %[kb], 0x370000\n\tbuffer_load_dwordx4 %[go], %[rq], %[t] offen" QK_NTq " lds\n\t" \
          "s_waitcnt vmcnt(32)\n\tds_read_b64_tr_b16 v[120:121], %[rd0] offset:0\n\tds_read_b64_tr_b16 v[122:123], %[rd0] offset:128\n\tds_read_b64_tr_b16 v[124:125], %[rd0] offset:4096\n\tds_read_b64_tr_b16 v[126:127], %[rd0] offset:4224\n\tds_read_b64_tr_b16 a[104:105], %[rd1] offset:0\n\tds_read_b64_tr_b16 a[106:107], %[rd1] offset:128\n\tds_read_b64_tr_b16 a[108:109], %[rd1] offset:4096\n\tds_read_b64_tr_b16 a[110:111], %[rd1] offset:4224\n\tds_read_b64_tr_b16 a[208:209], %[rd2] offset:0\n\tds_read_b64_tr_b16 a[210:211], %[rd2] offset:128\n\tds_read_b64_tr_b16 a[212:213], %[rd2] offset:4096\n\tds_read_b64_tr_b16 a[214:215], %[rd2] offset:4224\n\tds_read_b64_tr_b16 a[216:217], %[rd3] offset:0\n\tds_read_b64_tr_b16 a[218:219], %[rd3] offset:128\n\tds_read_b64_tr_b16 a[220:221], %[rd3] offset:4096\n\tds_read_b64_tr_b16 a[222:223], %[rd3] offset:4224\n\ts_waitcnt lgkmcnt(0)\n\t" \
          "s_add_u32 m0, %[lw], 0\n\ts_xor_b32 %[t], %[kb], 0x280000\n\tbuffer_load_dwordx4 %[go], %[rq], %[t] offen" QK_NTq " lds\n\ts_add_u32 m0, %[lw], 1024\n\ts_xor_b32 %[t], %[kb], 0x290000\n\tbuffer_load_dwordx4 %[go], %[rq], %[t] offen" QK_NTq " lds\n\ts_add_u32 m0, %[lw], 2048\n\ts_xor_b32 %[t], %[kb], 0x2a0000\n\tbuffer_load_dwordx4 %[go], %[rq], %[t] offen" QK_NTq " lds\n\ts_add_u32 m0, %[lw], 3072\n\ts_xor_b32 %[t], %[kb], 0x2b0000\n\tbuffer_load_dwordx4 %[go], %[rq], %[t] offen" QK_NTq " lds\n\ts_add_u32 m0, %[lw], 4096\n\ts_xor_b32 %[t], %[kb], 0x2c0000\n\tbuffer_load_dwordx4 %[go], %[rq], %[t] offen" QK_NTq " lds\n\ts_add_u32 m0, %[lw], 5120\n\ts_xor_b32 %[t], %[kb], 0x2d0000\n\tbuffer_load_dwordx4 %[go], %[rq], %[t] offen" QK_NTq " lds\n\ts_add_u32 m0, %[lw], 6144\n\ts_xor_b32 %[t], %[kb], 0x2e0000\n\tbuffer_load_dwordx4 %[go], %[rq], %[t] offen" QK_NTq " lds\n\ts_add_u32 m0, %[lw], 7168\n\ts_xor_b32 %[t], %[kb], 0x2f0000\n\tbuffer_load_dwordx4 %[go], %[rq], %[t] offen" QK_NTq " lds\n\t" \
          "s_waitcnt vmcnt(32)\n\tds_read_b64_tr_b16 a[16:17], %[rd0] offset:8192\n\tds_read_b64_tr_b16 a[18:19], %[rd0] offset:8320\n\tds_read_b64_tr_b16 a[20:21], %[rd0] offset:12288\n\tds_read_b64_tr_b16 a[22:23], %[rd0] offset:12416\n\tds_read_b64_tr_b16 a[112:113], %[rd1] offset:8192\n\tds_read_b64_tr_b16 a[114:115], %[rd1] offset:8320\n\tds_read_b64_tr_b16 a[116:117], %[rd1] offset:12288\n\tds_read_b64_tr_b16 a[118:119], %[rd1] offset:12416\n\tds_read_b64_tr_b16 a[224:225], %[rd2] offset:8192\n\tds_read_b64_tr_b16 a[226:227], %[rd2] offset:8320\n\tds_read_b64_tr_b16 a[228:229], %[rd2] offset:12288\n\tds_read_b64_tr_b16 a[230:231], %[rd2] offset:12416\n\tds_read_b64_tr_b16 a[232:233], %[rd3] offset:8192\n\tds_read_b64_tr_b16 a[234:235], %[rd3] offset:8320\n\tds_read_b64_tr_b16 a[236:237], %[rd3] offset:12288\n\tds_read_b64_tr_b16 a[238:239], %[rd3] offset:12416\n\ts_waitcnt lgkmcnt(0)\n\t" \
          "s_add_u32 m0, %[lw], 8192\n\ts_xor_b32 %[t], %[kb], 0x200000\n\tbuffer_load_dwordx4 %[go], %[rq], %[t] offen" QK_NTq " lds\n\ts_add_u32 m0, %[lw], 9216\n\ts_xor_b32 %[t], %[kb], 0x210000\n\tbuffer_load_dwordx4 %[go], %[rq], %[t] offen" QK_NTq " lds\n\ts_add_u32 m0, %[lw], 10240\n\ts_xor_b32 %[t], %[kb], 0x220000\n\tbuffer_load_dwordx4 %[go], %[rq], %[t] offen" QK_NTq " lds\n\ts_add_u32 m0, %[lw], 11264\n\ts_xor_b32 %[t], %[kb], 0x230000\n\tbuffer_load_dwordx4 %[go], %[rq], %[t] offen" QK_NTq " lds\n\ts_add_u32 m0, %[lw], 12288\n\ts_xor_b32 %[t], %[kb], 0x240000\n\tbuffer_load_dwordx4 %[go], %[rq], %[t] offen" QK_NTq " lds\n\ts_add_u32 m0, %[lw], 13312\n\ts_xor_b32 %[t], %[kb], 0x250000\n\tbuffer_load_dwordx4 %[go], %[rq], %[t] offen" QK_NTq " lds\n\ts_add_u32 m0, %[lw], 14336\n\ts_xor_b32 %[t], %[kb], 0x260000\n\tbuffer_load_dwordx4 %[go], %[rq], %[t] offen" QK_NTq " lds\n\ts_add_u32 m0, %[lw], 15360\n\ts_xor_b32 %[t], %[kb], 0x270000\n\tbuffer_load_dwordx4 %[go], %[rq], %[t] offen" QK_NTq " lds\n\t" \
          "s_waitcnt vmcnt(32)\n\tds_read_b64_tr_b16 a[56:57], %[rd0] offset:16384\n\tds_read_b64_tr_b16 a[58:59], %[rd0] offset:16512\n\tds_read_b64_tr_b16 a[60:61], %[rd0] offset:20480\n\tds_read_b64_tr_b16 a[62:63], %[rd0] offset:20608\n\tds_read_b64_tr_b16 a[120:121], %[rd1] offset:16384\n\tds_read_b64_tr_b16 a[122:123], %[rd1] offset:16512\n\tds_read_b64_tr_b16 a[124:125], %[rd1] offset:20480\n\tds_read_b64_tr_b16 a[126:127], %[rd1] offset:20608\n\tds_read_b64_tr_b16 a[240:241], %[rd2] offset:16384\n\tds_read_b64_tr_b16 a[242:243], %[rd2] offset:16512\n\tds_read_b64_tr_b16 a[244:245], %[rd2] offset:20480\n\tds_read_b64_tr_b16 a[246:247], %[rd2] offset:20608\n\tds_read_b64_tr_b16 a[248:249], %[rd3] offset:16384\n\tds_read_b64_tr_b16 a[250:251], %[rd3] offset:16512\n\tds_read_b64_tr_b16 a[252:253], %[rd3] offset:20480\n\tds_read_b64_tr_b16 a[254:255], %[rd3] offset:20608\n\ts_waitcnt lgkmcnt(0)\n\t" \
          "s_add_u32 m0, %[lw], 16384\n\ts_xor_b32 %[t], %[kb], 0x180000\n\tbuffer_load_dwordx4 %[go], %[rq], %[t] offen" QK_NTq " lds\n\ts_add_u32 m0, %[lw], 17408\n\ts_xor_b32 %[t], %[kb], 0x190000\n\tbuffer_load_dwordx4 %[go], %[rq], %[t] offen" QK_NTq " lds\n\ts_add_u32 m0, %[lw], 18432\n\ts_xor_b32 %[t], %[kb], 0x1a0000\n\tbuffer_load_dwordx4 %[go], %[rq], %[t] offen" QK_NTq " lds\n\ts_add_u32 m0, %[lw], 19456\n\ts_xor_b32 %[t], %[kb], 0x1b0000\n\tbuffer_load_dwordx4 %[go], %[rq], %[t] offen" QK_NTq " lds\n\ts_add_u32 m0, %[lw], 20480\n\ts_xor_b32 %[t], %[kb], 0x1c0000\n\tbuffer_load_dwordx4 %[go], %[rq], %[t] offen" QK_NTq " lds\n\ts_add_u32 m0, %[lw], 21504\n\ts_xor_b32 %[t], %[kb], 0x1d0000\n\tbuffer_load_dwordx4 %[go], %[rq], %[t] offen" QK_NTq " lds\n\ts_add_u32 m0, %[lw], 22528\n\ts_xor_b32 %[t], %[kb], 0x1e0000\n\tbuffer_load_dwordx4 %[go], %[rq], %[t] offen" QK_NTq " lds\n\ts_add_u32 m0, %[lw], 23552\n\ts_xor_b32 %[t], %[kb], 0x1f0000\n\tbuffer_load_dwordx4 %[go], %[rq], %[t] offen" QK_NTq " lds\n\t" \
          "s_waitcnt vmcnt(32)\n\tds_read_b64_tr_b16 a[24:25], %[rd0] offset:24576\n\tds_read_b64_tr_b16 a[26:27], %[rd0] offset:24704\n\tds_read_b64_tr_b16 a[28:29], %[rd0] offset:28672\n\tds_read_b64_tr_b16 a[30:31], %[rd0] offset:28800\n\tds_read_b64_tr_b16 a[32:33], %[rd1] offset:24576\n\tds_read_b64_tr_b16 a[34:35], %[rd1] offset:24704\n\tds_read_b64_tr_b16 a[36:37], %[rd1] offset:28672\n\tds_read_b64_tr_b16 a[38:39], %[rd1] offset:28800\n\tds_read_b64_tr_b16 a[40:41], %[rd2] offset:24576\n\tds_read_b64_tr_b16 a[42:43], %[rd2] offset:24704\n\tds_read_b64_tr_b16 a[44:45], %[rd2] offset:28672\n\tds_read_b64_tr_b16 a[46:47], %[rd2] offset:28800\n\tds_read_b64_tr_b16 a[48:49], %[rd3] offset:24576\n\tds_read_b64_tr_b16 a[50:51], %[rd3] offset:24704\n\tds_read_b64_tr_b16 a[52:53], %[rd3] offset:28672\n\tds_read_b64_tr_b16 a[54:55], %[rd3] offset:28800\n\ts_waitcnt lgkmcnt(0)\n\t" \
          "s_add_u32 m0, %[lw], 24576\n\ts_xor_b32 %[t], %[kb], 0x100000\n\tbuffer_load_dwordx4 %[go], %[rq], %[t] offen" QK_NTq " lds\n\ts_add_u32 m0, %[lw], 25600\n\ts_xor_b32 %[t], %[kb], 0x110000\n\tbuffer_load_dwordx4 %[go], %[rq], %[t] offen" QK_NTq " lds\n\ts_add_u32 m0, %[lw], 26624\n\ts_xor_b32 %[t], %[kb], 0x120000\n\tbuffer_load_dwordx4 %[go], %[rq], %[t] offen" QK_NTq " lds\n\ts_add_u32 m0, %[lw], 27648\n\ts_xor_b32 %[t], %[kb], 0x130000\n\tbuffer_load_dwordx4 %[go], %[rq], %[t] offen" QK_NTq " lds\n\ts_add_u32 m0, %[lw], 28672\n\ts_xor_b32 %[t], %[kb], 0x140000\n\tbuffer_load_dwordx4 %[go], %[rq], %[t] offen" QK_NTq " lds\n\ts_add_u32 m0, %[lw], 29696\n\ts_xor_b32 %[t], %[kb], 0x150000\n\tbuffer_load_dwordx4 %[go], %[rq], %[t] offen" QK_NTq " lds\n\ts_add_u32 m0, %[lw], 30720\n\ts_xor_b32 %[t], %[kb], 0x160000\n\tbuffer_load_dwordx4 %[go], %[rq], %[t] offen" QK_NTq " lds\n\ts_add_u32 m0, %[lw], 31744\n\ts_xor_b32 %[t], %[kb], 0x170000\n\tbuffer_load_dwordx4 %[go], %[rq], %[t] offen" QK_NTq " lds\n\t" \
          "s_waitcnt vmcnt(32)\n\tds_read_b64_tr_b16 v[160:161], %[rd0] offset:32768\n\tds_read_b64_tr_b16 v[162:163], %[rd0] offset:32896\n\tds_read_b64_tr_b16 v[164:165], %[rd0] offset:36864\n\tds_read_b64_tr_b16 v[166:167], %[rd0] offset:36992\n\tds_read_b64_tr_b16 v[128:129], %[rd1] offset:32768\n\tds_read_b64_tr_b16 v[130:131], %[rd1] offset:32896\n\tds_read_b64_tr_b16 v[132:133], %[rd1] offset:36864\n\tds_read_b64_tr_b16 v[134:135], %[rd1] offset:36992\n\tds_read_b64_tr_b16 a[0:1], %[rd2] offset:32768\n\tds_read_b64_tr_b16 a[2:3], %[rd2] offset:32896\n\tds_read_b64_tr_b16 a[4:5], %[rd2] offset:36864\n\tds_read_b64_tr_b16 a[6:7], %[rd2] offset:36992\n\tds_read_b64_tr_b16 a[8:9], %[rd3] offset:32768\n\tds_read_b64_tr_b16 a[10:11], %[rd3] offset:32896\n\tds_read_b64_tr_b16 a[12:13], %[rd3] offset:36864\n\tds_read_b64_tr_b16 a[14:15], %[rd3] offset:36992\n\ts_waitcnt lgkmcnt(0)\n\t" \
          "s_add_u32 m0, %[lw], 32768\n\ts_xor_b32 %[t], %[kb], 0x80000\n\tbuffer_load_dwordx4 %[go], %[rq], %[t] offen" QK_NTq " lds\n\ts_add_u32 m0, %[lw], 33792\n\ts_xor_b32 %[t], %[kb], 0x90000\n\tbuffer_load_dwordx4 %[go], %[rq], %[t] offen" QK_NTq " lds\n\ts_add_u32 m0, %[lw], 34816\n\ts_xor_b32 %[t], %[kb], 0xa0000\n\tbuffer_load_dwordx4 %[go], %[rq], %[t] offen" QK_NTq " lds\n\ts_add_u32 m0, %[lw], 35840\n\ts_xor_b32 %[t], %[kb], 0xb0000\n\tbuffer_load_dwordx4 %[go], %[rq], %[t] offen" QK_NTq " lds\n\ts_add_u32 m0, %[lw], 36864\n\ts_xor_b32 %[t], %[kb], 0xc0000\n\tbuffer_load_dwordx4 %[go], %[rq], %[t] offen" QK_NTq " lds\n\ts_add_u32 m0, %[lw], 37888\n\ts_xor_b32 %[t], %[kb], 0xd0000\n\tbuffer_load_dwordx4 %[go], %[rq], %[t] offen" QK_NTq " lds\n\ts_add_u32 m0, %[lw], 38912\n\ts_xor_b32 %[t], %[kb], 0xe0000\n\tbuffer_load_dwordx4 %[go], %[rq], %[t] offen" QK_NTq " lds\n\ts_add_u32 m0, %[lw], 39936\n\ts_xor_b32 %[t], %[kb], 0xf0000\n\tbuffer_load_dwordx4 %[go], %[rq], %[t] offen" QK_NTq " lds\n\t" \
          "s_waitcnt vmcnt(32)\n\tds_read_b64_tr_b16 v[104:105], %[rd0] offset:0\n\tds_read_b64_tr_b16 v[106:107], %[rd0] offset:128\n\tds_read_b64_tr_b16 v[108:109], %[rd0] offset:4096\n\tds_read_b64_tr_b16 v[110:111], %[rd0] offset:4224\n\tds_read_b64_tr_b16 v[168:169], %[rd1] offset:0\n\tds_read_b64_tr_b16 v[170:171], %[rd1] offset:128\n\tds_read_b64_tr_b16 v[172:173], %[rd1] offset:4096\n\tds_read_b64_tr_b16 v[174:175], %[rd1] offset:4224\n\tds_read_b64_tr_b16 v[112:113], %[rd2] offset:0\n\tds_read_b64_tr_b16 v[114:115], %[rd2] offset:128\n\tds_read_b64_tr_b16 v[116:117], %[rd2] offset:4096\n\tds_read_b64_tr_b16 v[118:119], %[rd2] offset:4224\n\tds_read_b64_tr_b16 v[176:177], %[rd3] offset:0\n\tds_read_b64_tr_b16 v[178:179], %[rd3] offset:128\n\tds_read_b64_tr_b16 v[180:181], %[rd3] offset:4096\n\tds_read_b64_tr_b16 v[182:183], %[rd3] offset:4224\n\ts_waitcnt lgkmcnt(0)\n\t" \
          "s_add_u32 m0, %[lw], 0\n\ts_xor_b32 %[t], %[kb], 0x0\n\tbuffer_load_dwordx4 %[go], %[rq], %[t] offen" QK_NTq " lds\n\ts_add_u32 m0, %[lw], 1024\n\ts_xor_b32 %[t], %[kb], 0x10000\n\tbuffer_load_dwordx4 %[go], %[rq], %[t] offen" QK_NTq " lds\n\ts_add_u32 m0, %[lw], 2048\n\ts_xor_b32 %[t], %[kb], 0x20000\n\tbuffer_load_dwordx4 %[go], %[rq], %[t] offen" QK_NTq " lds\n\ts_add_u32 m0, %[lw], 3072\n\ts_xor_b32 %[t], %[kb], 0x30000\n\tbuffer_load_dwordx4 %[go], %[rq], %[t] offen" QK_NTq " lds\n\ts_add_u32 m0, %[lw], 4096\n\ts_xor_b32 %[t], %[kb], 0x40000\n\tbuffer_load_dwordx4 %[go], %[rq], %[t] offen" QK_NTq " lds\n\ts_add_u32 m0, %[lw], 5120\n\ts_xor_b32 %[t], %[kb], 0x50000\n\tbuffer_load_dwordx4 %[go], %[rq], %[t] offen" QK_NTq " lds\n\ts_add_u32 m0, %[lw], 6144\n\ts_xor_b32 %[t], %[kb], 0x60000\n\tbuffer_load_dwordx4 %[go], %[rq], %[t] offen" QK_NTq " lds\n\ts_add_u32 m0, %[lw], 7168\n\ts_xor_b32 %[t], %[kb], 0x70000\n\tbuffer_load_dwordx4 %[go], %[rq], %[t] offen" QK_NTq " lds\n\t" \
          "s_waitcnt vmcnt(32)\n\tds_read_b64_tr_b16 v[184:185], %[rd0] offset:8192\n\tds_read_b64_tr_b16 v[186:187], %[rd0] offset:8320\n\tds_read_b64_tr_b16 v[188:189], %[rd0] offset:12288\n\tds_read_b64_tr_b16 v[190:191], %[rd0] offset:12416\n\tds_read_b64_tr_b16 v[88:89], %[rd1] offset:8192\n\tds_read_b64_tr_b16 v[90:91], %[rd1] offset:8320\n\tds_read_b64_tr_b16 v[92:93], %[rd1] offset:12288\n\tds_read_b64_tr_b16 v[94:95], %[rd1] offset:12416\n\tds_read_b64_tr_b16 v[192:193], %[rd2] offset:8192\n\tds_read_b64_tr_b16 v[194:195], %[rd2] offset:8320\n\tds_read_b64_tr_b16 v[196:197], %[rd2] offset:12288\n\tds_read_b64_tr_b16 v[198:199], %[rd2] offset:12416\n\tds_read_b64_tr_b16 v[96:97], %[rd3] offset:8192\n\tds_read_b64_tr_b16 v[98:99], %[rd3] offset:8320\n\tds_read_b64_tr_b16 v[100:101], %[rd3] offset:12288\n\tds_read_b64_tr_b16 v[102:103], %[rd3] offset:12416\n\ts_waitcnt lgkmcnt(0)\n\tds_write_b128 %[own], v[136:139] offset:8192\n\tds_write_b128 %[own], v[140:143] offset:9216\n\tds_write_b128 %[own], v[144:147] offset:10240\n\tds_write_b128 %[own], v[148:151] offset:11264\n\tds_write_b128 %[own], v[152:155] offset:12288\n\tds_write_b128 %[own], v[156:159] offset:13312\n\tds_write_b128 %[own], v[160:163] offset:14336\n\tds_write_b128 %[own], v[164:167] offset:15360\n\ts_waitcnt lgkmcnt(0)\n\t" \
          "buffer_load_dwordx4 %[rec], %[ro], %[rr], 0 offen sc1\n\t" \
          "s_waitcnt vmcnt(25)\n\tds_read_b64_tr_b16 v[64:65], %[rd0] offset:16384\n\tds_read_b64_tr_b16 v[66:67], %[rd0] offset:16512\n\tds_read_b64_tr_b16 v[68:69], %[rd0] offset:20480\n\tds_read_b64_tr_b16 v[70:71], %[rd0] offset:20608\n\tds_read_b64_tr_b16 v[200:201], %[rd1] offset:16384\n\tds_read_b64_tr_b16 v[202:203], %[rd1] offset:16512\n\tds_read_b64_tr_b16 v[204:205], %[rd1] offset:20480\n\tds_read_b64_tr_b16 v[206:207], %[rd1] offset:20608\n\tds_read_b64_tr_b16 v[72:73], %[rd2] offset:16384\n\tds_read_b64_tr_b16 v[74:75], %[rd2] offset:16512\n\tds_read_b64_tr_b16 v[76:77], %[rd2] offset:20480\n\tds_read_b64_tr_b16 v[78:79], %[rd2] offset:20608\n\tds_read_b64_tr_b16 v[208:209], %[rd3] offset:16384\n\tds_read_b64_tr_b16 v[210:211], %[rd3] offset:16512\n\tds_read_b64_tr_b16 v[212:213], %[rd3] offset:20480\n\tds_read_b64_tr_b16 v[214:215], %[rd3] offset:20608\n\ts_waitcnt lgkmcnt(0)\n\tds_write_b128 %[own], v[168:171] offset:16384\n\tds_write_b128 %[own], v[172:175] offset:17408\n\tds_write_b128 %[own], v[176:179] offset:18432\n\tds_write_b128 %[own], v[180:183] offset:19456\n\tds_write_b128 %[own], v[184:187] offset:20480\n\tds_write_b128 %[own], v[188:191] offset:21504\n\tds_write_b128 %[own], v[192:195] offset:22528\n\tds_write_b128 %[own], v[196:199] offset:23552\n\ts_waitcnt lgkmcnt(0)\n\t" \
          "s_waitcnt vmcnt(17)\n\tds_read_b64_tr_b16 v[216:217], %[rd0] offset:24576\n\tds_read_b64_tr_b16 v[218:219], %[rd0] offset:24704\n\tds_read_b64_tr_b16 v[220:221], %[rd0] offset:28672\n\tds_read_b64_tr_b16 v[222:223], %[rd0] offset:28800\n\tds_read_b64_tr_b16 v[48:49], %[rd1] offset:24576\n\tds_read_b64_tr_b16 v[50:51], %[rd1] offset:24704\n\tds_read_b64_tr_b16 v[52:53], %[rd1] offset:28672\n\tds_read_b64_tr_b16 v[54:55], %[rd1] offset:28800\n\tds_read_b64_tr_b16 v[224:225], %[rd2] offset:24576\n\tds_read_b64_tr_b16 v[226:227], %[rd2] offset:24704\n\tds_read_b64_tr_b16 v[228:229], %[rd2] offset:28672\n\tds_read_b64_tr_b16 v[230:231], %[rd2] offset:28800\n\tds_read_b64_tr_b16 v[56:57], %[rd3] offset:24576\n\tds_read_b64_tr_b16 v[58:59], %[rd3] offset:24704\n\tds_read_b64_tr_b16 v[60:61], %[rd3] offset:28672\n\tds_read_b64_tr_b16 v[62:63], %[rd3] offset:28800\n\ts_waitcnt lgkmcnt(0)\n\tds_write_b128 %[own], v[200:203] offset:24576\n\tds_write_b128 %[own], v[204:207] offset:25600\n\tds_write_b128 %[own], v[208:211] offset:26624\n\tds_write_b128 %[own], v[212:215] offset:27648\n\tds_write_b128 %[own], v[216:219] offset:28672\n\tds_write_b128 %[own], v[220:223] offset:29696\n\tds_write_b128 %[own], v[224:227] offset:30720\n\tds_write_b128 %[own], v[228:231] offset:31744\n\ts_waitcnt lgkmcnt(0)\n\t" \
          "s_waitcnt vmcnt(9)\n\tds_read_b64_tr_b16 v[24:25], %[rd0] offset:32768\n\tds_read_b64_tr_b16 v[26:27], %[rd0] offset:32896\n\tds_read_b64_tr_b16 v[28:29], %[rd0] offset:36864\n\tds_read_b64_tr_b16 v[30:31], %[rd0] offset:36992\n\tds_read_b64_tr_b16 v[136:137], %[rd1] offset:32768\n\tds_read_b64_tr_b16 v[138:139], %[rd1] offset:32896\n\tds_read_b64_tr_b16 v[140:141], %[rd1] offset:36864\n\tds_read_b64_tr_b16 v[142:143], %[rd1] offset:36992\n\tds_read_b64_tr_b16 v[32:33], %[rd2] offset:32768\n\tds_read_b64_tr_b16 v[34:35], %[rd2] offset:32896\n\tds_read_b64_tr_b16 v[36:37], %[rd2] offset:36864\n\tds_read_b64_tr_b16 v[38:39], %[rd2] offset:36992\n\tds_read_b64_tr_b16 v[144:145], %[rd3] offset:32768\n\tds_read_b64_tr_b16 v[146:147], %[rd3] offset:32896\n\tds_read_b64_tr_b16 v[148:149], %[rd3] offset:36864\n\tds_read_b64_tr_b16 v[150:151], %[rd3] offset:36992\n\ts_waitcnt lgkmcnt(0)\n\t" \
          "s_waitcnt vmcnt(0)\n\tds_read_b64_tr_b16 v[152:153], %[rd0] offset:0\n\tds_read_b64_tr_b16 v[154:155], %[rd0] offset:128\n\tds_read_b64_tr_b16 v[156:157], %[rd0] offset:4096\n\tds_read_b64_tr_b16 v[158:159], %[rd0] offset:4224\n\tds_read_b64_tr_b16 v[8:9], %[rd1] offset:0\n\tds_read_b64_tr_b16 v[10:11], %[rd1] offset:128\n\tds_read_b64_tr_b16 v[12:13], %[rd1] offset:4096\n\tds_read_b64_tr_b16 v[14:15], %[rd1] offset:4224\n\tds_read_b64_tr_b16 v[160:161], %[rd2] offset:0\n\tds_read_b64_tr_b16 v[162:163], %[rd2] offset:128\n\tds_read_b64_tr_b16 v[164:165], %[rd2] offset:4096\n\tds_read_b64_tr_b16 v[166:167], %[rd2] offset:4224\n\tds_read_b64_tr_b16 v[16:17], %[rd3] offset:0\n\tds_read_b64_tr_b16 v[18:19], %[rd3] offset:128\n\tds_read_b64_tr_b16 v[20:21], %[rd3] offset:4096\n\tds_read_b64_tr_b16 v[22:23], %[rd3] offset:4224\n\ts_waitcnt lgkmcnt(0)\n\tds_write_b128 %[own], v[136:139] offset:0\n\tds_write_b128 %[own], v[140:143] offset:1024\n\tds_write_b128 %[own], v[144:147] offset:2048\n\tds_write_b128 %[own], v[148:151] offset:3072\n\tds_write_b128 %[own], v[152:155] offset:4096\n\tds_write_b128 %[own], v[156:159] offset:5120\n\tds_write_b128 %[own], v[160:163] offset:6144\n\tds_write_b128 %[own], v[164:167] offset:7168\n\ts_waitcnt lgkmcnt(0)\n\t"
#define QK_DEFW "=&{a[0:31]}"(wa[0]), "=&{a[32:63]}"(wa[1]), "=&{a[64:95]}"(wa[2]), "=&{a[96:127]}"(wa[3]), "=&{a[128:159]}"(wa[4]), "=&{a[160:191]}"(wa[5]), "=&{a[192:223]}"(wa[6]), "=&{a[224:255]}"(wa[7]), "=&{v[8:39]}"(wv_[0]), "=&{v[40:71]}"(wv_[1]), "=&{v[72:103]}"(wv_[2]), "=&{v[104:135]}"(wv_[3]), "=&{v[136:167]}"(wv_[4]), "=&{v[168:199]}"(wv_[5]), "=&{v[200:231]}"(wv_[6])
#define QK_STEP \
          "ds_read_b128 v[200:203], %[own] offset:4096\n\t" \
          "ds_read_b128 v[204:207], %[own] offset:5120\n\t" \
          "ds_read_b128 v[208:211], %[own] offset:6144\n\t" \
          "ds_read_b128 v[212:215], %[own] offset:7168\n\t" \
          "ds_read_b128 v[216:219], %[own] offset:8192\n\t" \
          "ds_read_b128 v[220:223], %[own] offset:9216\n\t" \
          "ds_read_b128 v[224:227], %[own] offset:0\n\t" \
          "ds_read_b128 v[228:231], %[own] offset:1024\n\t" \
          "v_mov_b64 v[168:169], 0\n\t" \
          "v_mov_b64 v[170:171], 0\n\t" \
          "v_mov_b64 v[172:173], 0\n\t" \
          "v_mov_b64 v[174:175], 0\n\t" \
          "v_mov_b64 v[176:177], 0\n\t" \
          "v_mov_b64 v[178:179], 0\n\t" \
          "v_mov_b64 v[180:181], 0\n\t" \
          "v_mov_b64 v[182:183], 0\n\t" \
          "v_mov_b64 v[184:185], 0\n\t" \
          "v_mov_b64 v[186:187], 0\n\t" \
          "v_mov_b64 v[188:189], 0\n\t" \
          "v_mov_b64 v[190:191], 0\n\t" \
          "v_mov_b64 v[192:193], 0\n\t" \
          "v_mov_b64 v[194:195], 0\n\t" \
          "v_mov_b64 v[196:197], 0\n\t" \
          "v_mov_b64 v[198:199], 0\n\t" \
          "s_mov_b32 %[ann], %[t]\n\t" \
          "s_mov_b32 %[fl], 0\n\t" \
          "s_mov_b64 %[en], -1\n\t" \
          "s_waitcnt lgkmcnt(0)\n\t" \
          "s_cmp_eq_u32 %[w], 3\n\t" \
          "s_cbranch_scc0 Lqk_z%=\n\t" \
          "s_branch Lqk_c%=\n\t" \
          "Lqk_fail%=:\n\t" \
          "s_memrealtime vcc\n\t" \
          "s_waitcnt lgkmcnt(0)\n\t" \
          "s_add_u32 %[ann], vcc_lo, %[ald0]\n\t" \
          "v_mbcnt_lo_u32_b32 %[al], -1, 0\n\t" \
          "v_lshlrev_b32 %[al], 2, %[al]\n\t" \
          "s_and_b32 vcc_lo, %[ann], 0xffff\n\t" \
          "s_or_b32 vcc_lo, vcc_lo, 0x10000\n\t" \
          "v_mov_b32 %[m], vcc_lo\n\t" \
          "s_mov_b64 exec, %[alx]\n\t" \
          "buffer_store_dword %[m], %[al], %[rsm], %[als] offen sc1\n\t" \
          "s_mov_b64 exec, -1\n\t" \
          "buffer_load_dwordx4 v[136:139], %[ml0], %[rsm], %[mb0] offen sc1\n\t" \
          "buffer_load_dwordx4 v[140:143], %[ml1], %[rsm], %[mb0] offen sc1\n\t" \
          "buffer_load_dwordx4 v[144:147], %[ml0], %[rsm], %[mb1] offen sc1\n\t" \
          "buffer_load_dwordx4 v[148:151], %[ml1], %[rsm], %[mb1] offen sc1\n\t" \
          "buffer_load_dwordx4 v[152:155], %[ml0], %[rsm], %[mb2] offen sc1\n\t" \
          "buffer_load_dwordx4 v[156:159], %[ml1], %[rsm], %[mb2] offen sc1\n\t" \
          "buffer_load_dwordx4 v[160:163], %[ml0], %[rsm], %[mb3] offen sc1\n\t" \
          "buffer_load_dwordx4 v[164:167], %[ml1], %[rsm], %[mb3] offen sc1\n\t" \
          "ds_read_b128 v[200:203], %[own] offset:4096\n\t" \
          "ds_read_b128 v[204:207], %[own] offset:5120\n\t" \
          "ds_read_b128 v[208:211], %[own] offset:6144\n\t" \
          "ds_read_b128 v[212:215], %[own] offset:7168\n\t" \
          "ds_read_b128 v[216:219], %[own] offset:8192\n\t" \
          "ds_read_b128 v[220:223], %[own] offset:9216\n\t" \
          "ds_read_b128 v[224:227], %[own] offset:0\n\t" \
          "ds_read_b128 v[228:231], %[own] offset:1024\n\t" \
          "v_mov_b64 v[168:169], 0\n\t" \
          "v_mov_b64 v[170:171], 0\n\t" \
          "v_mov_b64 v[172:173], 0\n\t" \
          "v_mov_b64 v[174:175], 0\n\t" \
          "v_mov_b64 v[176:177], 0\n\t" \
          "v_mov_b64 v[178:179], 0\n\t" \
          "v_mov_b64 v[180:181], 0\n\t" \
          "v_mov_b64 v[182:183], 0\n\t" \
          "v_mov_b64 v[184:185], 0\n\t" \
          "v_mov_b64 v[186:187], 0\n\t" \
          "v_mov_b64 v[188:189], 0\n\t" \
          "v_mov_b64 v[190:191], 0\n\t" \
          "v_mov_b64 v[192:193], 0\n\t" \
          "v_mov_b64 v[194:195], 0\n\t" \
          "v_mov_b64 v[196:197], 0\n\t" \
          "v_mov_b64 v[198:199], 0\n\t" \
          "s_mov_b32 %[fl], 1\n\t" \
          "s_mov_b64 %[en], 0\n\t" \
          "s_mov_b32 %[cnt], 0\n\t" \
          "s_waitcnt vmcnt(7)\n\t" \
          "v_max3_u32 %[m], v136, v137, v138\n\t" \
          "v_max_u32 %[m], %[m], v139\n\t" \
          "v_cmp_eq_u32 vcc, -1, %[m]\n\t" \
          "s_cbranch_vccnz Lqk_p0_%=\n\t" \
          "Lqk_k0_%=:\n\t" \
          "s_waitcnt vmcnt(6)\n\t" \
          "v_max3_u32 %[m], v140, v141, v142\n\t" \
          "v_max_u32 %[m], %[m], v143\n\t" \
          "v_cmp_eq_u32 vcc, -1, %[m]\n\t" \
          "s_cbranch_vccnz Lqk_p1_%=\n\t" \
          "Lqk_k1_%=:\n\t" \
          "s_waitcnt vmcnt(5)\n\t" \
          "v_max3_u32 %[m], v144, v145, v146\n\t" \
          "v_max_u32 %[m], %[m], v147\n\t" \
          "v_cmp_eq_u32 vcc, -1, %[m]\n\t" \
          "s_cbranch_vccnz Lqk_p2_%=\n\t" \
          "Lqk_k2_%=:\n\t" \
          "s_waitcnt vmcnt(4)\n\t" \
          "v_max3_u32 %[m], v148, v149, v150\n\t" \
          "v_max_u32 %[m], %[m], v151\n\t" \
          "v_cmp_eq_u32 vcc, -1, %[m]\n\t" \
          "s_cbranch_vccnz Lqk_p3_%=\n\t" \
          "Lqk_k3_%=:\n\t" \
          "s_waitcnt vmcnt(3)\n\t" \
          "v_max3_u32 %[m], v152, v153, v154\n\t" \
          "v_max_u32 %[m], %[m], v155\n\t" \
          "v_cmp_eq_u32 vcc, -1, %[m]\n\t" \
          "s_cbranch_vccnz Lqk_p4_%=\n\t" \
          "Lqk_k4_%=:\n\t" \
          "s_waitcnt vmcnt(2)\n\t" \
          "v_max3_u32 %[m], v156, v157, v158\n\t" \
          "v_max_u32 %[m], %[m], v159\n\t" \
          "v_cmp_eq_u32 vcc, -1, %[m]\n\t" \
          "s_cbranch_vccnz Lqk_p5_%=\n\t" \
          "Lqk_k5_%=:\n\t" \
          "s_waitcnt vmcnt(1)\n\t" \
          "v_max3_u32 %[m], v160, v161, v162\n\t" \
          "v_max_u32 %[m], %[m], v163\n\t" \
          "v_cmp_eq_u32 vcc, -1, %[m]\n\t" \
          "s_cbranch_vccnz Lqk_p6_%=\n\t" \
          "Lqk_k6_%=:\n\t" \
          "s_waitcnt vmcnt(0)\n\t" \
          "v_max3_u32 %[m], v164, v165, v166\n\t" \
          "v_max_u32 %[m], %[m], v167\n\t" \
          "v_cmp_eq_u32 vcc, -1, %[m]\n\t" \
          "s_cbranch_vccnz Lqk_p7_%=\n\t" \
          "Lqk_k7_%=:\n\t" \
          "s_memrealtime vcc\n\t" \
          "s_waitcnt lgkmcnt(0)\n\t" \
          "s_add_u32 vcc_lo, vcc_lo, %[ald]\n\t" \
          "s_sub_i32 vcc_hi, vcc_lo, %[ann]\n\t" \
          "s_cmp_gt_i32 vcc_hi, 0\n\t" \
          "s_cbranch_scc0 Lqk_go%=\n\t" \
          "s_mov_b32 %[ann], vcc_lo\n\t" \
          "v_mbcnt_lo_u32_b32 %[al], -1, 0\n\t" \
          "v_lshlrev_b32 %[al], 2, %[al]\n\t" \
          "s_and_b32 vcc_lo, %[ann], 0xffff\n\t" \
          "s_or_b32 vcc_lo, vcc_lo, 0x10000\n\t" \
          "v_mov_b32 %[m], vcc_lo\n\t" \
          "s_mov_b64 exec, %[alx]\n\t" \
          "buffer_store_dword %[m], %[al], %[rsm], %[als] offen sc1\n\t" \
          "s_mov_b64 exec, -1\n\t" \
          "s_branch Lqk_go%=\n\t" \
          "Lqk_p0_%=:\n\t" \
          "s_cmp_lt_i32 %[cnt], 0\n\t" \
          "s_cbranch_scc1 Lqk_k0_%=\n\t" \
          "s_add_u32 %[cnt], %[cnt], 1\n\t" \
          "s_and_b32 vcc_lo, %[cnt], 3\n\t" \
          "s_cbranch_scc1 Lqk_q0_%=\n\t" \
          "s_getpc_b64 s[48:49]\n\t" \
          "s_branch Lqk_spin%=\n\t" \
          "Lqk_q0_%=:\n\t" \
          "buffer_load_dwordx4 v[136:139], %[ml0], %[rsm], %[mb0] offen sc1\n\t" \
          "s_waitcnt vmcnt(0)\n\t" \
          "v_max3_u32 %[m], v136, v137, v138\n\t" \
          "v_max_u32 %[m], %[m], v139\n\t" \
          "v_cmp_eq_u32 vcc, -1, %[m]\n\t" \
          "s_cbranch_vccnz Lqk_p0_%=\n\t" \
          "buffer_load_dwordx4 v[140:143], %[ml1], %[rsm], %[mb0] offen sc1\n\t" \
          "buffer_load_dwordx4 v[144:147], %[ml0], %[rsm], %[mb1] offen sc1\n\t" \
          "buffer_load_dwordx4 v[148:151], %[ml1], %[rsm], %[mb1] offen sc1\n\t" \
          "buffer_load_dwordx4 v[152:155], %[ml0], %[rsm], %[mb2] offen sc1\n\t" \
          "buffer_load_dwordx4 v[156:159], %[ml1], %[rsm], %[mb2] offen sc1\n\t" \
          "buffer_load_dwordx4 v[160:163], %[ml0], %[rsm], %[mb3] offen sc1\n\t" \
          "buffer_load_dwordx4 v[164:167], %[ml1], %[rsm], %[mb3] offen sc1\n\t" \
          "s_branch Lqk_k0_%=\n\t" \
          "Lqk_p1_%=:\n\t" \
          "s_cmp_lt_i32 %[cnt], 0\n\t" \
          "s_cbranch_scc1 Lqk_k1_%=\n\t" \
          "s_add_u32 %[cnt], %[cnt], 1\n\t" \
          "s_and_b32 vcc_lo, %[cnt], 3\n\t" \
          "s_cbranch_scc1 Lqk_q1_%=\n\t" \
          "s_getpc_b64 s[48:49]\n\t" \
          "s_branch Lqk_spin%=\n\t" \
          "Lqk_q1_%=:\n\t" \
          "buffer_load_dwordx4 v[140:143], %[ml1], %[rsm], %[mb0] offen sc1\n\t" \
          "s_waitcnt vmcnt(0)\n\t" \
          "v_max3_u32 %[m], v140, v141, v142\n\t" \
          "v_max_u32 %[m], %[m], v143\n\t" \
          "v_cmp_eq_u32 vcc, -1, %[m]\n\t" \
          "s_cbranch_vccnz Lqk_p1_%=\n\t" \
          "buffer_load_dwordx4 v[144:147], %[ml0], %[rsm], %[mb1] offen sc1\n\t" \
          "buffer_load_dwordx4 v[148:151], %[ml1], %[rsm], %[mb1] offen sc1\n\t" \
          "buffer_load_dwordx4 v[152:155], %[ml0], %[rsm], %[mb2] offen sc1\n\t" \
          "buffer_load_dwordx4 v[156:159], %[ml1], %[rsm], %[mb2] offen sc1\n\t" \
          "buffer_load_dwordx4 v[160:163], %[ml0], %[rsm], %[mb3] offen sc1\n\t" \
          "buffer_load_dwordx4 v[164:167], %[ml1], %[rsm], %[mb3] offen sc1\n\t" \
          "s_branch Lqk_k1_%=\n\t" \
          "Lqk_p2_%=:\n\t" \
          "s_cmp_lt_i32 %[cnt], 0\n\t" \
          "s_cbranch_scc1 Lqk_k2_%=\n\t" \
          "s_add_u32 %[cnt], %[cnt], 1\n\t" \
          "s_and_b32 vcc_lo, %[cnt], 3\n\t" \
          "s_cbranch_scc1 Lqk_q2_%=\n\t" \
          "s_getpc_b64 s[48:49]\n\t" \
          "s_branch Lqk_spin%=\n\t" \
          "Lqk_q2_%=:\n\t" \
          "buffer_load_dwordx4 v[144:147], %[ml0], %[rsm], %[mb1] offen sc1\n\t" \
          "s_waitcnt vmcnt(0)\n\t" \
          "v_max3_u32 %[m], v144, v145, v146\n\t" \
          "v_max_u32 %[m], %[m], v147\n\t" \
          "v_cmp_eq_u32 vcc, -1, %[m]\n\t" \
          "s_cbranch_vccnz Lqk_p2_%=\n\t" \
          "buffer_load_dwordx4 v[148:151], %[ml1], %[rsm], %[mb1] offen sc1\n\t" \
          "buffer_load_dwordx4 v[152:155], %[ml0], %[rsm], %[mb2] offen sc1\n\t" \
          "buffer_load_dwordx4 v[156:159], %[ml1], %[rsm], %[mb2] offen sc1\n\t" \
          "buffer_load_dwordx4 v[160:163], %[ml0], %[rsm], %[mb3] offen sc1\n\t" \
          "buffer_load_dwordx4 v[164:167], %[ml1], %[rsm], %[mb3] offen sc1\n\t" \
          "s_branch Lqk_k2_%=\n\t" \
          "Lqk_p3_%=:\n\t" \
          "s_cmp_lt_i32 %[cnt], 0\n\t" \
          "s_cbranch_scc1 Lqk_k3_%=\n\t" \
          "s_add_u32 %[cnt], %[cnt], 1\n\t" \
          "s_and_b32 vcc_lo, %[cnt], 3\n\t" \
          "s_cbranch_scc1 Lqk_q3_%=\n\t" \
          "s_getpc_b64 s[48:49]\n\t" \
          "s_branch Lqk_spin%=\n\t" \
          "Lqk_q3_%=:\n\t" \
          "buffer_load_dwordx4 v[148:151], %[ml1], %[rsm], %[mb1] offen sc1\n\t" \
          "s_waitcnt vmcnt(0)\n\t" \
          "v_max3_u32 %[m], v148, v149, v150\n\t" \
          "v_max_u32 %[m], %[m], v151\n\t" \
          "v_cmp_eq_u32 vcc, -1, %[m]\n\t" \
          "s_cbranch_vccnz Lqk_p3_%=\n\t" \
          "buffer_load_dwordx4 v[152:155], %[ml0], %[rsm], %[mb2] offen sc1\n\t" \
          "buffer_load_dwordx4 v[156:159], %[ml1], %[rsm], %[mb2] offen sc1\n\t" \
          "buffer_load_dwordx4 v[160:163], %[ml0], %[rsm], %[mb3] offen sc1\n\t" \
          "buffer_load_dwordx4 v[164:167], %[ml1], %[rsm], %[mb3] offen sc1\n\t" \
          "s_branch Lqk_k3_%=\n\t" \
          "Lqk_p4_%=:\n\t" \
          "s_cmp_lt_i32 %[cnt], 0\n\t" \
          "s_cbranch_scc1 Lqk_k4_%=\n\t" \
          "s_add_u32 %[cnt], %[cnt], 1\n\t" \
          "s_and_b32 vcc_lo, %[cnt], 3\n\t" \
          "s_cbranch_scc1 Lqk_q4_%=\n\t" \
          "s_getpc_b64 s[48:49]\n\t" \
          "s_branch Lqk_spin%=\n\t" \
          "Lqk_q4_%=:\n\t" \
          "buffer_load_dwordx4 v[152:155], %[ml0], %[rsm], %[mb2] offen sc1\n\t" \
          "s_waitcnt vmcnt(0)\n\t" \
          "v_max3_u32 %[m], v152, v153, v154\n\t" \
          "v_max_u32 %[m], %[m], v155\n\t" \
          "v_cmp_eq_u32 vcc, -1, %[m]\n\t" \
          "s_cbranch_vccnz Lqk_p4_%=\n\t" \
          "buffer_load_dwordx4 v[156:159], %[ml1], %[rsm], %[mb2] offen sc1\n\t" \
          "buffer_load_dwordx4 v[160:163], %[ml0], %[rsm], %[mb3] offen sc1\n\t" \
          "buffer_load_dwordx4 v[164:167], %[ml1], %[rsm], %[mb3] offen sc1\n\t" \
          "s_branch Lqk_k4_%=\n\t" \
          "Lqk_p5_%=:\n\t" \
          "s_cmp_lt_i32 %[cnt], 0\n\t" \
          "s_cbranch_scc1 Lqk_k5_%=\n\t" \
          "s_add_u32 %[cnt], %[cnt], 1\n\t" \
          "s_and_b32 vcc_lo, %[cnt], 3\n\t" \
          "s_cbranch_scc1 Lqk_q5_%=\n\t" \
          "s_getpc_b64 s[48:49]\n\t" \
          "s_branch Lqk_spin%=\n\t" \
          "Lqk_q5_%=:\n\t" \
          "buffer_load_dwordx4 v[156:159], %[ml1], %[rsm], %[mb2] offen sc1\n\t" \
          "s_waitcnt vmcnt(0)\n\t" \
          "v_max3_u32 %[m], v156, v157, v158\n\t" \
          "v_max_u32 %[m], %[m], v159\n\t" \
          "v_cmp_eq_u32 vcc, -1, %[m]\n\t" \
          "s_cbranch_vccnz Lqk_p5_%=\n\t" \
          "buffer_load_dwordx4 v[160:163], %[ml0], %[rsm], %[mb3] offen sc1\n\t" \
          "buffer_load_dwordx4 v[164:167], %[ml1], %[rsm], %[mb3] offen sc1\n\t" \
          "s_branch Lqk_k5_%=\n\t" \
          "Lqk_p6_%=:\n\t" \
          "s_cmp_lt_i32 %[cnt], 0\n\t" \
          "s_cbranch_scc1 Lqk_k6_%=\n\t" \
          "s_add_u32 %[cnt], %[cnt], 1\n\t" \
          "s_and_b32 vcc_lo, %[cnt], 3\n\t" \
          "s_cbranch_scc1 Lqk_q6_%=\n\t" \
          "s_getpc_b64 s[48:49]\n\t" \
          "s_branch Lqk_spin%=\n\t" \
          "Lqk_q6_%=:\n\t" \
          "buffer_load_dwordx4 v[160:163], %[ml0], %[rsm], %[mb3] offen sc1\n\t" \
          "s_waitcnt vmcnt(0)\n\t" \
          "v_max3_u32 %[m], v160, v161, v162\n\t" \
          "v_max_u32 %[m], %[m], v163\n\t" \
          "v_cmp_eq_u32 vcc, -1, %[m]\n\t" \
          "s_cbranch_vccnz Lqk_p6_%=\n\t" \
          "buffer_load_dwordx4 v[164:167], %[ml1], %[rsm], %[mb3] offen sc1\n\t" \
          "s_branch Lqk_k6_%=\n\t" \
          "Lqk_p7_%=:\n\t" \
          "s_cmp_lt_i32 %[cnt], 0\n\t" \
          "s_cbranch_scc1 Lqk_k7_%=\n\t" \
          "s_add_u32 %[cnt], %[cnt], 1\n\t" \
          "s_and_b32 vcc_lo, %[cnt], 3\n\t" \
          "s_cbranch_scc1 Lqk_q7_%=\n\t" \
          "s_getpc_b64 s[48:49]\n\t" \
          "s_branch Lqk_spin%=\n\t" \
          "Lqk_q7_%=:\n\t" \
          "buffer_load_dwordx4 v[164:167], %[ml1], %[rsm], %[mb3] offen sc1\n\t" \
          "s_waitcnt vmcnt(0)\n\t" \
          "v_max3_u32 %[m], v164, v165, v166\n\t" \
          "v_max_u32 %[m], %[m], v167\n\t" \
          "v_cmp_eq_u32 vcc, -1, %[m]\n\t" \
          "s_cbranch_vccnz Lqk_p7_%=\n\t" \
          "s_branch Lqk_k7_%=\n\t" \
          "Lqk_spin%=:\n\t" \
          "s_memrealtime vcc\n\t" \
          "s_waitcnt lgkmcnt(0)\n\t" \
          "s_add_u32 %[ann], vcc_lo, %[ald0]\n\t" \
          "v_mbcnt_lo_u32_b32 %[al], -1, 0\n\t" \
          "v_lshlrev_b32 %[al], 2, %[al]\n\t" \
          "s_and_b32 vcc_lo, %[ann], 0xffff\n\t" \
          "s_or_b32 vcc_lo, vcc_lo, 0x10000\n\t" \
          "v_mov_b32 %[m], vcc_lo\n\t" \
          "s_mov_b64 exec, %[alx]\n\t" \
          "buffer_store_dword %[m], %[al], %[rsm], %[als] offen sc1\n\t" \
          "s_mov_b64 exec, -1\n\t" \
          "buffer_load_dword %[al], off, %[rsc], %[tmo] sc1\n\t" \
          "s_add_u32 s48, s48, 4\n\t" \
          "s_addc_u32 s49, s49, 0\n\t" \
          "s_waitcnt vmcnt(0)\n\t" \
          "v_readfirstlane_b32 vcc_lo, %[al]\n\t" \
          "s_lshr_b32 vcc_hi, %[cnt], 16\n\t" \
          "s_or_b32 vcc_lo, vcc_lo, vcc_hi\n\t" \
          "s_cbranch_scc0 Lqk_back%=\n\t" \
          "v_mov_b32 %[al], 1\n\t" \
          "buffer_store_dword %[al], off, %[rsc], %[tmo] sc1\n\t" \
          "s_mov_b32 %[cnt], -1\n\t" \
          "Lqk_back%=:\n\t" \
          "s_setpc_b64 s[48:49]\n\t" \
          "Lqk_s%=:\n\t" \
          "s_sleep 2\n\t" \
          "Lqk_c%=:\n\t" \
          "s_memrealtime vcc\n\t" \
          "s_waitcnt lgkmcnt(0)\n\t" \
          "s_sub_i32 vcc_lo, %[t], vcc_lo\n\t" \
          "s_cmp_gt_i32 vcc_lo, 16\n\t" \
          "s_cbranch_scc1 Lqk_s%=\n\t" \
          "Lqk_w%=:\n\t" \
          "s_memrealtime s[50:51]\n\t" \
          "s_getpc_b64 s[48:49]\n\t" \
          "Lqk_y%=:\n\t" \
          "s_add_u32 s48, s48, Lqk_z%=-Lqk_y%=\n\t" \
          "s_addc_u32 s49, s49, 0\n\t" \
          "Lqk_v%=:\n\t" \
          "s_memrealtime s[52:53]\n\t" \
          "s_waitcnt lgkmcnt(1)\n\t" \
          "s_sub_i32 vcc_lo, %[t], s50\n\t" \
          "s_cmp_gt_i32 vcc_lo, 3\n\t" \
          "s_cbranch_scc0 Lqk_x%=\n\t" \
          "s_memrealtime s[50:51]\n\t" \
          "s_waitcnt lgkmcnt(1)\n\t" \
          "s_sub_i32 vcc_lo, %[t], s52\n\t" \
          "s_cmp_gt_i32 vcc_lo, 3\n\t" \
          "s_cbranch_scc0 Lqk_x%=\n\t" \
          "s_memrealtime s[52:53]\n\t" \
          "s_waitcnt lgkmcnt(1)\n\t" \
          "s_sub_i32 vcc_lo, %[t], s50\n\t" \
          "s_cmp_gt_i32 vcc_lo, 3\n\t" \
          "s_cbranch_scc0 Lqk_x%=\n\t" \
          "s_memrealtime s[50:51]\n\t" \
          "s_waitcnt lgkmcnt(1)\n\t" \
          "s_sub_i32 vcc_lo, %[t], s52\n\t" \
          "s_cmp_gt_i32 vcc_lo, 3\n\t" \
          "s_cbranch_scc0 Lqk_x%=\n\t" \
          "s_memrealtime s[52:53]\n\t" \
          "s_waitcnt lgkmcnt(1)\n\t" \
          "s_sub_i32 vcc_lo, %[t], s50\n\t" \
          "s_cmp_gt_i32 vcc_lo, 3\n\t" \
          "s_cbranch_scc0 Lqk_x%=\n\t" \
          "s_memrealtime s[50:51]\n\t" \
          "s_waitcnt lgkmcnt(1)\n\t" \
          "s_sub_i32 vcc_lo, %[t], s52\n\t" \
          "s_cmp_gt_i32 vcc_lo, 3\n\t" \
          "s_cbranch_scc0 Lqk_x%=\n\t" \
          "s_memrealtime s[52:53]\n\t" \
          "s_waitcnt lgkmcnt(1)\n\t" \
          "s_sub_i32 vcc_lo, %[t], s50\n\t" \
          "s_cmp_gt_i32 vcc_lo, 3\n\t" \
          "s_cbranch_scc0 Lqk_x%=\n\t" \
          "s_memrealtime s[50:51]\n\t" \
          "s_waitcnt lgkmcnt(1)\n\t" \
          "s_sub_i32 vcc_lo, %[t], s52\n\t" \
          "s_cmp_gt_i32 vcc_lo, 3\n\t" \
          "s_cbranch_scc0 Lqk_x%=\n\t" \
          "s_branch Lqk_v%=\n\t" \
          "Lqk_x%=:\n\t" \
          "s_max_i32 vcc_lo, vcc_lo, 0\n\t" \
          "s_lshl_b32 vcc_lo, vcc_lo, 2\n\t" \
          "s_sub_u32 s48, s48, vcc_lo\n\t" \
          "s_subb_u32 s49, s49, 0\n\t" \
          "s_setpc_b64 s[48:49]\n\t" \
          "s_nop 5\n\t" \
          "s_nop 5\n\t" \
          "s_nop 5\n\t" \
          "Lqk_z%=:\n\t" \
          "s_barrier\n\t" \
          "buffer_load_dwordx4 v[136:139], %[ml0], %[rsm], %[mb0] offen sc1\n\t" \
          "s_nop 9\n\t" \
          "buffer_load_dwordx4 v[140:143], %[ml1], %[rsm], %[mb0] offen sc1\n\t" \
          "s_nop 9\n\t" \
          "buffer_load_dwordx4 v[144:147], %[ml0], %[rsm], %[mb1] offen sc1\n\t" \
          "s_nop 9\n\t" \
          "buffer_load_dwordx4 v[148:151], %[ml1], %[rsm], %[mb1] offen sc1\n\t" \
          "s_nop 9\n\t" \
          "buffer_load_dwordx4 v[152:155], %[ml0], %[rsm], %[mb2] offen sc1\n\t" \
          "s_nop 9\n\t" \
          "buffer_load_dwordx4 v[156:159], %[ml1], %[rsm], %[mb2] offen sc1\n\t" \
          "s_nop 9\n\t" \
          "buffer_load_dwordx4 v[160:163], %[ml0], %[rsm], %[mb3] offen sc1\n\t" \
          "s_nop 9\n\t" \
          "buffer_load_dwordx4 v[164:167], %[ml1], %[rsm], %[mb3] offen sc1\n\t" \
          "s_waitcnt vmcnt(7)\n\t" \
          "Lqk_go%=:\n\t" \
          "v_smfmac_f32_16x16x64_bf16 v[168:171], v[136:139], v[200:207], %[idx]\n\t" \
          "ds_read_b128 v[200:203], %[own] offset:2048\n\t" \
          "v_smfmac_f32_16x16x64_bf16 v[172:175], v[136:139], v[8:15], %[idx]\n\t" \
          "ds_read_b128 v[204:207], %[own] offset:3072\n\t" \
          "v_smfmac_f32_16x16x64_bf16 v[176:179], v[136:139], v[208:215], %[idx]\n\t" \
          "ds_read_b128 v[208:211], %[own] offset:28672\n\t" \
          "v_smfmac_f32_16x16x64_bf16 v[180:183], v[136:139], v[16:23], %[idx]\n\t" \
          "ds_read_b128 v[212:215], %[own] offset:29696\n\t" \
          "s_waitcnt vmcnt(6)\n\t" \
          "v_smfmac_f32_16x16x64_bf16 v[184:187], v[136:139], v[216:223], %[idx]\n\t" \
          "ds_read_b128 v[216:219], %[own] offset:30720\n\t" \
          "v_smfmac_f32_16x16x64_bf16 v[168:171], v[140:143], v[24:31], %[idx]\n\t" \
          "ds_read_b128 v[220:223], %[own] offset:31744\n\t" \
          "v_smfmac_f32_16x16x64_bf16 v[172:175], v[140:143], v[224:231], %[idx]\n\t" \
          "ds_read_b128 v[224:227], %[own] offset:10240\n\t" \
          "v_smfmac_f32_16x16x64_bf16 v[176:179], v[140:143], v[32:39], %[idx]\n\t" \
          "ds_read_b128 v[228:231], %[own] offset:11264\n\t" \
          "s_waitcnt lgkmcnt(6)\n\t" \
          "v_smfmac_f32_16x16x64_bf16 v[180:183], v[140:143], v[200:207], %[idx]\n\t" \
          "ds_read_b128 v[200:203], %[own] offset:24576\n\t" \
          "v_max3_u32 %[m], v136, v137, v138\n\t" \
          "v_smfmac_f32_16x16x64_bf16 v[184:187], v[140:143], v[40:47], %[idx]\n\t" \
          "ds_read_b128 v[204:207], %[own] offset:25600\n\t" \
          "s_waitcnt vmcnt(5) lgkmcnt(6)\n\t" \
          "v_smfmac_f32_16x16x64_bf16 v[168:171], v[144:147], v[208:215], %[idx]\n\t" \
          "ds_read_b128 v[208:211], %[own] offset:26624\n\t" \
          "v_max3_u32 %[m], v138, v139, %[m]\n\t" \
          "v_smfmac_f32_16x16x64_bf16 v[172:175], v[144:147], v[48:55], %[idx]\n\t" \
          "ds_read_b128 v[212:215], %[own] offset:27648\n\t" \
          "s_waitcnt lgkmcnt(6)\n\t" \
          "v_smfmac_f32_16x16x64_bf16 v[176:179], v[144:147], v[216:223], %[idx]\n\t" \
          "ds_read_b128 v[216:219], %[own] offset:20480\n\t" \
          "v_cmp_eq_u32 %[bad1], -1, %[m]\n\t" \
          "v_smfmac_f32_16x16x64_bf16 v[180:183], v[144:147], v[56:63], %[idx]\n\t" \
          "ds_read_b128 v[220:223], %[own] offset:21504\n\t" \
          "s_waitcnt vmcnt(4) lgkmcnt(6)\n\t" \
          "v_smfmac_f32_16x16x64_bf16 v[184:187], v[144:147], v[224:231], %[idx]\n\t" \
          "ds_read_b128 v[224:227], %[own] offset:22528\n\t" \
          "s_and_b64 %[bad1], %[bad1], %[en]\n\t" \
          "v_smfmac_f32_16x16x64_bf16 v[168:171], v[148:151], v[64:71], %[idx]\n\t" \
          "ds_read_b128 v[228:231], %[own] offset:23552\n\t" \
          "s_waitcnt lgkmcnt(6)\n\t" \
          "v_smfmac_f32_16x16x64_bf16 v[172:175], v[148:151], v[200:207], %[idx]\n\t" \
          "ds_read_b128 v[200:203], %[own] offset:12288\n\t" \
          "s_cbranch_scc1 Lqk_fail%=\n\t" \
          "v_smfmac_f32_16x16x64_bf16 v[176:179], v[148:151], v[72:79], %[idx]\n\t" \
          "ds_read_b128 v[204:207], %[own] offset:13312\n\t" \
          "s_waitcnt lgkmcnt(6)\n\t" \
          "v_smfmac_f32_16x16x64_bf16 v[180:183], v[148:151], v[208:215], %[idx]\n\t" \
          "ds_read_b128 v[208:211], %[own] offset:16384\n\t" \
          "v_max3_u32 %[m], v140, v141, %[m]\n\t" \
          "v_smfmac_f32_16x16x64_bf16 v[184:187], v[148:151], v[80:87], %[idx]\n\t" \
          "ds_read_b128 v[212:215], %[own] offset:17408\n\t" \
          "s_waitcnt vmcnt(3) lgkmcnt(6)\n\t" \
          "v_smfmac_f32_16x16x64_bf16 v[168:171], v[152:155], v[216:223], %[idx]\n\t" \
          "ds_read_b128 v[216:219], %[own] offset:18432\n\t" \
          "v_max3_u32 %[m], v142, v143, %[m]\n\t" \
          "v_smfmac_f32_16x16x64_bf16 v[172:175], v[152:155], v[88:95], %[idx]\n\t" \
          "ds_read_b128 v[220:223], %[own] offset:19456\n\t" \
          "s_waitcnt lgkmcnt(6)\n\t" \
          "v_smfmac_f32_16x16x64_bf16 v[176:179], v[152:155], v[224:231], %[idx]\n\t" \
          "ds_read_b128 v[224:227], %[own] offset:14336\n\t" \
          "v_max3_u32 %[m], v144, v145, %[m]\n\t" \
          "v_smfmac_f32_16x16x64_bf16 v[180:183], v[152:155], v[96:103], %[idx]\n\t" \
          "ds_read_b128 v[228:231], %[own] offset:15360\n\t" \
          "s_waitcnt vmcnt(2) lgkmcnt(6)\n\t" \
          "v_smfmac_f32_16x16x64_bf16 v[184:187], v[152:155], v[200:207], %[idx]\n\t" \
          "v_max3_u32 %[m], v146, v147, %[m]\n\t" \
          "v_smfmac_f32_16x16x64_bf16 v[168:171], v[156:159], v[104:111], %[idx]\n\t" \
          "s_waitcnt lgkmcnt(4)\n\t" \
          "v_max3_u32 %[m], v148, v149, %[m]\n\t" \
          "v_smfmac_f32_16x16x64_bf16 v[172:175], v[156:159], v[208:215], %[idx]\n\t" \
          "v_max3_u32 %[m], v150, v151, %[m]\n\t" \
          "v_smfmac_f32_16x16x64_bf16 v[176:179], v[156:159], v[112:119], %[idx]\n\t" \
          "s_waitcnt lgkmcnt(2)\n\t" \
          "v_max3_u32 %[m], v152, v153, %[m]\n\t" \
          "v_smfmac_f32_16x16x64_bf16 v[180:183], v[156:159], v[216:223], %[idx]\n\t" \
          "ds_read_b128 v[216:219], %[own] offset:32768\n\t" \
          "v_max3_u32 %[m], v154, v155, %[m]\n\t" \
          "v_smfmac_f32_16x16x64_bf16 v[184:187], v[156:159], v[120:127], %[idx]\n\t" \
          "ds_read_b128 v[220:223], %[own] offset:33792\n\t" \
          "s_waitcnt vmcnt(1) lgkmcnt(2)\n\t" \
          "v_smfmac_f32_16x16x64_bf16 v[168:171], v[160:163], v[224:231], %[idx]\n\t" \
          "ds_read_b64_tr_b16 v[224:225], %[vxp]\n\t" \
          "v_max3_u32 %[m], v156, v157, %[m]\n\t" \
          "v_smfmac_f32_16x16x64_bf16 v[172:175], v[160:163], v[128:135], %[idx]\n\t" \
          "ds_read_b64_tr_b16 v[226:227], %[vxp] offset:1024\n\t" \
          "v_max3_u32 %[m], v158, v159, %[m]\n\t" \
          "v_smfmac_f32_16x16x64_bf16 v[176:179], v[160:163], a[0:7], %[idx]\n\t" \
          "ds_read_b64_tr_b16 v[228:229], %[vxp] offset:8\n\t" \
          "v_max3_u32 %[m], v160, v161, %[m]\n\t" \
          "v_smfmac_f32_16x16x64_bf16 v[180:183], v[160:163], a[8:15], %[idx]\n\t" \
          "ds_read_b64_tr_b16 v[230:231], %[vxp] offset:1032\n\t" \
          "v_smfmac_f32_16x16x64_bf16 v[184:187], v[160:163], a[16:23], %[idx]\n\t" \
          "s_waitcnt vmcnt(0)\n\t" \
          "v_max3_u32 %[m], v162, v163, %[m]\n\t" \
          "v_smfmac_f32_16x16x64_bf16 v[168:171], v[164:167], a[24:31], %[idx]\n\t" \
          "v_max3_u32 %[m], v164, v165, %[m]\n\t" \
          "v_smfmac_f32_16x16x64_bf16 v[172:175], v[164:167], a[32:39], %[idx]\n\t" \
          "v_max3_u32 %[m], v166, v167, %[m]\n\t" \
          "v_smfmac_f32_16x16x64_bf16 v[176:179], v[164:167], a[40:47], %[idx]\n\t" \
          "v_cvt_pk_bf16_f32 v200, v168, v169\n\t" \
          "v_cvt_pk_bf16_f32 v201, v170, v171\n\t" \
          "v_smfmac_f32_16x16x64_bf16 v[180:183], v[164:167], a[48:55], %[idx]\n\t" \
          "ds_write_b64 %[own], v[200:201] offset:36864\n\t" \
          "v_cvt_pk_bf16_f32 v202, v172, v173\n\t" \
          "v_cvt_pk_bf16_f32 v203, v174, v175\n\t" \
          "v_smfmac_f32_16x16x64_bf16 v[184:187], v[164:167], a[56:63], %[idx]\n\t" \
          "ds_write_b64 %[own], v[202:203] offset:36872\n\t" \
          "v_smfmac_f32_16x16x64_bf16 v[188:191], v[136:139], a[64:71], %[idx]\n\t" \
          "v_cmp_eq_u32 %[bad1], -1, %[m]\n\t" \
          "v_smfmac_f32_16x16x64_bf16 v[188:191], v[140:143], a[72:79], %[idx]\n\t" \
          "v_cvt_pk_bf16_f32 v208, v176, v177\n\t" \
          "v_smfmac_f32_16x16x64_bf16 v[188:191], v[144:147], a[80:87], %[idx]\n\t" \
          "s_and_b64 %[bad1], %[bad1], %[en]\n\t" \
          "s_cbranch_scc1 Lqk_fail%=\n\t" \
          "s_waitcnt lgkmcnt(0)\n\t" \
          "s_barrier\n\t" \
          "v_cvt_pk_bf16_f32 v209, v178, v179\n\t" \
          "v_smfmac_f32_16x16x64_bf16 v[188:191], v[148:151], a[88:95], %[idx]\n\t" \
          "ds_read_b128 v[210:213], %[p1] offset:36864\n\t" \
          "v_cvt_pk_bf16_f32 v214, v180, v181\n\t" \
          "v_smfmac_f32_16x16x64_bf16 v[188:191], v[152:155], a[96:103], %[idx]\n\t" \
          "v_cvt_pk_bf16_f32 v215, v182, v183\n\t" \
          "v_smfmac_f32_16x16x64_bf16 v[188:191], v[156:159], a[104:111], %[idx]\n\t" \
          "v_cvt_pk_bf16_f32 v204, v184, v185\n\t" \
          "v_smfmac_f32_16x16x64_bf16 v[188:191], v[160:163], a[112:119], %[idx]\n\t" \
          "v_cvt_pk_bf16_f32 v205, v186, v187\n\t" \
          "v_smfmac_f32_16x16x64_bf16 v[188:191], v[164:167], a[120:127], %[idx]\n\t" \
          "s_waitcnt lgkmcnt(0)\n\t" \
          "v_mfma_f32_16x16x32_bf16 v[168:171], v[208:211], %[jk], 0\n\t" \
          "v_mov_b32 %[m], -1\n\t" \
          "v_smfmac_f32_16x16x64_bf16 v[192:195], v[136:139], a[128:135], %[idx]\n\t" \
          "v_mfma_f32_16x16x32_bf16 v[172:175], v[212:215], %[jk], 0\n\t" \
          "v_cvt_pk_bf16_f32 v176, v168, v169\n\t" \
          "v_cvt_pk_bf16_f32 v177, v170, v171\n\t" \
          "v_smfmac_f32_16x16x64_bf16 v[196:199], v[136:139], a[136:143], %[idx]\n\t" \
          "ds_write_b64 %[own], v[204:205] offset:37888\n\t" \
          "v_cvt_pk_bf16_f32 v206, v188, v189\n\t" \
          "v_smfmac_f32_16x16x64_bf16 v[192:195], v[140:143], a[144:151], %[idx]\n\t" \
          "buffer_store_dword %[m], %[ho], %[rsm], %[rb] offen sc1\n\t" \
          "v_cvt_pk_bf16_f32 v178, v172, v173\n\t" \
          "v_cvt_pk_bf16_f32 v179, v174, v175\n\t" \
          "v_smfmac_f32_16x16x64_bf16 v[196:199], v[140:143], a[152:159], %[idx]\n\t" \
          "v_cvt_pk_bf16_f32 v207, v190, v191\n\t" \
          "v_mfma_f32_16x16x32_bf16 v[208:211], v[216:219], v[176:179], 0\n\t" \
          "v_mov_b32 v168, %[one]\n\t" \
          "v_mov_b32 v169, %[one]\n\t" \
          "v_mfma_f32_16x16x32_bf16 v[212:215], v[220:223], v[176:179], 0\n\t" \
          "v_mov_b32 v170, %[one]\n\t" \
          "v_mov_b32 v171, %[one]\n\t" \
          "v_smfmac_f32_16x16x64_bf16 v[192:195], v[144:147], a[160:167], %[idx]\n\t" \
          "ds_write_b64 %[own], v[206:207] offset:37896\n\t" \
          "v_smfmac_f32_16x16x64_bf16 v[196:199], v[144:147], a[168:175], %[idx]\n\t" \
          "buffer_store_dword %[m], %[ho], %[rsm], %[rb] offen offset:256 sc1\n\t" \
          "v_cndmask_b32 v208, v208, v212, %[twin]\n\t" \
          "v_cndmask_b32 v209, v209, v213, %[twin]\n\t" \
          "v_smfmac_f32_16x16x64_bf16 v[192:195], v[148:151], a[176:183], %[idx]\n\t" \
          "ds_write_b64 %[own], v[208:209] offset:38912\n\t" \
          "v_cndmask_b32 v210, v210, v214, %[twin]\n\t" \
          "v_cndmask_b32 v211, v211, v215, %[twin]\n\t" \
          "v_smfmac_f32_16x16x64_bf16 v[196:199], v[148:151], a[184:191], %[idx]\n\t" \
          "ds_write_b64 %[own], v[210:211] offset:38920\n\t" \
          "v_smfmac_f32_16x16x64_bf16 v[192:195], v[152:155], a[192:199], %[idx]\n\t" \
          "v_mul_f32 v212, %[qs], v208\n\t" \
          "v_smfmac_f32_16x16x64_bf16 v[196:199], v[152:155], a[200:207], %[idx]\n\t" \
          "buffer_load_dword v232, %[ho], %[rsm], %[vb] offen sc1\n\t" \
          "v_mul_f32 v213, %[qs], v209\n\t" \
          "v_smfmac_f32_16x16x64_bf16 v[192:195], v[156:159], a[208:215], %[idx]\n\t" \
          "buffer_load_dword v234, %[ho], %[rsm], %[vb] offen offset:256 sc1\n\t" \
          "v_mul_f32 v214, %[qs], v210\n\t" \
          "v_smfmac_f32_16x16x64_bf16 v[196:199], v[156:159], a[216:223], %[idx]\n\t" \
          "s_waitcnt lgkmcnt(0)\n\t" \
          "s_barrier\n\t" \
          "v_mul_f32 v215, %[qs], v211\n\t" \
          "v_smfmac_f32_16x16x64_bf16 v[192:195], v[160:163], a[224:231], %[idx]\n\t" \
          "ds_read_b128 v[138:141], %[p1] offset:37888\n\t" \
          "v_smfmac_f32_16x16x64_bf16 v[196:199], v[160:163], a[232:239], %[idx]\n\t" \
          "ds_read_b128 v[216:219], %[p1] offset:38912\n\t" \
          "v_smfmac_f32_16x16x64_bf16 v[192:195], v[164:167], a[240:247], %[idx]\n\t" \
          "ds_read_b128 v[220:223], %[p2] offset:38912\n\t" \
          "v_smfmac_f32_16x16x64_bf16 v[196:199], v[164:167], a[248:255], %[idx]\n\t" \
          "ds_read_b128 v[200:203], %[p3] offset:38912\n\t" \
          "s_nop 0\n\t" \
          "s_waitcnt lgkmcnt(2)\n\t" \
          "v_cvt_pk_bf16_f32 v136, v192, v193\n\t" \
          "v_cvt_pk_bf16_f32 v137, v194, v195\n\t" \
          "v_fma_f32 v212, v216, %[qs], v212\n\t" \
          "v_fma_f32 v213, v217, %[qs], v213\n\t" \
          "v_cvt_pk_bf16_f32 v142, v196, v197\n\t" \
          "v_cvt_pk_bf16_f32 v143, v198, v199\n\t" \
          "v_mfma_f32_16x16x32_bf16 v[144:147], v[136:139], %[jk], 0\n\t" \
          "v_fma_f32 v214, v218, %[qs], v214\n\t" \
          "v_mfma_f32_16x16x32_bf16 v[148:151], v[140:143], %[jk], 0\n\t" \
          "v_fma_f32 v215, v219, %[qs], v215\n\t" \
          "s_waitcnt lgkmcnt(0)\n\t" \
          "v_fma_f32 v212, v220, %[qs], v212\n\t" \
          "v_fma_f32 v213, v221, %[qs], v213\n\t" \
          "v_cvt_pk_bf16_f32 v152, v144, v145\n\t" \
          "v_cvt_pk_bf16_f32 v153, v146, v147\n\t" \
          "v_fma_f32 v214, v222, %[qs], v214\n\t" \
          "v_fma_f32 v215, v223, %[qs], v215\n\t" \
          "v_cvt_pk_bf16_f32 v154, v148, v149\n\t" \
          "v_cvt_pk_bf16_f32 v155, v150, v151\n\t" \
          "v_pk_fma_f32 v[212:213], v[200:201], %[qs2], v[212:213] op_sel_hi:[1,0,1]\n\t" \
          "v_pk_fma_f32 v[214:215], v[202:203], %[qs2], v[214:215] op_sel_hi:[1,0,1]\n\t" \
          "v_mfma_f32_16x16x32_bf16 v[156:159], v[152:155], v[176:179], 0\n\t" \
          "v_exp_f32 v180, v212\n\t" \
          "v_mfma_f32_16x16x32_bf16 v[196:199], v[168:171], v[168:171], 0\n\t" \
          "v_exp_f32 v181, v213\n\t" \
          "v_mfma_f32_16x16x32_bf16 v[196:199], v[168:171], v[168:171], 0\n\t" \
          "ds_write_b64 %[own], v[156:157] offset:39936\n\t" \
          "v_exp_f32 v182, v214\n\t" \
          "v_mov_b32 v137, 0\n\t" \
          "v_exp_f32 v183, v215\n\t" \
          "v_cvt_pk_bf16_f32 v172, v180, v181\n\t" \
          "v_cvt_pk_bf16_f32 v173, v182, v183\n\t" \
          "v_pk_mul_f32 v[160:161], v[156:157], %[qs2] op_sel_hi:[1,0]\n\t" \
          "v_mov_b32_dpp v174, v172 quad_perm:[2,3,0,1] row_mask:0xf bank_mask:0xf\n\t" \
          "v_mov_b32_dpp v175, v173 quad_perm:[2,3,0,1] row_mask:0xf bank_mask:0xf\n\t" \
          "s_nop 0\n\t" \
          "s_waitcnt lgkmcnt(0)\n\t" \
          "v_mfma_f32_16x16x32_bf16 v[184:187], v[172:175], v[168:171], 0\n\t" \
          "s_barrier\n\t" \
          "ds_read_b64 v[144:145], %[p1] offset:39936\n\t" \
          "ds_read_b64 v[146:147], %[p2] offset:39936\n\t" \
          "v_mfma_f32_16x16x32_bf16 v[188:191], v[172:175], v[224:227], 0\n\t" \
          "ds_read_b64 v[148:149], %[p3] offset:39936\n\t" \
          "v_mfma_f32_16x16x32_bf16 v[192:195], v[172:175], v[228:231], 0\n\t" \
          "buffer_load_dword %[al], %[alo], %[rsm], %[mb] offen sc1\n\t" \
          "s_waitcnt lgkmcnt(2)\n\t" \
          "v_fma_f32 v160, v144, %[qs], v160\n\t" \
          "v_fma_f32 v161, v145, %[qs], v161\n\t" \
          "s_waitcnt lgkmcnt(1)\n\t" \
          "v_fma_f32 v160, v146, %[qs], v160\n\t" \
          "v_fma_f32 v161, v147, %[qs], v161\n\t" \
          "s_waitcnt lgkmcnt(0)\n\t" \
          "v_fma_f32 v160, v148, %[qs], v160\n\t" \
          "v_fma_f32 v161, v149, %[qs], v161\n\t" \
          "v_exp_f32 v162, v160\n\t" \
          "v_exp_f32 v163, v161\n\t" \
          "v_max_f32 v182, v160, v161\n\t" \
          "v_cvt_pk_bf16_f32 v136, v162, v163\n\t" \
          "v_cmp_nge_f32 vcc, %[th], v182\n\t" \
          "s_waitcnt vmcnt(1)\n\t" \
          "v_mfma_f32_16x16x16_bf16 v[164:167], v[136:137], v[168:169], v[184:187]\n\t" \
          "v_max_f32 v180, v184, v185\n\t" \
          "v_min_f32 v181, v184, v185\n\t" \
          "v_mfma_f32_16x16x16_bf16 v[188:191], v[136:137], v[232:233], v[188:191]\n\t" \
          "v_cmp_nge_f32 %[b2], %[hi], v180\n\t" \
          "v_cmp_nle_f32 %[b3], %[lo], v181\n\t" \
          "v_mfma_f32_16x16x16_bf16 v[192:195], v[136:137], v[234:235], v[192:195]\n\t" \
          "v_max_u32 v183, v232, v234\n\t" \
          "v_cmp_eq_u32 %[bad], -1, v183\n\t" \
          "v_rcp_f32 v162, v164\n\t" \
          "s_or_b64 %[b2], %[b2], %[b3]\n\t" \
          "v_rcp_f32 v163, v165\n\t" \
          "s_or_b64 %[b2], %[b2], vcc\n\t" \
          "v_pk_mul_f32 v[156:157], v[188:189], v[162:163]\n\t" \
          "s_or_b64 %[bad], %[bad], %[b2]\n\t" \
          "s_cselect_b64 exec, 0, -1\n\t" \
          "v_pk_mul_f32 v[158:159], v[192:193], v[162:163]\n\t" \
          "s_nop 0\n\t" \
          "v_cvt_pk_bf16_f32 v156, v156, v158\n\t" \
          "v_cvt_pk_bf16_f32 v157, v157, v159\n\t" \
          "v_min_u32 v156, -2, v156\n\t" \
          "v_min_u32 v157, -2, v157\n\t" \
          "buffer_store_dword v156, %[ho], %[rsm], %[ob] offen sc1\n\t" \
          "buffer_store_dword v157, %[ho], %[rsm], %[ob] offen offset:256 sc1\n\t" \
          "s_memrealtime %[po]\n\t" \
          "s_mov_b64 exec, -1\n\t" \
          "s_waitcnt lgkmcnt(0)\n\t"
constexpr int QK_SX = 12, QK_SM = 24, QK_KM = 16, QK_VX = 24, QK_KV = 32768, QK_EX = 36864;
#define VO_BODY \
          "v_mov_b64 v[224:225], 0\n\t" \
          "v_mov_b64 v[226:227], 0\n\t" \
          "v_mov_b64 v[228:229], 0\n\t" \
          "v_mov_b64 v[230:231], 0\n\t" \
          "v_mov_b64 v[232:233], 0\n\t" \
          "v_mov_b64 v[234:235], 0\n\t" \
          "v_mov_b64 v[236:237], 0\n\t" \
          "v_mov_b64 v[238:239], 0\n\t" \
          "v_mov_b64 v[176:177], 0\n\t" \
          "v_mov_b64 v[178:179], 0\n\t" \
          "v_mov_b64 v[180:181], 0\n\t" \
          "v_mov_b64 v[182:183], 0\n\t" \
          "v_mov_b64 v[184:185], 0\n\t" \
          "v_mov_b64 v[186:187], 0\n\t" \
          "v_mov_b64 v[188:189], 0\n\t" \
          "v_mov_b64 v[190:191], 0\n\t" \
          "s_mov_b64 %[en], -1\n\t" \
          "s_cmp_eq_u32 %[s1], 0\n\t" \
          "s_cbranch_scc1 Lv_n%=\n\t" \
          "s_cmp_eq_u32 %[w], 3\n\t" \
          "s_cbranch_scc0 Lo_z%=\n\t" \
          "s_branch Lo_c%=\n\t" \
          "Lo_f0_%=:\n\t" \
          "buffer_load_dwordx4 v[192:195], %[oo0], %[rsm], %[ob] offen sc1\n\t" \
          "Lo_f1_%=:\n\t" \
          "buffer_load_dwordx4 v[196:199], %[oo1], %[rsm], %[ob] offen sc1\n\t" \
          "Lo_f2_%=:\n\t" \
          "buffer_load_dwordx4 v[200:203], %[oo2], %[rsm], %[ob] offen sc1\n\t" \
          "Lo_f3_%=:\n\t" \
          "buffer_load_dwordx4 v[204:207], %[oo3], %[rsm], %[ob] offen sc1\n\t" \
          "Lo_f4_%=:\n\t" \
          "buffer_load_dwordx4 v[208:211], %[oo4], %[rsm], %[ob] offen sc1\n\t" \
          "Lo_f5_%=:\n\t" \
          "buffer_load_dwordx4 v[212:215], %[oo5], %[rsm], %[ob] offen sc1\n\t" \
          "Lo_f6_%=:\n\t" \
          "buffer_load_dwordx4 v[216:219], %[oo6], %[rsm], %[ob] offen sc1\n\t" \
          "Lo_f7_%=:\n\t" \
          "buffer_load_dwordx4 v[220:223], %[oo7], %[rsm], %[ob] offen sc1\n\t" \
          "v_mov_b32 v242, 0x10000\n\t" \
          "s_mov_b64 exec, %[alx]\n\t" \
          "buffer_store_dword v242, %[l4], %[rsm], %[als] offen sc1\n\t" \
          "s_mov_b64 exec, -1\n\t" \
          "v_mov_b32 v243, %[s1]\n\t" \
          "ds_write_b32 %[fa], v243\n\t" \
          "v_mov_b64 v[224:225], 0\n\t" \
          "v_mov_b64 v[226:227], 0\n\t" \
          "v_mov_b64 v[228:229], 0\n\t" \
          "v_mov_b64 v[230:231], 0\n\t" \
          "v_mov_b64 v[232:233], 0\n\t" \
          "v_mov_b64 v[234:235], 0\n\t" \
          "v_mov_b64 v[236:237], 0\n\t" \
          "v_mov_b64 v[238:239], 0\n\t" \
          "s_nop 1\n\t" \
          "s_mov_b32 %[cnt], 0\n\t" \
          "s_waitcnt vmcnt(7)\n\t" \
          "v_max3_u32 v240, v192, v193, v194\n\t" \
          "v_max_u32 v240, v240, v195\n\t" \
          "v_cmp_eq_u32 vcc, -1, v240\n\t" \
          "s_cbranch_vccnz Lo_p0_%=\n\t" \
          "Lo_k0_%=:\n\t" \
          "v_smfmac_f32_16x16x64_bf16 v[224:227], v[192:195], a[0:7], %[idx]\n\t" \
          "v_smfmac_f32_16x16x64_bf16 v[228:231], v[192:195], a[8:15], %[idx]\n\t" \
          "v_smfmac_f32_16x16x64_bf16 v[232:235], v[192:195], a[16:23], %[idx]\n\t" \
          "v_smfmac_f32_16x16x64_bf16 v[236:239], v[192:195], a[24:31], %[idx]\n\t" \
          "s_waitcnt vmcnt(6)\n\t" \
          "v_max3_u32 v240, v196, v197, v198\n\t" \
          "v_max_u32 v240, v240, v199\n\t" \
          "v_cmp_eq_u32 vcc, -1, v240\n\t" \
          "s_cbranch_vccnz Lo_p1_%=\n\t" \
          "Lo_k1_%=:\n\t" \
          "v_smfmac_f32_16x16x64_bf16 v[224:227], v[196:199], a[32:39], %[idx]\n\t" \
          "v_smfmac_f32_16x16x64_bf16 v[228:231], v[196:199], a[40:47], %[idx]\n\t" \
          "v_smfmac_f32_16x16x64_bf16 v[232:235], v[196:199], a[48:55], %[idx]\n\t" \
          "v_smfmac_f32_16x16x64_bf16 v[236:239], v[196:199], a[56:63], %[idx]\n\t" \
          "s_waitcnt vmcnt(5)\n\t" \
          "v_max3_u32 v240, v200, v201, v202\n\t" \
          "v_max_u32 v240, v240, v203\n\t" \
          "v_cmp_eq_u32 vcc, -1, v240\n\t" \
          "s_cbranch_vccnz Lo_p2_%=\n\t" \
          "Lo_k2_%=:\n\t" \
          "v_smfmac_f32_16x16x64_bf16 v[224:227], v[200:203], a[64:71], %[idx]\n\t" \
          "v_smfmac_f32_16x16x64_bf16 v[228:231], v[200:203], a[72:79], %[idx]\n\t" \
          "v_smfmac_f32_16x16x64_bf16 v[232:235], v[200:203], a[80:87], %[idx]\n\t" \
          "v_smfmac_f32_16x16x64_bf16 v[236:239], v[200:203], a[88:95], %[idx]\n\t" \
          "s_waitcnt vmcnt(4)\n\t" \
          "v_max3_u32 v240, v204, v205, v206\n\t" \
          "v_max_u32 v240, v240, v207\n\t" \
          "v_cmp_eq_u32 vcc, -1, v240\n\t" \
          "s_cbranch_vccnz Lo_p3_%=\n\t" \
          "Lo_k3_%=:\n\t" \
          "v_smfmac_f32_16x16x64_bf16 v[224:227], v[204:207], a[96:103], %[idx]\n\t" \
          "v_smfmac_f32_16x16x64_bf16 v[228:231], v[204:207], a[104:111], %[idx]\n\t" \
          "v_smfmac_f32_16x16x64_bf16 v[232:235], v[204:207], a[112:119], %[idx]\n\t" \
          "v_smfmac_f32_16x16x64_bf16 v[236:239], v[204:207], a[120:127], %[idx]\n\t" \
          "s_waitcnt vmcnt(3)\n\t" \
          "v_max3_u32 v240, v208, v209, v210\n\t" \
          "v_max_u32 v240, v240, v211\n\t" \
          "v_cmp_eq_u32 vcc, -1, v240\n\t" \
          "s_cbranch_vccnz Lo_p4_%=\n\t" \
          "Lo_k4_%=:\n\t" \
          "v_smfmac_f32_16x16x64_bf16 v[224:227], v[208:211], a[128:135], %[idx]\n\t" \
          "v_smfmac_f32_16x16x64_bf16 v[228:231], v[208:211], a[136:143], %[idx]\n\t" \
          "v_smfmac_f32_16x16x64_bf16 v[232:235], v[208:211], a[144:151], %[idx]\n\t" \
          "v_smfmac_f32_16x16x64_bf16 v[236:239], v[208:211], a[152:159], %[idx]\n\t" \
          "s_waitcnt vmcnt(2)\n\t" \
          "v_max3_u32 v240, v212, v213, v214\n\t" \
          "v_max_u32 v240, v240, v215\n\t" \
          "v_cmp_eq_u32 vcc, -1, v240\n\t" \
          "s_cbranch_vccnz Lo_p5_%=\n\t" \
          "Lo_k5_%=:\n\t" \
          "v_smfmac_f32_16x16x64_bf16 v[224:227], v[212:215], a[160:167], %[idx]\n\t" \
          "v_smfmac_f32_16x16x64_bf16 v[228:231], v[212:215], a[168:175], %[idx]\n\t" \
          "v_smfmac_f32_16x16x64_bf16 v[232:235], v[212:215], a[176:183], %[idx]\n\t" \
          "v_smfmac_f32_16x16x64_bf16 v[236:239], v[212:215], a[184:191], %[idx]\n\t" \
          "s_waitcnt vmcnt(1)\n\t" \
          "v_max3_u32 v240, v216, v217, v218\n\t" \
          "v_max_u32 v240, v240, v219\n\t" \
          "v_cmp_eq_u32 vcc, -1, v240\n\t" \
          "s_cbranch_vccnz Lo_p6_%=\n\t" \
          "Lo_k6_%=:\n\t" \
          "v_smfmac_f32_16x16x64_bf16 v[224:227], v[216:219], a[192:199], %[idx]\n\t" \
          "v_smfmac_f32_16x16x64_bf16 v[228:231], v[216:219], a[200:207], %[idx]\n\t" \
          "v_smfmac_f32_16x16x64_bf16 v[232:235], v[216:219], a[208:215], %[idx]\n\t" \
          "v_smfmac_f32_16x16x64_bf16 v[236:239], v[216:219], a[216:223], %[idx]\n\t" \
          "s_waitcnt vmcnt(0)\n\t" \
          "v_max3_u32 v240, v220, v221, v222\n\t" \
          "v_max_u32 v240, v240, v223\n\t" \
          "v_cmp_eq_u32 vcc, -1, v240\n\t" \
          "s_cbranch_vccnz Lo_p7_%=\n\t" \
          "Lo_k7_%=:\n\t" \
          "v_smfmac_f32_16x16x64_bf16 v[224:227], v[220:223], a[224:231], %[idx]\n\t" \
          "v_smfmac_f32_16x16x64_bf16 v[228:231], v[220:223], a[232:239], %[idx]\n\t" \
          "v_smfmac_f32_16x16x64_bf16 v[232:235], v[220:223], a[240:247], %[idx]\n\t" \
          "v_smfmac_f32_16x16x64_bf16 v[236:239], v[220:223], a[248:255], %[idx]\n\t" \
          "s_nop 7\n\t" \
          "v_add_f32 v242, v224, v225\n\t" \
          "v_add_f32 v244, v234, v235\n\t" \
          "s_cmp_lg_u32 %[w], 0\n\t" \
          "s_waitcnt lgkmcnt(0)\n\t" \
          "s_branch Lo_tail%=\n\t" \
          "Lo_p0_%=:\n\t" \
          "s_cmp_lt_i32 %[cnt], 0\n\t" \
          "s_cbranch_scc1 Lo_k0_%=\n\t" \
          "s_add_u32 %[cnt], %[cnt], 1\n\t" \
          "s_and_b32 vcc_lo, %[cnt], 3\n\t" \
          "s_cbranch_scc1 Lo_q0_%=\n\t" \
          "s_getpc_b64 s[48:49]\n\t" \
          "s_branch Lo_spin%=\n\t" \
          "Lo_q0_%=:\n\t" \
          "buffer_load_dwordx4 v[192:195], %[oo0], %[rsm], %[ob] offen sc1\n\t" \
          "s_waitcnt vmcnt(0)\n\t" \
          "v_max3_u32 v240, v192, v193, v194\n\t" \
          "v_max_u32 v240, v240, v195\n\t" \
          "v_cmp_eq_u32 vcc, -1, v240\n\t" \
          "s_cbranch_vccnz Lo_p0_%=\n\t" \
          "buffer_load_dwordx4 v[196:199], %[oo1], %[rsm], %[ob] offen sc1\n\t" \
          "buffer_load_dwordx4 v[200:203], %[oo2], %[rsm], %[ob] offen sc1\n\t" \
          "buffer_load_dwordx4 v[204:207], %[oo3], %[rsm], %[ob] offen sc1\n\t" \
          "buffer_load_dwordx4 v[208:211], %[oo4], %[rsm], %[ob] offen sc1\n\t" \
          "buffer_load_dwordx4 v[212:215], %[oo5], %[rsm], %[ob] offen sc1\n\t" \
          "buffer_load_dwordx4 v[216:219], %[oo6], %[rsm], %[ob] offen sc1\n\t" \
          "buffer_load_dwordx4 v[220:223], %[oo7], %[rsm], %[ob] offen sc1\n\t" \
          "s_branch Lo_k0_%=\n\t" \
          "Lo_p1_%=:\n\t" \
          "s_cmp_lt_i32 %[cnt], 0\n\t" \
          "s_cbranch_scc1 Lo_k1_%=\n\t" \
          "s_add_u32 %[cnt], %[cnt], 1\n\t" \
          "s_and_b32 vcc_lo, %[cnt], 3\n\t" \
          "s_cbranch_scc1 Lo_q1_%=\n\t" \
          "s_getpc_b64 s[48:49]\n\t" \
          "s_branch Lo_spin%=\n\t" \
          "Lo_q1_%=:\n\t" \
          "buffer_load_dwordx4 v[196:199], %[oo1], %[rsm], %[ob] offen sc1\n\t" \
          "s_waitcnt vmcnt(0)\n\t" \
          "v_max3_u32 v240, v196, v197, v198\n\t" \
          "v_max_u32 v240, v240, v199\n\t" \
          "v_cmp_eq_u32 vcc, -1, v240\n\t" \
          "s_cbranch_vccnz Lo_p1_%=\n\t" \
          "buffer_load_dwordx4 v[200:203], %[oo2], %[rsm], %[ob] offen sc1\n\t" \
          "buffer_load_dwordx4 v[204:207], %[oo3], %[rsm], %[ob] offen sc1\n\t" \
          "buffer_load_dwordx4 v[208:211], %[oo4], %[rsm], %[ob] offen sc1\n\t" \
          "buffer_load_dwordx4 v[212:215], %[oo5], %[rsm], %[ob] offen sc1\n\t" \
          "buffer_load_dwordx4 v[216:219], %[oo6], %[rsm], %[ob] offen sc1\n\t" \
          "buffer_load_dwordx4 v[220:223], %[oo7], %[rsm], %[ob] offen sc1\n\t" \
          "s_branch Lo_k1_%=\n\t" \
          "Lo_p2_%=:\n\t" \
          "s_cmp_lt_i32 %[cnt], 0\n\t" \
          "s_cbranch_scc1 Lo_k2_%=\n\t" \
          "s_add_u32 %[cnt], %[cnt], 1\n\t" \
          "s_and_b32 vcc_lo, %[cnt], 3\n\t" \
          "s_cbranch_scc1 Lo_q2_%=\n\t" \
          "s_getpc_b64 s[48:49]\n\t" \
          "s_branch Lo_spin%=\n\t" \
          "Lo_q2_%=:\n\t" \
          "buffer_load_dwordx4 v[200:203], %[oo2], %[rsm], %[ob] offen sc1\n\t" \
          "s_waitcnt vmcnt(0)\n\t" \
          "v_max3_u32 v240, v200, v201, v202\n\t" \
          "v_max_u32 v240, v240, v203\n\t" \
          "v_cmp_eq_u32 vcc, -1, v240\n\t" \
          "s_cbranch_vccnz Lo_p2_%=\n\t" \
          "buffer_load_dwordx4 v[204:207], %[oo3], %[rsm], %[ob] offen sc1\n\t" \
          "buffer_load_dwordx4 v[208:211], %[oo4], %[rsm], %[ob] offen sc1\n\t" \
          "buffer_load_dwordx4 v[212:215], %[oo5], %[rsm], %[ob] offen sc1\n\t" \
          "buffer_load_dwordx4 v[216:219], %[oo6], %[rsm], %[ob] offen sc1\n\t" \
          "buffer_load_dwordx4 v[220:223], %[oo7], %[rsm], %[ob] offen sc1\n\t" \
          "s_branch Lo_k2_%=\n\t" \
          "Lo_p3_%=:\n\t" \
          "s_cmp_lt_i32 %[cnt], 0\n\t" \
          "s_cbranch_scc1 Lo_k3_%=\n\t" \
          "s_add_u32 %[cnt], %[cnt], 1\n\t" \
          "s_and_b32 vcc_lo, %[cnt], 3\n\t" \
          "s_cbranch_scc1 Lo_q3_%=\n\t" \
          "s_getpc_b64 s[48:49]\n\t" \
          "s_branch Lo_spin%=\n\t" \
          "Lo_q3_%=:\n\t" \
          "buffer_load_dwordx4 v[204:207], %[oo3], %[rsm], %[ob] offen sc1\n\t" \
          "s_waitcnt vmcnt(0)\n\t" \
          "v_max3_u32 v240, v204, v205, v206\n\t" \
          "v_max_u32 v240, v240, v207\n\t" \
          "v_cmp_eq_u32 vcc, -1, v240\n\t" \
          "s_cbranch_vccnz Lo_p3_%=\n\t" \
          "buffer_load_dwordx4 v[208:211], %[oo4], %[rsm], %[ob] offen sc1\n\t" \
          "buffer_load_dwordx4 v[212:215], %[oo5], %[rsm], %[ob] offen sc1\n\t" \
          "buffer_load_dwordx4 v[216:219], %[oo6], %[rsm], %[ob] offen sc1\n\t" \
          "buffer_load_dwordx4 v[220:223], %[oo7], %[rsm], %[ob] offen sc1\n\t" \
          "s_branch Lo_k3_%=\n\t" \
          "Lo_p4_%=:\n\t" \
          "s_cmp_lt_i32 %[cnt], 0\n\t" \
          "s_cbranch_scc1 Lo_k4_%=\n\t" \
          "s_add_u32 %[cnt], %[cnt], 1\n\t" \
          "s_and_b32 vcc_lo, %[cnt], 3\n\t" \
          "s_cbranch_scc1 Lo_q4_%=\n\t" \
          "s_getpc_b64 s[48:49]\n\t" \
          "s_branch Lo_spin%=\n\t" \
          "Lo_q4_%=:\n\t" \
          "buffer_load_dwordx4 v[208:211], %[oo4], %[rsm], %[ob] offen sc1\n\t" \
          "s_waitcnt vmcnt(0)\n\t" \
          "v_max3_u32 v240, v208, v209, v210\n\t" \
          "v_max_u32 v240, v240, v211\n\t" \
          "v_cmp_eq_u32 vcc, -1, v240\n\t" \
          "s_cbranch_vccnz Lo_p4_%=\n\t" \
          "buffer_load_dwordx4 v[212:215], %[oo5], %[rsm], %[ob] offen sc1\n\t" \
          "buffer_load_dwordx4 v[216:219], %[oo6], %[rsm], %[ob] offen sc1\n\t" \
          "buffer_load_dwordx4 v[220:223], %[oo7], %[rsm], %[ob] offen sc1\n\t" \
          "s_branch Lo_k4_%=\n\t" \
          "Lo_p5_%=:\n\t" \
          "s_cmp_lt_i32 %[cnt], 0\n\t" \
          "s_cbranch_scc1 Lo_k5_%=\n\t" \
          "s_add_u32 %[cnt], %[cnt], 1\n\t" \
          "s_and_b32 vcc_lo, %[cnt], 3\n\t" \
          "s_cbranch_scc1 Lo_q5_%=\n\t" \
          "s_getpc_b64 s[48:49]\n\t" \
          "s_branch Lo_spin%=\n\t" \
          "Lo_q5_%=:\n\t" \
          "buffer_load_dwordx4 v[212:215], %[oo5], %[rsm], %[ob] offen sc1\n\t" \
          "s_waitcnt vmcnt(0)\n\t" \
          "v_max3_u32 v240, v212, v213, v214\n\t" \
          "v_max_u32 v240, v240, v215\n\t" \
          "v_cmp_eq_u32 vcc, -1, v240\n\t" \
          "s_cbranch_vccnz Lo_p5_%=\n\t" \
          "buffer_load_dwordx4 v[216:219], %[oo6], %[rsm], %[ob] offen sc1\n\t" \
          "buffer_load_dwordx4 v[220:223], %[oo7], %[rsm], %[ob] offen sc1\n\t" \
          "s_branch Lo_k5_%=\n\t" \
          "Lo_p6_%=:\n\t" \
          "s_cmp_lt_i32 %[cnt], 0\n\t" \
          "s_cbranch_scc1 Lo_k6_%=\n\t" \
          "s_add_u32 %[cnt], %[cnt], 1\n\t" \
          "s_and_b32 vcc_lo, %[cnt], 3\n\t" \
          "s_cbranch_scc1 Lo_q6_%=\n\t" \
          "s_getpc_b64 s[48:49]\n\t" \
          "s_branch Lo_spin%=\n\t" \
          "Lo_q6_%=:\n\t" \
          "buffer_load_dwordx4 v[216:219], %[oo6], %[rsm], %[ob] offen sc1\n\t" \
          "s_waitcnt vmcnt(0)\n\t" \
          "v_max3_u32 v240, v216, v217, v218\n\t" \
          "v_max_u32 v240, v240, v219\n\t" \
          "v_cmp_eq_u32 vcc, -1, v240\n\t" \
          "s_cbranch_vccnz Lo_p6_%=\n\t" \
          "buffer_load_dwordx4 v[220:223], %[oo7], %[rsm], %[ob] offen sc1\n\t" \
          "s_branch Lo_k6_%=\n\t" \
          "Lo_p7_%=:\n\t" \
          "s_cmp_lt_i32 %[cnt], 0\n\t" \
          "s_cbranch_scc1 Lo_k7_%=\n\t" \
          "s_add_u32 %[cnt], %[cnt], 1\n\t" \
          "s_and_b32 vcc_lo, %[cnt], 3\n\t" \
          "s_cbranch_scc1 Lo_q7_%=\n\t" \
          "s_getpc_b64 s[48:49]\n\t" \
          "s_branch Lo_spin%=\n\t" \
          "Lo_q7_%=:\n\t" \
          "buffer_load_dwordx4 v[220:223], %[oo7], %[rsm], %[ob] offen sc1\n\t" \
          "s_waitcnt vmcnt(0)\n\t" \
          "v_max3_u32 v240, v220, v221, v222\n\t" \
          "v_max_u32 v240, v240, v223\n\t" \
          "v_cmp_eq_u32 vcc, -1, v240\n\t" \
          "s_cbranch_vccnz Lo_p7_%=\n\t" \
          "s_branch Lo_k7_%=\n\t" \
          "Lo_spin%=:\n\t" \
          "buffer_load_dword v241, off, %[rsc], %[tmo] sc1\n\t" \
          "s_add_u32 s48, s48, 4\n\t" \
          "s_addc_u32 s49, s49, 0\n\t" \
          "s_waitcnt vmcnt(0)\n\t" \
          "v_readfirstlane_b32 vcc_lo, v241\n\t" \
          "s_lshr_b32 vcc_hi, %[cnt], 16\n\t" \
          "s_or_b32 vcc_lo, vcc_lo, vcc_hi\n\t" \
          "s_cbranch_scc0 Lo_back%=\n\t" \
          "v_mov_b32 v241, 1\n\t" \
          "buffer_store_dword v241, off, %[rsc], %[tmo] sc1\n\t" \
          "s_mov_b32 %[cnt], -1\n\t" \
          "Lo_back%=:\n\t" \
          "s_setpc_b64 s[48:49]\n\t" \
          "Lo_fa%=:\n\t" \
          "s_add_u32 %[ta], %[pm], %[shf]\n\t" \
          "s_branch Lo_fb%=\n\t" \
          "Lo_ka%=:\n\t" \
          "s_add_u32 vcc_lo, %[ta], %[r12]\n\t" \
          "s_min_u32 vcc_lo, vcc_lo, -2\n\t" \
          "v_mov_b32 v252, vcc_lo\n\t" \
          "v_mov_b32 v253, %[rd]\n\t" \
          "s_mov_b64 exec, 1\n\t" \
          "buffer_store_dwordx2 v[252:253], off, %[rsm], %[rco] sc1\n\t" \
          "s_mov_b64 exec, -1\n\t" \
          "s_branch Lo_kb%=\n\t" \
          "Lv_fail%=:\n\t" \
          "buffer_load_dwordx4 v[192:195], %[ga0], %[rsm], %[mbn] offen sc1\n\t" \
          "buffer_load_dwordx4 v[196:199], %[ga1], %[rsm], %[mbn] offen sc1\n\t" \
          "buffer_load_dwordx4 v[200:203], %[ga2], %[rsm], %[mbn] offen sc1\n\t" \
          "buffer_load_dwordx4 v[204:207], %[ga3], %[rsm], %[mbn] offen sc1\n\t" \
          "s_nop 3\n\t" \
          "v_mov_b64 v[176:177], 0\n\t" \
          "v_mov_b64 v[178:179], 0\n\t" \
          "v_mov_b64 v[180:181], 0\n\t" \
          "v_mov_b64 v[182:183], 0\n\t" \
          "v_mov_b64 v[184:185], 0\n\t" \
          "v_mov_b64 v[186:187], 0\n\t" \
          "v_mov_b64 v[188:189], 0\n\t" \
          "v_mov_b64 v[190:191], 0\n\t" \
          "s_mov_b64 %[en], 0\n\t" \
          "s_mov_b32 %[cnt], 0\n\t" \
          "s_waitcnt vmcnt(3)\n\t" \
          "v_max3_u32 v240, v192, v193, v194\n\t" \
          "v_max_u32 v240, v240, v195\n\t" \
          "v_cmp_eq_u32 vcc, -1, v240\n\t" \
          "s_cbranch_vccnz Lv_p0_%=\n\t" \
          "Lv_k0_%=:\n\t" \
          "s_waitcnt vmcnt(2)\n\t" \
          "v_max3_u32 v240, v196, v197, v198\n\t" \
          "v_max_u32 v240, v240, v199\n\t" \
          "v_cmp_eq_u32 vcc, -1, v240\n\t" \
          "s_cbranch_vccnz Lv_p1_%=\n\t" \
          "Lv_k1_%=:\n\t" \
          "s_waitcnt vmcnt(1)\n\t" \
          "v_max3_u32 v240, v200, v201, v202\n\t" \
          "v_max_u32 v240, v240, v203\n\t" \
          "v_cmp_eq_u32 vcc, -1, v240\n\t" \
          "s_cbranch_vccnz Lv_p2_%=\n\t" \
          "Lv_k2_%=:\n\t" \
          "s_waitcnt vmcnt(0)\n\t" \
          "v_max3_u32 v240, v204, v205, v206\n\t" \
          "v_max_u32 v240, v240, v207\n\t" \
          "v_cmp_eq_u32 vcc, -1, v240\n\t" \
          "s_cbranch_vccnz Lv_p3_%=\n\t" \
          "Lv_k3_%=:\n\t" \
          "s_branch Lv_go%=\n\t" \
          "Lv_p0_%=:\n\t" \
          "s_cmp_lt_i32 %[cnt], 0\n\t" \
          "s_cbranch_scc1 Lv_k0_%=\n\t" \
          "s_add_u32 %[cnt], %[cnt], 1\n\t" \
          "s_and_b32 vcc_lo, %[cnt], 3\n\t" \
          "s_cbranch_scc1 Lv_q0_%=\n\t" \
          "s_getpc_b64 s[48:49]\n\t" \
          "s_branch Lv_spin%=\n\t" \
          "Lv_q0_%=:\n\t" \
          "buffer_load_dwordx4 v[192:195], %[ga0], %[rsm], %[mbn] offen sc1\n\t" \
          "s_waitcnt vmcnt(0)\n\t" \
          "v_max3_u32 v240, v192, v193, v194\n\t" \
          "v_max_u32 v240, v240, v195\n\t" \
          "v_cmp_eq_u32 vcc, -1, v240\n\t" \
          "s_cbranch_vccnz Lv_p0_%=\n\t" \
          "buffer_load_dwordx4 v[196:199], %[ga1], %[rsm], %[mbn] offen sc1\n\t" \
          "buffer_load_dwordx4 v[200:203], %[ga2], %[rsm], %[mbn] offen sc1\n\t" \
          "buffer_load_dwordx4 v[204:207], %[ga3], %[rsm], %[mbn] offen sc1\n\t" \
          "s_branch Lv_k0_%=\n\t" \
          "Lv_p1_%=:\n\t" \
          "s_cmp_lt_i32 %[cnt], 0\n\t" \
          "s_cbranch_scc1 Lv_k1_%=\n\t" \
          "s_add_u32 %[cnt], %[cnt], 1\n\t" \
          "s_and_b32 vcc_lo, %[cnt], 3\n\t" \
          "s_cbranch_scc1 Lv_q1_%=\n\t" \
          "s_getpc_b64 s[48:49]\n\t" \
          "s_branch Lv_spin%=\n\t" \
          "Lv_q1_%=:\n\t" \
          "buffer_load_dwordx4 v[196:199], %[ga1], %[rsm], %[mbn] offen sc1\n\t" \
          "s_waitcnt vmcnt(0)\n\t" \
          "v_max3_u32 v240, v196, v197, v198\n\t" \
          "v_max_u32 v240, v240, v199\n\t" \
          "v_cmp_eq_u32 vcc, -1, v240\n\t" \
          "s_cbranch_vccnz Lv_p1_%=\n\t" \
          "buffer_load_dwordx4 v[200:203], %[ga2], %[rsm], %[mbn] offen sc1\n\t" \
          "buffer_load_dwordx4 v[204:207], %[ga3], %[rsm], %[mbn] offen sc1\n\t" \
          "s_branch Lv_k1_%=\n\t" \
          "Lv_p2_%=:\n\t" \
          "s_cmp_lt_i32 %[cnt], 0\n\t" \
          "s_cbranch_scc1 Lv_k2_%=\n\t" \
          "s_add_u32 %[cnt], %[cnt], 1\n\t" \
          "s_and_b32 vcc_lo, %[cnt], 3\n\t" \
          "s_cbranch_scc1 Lv_q2_%=\n\t" \
          "s_getpc_b64 s[48:49]\n\t" \
          "s_branch Lv_spin%=\n\t" \
          "Lv_q2_%=:\n\t" \
          "buffer_load_dwordx4 v[200:203], %[ga2], %[rsm], %[mbn] offen sc1\n\t" \
          "s_waitcnt vmcnt(0)\n\t" \
          "v_max3_u32 v240, v200, v201, v202\n\t" \
          "v_max_u32 v240, v240, v203\n\t" \
          "v_cmp_eq_u32 vcc, -1, v240\n\t" \
          "s_cbranch_vccnz Lv_p2_%=\n\t" \
          "buffer_load_dwordx4 v[204:207], %[ga3], %[rsm], %[mbn] offen sc1\n\t" \
          "s_branch Lv_k2_%=\n\t" \
          "Lv_p3_%=:\n\t" \
          "s_cmp_lt_i32 %[cnt], 0\n\t" \
          "s_cbranch_scc1 Lv_k3_%=\n\t" \
          "s_add_u32 %[cnt], %[cnt], 1\n\t" \
          "s_and_b32 vcc_lo, %[cnt], 3\n\t" \
          "s_cbranch_scc1 Lv_q3_%=\n\t" \
          "s_getpc_b64 s[48:49]\n\t" \
          "s_branch Lv_spin%=\n\t" \
          "Lv_q3_%=:\n\t" \
          "buffer_load_dwordx4 v[204:207], %[ga3], %[rsm], %[mbn] offen sc1\n\t" \
          "s_waitcnt vmcnt(0)\n\t" \
          "v_max3_u32 v240, v204, v205, v206\n\t" \
          "v_max_u32 v240, v240, v207\n\t" \
          "v_cmp_eq_u32 vcc, -1, v240\n\t" \
          "s_cbranch_vccnz Lv_p3_%=\n\t" \
          "s_branch Lv_k3_%=\n\t" \
          "Lv_spin%=:\n\t" \
          "buffer_load_dword v241, off, %[rsc], %[tmo] sc1\n\t" \
          "s_add_u32 s48, s48, 4\n\t" \
          "s_addc_u32 s49, s49, 0\n\t" \
          "s_waitcnt vmcnt(0)\n\t" \
          "v_readfirstlane_b32 vcc_lo, v241\n\t" \
          "s_lshr_b32 vcc_hi, %[cnt], 16\n\t" \
          "s_or_b32 vcc_lo, vcc_lo, vcc_hi\n\t" \
          "s_cbranch_scc0 Lv_back%=\n\t" \
          "v_mov_b32 v241, 1\n\t" \
          "buffer_store_dword v241, off, %[rsc], %[tmo] sc1\n\t" \
          "s_mov_b32 %[cnt], -1\n\t" \
          "Lv_back%=:\n\t" \
          "s_setpc_b64 s[48:49]\n\t" \
          "Lo_al%=:\n\t" \
          "v_subrev_u32 v252, %[t], v255\n\t" \
          "v_bfe_i32 v252, v252, 0, 16\n\t" \
          "v_cndmask_b32 v252, 0, v252, vcc\n\t" \
          "v_cmp_gt_u32 vcc, 1000, v252\n\t" \
          "s_nop 1\n\t" \
          "v_cndmask_b32 v252, 0, v252, vcc\n\t" \
          "s_nop 1\n\t" \
          "v_max_i32_dpp v252, v252, v252 row_ror:8 row_mask:0xf bank_mask:0xf\n\t" \
          "s_nop 1\n\t" \
          "v_max_i32_dpp v252, v252, v252 row_ror:4 row_mask:0xf bank_mask:0xf\n\t" \
          "s_nop 1\n\t" \
          "v_max_i32_dpp v252, v252, v252 row_ror:2 row_mask:0xf bank_mask:0xf\n\t" \
          "s_nop 1\n\t" \
          "v_max_i32_dpp v252, v252, v252 row_ror:1 row_mask:0xf bank_mask:0xf\n\t" \
          "s_nop 0\n\t" \
          "v_readfirstlane_b32 vcc_lo, v252\n\t" \
          "s_add_u32 %[t], %[t], vcc_lo\n\t" \
          "s_memrealtime vcc\n\t" \
          "s_waitcnt lgkmcnt(0)\n\t" \
          "s_sub_i32 vcc_lo, %[t], vcc_lo\n\t" \
          "s_cmp_gt_i32 vcc_lo, %[lk]\n\t" \
          "s_cbranch_scc0 Lo_w%=\n\t" \
          "Lo_s%=:\n\t" \
          "s_sleep 1\n\t" \
          "Lo_c%=:\n\t" \
          "s_memrealtime vcc\n\t" \
          "s_waitcnt lgkmcnt(0)\n\t" \
          "s_sub_i32 vcc_lo, %[t], vcc_lo\n\t" \
          "s_cmp_gt_i32 vcc_lo, %[lk]\n\t" \
          "s_cbranch_scc1 Lo_s%=\n\t" \
          "buffer_load_dword v255, %[al1], %[rsm], %[mb] offen sc1\n\t" \
          "s_waitcnt vmcnt(0)\n\t" \
          "v_lshrrev_b32 v252, 16, v255\n\t" \
          "v_cmp_eq_u32 vcc, 1, v252\n\t" \
          "s_cbranch_vccnz Lo_al%=\n\t" \
          "Lo_w%=:\n\t" \
          "s_memrealtime s[50:51]\n\t" \
          "s_getpc_b64 s[48:49]\n\t" \
          "Lo_y%=:\n\t" \
          "s_add_u32 s48, s48, Lo_z%=-Lo_y%=\n\t" \
          "s_addc_u32 s49, s49, 0\n\t" \
          "Lo_v%=:\n\t" \
          "s_memrealtime s[52:53]\n\t" \
          "s_waitcnt lgkmcnt(1)\n\t" \
          "s_sub_i32 vcc_lo, %[t], s50\n\t" \
          "s_cmp_gt_i32 vcc_lo, 3\n\t" \
          "s_cbranch_scc0 Lo_x%=\n\t" \
          "s_memrealtime s[50:51]\n\t" \
          "s_waitcnt lgkmcnt(1)\n\t" \
          "s_sub_i32 vcc_lo, %[t], s52\n\t" \
          "s_cmp_gt_i32 vcc_lo, 3\n\t" \
          "s_cbranch_scc0 Lo_x%=\n\t" \
          "s_memrealtime s[52:53]\n\t" \
          "s_waitcnt lgkmcnt(1)\n\t" \
          "s_sub_i32 vcc_lo, %[t], s50\n\t" \
          "s_cmp_gt_i32 vcc_lo, 3\n\t" \
          "s_cbranch_scc0 Lo_x%=\n\t" \
          "s_memrealtime s[50:51]\n\t" \
          "s_waitcnt lgkmcnt(1)\n\t" \
          "s_sub_i32 vcc_lo, %[t], s52\n\t" \
          "s_cmp_gt_i32 vcc_lo, 3\n\t" \
          "s_cbranch_scc0 Lo_x%=\n\t" \
          "s_memrealtime s[52:53]\n\t" \
          "s_waitcnt lgkmcnt(1)\n\t" \
          "s_sub_i32 vcc_lo, %[t], s50\n\t" \
          "s_cmp_gt_i32 vcc_lo, 3\n\t" \
          "s_cbranch_scc0 Lo_x%=\n\t" \
          "s_memrealtime s[50:51]\n\t" \
          "s_waitcnt lgkmcnt(1)\n\t" \
          "s_sub_i32 vcc_lo, %[t], s52\n\t" \
          "s_cmp_gt_i32 vcc_lo, 3\n\t" \
          "s_cbranch_scc0 Lo_x%=\n\t" \
          "s_memrealtime s[52:53]\n\t" \
          "s_waitcnt lgkmcnt(1)\n\t" \
          "s_sub_i32 vcc_lo, %[t], s50\n\t" \
          "s_cmp_gt_i32 vcc_lo, 3\n\t" \
          "s_cbranch_scc0 Lo_x%=\n\t" \
          "s_memrealtime s[50:51]\n\t" \
          "s_waitcnt lgkmcnt(1)\n\t" \
          "s_sub_i32 vcc_lo, %[t], s52\n\t" \
          "s_cmp_gt_i32 vcc_lo, 3\n\t" \
          "s_cbranch_scc0 Lo_x%=\n\t" \
          "s_branch Lo_v%=\n\t" \
          "Lo_x%=:\n\t" \
          "s_max_i32 vcc_lo, vcc_lo, 0\n\t" \
          "s_lshl_b32 vcc_lo, vcc_lo, 2\n\t" \
          "s_sub_u32 s48, s48, vcc_lo\n\t" \
          "s_subb_u32 s49, s49, 0\n\t" \
          "s_setpc_b64 s[48:49]\n\t" \
          "s_nop 5\n\t" \
          "s_nop 5\n\t" \
          "s_nop 5\n\t" \
          "Lo_z%=:\n\t" \
          "s_barrier\n\t" \
          VO_ST0 \
          "buffer_load_dwordx4 v[192:195], %[oo0], %[rsm], %[ob] offen sc1\n\t" \
          "s_nop 9\n\t" \
          "buffer_load_dwordx4 v[196:199], %[oo1], %[rsm], %[ob] offen sc1\n\t" \
          "s_nop 9\n\t" \
          "buffer_load_dwordx4 v[200:203], %[oo2], %[rsm], %[ob] offen sc1\n\t" \
          "s_nop 9\n\t" \
          "buffer_load_dwordx4 v[204:207], %[oo3], %[rsm], %[ob] offen sc1\n\t" \
          "s_nop 9\n\t" \
          "buffer_load_dwordx4 v[208:211], %[oo4], %[rsm], %[ob] offen sc1\n\t" \
          "s_nop 9\n\t" \
          "buffer_load_dwordx4 v[212:215], %[oo5], %[rsm], %[ob] offen sc1\n\t" \
          "s_nop 9\n\t" \
          "buffer_load_dwordx4 v[216:219], %[oo6], %[rsm], %[ob] offen sc1\n\t" \
          "s_nop 9\n\t" \
          "buffer_load_dwordx4 v[220:223], %[oo7], %[rsm], %[ob] offen sc1\n\t" \
          "s_waitcnt vmcnt(7)\n\t" \
          "v_smfmac_f32_16x16x64_bf16 v[224:227], v[192:195], a[0:7], %[idx]\n\t" \
          "v_max3_u32 v240, v192, v193, v194\n\t" \
          "v_smfmac_f32_16x16x64_bf16 v[228:231], v[192:195], a[8:15], %[idx]\n\t" \
          "v_max3_u32 v240, v194, v195, v240\n\t" \
          "v_smfmac_f32_16x16x64_bf16 v[232:235], v[192:195], a[16:23], %[idx]\n\t" \
          "v_cmp_eq_u32 vcc, -1, v240\n\t" \
          "v_smfmac_f32_16x16x64_bf16 v[236:239], v[192:195], a[24:31], %[idx]\n\t" \
          "s_waitcnt vmcnt(6)\n\t" \
          "v_smfmac_f32_16x16x64_bf16 v[224:227], v[196:199], a[32:39], %[idx]\n\t" \
          "s_cbranch_vccnz Lo_f0_%=\n\t" \
          "v_max3_u32 v240, v196, v197, v198\n\t" \
          "v_smfmac_f32_16x16x64_bf16 v[228:231], v[196:199], a[40:47], %[idx]\n\t" \
          "v_max3_u32 v240, v198, v199, v240\n\t" \
          "v_smfmac_f32_16x16x64_bf16 v[232:235], v[196:199], a[48:55], %[idx]\n\t" \
          "v_cmp_eq_u32 vcc, -1, v240\n\t" \
          "v_smfmac_f32_16x16x64_bf16 v[236:239], v[196:199], a[56:63], %[idx]\n\t" \
          "s_waitcnt vmcnt(5)\n\t" \
          "v_smfmac_f32_16x16x64_bf16 v[224:227], v[200:203], a[64:71], %[idx]\n\t" \
          "s_cbranch_vccnz Lo_f1_%=\n\t" \
          "v_max3_u32 v240, v200, v201, v202\n\t" \
          "v_smfmac_f32_16x16x64_bf16 v[228:231], v[200:203], a[72:79], %[idx]\n\t" \
          "v_max3_u32 v240, v202, v203, v240\n\t" \
          "v_smfmac_f32_16x16x64_bf16 v[232:235], v[200:203], a[80:87], %[idx]\n\t" \
          "v_cmp_eq_u32 vcc, -1, v240\n\t" \
          "v_smfmac_f32_16x16x64_bf16 v[236:239], v[200:203], a[88:95], %[idx]\n\t" \
          "s_waitcnt vmcnt(4)\n\t" \
          "v_smfmac_f32_16x16x64_bf16 v[224:227], v[204:207], a[96:103], %[idx]\n\t" \
          "s_cbranch_vccnz Lo_f2_%=\n\t" \
          "v_max3_u32 v240, v204, v205, v206\n\t" \
          "v_smfmac_f32_16x16x64_bf16 v[228:231], v[204:207], a[104:111], %[idx]\n\t" \
          "v_max3_u32 v240, v206, v207, v240\n\t" \
          "v_smfmac_f32_16x16x64_bf16 v[232:235], v[204:207], a[112:119], %[idx]\n\t" \
          "v_cmp_eq_u32 vcc, -1, v240\n\t" \
          "v_smfmac_f32_16x16x64_bf16 v[236:239], v[204:207], a[120:127], %[idx]\n\t" \
          "s_waitcnt vmcnt(3)\n\t" \
          "v_smfmac_f32_16x16x64_bf16 v[224:227], v[208:211], a[128:135], %[idx]\n\t" \
          "s_cbranch_vccnz Lo_f3_%=\n\t" \
          "v_max3_u32 v240, v208, v209, v210\n\t" \
          "v_smfmac_f32_16x16x64_bf16 v[228:231], v[208:211], a[136:143], %[idx]\n\t" \
          "v_max3_u32 v240, v210, v211, v240\n\t" \
          "v_smfmac_f32_16x16x64_bf16 v[232:235], v[208:211], a[144:151], %[idx]\n\t" \
          "v_cmp_eq_u32 vcc, -1, v240\n\t" \
          "v_smfmac_f32_16x16x64_bf16 v[236:239], v[208:211], a[152:159], %[idx]\n\t" \
          "s_waitcnt vmcnt(2)\n\t" \
          "v_smfmac_f32_16x16x64_bf16 v[224:227], v[212:215], a[160:167], %[idx]\n\t" \
          "s_cbranch_vccnz Lo_f4_%=\n\t" \
          "v_max3_u32 v240, v212, v213, v214\n\t" \
          "v_smfmac_f32_16x16x64_bf16 v[228:231], v[212:215], a[168:175], %[idx]\n\t" \
          "v_max3_u32 v240, v214, v215, v240\n\t" \
          "v_smfmac_f32_16x16x64_bf16 v[232:235], v[212:215], a[176:183], %[idx]\n\t" \
          "v_cmp_eq_u32 vcc, -1, v240\n\t" \
          "v_smfmac_f32_16x16x64_bf16 v[236:239], v[212:215], a[184:191], %[idx]\n\t" \
          "s_waitcnt vmcnt(1)\n\t" \
          "v_smfmac_f32_16x16x64_bf16 v[224:227], v[216:219], a[192:199], %[idx]\n\t" \
          "s_cbranch_vccnz Lo_f5_%=\n\t" \
          "v_max3_u32 v240, v216, v217, v218\n\t" \
          "v_smfmac_f32_16x16x64_bf16 v[228:231], v[216:219], a[200:207], %[idx]\n\t" \
          "v_max3_u32 v240, v218, v219, v240\n\t" \
          "v_smfmac_f32_16x16x64_bf16 v[232:235], v[216:219], a[208:215], %[idx]\n\t" \
          "v_cmp_eq_u32 vcc, -1, v240\n\t" \
          "v_smfmac_f32_16x16x64_bf16 v[236:239], v[216:219], a[216:223], %[idx]\n\t" \
          "s_waitcnt vmcnt(0)\n\t" \
          "v_smfmac_f32_16x16x64_bf16 v[224:227], v[220:223], a[224:231], %[idx]\n\t" \
          "s_cbranch_vccnz Lo_f6_%=\n\t" \
          "v_max3_u32 v240, v220, v221, v222\n\t" \
          "v_smfmac_f32_16x16x64_bf16 v[232:235], v[220:223], a[240:247], %[idx]\n\t" \
          "v_max3_u32 v240, v222, v223, v240\n\t" \
          "v_smfmac_f32_16x16x64_bf16 v[228:231], v[220:223], a[232:239], %[idx]\n\t" \
          "v_cmp_eq_u32 vcc, -1, v240\n\t" \
          "v_smfmac_f32_16x16x64_bf16 v[236:239], v[220:223], a[248:255], %[idx]\n\t" \
          VO_ST1 \
          "s_cmp_lg_u32 %[w], 0\n\t" \
          "v_add_f32 v242, v224, v225\n\t" \
          "v_add_f32 v244, v234, v235\n\t" \
          "s_cbranch_vccnz Lo_f7_%=\n\t" \
          "Lo_tail%=:\n\t" \
          "v_add_f32 v243, v228, v229\n\t" \
          "v_add_f32 v242, v242, v244\n\t" \
          "s_nop 0\n\t" \
          "v_add_f32 v243, v243, v238\n\t" \
          "s_cbranch_scc0 Lo_w0%=\n\t" \
          "v_add_f32 v243, v243, v239\n\t" \
          "ds_write_b64 %[rw], v[242:243]\n\t" \
          "s_waitcnt lgkmcnt(0)\n\t" \
          "s_barrier\n\t" \
          "s_cmp_eq_u32 %[w], 3\n\t" \
          "s_cbranch_scc0 Lv_z%=\n\t" \
          "s_sleep 1\n\t" \
          "s_branch Lo_end%=\n\t" \
          "Lo_w0%=:\n\t" \
          "v_add_f32 v243, v243, v239\n\t" \
          "s_barrier\n\t" \
          "ds_read_b64 v[244:245], %[rw] offset:512\n\t" \
          "ds_read_b64 v[246:247], %[rw] offset:1024\n\t" \
          "ds_read_b64 v[248:249], %[rw] offset:1536\n\t" \
          "s_waitcnt lgkmcnt(2)\n\t" \
          "v_pk_add_f32 v[242:243], v[242:243], v[244:245]\n\t" \
          "s_waitcnt lgkmcnt(1)\n\t" \
          "v_pk_add_f32 v[242:243], v[242:243], v[246:247]\n\t" \
          "s_waitcnt lgkmcnt(0)\n\t" \
          "v_pk_add_f32 v[242:243], v[242:243], v[248:249]\n\t" \
          "s_nop 0\n\t" \
          "v_cvt_pk_bf16_f32 v242, v242, v243\n\t" \
          "v_min_u32 v242, -2, v242\n\t" \
          "buffer_store_dword v242, %[mo], %[rsm], %[mbn] offen sc1\n\t" \
          VO_ST3 \
          "s_branch Lv_z%=\n\t" \
          "Lo_end%=:\n\t" \
          VO_ST3 \
          "s_memrealtime vcc\n\t" \
          "ds_read_b32 v241, %[fa]\n\t" \
          "s_add_u32 %[ta], %[t], %[d2]\n\t" \
          "s_waitcnt lgkmcnt(0)\n\t" \
          "s_mov_b32 %[pm], vcc_lo\n\t" \
          "v_readfirstlane_b32 %[fw], v241\n\t" \
          "s_add_u32 vcc_lo, vcc_lo, %[mn]\n\t" \
          "s_sub_i32 vcc_hi, vcc_lo, %[ta]\n\t" \
          "s_cmp_gt_i32 vcc_hi, 0\n\t" \
          "s_cselect_b32 %[ta], vcc_lo, %[ta]\n\t" \
          "s_cmp_eq_u32 %[fw], %[s1]\n\t" \
          "s_cbranch_scc1 Lo_fa%=\n\t" \
          "Lo_fb%=:\n\t" \
          "s_cmp_eq_u32 %[rd], 0\n\t" \
          "s_cbranch_scc0 Lo_ka%=\n\t" \
          "Lo_kb%=:\n\t" \
          "s_add_u32 %[cnt], %[ta], %[dv]\n\t" \
          "Lv_w%=:\n\t" \
          "s_memrealtime s[50:51]\n\t" \
          "s_getpc_b64 s[48:49]\n\t" \
          "Lv_y%=:\n\t" \
          "s_add_u32 s48, s48, Lv_z%=-Lv_y%=\n\t" \
          "s_addc_u32 s49, s49, 0\n\t" \
          "Lv_v%=:\n\t" \
          "s_memrealtime s[52:53]\n\t" \
          "s_waitcnt lgkmcnt(1)\n\t" \
          "s_sub_i32 vcc_lo, %[cnt], s50\n\t" \
          "s_cmp_gt_i32 vcc_lo, 3\n\t" \
          "s_cbranch_scc0 Lv_x%=\n\t" \
          "s_memrealtime s[50:51]\n\t" \
          "s_waitcnt lgkmcnt(1)\n\t" \
          "s_sub_i32 vcc_lo, %[cnt], s52\n\t" \
          "s_cmp_gt_i32 vcc_lo, 3\n\t" \
          "s_cbranch_scc0 Lv_x%=\n\t" \
          "s_memrealtime s[52:53]\n\t" \
          "s_waitcnt lgkmcnt(1)\n\t" \
          "s_sub_i32 vcc_lo, %[cnt], s50\n\t" \
          "s_cmp_gt_i32 vcc_lo, 3\n\t" \
          "s_cbranch_scc0 Lv_x%=\n\t" \
          "s_memrealtime s[50:51]\n\t" \
          "s_waitcnt lgkmcnt(1)\n\t" \
          "s_sub_i32 vcc_lo, %[cnt], s52\n\t" \
          "s_cmp_gt_i32 vcc_lo, 3\n\t" \
          "s_cbranch_scc0 Lv_x%=\n\t" \
          "s_memrealtime s[52:53]\n\t" \
          "s_waitcnt lgkmcnt(1)\n\t" \
          "s_sub_i32 vcc_lo, %[cnt], s50\n\t" \
          "s_cmp_gt_i32 vcc_lo, 3\n\t" \
          "s_cbranch_scc0 Lv_x%=\n\t" \
          "s_memrealtime s[50:51]\n\t" \
          "s_waitcnt lgkmcnt(1)\n\t" \
          "s_sub_i32 vcc_lo, %[cnt], s52\n\t" \
          "s_cmp_gt_i32 vcc_lo, 3\n\t" \
          "s_cbranch_scc0 Lv_x%=\n\t" \
          "s_memrealtime s[52:53]\n\t" \
          "s_waitcnt lgkmcnt(1)\n\t" \
          "s_sub_i32 vcc_lo, %[cnt], s50\n\t" \
          "s_cmp_gt_i32 vcc_lo, 3\n\t" \
          "s_cbranch_scc0 Lv_x%=\n\t" \
          "s_memrealtime s[50:51]\n\t" \
          "s_waitcnt lgkmcnt(1)\n\t" \
          "s_sub_i32 vcc_lo, %[cnt], s52\n\t" \
          "s_cmp_gt_i32 vcc_lo, 3\n\t" \
          "s_cbranch_scc0 Lv_x%=\n\t" \
          "s_branch Lv_v%=\n\t" \
          "Lv_x%=:\n\t" \
          "s_max_i32 vcc_lo, vcc_lo, 0\n\t" \
          "s_lshl_b32 vcc_lo, vcc_lo, 2\n\t" \
          "s_sub_u32 s48, s48, vcc_lo\n\t" \
          "s_subb_u32 s49, s49, 0\n\t" \
          "s_setpc_b64 s[48:49]\n\t" \
          "s_nop 5\n\t" \
          "s_nop 5\n\t" \
          "s_nop 5\n\t" \
          "Lv_z%=:\n\t" \
          "s_barrier\n\t" \
          "Lv_n%=:\n\t" \
          "buffer_load_dwordx4 v[192:195], %[ga0], %[rsm], %[mbn] offen sc1\n\t" \
          "s_nop 15\n\t" \
          "s_nop 5\n\t" \
          "buffer_load_dwordx4 v[196:199], %[ga1], %[rsm], %[mbn] offen sc1\n\t" \
          "s_nop 15\n\t" \
          "s_nop 5\n\t" \
          "buffer_load_dwordx4 v[200:203], %[ga2], %[rsm], %[mbn] offen sc1\n\t" \
          "s_nop 15\n\t" \
          "s_nop 5\n\t" \
          "buffer_load_dwordx4 v[204:207], %[ga3], %[rsm], %[mbn] offen sc1\n\t" \
          "buffer_load_dword %[al], %[alo], %[rsm], %[mb] offen sc1\n\t" \
          "Lv_go%=:\n\t" \
          "s_waitcnt vmcnt(4)\n\t" \
          "v_smfmac_f32_16x16x64_bf16 v[176:179], v[192:195], v[8:15], %[idx]\n\t" \
          "v_max3_u32 v240, v192, v193, v194\n\t" \
          "v_smfmac_f32_16x16x64_bf16 v[180:183], v[192:195], v[16:23], %[idx]\n\t" \
          "v_max3_u32 v240, v194, v195, v240\n\t" \
          "v_smfmac_f32_16x16x64_bf16 v[184:187], v[192:195], v[24:31], %[idx]\n\t" \
          "buffer_load_dwordx2 %[rec], off, %[rsm], %[rco] sc1\n\t" \
          "v_smfmac_f32_16x16x64_bf16 v[188:191], v[192:195], v[32:39], %[idx]\n\t" \
          "s_waitcnt vmcnt(4)\n\t" \
          "v_smfmac_f32_16x16x64_bf16 v[176:179], v[196:199], v[40:47], %[idx]\n\t" \
          "v_max3_u32 v240, v196, v197, v240\n\t" \
          "v_smfmac_f32_16x16x64_bf16 v[180:183], v[196:199], v[48:55], %[idx]\n\t" \
          "v_max3_u32 v240, v198, v199, v240\n\t" \
          "v_smfmac_f32_16x16x64_bf16 v[184:187], v[196:199], v[56:63], %[idx]\n\t" \
          "v_smfmac_f32_16x16x64_bf16 v[188:191], v[196:199], v[64:71], %[idx]\n\t" \
          "s_waitcnt vmcnt(3)\n\t" \
          "v_smfmac_f32_16x16x64_bf16 v[176:179], v[200:203], v[72:79], %[idx]\n\t" \
          "v_max3_u32 v240, v200, v201, v240\n\t" \
          "v_smfmac_f32_16x16x64_bf16 v[180:183], v[200:203], v[80:87], %[idx]\n\t" \
          "v_max3_u32 v240, v202, v203, v240\n\t" \
          "v_smfmac_f32_16x16x64_bf16 v[184:187], v[200:203], v[88:95], %[idx]\n\t" \
          "v_smfmac_f32_16x16x64_bf16 v[188:191], v[200:203], v[96:103], %[idx]\n\t" \
          "s_waitcnt vmcnt(2)\n\t" \
          "v_smfmac_f32_16x16x64_bf16 v[176:179], v[204:207], v[104:111], %[idx]\n\t" \
          "v_max3_u32 v240, v204, v205, v240\n\t" \
          "v_smfmac_f32_16x16x64_bf16 v[180:183], v[204:207], v[112:119], %[idx]\n\t" \
          "v_max3_u32 v240, v206, v207, v240\n\t" \
          "v_smfmac_f32_16x16x64_bf16 v[184:187], v[204:207], v[120:127], %[idx]\n\t" \
          "v_cmp_eq_u32 vcc, -1, v240\n\t" \
          "v_smfmac_f32_16x16x64_bf16 v[188:191], v[204:207], v[128:135], %[idx]\n\t" \
          "s_and_b64 vcc, vcc, %[en]\n\t" \
          "v_add_f32 v242, v176, v177\n\t" \
          "v_add_f32 v243, v178, v179\n\t" \
          "v_add_f32 v244, v180, v181\n\t" \
          "v_add_f32 v245, v182, v183\n\t" \
          "s_cbranch_scc1 Lv_fail%=\n\t" \
          "v_add_f32 v246, v184, v185\n\t" \
          "v_add_f32 v247, v186, v187\n\t" \
          "ds_write_b64 %[vw0], v[242:243]\n\t" \
          "ds_write_b64 %[vw1], v[244:245]\n\t" \
          "ds_write_b64 %[vw2], v[246:247]\n\t" \
          "v_add_f32 v248, v188, v189\n\t" \
          "v_add_f32 v249, v190, v191\n\t" \
          "v_mfma_f32_16x16x32_bf16 v[208:211], v[212:215], v[212:215], 0\n\t" \
          "v_mfma_f32_16x16x32_bf16 v[208:211], v[212:215], v[212:215], 0\n\t" \
          "s_waitcnt lgkmcnt(0)\n\t" \
          "s_barrier\n\t" \
          "ds_read_b64 v[242:243], %[vr] offset:0\n\t" \
          "v_mfma_f32_16x16x32_bf16 v[208:211], v[212:215], v[212:215], 0\n\t" \
          "ds_read_b64 v[244:245], %[vr] offset:2064\n\t" \
          "ds_read_b64 v[246:247], %[vr] offset:4128\n\t" \
          "v_mfma_f32_16x16x32_bf16 v[208:211], v[212:215], v[212:215], 0\n\t" \
          "s_waitcnt lgkmcnt(2)\n\t" \
          "v_add_f32 v248, v248, v242\n\t" \
          "v_add_f32 v249, v249, v243\n\t" \
          "s_waitcnt lgkmcnt(1)\n\t" \
          "v_add_f32 v248, v248, v244\n\t" \
          "v_add_f32 v249, v249, v245\n\t" \
          "s_waitcnt lgkmcnt(0)\n\t" \
          "v_add_f32 v248, v248, v246\n\t" \
          "v_add_f32 v249, v249, v247\n\t" \
          "v_cvt_pk_bf16_f32 v242, v248, v249\n\t" \
          "v_min_u32 v242, -2, v242\n\t" \
          "buffer_store_dword v242, %[vo], %[rsm], %[vb] offen sc1\n\t" \
          "s_memrealtime %[te]\n\t" \
          "s_waitcnt vmcnt(1) lgkmcnt(0)\n\t"
#define VO_W "{a[0:31]}"(wa[0]), "{a[32:63]}"(wa[1]), "{a[64:95]}"(wa[2]), "{a[96:127]}"(wa[3]), "{a[128:159]}"(wa[4]), "{a[160:191]}"(wa[5]), "{a[192:223]}"(wa[6]), "{a[224:255]}"(wa[7]), "{v[8:39]}"(wv_[0]), "{v[40:71]}"(wv_[1]), "{v[72:103]}"(wv_[2]), "{v[104:135]}"(wv_[3])
#define VO_DEFW "=&{a[0:31]}"(wa[0]), "=&{a[32:63]}"(wa[1]), "=&{a[64:95]}"(wa[2]), "=&{a[96:127]}"(wa[3]), "=&{a[128:159]}"(wa[4]), "=&{a[160:191]}"(wa[5]), "=&{a[192:223]}"(wa[6]), "=&{a[224:255]}"(wa[7]), "=&{v[8:39]}"(wv_[0]), "=&{v[40:71]}"(wv_[1]), "=&{v[72:103]}"(wv_[2]), "=&{v[104:135]}"(wv_[3])
#define VO_LOADER \
          "s_mov_b64 s[48:49], %[wo]\n\ts_mov_b32 s50, %[wsz]\n\ts_mov_b32 s51, 0x20000\n\ts_nop 4\n\ts_add_u32 m0, %[stg], 0\n\ts_xor_b32 %[t], %[vk], 0x0\n\t" \
          "buffer_load_dwordx4 %[sv], %[rv], %[t] offen lds\n\ts_add_u32 m0, %[stg], 1024\n\ts_xor_b32 %[t], %[vk], 0x10000\n\tbuffer_load_dwordx4 %[sv], %[rv], %[t] offen lds\n\ts_add_u32 m0, %[stg], 2048\n\ts_xor_b32 %[t], %[vk], 0x20000\n\t" \
          "buffer_load_dwordx4 %[sv], %[rv], %[t] offen lds\n\ts_add_u32 m0, %[stg], 3072\n\ts_xor_b32 %[t], %[vk], 0x30000\n\tbuffer_load_dwordx4 %[sv], %[rv], %[t] offen lds\n\ts_add_u32 m0, %[stg], 4096\n\ts_xor_b32 %[t], %[vk], 0x40000\n\t" \
          "buffer_load_dwordx4 %[sv], %[rv], %[t] offen lds\n\ts_add_u32 m0, %[stg], 5120\n\ts_xor_b32 %[t], %[vk], 0x50000\n\tbuffer_load_dwordx4 %[sv], %[rv], %[t] offen lds\n\ts_add_u32 m0, %[stg], 6144\n\ts_xor_b32 %[t], %[vk], 0x60000\n\t" \
          "buffer_load_dwordx4 %[sv], %[rv], %[t] offen lds\n\ts_add_u32 m0, %[stg], 7168\n\ts_xor_b32 %[t], %[vk], 0x70000\n\tbuffer_load_dwordx4 %[sv], %[rv], %[t] offen lds\n\ts_add_u32 m0, %[stg], 8192\n\ts_xor_b32 %[t], %[vk], 0x100000\n\t" \
          "buffer_load_dwordx4 %[sv], %[rv], %[t] offen lds\n\ts_add_u32 m0, %[stg], 9216\n\ts_xor_b32 %[t], %[vk], 0x110000\n\tbuffer_load_dwordx4 %[sv], %[rv], %[t] offen lds\n\ts_add_u32 m0, %[stg], 10240\n\ts_xor_b32 %[t], %[vk], 0x120000\n\t" \
          "buffer_load_dwordx4 %[sv], %[rv], %[t] offen lds\n\ts_add_u32 m0, %[stg], 11264\n\ts_xor_b32 %[t], %[vk], 0x130000\n\tbuffer_load_dwordx4 %[sv], %[rv], %[t] offen lds\n\ts_add_u32 m0, %[stg], 12288\n\ts_xor_b32 %[t], %[vk], 0x140000\n\t" \
          "buffer_load_dwordx4 %[sv], %[rv], %[t] offen lds\n\ts_add_u32 m0, %[stg], 13312\n\ts_xor_b32 %[t], %[vk], 0x150000\n\tbuffer_load_dwordx4 %[sv], %[rv], %[t] offen lds\n\ts_add_u32 m0, %[stg], 14336\n\ts_xor_b32 %[t], %[vk], 0x160000\n\t" \
          "buffer_load_dwordx4 %[sv], %[rv], %[t] offen lds\n\ts_add_u32 m0, %[stg], 15360\n\ts_xor_b32 %[t], %[vk], 0x170000\n\tbuffer_load_dwordx4 %[sv], %[rv], %[t] offen lds\n\ts_add_u32 m0, %[stg], 16384\n\ts_xor_b32 %[t], %[vk], 0x200000\n\t" \
          "buffer_load_dwordx4 %[sv], %[rv], %[t] offen lds\n\ts_add_u32 m0, %[stg], 17408\n\ts_xor_b32 %[t], %[vk], 0x210000\n\tbuffer_load_dwordx4 %[sv], %[rv], %[t] offen lds\n\ts_add_u32 m0, %[stg], 18432\n\ts_xor_b32 %[t], %[vk], 0x220000\n\t" \
          "buffer_load_dwordx4 %[sv], %[rv], %[t] offen lds\n\ts_add_u32 m0, %[stg], 19456\n\ts_xor_b32 %[t], %[vk], 0x230000\n\tbuffer_load_dwordx4 %[sv], %[rv], %[t] offen lds\n\ts_add_u32 m0, %[stg], 20480\n\ts_xor_b32 %[t], %[vk], 0x240000\n\t" \
          "buffer_load_dwordx4 %[sv], %[rv], %[t] offen lds\n\ts_add_u32 m0, %[stg], 21504\n\ts_xor_b32 %[t], %[vk], 0x250000\n\tbuffer_load_dwordx4 %[sv], %[rv], %[t] offen lds\n\ts_add_u32 m0, %[stg], 22528\n\ts_xor_b32 %[t], %[vk], 0x260000\n\t" \
          "buffer_load_dwordx4 %[sv], %[rv], %[t] offen lds\n\ts_add_u32 m0, %[stg], 23552\n\ts_xor_b32 %[t], %[vk], 0x270000\n\tbuffer_load_dwordx4 %[sv], %[rv], %[t] offen lds\n\ts_add_u32 m0, %[stg], 24576\n\ts_xor_b32 %[t], %[vk], 0x300000\n\t" \
          "buffer_load_dwordx4 %[sv], %[rv], %[t] offen lds\n\ts_add_u32 m0, %[stg], 25600\n\ts_xor_b32 %[t], %[vk], 0x310000\n\tbuffer_load_dwordx4 %[sv], %[rv], %[t] offen lds\n\ts_add_u32 m0, %[stg], 26624\n\ts_xor_b32 %[t], %[vk], 0x320000\n\t" \
          "buffer_load_dwordx4 %[sv], %[rv], %[t] offen lds\n\ts_add_u32 m0, %[stg], 27648\n\ts_xor_b32 %[t], %[vk], 0x330000\n\tbuffer_load_dwordx4 %[sv], %[rv], %[t] offen lds\n\ts_add_u32 m0, %[stg], 28672\n\ts_xor_b32 %[t], %[vk], 0x340000\n\t" \
          "buffer_load_dwordx4 %[sv], %[rv], %[t] offen lds\n\ts_add_u32 m0, %[stg], 29696\n\ts_xor_b32 %[t], %[vk], 0x350000\n\tbuffer_load_dwordx4 %[sv], %[rv], %[t] offen lds\n\ts_add_u32 m0, %[stg], 30720\n\ts_xor_b32 %[t], %[vk], 0x360000\n\t" \
          "buffer_load_dwordx4 %[sv], %[rv], %[t] offen lds\n\ts_add_u32 m0, %[stg], 31744\n\ts_xor_b32 %[t], %[vk], 0x370000\n\tbuffer_load_dwordx4 %[sv], %[rv], %[t] offen lds\n\ts_waitcnt vmcnt(16)\n\tds_read_b64_tr_b16 v[8:9], %[vb0] offset:0\n\t" \
          "ds_read_b64_tr_b16 v[10:11], %[vb0] offset:128\n\tds_read_b64_tr_b16 v[12:13], %[vb0] offset:4096\n\tds_read_b64_tr_b16 v[14:15], %[vb0] offset:4224\n\tds_read_b64_tr_b16 v[16:17], %[vb1] offset:0\n\tds_read_b64_tr_b16 v[18:19], %[vb1] offset:128\n\tds_read_b64_tr_b16 v[20:21], %[vb1] offset:4096\n\t" \
          "ds_read_b64_tr_b16 v[22:23], %[vb1] offset:4224\n\tds_read_b64_tr_b16 v[24:25], %[vb2] offset:0\n\tds_read_b64_tr_b16 v[26:27], %[vb2] offset:128\n\tds_read_b64_tr_b16 v[28:29], %[vb2] offset:4096\n\tds_read_b64_tr_b16 v[30:31], %[vb2] offset:4224\n\tds_read_b64_tr_b16 v[32:33], %[vb3] offset:0\n\t" \
          "ds_read_b64_tr_b16 v[34:35], %[vb3] offset:128\n\tds_read_b64_tr_b16 v[36:37], %[vb3] offset:4096\n\tds_read_b64_tr_b16 v[38:39], %[vb3] offset:4224\n\tds_read_b64_tr_b16 v[40:41], %[vb0] offset:8192\n\tds_read_b64_tr_b16 v[42:43], %[vb0] offset:8320\n\tds_read_b64_tr_b16 v[44:45], %[vb0] offset:12288\n\t" \
          "ds_read_b64_tr_b16 v[46:47], %[vb0] offset:12416\n\tds_read_b64_tr_b16 v[48:49], %[vb1] offset:8192\n\tds_read_b64_tr_b16 v[50:51], %[vb1] offset:8320\n\tds_read_b64_tr_b16 v[52:53], %[vb1] offset:12288\n\tds_read_b64_tr_b16 v[54:55], %[vb1] offset:12416\n\tds_read_b64_tr_b16 v[56:57], %[vb2] offset:8192\n\t" \
          "ds_read_b64_tr_b16 v[58:59], %[vb2] offset:8320\n\tds_read_b64_tr_b16 v[60:61], %[vb2] offset:12288\n\tds_read_b64_tr_b16 v[62:63], %[vb2] offset:12416\n\tds_read_b64_tr_b16 v[64:65], %[vb3] offset:8192\n\tds_read_b64_tr_b16 v[66:67], %[vb3] offset:8320\n\tds_read_b64_tr_b16 v[68:69], %[vb3] offset:12288\n\t" \
          "ds_read_b64_tr_b16 v[70:71], %[vb3] offset:12416\n\ts_waitcnt lgkmcnt(0)\n\ts_add_u32 %[ho], %[k0], 1\n\ts_and_b32 %[ho], %[ho], 2\n\ts_or_b32 %[ho], %[ho], 0\n\ts_lshl_b32 %[ho], %[ho], 20\n\t" \
          "s_xor_b32 %[ho], %[ho], %[hx]\n\ts_add_u32 m0, %[stg], 0\n\ts_add_u32 %[t], %[ho], 0\n\tbuffer_load_dwordx4 %[so], s[48:51], %[t] offen lds\n\ts_add_u32 m0, %[stg], 1024\n\ts_add_u32 %[t], %[ho], 16384\n\t" \
          "buffer_load_dwordx4 %[so], s[48:51], %[t] offen lds\n\ts_add_u32 m0, %[stg], 2048\n\ts_add_u32 %[t], %[ho], 32768\n\tbuffer_load_dwordx4 %[so], s[48:51], %[t] offen lds\n\ts_add_u32 m0, %[stg], 3072\n\ts_add_u32 %[t], %[ho], 49152\n\t" \
          "buffer_load_dwordx4 %[so], s[48:51], %[t] offen lds\n\ts_add_u32 m0, %[stg], 4096\n\ts_add_u32 %[t], %[ho], 65536\n\tbuffer_load_dwordx4 %[so], s[48:51], %[t] offen lds\n\ts_add_u32 m0, %[stg], 5120\n\ts_add_u32 %[t], %[ho], 81920\n\t" \
          "buffer_load_dwordx4 %[so], s[48:51], %[t] offen lds\n\ts_add_u32 m0, %[stg], 6144\n\ts_add_u32 %[t], %[ho], 98304\n\tbuffer_load_dwordx4 %[so], s[48:51], %[t] offen lds\n\ts_add_u32 m0, %[stg], 7168\n\ts_add_u32 %[t], %[ho], 114688\n\t" \
          "buffer_load_dwordx4 %[so], s[48:51], %[t] offen lds\n\ts_add_u32 m0, %[stg], 8192\n\ts_add_u32 %[t], %[ho], 131072\n\tbuffer_load_dwordx4 %[so], s[48:51], %[t] offen lds\n\ts_add_u32 m0, %[stg], 9216\n\ts_add_u32 %[t], %[ho], 147456\n\t" \
          "buffer_load_dwordx4 %[so], s[48:51], %[t] offen lds\n\ts_add_u32 m0, %[stg], 10240\n\ts_add_u32 %[t], %[ho], 163840\n\tbuffer_load_dwordx4 %[so], s[48:51], %[t] offen lds\n\ts_add_u32 m0, %[stg], 11264\n\ts_add_u32 %[t], %[ho], 180224\n\t" \
          "buffer_load_dwordx4 %[so], s[48:51], %[t] offen lds\n\ts_add_u32 m0, %[stg], 12288\n\ts_add_u32 %[t], %[ho], 196608\n\tbuffer_load_dwordx4 %[so], s[48:51], %[t] offen lds\n\ts_add_u32 m0, %[stg], 13312\n\ts_add_u32 %[t], %[ho], 212992\n\t" \
          "buffer_load_dwordx4 %[so], s[48:51], %[t] offen lds\n\ts_add_u32 m0, %[stg], 14336\n\ts_add_u32 %[t], %[ho], 229376\n\tbuffer_load_dwordx4 %[so], s[48:51], %[t] offen lds\n\ts_add_u32 m0, %[stg], 15360\n\ts_add_u32 %[t], %[ho], 245760\n\t" \
          "buffer_load_dwordx4 %[so], s[48:51], %[t] offen lds\n\ts_waitcnt vmcnt(16)\n\tds_read_b64_tr_b16 v[72:73], %[vb0] offset:16384\n\tds_read_b64_tr_b16 v[74:75], %[vb0] offset:16512\n\tds_read_b64_tr_b16 v[76:77], %[vb0] offset:20480\n\tds_read_b64_tr_b16 v[78:79], %[vb0] offset:20608\n\t" \
          "ds_read_b64_tr_b16 v[80:81], %[vb1] offset:16384\n\tds_read_b64_tr_b16 v[82:83], %[vb1] offset:16512\n\tds_read_b64_tr_b16 v[84:85], %[vb1] offset:20480\n\tds_read_b64_tr_b16 v[86:87], %[vb1] offset:20608\n\tds_read_b64_tr_b16 v[88:89], %[vb2] offset:16384\n\tds_read_b64_tr_b16 v[90:91], %[vb2] offset:16512\n\t" \
          "ds_read_b64_tr_b16 v[92:93], %[vb2] offset:20480\n\tds_read_b64_tr_b16 v[94:95], %[vb2] offset:20608\n\tds_read_b64_tr_b16 v[96:97], %[vb3] offset:16384\n\tds_read_b64_tr_b16 v[98:99], %[vb3] offset:16512\n\tds_read_b64_tr_b16 v[100:101], %[vb3] offset:20480\n\tds_read_b64_tr_b16 v[102:103], %[vb3] offset:20608\n\t" \
          "ds_read_b64_tr_b16 v[104:105], %[vb0] offset:24576\n\tds_read_b64_tr_b16 v[106:107], %[vb0] offset:24704\n\tds_read_b64_tr_b16 v[108:109], %[vb0] offset:28672\n\tds_read_b64_tr_b16 v[110:111], %[vb0] offset:28800\n\tds_read_b64_tr_b16 v[112:113], %[vb1] offset:24576\n\tds_read_b64_tr_b16 v[114:115], %[vb1] offset:24704\n\t" \
          "ds_read_b64_tr_b16 v[116:117], %[vb1] offset:28672\n\tds_read_b64_tr_b16 v[118:119], %[vb1] offset:28800\n\tds_read_b64_tr_b16 v[120:121], %[vb2] offset:24576\n\tds_read_b64_tr_b16 v[122:123], %[vb2] offset:24704\n\tds_read_b64_tr_b16 v[124:125], %[vb2] offset:28672\n\tds_read_b64_tr_b16 v[126:127], %[vb2] offset:28800\n\t" \
          "ds_read_b64_tr_b16 v[128:129], %[vb3] offset:24576\n\tds_read_b64_tr_b16 v[130:131], %[vb3] offset:24704\n\tds_read_b64_tr_b16 v[132:133], %[vb3] offset:28672\n\tds_read_b64_tr_b16 v[134:135], %[vb3] offset:28800\n\ts_waitcnt lgkmcnt(0)\n\ts_add_u32 %[ho], %[k0], 0\n\t" \
          "s_and_b32 %[ho], %[ho], 2\n\ts_or_b32 %[ho], %[ho], 1\n\ts_lshl_b32 %[ho], %[ho], 20\n\ts_xor_b32 %[ho], %[ho], %[hx]\n\ts_add_u32 m0, %[stg], 16384\n\ts_add_u32 %[t], %[ho], 0\n\t" \
          "buffer_load_dwordx4 %[so], s[48:51], %[t] offen lds\n\ts_add_u32 m0, %[stg], 17408\n\ts_add_u32 %[t], %[ho], 16384\n\tbuffer_load_dwordx4 %[so], s[48:51], %[t] offen lds\n\ts_add_u32 m0, %[stg], 18432\n\ts_add_u32 %[t], %[ho], 32768\n\t" \
          "buffer_load_dwordx4 %[so], s[48:51], %[t] offen lds\n\ts_add_u32 m0, %[stg], 19456\n\ts_add_u32 %[t], %[ho], 49152\n\tbuffer_load_dwordx4 %[so], s[48:51], %[t] offen lds\n\ts_add_u32 m0, %[stg], 20480\n\ts_add_u32 %[t], %[ho], 65536\n\t" \
          "buffer_load_dwordx4 %[so], s[48:51], %[t] offen lds\n\ts_add_u32 m0, %[stg], 21504\n\ts_add_u32 %[t], %[ho], 81920\n\tbuffer_load_dwordx4 %[so], s[48:51], %[t] offen lds\n\ts_add_u32 m0, %[stg], 22528\n\ts_add_u32 %[t], %[ho], 98304\n\t" \
          "buffer_load_dwordx4 %[so], s[48:51], %[t] offen lds\n\ts_add_u32 m0, %[stg], 23552\n\ts_add_u32 %[t], %[ho], 114688\n\tbuffer_load_dwordx4 %[so], s[48:51], %[t] offen lds\n\ts_add_u32 m0, %[stg], 24576\n\ts_add_u32 %[t], %[ho], 131072\n\t" \
          "buffer_load_dwordx4 %[so], s[48:51], %[t] offen lds\n\ts_add_u32 m0, %[stg], 25600\n\ts_add_u32 %[t], %[ho], 147456\n\tbuffer_load_dwordx4 %[so], s[48:51], %[t] offen lds\n\ts_add_u32 m0, %[stg], 26624\n\ts_add_u32 %[t], %[ho], 163840\n\t" \
          "buffer_load_dwordx4 %[so], s[48:51], %[t] offen lds\n\ts_add_u32 m0, %[stg], 27648\n\ts_add_u32 %[t], %[ho], 180224\n\tbuffer_load_dwordx4 %[so], s[48:51], %[t] offen lds\n\ts_add_u32 m0, %[stg], 28672\n\ts_add_u32 %[t], %[ho], 196608\n\t" \
          "buffer_load_dwordx4 %[so], s[48:51], %[t] offen lds\n\ts_add_u32 m0, %[stg], 29696\n\ts_add_u32 %[t], %[ho], 212992\n\tbuffer_load_dwordx4 %[so], s[48:51], %[t] offen lds\n\ts_add_u32 m0, %[stg], 30720\n\ts_add_u32 %[t], %[ho], 229376\n\t" \
          "buffer_load_dwordx4 %[so], s[48:51], %[t] offen lds\n\ts_add_u32 m0, %[stg], 31744\n\ts_add_u32 %[t], %[ho], 245760\n\tbuffer_load_dwordx4 %[so], s[48:51], %[t] offen lds\n\ts_mov_b32 %[n], 8\n\ts_cmp_eq_u32 %[k0], 2\n\t" \
          "s_cbranch_scc1 Lw_2_%=\n\ts_cmp_eq_u32 %[k0], 0\n\ts_cbranch_scc1 Lw_0_%=\n\ts_waitcnt vmcnt(0)\n\ts_cmp_eq_u32 %[k0], 1\n\ts_cbranch_scc1 Lw_1_%=\n\t" \
          "s_branch Lw_3_%=\n\tLw_0_%=:\n\ts_waitcnt vmcnt(16)\n\tds_read_b64_tr_b16 a[0:1], %[ob] offset:0\n\tds_read_b64_tr_b16 a[2:3], %[ob] offset:128\n\tds_read_b64_tr_b16 a[4:5], %[ob] offset:8192\n\t" \
          "ds_read_b64_tr_b16 a[6:7], %[ob] offset:8320\n\tds_read_b64_tr_b16 a[8:9], %[ob] offset:32\n\tds_read_b64_tr_b16 a[10:11], %[ob] offset:160\n\tds_read_b64_tr_b16 a[12:13], %[ob] offset:8224\n\tds_read_b64_tr_b16 a[14:15], %[ob] offset:8352\n\tds_read_b64_tr_b16 a[16:17], %[ob] offset:1024\n\t" \
          "ds_read_b64_tr_b16 a[18:19], %[ob] offset:1152\n\tds_read_b64_tr_b16 a[20:21], %[ob] offset:9216\n\tds_read_b64_tr_b16 a[22:23], %[ob] offset:9344\n\tds_read_b64_tr_b16 a[24:25], %[ob] offset:1056\n\tds_read_b64_tr_b16 a[26:27], %[ob] offset:1184\n\tds_read_b64_tr_b16 a[28:29], %[ob] offset:9248\n\t" \
          "ds_read_b64_tr_b16 a[30:31], %[ob] offset:9376\n\ts_cmp_gt_u32 %[n], 2\n\ts_cselect_b32 s50, s50, 0\n\ts_cmp_gt_u32 %[n], 6\n\ts_cselect_b32 %[ho], 0x2, 0x6\n\ts_lshl_b32 %[ho], %[ho], 20\n\t" \
          "s_xor_b32 %[ho], %[ho], %[hx]\n\ts_waitcnt lgkmcnt(0)\n\ts_add_u32 m0, %[stg], 0\n\ts_add_u32 %[t], %[ho], 0\n\tbuffer_load_dwordx4 %[so], s[48:51], %[t] offen lds\n\ts_add_u32 m0, %[stg], 1024\n\t" \
          "s_add_u32 %[t], %[ho], 16384\n\tbuffer_load_dwordx4 %[so], s[48:51], %[t] offen lds\n\ts_add_u32 m0, %[stg], 2048\n\ts_add_u32 %[t], %[ho], 32768\n\tbuffer_load_dwordx4 %[so], s[48:51], %[t] offen lds\n\ts_add_u32 m0, %[stg], 3072\n\t" \
          "s_add_u32 %[t], %[ho], 49152\n\tbuffer_load_dwordx4 %[so], s[48:51], %[t] offen lds\n\ts_add_u32 m0, %[stg], 4096\n\ts_add_u32 %[t], %[ho], 65536\n\tbuffer_load_dwordx4 %[so], s[48:51], %[t] offen lds\n\ts_add_u32 m0, %[stg], 5120\n\t" \
          "s_add_u32 %[t], %[ho], 81920\n\tbuffer_load_dwordx4 %[so], s[48:51], %[t] offen lds\n\ts_add_u32 m0, %[stg], 6144\n\ts_add_u32 %[t], %[ho], 98304\n\tbuffer_load_dwordx4 %[so], s[48:51], %[t] offen lds\n\ts_add_u32 m0, %[stg], 7168\n\t" \
          "s_add_u32 %[t], %[ho], 114688\n\tbuffer_load_dwordx4 %[so], s[48:51], %[t] offen lds\n\ts_add_u32 m0, %[stg], 8192\n\ts_add_u32 %[t], %[ho], 131072\n\tbuffer_load_dwordx4 %[so], s[48:51], %[t] offen lds\n\ts_add_u32 m0, %[stg], 9216\n\t" \
          "s_add_u32 %[t], %[ho], 147456\n\tbuffer_load_dwordx4 %[so], s[48:51], %[t] offen lds\n\ts_add_u32 m0, %[stg], 10240\n\ts_add_u32 %[t], %[ho], 163840\n\tbuffer_load_dwordx4 %[so], s[48:51], %[t] offen lds\n\ts_add_u32 m0, %[stg], 11264\n\t" \
          "s_add_u32 %[t], %[ho], 180224\n\tbuffer_load_dwordx4 %[so], s[48:51], %[t] offen lds\n\ts_add_u32 m0, %[stg], 12288\n\ts_add_u32 %[t], %[ho], 196608\n\tbuffer_load_dwordx4 %[so], s[48:51], %[t] offen lds\n\ts_add_u32 m0, %[stg], 13312\n\t" \
          "s_add_u32 %[t], %[ho], 212992\n\tbuffer_load_dwordx4 %[so], s[48:51], %[t] offen lds\n\ts_add_u32 m0, %[stg], 14336\n\ts_add_u32 %[t], %[ho], 229376\n\tbuffer_load_dwordx4 %[so], s[48:51], %[t] offen lds\n\ts_add_u32 m0, %[stg], 15360\n\t" \
          "s_add_u32 %[t], %[ho], 245760\n\tbuffer_load_dwordx4 %[so], s[48:51], %[t] offen lds\n\ts_sub_u32 %[n], %[n], 1\n\ts_cmp_eq_u32 %[n], 4\n\ts_cbranch_scc1 Lw_5_%=\n\tLw_1_%=:\n\t" \
          "s_waitcnt vmcnt(16)\n\tds_read_b64_tr_b16 a[32:33], %[ob] offset:16384\n\tds_read_b64_tr_b16 a[34:35], %[ob] offset:16512\n\tds_read_b64_tr_b16 a[36:37], %[ob] offset:24576\n\tds_read_b64_tr_b16 a[38:39], %[ob] offset:24704\n\tds_read_b64_tr_b16 a[40:41], %[ob] offset:16416\n\t" \
          "ds_read_b64_tr_b16 a[42:43], %[ob] offset:16544\n\tds_read_b64_tr_b16 a[44:45], %[ob] offset:24608\n\tds_read_b64_tr_b16 a[46:47], %[ob] offset:24736\n\tds_read_b64_tr_b16 a[48:49], %[ob] offset:17408\n\tds_read_b64_tr_b16 a[50:51], %[ob] offset:17536\n\tds_read_b64_tr_b16 a[52:53], %[ob] offset:25600\n\t" \
          "ds_read_b64_tr_b16 a[54:55], %[ob] offset:25728\n\tds_read_b64_tr_b16 a[56:57], %[ob] offset:17440\n\tds_read_b64_tr_b16 a[58:59], %[ob] offset:17568\n\tds_read_b64_tr_b16 a[60:61], %[ob] offset:25632\n\tds_read_b64_tr_b16 a[62:63], %[ob] offset:25760\n\ts_cmp_gt_u32 %[n], 2\n\t" \
          "s_cselect_b32 s50, s50, 0\n\ts_cmp_gt_u32 %[n], 6\n\ts_cselect_b32 %[ho], 0x3, 0x7\n\ts_lshl_b32 %[ho], %[ho], 20\n\ts_xor_b32 %[ho], %[ho], %[hx]\n\ts_waitcnt lgkmcnt(0)\n\t" \
          "s_add_u32 m0, %[stg], 16384\n\ts_add_u32 %[t], %[ho], 0\n\tbuffer_load_dwordx4 %[so], s[48:51], %[t] offen lds\n\ts_add_u32 m0, %[stg], 17408\n\ts_add_u32 %[t], %[ho], 16384\n\tbuffer_load_dwordx4 %[so], s[48:51], %[t] offen lds\n\t" \
          "s_add_u32 m0, %[stg], 18432\n\ts_add_u32 %[t], %[ho], 32768\n\tbuffer_load_dwordx4 %[so], s[48:51], %[t] offen lds\n\ts_add_u32 m0, %[stg], 19456\n\ts_add_u32 %[t], %[ho], 49152\n\tbuffer_load_dwordx4 %[so], s[48:51], %[t] offen lds\n\t" \
          "s_add_u32 m0, %[stg], 20480\n\ts_add_u32 %[t], %[ho], 65536\n\tbuffer_load_dwordx4 %[so], s[48:51], %[t] offen lds\n\ts_add_u32 m0, %[stg], 21504\n\ts_add_u32 %[t], %[ho], 81920\n\tbuffer_load_dwordx4 %[so], s[48:51], %[t] offen lds\n\t" \
          "s_add_u32 m0, %[stg], 22528\n\ts_add_u32 %[t], %[ho], 98304\n\tbuffer_load_dwordx4 %[so], s[48:51], %[t] offen lds\n\ts_add_u32 m0, %[stg], 23552\n\ts_add_u32 %[t], %[ho], 114688\n\tbuffer_load_dwordx4 %[so], s[48:51], %[t] offen lds\n\t" \
          "s_add_u32 m0, %[stg], 24576\n\ts_add_u32 %[t], %[ho], 131072\n\tbuffer_load_dwordx4 %[so], s[48:51], %[t] offen lds\n\ts_add_u32 m0, %[stg], 25600\n\ts_add_u32 %[t], %[ho], 147456\n\tbuffer_load_dwordx4 %[so], s[48:51], %[t] offen lds\n\t" \
          "s_add_u32 m0, %[stg], 26624\n\ts_add_u32 %[t], %[ho], 163840\n\tbuffer_load_dwordx4 %[so], s[48:51], %[t] offen lds\n\ts_add_u32 m0, %[stg], 27648\n\ts_add_u32 %[t], %[ho], 180224\n\tbuffer_load_dwordx4 %[so], s[48:51], %[t] offen lds\n\t" \
          "s_add_u32 m0, %[stg], 28672\n\ts_add_u32 %[t], %[ho], 196608\n\tbuffer_load_dwordx4 %[so], s[48:51], %[t] offen lds\n\ts_add_u32 m0, %[stg], 29696\n\ts_add_u32 %[t], %[ho], 212992\n\tbuffer_load_dwordx4 %[so], s[48:51], %[t] offen lds\n\t" \
          "s_add_u32 m0, %[stg], 30720\n\ts_add_u32 %[t], %[ho], 229376\n\tbuffer_load_dwordx4 %[so], s[48:51], %[t] offen lds\n\ts_add_u32 m0, %[stg], 31744\n\ts_add_u32 %[t], %[ho], 245760\n\tbuffer_load_dwordx4 %[so], s[48:51], %[t] offen lds\n\t" \
          "s_sub_u32 %[n], %[n], 1\n\ts_cmp_eq_u32 %[n], 4\n\ts_cbranch_scc1 Lw_6_%=\n\tLw_2_%=:\n\ts_waitcnt vmcnt(16)\n\tds_read_b64_tr_b16 a[64:65], %[ob] offset:0\n\t" \
          "ds_read_b64_tr_b16 a[66:67], %[ob] offset:128\n\tds_read_b64_tr_b16 a[68:69], %[ob] offset:8192\n\tds_read_b64_tr_b16 a[70:71], %[ob] offset:8320\n\tds_read_b64_tr_b16 a[72:73], %[ob] offset:32\n\tds_read_b64_tr_b16 a[74:75], %[ob] offset:160\n\tds_read_b64_tr_b16 a[76:77], %[ob] offset:8224\n\t" \
          "ds_read_b64_tr_b16 a[78:79], %[ob] offset:8352\n\tds_read_b64_tr_b16 a[80:81], %[ob] offset:1024\n\tds_read_b64_tr_b16 a[82:83], %[ob] offset:1152\n\tds_read_b64_tr_b16 a[84:85], %[ob] offset:9216\n\tds_read_b64_tr_b16 a[86:87], %[ob] offset:9344\n\tds_read_b64_tr_b16 a[88:89], %[ob] offset:1056\n\t" \
          "ds_read_b64_tr_b16 a[90:91], %[ob] offset:1184\n\tds_read_b64_tr_b16 a[92:93], %[ob] offset:9248\n\tds_read_b64_tr_b16 a[94:95], %[ob] offset:9376\n\ts_cmp_gt_u32 %[n], 2\n\ts_cselect_b32 s50, s50, 0\n\ts_cmp_gt_u32 %[n], 6\n\t" \
          "s_cselect_b32 %[ho], 0x0, 0x4\n\ts_lshl_b32 %[ho], %[ho], 20\n\ts_xor_b32 %[ho], %[ho], %[hx]\n\ts_waitcnt lgkmcnt(0)\n\ts_add_u32 m0, %[stg], 0\n\ts_add_u32 %[t], %[ho], 0\n\t" \
          "buffer_load_dwordx4 %[so], s[48:51], %[t] offen lds\n\ts_add_u32 m0, %[stg], 1024\n\ts_add_u32 %[t], %[ho], 16384\n\tbuffer_load_dwordx4 %[so], s[48:51], %[t] offen lds\n\ts_add_u32 m0, %[stg], 2048\n\ts_add_u32 %[t], %[ho], 32768\n\t" \
          "buffer_load_dwordx4 %[so], s[48:51], %[t] offen lds\n\ts_add_u32 m0, %[stg], 3072\n\ts_add_u32 %[t], %[ho], 49152\n\tbuffer_load_dwordx4 %[so], s[48:51], %[t] offen lds\n\ts_add_u32 m0, %[stg], 4096\n\ts_add_u32 %[t], %[ho], 65536\n\t" \
          "buffer_load_dwordx4 %[so], s[48:51], %[t] offen lds\n\ts_add_u32 m0, %[stg], 5120\n\ts_add_u32 %[t], %[ho], 81920\n\tbuffer_load_dwordx4 %[so], s[48:51], %[t] offen lds\n\ts_add_u32 m0, %[stg], 6144\n\ts_add_u32 %[t], %[ho], 98304\n\t" \
          "buffer_load_dwordx4 %[so], s[48:51], %[t] offen lds\n\ts_add_u32 m0, %[stg], 7168\n\ts_add_u32 %[t], %[ho], 114688\n\tbuffer_load_dwordx4 %[so], s[48:51], %[t] offen lds\n\ts_add_u32 m0, %[stg], 8192\n\ts_add_u32 %[t], %[ho], 131072\n\t" \
          "buffer_load_dwordx4 %[so], s[48:51], %[t] offen lds\n\ts_add_u32 m0, %[stg], 9216\n\ts_add_u32 %[t], %[ho], 147456\n\tbuffer_load_dwordx4 %[so], s[48:51], %[t] offen lds\n\ts_add_u32 m0, %[stg], 10240\n\ts_add_u32 %[t], %[ho], 163840\n\t" \
          "buffer_load_dwordx4 %[so], s[48:51], %[t] offen lds\n\ts_add_u32 m0, %[stg], 11264\n\ts_add_u32 %[t], %[ho], 180224\n\tbuffer_load_dwordx4 %[so], s[48:51], %[t] offen lds\n\ts_add_u32 m0, %[stg], 12288\n\ts_add_u32 %[t], %[ho], 196608\n\t" \
          "buffer_load_dwordx4 %[so], s[48:51], %[t] offen lds\n\ts_add_u32 m0, %[stg], 13312\n\ts_add_u32 %[t], %[ho], 212992\n\tbuffer_load_dwordx4 %[so], s[48:51], %[t] offen lds\n\ts_add_u32 m0, %[stg], 14336\n\ts_add_u32 %[t], %[ho], 229376\n\t" \
          "buffer_load_dwordx4 %[so], s[48:51], %[t] offen lds\n\ts_add_u32 m0, %[stg], 15360\n\ts_add_u32 %[t], %[ho], 245760\n\tbuffer_load_dwordx4 %[so], s[48:51], %[t] offen lds\n\ts_sub_u32 %[n], %[n], 1\n\ts_cmp_eq_u32 %[n], 4\n\t" \
          "s_cbranch_scc1 Lw_7_%=\n\tLw_3_%=:\n\ts_waitcnt vmcnt(16)\n\tds_read_b64_tr_b16 a[96:97], %[ob] offset:16384\n\tds_read_b64_tr_b16 a[98:99], %[ob] offset:16512\n\tds_read_b64_tr_b16 a[100:101], %[ob] offset:24576\n\t" \
          "ds_read_b64_tr_b16 a[102:103], %[ob] offset:24704\n\tds_read_b64_tr_b16 a[104:105], %[ob] offset:16416\n\tds_read_b64_tr_b16 a[106:107], %[ob] offset:16544\n\tds_read_b64_tr_b16 a[108:109], %[ob] offset:24608\n\tds_read_b64_tr_b16 a[110:111], %[ob] offset:24736\n\tds_read_b64_tr_b16 a[112:113], %[ob] offset:17408\n\t" \
          "ds_read_b64_tr_b16 a[114:115], %[ob] offset:17536\n\tds_read_b64_tr_b16 a[116:117], %[ob] offset:25600\n\tds_read_b64_tr_b16 a[118:119], %[ob] offset:25728\n\tds_read_b64_tr_b16 a[120:121], %[ob] offset:17440\n\tds_read_b64_tr_b16 a[122:123], %[ob] offset:17568\n\tds_read_b64_tr_b16 a[124:125], %[ob] offset:25632\n\t" \
          "ds_read_b64_tr_b16 a[126:127], %[ob] offset:25760\n\ts_cmp_gt_u32 %[n], 2\n\ts_cselect_b32 s50, s50, 0\n\ts_cmp_gt_u32 %[n], 6\n\ts_cselect_b32 %[ho], 0x1, 0x5\n\ts_lshl_b32 %[ho], %[ho], 20\n\t" \
          "s_xor_b32 %[ho], %[ho], %[hx]\n\ts_waitcnt lgkmcnt(0)\n\ts_add_u32 m0, %[stg], 16384\n\ts_add_u32 %[t], %[ho], 0\n\tbuffer_load_dwordx4 %[so], s[48:51], %[t] offen lds\n\ts_add_u32 m0, %[stg], 17408\n\t" \
          "s_add_u32 %[t], %[ho], 16384\n\tbuffer_load_dwordx4 %[so], s[48:51], %[t] offen lds\n\ts_add_u32 m0, %[stg], 18432\n\ts_add_u32 %[t], %[ho], 32768\n\tbuffer_load_dwordx4 %[so], s[48:51], %[t] offen lds\n\ts_add_u32 m0, %[stg], 19456\n\t" \
          "s_add_u32 %[t], %[ho], 49152\n\tbuffer_load_dwordx4 %[so], s[48:51], %[t] offen lds\n\ts_add_u32 m0, %[stg], 20480\n\ts_add_u32 %[t], %[ho], 65536\n\tbuffer_load_dwordx4 %[so], s[48:51], %[t] offen lds\n\ts_add_u32 m0, %[stg], 21504\n\t" \
          "s_add_u32 %[t], %[ho], 81920\n\tbuffer_load_dwordx4 %[so], s[48:51], %[t] offen lds\n\ts_add_u32 m0, %[stg], 22528\n\ts_add_u32 %[t], %[ho], 98304\n\tbuffer_load_dwordx4 %[so], s[48:51], %[t] offen lds\n\ts_add_u32 m0, %[stg], 23552\n\t" \
          "s_add_u32 %[t], %[ho], 114688\n\tbuffer_load_dwordx4 %[so], s[48:51], %[t] offen lds\n\ts_add_u32 m0, %[stg], 24576\n\ts_add_u32 %[t], %[ho], 131072\n\tbuffer_load_dwordx4 %[so], s[48:51], %[t] offen lds\n\ts_add_u32 m0, %[stg], 25600\n\t" \
          "s_add_u32 %[t], %[ho], 147456\n\tbuffer_load_dwordx4 %[so], s[48:51], %[t] offen lds\n\ts_add_u32 m0, %[stg], 26624\n\ts_add_u32 %[t], %[ho], 163840\n\tbuffer_load_dwordx4 %[so], s[48:51], %[t] offen lds\n\ts_add_u32 m0, %[stg], 27648\n\t" \
          "s_add_u32 %[t], %[ho], 180224\n\tbuffer_load_dwordx4 %[so], s[48:51], %[t] offen lds\n\ts_add_u32 m0, %[stg], 28672\n\ts_add_u32 %[t], %[ho], 196608\n\tbuffer_load_dwordx4 %[so], s[48:51], %[t] offen lds\n\ts_add_u32 m0, %[stg], 29696\n\t" \
          "s_add_u32 %[t], %[ho], 212992\n\tbuffer_load_dwordx4 %[so], s[48:51], %[t] offen lds\n\ts_add_u32 m0, %[stg], 30720\n\ts_add_u32 %[t], %[ho], 229376\n\tbuffer_load_dwordx4 %[so], s[48:51], %[t] offen lds\n\ts_add_u32 m0, %[stg], 31744\n\t" \
          "s_add_u32 %[t], %[ho], 245760\n\tbuffer_load_dwordx4 %[so], s[48:51], %[t] offen lds\n\ts_sub_u32 %[n], %[n], 1\n\ts_cmp_eq_u32 %[n], 4\n\ts_cbranch_scc1 Lw_4_%=\n\ts_branch Lw_0_%=\n\t" \
          "Lw_4_%=:\n\ts_waitcnt vmcnt(16)\n\tds_read_b64_tr_b16 a[128:129], %[ob] offset:0\n\tds_read_b64_tr_b16 a[130:131], %[ob] offset:128\n\tds_read_b64_tr_b16 a[132:133], %[ob] offset:8192\n\tds_read_b64_tr_b16 a[134:135], %[ob] offset:8320\n\t" \
          "ds_read_b64_tr_b16 a[136:137], %[ob] offset:32\n\tds_read_b64_tr_b16 a[138:139], %[ob] offset:160\n\tds_read_b64_tr_b16 a[140:141], %[ob] offset:8224\n\tds_read_b64_tr_b16 a[142:143], %[ob] offset:8352\n\tds_read_b64_tr_b16 a[144:145], %[ob] offset:1024\n\tds_read_b64_tr_b16 a[146:147], %[ob] offset:1152\n\t" \
          "ds_read_b64_tr_b16 a[148:149], %[ob] offset:9216\n\tds_read_b64_tr_b16 a[150:151], %[ob] offset:9344\n\tds_read_b64_tr_b16 a[152:153], %[ob] offset:1056\n\tds_read_b64_tr_b16 a[154:155], %[ob] offset:1184\n\tds_read_b64_tr_b16 a[156:157], %[ob] offset:9248\n\tds_read_b64_tr_b16 a[158:159], %[ob] offset:9376\n\t" \
          "s_cmp_gt_u32 %[n], 2\n\ts_cselect_b32 s50, s50, 0\n\ts_cmp_gt_u32 %[n], 6\n\ts_cselect_b32 %[ho], 0x2, 0x6\n\ts_lshl_b32 %[ho], %[ho], 20\n\ts_xor_b32 %[ho], %[ho], %[hx]\n\t" \
          "s_waitcnt lgkmcnt(0)\n\ts_add_u32 m0, %[stg], 0\n\ts_add_u32 %[t], %[ho], 0\n\tbuffer_load_dwordx4 %[so], s[48:51], %[t] offen lds\n\ts_add_u32 m0, %[stg], 1024\n\ts_add_u32 %[t], %[ho], 16384\n\t" \
          "buffer_load_dwordx4 %[so], s[48:51], %[t] offen lds\n\ts_add_u32 m0, %[stg], 2048\n\ts_add_u32 %[t], %[ho], 32768\n\tbuffer_load_dwordx4 %[so], s[48:51], %[t] offen lds\n\ts_add_u32 m0, %[stg], 3072\n\ts_add_u32 %[t], %[ho], 49152\n\t" \
          "buffer_load_dwordx4 %[so], s[48:51], %[t] offen lds\n\ts_add_u32 m0, %[stg], 4096\n\ts_add_u32 %[t], %[ho], 65536\n\tbuffer_load_dwordx4 %[so], s[48:51], %[t] offen lds\n\ts_add_u32 m0, %[stg], 5120\n\ts_add_u32 %[t], %[ho], 81920\n\t" \
          "buffer_load_dwordx4 %[so], s[48:51], %[t] offen lds\n\ts_add_u32 m0, %[stg], 6144\n\ts_add_u32 %[t], %[ho], 98304\n\tbuffer_load_dwordx4 %[so], s[48:51], %[t] offen lds\n\ts_add_u32 m0, %[stg], 7168\n\ts_add_u32 %[t], %[ho], 114688\n\t" \
          "buffer_load_dwordx4 %[so], s[48:51], %[t] offen lds\n\ts_add_u32 m0, %[stg], 8192\n\ts_add_u32 %[t], %[ho], 131072\n\tbuffer_load_dwordx4 %[so], s[48:51], %[t] offen lds\n\ts_add_u32 m0, %[stg], 9216\n\ts_add_u32 %[t], %[ho], 147456\n\t" \
          "buffer_load_dwordx4 %[so], s[48:51], %[t] offen lds\n\ts_add_u32 m0, %[stg], 10240\n\ts_add_u32 %[t], %[ho], 163840\n\tbuffer_load_dwordx4 %[so], s[48:51], %[t] offen lds\n\ts_add_u32 m0, %[stg], 11264\n\ts_add_u32 %[t], %[ho], 180224\n\t" \
          "buffer_load_dwordx4 %[so], s[48:51], %[t] offen lds\n\ts_add_u32 m0, %[stg], 12288\n\ts_add_u32 %[t], %[ho], 196608\n\tbuffer_load_dwordx4 %[so], s[48:51], %[t] offen lds\n\ts_add_u32 m0, %[stg], 13312\n\ts_add_u32 %[t], %[ho], 212992\n\t" \
          "buffer_load_dwordx4 %[so], s[48:51], %[t] offen lds\n\ts_add_u32 m0, %[stg], 14336\n\ts_add_u32 %[t], %[ho], 229376\n\tbuffer_load_dwordx4 %[so], s[48:51], %[t] offen lds\n\ts_add_u32 m0, %[stg], 15360\n\ts_add_u32 %[t], %[ho], 245760\n\t" \
          "buffer_load_dwordx4 %[so], s[48:51], %[t] offen lds\n\ts_sub_u32 %[n], %[n], 1\n\ts_cmp_eq_u32 %[n], 0\n\ts_cbranch_scc1 Lw_end%=\n\tLw_5_%=:\n\ts_waitcnt vmcnt(16)\n\t" \
          "ds_read_b64_tr_b16 a[160:161], %[ob] offset:16384\n\tds_read_b64_tr_b16 a[162:163], %[ob] offset:16512\n\tds_read_b64_tr_b16 a[164:165], %[ob] offset:24576\n\tds_read_b64_tr_b16 a[166:167], %[ob] offset:24704\n\tds_read_b64_tr_b16 a[168:169], %[ob] offset:16416\n\tds_read_b64_tr_b16 a[170:171], %[ob] offset:16544\n\t" \
          "ds_read_b64_tr_b16 a[172:173], %[ob] offset:24608\n\tds_read_b64_tr_b16 a[174:175], %[ob] offset:24736\n\tds_read_b64_tr_b16 a[176:177], %[ob] offset:17408\n\tds_read_b64_tr_b16 a[178:179], %[ob] offset:17536\n\tds_read_b64_tr_b16 a[180:181], %[ob] offset:25600\n\tds_read_b64_tr_b16 a[182:183], %[ob] offset:25728\n\t" \
          "ds_read_b64_tr_b16 a[184:185], %[ob] offset:17440\n\tds_read_b64_tr_b16 a[186:187], %[ob] offset:17568\n\tds_read_b64_tr_b16 a[188:189], %[ob] offset:25632\n\tds_read_b64_tr_b16 a[190:191], %[ob] offset:25760\n\ts_cmp_gt_u32 %[n], 2\n\ts_cselect_b32 s50, s50, 0\n\t" \
          "s_cmp_gt_u32 %[n], 6\n\ts_cselect_b32 %[ho], 0x3, 0x7\n\ts_lshl_b32 %[ho], %[ho], 20\n\ts_xor_b32 %[ho], %[ho], %[hx]\n\ts_waitcnt lgkmcnt(0)\n\ts_add_u32 m0, %[stg], 16384\n\t" \
          "s_add_u32 %[t], %[ho], 0\n\tbuffer_load_dwordx4 %[so], s[48:51], %[t] offen lds\n\ts_add_u32 m0, %[stg], 17408\n\ts_add_u32 %[t], %[ho], 16384\n\tbuffer_load_dwordx4 %[so], s[48:51], %[t] offen lds\n\ts_add_u32 m0, %[stg], 18432\n\t" \
          "s_add_u32 %[t], %[ho], 32768\n\tbuffer_load_dwordx4 %[so], s[48:51], %[t] offen lds\n\ts_add_u32 m0, %[stg], 19456\n\ts_add_u32 %[t], %[ho], 49152\n\tbuffer_load_dwordx4 %[so], s[48:51], %[t] offen lds\n\ts_add_u32 m0, %[stg], 20480\n\t" \
          "s_add_u32 %[t], %[ho], 65536\n\tbuffer_load_dwordx4 %[so], s[48:51], %[t] offen lds\n\ts_add_u32 m0, %[stg], 21504\n\ts_add_u32 %[t], %[ho], 81920\n\tbuffer_load_dwordx4 %[so], s[48:51], %[t] offen lds\n\ts_add_u32 m0, %[stg], 22528\n\t" \
          "s_add_u32 %[t], %[ho], 98304\n\tbuffer_load_dwordx4 %[so], s[48:51], %[t] offen lds\n\ts_add_u32 m0, %[stg], 23552\n\ts_add_u32 %[t], %[ho], 114688\n\tbuffer_load_dwordx4 %[so], s[48:51], %[t] offen lds\n\ts_add_u32 m0, %[stg], 24576\n\t" \
          "s_add_u32 %[t], %[ho], 131072\n\tbuffer_load_dwordx4 %[so], s[48:51], %[t] offen lds\n\ts_add_u32 m0, %[stg], 25600\n\ts_add_u32 %[t], %[ho], 147456\n\tbuffer_load_dwordx4 %[so], s[48:51], %[t] offen lds\n\ts_add_u32 m0, %[stg], 26624\n\t" \
          "s_add_u32 %[t], %[ho], 163840\n\tbuffer_load_dwordx4 %[so], s[48:51], %[t] offen lds\n\ts_add_u32 m0, %[stg], 27648\n\ts_add_u32 %[t], %[ho], 180224\n\tbuffer_load_dwordx4 %[so], s[48:51], %[t] offen lds\n\ts_add_u32 m0, %[stg], 28672\n\t" \
          "s_add_u32 %[t], %[ho], 196608\n\tbuffer_load_dwordx4 %[so], s[48:51], %[t] offen lds\n\ts_add_u32 m0, %[stg], 29696\n\ts_add_u32 %[t], %[ho], 212992\n\tbuffer_load_dwordx4 %[so], s[48:51], %[t] offen lds\n\ts_add_u32 m0, %[stg], 30720\n\t" \
          "s_add_u32 %[t], %[ho], 229376\n\tbuffer_load_dwordx4 %[so], s[48:51], %[t] offen lds\n\ts_add_u32 m0, %[stg], 31744\n\ts_add_u32 %[t], %[ho], 245760\n\tbuffer_load_dwordx4 %[so], s[48:51], %[t] offen lds\n\ts_sub_u32 %[n], %[n], 1\n\t" \
          "s_cmp_eq_u32 %[n], 0\n\ts_cbranch_scc1 Lw_end%=\n\tLw_6_%=:\n\ts_waitcnt vmcnt(16)\n\tbuffer_load_dwordx4 %[rec], %[ro], %[rr], 0 offen sc1\n\tds_read_b64_tr_b16 a[192:193], %[ob] offset:0\n\t" \
          "ds_read_b64_tr_b16 a[194:195], %[ob] offset:128\n\tds_read_b64_tr_b16 a[196:197], %[ob] offset:8192\n\tds_read_b64_tr_b16 a[198:199], %[ob] offset:8320\n\tds_read_b64_tr_b16 a[200:201], %[ob] offset:32\n\tds_read_b64_tr_b16 a[202:203], %[ob] offset:160\n\tds_read_b64_tr_b16 a[204:205], %[ob] offset:8224\n\t" \
          "ds_read_b64_tr_b16 a[206:207], %[ob] offset:8352\n\tds_read_b64_tr_b16 a[208:209], %[ob] offset:1024\n\tds_read_b64_tr_b16 a[210:211], %[ob] offset:1152\n\tds_read_b64_tr_b16 a[212:213], %[ob] offset:9216\n\tds_read_b64_tr_b16 a[214:215], %[ob] offset:9344\n\tds_read_b64_tr_b16 a[216:217], %[ob] offset:1056\n\t" \
          "ds_read_b64_tr_b16 a[218:219], %[ob] offset:1184\n\tds_read_b64_tr_b16 a[220:221], %[ob] offset:9248\n\tds_read_b64_tr_b16 a[222:223], %[ob] offset:9376\n\ts_cmp_gt_u32 %[n], 2\n\ts_cselect_b32 s50, s50, 0\n\ts_cmp_gt_u32 %[n], 6\n\t" \
          "s_cselect_b32 %[ho], 0x0, 0x4\n\ts_lshl_b32 %[ho], %[ho], 20\n\ts_xor_b32 %[ho], %[ho], %[hx]\n\ts_waitcnt lgkmcnt(0)\n\ts_add_u32 m0, %[stg], 0\n\ts_add_u32 %[t], %[ho], 0\n\t" \
          "buffer_load_dwordx4 %[so], s[48:51], %[t] offen lds\n\ts_add_u32 m0, %[stg], 1024\n\ts_add_u32 %[t], %[ho], 16384\n\tbuffer_load_dwordx4 %[so], s[48:51], %[t] offen lds\n\ts_add_u32 m0, %[stg], 2048\n\ts_add_u32 %[t], %[ho], 32768\n\t" \
          "buffer_load_dwordx4 %[so], s[48:51], %[t] offen lds\n\ts_add_u32 m0, %[stg], 3072\n\ts_add_u32 %[t], %[ho], 49152\n\tbuffer_load_dwordx4 %[so], s[48:51], %[t] offen lds\n\ts_add_u32 m0, %[stg], 4096\n\ts_add_u32 %[t], %[ho], 65536\n\t" \
          "buffer_load_dwordx4 %[so], s[48:51], %[t] offen lds\n\ts_add_u32 m0, %[stg], 5120\n\ts_add_u32 %[t], %[ho], 81920\n\tbuffer_load_dwordx4 %[so], s[48:51], %[t] offen lds\n\ts_add_u32 m0, %[stg], 6144\n\ts_add_u32 %[t], %[ho], 98304\n\t" \
          "buffer_load_dwordx4 %[so], s[48:51], %[t] offen lds\n\ts_add_u32 m0, %[stg], 7168\n\ts_add_u32 %[t], %[ho], 114688\n\tbuffer_load_dwordx4 %[so], s[48:51], %[t] offen lds\n\ts_add_u32 m0, %[stg], 8192\n\ts_add_u32 %[t], %[ho], 131072\n\t" \
          "buffer_load_dwordx4 %[so], s[48:51], %[t] offen lds\n\ts_add_u32 m0, %[stg], 9216\n\ts_add_u32 %[t], %[ho], 147456\n\tbuffer_load_dwordx4 %[so], s[48:51], %[t] offen lds\n\ts_add_u32 m0, %[stg], 10240\n\ts_add_u32 %[t], %[ho], 163840\n\t" \
          "buffer_load_dwordx4 %[so], s[48:51], %[t] offen lds\n\ts_add_u32 m0, %[stg], 11264\n\ts_add_u32 %[t], %[ho], 180224\n\tbuffer_load_dwordx4 %[so], s[48:51], %[t] offen lds\n\ts_add_u32 m0, %[stg], 12288\n\ts_add_u32 %[t], %[ho], 196608\n\t" \
          "buffer_load_dwordx4 %[so], s[48:51], %[t] offen lds\n\ts_add_u32 m0, %[stg], 13312\n\ts_add_u32 %[t], %[ho], 212992\n\tbuffer_load_dwordx4 %[so], s[48:51], %[t] offen lds\n\ts_add_u32 m0, %[stg], 14336\n\ts_add_u32 %[t], %[ho], 229376\n\t" \
          "buffer_load_dwordx4 %[so], s[48:51], %[t] offen lds\n\ts_add_u32 m0, %[stg], 15360\n\ts_add_u32 %[t], %[ho], 245760\n\tbuffer_load_dwordx4 %[so], s[48:51], %[t] offen lds\n\ts_sub_u32 %[n], %[n], 1\n\ts_cmp_eq_u32 %[n], 0\n\t" \
          "s_cbranch_scc1 Lw_end%=\n\tLw_7_%=:\n\ts_waitcnt vmcnt(16)\n\tds_read_b64_tr_b16 a[224:225], %[ob] offset:16384\n\tds_read_b64_tr_b16 a[226:227], %[ob] offset:16512\n\tds_read_b64_tr_b16 a[228:229], %[ob] offset:24576\n\t" \
          "ds_read_b64_tr_b16 a[230:231], %[ob] offset:24704\n\tds_read_b64_tr_b16 a[232:233], %[ob] offset:16416\n\tds_read_b64_tr_b16 a[234:235], %[ob] offset:16544\n\tds_read_b64_tr_b16 a[236:237], %[ob] offset:24608\n\tds_read_b64_tr_b16 a[238:239], %[ob] offset:24736\n\tds_read_b64_tr_b16 a[240:241], %[ob] offset:17408\n\t" \
          "ds_read_b64_tr_b16 a[242:243], %[ob] offset:17536\n\tds_read_b64_tr_b16 a[244:245], %[ob] offset:25600\n\tds_read_b64_tr_b16 a[246:247], %[ob] offset:25728\n\tds_read_b64_tr_b16 a[248:249], %[ob] offset:17440\n\tds_read_b64_tr_b16 a[250:251], %[ob] offset:17568\n\tds_read_b64_tr_b16 a[252:253], %[ob] offset:25632\n\t" \
          "ds_read_b64_tr_b16 a[254:255], %[ob] offset:25760\n\ts_cmp_gt_u32 %[n], 2\n\ts_cselect_b32 s50, s50, 0\n\ts_cmp_gt_u32 %[n], 6\n\ts_cselect_b32 %[ho], 0x1, 0x5\n\ts_lshl_b32 %[ho], %[ho], 20\n\t" \
          "s_xor_b32 %[ho], %[ho], %[hx]\n\ts_waitcnt lgkmcnt(0)\n\ts_add_u32 m0, %[stg], 16384\n\ts_add_u32 %[t], %[ho], 0\n\tbuffer_load_dwordx4 %[so], s[48:51], %[t] offen lds\n\ts_add_u32 m0, %[stg], 17408\n\t" \
          "s_add_u32 %[t], %[ho], 16384\n\tbuffer_load_dwordx4 %[so], s[48:51], %[t] offen lds\n\ts_add_u32 m0, %[stg], 18432\n\ts_add_u32 %[t], %[ho], 32768\n\tbuffer_load_dwordx4 %[so], s[48:51], %[t] offen lds\n\ts_add_u32 m0, %[stg], 19456\n\t" \
          "s_add_u32 %[t], %[ho], 49152\n\tbuffer_load_dwordx4 %[so], s[48:51], %[t] offen lds\n\ts_add_u32 m0, %[stg], 20480\n\ts_add_u32 %[t], %[ho], 65536\n\tbuffer_load_dwordx4 %[so], s[48:51], %[t] offen lds\n\ts_add_u32 m0, %[stg], 21504\n\t" \
          "s_add_u32 %[t], %[ho], 81920\n\tbuffer_load_dwordx4 %[so], s[48:51], %[t] offen lds\n\ts_add_u32 m0, %[stg], 22528\n\ts_add_u32 %[t], %[ho], 98304\n\tbuffer_load_dwordx4 %[so], s[48:51], %[t] offen lds\n\ts_add_u32 m0, %[stg], 23552\n\t" \
          "s_add_u32 %[t], %[ho], 114688\n\tbuffer_load_dwordx4 %[so], s[48:51], %[t] offen lds\n\ts_add_u32 m0, %[stg], 24576\n\ts_add_u32 %[t], %[ho], 131072\n\tbuffer_load_dwordx4 %[so], s[48:51], %[t] offen lds\n\ts_add_u32 m0, %[stg], 25600\n\t" \
          "s_add_u32 %[t], %[ho], 147456\n\tbuffer_load_dwordx4 %[so], s[48:51], %[t] offen lds\n\ts_add_u32 m0, %[stg], 26624\n\ts_add_u32 %[t], %[ho], 163840\n\tbuffer_load_dwordx4 %[so], s[48:51], %[t] offen lds\n\ts_add_u32 m0, %[stg], 27648\n\t" \
          "s_add_u32 %[t], %[ho], 180224\n\tbuffer_load_dwordx4 %[so], s[48:51], %[t] offen lds\n\ts_add_u32 m0, %[stg], 28672\n\ts_add_u32 %[t], %[ho], 196608\n\tbuffer_load_dwordx4 %[so], s[48:51], %[t] offen lds\n\ts_add_u32 m0, %[stg], 29696\n\t" \
          "s_add_u32 %[t], %[ho], 212992\n\tbuffer_load_dwordx4 %[so], s[48:51], %[t] offen lds\n\ts_add_u32 m0, %[stg], 30720\n\ts_add_u32 %[t], %[ho], 229376\n\tbuffer_load_dwordx4 %[so], s[48:51], %[t] offen lds\n\ts_add_u32 m0, %[stg], 31744\n\t" \
          "s_add_u32 %[t], %[ho], 245760\n\tbuffer_load_dwordx4 %[so], s[48:51], %[t] offen lds\n\ts_sub_u32 %[n], %[n], 1\n\ts_cmp_eq_u32 %[n], 0\n\ts_cbranch_scc1 Lw_end%=\n\ts_branch Lw_4_%=\n\t" \
          "Lw_end%=:\n\ts_waitcnt vmcnt(0)\n\ts_barrier\n\t"
#define VO_CLOB "v176", "v177", "v178", "v179", "v180", "v181", "v182", "v183", "v184", "v185", "v186", "v187", "v188", "v189", "v190", "v191", "v192", "v193", "v194", "v195", "v196", "v197", "v198", "v199", "v200", "v201", "v202", "v203", "v204", "v205", "v206", "v207", "v208", "v209", "v210", "v211", "v212", "v213", "v214", "v215", "v216", "v217", "v218", "v219", "v220", "v221", "v222", "v223", "v224", "v225", "v226", "v227", "v228", "v229", "v230", "v231", "v232", "v233", "v234", "v235", "v236", "v237", "v238", "v239", "v240", "v241", "v242", "v243", "v244", "v245", "v246", "v247", "v248", "v249", "v250", "v251", "v252", "v253", "v254", "v255", "s48", "s49", "s50", "s51", "s52", "s53"
__global__ void __launch_bounds__(256, 1) k_fused(FParams P) {
  __shared__ __attribute__((aligned(16))) char Lraw[LDS_ALL];
  unsigned long long pc0;
  asm volatile("s_getpc_b64 %0\nLfused_pc0:" : "=s"(pc0));
  const int w = __builtin_amdgcn_readfirstlane(threadIdx.x >> 6);
  unsigned cz; unsigned long long tag;
  {
    int clen; asm volatile("s_mov_b32 %0, Lfused_end-Lfused_pc0" : "=s"(clen));
    const int o = ((blockIdx.x >> 3) * 4 + w) * 1024 + (threadIdx.x & 7) * 128;
    asm volatile("s_nop 4\n\tbuffer_load_dword %0, %1, %2, 0 offen" : "=v"(cz) : "v"(o), "s"(__builtin_amdgcn_make_buffer_rsrc((void*)pc0, 0, clen, 0x00020000))); }
  if ((blockIdx.x & 7) >= 3) {
    asm volatile("Lfused_worker:");
    worker(P, cz, Lraw, w);
    asm volatile("Lfused_end:");
    return;
  }
  unsigned* tmo = &P.ctl->tmo[0]; unsigned long long* prof = P.prof; (void)prof;
  const int xq = blockIdx.x & 7, slot = blockIdx.x >> 3, nq = xq < 2 ? 12 : 8;
  const int b = slot < nq ? 12 * xq + slot : NQK + 20 * xq + (slot - nq);
  const int alw = b < NQK ? (b % 9 == 0 ? (b / 9) * 4 + w : -1) : (b == 33 ? 16 + w : (b == 95 ? 20 + w : -1));
  const int xr = xq;
  const unsigned long long alx = (unsigned)__builtin_amdgcn_readfirstlane(alw >= 0 ? -1 : 0);
  const __amdgpu_buffer_rsrc_t rs_m = __builtin_amdgcn_make_buffer_rsrc(P.ring, 0, RSIZE, 0x00020000), rs_r = __builtin_amdgcn_make_buffer_rsrc(P.rdy, 0, RDY_SIZE, 0x00020000);
  const __amdgpu_buffer_rsrc_t rs_c = __builtin_amdgcn_make_buffer_rsrc(P.ctl, 0, sizeof(Ctl), 0x00020000);
  v4u rdy;
#define LANE_CONSTS const int lane = opaque_lane(), tid = 64 * w + lane, l15 = lane & 15, lq = lane >> 4, idx = (lane & 1) ? 0xEEEE : 0x4444; \
  const int mem_lane_off = (((lq >> 1) * 2 + (l15 >> 3)) * 16 + (lq & 1) * 8 + (l15 & 7)) * 16 + dbl(w * 4096); \
  const int wrow = 8 * (lq >> 1) + 2 * (lq & 1) + 4 * (l15 >> 3) + 16 * ((l15 >> 2) & 1), wsw = (wrow >> 1) & 3
  unsigned tA = 0;
  if (b < NQK) {
    const int h = b;
    LANE_CONSTS;
    v32u wa[8], wv_[7];
    const unsigned lb0 = (unsigned)(size_t)Lraw;
    const unsigned own = lb0 + w * 40960 + lane * 16;
    {
      const __amdgpu_buffer_rsrc_t rq = __builtin_amdgcn_make_buffer_rsrc((void*)(P.Wq + h * DHA + 64 * (w >> 1)), 0, 0x7fffffff, 0x00020000), rk = __builtin_amdgcn_make_buffer_rsrc((void*)(P.Wk + h * DHA + 64 * (w >> 1)), 0, 0x7fffffff, 0x00020000);
      const int go = (lane >> 3) * HDA * 2 + (((((lane & 7) >> 1) ^ ((lane >> 4) & 3)) << 1) | (lane & 1)) * 16;
      unsigned rd[4];
#pragma unroll
      for (int t = 0; t < 4; ++t) rd[t] = lb0 + w * 40960 + wrow * 128 + (l15 & 3) * 8 + ((t ^ (2 * (~w & 1)) ^ wsw) << 5);
#define QK_NTq ""
#define QK_NTk ""
      int t_;
      asm volatile(QK_LOADER : QK_DEFW, [t] "=&s"(t_), [rec] "=&v"(rdy)
                   : [rd0] "v"(rd[0]), [rd1] "v"(rd[1]), [rd2] "v"(rd[2]), [rd3] "v"(rd[3]), [own] "v"(own), [go] "v"(go), [ro] "v"(lane * 128), [rr] "s"(rs_r), [rq] "s"(rq), [rk] "s"(rk), [lw] "s"(lb0 + w * 40960), [kb] "s"((8 * (w & 1) + 2 * xr + (w >> 1)) * 0x80000) : "memory", "scc");
    }
    asm volatile("" :: "v"(cz));
    const unsigned long long parq = rdy_wait(P, rdy, tag);
    const int rpl = (int)(parq >> 63) * 8192;
    const __amdgpu_buffer_rsrc_t rs_x = __builtin_amdgcn_make_buffer_rsrc(P.qkvA, 0, QSIZE, 0x00020000);
    const int xk = qoff(parq, 16 + (h >> 1)) + (h & 1) * 4096, xv = qoff(parq, 32 + (h >> 1)) + (h & 1) * 4096;
    const __amdgpu_buffer_rsrc_t rs_k = __builtin_amdgcn_make_buffer_rsrc(P.kvm, 0, NSEG * KVSEG, 0x00020000);
#define PF_ADDR const int tz = opaque_lane()
#define KV_SEGS(v) (min(__builtin_amdgcn_readlane(v, 0), __builtin_amdgcn_readlane(v, 1)) >> 1)
#define WAIT_KV(sg) do { for (unsigned spins = 0;; ++spins) { const unsigned v_ = ld_agent(&P.ctl->prg[(tz & 1) * 32 + h][0]); kvok = KV_SEGS(v_); if (kvok > (sg)) break; \
        if (spin_fail(spins, tmo, 3u)) break; __builtin_amdgcn_s_sleep(8); } } while (0)
    int kvok = 0;
#define LOAD_KV(sg) do { const int so_ = (sg) * 2 * QBLK + w * 1024; \
        _Pragma("unroll") for (int q = 0; q < 4; ++q) \
          asm volatile("s_mov_b32 m0, %0\n\ts_nop 4\n\tbuffer_load_dwordx4 %1, %2, %3 offen sc1 lds" :: "s"(lb0 + w * 40960 + QK_KV + q * 1024), "v"((q < 2 ? tz : tz ^ ((tz >> 1) & 8)) * 16), "s"(rs_x), \
                       "s"(so_ + (q & 1) * QBLK + ((q >> 1) ? xv : xk)) : "memory"); } while (0)
    unsigned pz = 0;
#define LD_ASYNC(dst, voff, rs, soff) asm volatile("s_nop 4\n\tbuffer_load_dword %0, %1, %2, %3 offen sc1" : "=v"(dst) : "v"(voff), "s"(rs), "s"(soff) : "memory")
    {
      PF_ADDR;
      WAIT_KV(0);
      LOAD_KV(0);
      LD_ASYNC(pz, kvok > 1 && tz < 32 ? tz * 128 : 0x7ffffff0, rs_x, (2 + (w & 1)) * QBLK + ((w >> 1) ? xv : xk));
    }
    const float QS = 0.08838834764831845f * 1.4426950408889634f;
    const unsigned j0 = lq == (l15 >> 2) && !(l15 & 1) ? 0x3f803f80u : 0u, j1 = lq == (l15 >> 2) && (l15 & 1) ? 0x3f803f80u : 0u;
    const v4u JK = {j0, j1, j0, j1};
    const unsigned vxp = lb0 + w * 40960 + QK_KV + 2048 + ((l15 & 3) * 16 + ((4 * lq + (l15 >> 2)) ^ ((l15 & 1) << 3))) * 16;
    const unsigned p1 = lb0 + (w ^ 1) * 40960 + lane * 16, p2 = lb0 + (w ^ 2) * 40960 + lane * 16, p3 = lb0 + (w ^ 3) * 40960 + lane * 16;
    unsigned pot = 0, kfl = 0, d1p = 140;
    const int mlo = mem_lane_off - dbl(w * 4096) + (w & 1) * 16384 + (w >> 1) * 1024, hoff = dbl(h * 2048) + w * 512 + lane * 4;
    v4u vm = {0u, 0u, 0u, 0u};
    const int alo = lane < 16 ? ALM + lane * 128 : (lane >= 62 ? RECO + (lane - 62) * 4 : 0x7ffffff0);
    for (int s = 0; s < NSEG; ++s) {
      int mb = RBASE(s) + ROFF_M, vb = RBASE(s) + ROFF_V, ob = RBASE(s) + ROFF_O, mb0 = mb + xr * 2048;
      unsigned al, ann, fl; unsigned long long bad, bad1, b2, b3, po_;
      if (__builtin_expect(s == 1, 0)) {
        unsigned recD; unsigned* bw = (unsigned*)(Lraw + QK_EX + 1024);
        if (w == 0) { rec_get(tA, recD, rs_m, mb + RECO, tmo, 8); *bw = tA - (recD & 0xffffu) - (recD >> 16); }
        lds_barrier();
        tA = __builtin_amdgcn_readfirstlane(*bw);
      }
      { int rem = (int)(tA - rtnow());
        if (s == 0 || rem > 20000) tA -= rem; }
      tA = __builtin_amdgcn_readfirstlane(tA); d1p = __builtin_amdgcn_readfirstlane(d1p);
#define QK_STO
      { unsigned cnt; unsigned long long en;
        asm volatile(QK_STEP
          : "=&{v[136:167]}"(wv_[4]), "=&{v[168:199]}"(wv_[5]), "=&{v[200:231]}"(wv_[6]), "+{v[232:235]}"(vm), [m] "+v"(pz), "+v"(kfl), [al] "=&v"(al),
            [bad] "=&s"(bad), [bad1] "=&s"(bad1), [b2] "=&s"(b2), [b3] "=&s"(b3), [po] "=&s"(po_), [cnt] "=&s"(cnt), [en] "=&s"(en), [ann] "=&s"(ann), [fl] "=&s"(fl) QK_STO
          : "{a[0:31]}"(wa[0]), "{a[32:63]}"(wa[1]), "{a[64:95]}"(wa[2]), "{a[96:127]}"(wa[3]), "{a[128:159]}"(wa[4]), "{a[160:191]}"(wa[5]), "{a[192:223]}"(wa[6]), "{a[224:255]}"(wa[7]),
            "{v[8:39]}"(wv_[0]), "{v[40:71]}"(wv_[1]), "{v[72:103]}"(wv_[2]), "{v[104:135]}"(wv_[3]),
            [own] "v"(own), [p1] "v"(p1), [p2] "v"(p2), [p3] "v"(p3), [vxp] "v"(vxp), [ml0] "v"(mlo), [ml1] "v"(mlo ^ 1024), [jk] "v"(JK), [ho] "v"(hoff), [idx] "v"(idx), [alo] "v"(alo),
            [rsm] "s"(rs_m), [rsc] "s"(rs_c), [mb] "s"(mb), [vb] "s"(vb), [ob] "s"(ob), [rb] "s"(RBASE(s + 2) + ROFF_O), [twin] "s"(0xccccccccccccccccull),
            [one] "s"(0x3f803f80u), [qs] "s"(QS), [qs2] "s"((unsigned long long)__float_as_uint(QS)), [hi] "s"(0x1p40f), [lo] "s"(0x1p-40f), [th] "s"(40.f), [t] "s"(tA), [w] "s"(w), [ald0] "s"(d1p + P.sh0),
            [tmo] "s"((int)offsetof(Ctl, tmo)), [mb0] "s"(mb0), [mb1] "s"(mb0 ^ 2048), [mb2] "s"(mb0 ^ 4096), [mb3] "s"(mb0 ^ 6144), [ald] "s"(d1p - P.sh1), [als] "s"(mb + ALM + (alw & 15) * 128), [alx] "s"(alx)
          : "vcc", "scc", "s48", "s49", "s50", "s51", "s52", "s53", "memory"); }
      {
        const v4u Km = {wv_[4][QK_KM], wv_[4][QK_KM + 1], wv_[4][QK_KM + 2], wv_[4][QK_KM + 3]};
        if (__builtin_expect(bad != 0, 0)) {
#pragma unroll 1
          for (unsigned fails = 0; !__all((vm.x > vm.z ? vm.x : vm.z) != 0xffffffffu); ++fails) {
            __builtin_amdgcn_s_sleep(2);
            vm.x = __builtin_amdgcn_raw_buffer_load_b32(rs_m, hoff, vb, 16); vm.z = __builtin_amdgcn_raw_buffer_load_b32(rs_m, hoff, vb + 256, 16);
            if (spin_fail(fails, tmo, 1u)) break;
          }
          float sx[4], sm[2];
#pragma unroll
          for (int e = 0; e < 4; ++e) sx[e] = __uint_as_float(wv_[6][QK_SX + e]);
          sm[0] = __uint_as_float(wv_[4][QK_SM]); sm[1] = __uint_as_float(wv_[4][QK_SM + 1]);
          float mx = fmaxf(fmaxf(sx[0], sx[1]), fmaxf(sx[2], sx[3]));
          mx = fmaxf(mx, __int_as_float(__builtin_amdgcn_update_dpp(0, __float_as_int(mx), 0x4E, 0xf, 0xf, false)));
          mx = xmax32(xmax16(fmaxf(mx, fmaxf(sm[0], sm[1]))));
          if (b2 == 0) mx = 0.f;
          v4u px; px.x = pk2(__builtin_amdgcn_exp2f(sx[0] - mx), __builtin_amdgcn_exp2f(sx[1] - mx)); px.y = pk2(__builtin_amdgcn_exp2f(sx[2] - mx), __builtin_amdgcn_exp2f(sx[3] - mx));
          px.z = __builtin_amdgcn_update_dpp(0, px.x, 0x4E, 0xf, 0xf, false); px.w = __builtin_amdgcn_update_dpp(0, px.y, 0x4E, 0xf, 0xf, false);
          const v2u pm = {pk2(__builtin_amdgcn_exp2f(sm[0] - mx), __builtin_amdgcn_exp2f(sm[1] - mx)), 0u};
          const v4u on = {0x3f803f80u, 0x3f803f80u, 0x3f803f80u, 0x3f803f80u};
          const v4u vx0 = {wv_[6][QK_VX], wv_[6][QK_VX + 1], wv_[6][QK_VX + 2], wv_[6][QK_VX + 3]}, vx1 = {wv_[6][QK_VX + 4], wv_[6][QK_VX + 5], wv_[6][QK_VX + 6], wv_[6][QK_VX + 7]};
          f32x4 c0, c1, ls;
          asm volatile("s_nop 1\n\t"
                       "v_mfma_f32_16x16x32_bf16 %0, %3, %4, 0\n\t"
                       "v_mfma_f32_16x16x32_bf16 %1, %3, %5, 0\n\t"
                       "v_mfma_f32_16x16x32_bf16 %2, %3, %6, 0\n\t"
                       "s_nop 7\n\t"
                       "v_mfma_f32_16x16x16_bf16 %0, %7, v[232:233], %0\n\t"
                       "v_mfma_f32_16x16x16_bf16 %1, %7, v[234:235], %1\n\t"
                       "v_mfma_f32_16x16x16_bf16 %2, %7, v[168:169], %2\n\t"
                       "s_nop 7"
                       : "=&v"(c0), "=&v"(c1), "=&v"(ls) : "v"(px), "v"(vx0), "v"(vx1), "{v[168:171]}"(on), "v"(pm), "{v[232:235]}"(vm));
          const float i0 = __builtin_amdgcn_rcpf(ls[0]), i1 = __builtin_amdgcn_rcpf(ls[1]);
          __builtin_amdgcn_raw_buffer_store_b32(nosent(pk2(c0[0] * i0, c1[0] * i0)), rs_m, hoff, ob, 16);
          __builtin_amdgcn_raw_buffer_store_b32(nosent(pk2(c0[1] * i1, c1[1] * i1)), rs_m, hoff + 256, ob, 16);
          po_ = rtnow();
        }
        SB0;
        const unsigned po = __builtin_amdgcn_readfirstlane((unsigned)po_);
        fl = __builtin_amdgcn_readfirstlane(fl | (bad != 0));
        __builtin_amdgcn_raw_buffer_store_b32(po - tA, rs_m, (h | (w ^ 3) | opaque_lane() | fl) == 0 ? RECO + 128 : 0x7ffffff0, mb, 16);
        asm volatile("s_waitcnt vmcnt(%1)" : "+v"(al) : "i"(3) : "memory");
        pot = po;
      if (s < NSEG - 1) {
        unsigned recD = __builtin_amdgcn_readlane(al, 63);
        tA = __builtin_amdgcn_readlane(al, 62);
        if (s > 0) {
          if (__builtin_expect(tA == 0xffffffffu || recD == 0xffffffffu, 0)) rec_get(tA, recD, rs_m, mb + RECO, tmo);
          d1p = recD & 0xffffu;
          unsigned tB = tA - (recD >> 16);
          al_eval(al, tB);
          if (__builtin_expect((int)(pot + P.sh3 - tB) > 0, 0)) tB = pot + P.sh3;
          tA = tB + (recD >> 16);
        } else tA = pot;
        PF_ADDR;
        kvok = max(kvok, KV_SEGS(kfl));
        if (__builtin_expect(kvok <= s + 1, 0)) WAIT_KV(s + 1);
        LOAD_KV(s + 1);
        LD_ASYNC(kfl, kvok < s + 5 && kvok < NSEG && tz < 2 ? tz * 4096 : 0x7ffffff0, rs_c, (int)offsetof(Ctl, prg) + h * 128);
        const int po2 = s + 2 < kvok && !(tz & 32) ? tz * 128 : 0x7ffffff0;
        LD_ASYNC(pz, po2, rs_x, (2 * (s + 2) + (w & 1)) * QBLK + ((w >> 1) ? xv : xk));
      }
      { const int lz = opaque_lane(), kb = s * KVSEG + h * 4096 + w * 512, rb = RBASE(s + 2);
        __builtin_amdgcn_raw_buffer_store_b128((v4u){nosent(Km.x), nosent(Km.y), nosent(Km.z), nosent(Km.w)}, rs_k, (lz & 2) ? 0x7ffffff0 : (2 * (lz >> 2) + (lz & 1)) * 16, kb, 16);
        __builtin_amdgcn_raw_buffer_store_b32(vm.x, rs_k, lz * 4 + 2048, kb, 16); __builtin_amdgcn_raw_buffer_store_b32(vm.z, rs_k, lz * 4 + 2304, kb, 16);
        if (b % 9 == 0 && w == 0) __builtin_amdgcn_raw_buffer_store_b128((v4u){~0u, ~0u, ~0u, ~0u}, rs_m, lz < 32 ? ROFF_M + ALM + alw * 128 + lz * 16 : (lz == 32 && b == 0 ? ROFF_M + RECO + 128 : 0x7ffffff0), rb, 16); }
      }
    }
    check_in(P, tag, w);
  } else {
    LANE_CONSTS;
    const unsigned c00 = (unsigned)now(), t00 = rtnow();
    const int c = b - NQK, cg = c >> 1, rh = c & 1;
    v32u wa[8], wv_[4];
#define VCH(j) (8 * (w & 1) + 2 * ((j) ^ xq) + (w >> 1))
#define OHEAD(i) (4 * ((i) ^ (2 * xq)) + w)
    {
      const unsigned stg = (unsigned)(size_t)Lraw + w * 32768;
      const unsigned vb = stg + wrow * 128 + (l15 & 3) * 8 + ((w ^ 3 ^ wsw) << 5);
      const __amdgpu_buffer_rsrc_t rv = __builtin_amdgcn_make_buffer_rsrc((void*)(P.Wv + 64 * c), 0, DM * HDA * 2 - 128 * c, 0x00020000);
      int t_, ho, n_;
      asm volatile(VO_LOADER : VO_DEFW, [t] "=&s"(t_), [ho] "=&s"(ho), [n] "=&s"(n_), [rec] "=&v"(rdy)
        : [sv] "v"((lane >> 3) * HDA * 2 + (((((lane & 7) >> 1) ^ ((lane >> 4) & 3)) << 1) | (lane & 1)) * 16), [so] "v"((lane >> 3) * DM * 2 + (lane & 7) * 16), [vb0] "v"(vb), [vb1] "v"(vb ^ 32), [vb2] "v"(vb ^ 64), [vb3] "v"(vb ^ 96),
          [ob] "v"(stg + (32 * (lq >> 1) + 2 * (lq & 1) + 4 * (l15 >> 3) + 16 * ((l15 >> 2) & 1)) * 128 + (cg & 1) * 64 + (l15 & 3) * 8),
          [ro] "v"(lane * 128), [rr] "s"(rs_r), [stg] "s"(stg), [rv] "s"(rv), [vk] "s"(VCH(0) * 0x80000), [wo] "s"(P.Wo + 64 * (cg >> 1)), [wsz] "s"(HDA * DM * 2 - 128 * (cg >> 1)), [hx] "s"(OHEAD(0) * 0x40000), [k0] "s"(c & 3)
        : "s48", "s49", "s50", "s51", "s52", "s53", "scc", "memory");
    }
    asm volatile("" :: "v"(cz));
    const int rpl = (int)(rdy_wait(P, rdy, tag) >> 63) * 8192;
    const unsigned lb0 = (unsigned)(size_t)Lraw;
    unsigned vw[3];
#pragma unroll
    for (int t = 0; t < 3; ++t) vw[t] = lb0 + t * 2064 + ((t ^ w ^ 3) * 64 + lane) * 8;
    const unsigned vr = lb0 + tid * 8, rw = lb0 + 8192 + tid * 8, fa = lb0 + 16384;
    int oo[8];
#pragma unroll
    for (int i = 0; i < 8; ++i) oo[i] = dbl(OHEAD(i) * 2048) + ((lq * 2 + rh) * 16 + l15) * 16;
    int ga[4];
#pragma unroll
    for (int j = 0; j < 4; ++j) ga[j] = mem_lane_off - dbl(w * 4096) + dbl(VCH(j) * 1024);
    const int vo = dbl(c * 1024) + tid * 4, mo = dbl(c * 256) + (32 * (l15 >> 3) + 8 * lq + (l15 & 7)) * 4;
    const bool keeper = (c == 0 && w == 3);
    int d1 = P.dmv * 16, d2 = P.dov * 16, m1 = (P.dq2 + P.im) * 16, m2 = (P.dq + P.im) * 16;
    int LV = 70;
    bool prev_clean = false;
    unsigned recD = 140;
    const float tpc = 16.f * (float)(rtnow() - t00) / (float)((unsigned)now() - c00);
    int e1 = __builtin_amdgcn_readfirstlane((int)(P.dmv * tpc)), e2 = __builtin_amdgcn_readfirstlane((int)(P.dov * tpc));
    d1 = e1 + m1; d2 = e2 + m2;
    if (tid < 32) ((int*)(Lraw + 16384))[tid] = 0;
    const int alk = !keeper ? 0x7ffffff0 : (lane < 24 ? ALM + lane * 128 : (lane == 63 ? RECO + 128 : 0x7ffffff0));
    const int al1 = lane < 16 ? ALM + lane * 128 : 0x7ffffff0;
    const int boot = c == 0 ? P.pad & 255 : P.pad >> 8;
    unsigned tB = 0; int mb = RBASE(0) + ROFF_M, ob = 0;
    for (int s = 0; s < NSEG; ++s) {
      unsigned alr, pm, fw; unsigned long long te; v2u rec;
      const int mbn = RBASE(s) + ROFF_M, d2r = recD >> 16;
      const unsigned r1 = (d1 >> 4) + (s - 1u < 2u ? (P.boot2 >> (8 * s - 8)) & 255 : 0), r2 = (d2 >> 4) + (s - 1u < 2u ? (P.boot3 >> (8 * s - 8)) & 255 : 0);
#define VO_ST0
#define VO_ST1
#define VO_ST3
#define VO_STO
      { unsigned cnt; unsigned long long en;
        asm volatile(VO_BODY : [al] "=&v"(alr), [rec] "=&v"(rec), [te] "=&s"(te), [cnt] "=&s"(cnt), [en] "=&s"(en), [t] "+s"(tB), [ta] "=&s"(tA), [pm] "=&s"(pm), [fw] "=&s"(fw) VO_STO
          : VO_W, [oo0] "v"(oo[0]), [oo1] "v"(oo[1]), [oo2] "v"(oo[2]), [oo3] "v"(oo[3]), [oo4] "v"(oo[4]), [oo5] "v"(oo[5]), [oo6] "v"(oo[6]), [oo7] "v"(oo[7]), [idx] "v"(idx), [rw] "v"(rw), [mo] "v"(mo), [fa] "v"(fa), [l4] "v"(lane * 4), [al1] "v"(al1),
            [ga0] "v"(ga[0]), [ga1] "v"(ga[1]), [ga2] "v"(ga[2]), [ga3] "v"(ga[3]), [alo] "v"(alk), [vw0] "v"(vw[0]), [vw1] "v"(vw[1]), [vw2] "v"(vw[2]), [vr] "v"(vr), [vo] "v"(vo),
            [w] "s"(w), [rsm] "s"(rs_m), [rsc] "s"(rs_c), [ob] "s"(ob), [mb] "s"(mb), [mbn] "s"(mbn), [vb] "s"(RBASE(s) + ROFF_V), [rco] "s"(mbn + RECO), [tmo] "s"((int)offsetof(Ctl, tmo)), [s1] "s"(s),
            [lk] "s"(P.lk1), [als] "s"(mb + ALM + (alw & 31) * 128), [alx] "s"(alx), [dv] "s"(P.dv),
            [d2] "s"(d2r), [mn] "s"(s == 1 ? boot : 10), [shf] "s"(s == 1 ? boot : P.sh4), [r12] "s"(r1 + r2), [rd] "s"(keeper ? r1 | (r2 << 16) : 0u)
          : VO_CLOB, "vcc", "scc", "memory"); }
      const unsigned tv = __builtin_amdgcn_readfirstlane((unsigned)te);
      if (s >= 2 && (w == 1 || w == 2)) {
        const int lz = opaque_lane();
        __builtin_amdgcn_raw_buffer_store_b128((v4u){~0u, ~0u, ~0u, ~0u}, rs_m, w == 1 ? ROFF_V + dbl(c * 1024) + lz * 16 : (lz < 16 ? ROFF_M + dbl(c * 256) + lz * 16 :
                                               (lz >= 32 && alw >= 0 ? ROFF_M + ALM + (alw - 6) * 128 + lz * 16 : (lz == 16 && c == 0 ? ROFF_M + RECO : 0x7ffffff0))), RBASE(s + 2), 16);
      }
      if (c == 2 && tid == 0 && s > 0) __hip_atomic_store(&P.ctl->prog[0], (unsigned)(s - 1), RLX_AGENT);
      if (keeper && s > 0) {
        const unsigned long long f = __ballot((alr >> 16) == 1u);
        const bool kclean = __builtin_amdgcn_readfirstlane(fw) != s, okA = (f & 0xffff) == 0, okB = kclean && ((f >> 16) & 255) == 0;
        const int kx1 = __builtin_amdgcn_readlane(alr, 63), kx2 = __builtin_amdgcn_readfirstlane(pm - tB);
        if (s > 1) {
          const int gain = s < 5 ? P.gain0 : P.gain;
          if (okA) {
            if (kx1 != -1) { int x = kx1 * 16 - e1; x = x > 320 ? 320 : x; e1 += (x * gain) >> 4; }
            if (!okB) m1 = m1 < 60 * 16 ? m1 + P.inc : m1; else if (m1 > P.dq2 * 16) m1 -= P.dec;
            if (m2 > P.dq * 16) m2 -= P.dec;
          } else if (prev_clean) m2 = m2 < 60 * 16 ? m2 + P.inc : m2;
          if (kclean) { int x = kx2 * 16 - e2; x = x > 320 ? 320 : x; e2 += (x * gain) >> 4; }
          d1 = e1 + m1; d2 = e2 + m2;
        }
        prev_clean = okB;
      }
      tB = tv; recD = 0;
      if (s > 0) {
        unsigned recA = __builtin_amdgcn_readfirstlane(rec.x); recD = __builtin_amdgcn_readfirstlane(rec.y);
        if (__builtin_expect(recA == 0xffffffffu || recD == 0xffffffffu, 0)) rec_get(recA, recD, rs_m, mbn + RECO, tmo);
        tB = recA - (recD >> 16);
        if ((int)(tB - tv) > 20000) tB = tv;
      }
      tB = __builtin_amdgcn_readfirstlane(tB); recD = __builtin_amdgcn_readfirstlane(recD);
      mb = mbn; ob = RBASE(s) + ROFF_O;
    }
    check_in(P, tag, w);
  }
}

extern "C" void kernel_launch(void* const* d_in, const int* in_sizes, int n_in,
                              void* d_out, int out_size, void* d_ws, size_t ws_size,
                              hipStream_t stream) {
  const u16* A_x = (const u16*)d_in[0];
  const u16* A_mem0 = (const u16*)d_in[1];
  const u16* A_Wq = (const u16*)d_in[2];
  const u16* A_Wk = (const u16*)d_in[3];
  const u16* A_Wv = (const u16*)d_in[4];
  const u16* A_Wo = (const u16*)d_in[5];
  const u16* B_x = (const u16*)d_in[7];
  const u16* B_wqkv = (const u16*)d_in[8];
  const u16* B_wo = (const u16*)d_in[9];
  u16* out = (u16*)d_out;

  char* ws = (char*)d_ws;
  size_t off = 0;
  auto alloc = [&](size_t bytes) { char* p = ws + off; off += (bytes + 255) & ~(size_t)255; return p; };
  char* rdy = alloc(RDY_SIZE);
  Ctl* ctl = (Ctl*)alloc(sizeof(Ctl));
  unsigned long long* prof = (unsigned long long*)alloc(96 * 4 * 72 * 8 + 8 * 256 * 16 + 64 * 384 * 8);
  char* kvm = alloc((size_t)NSEG * KVSEG);
  off += (size_t)(-(long long)(size_t)(ws + off)) & (QPAGE - 1);
  char* ring = alloc(QPAGE);
  u16* qkvA = (u16*)alloc(QSIZE);

  FParams cp;
  cp.Ax = A_x; cp.mem0 = A_mem0; cp.Bx = B_x; cp.Wq = A_Wq; cp.Wk = A_Wk; cp.Wv = A_Wv; cp.Wo = A_Wo; cp.wqkvB = B_wqkv; cp.woB = B_wo;
  cp.qkvA = qkvA; cp.out = out; cp.kvm = kvm; cp.ring = ring; cp.rdy = rdy;
  cp.ctl = ctl; cp.prof = prof;
  cp.dq = DQ; cp.dq2 = DQ2; cp.dmv = DMV; cp.dov = DOV; cp.gain = GAIN; cp.inc = MINC; cp.dec = MDEC; cp.pb = PBV; cp.stag = STAG; cp.sh1 = SH1; cp.sh2 = SH2; cp.sh3 = SH3; cp.dv = DVV; cp.pad = BOOT; cp.px = PXV; cp.probes = PROBES; cp.jb = JB; cp.boot2 = BOOT2; cp.boot3 = BOOT3; cp.im = IMARG; cp.gain0 = GAIN0; cp.oj1 = OJ1; cp.lk1 = LK1; cp.lk4 = LK4; cp.sh0 = SH0; cp.sh4 = SH4;
  k_fused<<<256, 256, 0, stream>>>(cp);
}
```

```cpp
#include <hip/hip_runtime.h>
#include <hip/hip_bf16.h>
#include <stdint.h>
#include <stddef.h>

#define DQ 13
#define DQ2 11
#define DMV 2440
#define DOV 1410
#define GAIN 4
#define GAIN0 8
#define MINC 32
#define MDEC 3
#define STAG 1000
#define SH1 14
#define SH2 75
#define SH3 6
#define DVV 0
#define BOOT 0x2850
#define PXV (1200 * 65536 + 32)
#define PBV (1800 * 65536 + 3)
#define PROBES 4
#define JB 80
#define BOOT2 0x0505
#define BOOT3 0x0a14
#define IMARG 3
#define PFR 0
#define OJ1 48
#define LK1 34
#define LK4 38
#define SH0 10
#define SH4 24
#define VSLOT 60
#define TP(run, fixed) (fixed)
typedef unsigned short u16;
typedef __attribute__((ext_vector_type(4))) float f32x4;
typedef unsigned v4u __attribute__((ext_vector_type(4)));

constexpr int S_ = 2048, DM = 1024, HA = 32, DHA = 128, HDA = 4096, MEM = 8, SEG = 32, NSEG = 64;
constexpr int QPAGE = 2 << 20, QBLK = 16384, NPAGE = 60, QSIZE = NPAGE * QPAGE;
__device__ __forceinline__ int qoff(unsigned long long parq, int n) { return n * QPAGE + (int)((parq >> n) & 1) * 8192; }
constexpr int DB = 1024, HB = 32, DHB = 32, NQKVB = 3 * DB;

constexpr int ROFF_O = 0, ROFF_V = 131072, ROFF_M = 262144, RSLOT = 327680, RSIZE = 4 * RSLOT, KVSEG = 131072, SSEG0 = 60;
typedef __attribute__((ext_vector_type(8))) __bf16 bfx8;
typedef __attribute__((ext_vector_type(2))) __bf16 bfx2;
typedef __attribute__((ext_vector_type(2))) float f32x2;
typedef unsigned v2u __attribute__((ext_vector_type(2)));
typedef unsigned v8u __attribute__((ext_vector_type(8)));
typedef unsigned v32u __attribute__((ext_vector_type(32)));
constexpr int NQK = 32, NVO = 64;
constexpr int VXLD = 144, LDS_ALL = 163840;
template <int I> struct IC { static constexpr int v = I; };
template <int I, int N, class F> __device__ __forceinline__ void sfor(F&& f) { if constexpr (I < N) { f(IC<I>{}); sfor<I + 1, N>(f); } }
#define SFOR(u, a, b) sfor<a, b>([&](auto u##_) __attribute__((always_inline)) { constexpr int u = decltype(u##_)::v;
#define SEND });

__device__ __forceinline__ void lds_barrier() { asm volatile("s_waitcnt lgkmcnt(0)" ::: "memory"); __builtin_amdgcn_s_barrier(); asm volatile("" ::: "memory"); }
template <int N> __device__ __forceinline__ float dpp_ror(float v) {
  return __int_as_float(__builtin_amdgcn_update_dpp(0, __float_as_int(v), 0x120 + N, 0xf, 0xf, false));
}
__device__ __forceinline__ unsigned pk2(float a, float b) { f32x2 t = {a, b}; return __builtin_bit_cast(unsigned, __builtin_convertvector(t, bfx2)); }
__device__ __forceinline__ unsigned nosent(unsigned x) { return x < 0xfffffffeu ? x : 0xfffffffeu; }
__device__ __forceinline__ unsigned umax3(unsigned a, unsigned b, unsigned c) { unsigned m = a > b ? a : b; return m > c ? m : c; }
__device__ __forceinline__ unsigned chk4(unsigned m, v4u v) { return umax3(umax3(m, v.x, v.y), v.z, v.w); }
__device__ __forceinline__ unsigned long long now() { return __builtin_amdgcn_s_memtime(); }
__device__ __forceinline__ f32x4 mf(v4u a, v4u b, f32x4 c) {
  return __builtin_amdgcn_mfma_f32_16x16x32_bf16(__builtin_bit_cast(bfx8, a), __builtin_bit_cast(bfx8, b), c, 0, 0, 0);
}
__device__ __forceinline__ v2u trrd(const u16* p) {
  typedef __attribute__((ext_vector_type(4))) short v4i16_t;
  return __builtin_bit_cast(v2u, __builtin_amdgcn_ds_read_tr16_b64_v4i16((__attribute__((address_space(3))) v4i16_t*)p));
}
__device__ __forceinline__ v8u cat8(v4u a, v4u b) { return __builtin_shufflevector(a, b, 0, 1, 2, 3, 4, 5, 6, 7); }
__device__ __forceinline__ float xmax16(float x) { auto r = __builtin_amdgcn_permlane16_swap(__float_as_uint(x), __float_as_uint(x), false, false); return fmaxf(__uint_as_float(r[0]), __uint_as_float(r[1])); }
__device__ __forceinline__ float xmax32(float x) { auto r = __builtin_amdgcn_permlane32_swap(__float_as_uint(x), __float_as_uint(x), false, false); return fmaxf(__uint_as_float(r[0]), __uint_as_float(r[1])); }
__device__ __forceinline__ float xsum16(float x) { auto r = __builtin_amdgcn_permlane16_swap(__float_as_uint(x), __float_as_uint(x), false, false); return __uint_as_float(r[0]) + __uint_as_float(r[1]); }
__device__ __forceinline__ float xsum32(float x) { auto r = __builtin_amdgcn_permlane32_swap(__float_as_uint(x), __float_as_uint(x), false, false); return __uint_as_float(r[0]) + __uint_as_float(r[1]); }
__device__ __forceinline__ int dbl(int off) { return ((off >> 13) << 14) | (off & 8191); }
__device__ __forceinline__ bool spin_fail(unsigned spins, unsigned* tmo, unsigned code) {
  if ((spins & 31u) != 31u) return false;
  if (spins > (1u << 15)) { __hip_atomic_store(tmo, code, __ATOMIC_RELAXED, __HIP_MEMORY_SCOPE_AGENT); return true; }
  return __builtin_amdgcn_readfirstlane(__hip_atomic_load(tmo, __ATOMIC_RELAXED, __HIP_MEMORY_SCOPE_AGENT)) != 0u;
}
#define SB0 __builtin_amdgcn_sched_barrier(0)
#define NTQ 0
#define KVFAKE 0
#define KVAUX 16
#define QGAP 0
#define PSL 48
#define TSL 2
__device__ __forceinline__ int opaque_lane() { int l; asm volatile("v_mbcnt_lo_u32_b32 %0, -1, 0\n\tv_mbcnt_hi_u32_b32 %0, -1, %0" : "=v"(l)); return l & 63; }

#define OUTAUX 16
namespace wg {
constexpr int LDS_USE = 131072;
__device__ __forceinline__ void* uni(const void* p) { const unsigned long long x = (unsigned long long)p; return (void*)(((unsigned long long)(unsigned)__builtin_amdgcn_readfirstlane(x >> 32) << 32) | (unsigned)__builtin_amdgcn_readfirstlane(x)); }
template <int MT>
__device__ __forceinline__ void gemm_tile(const u16* A, int lda, const u16* B, int ldb, int nst  , void* C, int ldc, unsigned long long parq  , char* lds, int w) {
  constexpr bool apack = true, cpack = false;
  constexpr int MI = MT / 32, PA = MT / 64, NP = PA + 4;
  constexpr int A_B = MT * 64, ST = A_B + 16384;
  const int lane = opaque_lane();
  const int l15 = lane & 15, lq = lane >> 4, wm = w >> 1, wn = w & 1;
  lda = __builtin_amdgcn_readfirstlane(lda); ldb = __builtin_amdgcn_readfirstlane(ldb); ldc = __builtin_amdgcn_readfirstlane(ldc); nst = __builtin_amdgcn_readfirstlane(nst);
  f32x4 acc[8][MI];
#pragma unroll
  for (int i = 0; i < 8; ++i)
#pragma unroll
    for (int j = 0; j < MI; ++j) acc[i][j] = (f32x4){0.f, 0.f, 0.f, 0.f};
  const unsigned lb = (unsigned)(size_t)lds;
  auto fsw = [](int x) { return ((x ^ (x >> 1)) & 1) << 1 | (x >> 1); };
  unsigned ao[2], bo[2][8];
  ao[0] = lb + wm * (MT / 2) * 64 + (apack ? lane * 16 : l15 * 64 + ((lq ^ fsw(l15 >> 2)) << 4)); ao[1] = ao[0] + 2 * ST;
  { const int hh = (l15 >> 2) | ((lq & 1) << 2);
#pragma unroll
    for (int i = 0; i < 8; ++i) { bo[0][i] = lb + A_B + (8 * lq + (l15 >> 2)) * 512 + ((16 * wn + ((2 * i) ^ (2 * hh)) + ((l15 >> 1) & 1)) << 4) + (l15 & 1) * 8; bo[1][i] = bo[0][i] + 2 * ST; } }
  __amdgpu_buffer_rsrc_t ra = __builtin_amdgcn_make_buffer_rsrc(uni(A), 0, 0x7fffffff, 0x00020000), rb = __builtin_amdgcn_make_buffer_rsrc(uni(B), 0, 0x7fffffff, 0x00020000);
  const int va = (16 * PA * w * lda + (apack ? lane * 8 : (lane >> 2) * lda + (((lane & 3) ^ fsw(lane >> 4)) << 3))) * 2, ska = __builtin_amdgcn_readfirstlane(apack ? 1024 : 64);
  const int vb0 = ((8 * w + (lane >> 5)) * ldb + (((lane & 31) ^ ((lane >> 5) << 1) ^ ((w & 1) << 3)) << 3)) * 2, vb1 = vb0 ^ 64;
  const int sa = 32 * lda, sb = 4 * ldb, skb = 64 * ldb;
  const unsigned ma = lb + PA * w * 1024, mb = lb + A_B + w * 4096;
#define PIECE1(NT, base, imm, vo, rs, so) do { if constexpr (NT) asm volatile("s_add_u32 m0, %0, %1\n\ts_nop 0\n\tbuffer_load_dwordx4 %2, %3, %4 offen nt lds" :: "s"(base), "i"(imm), "v"(vo), "s"(rs), "s"(so) : "memory", "scc"); \
    else asm volatile("s_add_u32 m0, %0, %1\n\ts_nop 0\n\tbuffer_load_dwordx4 %2, %3, %4 offen lds" :: "s"(base), "i"(imm), "v"(vo), "s"(rs), "s"(so) : "memory", "scc"); } while (0)
#define PIECE(q, st) do { \
    if ((q) < PA) PIECE1(false, ma, (st) * ST + (q) * 1024, va, ra, ka + (q) * sa); \
    else PIECE1(false, mb, (st) * ST + ((q) - PA) * 1024, (((q) - PA) & 1) ? vb1 : vb0, rb, kb + ((q) - PA) * sb); } while (0)
#define RDA(f, j, st) asm volatile("ds_read_b128 %0, %1 offset:%2" : "=v"(fa[f][j]) : "v"(ao[(st) >> 1]), "i"(((st) & 1) * ST + (j) * 1024) : "memory")
#define RDB(f, i, h, st) asm volatile("ds_read_b64_tr_b16 %0, %1 offset:%2" : "=v"(fb[f][i][h]) : "v"(bo[(st) >> 1][i]), "i"(((st) & 1) * ST + (h) * 2048) : "memory")
#define TIE(f) do { _Pragma("unroll") for (int j = 0; j < MI; ++j) asm volatile("" : "+v"(fa[f][j])); \
    _Pragma("unroll") for (int i = 0; i < 8; ++i) asm volatile("" : "+v"(fb[f][i][0]), "+v"(fb[f][i][1])); } while (0)
  v4u fa[2][MI]; v2u fb[2][8][2];
  int ka = qoff(parq, 0), kb = 0;
  asm volatile("s_nop 4" :: "s"(ra), "s"(rb), "s"(sa), "s"(sb), "s"(skb), "s"(ska));
#pragma unroll
  for (int st = 0; st < 4; ++st) {
#pragma unroll
    for (int q = 0; q < NP; ++q) PIECE(q, st);
    ka += ska; kb += skb;
  }
  asm volatile("s_waitcnt vmcnt(%0)\n\ts_barrier" :: "i"(3 * NP) : "memory");
#pragma unroll
  for (int i = 0; i < 8; ++i) { RDB(0, i, 0, 0); RDB(0, i, 1, 0); }
#pragma unroll
  for (int j = 0; j < MI; ++j) RDA(0, j, 0);
  int k4 = 0;
  do {
    if (apack && (k4 & 4)) ka = qoff(parq, (k4 + 4) >> 3);
    if (k4 + 4 >= nst) { ra = __builtin_amdgcn_make_buffer_rsrc(uni(C), 0, 0, 0x00020000); rb = ra; }
#pragma unroll
    for (int st = 0; st < 4; ++st) {
      constexpr int NM = 8 * MI, NR = 16 + MI;
      const int f = st & 1, sn = (st + 1) & 3;
      asm volatile("s_waitcnt vmcnt(%0) lgkmcnt(0)\n\ts_barrier" :: "i"(2 * NP) : "memory");
      TIE(f);
      SB0;
#pragma unroll
      for (int u = 0; u < NM; ++u) {
        const int j = u >> 3, i = u & 7;
        asm volatile("v_mfma_f32_16x16x32_bf16 %0, %1, %2, %0" : "+a"(acc[i][j]) : "v"((v4u){fb[f][i][0].x, fb[f][i][0].y, fb[f][i][1].x, fb[f][i][1].y}), "v"(fa[f][j]));
        SB0;
        constexpr int RS = MT == 256 ? 2 : 1;
        if (u % RS == 0 && u / RS < NR) {
          const int r = u / RS;
          if (r < 16) RDB(f ^ 1, r >> 1, r & 1, sn); else RDA(f ^ 1, r - 16, sn);
          if (MT == 64 && (u == 0 || u == 2)) RDA(f ^ 1, u >> 1, sn);
          SB0;
        }
        constexpr int PS = NM / 8;
        if (u % PS == PS - 1 && u / PS < NP) { PIECE(u / PS, st); SB0; }
      }
      ka += ska; kb += skb;
    }
    k4 += 4;
  } while (k4 < nst);
#undef PIECE
#undef PIECE1
#undef RDA
#undef RDB
  asm volatile("s_nop 7\n\ts_waitcnt lgkmcnt(0)" ::: "memory");
  SB0;
  TIE(0);
#undef TIE
  char* st = lds + LDS_USE + 2048 + w * 4096;
  const __amdgpu_buffer_rsrc_t rs = __builtin_amdgcn_make_buffer_rsrc(uni(C), 0, MT * ldc * 2 - 1536 * 2, 0x00020000);
  const int so = wm * (MT / 2) * ldc * 2 + (cpack ? wn * 4096 + lane * 16 : (lq * ldc + wn * 128 + l15 * 8) * 2), sq = cpack ? 1024 : 8 * ldc;
#pragma unroll
  for (int j = 0; j < MI; ++j) {
    v2u p[8]; v4u v[4];
#pragma unroll
    for (int i = 0; i < 8; ++i) { asm volatile("" : "+a"(acc[i][j])); p[i] = (v2u){pk2(acc[i][j][0], acc[i][j][1]), pk2(acc[i][j][2], acc[i][j][3])}; }
#pragma unroll
    for (int q = 0; q < 4; ++q) { v[q] = (v4u){p[2 * q].x, p[2 * q].y, p[2 * q + 1].x, p[2 * q + 1].y}; asm volatile("" : "+v"(v[q])); }
    if (!cpack) {
#pragma unroll
      for (int i = 0; i < 8; ++i) *(v2u*)(st + l15 * 256 + (((2 * i + (lq >> 1)) ^ l15) << 4) + (lq & 1) * 8) = (i & 1) ? (v2u){v[i >> 1].z, v[i >> 1].w} : (v2u){v[i >> 1].x, v[i >> 1].y};
      asm volatile("" ::: "memory");
#pragma unroll
      for (int q = 0; q < 4; ++q) v[q] = *(const v4u*)(st + (4 * q + lq) * 256 + ((l15 ^ (4 * q + lq)) << 4));
      asm volatile("" ::: "memory");
    }
#pragma unroll
    for (int q = 0; q < 4; ++q) {
      __builtin_amdgcn_raw_buffer_store_b128(v[q], rs, so, 32 * j * ldc + q * sq, OUTAUX);
    }
    SB0;
  }
  asm volatile("s_waitcnt vmcnt(0)" ::: "memory");
  __builtin_amdgcn_s_barrier();
}
}

struct Ctl {
  unsigned qhead[32];
  unsigned prg[192][32];
  unsigned cntAB[16][32];
  unsigned flagO[72][8][32];
  unsigned prog[32];
  unsigned tmo[32];
  unsigned chk[32];
};
struct FParams {
  const u16 *Ax, *mem0, *Bx, *Wq, *Wk, *Wv, *Wo, *wqkvB, *woB;
  u16 *qkvA, *out; char *kvm, *ring, *rdy;
  Ctl* ctl; unsigned long long* prof;
};
#define RLX_AGENT __ATOMIC_RELAXED, __HIP_MEMORY_SCOPE_AGENT
__device__ __forceinline__ unsigned ld_agent(const unsigned* p) { return __hip_atomic_load(p, RLX_AGENT); }
constexpr int RDY_SIZE = 64 * 128;
__device__ __forceinline__ v4u rdy_load(const FParams& F) { const int lz = opaque_lane(); return __builtin_amdgcn_raw_buffer_load_b128(__builtin_amdgcn_make_buffer_rsrc(F.rdy, 0, RDY_SIZE, 0x00020000), lz * 128, 0, 16); }
__device__ __forceinline__ unsigned long long rdy_tag(v4u r) { return (((unsigned long long)(unsigned)__builtin_amdgcn_readlane(r.y, 63) << 32) | (unsigned)__builtin_amdgcn_readlane(r.x, 63)) + 1; }
__device__ __forceinline__ unsigned long long rdy_wait(const FParams& F, v4u r, unsigned long long& tag) {
  tag = rdy_tag(r);
  for (unsigned spins = 0; (~__ballot(r.x == (unsigned)tag && r.y == (unsigned)(tag >> 32)) << 4) != 0 && spins < (1u << 16); ++spins) { __builtin_amdgcn_s_sleep(8); r = rdy_load(F); }
  asm volatile("" ::: "memory");
  return (__ballot(r.z & 1) & ~(15ull << 60)) | (__builtin_popcountll(__ballot(r.z & 4) << 4) > 30 ? 1ull << 63 : 0ull);
}
__device__ __forceinline__ void check_in(const FParams& F, unsigned long long tag, int w) {
  __syncthreads();
  if (w == 0 && opaque_lane() == 0 && __hip_atomic_fetch_add(&F.ctl->chk[0], 1u, RLX_AGENT) == 255u) __hip_atomic_store((unsigned long long*)(F.rdy + 63 * 128), tag, RLX_AGENT);
}
__device__ __forceinline__ void wg_wait(const unsigned* p, unsigned need, Ctl* ctl, int w) {
  if (w == 0) {
    for (unsigned spins = 0;; ++spins) {
      const bool ok = (p == nullptr) || ld_agent(p) >= need;
      if (__all(ok)) break;
      if (spin_fail(spins, &ctl->tmo[0], 2u)) break;
      __builtin_amdgcn_s_sleep(16);
    }
    __builtin_amdgcn_fence(__ATOMIC_ACQUIRE, "agent");
    asm volatile("s_waitcnt vmcnt(0)" ::: "memory");
  }
  __syncthreads();
}

#define PN_BODY(PN_NT) \
          "s_mov_b64 s[40:41], %[xa]\n\ts_mov_b32 s42, 0x400000\n\ts_mov_b32 s43, 0x20000\n\ts_nop 4\n\ts_add_u32 m0, %[wl], 65536\n\ts_mul_i32 %[t], %[ldb], 0\n\t" \
          "buffer_load_dwordx4 %[vw0], %[rw], %[t] offen" PN_NT " lds\n\ts_add_u32 m0, %[wl], 66560\n\ts_mul_i32 %[t], %[ldb], 4\n\tbuffer_load_dwordx4 %[vw0], %[rw], %[t] offen" PN_NT " lds\n\ts_add_u32 m0, %[wl], 67584\n\ts_mul_i32 %[t], %[ldb], 8\n\t" \
          "buffer_load_dwordx4 %[vw1], %[rw], %[t] offen" PN_NT " lds\n\ts_add_u32 m0, %[wl], 68608\n\ts_mul_i32 %[t], %[ldb], 12\n\tbuffer_load_dwordx4 %[vw1], %[rw], %[t] offen" PN_NT " lds\n\ts_add_u32 m0, %[wl], 81920\n\ts_mul_i32 %[t], %[ldb], 64\n\t" \
          "buffer_load_dwordx4 %[vw0], %[rw], %[t] offen" PN_NT " lds\n\ts_add_u32 m0, %[wl], 82944\n\ts_mul_i32 %[t], %[ldb], 68\n\tbuffer_load_dwordx4 %[vw0], %[rw], %[t] offen" PN_NT " lds\n\ts_add_u32 m0, %[wl], 83968\n\ts_mul_i32 %[t], %[ldb], 72\n\t" \
          "buffer_load_dwordx4 %[vw1], %[rw], %[t] offen" PN_NT " lds\n\ts_add_u32 m0, %[wl], 84992\n\ts_mul_i32 %[t], %[ldb], 76\n\tbuffer_load_dwordx4 %[vw1], %[rw], %[t] offen" PN_NT " lds\n\ts_add_u32 m0, %[sl], 0\n\ts_nop 0\n\t" \
          "buffer_load_dwordx4 %[vd0], s[40:43], %[xo] offen lds\n\tbuffer_load_dwordx4 %[vd0], s[40:43], %[xo] offen offset:1024 lds\n\tbuffer_load_dwordx4 %[vd1], s[40:43], %[xo] offen offset:2048 lds\n\tbuffer_load_dwordx4 %[vd1], s[40:43], %[xo] offen offset:3072 lds\n\ts_add_u32 m0, %[sl], 4096\n\ts_nop 0\n\t" \
          "buffer_load_dwordx4 %[vd2], s[40:43], %[xo] offen lds\n\tbuffer_load_dwordx4 %[vd2], s[40:43], %[xo] offen offset:1024 lds\n\tbuffer_load_dwordx4 %[vd3], s[40:43], %[xo] offen offset:2048 lds\n\tbuffer_load_dwordx4 %[vd3], s[40:43], %[xo] offen offset:3072 lds\n\ts_add_u32 %[xo], %[xo], 0x8000\n\ts_add_u32 %[sl], %[sl], 0x8000\n\t" \
          "s_bitset0_b32 %[sl], 17\n\ts_add_u32 m0, %[sl], 0\n\ts_nop 0\n\tbuffer_load_dwordx4 %[vd0], s[40:43], %[xo] offen lds\n\tbuffer_load_dwordx4 %[vd0], s[40:43], %[xo] offen offset:1024 lds\n\tbuffer_load_dwordx4 %[vd1], s[40:43], %[xo] offen offset:2048 lds\n\t" \
          "buffer_load_dwordx4 %[vd1], s[40:43], %[xo] offen offset:3072 lds\n\ts_add_u32 m0, %[sl], 4096\n\ts_nop 0\n\tbuffer_load_dwordx4 %[vd2], s[40:43], %[xo] offen lds\n\tbuffer_load_dwordx4 %[vd2], s[40:43], %[xo] offen offset:1024 lds\n\tbuffer_load_dwordx4 %[vd3], s[40:43], %[xo] offen offset:2048 lds\n\t" \
          "buffer_load_dwordx4 %[vd3], s[40:43], %[xo] offen offset:3072 lds\n\ts_add_u32 %[xo], %[xo], 0x8000\n\ts_add_u32 %[sl], %[sl], 0x8000\n\ts_bitset0_b32 %[sl], 17\n\ts_add_u32 m0, %[wl], 98304\n\ts_mul_i32 %[t], %[ldb], 128\n\t" \
          "buffer_load_dwordx4 %[vw0], %[rw], %[t] offen" PN_NT " lds\n\ts_add_u32 m0, %[wl], 99328\n\ts_mul_i32 %[t], %[ldb], 132\n\tbuffer_load_dwordx4 %[vw0], %[rw], %[t] offen" PN_NT " lds\n\ts_add_u32 m0, %[wl], 100352\n\ts_mul_i32 %[t], %[ldb], 136\n\t" \
          "buffer_load_dwordx4 %[vw1], %[rw], %[t] offen" PN_NT " lds\n\ts_add_u32 m0, %[wl], 101376\n\ts_mul_i32 %[t], %[ldb], 140\n\tbuffer_load_dwordx4 %[vw1], %[rw], %[t] offen" PN_NT " lds\n\ts_add_u32 m0, %[wl], 114688\n\ts_mul_i32 %[t], %[ldb], 192\n\t" \
          "buffer_load_dwordx4 %[vw0], %[rw], %[t] offen" PN_NT " lds\n\ts_add_u32 m0, %[wl], 115712\n\ts_mul_i32 %[t], %[ldb], 196\n\tbuffer_load_dwordx4 %[vw0], %[rw], %[t] offen" PN_NT " lds\n\ts_add_u32 m0, %[wl], 116736\n\ts_mul_i32 %[t], %[ldb], 200\n\t" \
          "buffer_load_dwordx4 %[vw1], %[rw], %[t] offen" PN_NT " lds\n\ts_add_u32 m0, %[wl], 117760\n\ts_mul_i32 %[t], %[ldb], 204\n\tbuffer_load_dwordx4 %[vw1], %[rw], %[t] offen" PN_NT " lds\n\ts_add_u32 m0, %[wl], 131072\n\ts_mul_i32 %[t], %[ldb], 256\n\t" \
          "buffer_load_dwordx4 %[vw0], %[rw], %[t] offen" PN_NT " lds\n\ts_add_u32 m0, %[wl], 132096\n\ts_mul_i32 %[t], %[ldb], 260\n\tbuffer_load_dwordx4 %[vw0], %[rw], %[t] offen" PN_NT " lds\n\ts_add_u32 m0, %[wl], 133120\n\ts_mul_i32 %[t], %[ldb], 264\n\t" \
          "buffer_load_dwordx4 %[vw1], %[rw], %[t] offen" PN_NT " lds\n\ts_add_u32 m0, %[wl], 134144\n\ts_mul_i32 %[t], %[ldb], 268\n\tbuffer_load_dwordx4 %[vw1], %[rw], %[t] offen" PN_NT " lds\n\ts_waitcnt vmcnt(32) lgkmcnt(0)\n\ts_barrier\n\t" \
          "s_add_u32 m0, %[wl], 147456\n\ts_mul_i32 %[t], %[ldb], 320\n\tbuffer_load_dwordx4 %[vw0], %[rw], %[t] offen" PN_NT " lds\n\ts_add_u32 m0, %[wl], 148480\n\ts_mul_i32 %[t], %[ldb], 324\n\tbuffer_load_dwordx4 %[vw0], %[rw], %[t] offen" PN_NT " lds\n\t" \
          "s_add_u32 m0, %[wl], 149504\n\ts_mul_i32 %[t], %[ldb], 328\n\tbuffer_load_dwordx4 %[vw1], %[rw], %[t] offen" PN_NT " lds\n\ts_add_u32 m0, %[wl], 150528\n\ts_mul_i32 %[t], %[ldb], 332\n\tbuffer_load_dwordx4 %[vw1], %[rw], %[t] offen" PN_NT " lds\n\t" \
          "ds_read_b64_tr_b16 a[0:1], %[rt0] offset:0\n\tds_read_b64_tr_b16 a[2:3], %[rt0] offset:1024\n\tds_read_b64_tr_b16 a[4:5], %[rt1] offset:0\n\tds_read_b64_tr_b16 a[6:7], %[rt1] offset:1024\n\tds_read_b64_tr_b16 a[8:9], %[rt0] offset:8192\n\tds_read_b64_tr_b16 a[10:11], %[rt0] offset:9216\n\t" \
          "ds_read_b64_tr_b16 a[12:13], %[rt1] offset:8192\n\tds_read_b64_tr_b16 a[14:15], %[rt1] offset:9216\n\ts_waitcnt vmcnt(32) lgkmcnt(0)\n\ts_barrier\n\ts_add_u32 m0, %[wl], 65536\n\ts_mul_i32 %[t], %[ldb], 384\n\t" \
          "buffer_load_dwordx4 %[vw0], %[rw], %[t] offen" PN_NT " lds\n\ts_add_u32 m0, %[wl], 66560\n\ts_mul_i32 %[t], %[ldb], 388\n\tbuffer_load_dwordx4 %[vw0], %[rw], %[t] offen" PN_NT " lds\n\ts_add_u32 m0, %[wl], 67584\n\ts_mul_i32 %[t], %[ldb], 392\n\t" \
          "buffer_load_dwordx4 %[vw1], %[rw], %[t] offen" PN_NT " lds\n\ts_add_u32 m0, %[wl], 68608\n\ts_mul_i32 %[t], %[ldb], 396\n\tbuffer_load_dwordx4 %[vw1], %[rw], %[t] offen" PN_NT " lds\n\tds_read_b64_tr_b16 a[16:17], %[rt0] offset:16384\n\tds_read_b64_tr_b16 a[18:19], %[rt0] offset:17408\n\t" \
          "ds_read_b64_tr_b16 a[20:21], %[rt1] offset:16384\n\tds_read_b64_tr_b16 a[22:23], %[rt1] offset:17408\n\tds_read_b64_tr_b16 a[24:25], %[rt0] offset:24576\n\tds_read_b64_tr_b16 a[26:27], %[rt0] offset:25600\n\tds_read_b64_tr_b16 a[28:29], %[rt1] offset:24576\n\tds_read_b64_tr_b16 a[30:31], %[rt1] offset:25600\n\t" \
          "s_waitcnt vmcnt(16) lgkmcnt(0)\n\ts_barrier\n\ts_add_u32 m0, %[wl], 81920\n\ts_mul_i32 %[t], %[ldb], 448\n\tbuffer_load_dwordx4 %[vw0], %[rw], %[t] offen" PN_NT " lds\n\ts_add_u32 m0, %[wl], 82944\n\t" \
          "s_mul_i32 %[t], %[ldb], 452\n\tbuffer_load_dwordx4 %[vw0], %[rw], %[t] offen" PN_NT " lds\n\ts_add_u32 m0, %[wl], 83968\n\ts_mul_i32 %[t], %[ldb], 456\n\tbuffer_load_dwordx4 %[vw1], %[rw], %[t] offen" PN_NT " lds\n\ts_add_u32 m0, %[wl], 84992\n\t" \
          "s_mul_i32 %[t], %[ldb], 460\n\tbuffer_load_dwordx4 %[vw1], %[rw], %[t] offen" PN_NT " lds\n\tds_read_b64_tr_b16 a[32:33], %[rt0] offset:32768\n\tds_read_b64_tr_b16 a[34:35], %[rt0] offset:33792\n\tds_read_b64_tr_b16 a[36:37], %[rt1] offset:32768\n\tds_read_b64_tr_b16 a[38:39], %[rt1] offset:33792\n\t" \
          "ds_read_b64_tr_b16 a[40:41], %[rt0] offset:40960\n\tds_read_b64_tr_b16 a[42:43], %[rt0] offset:41984\n\tds_read_b64_tr_b16 a[44:45], %[rt1] offset:40960\n\tds_read_b64_tr_b16 a[46:47], %[rt1] offset:41984\n\ts_waitcnt vmcnt(16) lgkmcnt(0)\n\ts_barrier\n\t" \
          "s_add_u32 m0, %[wl], 98304\n\ts_mul_i32 %[t], %[ldb], 512\n\tbuffer_load_dwordx4 %[vw0], %[rw], %[t] offen" PN_NT " lds\n\ts_add_u32 m0, %[wl], 99328\n\ts_mul_i32 %[t], %[ldb], 516\n\tbuffer_load_dwordx4 %[vw0], %[rw], %[t] offen" PN_NT " lds\n\t" \
          "s_add_u32 m0, %[wl], 100352\n\ts_mul_i32 %[t], %[ldb], 520\n\tbuffer_load_dwordx4 %[vw1], %[rw], %[t] offen" PN_NT " lds\n\ts_add_u32 m0, %[wl], 101376\n\ts_mul_i32 %[t], %[ldb], 524\n\tbuffer_load_dwordx4 %[vw1], %[rw], %[t] offen" PN_NT " lds\n\t" \
          "ds_read_b64_tr_b16 a[48:49], %[rt2] offset:0\n\tds_read_b64_tr_b16 a[50:51], %[rt2] offset:1024\n\tds_read_b64_tr_b16 a[52:53], %[rt3] offset:0\n\tds_read_b64_tr_b16 a[54:55], %[rt3] offset:1024\n\tds_read_b64_tr_b16 a[56:57], %[rt2] offset:8192\n\tds_read_b64_tr_b16 a[58:59], %[rt2] offset:9216\n\t" \
          "ds_read_b64_tr_b16 a[60:61], %[rt3] offset:8192\n\tds_read_b64_tr_b16 a[62:63], %[rt3] offset:9216\n\ts_waitcnt vmcnt(16) lgkmcnt(0)\n\ts_barrier\n\ts_add_u32 m0, %[wl], 114688\n\ts_mul_i32 %[t], %[ldb], 576\n\t" \
          "buffer_load_dwordx4 %[vw0], %[rw], %[t] offen" PN_NT " lds\n\ts_add_u32 m0, %[wl], 115712\n\ts_mul_i32 %[t], %[ldb], 580\n\tbuffer_load_dwordx4 %[vw0], %[rw], %[t] offen" PN_NT " lds\n\ts_add_u32 m0, %[wl], 116736\n\ts_mul_i32 %[t], %[ldb], 584\n\t" \
          "buffer_load_dwordx4 %[vw1], %[rw], %[t] offen" PN_NT " lds\n\ts_add_u32 m0, %[wl], 117760\n\ts_mul_i32 %[t], %[ldb], 588\n\tbuffer_load_dwordx4 %[vw1], %[rw], %[t] offen" PN_NT " lds\n\tds_read_b64_tr_b16 a[64:65], %[rt2] offset:16384\n\tds_read_b64_tr_b16 a[66:67], %[rt2] offset:17408\n\t" \
          "ds_read_b64_tr_b16 a[68:69], %[rt3] offset:16384\n\tds_read_b64_tr_b16 a[70:71], %[rt3] offset:17408\n\tds_read_b64_tr_b16 a[72:73], %[rt2] offset:24576\n\tds_read_b64_tr_b16 a[74:75], %[rt2] offset:25600\n\tds_read_b64_tr_b16 a[76:77], %[rt3] offset:24576\n\tds_read_b64_tr_b16 a[78:79], %[rt3] offset:25600\n\t" \
          "s_waitcnt vmcnt(16) lgkmcnt(0)\n\ts_barrier\n\ts_add_u32 m0, %[wl], 131072\n\ts_mul_i32 %[t], %[ldb], 640\n\tbuffer_load_dwordx4 %[vw0], %[rw], %[t] offen" PN_NT " lds\n\ts_add_u32 m0, %[wl], 132096\n\t" \
          "s_mul_i32 %[t], %[ldb], 644\n\tbuffer_load_dwordx4 %[vw0], %[rw], %[t] offen" PN_NT " lds\n\ts_add_u32 m0, %[wl], 133120\n\ts_mul_i32 %[t], %[ldb], 648\n\tbuffer_load_dwordx4 %[vw1], %[rw], %[t] offen" PN_NT " lds\n\ts_add_u32 m0, %[wl], 134144\n\t" \
          "s_mul_i32 %[t], %[ldb], 652\n\tbuffer_load_dwordx4 %[vw1], %[rw], %[t] offen" PN_NT " lds\n\tds_read_b64_tr_b16 a[80:81], %[rt2] offset:32768\n\tds_read_b64_tr_b16 a[82:83], %[rt2] offset:33792\n\tds_read_b64_tr_b16 a[84:85], %[rt3] offset:32768\n\tds_read_b64_tr_b16 a[86:87], %[rt3] offset:33792\n\t" \
          "ds_read_b64_tr_b16 a[88:89], %[rt2] offset:40960\n\tds_read_b64_tr_b16 a[90:91], %[rt2] offset:41984\n\tds_read_b64_tr_b16 a[92:93], %[rt3] offset:40960\n\tds_read_b64_tr_b16 a[94:95], %[rt3] offset:41984\n\ts_waitcnt vmcnt(16) lgkmcnt(0)\n\ts_barrier\n\t" \
          "s_add_u32 m0, %[wl], 147456\n\ts_mul_i32 %[t], %[ldb], 704\n\tbuffer_load_dwordx4 %[vw0], %[rw], %[t] offen" PN_NT " lds\n\ts_add_u32 m0, %[wl], 148480\n\ts_mul_i32 %[t], %[ldb], 708\n\tbuffer_load_dwordx4 %[vw0], %[rw], %[t] offen" PN_NT " lds\n\t" \
          "s_add_u32 m0, %[wl], 149504\n\ts_mul_i32 %[t], %[ldb], 712\n\tbuffer_load_dwordx4 %[vw1], %[rw], %[t] offen" PN_NT " lds\n\ts_add_u32 m0, %[wl], 150528\n\ts_mul_i32 %[t], %[ldb], 716\n\tbuffer_load_dwordx4 %[vw1], %[rw], %[t] offen" PN_NT " lds\n\t" \
          "ds_read_b64_tr_b16 a[96:97], %[rt0] offset:0\n\tds_read_b64_tr_b16 a[98:99], %[rt0] offset:1024\n\tds_read_b64_tr_b16 a[100:101], %[rt1] offset:0\n\tds_read_b64_tr_b16 a[102:103], %[rt1] offset:1024\n\tds_read_b64_tr_b16 a[104:105], %[rt0] offset:8192\n\tds_read_b64_tr_b16 a[106:107], %[rt0] offset:9216\n\t" \
          "ds_read_b64_tr_b16 a[108:109], %[rt1] offset:8192\n\tds_read_b64_tr_b16 a[110:111], %[rt1] offset:9216\n\ts_waitcnt vmcnt(16) lgkmcnt(0)\n\ts_barrier\n\ts_add_u32 m0, %[wl], 65536\n\ts_mul_i32 %[t], %[ldb], 768\n\t" \
          "buffer_load_dwordx4 %[vw0], %[rw], %[t] offen" PN_NT " lds\n\ts_add_u32 m0, %[wl], 66560\n\ts_mul_i32 %[t], %[ldb], 772\n\tbuffer_load_dwordx4 %[vw0], %[rw], %[t] offen" PN_NT " lds\n\ts_add_u32 m0, %[wl], 67584\n\ts_mul_i32 %[t], %[ldb], 776\n\t" \
          "buffer_load_dwordx4 %[vw1], %[rw], %[t] offen" PN_NT " lds\n\ts_add_u32 m0, %[wl], 68608\n\ts_mul_i32 %[t], %[ldb], 780\n\tbuffer_load_dwordx4 %[vw1], %[rw], %[t] offen" PN_NT " lds\n\tds_read_b64_tr_b16 a[112:113], %[rt0] offset:16384\n\tds_read_b64_tr_b16 a[114:115], %[rt0] offset:17408\n\t" \
          "ds_read_b64_tr_b16 a[116:117], %[rt1] offset:16384\n\tds_read_b64_tr_b16 a[118:119], %[rt1] offset:17408\n\tds_read_b64_tr_b16 a[120:121], %[rt0] offset:24576\n\tds_read_b64_tr_b16 a[122:123], %[rt0] offset:25600\n\tds_read_b64_tr_b16 a[124:125], %[rt1] offset:24576\n\tds_read_b64_tr_b16 a[126:127], %[rt1] offset:25600\n\t" \
          "s_waitcnt vmcnt(16) lgkmcnt(0)\n\ts_barrier\n\ts_add_u32 m0, %[wl], 81920\n\ts_mul_i32 %[t], %[ldb], 832\n\tbuffer_load_dwordx4 %[vw0], %[rw], %[t] offen" PN_NT " lds\n\ts_add_u32 m0, %[wl], 82944\n\t" \
          "s_mul_i32 %[t], %[ldb], 836\n\tbuffer_load_dwordx4 %[vw0], %[rw], %[t] offen" PN_NT " lds\n\ts_add_u32 m0, %[wl], 83968\n\ts_mul_i32 %[t], %[ldb], 840\n\tbuffer_load_dwordx4 %[vw1], %[rw], %[t] offen" PN_NT " lds\n\ts_add_u32 m0, %[wl], 84992\n\t" \
          "s_mul_i32 %[t], %[ldb], 844\n\tbuffer_load_dwordx4 %[vw1], %[rw], %[t] offen" PN_NT " lds\n\tds_read_b64_tr_b16 a[128:129], %[rt0] offset:32768\n\tds_read_b64_tr_b16 a[130:131], %[rt0] offset:33792\n\tds_read_b64_tr_b16 a[132:133], %[rt1] offset:32768\n\tds_read_b64_tr_b16 a[134:135], %[rt1] offset:33792\n\t" \
          "ds_read_b64_tr_b16 a[136:137], %[rt0] offset:40960\n\tds_read_b64_tr_b16 a[138:139], %[rt0] offset:41984\n\tds_read_b64_tr_b16 a[140:141], %[rt1] offset:40960\n\tds_read_b64_tr_b16 a[142:143], %[rt1] offset:41984\n\ts_waitcnt vmcnt(16) lgkmcnt(0)\n\ts_barrier\n\t" \
          "s_add_u32 m0, %[wl], 98304\n\ts_mul_i32 %[t], %[ldb], 896\n\tbuffer_load_dwordx4 %[vw0], %[rw], %[t] offen" PN_NT " lds\n\ts_add_u32 m0, %[wl], 99328\n\ts_mul_i32 %[t], %[ldb], 900\n\tbuffer_load_dwordx4 %[vw0], %[rw], %[t] offen" PN_NT " lds\n\t" \
          "s_add_u32 m0, %[wl], 100352\n\ts_mul_i32 %[t], %[ldb], 904\n\tbuffer_load_dwordx4 %[vw1], %[rw], %[t] offen" PN_NT " lds\n\ts_add_u32 m0, %[wl], 101376\n\ts_mul_i32 %[t], %[ldb], 908\n\tbuffer_load_dwordx4 %[vw1], %[rw], %[t] offen" PN_NT " lds\n\t" \
          "ds_read_b64_tr_b16 a[144:145], %[rt2] offset:0\n\tds_read_b64_tr_b16 a[146:147], %[rt2] offset:1024\n\tds_read_b64_tr_b16 a[148:149], %[rt3] offset:0\n\tds_read_b64_tr_b16 a[150:151], %[rt3] offset:1024\n\tds_read_b64_tr_b16 a[152:153], %[rt2] offset:8192\n\tds_read_b64_tr_b16 a[154:155], %[rt2] offset:9216\n\t" \
          "ds_read_b64_tr_b16 a[156:157], %[rt3] offset:8192\n\tds_read_b64_tr_b16 a[158:159], %[rt3] offset:9216\n\ts_waitcnt vmcnt(16) lgkmcnt(0)\n\ts_barrier\n\ts_add_u32 m0, %[wl], 114688\n\ts_mul_i32 %[t], %[ldb], 960\n\t" \
          "buffer_load_dwordx4 %[vw0], %[rw], %[t] offen" PN_NT " lds\n\ts_add_u32 m0, %[wl], 115712\n\ts_mul_i32 %[t], %[ldb], 964\n\tbuffer_load_dwordx4 %[vw0], %[rw], %[t] offen" PN_NT " lds\n\ts_add_u32 m0, %[wl], 116736\n\ts_mul_i32 %[t], %[ldb], 968\n\t" \
          "buffer_load_dwordx4 %[vw1], %[rw], %[t] offen" PN_NT " lds\n\ts_add_u32 m0, %[wl], 117760\n\ts_mul_i32 %[t], %[ldb], 972\n\tbuffer_load_dwordx4 %[vw1], %[rw], %[t] offen" PN_NT " lds\n\tds_read_b64_tr_b16 a[160:161], %[rt2] offset:16384\n\tds_read_b64_tr_b16 a[162:163], %[rt2] offset:17408\n\t" \
          "ds_read_b64_tr_b16 a[164:165], %[rt3] offset:16384\n\tds_read_b64_tr_b16 a[166:167], %[rt3] offset:17408\n\tds_read_b64_tr_b16 a[168:169], %[rt2] offset:24576\n\tds_read_b64_tr_b16 a[170:171], %[rt2] offset:25600\n\tds_read_b64_tr_b16 a[172:173], %[rt3] offset:24576\n\tds_read_b64_tr_b16 a[174:175], %[rt3] offset:25600\n\t" \
          "s_waitcnt vmcnt(16) lgkmcnt(0)\n\ts_barrier\n\tbuffer_load_dwordx4 v[40:43], %[ro], %[rr], 0 offen sc1\n\tds_read_b64_tr_b16 a[176:177], %[rt2] offset:32768\n\tds_read_b64_tr_b16 a[178:179], %[rt2] offset:33792\n\tds_read_b64_tr_b16 a[180:181], %[rt3] offset:32768\n\t" \
          "ds_read_b64_tr_b16 a[182:183], %[rt3] offset:33792\n\tds_read_b64_tr_b16 a[184:185], %[rt2] offset:40960\n\tds_read_b64_tr_b16 a[186:187], %[rt2] offset:41984\n\tds_read_b64_tr_b16 a[188:189], %[rt3] offset:40960\n\tds_read_b64_tr_b16 a[190:191], %[rt3] offset:41984\n\ts_waitcnt vmcnt(13) lgkmcnt(0)\n\t" \
          "s_barrier\n\tds_read_b64_tr_b16 a[192:193], %[rt0] offset:0\n\tds_read_b64_tr_b16 a[194:195], %[rt0] offset:1024\n\tds_read_b64_tr_b16 a[196:197], %[rt1] offset:0\n\tds_read_b64_tr_b16 a[198:199], %[rt1] offset:1024\n\tds_read_b64_tr_b16 a[200:201], %[rt0] offset:8192\n\t" \
          "ds_read_b64_tr_b16 a[202:203], %[rt0] offset:9216\n\tds_read_b64_tr_b16 a[204:205], %[rt1] offset:8192\n\tds_read_b64_tr_b16 a[206:207], %[rt1] offset:9216\n\ts_waitcnt vmcnt(9) lgkmcnt(0)\n\ts_barrier\n\tds_read_b64_tr_b16 a[208:209], %[rt0] offset:16384\n\t" \
          "ds_read_b64_tr_b16 a[210:211], %[rt0] offset:17408\n\tds_read_b64_tr_b16 a[212:213], %[rt1] offset:16384\n\tds_read_b64_tr_b16 a[214:215], %[rt1] offset:17408\n\tds_read_b64_tr_b16 a[216:217], %[rt0] offset:24576\n\tds_read_b64_tr_b16 a[218:219], %[rt0] offset:25600\n\tds_read_b64_tr_b16 a[220:221], %[rt1] offset:24576\n\t" \
          "ds_read_b64_tr_b16 a[222:223], %[rt1] offset:25600\n\ts_waitcnt vmcnt(5) lgkmcnt(0)\n\ts_barrier\n\tds_read_b64_tr_b16 a[224:225], %[rt0] offset:32768\n\tds_read_b64_tr_b16 a[226:227], %[rt0] offset:33792\n\tds_read_b64_tr_b16 a[228:229], %[rt1] offset:32768\n\t" \
          "ds_read_b64_tr_b16 a[230:231], %[rt1] offset:33792\n\tds_read_b64_tr_b16 a[232:233], %[rt0] offset:40960\n\tds_read_b64_tr_b16 a[234:235], %[rt0] offset:41984\n\tds_read_b64_tr_b16 a[236:237], %[rt1] offset:40960\n\tds_read_b64_tr_b16 a[238:239], %[rt1] offset:41984\n\ts_waitcnt vmcnt(0) lgkmcnt(0)\n\t" \
          "s_barrier\n\tds_read_b64_tr_b16 a[240:241], %[rt2] offset:0\n\tds_read_b64_tr_b16 a[242:243], %[rt2] offset:1024\n\tds_read_b64_tr_b16 a[244:245], %[rt3] offset:0\n\tds_read_b64_tr_b16 a[246:247], %[rt3] offset:1024\n\tds_read_b64_tr_b16 a[248:249], %[rt2] offset:8192\n\t" \
          "ds_read_b64_tr_b16 a[250:251], %[rt2] offset:9216\n\tds_read_b64_tr_b16 a[252:253], %[rt3] offset:8192\n\tds_read_b64_tr_b16 a[254:255], %[rt3] offset:9216\n\ts_waitcnt lgkmcnt(0)\n\ts_barrier\n\ts_add_u32 m0, %[sl], 0\n\t" \
          "s_nop 0\n\tbuffer_load_dwordx4 %[vd0], s[40:43], %[xo] offen lds\n\tbuffer_load_dwordx4 %[vd0], s[40:43], %[xo] offen offset:1024 lds\n\tbuffer_load_dwordx4 %[vd1], s[40:43], %[xo] offen offset:2048 lds\n\tbuffer_load_dwordx4 %[vd1], s[40:43], %[xo] offen offset:3072 lds\n\ts_add_u32 m0, %[sl], 4096\n\t" \
          "s_nop 0\n\tbuffer_load_dwordx4 %[vd2], s[40:43], %[xo] offen lds\n\tbuffer_load_dwordx4 %[vd2], s[40:43], %[xo] offen offset:1024 lds\n\tbuffer_load_dwordx4 %[vd3], s[40:43], %[xo] offen offset:2048 lds\n\tbuffer_load_dwordx4 %[vd3], s[40:43], %[xo] offen offset:3072 lds\n\ts_add_u32 %[xo], %[xo], 0x8000\n\t" \
          "s_add_u32 %[sl], %[sl], 0x8000\n\ts_bitset0_b32 %[sl], 17\n\ts_mov_b32 %[t], 0\n\tv_readlane_b32 s44, v40, 63\n\tv_readlane_b32 s45, v41, 63\n\ts_add_u32 s44, s44, 1\n\t" \
          "s_addc_u32 s45, s45, 0\n\tLpn_r%=:\n\tv_cmp_eq_u64 vcc, s[44:45], v[40:41]\n\ts_add_u32 %[t], %[t], 0x10000\n\ts_cbranch_scc1 Lpn_g%=\n\ts_or_b32 vcc_hi, vcc_hi, 0xf0000000\n\t" \
          "s_and_b32 vcc_lo, vcc_lo, vcc_hi\n\ts_cmp_eq_u32 vcc_lo, -1\n\ts_cbranch_scc1 Lpn_g%=\n\ts_sleep 8\n\tbuffer_load_dwordx4 v[40:43], %[ro], %[rr], 0 offen sc1\n\ts_waitcnt vmcnt(0)\n\t" \
          "s_branch Lpn_r%=\n\tLpn_g%=:\n\tv_readlane_b32 %[t], v42, %[pl]\n\ts_and_b32 %[t], %[t], 1\n\ts_lshl_b32 %[t], %[t], 13\n\ts_add_u32 %[co], %[co], %[t]\n\t" \
          "ds_read_b128 v[64:67], %[ra0] offset:0\n\tds_read_b128 v[68:71], %[ra1] offset:0\n\tds_read_b128 v[72:75], %[ra2] offset:0\n\tds_read_b128 v[76:79], %[ra3] offset:0\n\tds_read_b128 v[80:83], %[ra0] offset:256\n\tds_read_b128 v[84:87], %[ra1] offset:256\n\t" \
          "ds_read_b128 v[88:91], %[ra2] offset:256\n\tds_read_b128 v[92:95], %[ra3] offset:256\n\ts_mov_b64 %[sx], 0\n\tLpn_loop%=:\n\ts_waitcnt lgkmcnt(6)\n\tv_mfma_f32_16x16x32_bf16 v[48:51], a[0:3], v[64:67], 0\n\t" \
          "ds_read_b128 v[96:99], %[ra0] offset:512\n\tv_mfma_f32_16x16x32_bf16 v[52:55], a[4:7], v[64:67], 0\n\ts_add_u32 %[t], %[j], 3\n\ts_cmp_lt_u32 %[t], %[je]\n\ts_cselect_b32 s42, s42, 0\n\tv_mfma_f32_16x16x32_bf16 v[48:51], a[8:11], v[68:71], v[48:51]\n\t" \
          "ds_read_b128 v[100:103], %[ra1] offset:512\n\tv_mfma_f32_16x16x32_bf16 v[52:55], a[12:15], v[68:71], v[52:55]\n\tv_cvt_pk_bf16_f32 v44, v56, v57\n\tv_cvt_pk_bf16_f32 v45, v58, v59\n\ts_waitcnt lgkmcnt(6)\n\tv_mfma_f32_16x16x32_bf16 v[48:51], a[16:19], v[72:75], v[48:51]\n\t" \
          "ds_read_b128 v[104:107], %[ra2] offset:512\n\tv_mfma_f32_16x16x32_bf16 v[52:55], a[20:23], v[72:75], v[52:55]\n\tv_cvt_pk_bf16_f32 v46, v60, v61\n\tv_cvt_pk_bf16_f32 v47, v62, v63\n\tv_mfma_f32_16x16x32_bf16 v[48:51], a[24:27], v[76:79], v[48:51]\n\tds_read_b128 v[108:111], %[ra3] offset:512\n\t" \
          "v_mfma_f32_16x16x32_bf16 v[52:55], a[28:31], v[76:79], v[52:55]\n\ts_mov_b64 exec, %[sx]\n\tbuffer_store_dwordx4 v[44:47], %[so], %[rc], %[co] offen sc1\n\ts_mov_b64 exec, -1\n\ts_waitcnt lgkmcnt(6)\n\tv_mfma_f32_16x16x32_bf16 v[48:51], a[32:35], v[80:83], v[48:51]\n\t" \
          "ds_read_b128 v[112:115], %[ra0] offset:768\n\tv_mfma_f32_16x16x32_bf16 v[52:55], a[36:39], v[80:83], v[52:55]\n\ts_mov_b64 exec, %[fx]\n\tbuffer_store_dword %[fv], off, %[rf], %[fo] sc1\n\ts_mov_b64 exec, -1\n\tv_mfma_f32_16x16x32_bf16 v[48:51], a[40:43], v[84:87], v[48:51]\n\t" \
          "ds_read_b128 v[116:119], %[ra1] offset:768\n\tv_mfma_f32_16x16x32_bf16 v[52:55], a[44:47], v[84:87], v[52:55]\n\ts_add_u32 %[co], %[co], %[cs]\n\ts_mov_b32 %[cs], 0x4000\n\ts_mov_b64 %[sx], -1\n\ts_waitcnt lgkmcnt(6)\n\t" \
          "v_mfma_f32_16x16x32_bf16 v[48:51], a[48:51], v[88:91], v[48:51]\n\tds_read_b128 v[120:123], %[ra2] offset:768\n\ts_add_u32 m0, %[sl], 0\n\tv_mfma_f32_16x16x32_bf16 v[52:55], a[52:55], v[88:91], v[52:55]\n\tbuffer_load_dwordx4 %[vd0], s[40:43], %[xo] offen lds\n\tv_mfma_f32_16x16x32_bf16 v[48:51], a[56:59], v[92:95], v[48:51]\n\t" \
          "ds_read_b128 v[124:127], %[ra3] offset:768\n\tv_mfma_f32_16x16x32_bf16 v[52:55], a[60:63], v[92:95], v[52:55]\n\ts_waitcnt lgkmcnt(6)\n\tv_mfma_f32_16x16x32_bf16 v[48:51], a[64:67], v[96:99], v[48:51]\n\tds_read_b128 v[64:67], %[ra0] offset:1024\n\tv_mfma_f32_16x16x32_bf16 v[52:55], a[68:71], v[96:99], v[52:55]\n\t" \
          "buffer_load_dwordx4 %[vd0], s[40:43], %[xo] offen offset:1024 lds\n\tv_mfma_f32_16x16x32_bf16 v[48:51], a[72:75], v[100:103], v[48:51]\n\tds_read_b128 v[68:71], %[ra1] offset:1024\n\tv_mfma_f32_16x16x32_bf16 v[52:55], a[76:79], v[100:103], v[52:55]\n\ts_waitcnt lgkmcnt(6)\n\tv_mfma_f32_16x16x32_bf16 v[48:51], a[80:83], v[104:107], v[48:51]\n\t" \
          "ds_read_b128 v[72:75], %[ra2] offset:1024\n\tv_mfma_f32_16x16x32_bf16 v[52:55], a[84:87], v[104:107], v[52:55]\n\tbuffer_load_dwordx4 %[vd1], s[40:43], %[xo] offen offset:2048 lds\n\tv_mfma_f32_16x16x32_bf16 v[48:51], a[88:91], v[108:111], v[48:51]\n\tds_read_b128 v[76:79], %[ra3] offset:1024\n\tv_mfma_f32_16x16x32_bf16 v[52:55], a[92:95], v[108:111], v[52:55]\n\t" \
          "s_waitcnt lgkmcnt(6)\n\tv_mfma_f32_16x16x32_bf16 v[48:51], a[96:99], v[112:115], v[48:51]\n\tds_read_b128 v[80:83], %[ra0] offset:1280\n\tv_mfma_f32_16x16x32_bf16 v[52:55], a[100:103], v[112:115], v[52:55]\n\tbuffer_load_dwordx4 %[vd1], s[40:43], %[xo] offen offset:3072 lds\n\tv_mfma_f32_16x16x32_bf16 v[48:51], a[104:107], v[116:119], v[48:51]\n\t" \
          "ds_read_b128 v[84:87], %[ra1] offset:1280\n\tv_mfma_f32_16x16x32_bf16 v[52:55], a[108:111], v[116:119], v[52:55]\n\ts_waitcnt lgkmcnt(6)\n\tv_mfma_f32_16x16x32_bf16 v[48:51], a[112:115], v[120:123], v[48:51]\n\tds_read_b128 v[88:91], %[ra2] offset:1280\n\ts_add_u32 m0, %[sl], 4096\n\t" \
          "v_mfma_f32_16x16x32_bf16 v[52:55], a[116:119], v[120:123], v[52:55]\n\tbuffer_load_dwordx4 %[vd2], s[40:43], %[xo] offen lds\n\tv_mfma_f32_16x16x32_bf16 v[48:51], a[120:123], v[124:127], v[48:51]\n\tds_read_b128 v[92:95], %[ra3] offset:1280\n\tv_mfma_f32_16x16x32_bf16 v[52:55], a[124:127], v[124:127], v[52:55]\n\ts_waitcnt lgkmcnt(6)\n\t" \
          "v_mfma_f32_16x16x32_bf16 v[48:51], a[128:131], v[64:67], v[48:51]\n\tds_read_b128 v[96:99], %[ra0] offset:1536\n\tv_mfma_f32_16x16x32_bf16 v[52:55], a[132:135], v[64:67], v[52:55]\n\tbuffer_load_dwordx4 %[vd2], s[40:43], %[xo] offen offset:1024 lds\n\tv_mfma_f32_16x16x32_bf16 v[48:51], a[136:139], v[68:71], v[48:51]\n\tds_read_b128 v[100:103], %[ra1] offset:1536\n\t" \
          "v_mfma_f32_16x16x32_bf16 v[52:55], a[140:143], v[68:71], v[52:55]\n\ts_waitcnt lgkmcnt(6)\n\tv_mfma_f32_16x16x32_bf16 v[48:51], a[144:147], v[72:75], v[48:51]\n\tds_read_b128 v[104:107], %[ra2] offset:1536\n\tv_mfma_f32_16x16x32_bf16 v[52:55], a[148:151], v[72:75], v[52:55]\n\tbuffer_load_dwordx4 %[vd3], s[40:43], %[xo] offen offset:2048 lds\n\t" \
          "v_mfma_f32_16x16x32_bf16 v[48:51], a[152:155], v[76:79], v[48:51]\n\tds_read_b128 v[108:111], %[ra3] offset:1536\n\tv_mfma_f32_16x16x32_bf16 v[52:55], a[156:159], v[76:79], v[52:55]\n\ts_waitcnt lgkmcnt(6)\n\tv_mfma_f32_16x16x32_bf16 v[48:51], a[160:163], v[80:83], v[48:51]\n\tds_read_b128 v[112:115], %[ra0] offset:1792\n\t" \
          "v_mfma_f32_16x16x32_bf16 v[52:55], a[164:167], v[80:83], v[52:55]\n\tbuffer_load_dwordx4 %[vd3], s[40:43], %[xo] offen offset:3072 lds\n\tv_mfma_f32_16x16x32_bf16 v[48:51], a[168:171], v[84:87], v[48:51]\n\tds_read_b128 v[116:119], %[ra1] offset:1792\n\tv_mfma_f32_16x16x32_bf16 v[52:55], a[172:175], v[84:87], v[52:55]\n\ts_waitcnt lgkmcnt(6)\n\t" \
          "v_mfma_f32_16x16x32_bf16 v[48:51], a[176:179], v[88:91], v[48:51]\n\tds_read_b128 v[120:123], %[ra2] offset:1792\n\tv_mfma_f32_16x16x32_bf16 v[52:55], a[180:183], v[88:91], v[52:55]\n\tv_mfma_f32_16x16x32_bf16 v[48:51], a[184:187], v[92:95], v[48:51]\n\tds_read_b128 v[124:127], %[ra3] offset:1792\n\tv_mfma_f32_16x16x32_bf16 v[52:55], a[188:191], v[92:95], v[52:55]\n\t" \
          "s_add_u32 %[xo], %[xo], 0x8000\n\ts_add_u32 %[sl], %[sl], 0x8000\n\ts_bitset0_b32 %[sl], 17\n\ts_waitcnt lgkmcnt(6)\n\tv_mfma_f32_16x16x32_bf16 v[48:51], a[192:195], v[96:99], v[48:51]\n\tv_mfma_f32_16x16x32_bf16 v[52:55], a[196:199], v[96:99], v[52:55]\n\t" \
          "v_mfma_f32_16x16x32_bf16 v[48:51], a[200:203], v[100:103], v[48:51]\n\tv_mfma_f32_16x16x32_bf16 v[52:55], a[204:207], v[100:103], v[52:55]\n\ts_waitcnt lgkmcnt(4)\n\tv_mfma_f32_16x16x32_bf16 v[48:51], a[208:211], v[104:107], v[48:51]\n\tv_mfma_f32_16x16x32_bf16 v[52:55], a[212:215], v[104:107], v[52:55]\n\tv_mfma_f32_16x16x32_bf16 v[48:51], a[216:219], v[108:111], v[48:51]\n\t" \
          "v_mfma_f32_16x16x32_bf16 v[52:55], a[220:223], v[108:111], v[52:55]\n\ts_waitcnt vmcnt(18) lgkmcnt(0)\n\ts_barrier\n\tv_mfma_f32_16x16x32_bf16 v[48:51], a[224:227], v[112:115], v[48:51]\n\tds_read_b128 v[64:67], %[ra0] offset:32768\n\tv_mfma_f32_16x16x32_bf16 v[52:55], a[228:231], v[112:115], v[52:55]\n\t" \
          "ds_read_b128 v[68:71], %[ra1] offset:32768\n\tv_mfma_f32_16x16x32_bf16 v[48:51], a[232:235], v[116:119], v[48:51]\n\tds_read_b128 v[72:75], %[ra2] offset:32768\n\tv_mfma_f32_16x16x32_bf16 v[52:55], a[236:239], v[116:119], v[52:55]\n\tds_read_b128 v[76:79], %[ra3] offset:32768\n\tv_mfma_f32_16x16x32_bf16 v[48:51], a[240:243], v[120:123], v[48:51]\n\t" \
          "ds_read_b128 v[80:83], %[ra0] offset:33024\n\tv_mfma_f32_16x16x32_bf16 v[52:55], a[244:247], v[120:123], v[52:55]\n\tds_read_b128 v[84:87], %[ra1] offset:33024\n\tv_mfma_f32_16x16x32_bf16 v[48:51], a[248:251], v[124:127], v[48:51]\n\tds_read_b128 v[88:91], %[ra2] offset:33024\n\tv_mfma_f32_16x16x32_bf16 v[52:55], a[252:255], v[124:127], v[52:55]\n\t" \
          "ds_read_b128 v[92:95], %[ra3] offset:33024\n\ts_waitcnt lgkmcnt(6)\n\tv_mfma_f32_16x16x32_bf16 v[56:59], a[0:3], v[64:67], 0\n\tds_read_b128 v[96:99], %[ra0] offset:33280\n\tv_mfma_f32_16x16x32_bf16 v[60:63], a[4:7], v[64:67], 0\n\ts_add_u32 %[t], %[j], 4\n\t" \
          "s_cmp_lt_u32 %[t], %[je]\n\ts_cselect_b32 s42, s42, 0\n\tv_mfma_f32_16x16x32_bf16 v[56:59], a[8:11], v[68:71], v[56:59]\n\tds_read_b128 v[100:103], %[ra1] offset:33280\n\tv_mfma_f32_16x16x32_bf16 v[60:63], a[12:15], v[68:71], v[60:63]\n\tv_cvt_pk_bf16_f32 v44, v48, v49\n\t" \
          "v_cvt_pk_bf16_f32 v45, v50, v51\n\ts_waitcnt lgkmcnt(6)\n\tv_mfma_f32_16x16x32_bf16 v[56:59], a[16:19], v[72:75], v[56:59]\n\tds_read_b128 v[104:107], %[ra2] offset:33280\n\tv_mfma_f32_16x16x32_bf16 v[60:63], a[20:23], v[72:75], v[60:63]\n\tv_cvt_pk_bf16_f32 v46, v52, v53\n\t" \
          "v_cvt_pk_bf16_f32 v47, v54, v55\n\tv_mfma_f32_16x16x32_bf16 v[56:59], a[24:27], v[76:79], v[56:59]\n\tds_read_b128 v[108:111], %[ra3] offset:33280\n\tv_mfma_f32_16x16x32_bf16 v[60:63], a[28:31], v[76:79], v[60:63]\n\tbuffer_store_dwordx4 v[44:47], %[so], %[rc], %[co] offen sc1\n\ts_waitcnt lgkmcnt(6)\n\t" \
          "v_mfma_f32_16x16x32_bf16 v[56:59], a[32:35], v[80:83], v[56:59]\n\tds_read_b128 v[112:115], %[ra0] offset:33536\n\tv_mfma_f32_16x16x32_bf16 v[60:63], a[36:39], v[80:83], v[60:63]\n\tv_mov_b32 %[fv], %[j]\n\tv_mfma_f32_16x16x32_bf16 v[56:59], a[40:43], v[84:87], v[56:59]\n\tds_read_b128 v[116:119], %[ra1] offset:33536\n\t" \
          "v_mfma_f32_16x16x32_bf16 v[60:63], a[44:47], v[84:87], v[60:63]\n\ts_add_u32 %[co], %[co], 0x4000\n\ts_waitcnt lgkmcnt(6)\n\tv_mfma_f32_16x16x32_bf16 v[56:59], a[48:51], v[88:91], v[56:59]\n\tds_read_b128 v[120:123], %[ra2] offset:33536\n\ts_add_u32 m0, %[sl], 0\n\t" \
          "v_mfma_f32_16x16x32_bf16 v[60:63], a[52:55], v[88:91], v[60:63]\n\tbuffer_load_dwordx4 %[vd0], s[40:43], %[xo] offen lds\n\tv_mfma_f32_16x16x32_bf16 v[56:59], a[56:59], v[92:95], v[56:59]\n\tds_read_b128 v[124:127], %[ra3] offset:33536\n\tv_mfma_f32_16x16x32_bf16 v[60:63], a[60:63], v[92:95], v[60:63]\n\ts_waitcnt lgkmcnt(6)\n\t" \
          "v_mfma_f32_16x16x32_bf16 v[56:59], a[64:67], v[96:99], v[56:59]\n\tds_read_b128 v[64:67], %[ra0] offset:33792\n\tv_mfma_f32_16x16x32_bf16 v[60:63], a[68:71], v[96:99], v[60:63]\n\tbuffer_load_dwordx4 %[vd0], s[40:43], %[xo] offen offset:1024 lds\n\tv_mfma_f32_16x16x32_bf16 v[56:59], a[72:75], v[100:103], v[56:59]\n\tds_read_b128 v[68:71], %[ra1] offset:33792\n\t" \
          "v_mfma_f32_16x16x32_bf16 v[60:63], a[76:79], v[100:103], v[60:63]\n\ts_waitcnt lgkmcnt(6)\n\tv_mfma_f32_16x16x32_bf16 v[56:59], a[80:83], v[104:107], v[56:59]\n\tds_read_b128 v[72:75], %[ra2] offset:33792\n\tv_mfma_f32_16x16x32_bf16 v[60:63], a[84:87], v[104:107], v[60:63]\n\tbuffer_load_dwordx4 %[vd1], s[40:43], %[xo] offen offset:2048 lds\n\t" \
          "v_mfma_f32_16x16x32_bf16 v[56:59], a[88:91], v[108:111], v[56:59]\n\tds_read_b128 v[76:79], %[ra3] offset:33792\n\tv_mfma_f32_16x16x32_bf16 v[60:63], a[92:95], v[108:111], v[60:63]\n\ts_waitcnt lgkmcnt(6)\n\tv_mfma_f32_16x16x32_bf16 v[56:59], a[96:99], v[112:115], v[56:59]\n\tds_read_b128 v[80:83], %[ra0] offset:34048\n\t" \
          "v_mfma_f32_16x16x32_bf16 v[60:63], a[100:103], v[112:115], v[60:63]\n\tbuffer_load_dwordx4 %[vd1], s[40:43], %[xo] offen offset:3072 lds\n\tv_mfma_f32_16x16x32_bf16 v[56:59], a[104:107], v[116:119], v[56:59]\n\tds_read_b128 v[84:87], %[ra1] offset:34048\n\tv_mfma_f32_16x16x32_bf16 v[60:63], a[108:111], v[116:119], v[60:63]\n\ts_waitcnt lgkmcnt(6)\n\t" \
          "v_mfma_f32_16x16x32_bf16 v[56:59], a[112:115], v[120:123], v[56:59]\n\tds_read_b128 v[88:91], %[ra2] offset:34048\n\ts_add_u32 m0, %[sl], 4096\n\tv_mfma_f32_16x16x32_bf16 v[60:63], a[116:119], v[120:123], v[60:63]\n\tbuffer_load_dwordx4 %[vd2], s[40:43], %[xo] offen lds\n\tv_mfma_f32_16x16x32_bf16 v[56:59], a[120:123], v[124:127], v[56:59]\n\t" \
          "ds_read_b128 v[92:95], %[ra3] offset:34048\n\tv_mfma_f32_16x16x32_bf16 v[60:63], a[124:127], v[124:127], v[60:63]\n\ts_waitcnt lgkmcnt(6)\n\tv_mfma_f32_16x16x32_bf16 v[56:59], a[128:131], v[64:67], v[56:59]\n\tds_read_b128 v[96:99], %[ra0] offset:34304\n\tv_mfma_f32_16x16x32_bf16 v[60:63], a[132:135], v[64:67], v[60:63]\n\t" \
          "buffer_load_dwordx4 %[vd2], s[40:43], %[xo] offen offset:1024 lds\n\tv_mfma_f32_16x16x32_bf16 v[56:59], a[136:139], v[68:71], v[56:59]\n\tds_read_b128 v[100:103], %[ra1] offset:34304\n\tv_mfma_f32_16x16x32_bf16 v[60:63], a[140:143], v[68:71], v[60:63]\n\ts_waitcnt lgkmcnt(6)\n\tv_mfma_f32_16x16x32_bf16 v[56:59], a[144:147], v[72:75], v[56:59]\n\t" \
          "ds_read_b128 v[104:107], %[ra2] offset:34304\n\tv_mfma_f32_16x16x32_bf16 v[60:63], a[148:151], v[72:75], v[60:63]\n\tbuffer_load_dwordx4 %[vd3], s[40:43], %[xo] offen offset:2048 lds\n\tv_mfma_f32_16x16x32_bf16 v[56:59], a[152:155], v[76:79], v[56:59]\n\tds_read_b128 v[108:111], %[ra3] offset:34304\n\tv_mfma_f32_16x16x32_bf16 v[60:63], a[156:159], v[76:79], v[60:63]\n\t" \
          "s_waitcnt lgkmcnt(6)\n\tv_mfma_f32_16x16x32_bf16 v[56:59], a[160:163], v[80:83], v[56:59]\n\tds_read_b128 v[112:115], %[ra0] offset:34560\n\tv_mfma_f32_16x16x32_bf16 v[60:63], a[164:167], v[80:83], v[60:63]\n\tbuffer_load_dwordx4 %[vd3], s[40:43], %[xo] offen offset:3072 lds\n\tv_mfma_f32_16x16x32_bf16 v[56:59], a[168:171], v[84:87], v[56:59]\n\t" \
          "ds_read_b128 v[116:119], %[ra1] offset:34560\n\tv_mfma_f32_16x16x32_bf16 v[60:63], a[172:175], v[84:87], v[60:63]\n\ts_waitcnt lgkmcnt(6)\n\tv_mfma_f32_16x16x32_bf16 v[56:59], a[176:179], v[88:91], v[56:59]\n\tds_read_b128 v[120:123], %[ra2] offset:34560\n\tv_mfma_f32_16x16x32_bf16 v[60:63], a[180:183], v[88:91], v[60:63]\n\t" \
          "v_mfma_f32_16x16x32_bf16 v[56:59], a[184:187], v[92:95], v[56:59]\n\tds_read_b128 v[124:127], %[ra3] offset:34560\n\tv_mfma_f32_16x16x32_bf16 v[60:63], a[188:191], v[92:95], v[60:63]\n\ts_add_u32 %[xo], %[xo], 0x8000\n\ts_add_u32 %[sl], %[sl], 0x8000\n\ts_bitset0_b32 %[sl], 17\n\t" \
          "s_waitcnt lgkmcnt(6)\n\tv_mfma_f32_16x16x32_bf16 v[56:59], a[192:195], v[96:99], v[56:59]\n\tv_mfma_f32_16x16x32_bf16 v[60:63], a[196:199], v[96:99], v[60:63]\n\tv_xor_b32 %[ra0], 0x10000, %[ra0]\n\tv_mfma_f32_16x16x32_bf16 v[56:59], a[200:203], v[100:103], v[56:59]\n\tv_mfma_f32_16x16x32_bf16 v[60:63], a[204:207], v[100:103], v[60:63]\n\t" \
          "v_xor_b32 %[ra1], 0x10000, %[ra1]\n\ts_waitcnt lgkmcnt(4)\n\tv_mfma_f32_16x16x32_bf16 v[56:59], a[208:211], v[104:107], v[56:59]\n\tv_mfma_f32_16x16x32_bf16 v[60:63], a[212:215], v[104:107], v[60:63]\n\tv_xor_b32 %[ra2], 0x10000, %[ra2]\n\tv_mfma_f32_16x16x32_bf16 v[56:59], a[216:219], v[108:111], v[56:59]\n\t" \
          "s_add_u32 %[j], %[j], 2\n\ts_cmp_lt_u32 %[j], %[je]\n\tv_mfma_f32_16x16x32_bf16 v[60:63], a[220:223], v[108:111], v[60:63]\n\tv_xor_b32 %[ra3], 0x10000, %[ra3]\n\ts_waitcnt vmcnt(17) lgkmcnt(0)\n\ts_barrier\n\t" \
          "v_mfma_f32_16x16x32_bf16 v[56:59], a[224:227], v[112:115], v[56:59]\n\tds_read_b128 v[64:67], %[ra0] offset:0\n\tv_mfma_f32_16x16x32_bf16 v[60:63], a[228:231], v[112:115], v[60:63]\n\tds_read_b128 v[68:71], %[ra1] offset:0\n\tv_mfma_f32_16x16x32_bf16 v[56:59], a[232:235], v[116:119], v[56:59]\n\tds_read_b128 v[72:75], %[ra2] offset:0\n\t" \
          "v_mfma_f32_16x16x32_bf16 v[60:63], a[236:239], v[116:119], v[60:63]\n\tds_read_b128 v[76:79], %[ra3] offset:0\n\tv_mfma_f32_16x16x32_bf16 v[56:59], a[240:243], v[120:123], v[56:59]\n\tds_read_b128 v[80:83], %[ra0] offset:256\n\tv_mfma_f32_16x16x32_bf16 v[60:63], a[244:247], v[120:123], v[60:63]\n\tds_read_b128 v[84:87], %[ra1] offset:256\n\t" \
          "v_mfma_f32_16x16x32_bf16 v[56:59], a[248:251], v[124:127], v[56:59]\n\tds_read_b128 v[88:91], %[ra2] offset:256\n\tv_mfma_f32_16x16x32_bf16 v[60:63], a[252:255], v[124:127], v[60:63]\n\tds_read_b128 v[92:95], %[ra3] offset:256\n\ts_cbranch_scc1 Lpn_loop%=\n\ts_nop 7\n\t" \
          "v_mov_b32 %[fv], %[je]\n\tv_cvt_pk_bf16_f32 v44, v56, v57\n\tv_cvt_pk_bf16_f32 v45, v58, v59\n\tv_cvt_pk_bf16_f32 v46, v60, v61\n\tv_cvt_pk_bf16_f32 v47, v62, v63\n\tbuffer_store_dwordx4 v[44:47], %[so], %[rc], %[co] offen sc1\n\t" \
          "s_waitcnt vmcnt(0) lgkmcnt(0)\n\ts_barrier\n\ts_mov_b64 exec, %[fx]\n\tbuffer_store_dword %[fv], off, %[rf], %[fo] sc1\n\ts_mov_b64 exec, -1\n\t"
#define PN_CLOB "v44", "v45", "v46", "v47", "v48", "v49", "v50", "v51", "v52", "v53", "v54", "v55", "v56", "v57", "v58", "v59", "v60", "v61", "v62", "v63", "v64", "v65", "v66", "v67", "v68", "v69", "v70", "v71", "v72", "v73", "v74", "v75", "v76", "v77", "v78", "v79", "v80", "v81", "v82", "v83", "v84", "v85", "v86", "v87", "v88", "v89", "v90", "v91", "v92", "v93", "v94", "v95", "v96", "v97", "v98", "v99", "v100", "v101", "v102", "v103", "v104", "v105", "v106", "v107", "v108", "v109", "v110", "v111", "v112", "v113", "v114", "v115", "v116", "v117", "v118", "v119", "v120", "v121", "v122", "v123", "v124", "v125", "v126", "v127", "a0", "a1", "a2", "a3", "a4", "a5", "a6", "a7", "a8", "a9", "a10", "a11", "a12", "a13", "a14", "a15", "a16", "a17", "a18", "a19", "a20", "a21", "a22", "a23", "a24", "a25", "a26", "a27", "a28", "a29", "a30", "a31", "a32", "a33", "a34", "a35", "a36", "a37", "a38", "a39", "a40", "a41", "a42", "a43", "a44", "a45", "a46", "a47", "a48", "a49", "a50", "a51", "a52", "a53", "a54", "a55", "a56", "a57", "a58", "a59", "a60", "a61", "a62", "a63", "a64", "a65", "a66", "a67", "a68", "a69", "a70", "a71", "a72", "a73", "a74", "a75", "a76", "a77", "a78", "a79", "a80", "a81", "a82", "a83", "a84", "a85", "a86", "a87", "a88", "a89", "a90", "a91", "a92", "a93", "a94", "a95", "a96", "a97", "a98", "a99", "a100", "a101", "a102", "a103", "a104", "a105", "a106", "a107", "a108", "a109", "a110", "a111", "a112", "a113", "a114", "a115", "a116", "a117", "a118", "a119", "a120", "a121", "a122", "a123", "a124", "a125", "a126", "a127", "a128", "a129", "a130", "a131", "a132", "a133", "a134", "a135", "a136", "a137", "a138", "a139", "a140", "a141", "a142", "a143", "a144", "a145", "a146", "a147", "a148", "a149", "a150", "a151", "a152", "a153", "a154", "a155", "a156", "a157", "a158", "a159", "a160", "a161", "a162", "a163", "a164", "a165", "a166", "a167", "a168", "a169", "a170", "a171", "a172", "a173", "a174", "a175", "a176", "a177", "a178", "a179", "a180", "a181", "a182", "a183", "a184", "a185", "a186", "a187", "a188", "a189", "a190", "a191", "a192", "a193", "a194", "a195", "a196", "a197", "a198", "a199", "a200", "a201", "a202", "a203", "a204", "a205", "a206", "a207", "a208", "a209", "a210", "a211", "a212", "a213", "a214", "a215", "a216", "a217", "a218", "a219", "a220", "a221", "a222", "a223", "a224", "a225", "a226", "a227", "a228", "a229", "a230", "a231", "a232", "a233", "a234", "a235", "a236", "a237", "a238", "a239", "a240", "a241", "a242", "a243", "a244", "a245", "a246", "a247", "a248", "a249", "a250", "a251", "a252", "a253", "a254", "a255", "s40", "s41", "s42", "s43", "s44", "s45"
#define NTP 0
template <bool NT>
__device__ __forceinline__ v4u panel_task(const FParams& F, int p, int fi, int j, int je, int w) {
  const int lane = opaque_lane(), l15 = lane & 15, lq = lane >> 4, m = p >> 5, ldb = __builtin_amdgcn_readfirstlane(m < 3 ? HDA * 2 : NQKVB * 2);
  const __amdgpu_buffer_rsrc_t rw = __builtin_amdgcn_make_buffer_rsrc(wg::uni((m == 0 ? F.Wq : (m == 1 ? F.Wk : (m == 2 ? F.Wv : F.wqkvB))) + (p & 31) * 128), 0, DM * ldb - (p & 31) * 256, 0x00020000);
  const __amdgpu_buffer_rsrc_t rf = __builtin_amdgcn_make_buffer_rsrc(F.ctl, 0, sizeof(Ctl), 0x00020000), rr = __builtin_amdgcn_make_buffer_rsrc(F.rdy, 0, RDY_SIZE, 0x00020000);
  void* const xa = wg::uni(m < 3 ? F.Ax : F.Bx);
  int ra[4], vd[4], rt[4], vw[2], fv = 0, xo = w * 8192 + j * 32768, sl = w * 8192, co = j * QBLK, cs = 0, t; unsigned long long sx;
  v4u rec;
#pragma unroll
  for (int i = 0; i < 4; ++i) {
    ra[i] = l15 * 2048 + (((4 * i + lq) ^ l15) << 4); vd[i] = ((lane ^ (4 * w + i)) << 4) + (i >> 1) * 4096;
    rt[i] = 65536 + (i >> 1) * 49152 + (8 * lq + (l15 >> 2)) * 256 + (((2 * w + (i & 1)) ^ ((l15 >> 2) | ((lq & 1) << 2))) << 5) + (l15 & 3) * 8;
  }
  vw[0] = (16 * w + lq) * ldb + ((l15 ^ (lq << 1)) << 4); vw[1] = vw[0] ^ 128;
  const __amdgpu_buffer_rsrc_t rc = __builtin_amdgcn_make_buffer_rsrc((char*)F.qkvA + __builtin_amdgcn_readfirstlane((p >> 1) * QPAGE + (p & 1) * 4096 + w * 1024), 0, QPAGE - 8192 + 1024, 0x00020000);
#define PN_OPS : "=&{v[40:43]}"(rec), [ra0] "+v"(ra[0]), [ra1] "+v"(ra[1]), [ra2] "+v"(ra[2]), [ra3] "+v"(ra[3]), [fv] "+v"(fv), [xo] "+s"(xo), [sl] "+s"(sl), [co] "+s"(co), [cs] "+s"(cs), [t] "=&s"(t), [j] "+s"(j), [sx] "=&s"(sx) \
    : [vd0] "v"(vd[0]), [vd1] "v"(vd[1]), [vd2] "v"(vd[2]), [vd3] "v"(vd[3]), [rt0] "v"(rt[0]), [rt1] "v"(rt[1]), [rt2] "v"(rt[2]), [rt3] "v"(rt[3]), [vw0] "v"(vw[0]), [vw1] "v"(vw[1]), [ro] "v"(lane * 128), [so] "v"(lane * 16), \
      [xa] "s"(xa), [rw] "s"(rw), [rr] "s"(rr), [rc] "s"(rc), [rf] "s"(rf), [wl] "s"(w * 4096), [ldb] "s"(ldb), [fo] "s"((int)offsetof(Ctl, prg) + fi * 128), [je] "s"(je), \
      [fx] "s"((unsigned long long)(unsigned)__builtin_amdgcn_readfirstlane(w == 0)), [pl] "s"(p >> 1) : PN_CLOB, "vcc", "scc", "memory"
  if constexpr (NT) asm volatile(PN_BODY(" nt") PN_OPS); else asm volatile(PN_BODY("") PN_OPS);
  return rec;
}

struct AttnX { v4u q[2][4], kx[2][4], vr[8]; };
template <int AUX = 0>
__device__ __forceinline__ void attnA_load_x(const FParams& F, unsigned long long parq, int seg, int h, int lane, AttnX& X) {
  const __amdgpu_buffer_rsrc_t rs = __builtin_amdgcn_make_buffer_rsrc(F.qkvA, 0, QSIZE, 0x00020000);
  const int so = seg * 2 * QBLK + (h & 1) * 4096, oq = so + qoff(parq, h >> 1), ok = so + qoff(parq, 16 + (h >> 1)), ov = so + qoff(parq, 32 + (h >> 1));
#pragma unroll
  for (int i = 0; i < 8; ++i) X.vr[i] = __builtin_amdgcn_raw_buffer_load_b128(rs, lane * 16 + (i & 3) * 1024, ov + (i >> 2) * QBLK, AUX);
#pragma unroll
  for (int t = 0; t < 2; ++t)
#pragma unroll
    for (int j = 0; j < 4; ++j) {
      X.q[t][j] = __builtin_amdgcn_raw_buffer_load_b128(rs, lane * 16 + j * 1024, oq + t * QBLK, AUX);
      X.kx[t][j] = __builtin_amdgcn_raw_buffer_load_b128(rs, lane * 16 + j * 1024, ok + t * QBLK, AUX);
    }
}
__device__ __forceinline__ void attnA_stage_v(const AttnX& X, u16* vs  , int lane) {
#pragma unroll
  for (int i = 0; i < 8; ++i) *(v4u*)&vs[(16 * (i >> 2) + (lane & 15)) * VXLD + 32 * (i & 3) + 8 * (lane >> 4)] = X.vr[i];
}
template <int WHAT = 3>
__device__ __forceinline__ unsigned attnA_load_m(const FParams& F, int seg, int h, int lane, v4u (&km)[4], unsigned (&vm)[8]) {
  const int l15 = lane & 15, lq = lane >> 4;
  const __amdgpu_buffer_rsrc_t rk = __builtin_amdgcn_make_buffer_rsrc(F.kvm + (size_t)seg * KVSEG + h * 4096, 0, 4096, 0x00020000);
#pragma unroll
  for (int j = 0; j < 4; ++j)
    if (WHAT & 1) km[j] = __builtin_amdgcn_raw_buffer_load_b128(rk, (2 * (lane >> 2) + (lane & 1)) * 16 + 512 * j, 0, 16);
#pragma unroll
  for (int t = 0; t < 8; ++t)
    if (WHAT & 2) vm[t] = __builtin_amdgcn_raw_buffer_load_b32(rk, 2048 + (((l15 >> 3) * 4 + lq) * 16 + 8 * ((l15 >> 2) & 1) + (l15 & 3)) * 4 + 512 * (t >> 1) + 16 * (t & 1), 0, 16);
  unsigned m = 0;
#pragma unroll
  for (int j = 0; j < 4; ++j) if (WHAT & 1) m = chk4(m, km[j]);
#pragma unroll
  for (int t = 0; t < 8; t += 2) if (WHAT & 2) m = umax3(m, vm[t], vm[t + 1]);
  return m;
}
__device__ __forceinline__ void attnA_compute(const AttnX& X, const v4u (&km)[4], const unsigned (&vm)[8], const u16* vs, int lane, v4u (&ov)[2][4]) {
  const int l15 = lane & 15, lq = lane >> 4;
  const float scale = 0.08838834764831845f, L2E = 1.4426950408889634f;
  v4u px[2], pm[2];
#pragma unroll
  for (int t = 0; t < 2; ++t) {
    f32x4 s0 = {0.f,0.f,0.f,0.f}, s1 = s0, s2 = s0;
#pragma unroll
    for (int j = 0; j < 4; ++j) { s0 = mf(km[j], X.q[t][j], s0); s1 = mf(X.kx[0][j], X.q[t][j], s1); if (t == 1) s2 = mf(X.kx[1][j], X.q[t][j], s2); }
    float sv[12];
#pragma unroll
    for (int e = 0; e < 4; ++e) { sv[e] = s0[e]; sv[4 + e] = s1[e]; sv[8 + e] = s2[e]; }
#pragma unroll
    for (int e = 0; e < 12; e += 2) {
      unsigned p = pk2(sv[e], sv[e + 1]);
      p = pk2(__uint_as_float(p << 16) * scale, __uint_as_float(p & 0xffff0000u) * scale);
      sv[e] = __uint_as_float(p << 16); sv[e + 1] = __uint_as_float(p & 0xffff0000u);
    }
    bool ok[12];
#pragma unroll
    for (int e = 0; e < 4; ++e) { ok[e] = e < 2; ok[4 + e] = (t == 1) || (4 * lq + e <= l15); ok[8 + e] = (t == 1) && (4 * lq + e <= l15); }
    float mx = -INFINITY;
#pragma unroll
    for (int e = 0; e < 12; ++e) mx = ok[e] ? fmaxf(mx, sv[e]) : mx;
    mx = xmax32(xmax16(mx));
    float sum = 0.f;
#pragma unroll
    for (int e = 0; e < 12; ++e) { sv[e] = ok[e] ? __builtin_amdgcn_exp2f((sv[e] - mx) * L2E) : 0.f; sum += sv[e]; }
    sum = xsum32(xsum16(sum));
    const float inv = __builtin_amdgcn_rcpf(sum);
#pragma unroll
    for (int e = 0; e < 12; ++e) sv[e] = ok[e] ? sv[e] * inv : 0.f;
    px[t] = (v4u){pk2(sv[4], sv[5]), pk2(sv[6], sv[7]), pk2(sv[8], sv[9]), pk2(sv[10], sv[11])};
    pm[t] = (v4u){pk2(sv[0], sv[1]), 0u, 0u, 0u};
  }
  const u16* vp = &vs[(4 * lq + (l15 >> 2)) * VXLD + 16 * (l15 & 1) + 2 * (l15 & 2)];
#pragma unroll
  for (int u = 0; u < 4; ++u) {
    f32x4 o[2][2];
#pragma unroll
    for (int ab = 0; ab < 2; ++ab) {
      v2u lo = trrd(vp + 32 * u + 8 * ab), hi = trrd(vp + 16 * VXLD + 32 * u + 8 * ab);
      const v4u va = {lo.x, lo.y, hi.x, hi.y}, vb = {vm[2 * u + ab], 0u, 0u, 0u};
#pragma unroll
      for (int t = 0; t < 2; ++t) { o[t][ab] = mf(va, px[t], (f32x4){0.f,0.f,0.f,0.f}); o[t][ab] = mf(vb, pm[t], o[t][ab]); }
    }
#pragma unroll
    for (int t = 0; t < 2; ++t)
      ov[t][u] = (v4u){pk2(o[t][0][0], o[t][0][1]), pk2(o[t][0][2], o[t][0][3]), pk2(o[t][1][0], o[t][1][1]), pk2(o[t][1][2], o[t][1][3])};
  }
}
__device__ __forceinline__ void attnA_block(const FParams& F, unsigned long long parq, int r, int h, u16* vs, int lane, int w, unsigned* shw) {
  AttnX X[2]; v4u km[2][4]; unsigned vm[2][8];
  unsigned seen = 0;
  attnA_load_x(F, parq, 4 * r, h, lane, X[0]);
  bool have = false;
#pragma unroll
  for (int i = 0; i < 4; ++i) {
    const int seg = 4 * r + i, c = i & 1;
    v4u ov[2][4];
    if (!have) {
      if (seen < (unsigned)seg + 1) {
        if (w == 0) {
          for (unsigned spins = 0;; ++spins) {
            seen = __builtin_amdgcn_readfirstlane(ld_agent(&F.ctl->prog[0]));
            if (seen >= (unsigned)seg + 1 || spin_fail(spins, &F.ctl->tmo[0], 6u)) break;
            for (unsigned z = seen; z < (unsigned)seg + 1; ++z) __builtin_amdgcn_s_sleep(PSL);
          }
          *shw = seen;
        }
        __syncthreads();
        seen = __builtin_amdgcn_readfirstlane(*shw);
        __syncthreads();
      }
      asm volatile("" ::: "memory");
      (void)attnA_load_m(F, seg, h, lane, km[c], vm[c]);
    }
    if (i < 3) {
      attnA_load_x(F, parq, seg + 1, h, lane, X[c ^ 1]);
      have = seen >= (unsigned)seg + 2;
      (void)attnA_load_m(F, have ? seg + 1 : seg, h, lane, km[c ^ 1], vm[c ^ 1]);
    }
    SB0;
    attnA_stage_v(X[c], vs, lane);
    attnA_compute(X[c], km[c], vm[c], vs, lane, ov);
    const __amdgpu_buffer_rsrc_t rs = __builtin_amdgcn_make_buffer_rsrc(F.qkvA, 0, QSIZE, 0x00020000);
#pragma unroll
    for (int u = 0; u < 4; ++u)
#pragma unroll
      for (int t = 0; t < 2; ++t)
        __builtin_amdgcn_raw_buffer_store_b128(ov[t][u], rs, lane * 16 + u * 1024, qoff(parq, h >> 1) + (seg * 2 + t) * QBLK + (h & 1) * 4096, 16);
    SB0;
  }
}

constexpr int TSEG0 = 32, OSEG0 = 16, NSET = 4;
__device__ __forceinline__ void attn_stream(const FParams& F, unsigned long long parq, int set, int hg, char* lds, int w) {
  const int lane = opaque_lane(), l15 = lane & 15, lq = lane >> 4, h = 4 * hg + w;
  const float L2E = 1.4426950408889634f;
  auto rnd2 = [](float& a, float& b) { const float scale = 0.08838834764831845f; unsigned p = pk2(a, b); p = pk2(__uint_as_float(p << 16) * scale, __uint_as_float(p & 0xffff0000u) * scale); a = __uint_as_float(p << 16); b = __uint_as_float(p & 0xffff0000u); };
  Ctl* ctl = F.ctl;
  for (unsigned spins = 0;; ++spins) {
    if (__all(ld_agent(&ctl->prg[(lane < 2 ? lane : 3) * 32 + h][0]) >= 128u) || spin_fail(spins, &ctl->tmo[0], 2u)) break;
    __builtin_amdgcn_s_sleep(16);
  }
  asm volatile("" ::: "memory");
  u16* vs = (u16*)(lds + w * 16384);
  AttnX X; int seen = 0;
  attnA_load_x<16>(F, parq, TSEG0 + set, h, lane, X);
#pragma unroll 1
  for (int seg = TSEG0 + set; seg < NSEG; seg += NSET) {
    v4u km[4]; unsigned vm[8]; f32x4 o[2][8]; float mx[2], ls[2];
    attnA_stage_v(X, vs, lane);
    { v4u px[2];
#pragma unroll
      for (int t = 0; t < 2; ++t) {
        f32x4 s1 = {0.f, 0.f, 0.f, 0.f}, s2 = s1;
#pragma unroll
        for (int j = 0; j < 4; ++j) { s1 = mf(X.kx[0][j], X.q[t][j], s1); if (t == 1) s2 = mf(X.kx[1][j], X.q[t][j], s2); }
        float sv[8]; bool ok[8];
#pragma unroll
        for (int e = 0; e < 4; ++e) { sv[e] = s1[e]; sv[4 + e] = s2[e]; ok[e] = (t == 1) || (4 * lq + e <= l15); ok[4 + e] = (t == 1) && (4 * lq + e <= l15); }
#pragma unroll
        for (int e = 0; e < 8; e += 2) rnd2(sv[e], sv[e + 1]);
        float m = -INFINITY, sum = 0.f;
#pragma unroll
        for (int e = 0; e < 8; ++e) m = ok[e] ? fmaxf(m, sv[e]) : m;
        m = xmax32(xmax16(m));
#pragma unroll
        for (int e = 0; e < 8; ++e) { sv[e] = ok[e] ? __builtin_amdgcn_exp2f((sv[e] - m) * L2E) : 0.f; sum += sv[e]; }
        mx[t] = m; ls[t] = sum;
        px[t] = (v4u){pk2(sv[0], sv[1]), pk2(sv[2], sv[3]), pk2(sv[4], sv[5]), pk2(sv[6], sv[7])};
      }
      const u16* vp = &vs[(4 * lq + (l15 >> 2)) * VXLD + 16 * (l15 & 1) + 2 * (l15 & 2)];
#pragma unroll
      for (int i = 0; i < 8; ++i) {
        const v2u lo = trrd(vp + 32 * (i >> 1) + 8 * (i & 1)), hi = trrd(vp + 16 * VXLD + 32 * (i >> 1) + 8 * (i & 1));
#pragma unroll
        for (int t = 0; t < 2; ++t) { o[t][i] = mf((v4u){lo.x, lo.y, hi.x, hi.y}, px[t], (f32x4){0.f, 0.f, 0.f, 0.f}); asm volatile("" : "+v"(o[t][i])); }
      } }
    for (unsigned spins = 0; seen <= (seg < SSEG0 ? seg : seg - 3); ++spins) {
      seen = __builtin_amdgcn_readfirstlane(ld_agent(&ctl->prog[0]));
      if (seen > (seg < SSEG0 ? seg : seg - 3) || spin_fail(spins, &ctl->tmo[0], 6u)) break;
      __builtin_amdgcn_s_sleep(PSL);
    }
    asm volatile("" ::: "memory");
    for (unsigned spins = 0;; ++spins) {
      if (__all(attnA_load_m<1>(F, seg, h, lane, km, vm) != 0xffffffffu) || spin_fail(spins, &ctl->tmo[0], 4u)) break;
      __builtin_amdgcn_s_sleep(TSL);
    }
#define ELOG(i)
    ELOG(0);
    asm volatile("" ::: "memory");
    unsigned vc = attnA_load_m<2>(F, seg, h, lane, km, vm);
    v4u pm[2];
#pragma unroll
    for (int t = 0; t < 2; ++t) {
      f32x4 s0 = {0.f, 0.f, 0.f, 0.f};
#pragma unroll
      for (int j = 0; j < 4; ++j) s0 = mf(km[j], X.q[t][j], s0);
      float a = s0[0], c = s0[1];
      rnd2(a, c);
      const float m = fmaxf(mx[t], xmax32(xmax16(fmaxf(a, c)))), al = __builtin_amdgcn_exp2f((mx[t] - m) * L2E);
      a = __builtin_amdgcn_exp2f((a - m) * L2E); c = __builtin_amdgcn_exp2f((c - m) * L2E);
      const float inv = __builtin_amdgcn_rcpf(xsum32(xsum16(ls[t] * al + a + c))), sc = al * inv;
      pm[t] = (v4u){pk2(a * inv, c * inv), 0u, 0u, 0u};
#pragma unroll
      for (int i = 0; i < 8; ++i) { o[t][i] *= sc; asm volatile("" : "+v"(o[t][i])); }
    }
    SB0;
    for (unsigned spins = 0; !__all(vc != 0xffffffffu) && !spin_fail(spins, &ctl->tmo[0], 4u); ++spins) vc = attnA_load_m<2>(F, seg, h, lane, km, vm);
    const __amdgpu_buffer_rsrc_t rs = __builtin_amdgcn_make_buffer_rsrc(F.qkvA, 0, QSIZE, 0x00020000);
#pragma unroll
    for (int u = 0; u < 4; ++u)
#pragma unroll
      for (int t = 0; t < 2; ++t) {
        const f32x4 y0 = mf((v4u){vm[2 * u], 0u, 0u, 0u}, pm[t], o[t][2 * u]), y1 = mf((v4u){vm[2 * u + 1], 0u, 0u, 0u}, pm[t], o[t][2 * u + 1]);
        __builtin_amdgcn_raw_buffer_store_b128((v4u){pk2(y0[0], y0[1]), pk2(y0[2], y0[3]), pk2(y1[0], y1[1]), pk2(y1[2], y1[3])}, rs, lane * 16 + u * 1024, qoff(parq, h >> 1) + (seg * 2 + t) * QBLK + (h & 1) * 4096, 16);
      }
    SB0;
    asm volatile("s_waitcnt vmcnt(0)\n\ts_barrier" ::: "memory");
    if (w == 0 && lane < 32) __hip_atomic_store(&ctl->flagO[seg][hg][lane], 1u, RLX_AGENT);
    SB0;
    if (seg + NSET < NSEG) attnA_load_x<16>(F, parq, seg + NSET, h, lane, X);
    ELOG(2);
  }
}
#define OU_AUX ""
#define OU_BODY \
          "s_mov_b64 s[40:41], %[qa]\n\ts_mov_b32 s42, %[qs]\n\ts_mov_b32 s43, 0x20000\n\ts_mov_b64 s[44:45], %[qa]\n\ts_mov_b32 s47, 0x20000\n\ts_nop 4\n\t" \
          "s_add_u32 m0, %[wl], 0\n\ts_xor_b32 %[t], %[wo], 0x0\n\tbuffer_load_dwordx4 %[vw0], %[rw], %[t] offen lds\n\ts_add_u32 m0, %[wl], 1024\n\ts_xor_b32 %[t], %[wo], 0x4000\n\tbuffer_load_dwordx4 %[vw1], %[rw], %[t] offen lds\n\t" \
          "s_add_u32 m0, %[wl], 2048\n\ts_xor_b32 %[t], %[wo], 0x8000\n\tbuffer_load_dwordx4 %[vw0], %[rw], %[t] offen lds\n\ts_add_u32 m0, %[wl], 3072\n\ts_xor_b32 %[t], %[wo], 0xc000\n\tbuffer_load_dwordx4 %[vw1], %[rw], %[t] offen lds\n\t" \
          "s_add_u32 m0, %[wl], 4096\n\ts_xor_b32 %[t], %[wo], 0x10000\n\tbuffer_load_dwordx4 %[vw0], %[rw], %[t] offen lds\n\ts_add_u32 m0, %[wl], 5120\n\ts_xor_b32 %[t], %[wo], 0x14000\n\tbuffer_load_dwordx4 %[vw1], %[rw], %[t] offen lds\n\t" \
          "s_add_u32 m0, %[wl], 6144\n\ts_xor_b32 %[t], %[wo], 0x18000\n\tbuffer_load_dwordx4 %[vw0], %[rw], %[t] offen lds\n\ts_add_u32 m0, %[wl], 7168\n\ts_xor_b32 %[t], %[wo], 0x1c000\n\tbuffer_load_dwordx4 %[vw1], %[rw], %[t] offen lds\n\t" \
          "s_add_u32 m0, %[wl], 8192\n\ts_xor_b32 %[t], %[wo], 0x20000\n\tbuffer_load_dwordx4 %[vw0], %[rw], %[t] offen lds\n\ts_add_u32 m0, %[wl], 9216\n\ts_xor_b32 %[t], %[wo], 0x24000\n\tbuffer_load_dwordx4 %[vw1], %[rw], %[t] offen lds\n\t" \
          "s_add_u32 m0, %[wl], 10240\n\ts_xor_b32 %[t], %[wo], 0x28000\n\tbuffer_load_dwordx4 %[vw0], %[rw], %[t] offen lds\n\ts_add_u32 m0, %[wl], 11264\n\ts_xor_b32 %[t], %[wo], 0x2c000\n\tbuffer_load_dwordx4 %[vw1], %[rw], %[t] offen lds\n\t" \
          "s_add_u32 m0, %[wl], 12288\n\ts_xor_b32 %[t], %[wo], 0x30000\n\tbuffer_load_dwordx4 %[vw0], %[rw], %[t] offen lds\n\ts_add_u32 m0, %[wl], 13312\n\ts_xor_b32 %[t], %[wo], 0x34000\n\tbuffer_load_dwordx4 %[vw1], %[rw], %[t] offen lds\n\t" \
          "s_add_u32 m0, %[wl], 14336\n\ts_xor_b32 %[t], %[wo], 0x38000\n\tbuffer_load_dwordx4 %[vw0], %[rw], %[t] offen lds\n\ts_add_u32 m0, %[wl], 15360\n\ts_xor_b32 %[t], %[wo], 0x3c000\n\tbuffer_load_dwordx4 %[vw1], %[rw], %[t] offen lds\n\t" \
          "s_add_u32 m0, %[wl], 16384\n\ts_xor_b32 %[t], %[wo], 0x40000\n\tbuffer_load_dwordx4 %[vw0], %[rw], %[t] offen lds\n\ts_add_u32 m0, %[wl], 17408\n\ts_xor_b32 %[t], %[wo], 0x44000\n\tbuffer_load_dwordx4 %[vw1], %[rw], %[t] offen lds\n\t" \
          "s_add_u32 m0, %[wl], 18432\n\ts_xor_b32 %[t], %[wo], 0x48000\n\tbuffer_load_dwordx4 %[vw0], %[rw], %[t] offen lds\n\ts_add_u32 m0, %[wl], 19456\n\ts_xor_b32 %[t], %[wo], 0x4c000\n\tbuffer_load_dwordx4 %[vw1], %[rw], %[t] offen lds\n\t" \
          "s_add_u32 m0, %[wl], 20480\n\ts_xor_b32 %[t], %[wo], 0x50000\n\tbuffer_load_dwordx4 %[vw0], %[rw], %[t] offen lds\n\ts_add_u32 m0, %[wl], 21504\n\ts_xor_b32 %[t], %[wo], 0x54000\n\tbuffer_load_dwordx4 %[vw1], %[rw], %[t] offen lds\n\t" \
          "s_add_u32 m0, %[wl], 22528\n\ts_xor_b32 %[t], %[wo], 0x58000\n\tbuffer_load_dwordx4 %[vw0], %[rw], %[t] offen lds\n\ts_add_u32 m0, %[wl], 23552\n\ts_xor_b32 %[t], %[wo], 0x5c000\n\tbuffer_load_dwordx4 %[vw1], %[rw], %[t] offen lds\n\t" \
          "s_add_u32 m0, %[wl], 24576\n\ts_xor_b32 %[t], %[wo], 0x60000\n\tbuffer_load_dwordx4 %[vw0], %[rw], %[t] offen lds\n\ts_add_u32 m0, %[wl], 25600\n\ts_xor_b32 %[t], %[wo], 0x64000\n\tbuffer_load_dwordx4 %[vw1], %[rw], %[t] offen lds\n\t" \
          "s_add_u32 m0, %[wl], 26624\n\ts_xor_b32 %[t], %[wo], 0x68000\n\tbuffer_load_dwordx4 %[vw0], %[rw], %[t] offen lds\n\ts_add_u32 m0, %[wl], 27648\n\ts_xor_b32 %[t], %[wo], 0x6c000\n\tbuffer_load_dwordx4 %[vw1], %[rw], %[t] offen lds\n\t" \
          "s_add_u32 m0, %[wl], 28672\n\ts_xor_b32 %[t], %[wo], 0x70000\n\tbuffer_load_dwordx4 %[vw0], %[rw], %[t] offen lds\n\ts_add_u32 m0, %[wl], 29696\n\ts_xor_b32 %[t], %[wo], 0x74000\n\tbuffer_load_dwordx4 %[vw1], %[rw], %[t] offen lds\n\t" \
          "s_add_u32 m0, %[wl], 30720\n\ts_xor_b32 %[t], %[wo], 0x78000\n\tbuffer_load_dwordx4 %[vw0], %[rw], %[t] offen lds\n\ts_add_u32 m0, %[wl], 31744\n\ts_xor_b32 %[t], %[wo], 0x7c000\n\tbuffer_load_dwordx4 %[vw1], %[rw], %[t] offen lds\n\t" \
          "s_add_u32 m0, %[wl], 32768\n\ts_xor_b32 %[t], %[wo], 0x80000\n\tbuffer_load_dwordx4 %[vw0], %[rw], %[t] offen lds\n\ts_add_u32 m0, %[wl], 33792\n\ts_xor_b32 %[t], %[wo], 0x84000\n\tbuffer_load_dwordx4 %[vw1], %[rw], %[t] offen lds\n\t" \
          "s_add_u32 m0, %[wl], 34816\n\ts_xor_b32 %[t], %[wo], 0x88000\n\tbuffer_load_dwordx4 %[vw0], %[rw], %[t] offen lds\n\ts_add_u32 m0, %[wl], 35840\n\ts_xor_b32 %[t], %[wo], 0x8c000\n\tbuffer_load_dwordx4 %[vw1], %[rw], %[t] offen lds\n\t" \
          "s_add_u32 m0, %[wl], 36864\n\ts_xor_b32 %[t], %[wo], 0x90000\n\tbuffer_load_dwordx4 %[vw0], %[rw], %[t] offen lds\n\ts_add_u32 m0, %[wl], 37888\n\ts_xor_b32 %[t], %[wo], 0x94000\n\tbuffer_load_dwordx4 %[vw1], %[rw], %[t] offen lds\n\t" \
          "s_add_u32 m0, %[wl], 38912\n\ts_xor_b32 %[t], %[wo], 0x98000\n\tbuffer_load_dwordx4 %[vw0], %[rw], %[t] offen lds\n\ts_add_u32 m0, %[wl], 39936\n\ts_xor_b32 %[t], %[wo], 0x9c000\n\tbuffer_load_dwordx4 %[vw1], %[rw], %[t] offen lds\n\t" \
          "s_waitcnt vmcnt(32)\n\tds_read_b64_tr_b16 a[0:1], %[rt0] offset:0\n\tds_read_b64_tr_b16 a[2:3], %[rt0] offset:512\n\tds_read_b64_tr_b16 a[4:5], %[rt1] offset:0\n\tds_read_b64_tr_b16 a[6:7], %[rt1] offset:512\n\tds_read_b64_tr_b16 v[128:129], %[rt2] offset:0\n\t" \
          "ds_read_b64_tr_b16 v[130:131], %[rt2] offset:512\n\tds_read_b64_tr_b16 v[32:33], %[rt3] offset:0\n\tds_read_b64_tr_b16 v[34:35], %[rt3] offset:512\n\tds_read_b64_tr_b16 a[8:9], %[rt0] offset:4096\n\tds_read_b64_tr_b16 a[10:11], %[rt0] offset:4608\n\tds_read_b64_tr_b16 a[12:13], %[rt1] offset:4096\n\t" \
          "ds_read_b64_tr_b16 a[14:15], %[rt1] offset:4608\n\tds_read_b64_tr_b16 v[132:133], %[rt2] offset:4096\n\tds_read_b64_tr_b16 v[134:135], %[rt2] offset:4608\n\tds_read_b64_tr_b16 v[36:37], %[rt3] offset:4096\n\tds_read_b64_tr_b16 v[38:39], %[rt3] offset:4608\n\ts_waitcnt lgkmcnt(0)\n\t" \
          "s_add_u32 m0, %[wl], 0\n\ts_xor_b32 %[t], %[wo], 0xa0000\n\tbuffer_load_dwordx4 %[vw0], %[rw], %[t] offen lds\n\ts_add_u32 m0, %[wl], 1024\n\ts_xor_b32 %[t], %[wo], 0xa4000\n\tbuffer_load_dwordx4 %[vw1], %[rw], %[t] offen lds\n\t" \
          "s_add_u32 m0, %[wl], 2048\n\ts_xor_b32 %[t], %[wo], 0xa8000\n\tbuffer_load_dwordx4 %[vw0], %[rw], %[t] offen lds\n\ts_add_u32 m0, %[wl], 3072\n\ts_xor_b32 %[t], %[wo], 0xac000\n\tbuffer_load_dwordx4 %[vw1], %[rw], %[t] offen lds\n\t" \
          "s_add_u32 m0, %[wl], 4096\n\ts_xor_b32 %[t], %[wo], 0xb0000\n\tbuffer_load_dwordx4 %[vw0], %[rw], %[t] offen lds\n\ts_add_u32 m0, %[wl], 5120\n\ts_xor_b32 %[t], %[wo], 0xb4000\n\tbuffer_load_dwordx4 %[vw1], %[rw], %[t] offen lds\n\t" \
          "s_add_u32 m0, %[wl], 6144\n\ts_xor_b32 %[t], %[wo], 0xb8000\n\tbuffer_load_dwordx4 %[vw0], %[rw], %[t] offen lds\n\ts_add_u32 m0, %[wl], 7168\n\ts_xor_b32 %[t], %[wo], 0xbc000\n\tbuffer_load_dwordx4 %[vw1], %[rw], %[t] offen lds\n\t" \
          "s_waitcnt vmcnt(32)\n\tds_read_b64_tr_b16 a[16:17], %[rt0] offset:8192\n\tds_read_b64_tr_b16 a[18:19], %[rt0] offset:8704\n\tds_read_b64_tr_b16 a[20:21], %[rt1] offset:8192\n\tds_read_b64_tr_b16 a[22:23], %[rt1] offset:8704\n\tds_read_b64_tr_b16 v[136:137], %[rt2] offset:8192\n\t" \
          "ds_read_b64_tr_b16 v[138:139], %[rt2] offset:8704\n\tds_read_b64_tr_b16 v[40:41], %[rt3] offset:8192\n\tds_read_b64_tr_b16 v[42:43], %[rt3] offset:8704\n\tds_read_b64_tr_b16 a[24:25], %[rt0] offset:12288\n\tds_read_b64_tr_b16 a[26:27], %[rt0] offset:12800\n\tds_read_b64_tr_b16 a[28:29], %[rt1] offset:12288\n\t" \
          "ds_read_b64_tr_b16 a[30:31], %[rt1] offset:12800\n\tds_read_b64_tr_b16 v[140:141], %[rt2] offset:12288\n\tds_read_b64_tr_b16 v[142:143], %[rt2] offset:12800\n\tds_read_b64_tr_b16 v[44:45], %[rt3] offset:12288\n\tds_read_b64_tr_b16 v[46:47], %[rt3] offset:12800\n\ts_waitcnt lgkmcnt(0)\n\t" \
          "s_add_u32 m0, %[wl], 8192\n\ts_xor_b32 %[t], %[wo], 0xc0000\n\tbuffer_load_dwordx4 %[vw0], %[rw], %[t] offen lds\n\ts_add_u32 m0, %[wl], 9216\n\ts_xor_b32 %[t], %[wo], 0xc4000\n\tbuffer_load_dwordx4 %[vw1], %[rw], %[t] offen lds\n\t" \
          "s_add_u32 m0, %[wl], 10240\n\ts_xor_b32 %[t], %[wo], 0xc8000\n\tbuffer_load_dwordx4 %[vw0], %[rw], %[t] offen lds\n\ts_add_u32 m0, %[wl], 11264\n\ts_xor_b32 %[t], %[wo], 0xcc000\n\tbuffer_load_dwordx4 %[vw1], %[rw], %[t] offen lds\n\t" \
          "s_add_u32 m0, %[wl], 12288\n\ts_xor_b32 %[t], %[wo], 0xd0000\n\tbuffer_load_dwordx4 %[vw0], %[rw], %[t] offen lds\n\ts_add_u32 m0, %[wl], 13312\n\ts_xor_b32 %[t], %[wo], 0xd4000\n\tbuffer_load_dwordx4 %[vw1], %[rw], %[t] offen lds\n\t" \
          "s_add_u32 m0, %[wl], 14336\n\ts_xor_b32 %[t], %[wo], 0xd8000\n\tbuffer_load_dwordx4 %[vw0], %[rw], %[t] offen lds\n\ts_add_u32 m0, %[wl], 15360\n\ts_xor_b32 %[t], %[wo], 0xdc000\n\tbuffer_load_dwordx4 %[vw1], %[rw], %[t] offen lds\n\t" \
          "s_waitcnt vmcnt(32)\n\tds_read_b64_tr_b16 a[32:33], %[rt0] offset:16384\n\tds_read_b64_tr_b16 a[34:35], %[rt0] offset:16896\n\tds_read_b64_tr_b16 a[36:37], %[rt1] offset:16384\n\tds_read_b64_tr_b16 a[38:39], %[rt1] offset:16896\n\tds_read_b64_tr_b16 v[144:145], %[rt2] offset:16384\n\t" \
          "ds_read_b64_tr_b16 v[146:147], %[rt2] offset:16896\n\tds_read_b64_tr_b16 v[48:49], %[rt3] offset:16384\n\tds_read_b64_tr_b16 v[50:51], %[rt3] offset:16896\n\tds_read_b64_tr_b16 a[40:41], %[rt0] offset:20480\n\tds_read_b64_tr_b16 a[42:43], %[rt0] offset:20992\n\tds_read_b64_tr_b16 a[44:45], %[rt1] offset:20480\n\t" \
          "ds_read_b64_tr_b16 a[46:47], %[rt1] offset:20992\n\tds_read_b64_tr_b16 v[148:149], %[rt2] offset:20480\n\tds_read_b64_tr_b16 v[150:151], %[rt2] offset:20992\n\tds_read_b64_tr_b16 v[52:53], %[rt3] offset:20480\n\tds_read_b64_tr_b16 v[54:55], %[rt3] offset:20992\n\ts_waitcnt lgkmcnt(0)\n\t" \
          "s_add_u32 m0, %[wl], 16384\n\ts_xor_b32 %[t], %[wo], 0xe0000\n\tbuffer_load_dwordx4 %[vw0], %[rw], %[t] offen lds\n\ts_add_u32 m0, %[wl], 17408\n\ts_xor_b32 %[t], %[wo], 0xe4000\n\tbuffer_load_dwordx4 %[vw1], %[rw], %[t] offen lds\n\t" \
          "s_add_u32 m0, %[wl], 18432\n\ts_xor_b32 %[t], %[wo], 0xe8000\n\tbuffer_load_dwordx4 %[vw0], %[rw], %[t] offen lds\n\ts_add_u32 m0, %[wl], 19456\n\ts_xor_b32 %[t], %[wo], 0xec000\n\tbuffer_load_dwordx4 %[vw1], %[rw], %[t] offen lds\n\t" \
          "s_add_u32 m0, %[wl], 20480\n\ts_xor_b32 %[t], %[wo], 0xf0000\n\tbuffer_load_dwordx4 %[vw0], %[rw], %[t] offen lds\n\ts_add_u32 m0, %[wl], 21504\n\ts_xor_b32 %[t], %[wo], 0xf4000\n\tbuffer_load_dwordx4 %[vw1], %[rw], %[t] offen lds\n\t" \
          "s_add_u32 m0, %[wl], 22528\n\ts_xor_b32 %[t], %[wo], 0xf8000\n\tbuffer_load_dwordx4 %[vw0], %[rw], %[t] offen lds\n\ts_add_u32 m0, %[wl], 23552\n\ts_xor_b32 %[t], %[wo], 0xfc000\n\tbuffer_load_dwordx4 %[vw1], %[rw], %[t] offen lds\n\t" \
          "s_waitcnt vmcnt(32)\n\tds_read_b64_tr_b16 a[48:49], %[rt0] offset:24576\n\tds_read_b64_tr_b16 a[50:51], %[rt0] offset:25088\n\tds_read_b64_tr_b16 a[52:53], %[rt1] offset:24576\n\tds_read_b64_tr_b16 a[54:55], %[rt1] offset:25088\n\tds_read_b64_tr_b16 v[152:153], %[rt2] offset:24576\n\t" \
          "ds_read_b64_tr_b16 v[154:155], %[rt2] offset:25088\n\tds_read_b64_tr_b16 v[56:57], %[rt3] offset:24576\n\tds_read_b64_tr_b16 v[58:59], %[rt3] offset:25088\n\tds_read_b64_tr_b16 a[56:57], %[rt0] offset:28672\n\tds_read_b64_tr_b16 a[58:59], %[rt0] offset:29184\n\tds_read_b64_tr_b16 a[60:61], %[rt1] offset:28672\n\t" \
          "ds_read_b64_tr_b16 a[62:63], %[rt1] offset:29184\n\tds_read_b64_tr_b16 v[156:157], %[rt2] offset:28672\n\tds_read_b64_tr_b16 v[158:159], %[rt2] offset:29184\n\tds_read_b64_tr_b16 v[60:61], %[rt3] offset:28672\n\tds_read_b64_tr_b16 v[62:63], %[rt3] offset:29184\n\ts_waitcnt lgkmcnt(0)\n\t" \
          "s_add_u32 m0, %[wl], 24576\n\ts_xor_b32 %[t], %[wo], 0x100000\n\tbuffer_load_dwordx4 %[vw0], %[rw], %[t] offen lds\n\ts_add_u32 m0, %[wl], 25600\n\ts_xor_b32 %[t], %[wo], 0x104000\n\tbuffer_load_dwordx4 %[vw1], %[rw], %[t] offen lds\n\t" \
          "s_add_u32 m0, %[wl], 26624\n\ts_xor_b32 %[t], %[wo], 0x108000\n\tbuffer_load_dwordx4 %[vw0], %[rw], %[t] offen lds\n\ts_add_u32 m0, %[wl], 27648\n\ts_xor_b32 %[t], %[wo], 0x10c000\n\tbuffer_load_dwordx4 %[vw1], %[rw], %[t] offen lds\n\t" \
          "s_add_u32 m0, %[wl], 28672\n\ts_xor_b32 %[t], %[wo], 0x110000\n\tbuffer_load_dwordx4 %[vw0], %[rw], %[t] offen lds\n\ts_add_u32 m0, %[wl], 29696\n\ts_xor_b32 %[t], %[wo], 0x114000\n\tbuffer_load_dwordx4 %[vw1], %[rw], %[t] offen lds\n\t" \
          "s_add_u32 m0, %[wl], 30720\n\ts_xor_b32 %[t], %[wo], 0x118000\n\tbuffer_load_dwordx4 %[vw0], %[rw], %[t] offen lds\n\ts_add_u32 m0, %[wl], 31744\n\ts_xor_b32 %[t], %[wo], 0x11c000\n\tbuffer_load_dwordx4 %[vw1], %[rw], %[t] offen lds\n\t" \
          "s_waitcnt vmcnt(32)\n\tds_read_b64_tr_b16 a[64:65], %[rt0] offset:32768\n\tds_read_b64_tr_b16 a[66:67], %[rt0] offset:33280\n\tds_read_b64_tr_b16 a[68:69], %[rt1] offset:32768\n\tds_read_b64_tr_b16 a[70:71], %[rt1] offset:33280\n\tds_read_b64_tr_b16 v[160:161], %[rt2] offset:32768\n\t" \
          "ds_read_b64_tr_b16 v[162:163], %[rt2] offset:33280\n\tds_read_b64_tr_b16 v[64:65], %[rt3] offset:32768\n\tds_read_b64_tr_b16 v[66:67], %[rt3] offset:33280\n\tds_read_b64_tr_b16 a[72:73], %[rt0] offset:36864\n\tds_read_b64_tr_b16 a[74:75], %[rt0] offset:37376\n\tds_read_b64_tr_b16 a[76:77], %[rt1] offset:36864\n\t" \
          "ds_read_b64_tr_b16 a[78:79], %[rt1] offset:37376\n\tds_read_b64_tr_b16 v[164:165], %[rt2] offset:36864\n\tds_read_b64_tr_b16 v[166:167], %[rt2] offset:37376\n\tds_read_b64_tr_b16 v[68:69], %[rt3] offset:36864\n\tds_read_b64_tr_b16 v[70:71], %[rt3] offset:37376\n\ts_waitcnt lgkmcnt(0)\n\t" \
          "s_add_u32 m0, %[wl], 32768\n\ts_xor_b32 %[t], %[wo], 0x120000\n\tbuffer_load_dwordx4 %[vw0], %[rw], %[t] offen lds\n\ts_add_u32 m0, %[wl], 33792\n\ts_xor_b32 %[t], %[wo], 0x124000\n\tbuffer_load_dwordx4 %[vw1], %[rw], %[t] offen lds\n\t" \
          "s_add_u32 m0, %[wl], 34816\n\ts_xor_b32 %[t], %[wo], 0x128000\n\tbuffer_load_dwordx4 %[vw0], %[rw], %[t] offen lds\n\ts_add_u32 m0, %[wl], 35840\n\ts_xor_b32 %[t], %[wo], 0x12c000\n\tbuffer_load_dwordx4 %[vw1], %[rw], %[t] offen lds\n\t" \
          "s_add_u32 m0, %[wl], 36864\n\ts_xor_b32 %[t], %[wo], 0x130000\n\tbuffer_load_dwordx4 %[vw0], %[rw], %[t] offen lds\n\ts_add_u32 m0, %[wl], 37888\n\ts_xor_b32 %[t], %[wo], 0x134000\n\tbuffer_load_dwordx4 %[vw1], %[rw], %[t] offen lds\n\t" \
          "s_add_u32 m0, %[wl], 38912\n\ts_xor_b32 %[t], %[wo], 0x138000\n\tbuffer_load_dwordx4 %[vw0], %[rw], %[t] offen lds\n\ts_add_u32 m0, %[wl], 39936\n\ts_xor_b32 %[t], %[wo], 0x13c000\n\tbuffer_load_dwordx4 %[vw1], %[rw], %[t] offen lds\n\t" \
          "s_waitcnt vmcnt(32)\n\tds_read_b64_tr_b16 a[80:81], %[rt0] offset:0\n\tds_read_b64_tr_b16 a[82:83], %[rt0] offset:512\n\tds_read_b64_tr_b16 a[84:85], %[rt1] offset:0\n\tds_read_b64_tr_b16 a[86:87], %[rt1] offset:512\n\tds_read_b64_tr_b16 v[168:169], %[rt2] offset:0\n\t" \
          "ds_read_b64_tr_b16 v[170:171], %[rt2] offset:512\n\tds_read_b64_tr_b16 v[72:73], %[rt3] offset:0\n\tds_read_b64_tr_b16 v[74:75], %[rt3] offset:512\n\tds_read_b64_tr_b16 a[88:89], %[rt0] offset:4096\n\tds_read_b64_tr_b16 a[90:91], %[rt0] offset:4608\n\tds_read_b64_tr_b16 a[92:93], %[rt1] offset:4096\n\t" \
          "ds_read_b64_tr_b16 a[94:95], %[rt1] offset:4608\n\tds_read_b64_tr_b16 v[172:173], %[rt2] offset:4096\n\tds_read_b64_tr_b16 v[174:175], %[rt2] offset:4608\n\tds_read_b64_tr_b16 v[76:77], %[rt3] offset:4096\n\tds_read_b64_tr_b16 v[78:79], %[rt3] offset:4608\n\ts_waitcnt lgkmcnt(0)\n\t" \
          "s_add_u32 m0, %[wl], 0\n\ts_xor_b32 %[t], %[wo], 0x140000\n\tbuffer_load_dwordx4 %[vw0], %[rw], %[t] offen lds\n\ts_add_u32 m0, %[wl], 1024\n\ts_xor_b32 %[t], %[wo], 0x144000\n\tbuffer_load_dwordx4 %[vw1], %[rw], %[t] offen lds\n\t" \
          "s_add_u32 m0, %[wl], 2048\n\ts_xor_b32 %[t], %[wo], 0x148000\n\tbuffer_load_dwordx4 %[vw0], %[rw], %[t] offen lds\n\ts_add_u32 m0, %[wl], 3072\n\ts_xor_b32 %[t], %[wo], 0x14c000\n\tbuffer_load_dwordx4 %[vw1], %[rw], %[t] offen lds\n\t" \
          "s_add_u32 m0, %[wl], 4096\n\ts_xor_b32 %[t], %[wo], 0x150000\n\tbuffer_load_dwordx4 %[vw0], %[rw], %[t] offen lds\n\ts_add_u32 m0, %[wl], 5120\n\ts_xor_b32 %[t], %[wo], 0x154000\n\tbuffer_load_dwordx4 %[vw1], %[rw], %[t] offen lds\n\t" \
          "s_add_u32 m0, %[wl], 6144\n\ts_xor_b32 %[t], %[wo], 0x158000\n\tbuffer_load_dwordx4 %[vw0], %[rw], %[t] offen lds\n\ts_add_u32 m0, %[wl], 7168\n\ts_xor_b32 %[t], %[wo], 0x15c000\n\tbuffer_load_dwordx4 %[vw1], %[rw], %[t] offen lds\n\t" \
          "s_waitcnt vmcnt(32)\n\tds_read_b64_tr_b16 a[96:97], %[rt0] offset:8192\n\tds_read_b64_tr_b16 a[98:99], %[rt0] offset:8704\n\tds_read_b64_tr_b16 a[100:101], %[rt1] offset:8192\n\tds_read_b64_tr_b16 a[102:103], %[rt1] offset:8704\n\tds_read_b64_tr_b16 v[176:177], %[rt2] offset:8192\n\t" \
          "ds_read_b64_tr_b16 v[178:179], %[rt2] offset:8704\n\tds_read_b64_tr_b16 v[80:81], %[rt3] offset:8192\n\tds_read_b64_tr_b16 v[82:83], %[rt3] offset:8704\n\tds_read_b64_tr_b16 a[104:105], %[rt0] offset:12288\n\tds_read_b64_tr_b16 a[106:107], %[rt0] offset:12800\n\tds_read_b64_tr_b16 a[108:109], %[rt1] offset:12288\n\t" \
          "ds_read_b64_tr_b16 a[110:111], %[rt1] offset:12800\n\tds_read_b64_tr_b16 v[180:181], %[rt2] offset:12288\n\tds_read_b64_tr_b16 v[182:183], %[rt2] offset:12800\n\tds_read_b64_tr_b16 v[84:85], %[rt3] offset:12288\n\tds_read_b64_tr_b16 v[86:87], %[rt3] offset:12800\n\ts_waitcnt lgkmcnt(0)\n\t" \
          "s_add_u32 m0, %[wl], 8192\n\ts_xor_b32 %[t], %[wo], 0x160000\n\tbuffer_load_dwordx4 %[vw0], %[rw], %[t] offen lds\n\ts_add_u32 m0, %[wl], 9216\n\ts_xor_b32 %[t], %[wo], 0x164000\n\tbuffer_load_dwordx4 %[vw1], %[rw], %[t] offen lds\n\t" \
          "s_add_u32 m0, %[wl], 10240\n\ts_xor_b32 %[t], %[wo], 0x168000\n\tbuffer_load_dwordx4 %[vw0], %[rw], %[t] offen lds\n\ts_add_u32 m0, %[wl], 11264\n\ts_xor_b32 %[t], %[wo], 0x16c000\n\tbuffer_load_dwordx4 %[vw1], %[rw], %[t] offen lds\n\t" \
          "s_add_u32 m0, %[wl], 12288\n\ts_xor_b32 %[t], %[wo], 0x170000\n\tbuffer_load_dwordx4 %[vw0], %[rw], %[t] offen lds\n\ts_add_u32 m0, %[wl], 13312\n\ts_xor_b32 %[t], %[wo], 0x174000\n\tbuffer_load_dwordx4 %[vw1], %[rw], %[t] offen lds\n\t" \
          "s_add_u32 m0, %[wl], 14336\n\ts_xor_b32 %[t], %[wo], 0x178000\n\tbuffer_load_dwordx4 %[vw0], %[rw], %[t] offen lds\n\ts_add_u32 m0, %[wl], 15360\n\ts_xor_b32 %[t], %[wo], 0x17c000\n\tbuffer_load_dwordx4 %[vw1], %[rw], %[t] offen lds\n\t" \
          "s_waitcnt vmcnt(32)\n\tds_read_b64_tr_b16 a[112:113], %[rt0] offset:16384\n\tds_read_b64_tr_b16 a[114:115], %[rt0] offset:16896\n\tds_read_b64_tr_b16 a[116:117], %[rt1] offset:16384\n\tds_read_b64_tr_b16 a[118:119], %[rt1] offset:16896\n\tds_read_b64_tr_b16 v[184:185], %[rt2] offset:16384\n\t" \
          "ds_read_b64_tr_b16 v[186:187], %[rt2] offset:16896\n\tds_read_b64_tr_b16 v[88:89], %[rt3] offset:16384\n\tds_read_b64_tr_b16 v[90:91], %[rt3] offset:16896\n\tds_read_b64_tr_b16 a[120:121], %[rt0] offset:20480\n\tds_read_b64_tr_b16 a[122:123], %[rt0] offset:20992\n\tds_read_b64_tr_b16 a[124:125], %[rt1] offset:20480\n\t" \
          "ds_read_b64_tr_b16 a[126:127], %[rt1] offset:20992\n\tds_read_b64_tr_b16 v[188:189], %[rt2] offset:20480\n\tds_read_b64_tr_b16 v[190:191], %[rt2] offset:20992\n\tds_read_b64_tr_b16 v[92:93], %[rt3] offset:20480\n\tds_read_b64_tr_b16 v[94:95], %[rt3] offset:20992\n\ts_waitcnt lgkmcnt(0)\n\t" \
          "s_add_u32 m0, %[wl], 16384\n\ts_xor_b32 %[t], %[wo], 0x180000\n\tbuffer_load_dwordx4 %[vw0], %[rw], %[t] offen lds\n\ts_add_u32 m0, %[wl], 17408\n\ts_xor_b32 %[t], %[wo], 0x184000\n\tbuffer_load_dwordx4 %[vw1], %[rw], %[t] offen lds\n\t" \
          "s_add_u32 m0, %[wl], 18432\n\ts_xor_b32 %[t], %[wo], 0x188000\n\tbuffer_load_dwordx4 %[vw0], %[rw], %[t] offen lds\n\ts_add_u32 m0, %[wl], 19456\n\ts_xor_b32 %[t], %[wo], 0x18c000\n\tbuffer_load_dwordx4 %[vw1], %[rw], %[t] offen lds\n\t" \
          "s_add_u32 m0, %[wl], 20480\n\ts_xor_b32 %[t], %[wo], 0x190000\n\tbuffer_load_dwordx4 %[vw0], %[rw], %[t] offen lds\n\ts_add_u32 m0, %[wl], 21504\n\ts_xor_b32 %[t], %[wo], 0x194000\n\tbuffer_load_dwordx4 %[vw1], %[rw], %[t] offen lds\n\t" \
          "s_add_u32 m0, %[wl], 22528\n\ts_xor_b32 %[t], %[wo], 0x198000\n\tbuffer_load_dwordx4 %[vw0], %[rw], %[t] offen lds\n\ts_add_u32 m0, %[wl], 23552\n\ts_xor_b32 %[t], %[wo], 0x19c000\n\tbuffer_load_dwordx4 %[vw1], %[rw], %[t] offen lds\n\t" \
          "s_waitcnt vmcnt(32)\n\tds_read_b64_tr_b16 a[128:129], %[rt0] offset:24576\n\tds_read_b64_tr_b16 a[130:131], %[rt0] offset:25088\n\tds_read_b64_tr_b16 a[132:133], %[rt1] offset:24576\n\tds_read_b64_tr_b16 a[134:135], %[rt1] offset:25088\n\tds_read_b64_tr_b16 v[192:193], %[rt2] offset:24576\n\t" \
          "ds_read_b64_tr_b16 v[194:195], %[rt2] offset:25088\n\tds_read_b64_tr_b16 v[96:97], %[rt3] offset:24576\n\tds_read_b64_tr_b16 v[98:99], %[rt3] offset:25088\n\tds_read_b64_tr_b16 a[136:137], %[rt0] offset:28672\n\tds_read_b64_tr_b16 a[138:139], %[rt0] offset:29184\n\tds_read_b64_tr_b16 a[140:141], %[rt1] offset:28672\n\t" \
          "ds_read_b64_tr_b16 a[142:143], %[rt1] offset:29184\n\tds_read_b64_tr_b16 v[196:197], %[rt2] offset:28672\n\tds_read_b64_tr_b16 v[198:199], %[rt2] offset:29184\n\tds_read_b64_tr_b16 v[100:101], %[rt3] offset:28672\n\tds_read_b64_tr_b16 v[102:103], %[rt3] offset:29184\n\ts_waitcnt lgkmcnt(0)\n\t" \
          "s_add_u32 m0, %[wl], 24576\n\ts_xor_b32 %[t], %[wo], 0x1a0000\n\tbuffer_load_dwordx4 %[vw0], %[rw], %[t] offen lds\n\ts_add_u32 m0, %[wl], 25600\n\ts_xor_b32 %[t], %[wo], 0x1a4000\n\tbuffer_load_dwordx4 %[vw1], %[rw], %[t] offen lds\n\t" \
          "s_add_u32 m0, %[wl], 26624\n\ts_xor_b32 %[t], %[wo], 0x1a8000\n\tbuffer_load_dwordx4 %[vw0], %[rw], %[t] offen lds\n\ts_add_u32 m0, %[wl], 27648\n\ts_xor_b32 %[t], %[wo], 0x1ac000\n\tbuffer_load_dwordx4 %[vw1], %[rw], %[t] offen lds\n\t" \
          "s_add_u32 m0, %[wl], 28672\n\ts_xor_b32 %[t], %[wo], 0x1b0000\n\tbuffer_load_dwordx4 %[vw0], %[rw], %[t] offen lds\n\ts_add_u32 m0, %[wl], 29696\n\ts_xor_b32 %[t], %[wo], 0x1b4000\n\tbuffer_load_dwordx4 %[vw1], %[rw], %[t] offen lds\n\t" \
          "s_add_u32 m0, %[wl], 30720\n\ts_xor_b32 %[t], %[wo], 0x1b8000\n\tbuffer_load_dwordx4 %[vw0], %[rw], %[t] offen lds\n\ts_add_u32 m0, %[wl], 31744\n\ts_xor_b32 %[t], %[wo], 0x1bc000\n\tbuffer_load_dwordx4 %[vw1], %[rw], %[t] offen lds\n\t" \
          "s_waitcnt vmcnt(32)\n\tds_read_b64_tr_b16 a[144:145], %[rt0] offset:32768\n\tds_read_b64_tr_b16 a[146:147], %[rt0] offset:33280\n\tds_read_b64_tr_b16 a[148:149], %[rt1] offset:32768\n\tds_read_b64_tr_b16 a[150:151], %[rt1] offset:33280\n\tds_read_b64_tr_b16 v[200:201], %[rt2] offset:32768\n\t" \
          "ds_read_b64_tr_b16 v[202:203], %[rt2] offset:33280\n\tds_read_b64_tr_b16 v[104:105], %[rt3] offset:32768\n\tds_read_b64_tr_b16 v[106:107], %[rt3] offset:33280\n\tds_read_b64_tr_b16 a[152:153], %[rt0] offset:36864\n\tds_read_b64_tr_b16 a[154:155], %[rt0] offset:37376\n\tds_read_b64_tr_b16 a[156:157], %[rt1] offset:36864\n\t" \
          "ds_read_b64_tr_b16 a[158:159], %[rt1] offset:37376\n\tds_read_b64_tr_b16 v[204:205], %[rt2] offset:36864\n\tds_read_b64_tr_b16 v[206:207], %[rt2] offset:37376\n\tds_read_b64_tr_b16 v[108:109], %[rt3] offset:36864\n\tds_read_b64_tr_b16 v[110:111], %[rt3] offset:37376\n\ts_waitcnt lgkmcnt(0)\n\t" \
          "s_add_u32 m0, %[wl], 32768\n\ts_xor_b32 %[t], %[wo], 0x1c0000\n\tbuffer_load_dwordx4 %[vw0], %[rw], %[t] offen lds\n\ts_add_u32 m0, %[wl], 33792\n\ts_xor_b32 %[t], %[wo], 0x1c4000\n\tbuffer_load_dwordx4 %[vw1], %[rw], %[t] offen lds\n\t" \
          "s_add_u32 m0, %[wl], 34816\n\ts_xor_b32 %[t], %[wo], 0x1c8000\n\tbuffer_load_dwordx4 %[vw0], %[rw], %[t] offen lds\n\ts_add_u32 m0, %[wl], 35840\n\ts_xor_b32 %[t], %[wo], 0x1cc000\n\tbuffer_load_dwordx4 %[vw1], %[rw], %[t] offen lds\n\t" \
          "s_add_u32 m0, %[wl], 36864\n\ts_xor_b32 %[t], %[wo], 0x1d0000\n\tbuffer_load_dwordx4 %[vw0], %[rw], %[t] offen lds\n\ts_add_u32 m0, %[wl], 37888\n\ts_xor_b32 %[t], %[wo], 0x1d4000\n\tbuffer_load_dwordx4 %[vw1], %[rw], %[t] offen lds\n\t" \
          "s_add_u32 m0, %[wl], 38912\n\ts_xor_b32 %[t], %[wo], 0x1d8000\n\tbuffer_load_dwordx4 %[vw0], %[rw], %[t] offen lds\n\ts_add_u32 m0, %[wl], 39936\n\ts_xor_b32 %[t], %[wo], 0x1dc000\n\tbuffer_load_dwordx4 %[vw1], %[rw], %[t] offen lds\n\t" \
          "s_waitcnt vmcnt(32)\n\tds_read_b64_tr_b16 a[160:161], %[rt0] offset:0\n\tds_read_b64_tr_b16 a[162:163], %[rt0] offset:512\n\tds_read_b64_tr_b16 a[164:165], %[rt1] offset:0\n\tds_read_b64_tr_b16 a[166:167], %[rt1] offset:512\n\tds_read_b64_tr_b16 v[208:209], %[rt2] offset:0\n\t" \
          "ds_read_b64_tr_b16 v[210:211], %[rt2] offset:512\n\tds_read_b64_tr_b16 v[112:113], %[rt3] offset:0\n\tds_read_b64_tr_b16 v[114:115], %[rt3] offset:512\n\tds_read_b64_tr_b16 a[168:169], %[rt0] offset:4096\n\tds_read_b64_tr_b16 a[170:171], %[rt0] offset:4608\n\tds_read_b64_tr_b16 a[172:173], %[rt1] offset:4096\n\t" \
          "ds_read_b64_tr_b16 a[174:175], %[rt1] offset:4608\n\tds_read_b64_tr_b16 v[212:213], %[rt2] offset:4096\n\tds_read_b64_tr_b16 v[214:215], %[rt2] offset:4608\n\tds_read_b64_tr_b16 v[116:117], %[rt3] offset:4096\n\tds_read_b64_tr_b16 v[118:119], %[rt3] offset:4608\n\ts_waitcnt lgkmcnt(0)\n\t" \
          "s_add_u32 m0, %[wl], 0\n\ts_xor_b32 %[t], %[wo], 0x1e0000\n\tbuffer_load_dwordx4 %[vw0], %[rw], %[t] offen lds\n\ts_add_u32 m0, %[wl], 1024\n\ts_xor_b32 %[t], %[wo], 0x1e4000\n\tbuffer_load_dwordx4 %[vw1], %[rw], %[t] offen lds\n\t" \
          "s_add_u32 m0, %[wl], 2048\n\ts_xor_b32 %[t], %[wo], 0x1e8000\n\tbuffer_load_dwordx4 %[vw0], %[rw], %[t] offen lds\n\ts_add_u32 m0, %[wl], 3072\n\ts_xor_b32 %[t], %[wo], 0x1ec000\n\tbuffer_load_dwordx4 %[vw1], %[rw], %[t] offen lds\n\t" \
          "s_add_u32 m0, %[wl], 4096\n\ts_xor_b32 %[t], %[wo], 0x1f0000\n\tbuffer_load_dwordx4 %[vw0], %[rw], %[t] offen lds\n\ts_add_u32 m0, %[wl], 5120\n\ts_xor_b32 %[t], %[wo], 0x1f4000\n\tbuffer_load_dwordx4 %[vw1], %[rw], %[t] offen lds\n\t" \
          "s_add_u32 m0, %[wl], 6144\n\ts_xor_b32 %[t], %[wo], 0x1f8000\n\tbuffer_load_dwordx4 %[vw0], %[rw], %[t] offen lds\n\ts_add_u32 m0, %[wl], 7168\n\ts_xor_b32 %[t], %[wo], 0x1fc000\n\tbuffer_load_dwordx4 %[vw1], %[rw], %[t] offen lds\n\t" \
          "s_waitcnt vmcnt(32)\n\tds_read_b64_tr_b16 a[176:177], %[rt0] offset:8192\n\tds_read_b64_tr_b16 a[178:179], %[rt0] offset:8704\n\tds_read_b64_tr_b16 a[180:181], %[rt1] offset:8192\n\tds_read_b64_tr_b16 a[182:183], %[rt1] offset:8704\n\tds_read_b64_tr_b16 v[216:217], %[rt2] offset:8192\n\t" \
          "ds_read_b64_tr_b16 v[218:219], %[rt2] offset:8704\n\tds_read_b64_tr_b16 v[120:121], %[rt3] offset:8192\n\tds_read_b64_tr_b16 v[122:123], %[rt3] offset:8704\n\tds_read_b64_tr_b16 a[184:185], %[rt0] offset:12288\n\tds_read_b64_tr_b16 a[186:187], %[rt0] offset:12800\n\tds_read_b64_tr_b16 a[188:189], %[rt1] offset:12288\n\t" \
          "ds_read_b64_tr_b16 a[190:191], %[rt1] offset:12800\n\tds_read_b64_tr_b16 v[220:221], %[rt2] offset:12288\n\tds_read_b64_tr_b16 v[222:223], %[rt2] offset:12800\n\tds_read_b64_tr_b16 v[124:125], %[rt3] offset:12288\n\tds_read_b64_tr_b16 v[126:127], %[rt3] offset:12800\n\ts_waitcnt lgkmcnt(0)\n\t" \
          "ds_write_b128 %[own], v[32:35] offset:8192\n\tds_write_b128 %[own], v[36:39] offset:9216\n\tds_write_b128 %[own], v[40:43] offset:10240\n\tds_write_b128 %[own], v[44:47] offset:11264\n\tds_write_b128 %[own], v[48:51] offset:12288\n\tds_write_b128 %[own], v[52:55] offset:13312\n\t" \
          "ds_write_b128 %[own], v[56:59] offset:14336\n\tds_write_b128 %[own], v[60:63] offset:15360\n\ts_waitcnt lgkmcnt(0)\n\ts_waitcnt vmcnt(24)\n\tds_read_b64_tr_b16 a[192:193], %[rt0] offset:16384\n\tds_read_b64_tr_b16 a[194:195], %[rt0] offset:16896\n\t" \
          "ds_read_b64_tr_b16 a[196:197], %[rt1] offset:16384\n\tds_read_b64_tr_b16 a[198:199], %[rt1] offset:16896\n\tds_read_b64_tr_b16 v[224:225], %[rt2] offset:16384\n\tds_read_b64_tr_b16 v[226:227], %[rt2] offset:16896\n\tds_read_b64_tr_b16 v[32:33], %[rt3] offset:16384\n\tds_read_b64_tr_b16 v[34:35], %[rt3] offset:16896\n\t" \
          "ds_read_b64_tr_b16 a[200:201], %[rt0] offset:20480\n\tds_read_b64_tr_b16 a[202:203], %[rt0] offset:20992\n\tds_read_b64_tr_b16 a[204:205], %[rt1] offset:20480\n\tds_read_b64_tr_b16 a[206:207], %[rt1] offset:20992\n\tds_read_b64_tr_b16 v[228:229], %[rt2] offset:20480\n\tds_read_b64_tr_b16 v[230:231], %[rt2] offset:20992\n\t" \
          "ds_read_b64_tr_b16 v[36:37], %[rt3] offset:20480\n\tds_read_b64_tr_b16 v[38:39], %[rt3] offset:20992\n\ts_waitcnt lgkmcnt(0)\n\tds_write_b128 %[own], v[64:67] offset:16384\n\tds_write_b128 %[own], v[68:71] offset:17408\n\tds_write_b128 %[own], v[72:75] offset:18432\n\t" \
          "ds_write_b128 %[own], v[76:79] offset:19456\n\tds_write_b128 %[own], v[80:83] offset:20480\n\tds_write_b128 %[own], v[84:87] offset:21504\n\tds_write_b128 %[own], v[88:91] offset:22528\n\tds_write_b128 %[own], v[92:95] offset:23552\n\ts_waitcnt lgkmcnt(0)\n\t" \
          "s_waitcnt vmcnt(16)\n\tds_read_b64_tr_b16 a[208:209], %[rt0] offset:24576\n\tds_read_b64_tr_b16 a[210:211], %[rt0] offset:25088\n\tds_read_b64_tr_b16 a[212:213], %[rt1] offset:24576\n\tds_read_b64_tr_b16 a[214:215], %[rt1] offset:25088\n\tds_read_b64_tr_b16 v[232:233], %[rt2] offset:24576\n\t" \
          "ds_read_b64_tr_b16 v[234:235], %[rt2] offset:25088\n\tds_read_b64_tr_b16 v[40:41], %[rt3] offset:24576\n\tds_read_b64_tr_b16 v[42:43], %[rt3] offset:25088\n\tds_read_b64_tr_b16 a[216:217], %[rt0] offset:28672\n\tds_read_b64_tr_b16 a[218:219], %[rt0] offset:29184\n\tds_read_b64_tr_b16 a[220:221], %[rt1] offset:28672\n\t" \
          "ds_read_b64_tr_b16 a[222:223], %[rt1] offset:29184\n\tds_read_b64_tr_b16 v[236:237], %[rt2] offset:28672\n\tds_read_b64_tr_b16 v[238:239], %[rt2] offset:29184\n\tds_read_b64_tr_b16 v[44:45], %[rt3] offset:28672\n\tds_read_b64_tr_b16 v[46:47], %[rt3] offset:29184\n\ts_waitcnt lgkmcnt(0)\n\t" \
          "ds_write_b128 %[own], v[96:99] offset:24576\n\tds_write_b128 %[own], v[100:103] offset:25600\n\tds_write_b128 %[own], v[104:107] offset:26624\n\tds_write_b128 %[own], v[108:111] offset:27648\n\tds_write_b128 %[own], v[112:115] offset:28672\n\tds_write_b128 %[own], v[116:119] offset:29696\n\t" \
          "ds_write_b128 %[own], v[120:123] offset:30720\n\tds_write_b128 %[own], v[124:127] offset:31744\n\ts_waitcnt lgkmcnt(0)\n\ts_waitcnt vmcnt(8)\n\tds_read_b64_tr_b16 a[224:225], %[rt0] offset:32768\n\tds_read_b64_tr_b16 a[226:227], %[rt0] offset:33280\n\t" \
          "ds_read_b64_tr_b16 a[228:229], %[rt1] offset:32768\n\tds_read_b64_tr_b16 a[230:231], %[rt1] offset:33280\n\tds_read_b64_tr_b16 v[240:241], %[rt2] offset:32768\n\tds_read_b64_tr_b16 v[242:243], %[rt2] offset:33280\n\tds_read_b64_tr_b16 v[48:49], %[rt3] offset:32768\n\tds_read_b64_tr_b16 v[50:51], %[rt3] offset:33280\n\t" \
          "ds_read_b64_tr_b16 a[232:233], %[rt0] offset:36864\n\tds_read_b64_tr_b16 a[234:235], %[rt0] offset:37376\n\tds_read_b64_tr_b16 a[236:237], %[rt1] offset:36864\n\tds_read_b64_tr_b16 a[238:239], %[rt1] offset:37376\n\tds_read_b64_tr_b16 v[244:245], %[rt2] offset:36864\n\tds_read_b64_tr_b16 v[246:247], %[rt2] offset:37376\n\t" \
          "ds_read_b64_tr_b16 v[52:53], %[rt3] offset:36864\n\tds_read_b64_tr_b16 v[54:55], %[rt3] offset:37376\n\ts_waitcnt lgkmcnt(0)\n\ts_waitcnt vmcnt(0)\n\tds_read_b64_tr_b16 a[240:241], %[rt0] offset:0\n\tds_read_b64_tr_b16 a[242:243], %[rt0] offset:512\n\t" \
          "ds_read_b64_tr_b16 a[244:245], %[rt1] offset:0\n\tds_read_b64_tr_b16 a[246:247], %[rt1] offset:512\n\tds_read_b64_tr_b16 v[248:249], %[rt2] offset:0\n\tds_read_b64_tr_b16 v[250:251], %[rt2] offset:512\n\tds_read_b64_tr_b16 v[56:57], %[rt3] offset:0\n\tds_read_b64_tr_b16 v[58:59], %[rt3] offset:512\n\t" \
          "ds_read_b64_tr_b16 a[248:249], %[rt0] offset:4096\n\tds_read_b64_tr_b16 a[250:251], %[rt0] offset:4608\n\tds_read_b64_tr_b16 a[252:253], %[rt1] offset:4096\n\tds_read_b64_tr_b16 a[254:255], %[rt1] offset:4608\n\tds_read_b64_tr_b16 v[252:253], %[rt2] offset:4096\n\tds_read_b64_tr_b16 v[254:255], %[rt2] offset:4608\n\t" \
          "ds_read_b64_tr_b16 v[60:61], %[rt3] offset:4096\n\tds_read_b64_tr_b16 v[62:63], %[rt3] offset:4608\n\ts_waitcnt lgkmcnt(0)\n\tds_write_b128 %[own], v[32:35] offset:0\n\tds_write_b128 %[own], v[36:39] offset:1024\n\tds_write_b128 %[own], v[40:43] offset:2048\n\t" \
          "ds_write_b128 %[own], v[44:47] offset:3072\n\tds_write_b128 %[own], v[48:51] offset:4096\n\tds_write_b128 %[own], v[52:55] offset:5120\n\tds_write_b128 %[own], v[56:59] offset:6144\n\tds_write_b128 %[own], v[60:63] offset:7168\n\ts_waitcnt lgkmcnt(0)\n\t" \
          "Lou_wait%=:\n\ts_mov_b32 %[cnt], 0\n\tLou_poll%=:\n\ts_lshr_b32 %[t], %[j], 1\n\ts_cmp_lt_u32 %[t], %[av]\n\ts_cbranch_scc1 Lou_load%=\n\t" \
          "s_sleep 2\n\ts_lshl_b32 %[t], %[av], 10\n\ts_add_u32 %[t], %[t], %[fo]\n\tbuffer_load_dword %[fl], %[vf], %[rf], %[t] offen sc1\n\tbuffer_load_dword v48, off, %[rf], %[tmo] sc1\n\ts_waitcnt vmcnt(0)\n\t" \
          "v_cmp_eq_u32 vcc, 0, %[fl]\n\ts_nop 0\n\ts_ff1_i32_b64 %[t], vcc\n\ts_lshr_b32 %[t], %[t], 1\n\ts_add_u32 %[av], %[av], %[t]\n\tv_readfirstlane_b32 vcc_lo, v48\n\t" \
          "s_add_u32 %[cnt], %[cnt], 1\n\ts_lshr_b32 vcc_hi, %[cnt], 15\n\ts_or_b32 vcc_lo, vcc_lo, vcc_hi\n\ts_cbranch_scc0 Lou_poll%=\n\tv_mov_b32 v48, 7\n\tbuffer_store_dword v48, off, %[rf], %[tmo] sc1\n\t" \
          "s_mov_b32 %[av], 64\n\tLou_load%=:\n\tbuffer_load_dwordx4 v[64:67], %[vo0], s[40:43], %[pg0] offen sc1\n\tbuffer_load_dwordx4 v[68:71], %[vo0], s[40:43], %[pg0] offen offset:1024 sc1\n\tbuffer_load_dwordx4 v[72:75], %[vo0], s[40:43], %[pg0] offen offset:2048 sc1\n\tbuffer_load_dwordx4 v[76:79], %[vo0], s[40:43], %[pg0] offen offset:3072 sc1\n\t" \
          "buffer_load_dwordx4 v[80:83], %[vo1], s[40:43], %[pg0] offen sc1\n\tbuffer_load_dwordx4 v[84:87], %[vo1], s[40:43], %[pg0] offen offset:1024 sc1\n\tbuffer_load_dwordx4 v[88:91], %[vo1], s[40:43], %[pg0] offen offset:2048 sc1\n\tbuffer_load_dwordx4 v[92:95], %[vo1], s[40:43], %[pg0] offen offset:3072 sc1\n\tbuffer_load_dwordx4 v[96:99], %[vo0], s[40:43], %[pg1] offen sc1\n\tbuffer_load_dwordx4 v[100:103], %[vo0], s[40:43], %[pg1] offen offset:1024 sc1\n\t" \
          "buffer_load_dwordx4 v[104:107], %[vo0], s[40:43], %[pg1] offen offset:2048 sc1\n\tbuffer_load_dwordx4 v[108:111], %[vo0], s[40:43], %[pg1] offen offset:3072 sc1\n\tbuffer_load_dwordx4 v[112:115], %[vo1], s[40:43], %[pg1] offen sc1\n\tbuffer_load_dwordx4 v[116:119], %[vo1], s[40:43], %[pg1] offen offset:1024 sc1\n\tbuffer_load_dwordx4 v[120:123], %[vo1], s[40:43], %[pg1] offen offset:2048 sc1\n\tbuffer_load_dwordx4 v[124:127], %[vo1], s[40:43], %[pg1] offen offset:3072 sc1\n\t" \
          "Lou_top%=:\n\ts_lshl_b32 %[t], %[av], 10\n\ts_add_u32 %[t], %[t], %[fo]\n\tbuffer_load_dword %[fl], %[vf], %[rf], %[t] offen sc1\n\tds_read_b128 v[32:35], %[own] offset:8192\n\tds_read_b128 v[36:39], %[own] offset:9216\n\t" \
          "ds_read_b128 v[40:43], %[own] offset:10240\n\ts_waitcnt vmcnt(16) lgkmcnt(2)\n\tv_mfma_f32_16x16x32_bf16 v[48:51], a[0:3], v[64:67], 0\n\tds_read_b128 v[44:47], %[own] offset:11264\n\tv_mfma_f32_16x16x32_bf16 v[52:55], a[4:7], v[64:67], 0\n\tv_mfma_f32_16x16x32_bf16 v[56:59], v[128:131], v[64:67], 0\n\t" \
          "v_mfma_f32_16x16x32_bf16 v[60:63], v[32:35], v[64:67], 0\n\tbuffer_load_dwordx4 v[64:67], %[vo0], s[40:43], %[pg2] offen sc1\n\ts_waitcnt vmcnt(16) lgkmcnt(2)\n\tv_mfma_f32_16x16x32_bf16 v[48:51], a[8:11], v[68:71], v[48:51]\n\tds_read_b128 v[32:35], %[own] offset:12288\n\tv_mfma_f32_16x16x32_bf16 v[52:55], a[12:15], v[68:71], v[52:55]\n\t" \
          "v_mfma_f32_16x16x32_bf16 v[56:59], v[132:135], v[68:71], v[56:59]\n\tv_mfma_f32_16x16x32_bf16 v[60:63], v[36:39], v[68:71], v[60:63]\n\tbuffer_load_dwordx4 v[68:71], %[vo0], s[40:43], %[pg2] offen offset:1024 sc1\n\ts_waitcnt vmcnt(16) lgkmcnt(2)\n\tv_mfma_f32_16x16x32_bf16 v[48:51], a[16:19], v[72:75], v[48:51]\n\tds_read_b128 v[36:39], %[own] offset:13312\n\t" \
          "v_mfma_f32_16x16x32_bf16 v[52:55], a[20:23], v[72:75], v[52:55]\n\tv_mfma_f32_16x16x32_bf16 v[56:59], v[136:139], v[72:75], v[56:59]\n\tv_mfma_f32_16x16x32_bf16 v[60:63], v[40:43], v[72:75], v[60:63]\n\tbuffer_load_dwordx4 v[72:75], %[vo0], s[40:43], %[pg2] offen offset:2048 sc1\n\ts_waitcnt vmcnt(16) lgkmcnt(2)\n\tv_mfma_f32_16x16x32_bf16 v[48:51], a[24:27], v[76:79], v[48:51]\n\t" \
          "ds_read_b128 v[40:43], %[own] offset:14336\n\tv_mfma_f32_16x16x32_bf16 v[52:55], a[28:31], v[76:79], v[52:55]\n\tv_mfma_f32_16x16x32_bf16 v[56:59], v[140:143], v[76:79], v[56:59]\n\tv_mfma_f32_16x16x32_bf16 v[60:63], v[44:47], v[76:79], v[60:63]\n\tbuffer_load_dwordx4 v[76:79], %[vo0], s[40:43], %[pg2] offen offset:3072 sc1\n\ts_waitcnt vmcnt(16) lgkmcnt(2)\n\t" \
          "v_mfma_f32_16x16x32_bf16 v[48:51], a[32:35], v[80:83], v[48:51]\n\tds_read_b128 v[44:47], %[own] offset:15360\n\tv_mfma_f32_16x16x32_bf16 v[52:55], a[36:39], v[80:83], v[52:55]\n\tv_mfma_f32_16x16x32_bf16 v[56:59], v[144:147], v[80:83], v[56:59]\n\tv_mfma_f32_16x16x32_bf16 v[60:63], v[32:35], v[80:83], v[60:63]\n\tbuffer_load_dwordx4 v[80:83], %[vo1], s[40:43], %[pg2] offen sc1\n\t" \
          "s_waitcnt vmcnt(16) lgkmcnt(2)\n\tv_mfma_f32_16x16x32_bf16 v[48:51], a[40:43], v[84:87], v[48:51]\n\tds_read_b128 v[32:35], %[own] offset:16384\n\tv_mfma_f32_16x16x32_bf16 v[52:55], a[44:47], v[84:87], v[52:55]\n\tv_mfma_f32_16x16x32_bf16 v[56:59], v[148:151], v[84:87], v[56:59]\n\tv_mfma_f32_16x16x32_bf16 v[60:63], v[36:39], v[84:87], v[60:63]\n\t" \
          "buffer_load_dwordx4 v[84:87], %[vo1], s[40:43], %[pg2] offen offset:1024 sc1\n\ts_waitcnt vmcnt(16) lgkmcnt(2)\n\tv_mfma_f32_16x16x32_bf16 v[48:51], a[48:51], v[88:91], v[48:51]\n\tds_read_b128 v[36:39], %[own] offset:17408\n\tv_mfma_f32_16x16x32_bf16 v[52:55], a[52:55], v[88:91], v[52:55]\n\tv_mfma_f32_16x16x32_bf16 v[56:59], v[152:155], v[88:91], v[56:59]\n\t" \
          "v_mfma_f32_16x16x32_bf16 v[60:63], v[40:43], v[88:91], v[60:63]\n\tbuffer_load_dwordx4 v[88:91], %[vo1], s[40:43], %[pg2] offen offset:2048 sc1\n\ts_waitcnt vmcnt(16) lgkmcnt(2)\n\tv_mfma_f32_16x16x32_bf16 v[48:51], a[56:59], v[92:95], v[48:51]\n\tds_read_b128 v[40:43], %[own] offset:18432\n\tv_mfma_f32_16x16x32_bf16 v[52:55], a[60:63], v[92:95], v[52:55]\n\t" \
          "v_mfma_f32_16x16x32_bf16 v[56:59], v[156:159], v[92:95], v[56:59]\n\tv_mfma_f32_16x16x32_bf16 v[60:63], v[44:47], v[92:95], v[60:63]\n\tbuffer_load_dwordx4 v[92:95], %[vo1], s[40:43], %[pg2] offen offset:3072 sc1\n\ts_waitcnt vmcnt(16) lgkmcnt(2)\n\tv_mfma_f32_16x16x32_bf16 v[48:51], a[64:67], v[96:99], v[48:51]\n\tds_read_b128 v[44:47], %[own] offset:19456\n\t" \
          "v_mfma_f32_16x16x32_bf16 v[52:55], a[68:71], v[96:99], v[52:55]\n\tv_mfma_f32_16x16x32_bf16 v[56:59], v[160:163], v[96:99], v[56:59]\n\tv_mfma_f32_16x16x32_bf16 v[60:63], v[32:35], v[96:99], v[60:63]\n\tbuffer_load_dwordx4 v[96:99], %[vo0], s[40:43], %[pg3] offen sc1\n\ts_waitcnt vmcnt(16) lgkmcnt(2)\n\tv_mfma_f32_16x16x32_bf16 v[48:51], a[72:75], v[100:103], v[48:51]\n\t" \
          "ds_read_b128 v[32:35], %[own] offset:20480\n\tv_mfma_f32_16x16x32_bf16 v[52:55], a[76:79], v[100:103], v[52:55]\n\tv_mfma_f32_16x16x32_bf16 v[56:59], v[164:167], v[100:103], v[56:59]\n\tv_mfma_f32_16x16x32_bf16 v[60:63], v[36:39], v[100:103], v[60:63]\n\tbuffer_load_dwordx4 v[100:103], %[vo0], s[40:43], %[pg3] offen offset:1024 sc1\n\ts_waitcnt vmcnt(16) lgkmcnt(2)\n\t" \
          "v_mfma_f32_16x16x32_bf16 v[48:51], a[80:83], v[104:107], v[48:51]\n\tds_read_b128 v[36:39], %[own] offset:21504\n\tv_mfma_f32_16x16x32_bf16 v[52:55], a[84:87], v[104:107], v[52:55]\n\tv_mfma_f32_16x16x32_bf16 v[56:59], v[168:171], v[104:107], v[56:59]\n\tv_mfma_f32_16x16x32_bf16 v[60:63], v[40:43], v[104:107], v[60:63]\n\tbuffer_load_dwordx4 v[104:107], %[vo0], s[40:43], %[pg3] offen offset:2048 sc1\n\t" \
          "s_waitcnt vmcnt(16) lgkmcnt(2)\n\tv_mfma_f32_16x16x32_bf16 v[48:51], a[88:91], v[108:111], v[48:51]\n\tds_read_b128 v[40:43], %[own] offset:22528\n\tv_mfma_f32_16x16x32_bf16 v[52:55], a[92:95], v[108:111], v[52:55]\n\tv_mfma_f32_16x16x32_bf16 v[56:59], v[172:175], v[108:111], v[56:59]\n\tv_mfma_f32_16x16x32_bf16 v[60:63], v[44:47], v[108:111], v[60:63]\n\t" \
          "buffer_load_dwordx4 v[108:111], %[vo0], s[40:43], %[pg3] offen offset:3072 sc1\n\ts_waitcnt vmcnt(16) lgkmcnt(2)\n\tv_mfma_f32_16x16x32_bf16 v[48:51], a[96:99], v[112:115], v[48:51]\n\tds_read_b128 v[44:47], %[own] offset:23552\n\tv_mfma_f32_16x16x32_bf16 v[52:55], a[100:103], v[112:115], v[52:55]\n\tv_mfma_f32_16x16x32_bf16 v[56:59], v[176:179], v[112:115], v[56:59]\n\t" \
          "v_mfma_f32_16x16x32_bf16 v[60:63], v[32:35], v[112:115], v[60:63]\n\tbuffer_load_dwordx4 v[112:115], %[vo1], s[40:43], %[pg3] offen sc1\n\ts_waitcnt vmcnt(16) lgkmcnt(2)\n\tv_mfma_f32_16x16x32_bf16 v[48:51], a[104:107], v[116:119], v[48:51]\n\tds_read_b128 v[32:35], %[own] offset:24576\n\tv_mfma_f32_16x16x32_bf16 v[52:55], a[108:111], v[116:119], v[52:55]\n\t" \
          "v_mfma_f32_16x16x32_bf16 v[56:59], v[180:183], v[116:119], v[56:59]\n\tv_mfma_f32_16x16x32_bf16 v[60:63], v[36:39], v[116:119], v[60:63]\n\tbuffer_load_dwordx4 v[116:119], %[vo1], s[40:43], %[pg3] offen offset:1024 sc1\n\ts_waitcnt vmcnt(16) lgkmcnt(2)\n\tv_mfma_f32_16x16x32_bf16 v[48:51], a[112:115], v[120:123], v[48:51]\n\tds_read_b128 v[36:39], %[own] offset:25600\n\t" \
          "v_mfma_f32_16x16x32_bf16 v[52:55], a[116:119], v[120:123], v[52:55]\n\tv_mfma_f32_16x16x32_bf16 v[56:59], v[184:187], v[120:123], v[56:59]\n\tv_mfma_f32_16x16x32_bf16 v[60:63], v[40:43], v[120:123], v[60:63]\n\tbuffer_load_dwordx4 v[120:123], %[vo1], s[40:43], %[pg3] offen offset:2048 sc1\n\ts_waitcnt vmcnt(16) lgkmcnt(2)\n\tv_mfma_f32_16x16x32_bf16 v[48:51], a[120:123], v[124:127], v[48:51]\n\t" \
          "ds_read_b128 v[40:43], %[own] offset:26624\n\tv_mfma_f32_16x16x32_bf16 v[52:55], a[124:127], v[124:127], v[52:55]\n\tv_mfma_f32_16x16x32_bf16 v[56:59], v[188:191], v[124:127], v[56:59]\n\tv_mfma_f32_16x16x32_bf16 v[60:63], v[44:47], v[124:127], v[60:63]\n\tbuffer_load_dwordx4 v[124:127], %[vo1], s[40:43], %[pg3] offen offset:3072 sc1\n\ts_waitcnt vmcnt(15) lgkmcnt(2)\n\t" \
          "v_mfma_f32_16x16x32_bf16 v[48:51], a[128:131], v[64:67], v[48:51]\n\tds_read_b128 v[44:47], %[own] offset:27648\n\tv_mfma_f32_16x16x32_bf16 v[52:55], a[132:135], v[64:67], v[52:55]\n\tv_cmp_eq_u32 vcc, 0, %[fl]\n\ts_nop 0\n\ts_ff1_i32_b64 %[t], vcc\n\t" \
          "s_lshr_b32 %[t], %[t], 1\n\ts_add_u32 %[av], %[av], %[t]\n\tv_mfma_f32_16x16x32_bf16 v[56:59], v[192:195], v[64:67], v[56:59]\n\ts_lshl_b32 %[oo], %[j], 16\n\ts_cmp_lt_u32 %[j], %[j1]\n\ts_cselect_b32 %[t], 2, 4\n\t" \
          "s_add_u32 %[j], %[j], %[t]\n\ts_lshl_b32 %[t], %[t], 14\n\ts_add_u32 %[pg0], %[pg0], %[t]\n\ts_add_u32 %[pg1], %[pg1], %[t]\n\ts_add_u32 %[pg2], %[pg2], %[t]\n\ts_add_u32 %[pg3], %[pg3], %[t]\n\t" \
          "s_lshr_b32 %[t], %[j], 1\n\ts_cmp_lt_u32 %[t], %[av]\n\ts_cselect_b32 s46, s42, 0\n\tv_mfma_f32_16x16x32_bf16 v[60:63], v[32:35], v[64:67], v[60:63]\n\tbuffer_load_dwordx4 v[64:67], %[vo0], s[44:47], %[pg0] offen sc1\n\ts_waitcnt vmcnt(15) lgkmcnt(2)\n\t" \
          "v_mfma_f32_16x16x32_bf16 v[48:51], a[136:139], v[68:71], v[48:51]\n\tds_read_b128 v[32:35], %[own] offset:28672\n\tv_mfma_f32_16x16x32_bf16 v[52:55], a[140:143], v[68:71], v[52:55]\n\tv_mfma_f32_16x16x32_bf16 v[56:59], v[196:199], v[68:71], v[56:59]\n\tv_mfma_f32_16x16x32_bf16 v[60:63], v[36:39], v[68:71], v[60:63]\n\tbuffer_load_dwordx4 v[68:71], %[vo0], s[44:47], %[pg0] offen offset:1024 sc1\n\t" \
          "s_waitcnt vmcnt(15) lgkmcnt(2)\n\tv_mfma_f32_16x16x32_bf16 v[48:51], a[144:147], v[72:75], v[48:51]\n\tds_read_b128 v[36:39], %[own] offset:29696\n\tv_mfma_f32_16x16x32_bf16 v[52:55], a[148:151], v[72:75], v[52:55]\n\tv_mfma_f32_16x16x32_bf16 v[56:59], v[200:203], v[72:75], v[56:59]\n\tv_mfma_f32_16x16x32_bf16 v[60:63], v[40:43], v[72:75], v[60:63]\n\t" \
          "buffer_load_dwordx4 v[72:75], %[vo0], s[44:47], %[pg0] offen offset:2048 sc1\n\ts_waitcnt vmcnt(15) lgkmcnt(2)\n\tv_mfma_f32_16x16x32_bf16 v[48:51], a[152:155], v[76:79], v[48:51]\n\tds_read_b128 v[40:43], %[own] offset:30720\n\tv_mfma_f32_16x16x32_bf16 v[52:55], a[156:159], v[76:79], v[52:55]\n\tv_mfma_f32_16x16x32_bf16 v[56:59], v[204:207], v[76:79], v[56:59]\n\t" \
          "v_mfma_f32_16x16x32_bf16 v[60:63], v[44:47], v[76:79], v[60:63]\n\tbuffer_load_dwordx4 v[76:79], %[vo0], s[44:47], %[pg0] offen offset:3072 sc1\n\ts_waitcnt vmcnt(15) lgkmcnt(2)\n\tv_mfma_f32_16x16x32_bf16 v[48:51], a[160:163], v[80:83], v[48:51]\n\tds_read_b128 v[44:47], %[own] offset:31744\n\tv_mfma_f32_16x16x32_bf16 v[52:55], a[164:167], v[80:83], v[52:55]\n\t" \
          "v_mfma_f32_16x16x32_bf16 v[56:59], v[208:211], v[80:83], v[56:59]\n\tv_mfma_f32_16x16x32_bf16 v[60:63], v[32:35], v[80:83], v[60:63]\n\tbuffer_load_dwordx4 v[80:83], %[vo1], s[44:47], %[pg0] offen sc1\n\ts_waitcnt vmcnt(15) lgkmcnt(2)\n\tv_mfma_f32_16x16x32_bf16 v[48:51], a[168:171], v[84:87], v[48:51]\n\tds_read_b128 v[32:35], %[own] offset:0\n\t" \
          "v_mfma_f32_16x16x32_bf16 v[52:55], a[172:175], v[84:87], v[52:55]\n\tv_mfma_f32_16x16x32_bf16 v[56:59], v[212:215], v[84:87], v[56:59]\n\tv_mfma_f32_16x16x32_bf16 v[60:63], v[36:39], v[84:87], v[60:63]\n\tbuffer_load_dwordx4 v[84:87], %[vo1], s[44:47], %[pg0] offen offset:1024 sc1\n\ts_waitcnt vmcnt(15) lgkmcnt(2)\n\tv_mfma_f32_16x16x32_bf16 v[48:51], a[176:179], v[88:91], v[48:51]\n\t" \
          "ds_read_b128 v[36:39], %[own] offset:1024\n\tv_mfma_f32_16x16x32_bf16 v[52:55], a[180:183], v[88:91], v[52:55]\n\tv_mfma_f32_16x16x32_bf16 v[56:59], v[216:219], v[88:91], v[56:59]\n\tv_mfma_f32_16x16x32_bf16 v[60:63], v[40:43], v[88:91], v[60:63]\n\tbuffer_load_dwordx4 v[88:91], %[vo1], s[44:47], %[pg0] offen offset:2048 sc1\n\ts_waitcnt vmcnt(15) lgkmcnt(2)\n\t" \
          "v_mfma_f32_16x16x32_bf16 v[48:51], a[184:187], v[92:95], v[48:51]\n\tds_read_b128 v[40:43], %[own] offset:2048\n\tv_mfma_f32_16x16x32_bf16 v[52:55], a[188:191], v[92:95], v[52:55]\n\tv_mfma_f32_16x16x32_bf16 v[56:59], v[220:223], v[92:95], v[56:59]\n\tv_mfma_f32_16x16x32_bf16 v[60:63], v[44:47], v[92:95], v[60:63]\n\tbuffer_load_dwordx4 v[92:95], %[vo1], s[44:47], %[pg0] offen offset:3072 sc1\n\t" \
          "s_waitcnt vmcnt(15) lgkmcnt(2)\n\tv_mfma_f32_16x16x32_bf16 v[48:51], a[192:195], v[96:99], v[48:51]\n\tds_read_b128 v[44:47], %[own] offset:3072\n\tv_mfma_f32_16x16x32_bf16 v[52:55], a[196:199], v[96:99], v[52:55]\n\tv_mfma_f32_16x16x32_bf16 v[56:59], v[224:227], v[96:99], v[56:59]\n\tv_mfma_f32_16x16x32_bf16 v[60:63], v[32:35], v[96:99], v[60:63]\n\t" \
          "buffer_load_dwordx4 v[96:99], %[vo0], s[44:47], %[pg1] offen sc1\n\ts_waitcnt vmcnt(15) lgkmcnt(2)\n\tv_mfma_f32_16x16x32_bf16 v[48:51], a[200:203], v[100:103], v[48:51]\n\tds_read_b128 v[32:35], %[own] offset:4096\n\tv_mfma_f32_16x16x32_bf16 v[52:55], a[204:207], v[100:103], v[52:55]\n\tv_mfma_f32_16x16x32_bf16 v[56:59], v[228:231], v[100:103], v[56:59]\n\t" \
          "v_mfma_f32_16x16x32_bf16 v[60:63], v[36:39], v[100:103], v[60:63]\n\tbuffer_load_dwordx4 v[100:103], %[vo0], s[44:47], %[pg1] offen offset:1024 sc1\n\ts_waitcnt vmcnt(15) lgkmcnt(2)\n\tv_mfma_f32_16x16x32_bf16 v[48:51], a[208:211], v[104:107], v[48:51]\n\tds_read_b128 v[36:39], %[own] offset:5120\n\tv_mfma_f32_16x16x32_bf16 v[52:55], a[212:215], v[104:107], v[52:55]\n\t" \
          "v_mfma_f32_16x16x32_bf16 v[56:59], v[232:235], v[104:107], v[56:59]\n\tv_mfma_f32_16x16x32_bf16 v[60:63], v[40:43], v[104:107], v[60:63]\n\tbuffer_load_dwordx4 v[104:107], %[vo0], s[44:47], %[pg1] offen offset:2048 sc1\n\ts_waitcnt vmcnt(15) lgkmcnt(2)\n\tv_mfma_f32_16x16x32_bf16 v[48:51], a[216:219], v[108:111], v[48:51]\n\tds_read_b128 v[40:43], %[own] offset:6144\n\t" \
          "v_mfma_f32_16x16x32_bf16 v[52:55], a[220:223], v[108:111], v[52:55]\n\tv_mfma_f32_16x16x32_bf16 v[56:59], v[236:239], v[108:111], v[56:59]\n\tv_mfma_f32_16x16x32_bf16 v[60:63], v[44:47], v[108:111], v[60:63]\n\tbuffer_load_dwordx4 v[108:111], %[vo0], s[44:47], %[pg1] offen offset:3072 sc1\n\ts_waitcnt vmcnt(15) lgkmcnt(2)\n\tv_mfma_f32_16x16x32_bf16 v[48:51], a[224:227], v[112:115], v[48:51]\n\t" \
          "ds_read_b128 v[44:47], %[own] offset:7168\n\tv_mfma_f32_16x16x32_bf16 v[52:55], a[228:231], v[112:115], v[52:55]\n\tv_mfma_f32_16x16x32_bf16 v[56:59], v[240:243], v[112:115], v[56:59]\n\tv_mfma_f32_16x16x32_bf16 v[60:63], v[32:35], v[112:115], v[60:63]\n\tbuffer_load_dwordx4 v[112:115], %[vo1], s[44:47], %[pg1] offen sc1\n\ts_waitcnt vmcnt(15) lgkmcnt(2)\n\t" \
          "v_mfma_f32_16x16x32_bf16 v[48:51], a[232:235], v[116:119], v[48:51]\n\tv_mfma_f32_16x16x32_bf16 v[52:55], a[236:239], v[116:119], v[52:55]\n\tv_mfma_f32_16x16x32_bf16 v[56:59], v[244:247], v[116:119], v[56:59]\n\tv_mfma_f32_16x16x32_bf16 v[60:63], v[36:39], v[116:119], v[60:63]\n\tbuffer_load_dwordx4 v[116:119], %[vo1], s[44:47], %[pg1] offen offset:1024 sc1\n\ts_waitcnt vmcnt(15) lgkmcnt(1)\n\t" \
          "v_mfma_f32_16x16x32_bf16 v[48:51], a[240:243], v[120:123], v[48:51]\n\tv_mfma_f32_16x16x32_bf16 v[52:55], a[244:247], v[120:123], v[52:55]\n\tv_mfma_f32_16x16x32_bf16 v[56:59], v[248:251], v[120:123], v[56:59]\n\tv_mfma_f32_16x16x32_bf16 v[60:63], v[40:43], v[120:123], v[60:63]\n\tbuffer_load_dwordx4 v[120:123], %[vo1], s[44:47], %[pg1] offen offset:2048 sc1\n\ts_waitcnt vmcnt(15) lgkmcnt(0)\n\t" \
          "v_mfma_f32_16x16x32_bf16 v[48:51], a[248:251], v[124:127], v[48:51]\n\tv_mfma_f32_16x16x32_bf16 v[52:55], a[252:255], v[124:127], v[52:55]\n\tv_mfma_f32_16x16x32_bf16 v[56:59], v[252:255], v[124:127], v[56:59]\n\tv_mfma_f32_16x16x32_bf16 v[60:63], v[44:47], v[124:127], v[60:63]\n\tbuffer_load_dwordx4 v[124:127], %[vo1], s[44:47], %[pg1] offen offset:3072 sc1\n\ts_nop 6\n\t" \
          "ds_write_b128 %[rw_], v[48:51] offset:0\n\tds_write_b128 %[rw_], v[52:55] offset:1024\n\tds_write_b128 %[rw_], v[56:59] offset:2048\n\tds_write_b128 %[rw_], v[60:63] offset:3072\n\ts_waitcnt lgkmcnt(0)\n\ts_barrier\n\t" \
          "ds_read_b128 v[32:35], %[rr0] offset:0\n\tds_read_b128 v[36:39], %[rr0] offset:40960\n\tds_read_b128 v[40:43], %[rr1] offset:0\n\tds_read_b128 v[44:47], %[rr1] offset:40960\n\tv_xor_b32 %[rw_], 0x1000, %[rw_]\n\tv_xor_b32 %[rr0], 0x1000, %[rr0]\n\t" \
          "v_xor_b32 %[rr1], 0x1000, %[rr1]\n\ts_waitcnt lgkmcnt(0)\n\tv_pk_add_f32 v[32:33], v[32:33], v[36:37]\n\tv_pk_add_f32 v[34:35], v[34:35], v[38:39]\n\tv_pk_add_f32 v[40:41], v[40:41], v[44:45]\n\tv_pk_add_f32 v[42:43], v[42:43], v[46:47]\n\t" \
          "v_pk_add_f32 v[32:33], v[32:33], v[40:41]\n\tv_pk_add_f32 v[34:35], v[34:35], v[42:43]\n\ts_nop 0\n\tv_cvt_pk_bf16_f32 v44, v32, v33\n\tv_cvt_pk_bf16_f32 v45, v34, v35\n\tbuffer_store_dwordx2 v[44:45], %[so], %[rc], %[oo] offen" OU_AUX "\n\t" \
          "s_cmp_lg_u32 s46, 0\n\ts_cbranch_scc1 Lou_top%=\n\ts_cmp_lt_u32 %[j], 128\n\ts_cbranch_scc1 Lou_wait%=\n\ts_waitcnt vmcnt(0)\n\t"
#define OU_CLOB "v32", "v33", "v34", "v35", "v36", "v37", "v38", "v39", "v40", "v41", "v42", "v43", "v44", "v45", "v46", "v47", "v48", "v49", "v50", "v51", "v52", "v53", "v54", "v55", "v56", "v57", "v58", "v59", "v60", "v61", "v62", "v63", "v64", "v65", "v66", "v67", "v68", "v69", "v70", "v71", "v72", "v73", "v74", "v75", "v76", "v77", "v78", "v79", "v80", "v81", "v82", "v83", "v84", "v85", "v86", "v87", "v88", "v89", "v90", "v91", "v92", "v93", "v94", "v95", "v96", "v97", "v98", "v99", "v100", "v101", "v102", "v103", "v104", "v105", "v106", "v107", "v108", "v109", "v110", "v111", "v112", "v113", "v114", "v115", "v116", "v117", "v118", "v119", "v120", "v121", "v122", "v123", "v124", "v125", "v126", "v127", "v128", "v129", "v130", "v131", "v132", "v133", "v134", "v135", "v136", "v137", "v138", "v139", "v140", "v141", "v142", "v143", "v144", "v145", "v146", "v147", "v148", "v149", "v150", "v151", "v152", "v153", "v154", "v155", "v156", "v157", "v158", "v159", "v160", "v161", "v162", "v163", "v164", "v165", "v166", "v167", "v168", "v169", "v170", "v171", "v172", "v173", "v174", "v175", "v176", "v177", "v178", "v179", "v180", "v181", "v182", "v183", "v184", "v185", "v186", "v187", "v188", "v189", "v190", "v191", "v192", "v193", "v194", "v195", "v196", "v197", "v198", "v199", "v200", "v201", "v202", "v203", "v204", "v205", "v206", "v207", "v208", "v209", "v210", "v211", "v212", "v213", "v214", "v215", "v216", "v217", "v218", "v219", "v220", "v221", "v222", "v223", "v224", "v225", "v226", "v227", "v228", "v229", "v230", "v231", "v232", "v233", "v234", "v235", "v236", "v237", "v238", "v239", "v240", "v241", "v242", "v243", "v244", "v245", "v246", "v247", "v248", "v249", "v250", "v251", "v252", "v253", "v254", "v255", "a0", "a1", "a2", "a3", "a4", "a5", "a6", "a7", "a8", "a9", "a10", "a11", "a12", "a13", "a14", "a15", "a16", "a17", "a18", "a19", "a20", "a21", "a22", "a23", "a24", "a25", "a26", "a27", "a28", "a29", "a30", "a31", "a32", "a33", "a34", "a35", "a36", "a37", "a38", "a39", "a40", "a41", "a42", "a43", "a44", "a45", "a46", "a47", "a48", "a49", "a50", "a51", "a52", "a53", "a54", "a55", "a56", "a57", "a58", "a59", "a60", "a61", "a62", "a63", "a64", "a65", "a66", "a67", "a68", "a69", "a70", "a71", "a72", "a73", "a74", "a75", "a76", "a77", "a78", "a79", "a80", "a81", "a82", "a83", "a84", "a85", "a86", "a87", "a88", "a89", "a90", "a91", "a92", "a93", "a94", "a95", "a96", "a97", "a98", "a99", "a100", "a101", "a102", "a103", "a104", "a105", "a106", "a107", "a108", "a109", "a110", "a111", "a112", "a113", "a114", "a115", "a116", "a117", "a118", "a119", "a120", "a121", "a122", "a123", "a124", "a125", "a126", "a127", "a128", "a129", "a130", "a131", "a132", "a133", "a134", "a135", "a136", "a137", "a138", "a139", "a140", "a141", "a142", "a143", "a144", "a145", "a146", "a147", "a148", "a149", "a150", "a151", "a152", "a153", "a154", "a155", "a156", "a157", "a158", "a159", "a160", "a161", "a162", "a163", "a164", "a165", "a166", "a167", "a168", "a169", "a170", "a171", "a172", "a173", "a174", "a175", "a176", "a177", "a178", "a179", "a180", "a181", "a182", "a183", "a184", "a185", "a186", "a187", "a188", "a189", "a190", "a191", "a192", "a193", "a194", "a195", "a196", "a197", "a198", "a199", "a200", "a201", "a202", "a203", "a204", "a205", "a206", "a207", "a208", "a209", "a210", "a211", "a212", "a213", "a214", "a215", "a216", "a217", "a218", "a219", "a220", "a221", "a222", "a223", "a224", "a225", "a226", "a227", "a228", "a229", "a230", "a231", "a232", "a233", "a234", "a235", "a236", "a237", "a238", "a239", "a240", "a241", "a242", "a243", "a244", "a245", "a246", "a247", "a248", "a249", "a250", "a251", "a252", "a253", "a254", "a255", "s40", "s41", "s42", "s43", "s44", "s45", "s46", "s47"
__device__ __forceinline__ void out_unit(const FParams& F, unsigned long long parq, int set, int un, int w) {
  const int lane = opaque_lane(), l15 = lane & 15, lq = lane >> 4, r = un & 7;
  const __amdgpu_buffer_rsrc_t rw = __builtin_amdgcn_make_buffer_rsrc(wg::uni(F.Wo + 64 * un), 0, HDA * DM * 2 - 128 * un, 0x00020000);
  const __amdgpu_buffer_rsrc_t rc = __builtin_amdgcn_make_buffer_rsrc(wg::uni(F.out + 64 * un), 0, S_ * 2 * DM * 2 - 128 * un, 0x00020000);
  const __amdgpu_buffer_rsrc_t rf = __builtin_amdgcn_make_buffer_rsrc(F.ctl, 0, sizeof(Ctl), 0x00020000);
  const int own = w * 40960 + lane * 16;
  int j = (set < 2 ? 2 * OSEG0 : TP(F.oj1, OJ1)) + set, av = OSEG0, pg[4], t, cnt, oo, fl, rw_ = own + 32768, rr0 = 32768 + (l15 >> 2) * 1024 + ((l15 & 3) * 16 + 4 * w + lq) * 16, rr1 = rr0 + 81920;
#pragma unroll
  for (int i = 0; i < 4; ++i) pg[i] = __builtin_amdgcn_readfirstlane(qoff(parq, 4 * w + (i ^ (r >> 1))) + j * QBLK);
  const int rt = w * 40960 + (8 * lq + (l15 >> 2)) * 128 + (((l15 >> 3) | ((lq & 1) << 1)) << 5) + (l15 & 3) * 8, vw = (lane >> 3) * 2048 + (((lane & 7) ^ ((lq & 1) << 1)) << 4);
  asm volatile(OU_BODY
    : [j] "+s"(j), [av] "+s"(av), [pg0] "+s"(pg[0]), [pg1] "+s"(pg[1]), [pg2] "+s"(pg[2]), [pg3] "+s"(pg[3]), [t] "=&s"(t), [cnt] "=&s"(cnt), [oo] "=&s"(oo), [fl] "=&v"(fl), [rw_] "+v"(rw_), [rr0] "+v"(rr0), [rr1] "+v"(rr1)
    : [vo0] "v"(lane * 16 + (r & 1) * 4096), [vo1] "v"(lane * 16 + (~r & 1) * 4096), [vw0] "v"(vw), [vw1] "v"(vw ^ 64), [rt0] "v"(rt), [rt1] "v"(rt ^ 32), [rt2] "v"(rt ^ 64), [rt3] "v"(rt ^ 96), [own] "v"(own),
      [vf] "v"(lane < 8 ? (lane >> 1) * 1024 + (2 * w + (lane & 1)) * 128 : 0x7ffffff0), [so] "v"((4 * w + lq) * 4096 + l15 * 8),
      [qa] "s"(F.qkvA), [qs] "s"(QSIZE), [rw] "s"(rw), [rc] "s"(rc), [rf] "s"(rf), [wl] "s"(w * 40960), [wo] "s"((1024 * w + 128 * r) * 2048), [fo] "s"((int)offsetof(Ctl, flagO)), [tmo] "s"((int)offsetof(Ctl, tmo)), [j1] "s"(TP(F.oj1, OJ1))
    : OU_CLOB, "vcc", "scc", "memory");
}

__device__ __forceinline__ void attnB_task(const FParams& F, unsigned long long parq, int r, int pg, u16* vs  , int w) {
  const int lane = opaque_lane(), l15 = lane & 15, lq = lane >> 4;
  const __amdgpu_buffer_rsrc_t rs = __builtin_amdgcn_make_buffer_rsrc(F.qkvA, 0, QSIZE, 0x00020000);
  const int so = 8 * r * QBLK + w * 2048, oq = so + qoff(parq, 48 + pg), ok = so + qoff(parq, 52 + pg), ov = so + qoff(parq, 56 + pg), p0 = r ? -QBLK : 0;
  v4u q[2][8], k[2][9], v[2][9];
#pragma unroll
  for (int hh = 0; hh < 2; ++hh) {
#pragma unroll
    for (int i = 0; i < 9; ++i) v[hh][i] = __builtin_amdgcn_raw_buffer_load_b128(rs, lane * 16 + hh * 1024, ov + (i ? (i - 1) * QBLK : p0), 0);
#pragma unroll
    for (int i = 0; i < 9; ++i) { k[hh][i] = __builtin_amdgcn_raw_buffer_load_b128(rs, lane * 16 + hh * 1024, ok + (i ? (i - 1) * QBLK : p0), 0); if (i < 8) q[hh][i] = __builtin_amdgcn_raw_buffer_load_b128(rs, lane * 16 + hh * 1024, oq + i * QBLK, 0); }
  }
  const float scale = 0.17677669529663687f, L2E = 1.4426950408889634f;
  const bool par = l15 & 1, ownA = l15 - 4 * lq - par >= 0, ownB = l15 - 4 * lq - par - 2 >= 0;
  const unsigned sa = par ? 0x01000c0cu : 0x0c0c0100u, sb = par ? 0x03020c0cu : 0x0c0c0302u, sz = 0x0c0c0c0cu;
  const unsigned sel[4] = {ownA ? sz : sa, ownB ? sz : sb, ownA ? sa : sz, ownB ? sb : sz};
  const u16* vp = &vs[(4 * lq + (l15 >> 2)) * 40 + 16 * (l15 & 1) + 2 * (l15 & 2)];
#pragma unroll
  for (int hh = 0; hh < 2; ++hh) {
    SB0;
#pragma unroll
    for (int i = 0; i < 9; ++i) *(v4u*)&vs[(16 * i + l15) * 40 + 8 * lq] = v[hh][i];
#pragma unroll
    for (int i = 0; i < 8; ++i) {
      const f32x4 z = {0.f, 0.f, 0.f, 0.f}, s0 = mf(k[hh][i], q[hh][i], z), s1 = mf(k[hh][i + 1], q[hh][i], z);
      float a = ownA ? (par ? s1[1] : s1[0]) : (par ? s0[1] : s0[0]), b = ownB ? (par ? s1[3] : s1[2]) : (par ? s0[3] : s0[2]);
      unsigned p = pk2(a, b);
      p = pk2(__uint_as_float(p << 16) * scale, __uint_as_float(p & 0xffff0000u) * scale);
      a = __uint_as_float(p << 16); b = __uint_as_float(p & 0xffff0000u);
      const bool va = i > 0 || r > 0 || ownA, vb = i > 0 || r > 0 || ownB;
      const float mx = xmax32(xmax16(fmaxf(va ? a : -INFINITY, vb ? b : -INFINITY)));
      a = va ? __builtin_amdgcn_exp2f((a - mx) * L2E) : 0.f; b = vb ? __builtin_amdgcn_exp2f((b - mx) * L2E) : 0.f;
      const float inv = __builtin_amdgcn_rcpf(xsum32(xsum16(a + b)));
      p = pk2(a * inv, b * inv);
      const v4u px = {__builtin_amdgcn_perm(0u, p, sel[0]), __builtin_amdgcn_perm(0u, p, sel[1]), __builtin_amdgcn_perm(0u, p, sel[2]), __builtin_amdgcn_perm(0u, p, sel[3])};
      f32x4 o[2];
#pragma unroll
      for (int ab = 0; ab < 2; ++ab) { const v2u lo = trrd(vp + 16 * i * 40 + 8 * ab), hi = trrd(vp + 16 * (i + 1) * 40 + 8 * ab); o[ab] = mf((v4u){lo.x, lo.y, hi.x, hi.y}, px, z); }
      __builtin_amdgcn_raw_buffer_store_b128((v4u){pk2(o[0][0], o[0][1]), pk2(o[0][2], o[0][3]), pk2(o[1][0], o[1][1]), pk2(o[1][2], o[1][3])}, rs, lane * 16 + hh * 1024, oq + i * QBLK, 16);
    }
  }
}

constexpr int NTASK = 240;
__constant__ unsigned short TASKS[NTASK] = {
  5120, 5121, 5122, 5123, 5124, 5125, 5126, 5127, 5128, 5129, 5130, 5131, 5132, 5133, 5134, 5135, 5136, 5137, 5138, 5139, 5140, 5141, 5142, 5143,
  3072, 3073, 3074, 3075, 3076, 3077, 3078, 3079, 3080, 3081, 3082, 3083, 3084, 3085, 3086, 3087, 3088, 3089, 3090, 3091, 3092, 3093, 3094, 3095,
  6144, 6145, 6146, 6147, 6148, 6149, 6150, 6151, 5144, 5145, 5146, 5147, 5148, 5149, 5150, 5151, 4096, 4097, 4098, 4099, 4100, 4101, 4102, 4103,
  4104, 4105, 4106, 4107, 4108, 4109, 4110, 4111, 4112, 4113, 4114, 4115, 4116, 4117, 4118, 4119, 6152, 6153, 6154, 6155, 6156, 6157, 6158, 6159,
  5152, 5153, 5154, 5155, 5156, 5157, 5158, 5159, 5160, 5161, 5162, 5163, 5164, 5165, 5166, 5167, 3096, 3097, 3098, 3099, 3100, 3101, 3102, 3103,
  3104, 3105, 3106, 3107, 3108, 3109, 3110, 3111, 5168, 5169, 5170, 5171, 5172, 5173, 5174, 5175, 4120, 4121, 4122, 4123, 4124, 4125, 4126, 4127,
  4128, 4129, 4130, 4131, 4132, 4133, 4134, 4135, 5176, 5177, 5178, 5179, 5180, 5181, 5182, 5183, 7168, 7169, 7170, 7171, 7172, 7173, 7174, 7175,
  7176, 7177, 7178, 7179, 7180, 7181, 7182, 7183, 7184, 7185, 7186, 7187, 7188, 7189, 7190, 7191, 7192, 7193, 7194, 7195, 7196, 7197, 7198, 7199,
  3112, 3113, 3114, 3115, 3116, 3117, 3118, 3119, 3120, 3121, 3122, 3123, 3124, 3125, 3126, 3127, 3128, 3129, 3130, 3131, 3132, 3133, 3134, 3135,
  4136, 4137, 4138, 4139, 4140, 4141, 4142, 4143, 4144, 4145, 4146, 4147, 4148, 4149, 4150, 4151, 4152, 4153, 4154, 4155, 4156, 4157, 4158, 4159,
};
__device__ __forceinline__ const FParams& args() {
  typedef const __attribute__((address_space(4))) FParams* KP;
  KP p = (KP)__builtin_amdgcn_kernarg_segment_ptr(); asm volatile("" : "+s"(p)); return *(const FParams*)p;
}
__device__ __forceinline__ void wait_until(unsigned t) { while ((int)(t - (unsigned)__builtin_amdgcn_s_memrealtime()) > 0) __builtin_amdgcn_s_sleep(8); }
template <int AUX = 0>
__device__ __forceinline__ void touch(const void* base, int bytes, int voff, int soff, char* lds) {
  __builtin_amdgcn_raw_ptr_buffer_load_lds(__builtin_amdgcn_make_buffer_rsrc((void*)base, 0, bytes, 0x00020000), (__attribute__((address_space(3))) void*)(lds + wg::LDS_USE + 1024), 4, voff, soff, 0, AUX);
}
constexpr int RECO = 32768;
__device__ __forceinline__ void setup(const FParams& F, int n, int w) {
  const int lane = opaque_lane(), tid = 64 * w + lane;
  const v4u ff = {0xffffffffu, 0xffffffffu, 0xffffffffu, 0xffffffffu}, ep = rdy_load(F);
  const __amdgpu_buffer_rsrc_t rs = __builtin_amdgcn_make_buffer_rsrc(F.ring, 0, QPAGE + QSIZE, 0x00020000);
  const int o = ((lane & 16) ? RSIZE + n * 128 : (1 + n) * QPAGE) + ((lane >> 3) & 1) * 8192 + (lane & 7) * 16;
  __builtin_amdgcn_raw_buffer_store_b128(ff, rs, tid < 32 ? o : 0x7ffffff0, 0, 16);
#pragma unroll 2
  for (int i = n * 256 + tid; i < RSIZE / 16; i += NPAGE * 256) {
    v4u v = ff;
    const int m = i - ROFF_M / 16;
    if ((unsigned)m < 2048u) {
      const int q = (m >> 10) * 512 + (m & 511);
      const u16* src = F.mem0 + (((q >> 4) & 1) * 4 + ((q >> 1) & 3)) * DM + 32 * (q >> 5) + 8 * ((q >> 3) & 1) + 4 * (q & 1);
#pragma unroll
      for (int p = 0; p < 4; ++p) v[p] = nosent(src[p] | ((unsigned)src[p + 16] << 16));
    }
    __builtin_amdgcn_raw_buffer_store_b128(v, rs, i * 16, 0, 16);
  }
  const __amdgpu_buffer_rsrc_t rk = __builtin_amdgcn_make_buffer_rsrc(F.kvm + SSEG0 * KVSEG, 0, (NSEG - SSEG0) * KVSEG, 0x00020000);
#pragma unroll
  for (int q = 0; q < 3; ++q) __builtin_amdgcn_raw_buffer_store_b128(ff, rk, ((q * NPAGE + n) * 256 + tid) * 16, 0, 16);
  __builtin_amdgcn_raw_buffer_store_b128((v4u){0u, 0u, 0u, 0u}, __builtin_amdgcn_make_buffer_rsrc(F.ctl, 0, sizeof(Ctl), 0x00020000), w >= 2 ? (n * 128 + tid - 128) * 16 : 0x7ffffff0, 0, 16);
  static_assert(sizeof(Ctl) <= NPAGE * 128 * 16 && (NSEG - SSEG0) * KVSEG <= 3 * NPAGE * 4096, "");
  const unsigned long long tag = rdy_tag(ep);
  asm volatile("s_waitcnt vmcnt(0)" ::: "memory");
  __syncthreads();
  if (w == 0) {
    unsigned best[4] = {~0u, ~0u, ~0u, ~0u};
#pragma unroll 1
    for (int r = 0; r < TP(F.probes, PROBES); ++r)
#pragma unroll
      for (int t = 0; t < 4; ++t) {
        const unsigned long long t0 = __builtin_amdgcn_s_memtime();
        __builtin_amdgcn_raw_buffer_store_b128(ff, rs, (lane >> 3) == t ? o : 0x7ffffff0, 0, 16);
        asm volatile("s_waitcnt vmcnt(0)" ::: "memory");
        best[t] = min(best[t], (unsigned)(__builtin_amdgcn_s_memtime() - t0));
      }
    unsigned xcc; asm volatile("s_getreg_b32 %0, hwreg(HW_REG_XCC_ID)" : "=s"(xcc));
    const bool flip = ((xcc ^ (xcc - blockIdx.x + 1)) >> 2) & 1;
    const unsigned bits = ((best[1] < best[0]) == flip ? 1 : 0) | ((best[3] < best[2]) != flip ? 4 : 0);
    __builtin_amdgcn_raw_buffer_store_b128((v4u){(unsigned)tag, (unsigned)(tag >> 32), bits, 0u}, __builtin_amdgcn_make_buffer_rsrc(F.rdy, 0, RDY_SIZE, 0x00020000), lane < 8 ? n * 128 + lane * 16 : 0x7ffffff0, 0, 16);
  }
}
__device__ __forceinline__ void worker(const FParams& F, unsigned cz, char* lds, const int w0) {
#define lane opaque_lane()
#define tid (64 * w + opaque_lane())
  const int grp = (blockIdx.x & 7) - 3, slot = blockIdx.x >> 3;
#define TLOG(t, e)
  unsigned long long par, tag;
  { int w = w0; v4u rec;
    const unsigned t0 = (unsigned)__builtin_amdgcn_s_memrealtime();
    const int id = slot * 4 + w, tz = opaque_lane();
    if (grp != 2) {
      if (grp > 2) { if (slot < 30) setup(F, (grp - 3) * 30 + slot, w); TLOG(1024 + grp * 32 + slot, 0); wait_until(t0 + TP(F.stag, STAG)); }
      rec = panel_task<NTP>(F, (grp < 2 ? grp + 1 : 0) * 32 + slot, (grp - (grp > 2)) * 32 + slot, grp == 4 ? 64 : 0, grp == 3 ? 64 : 128, w);
    } else {
#pragma unroll
      for (int q = 0; q < 4; ++q) {
        const u16* base = q == 0 ? F.Wk : (q == 1 ? F.Wo : (q == 2 ? F.Wq : F.Wv));
        const int o3 = (384 - (int)(size_t)base) & 896, o63 = (8064 - (int)(size_t)base) & 8064;
        touch(base, DM * HDA * 2, (tz * 128 + id) * 1024 + o3, 0, lds);
        if (!(q & 1)) { touch(base, DM * HDA * 2, (tz * 128 + id) * 1024 + (o3 ^ 128), 0, lds); touch(base, DM * HDA * 2, tz < 16 ? ((tz >> 1) * 128 + id) * 8192 + (o63 ^ ((tz & 1) << 7)) : 0x7ffffff0, 0, lds); }
      }
#pragma unroll 1
      for (int q = id; q < 768; q += 128) {
        wait_until(t0 + (TP(F.px, PXV) >> 16) + ((q * (TP(F.px, PXV) & 0xffff)) >> 6));
        if (q < 512) touch(F.Ax, S_ * DM * 2, tz * 128, ((q & 1) * 256 + (q >> 1)) * 8192, lds); else touch(F.woB, DB * DB * 2, tz * 128, (q - 512) * 8192, lds);
      }
      if (slot < 28) wait_until(t0 + (TP(F.pb, PBV) >> 16));
      rec = rdy_load(F);
      asm volatile("s_waitcnt vmcnt(0)" ::: "memory");
      __syncthreads();
    }
    asm volatile("" :: "v"(cz));
    par = rdy_wait(F, rec, tag);
    if (grp == 2 || grp == 3) {
      const FParams& F = args();
      asm volatile("" : "+s"(w));
      const int j0 = grp == 2 ? 0 : TP(F.jb, JB), j1 = grp == 2 ? TP(F.jb, JB) : 128;
      if (slot < 24) (void)panel_task<true>(F, 96 + slot, 64 + grp * 32 + slot, j0, j1, w);
      else if (slot < 28) {
        const int id = (slot - 24) * 4 + w, tz = opaque_lane();
        int seen = j0;
#pragma unroll 1
        for (int j = j0 + (id >> 2); j < j1; j += 4) {
          for (unsigned spins = 0; j > seen + 16; ++spins) {
            seen = max(seen, __builtin_amdgcn_readfirstlane(ld_agent(&F.ctl->prg[64 + grp * 32 + id][0])));
            if (spin_fail(spins, &F.ctl->tmo[0], 2u)) break;
            __builtin_amdgcn_s_sleep(32);
          }
          touch<2>(F.Bx, S_ * DB * 2, tz * 128, (j * 4 + (id & 3)) * 8192, lds);
          touch(F.Bx, S_ * DB * 2, tz * 128 + 64, (j * 4 + (id & 3)) * 8192, lds);
        }
        asm volatile("s_waitcnt vmcnt(0)" ::: "memory");
      }
    }
    check_in(F, tag, w);
    TLOG(1024 + grp * 32 + slot, 1);
    if (grp < 2) {
      const FParams& F = args();
      asm volatile("" : "+s"(w));
      out_unit(F, par, 2 * grp + (slot >> 4), slot & 15, w);
      TLOG(1280 + grp * 32 + slot, 1);
      return;
    } }
  Ctl* ctl = F.ctl;
  int* sh = (int*)(lds + wg::LDS_USE);
  int next = -1;
  for (;;) {
    const FParams& F = args();
    int w = w0; asm volatile("" : "+s"(w));
    if (next < 0) {
      if (tid == 0) sh[0] = (int)__hip_atomic_fetch_add(&ctl->qhead[0], 1u, RLX_AGENT);
      __syncthreads();
      const int t = __builtin_amdgcn_readfirstlane(sh[0]);
      __syncthreads();
      { if (t >= NTASK) return; next = TASKS[t]; }
    }
    const int code = __builtin_amdgcn_readfirstlane(next); next = -1;
    const int tk = code >> 10, u = code & 1023;
    const u16 *A = nullptr, *Bt = nullptr; void* C = nullptr; int lda = 0, ldb = 0, ldc = 0, nk = 32, r = u >> 2, n = u & 3, nsig = 1;
    const unsigned* wp = nullptr; unsigned need = 0; unsigned* sig = nullptr;
    if (tk == 3) {
      const int lz = opaque_lane(); const bool hi = 8 * r >= TP(F.jb, JB);
      need = lz < 8 ? 8 * r + 8 : TP(F.jb, JB); sig = &ctl->cntAB[r][0];
      if ((lz & 7) < 6 && lz < (hi ? 16 : 8)) wp = &ctl->prg[(lz < 8 && hi ? 160 : 128) + ((lz & 7) >> 1) * 8 + 2 * n + (lz & 1)][0];
    } else if (tk == 4) {
      A = F.qkvA + r * 4 * QBLK + 24 * QPAGE; lda = QBLK / 32; Bt = F.woB + n * 256; ldb = DB;
      C = F.out + (size_t)r * 128 * 2 * DM + DM + n * 256; ldc = 2 * DM; need = 4; if (lane == 0) wp = &ctl->cntAB[r][0];
    } else if (tk == 5) {
      r = u >> 3; n = u & 7; sig = &ctl->flagO[4 * r][n][0]; nsig = 4; need = 8 * r + 8;
      const int lz = opaque_lane(); if (lz < 12) wp = &ctl->prg[((lz >> 2) + (lz >= 8 && r >= 8)) * 32 + 4 * n + (lz & 3)][0];
    } else if (tk == 6) {
      A = F.qkvA + r * 4 * QBLK; lda = QBLK / 32; Bt = F.Wo + n * 256; ldb = DM; nk = HDA / 32;
      C = F.out + (size_t)r * 128 * 2 * DM + n * 256; ldc = 2 * DM; need = 1; { const int lz = opaque_lane(); if (lz < 8) wp = &ctl->flagO[4 * r][lz][0]; }
    } else {
      attn_stream(F, par, u >> 3, u & 7, lds, w);
      TLOG(lastt, 1);
      return;
    }
    wg_wait(wp, need, ctl, w);
    if (tk == 5) {
      attnA_block(F, par, r, 4 * n + w, (u16*)(lds + w * 16384), opaque_lane(), w, (unsigned*)sh + 4);
      asm volatile("s_waitcnt vmcnt(0)" ::: "memory");
      __syncthreads();
    } else if (tk == 3) {
      attnB_task(F, par, r, n, (u16*)(lds + w * 16384), w);
      asm volatile("s_waitcnt vmcnt(0)" ::: "memory");
      __syncthreads();
    } else wg::gemm_tile<128>(A, lda, Bt, ldb, nk, C, ldc, par >> (tk == 4 ? 48 : 0), lds, w);
    if (sig && tid < nsig) __hip_atomic_fetch_add(sig + tid * 256, 1u, RLX_AGENT);
  }
}

#undef lane
#undef tid
__device__ __forceinline__ unsigned rtnow() { return (unsigned)__builtin_amdgcn_s_memrealtime(); }
#define RBASE(s) (((s) & 3) * RSLOT + rpl)
constexpr int ALM = RECO + 1024;
__device__ __forceinline__ float rowmax16(float f) { f = fmaxf(f, dpp_ror<8>(f)); f = fmaxf(f, dpp_ror<4>(f)); f = fmaxf(f, dpp_ror<2>(f)); return fmaxf(f, dpp_ror<1>(f)); }
__device__ __forceinline__ void al_eval(unsigned v, unsigned& t) {
  const bool set = (v >> 16) == 1u;
  if (__builtin_expect(((unsigned)__ballot(set) & 0xffffu) == 0, 1)) return;
  const int d = (short)(v - t);
  t += __builtin_amdgcn_readfirstlane((int)rowmax16(set && d > 0 && d < 1000 ? (float)d : 0.f));
}
__device__ __forceinline__ void rec_get(unsigned& recA, unsigned& recD, __amdgpu_buffer_rsrc_t rs, int off, unsigned* tmo) {
#pragma unroll 1
  for (unsigned fails = 0;; ++fails) {
    const v2u rec = __builtin_amdgcn_raw_buffer_load_b64(rs, off, 0, 16);
    recA = __builtin_amdgcn_readfirstlane(rec.x); recD = __builtin_amdgcn_readfirstlane(rec.y);
    if ((recA != 0xffffffffu && recD != 0xffffffffu) || spin_fail(fails, tmo, 1u)) break;
    __builtin_amdgcn_s_sleep(1);
  }
}

#define QK_LOADER \
          "s_nop 4\n\t" \
          "s_add_u32 m0, %[lw], 0\n\ts_xor_b32 %[t], %[kb], 0x0\n\tbuffer_load_dwordx4 %[go], %[rk], %[t] offen" QK_NTk " lds\n\ts_add_u32 m0, %[lw], 1024\n\ts_xor_b32 %[t], %[kb], 0x10000\n\tbuffer_load_dwordx4 %[go], %[rk], %[t] offen" QK_NTk " lds\n\ts_add_u32 m0, %[lw], 2048\n\ts_xor_b32 %[t], %[kb], 0x20000\n\tbuffer_load_dwordx4 %[go], %[rk], %[t] offen" QK_NTk " lds\n\ts_add_u32 m0, %[lw], 3072\n\ts_xor_b32 %[t], %[kb], 0x30000\n\tbuffer_load_dwordx4 %[go], %[rk], %[t] offen" QK_NTk " lds\n\ts_add_u32 m0, %[lw], 4096\n\ts_xor_b32 %[t], %[kb], 0x40000\n\tbuffer_load_dwordx4 %[go], %[rk], %[t] offen" QK_NTk " lds\n\ts_add_u32 m0, %[lw], 5120\n\ts_xor_b32 %[t], %[kb], 0x50000\n\tbuffer_load_dwordx4 %[go], %[rk], %[t] offen" QK_NTk " lds\n\ts_add_u32 m0, %[lw], 6144\n\ts_xor_b32 %[t], %[kb], 0x60000\n\tbuffer_load_dwordx4 %[go], %[rk], %[t] offen" QK_NTk " lds\n\ts_add_u32 m0, %[lw], 7168\n\ts_xor_b32 %[t], %[kb], 0x70000\n\tbuffer_load_dwordx4 %[go], %[rk], %[t] offen" QK_NTk " lds\n\t" \
          "s_add_u32 m0, %[lw], 8192\n\ts_xor_b32 %[t], %[kb], 0x80000\n\tbuffer_load_dwordx4 %[go], %[rk], %[t] offen" QK_NTk " lds\n\ts_add_u32 m0, %[lw], 9216\n\ts_xor_b32 %[t], %[kb], 0x90000\n\tbuffer_load_dwordx4 %[go], %[rk], %[t] offen" QK_NTk " lds\n\ts_add_u32 m0, %[lw], 10240\n\ts_xor_b32 %[t], %[kb], 0xa0000\n\tbuffer_load_dwordx4 %[go], %[rk], %[t] offen" QK_NTk " lds\n\ts_add_u32 m0, %[lw], 11264\n\ts_xor_b32 %[t], %[kb], 0xb0000\n\tbuffer_load_dwordx4 %[go], %[rk], %[t] offen" QK_NTk " lds\n\ts_add_u32 m0, %[lw], 12288\n\ts_xor_b32 %[t], %[kb], 0xc0000\n\tbuffer_load_dwordx4 %[go], %[rk], %[t] offen" QK_NTk " lds\n\ts_add_u32 m0, %[lw], 13312\n\ts_xor_b32 %[t], %[kb], 0xd0000\n\tbuffer_load_dwordx4 %[go], %[rk], %[t] offen" QK_NTk " lds\n\ts_add_u32 m0, %[lw], 14336\n\ts_xor_b32 %[t], %[kb], 0xe0000\n\tbuffer_load_dwordx4 %[go], %[rk], %[t] offen" QK_NTk " lds\n\ts_add_u32 m0, %[lw], 15360\n\ts_xor_b32 %[t], %[kb], 0xf0000\n\tbuffer_load_dwordx4 %[go], %[rk], %[t] offen" QK_NTk " lds\n\t" \
          "s_add_u32 m0, %[lw], 16384\n\ts_xor_b32 %[t], %[kb], 0x100000\n\tbuffer_load_dwordx4 %[go], %[rk], %[t] offen" QK_NTk " lds\n\ts_add_u32 m0, %[lw], 17408\n\ts_xor_b32 %[t], %[kb], 0x110000\n\tbuffer_load_dwordx4 %[go], %[rk], %[t] offen" QK_NTk " lds\n\ts_add_u32 m0, %[lw], 18432\n\ts_xor_b32 %[t], %[kb], 0x120000\n\tbuffer_load_dwordx4 %[go], %[rk], %[t] offen" QK_NTk " lds\n\ts_add_u32 m0, %[lw], 19456\n\ts_xor_b32 %[t], %[kb], 0x130000\n\tbuffer_load_dwordx4 %[go], %[rk], %[t] offen" QK_NTk " lds\n\ts_add_u32 m0, %[lw], 20480\n\ts_xor_b32 %[t], %[kb], 0x140000\n\tbuffer_load_dwordx4 %[go], %[rk], %[t] offen" QK_NTk " lds\n\ts_add_u32 m0, %[lw], 21504\n\ts_xor_b32 %[t], %[kb], 0x150000\n\tbuffer_load_dwordx4 %[go], %[rk], %[t] offen" QK_NTk " lds\n\ts_add_u32 m0, %[lw], 22528\n\ts_xor_b32 %[t], %[kb], 0x160000\n\tbuffer_load_dwordx4 %[go], %[rk], %[t] offen" QK_NTk " lds\n\ts_add_u32 m0, %[lw], 23552\n\ts_xor_b32 %[t], %[kb], 0x170000\n\tbuffer_load_dwordx4 %[go], %[rk], %[t] offen" QK_NTk " lds\n\t" \
          "s_add_u32 m0, %[lw], 24576\n\ts_xor_b32 %[t], %[kb], 0x180000\n\tbuffer_load_dwordx4 %[go], %[rk], %[t] offen" QK_NTk " lds\n\ts_add_u32 m0, %[lw], 25600\n\ts_xor_b32 %[t], %[kb], 0x190000\n\tbuffer_load_dwordx4 %[go], %[rk], %[t] offen" QK_NTk " lds\n\ts_add_u32 m0, %[lw], 26624\n\ts_xor_b32 %[t], %[kb], 0x1a0000\n\tbuffer_load_dwordx4 %[go], %[rk], %[t] offen" QK_NTk " lds\n\ts_add_u32 m0, %[lw], 27648\n\ts_xor_b32 %[t], %[kb], 0x1b0000\n\tbuffer_load_dwordx4 %[go], %[rk], %[t] offen" QK_NTk " lds\n\ts_add_u32 m0, %[lw], 28672\n\ts_xor_b32 %[t], %[kb], 0x1c0000\n\tbuffer_load_dwordx4 %[go], %[rk], %[t] offen" QK_NTk " lds\n\ts_add_u32 m0, %[lw], 29696\n\ts_xor_b32 %[t], %[kb], 0x1d0000\n\tbuffer_load_dwordx4 %[go], %[rk], %[t] offen" QK_NTk " lds\n\ts_add_u32 m0, %[lw], 30720\n\ts_xor_b32 %[t], %[kb], 0x1e0000\n\tbuffer_load_dwordx4 %[go], %[rk], %[t] offen" QK_NTk " lds\n\ts_add_u32 m0, %[lw], 31744\n\ts_xor_b32 %[t], %[kb], 0x1f0000\n\tbuffer_load_dwordx4 %[go], %[rk], %[t] offen" QK_NTk " lds\n\t" \
          "s_add_u32 m0, %[lw], 32768\n\ts_xor_b32 %[t], %[kb], 0x200000\n\tbuffer_load_dwordx4 %[go], %[rk], %[t] offen" QK_NTk " lds\n\ts_add_u32 m0, %[lw], 33792\n\ts_xor_b32 %[t], %[kb], 0x210000\n\tbuffer_load_dwordx4 %[go], %[rk], %[t] offen" QK_NTk " lds\n\ts_add_u32 m0, %[lw], 34816\n\ts_xor_b32 %[t], %[kb], 0x220000\n\tbuffer_load_dwordx4 %[go], %[rk], %[t] offen" QK_NTk " lds\n\ts_add_u32 m0, %[lw], 35840\n\ts_xor_b32 %[t], %[kb], 0x230000\n\tbuffer_load_dwordx4 %[go], %[rk], %[t] offen" QK_NTk " lds\n\ts_add_u32 m0, %[lw], 36864\n\ts_xor_b32 %[t], %[kb], 0x240000\n\tbuffer_load_dwordx4 %[go], %[rk], %[t] offen" QK_NTk " lds\n\ts_add_u32 m0, %[lw], 37888\n\ts_xor_b32 %[t], %[kb], 0x250000\n\tbuffer_load_dwordx4 %[go], %[rk], %[t] offen" QK_NTk " lds\n\ts_add_u32 m0, %[lw], 38912\n\ts_xor_b32 %[t], %[kb], 0x260000\n\tbuffer_load_dwordx4 %[go], %[rk], %[t] offen" QK_NTk " lds\n\ts_add_u32 m0, %[lw], 39936\n\ts_xor_b32 %[t], %[kb], 0x270000\n\tbuffer_load_dwordx4 %[go], %[rk], %[t] offen" QK_NTk " lds\n\t" \
          "s_waitcnt vmcnt(32)\n\tds_read_b64_tr_b16 v[136:137], %[rd0] offset:0\n\tds_read_b64_tr_b16 v[138:139], %[rd0] offset:128\n\tds_read_b64_tr_b16 v[140:141], %[rd0] offset:4096\n\tds_read_b64_tr_b16 v[142:143], %[rd0] offset:4224\n\tds_read_b64_tr_b16 a[64:65], %[rd1] offset:0\n\tds_read_b64_tr_b16 a[66:67], %[rd1] offset:128\n\tds_read_b64_tr_b16 a[68:69], %[rd1] offset:4096\n\tds_read_b64_tr_b16 a[70:71], %[rd1] offset:4224\n\tds_read_b64_tr_b16 a[128:129], %[rd2] offset:0\n\tds_read_b64_tr_b16 a[130:131], %[rd2] offset:128\n\tds_read_b64_tr_b16 a[132:133], %[rd2] offset:4096\n\tds_read_b64_tr_b16 a[134:135], %[rd2] offset:4224\n\tds_read_b64_tr_b16 a[136:137], %[rd3] offset:0\n\tds_read_b64_tr_b16 a[138:139], %[rd3] offset:128\n\tds_read_b64_tr_b16 a[140:141], %[rd3] offset:4096\n\tds_read_b64_tr_b16 a[142:143], %[rd3] offset:4224\n\ts_waitcnt lgkmcnt(0)\n\t" \
          "s_add_u32 m0, %[lw], 0\n\ts_xor_b32 %[t], %[kb], 0x280000\n\tbuffer_load_dwordx4 %[go], %[rk], %[t] offen" QK_NTk " lds\n\ts_add_u32 m0, %[lw], 1024\n\ts_xor_b32 %[t], %[kb], 0x290000\n\tbuffer_load_dwordx4 %[go], %[rk], %[t] offen" QK_NTk " lds\n\ts_add_u32 m0, %[lw], 2048\n\ts_xor_b32 %[t], %[kb], 0x2a0000\n\tbuffer_load_dwordx4 %[go], %[rk], %[t] offen" QK_NTk " lds\n\ts_add_u32 m0, %[lw], 3072\n\ts_xor_b32 %[t], %[kb], 0x2b0000\n\tbuffer_load_dwordx4 %[go], %[rk], %[t] offen" QK_NTk " lds\n\ts_add_u32 m0, %[lw], 4096\n\ts_xor_b32 %[t], %[kb], 0x2c0000\n\tbuffer_load_dwordx4 %[go], %[rk], %[t] offen" QK_NTk " lds\n\ts_add_u32 m0, %[lw], 5120\n\ts_xor_b32 %[t], %[kb], 0x2d0000\n\tbuffer_load_dwordx4 %[go], %[rk], %[t] offen" QK_NTk " lds\n\ts_add_u32 m0, %[lw], 6144\n\ts_xor_b32 %[t], %[kb], 0x2e0000\n\tbuffer_load_dwordx4 %[go], %[rk], %[t] offen" QK_NTk " lds\n\ts_add_u32 m0, %[lw], 7168\n\ts_xor_b32 %[t], %[kb], 0x2f0000\n\tbuffer_load_dwordx4 %[go], %[rk], %[t] offen" QK_NTk " lds\n\t" \
          "s_waitcnt vmcnt(32)\n\tds_read_b64_tr_b16 v[40:41], %[rd0] offset:8192\n\tds_read_b64_tr_b16 v[42:43], %[rd0] offset:8320\n\tds_read_b64_tr_b16 v[44:45], %[rd0] offset:12288\n\tds_read_b64_tr_b16 v[46:47], %[rd0] offset:12416\n\tds_read_b64_tr_b16 a[72:73], %[rd1] offset:8192\n\tds_read_b64_tr_b16 a[74:75], %[rd1] offset:8320\n\tds_read_b64_tr_b16 a[76:77], %[rd1] offset:12288\n\tds_read_b64_tr_b16 a[78:79], %[rd1] offset:12416\n\tds_read_b64_tr_b16 a[144:145], %[rd2] offset:8192\n\tds_read_b64_tr_b16 a[146:147], %[rd2] offset:8320\n\tds_read_b64_tr_b16 a[148:149], %[rd2] offset:12288\n\tds_read_b64_tr_b16 a[150:151], %[rd2] offset:12416\n\tds_read_b64_tr_b16 a[152:153], %[rd3] offset:8192\n\tds_read_b64_tr_b16 a[154:155], %[rd3] offset:8320\n\tds_read_b64_tr_b16 a[156:157], %[rd3] offset:12288\n\tds_read_b64_tr_b16 a[158:159], %[rd3] offset:12416\n\ts_waitcnt lgkmcnt(0)\n\t" \
          "s_add_u32 m0, %[lw], 8192\n\ts_xor_b32 %[t], %[kb], 0x300000\n\tbuffer_load_dwordx4 %[go], %[rk], %[t] offen" QK_NTk " lds\n\ts_add_u32 m0, %[lw], 9216\n\ts_xor_b32 %[t], %[kb], 0x310000\n\tbuffer_load_dwordx4 %[go], %[rk], %[t] offen" QK_NTk " lds\n\ts_add_u32 m0, %[lw], 10240\n\ts_xor_b32 %[t], %[kb], 0x320000\n\tbuffer_load_dwordx4 %[go], %[rk], %[t] offen" QK_NTk " lds\n\ts_add_u32 m0, %[lw], 11264\n\ts_xor_b32 %[t], %[kb], 0x330000\n\tbuffer_load_dwordx4 %[go], %[rk], %[t] offen" QK_NTk " lds\n\ts_add_u32 m0, %[lw], 12288\n\ts_xor_b32 %[t], %[kb], 0x340000\n\tbuffer_load_dwordx4 %[go], %[rk], %[t] offen" QK_NTk " lds\n\ts_add_u32 m0, %[lw], 13312\n\ts_xor_b32 %[t], %[kb], 0x350000\n\tbuffer_load_dwordx4 %[go], %[rk], %[t] offen" QK_NTk " lds\n\ts_add_u32 m0, %[lw], 14336\n\ts_xor_b32 %[t], %[kb], 0x360000\n\tbuffer_load_dwordx4 %[go], %[rk], %[t] offen" QK_NTk " lds\n\ts_add_u32 m0, %[lw], 15360\n\ts_xor_b32 %[t], %[kb], 0x370000\n\tbuffer_load_dwordx4 %[go], %[rk], %[t] offen" QK_NTk " lds\n\t" \
          "s_waitcnt vmcnt(32)\n\tds_read_b64_tr_b16 v[144:145], %[rd0] offset:16384\n\tds_read_b64_tr_b16 v[146:147], %[rd0] offset:16512\n\tds_read_b64_tr_b16 v[148:149], %[rd0] offset:20480\n\tds_read_b64_tr_b16 v[150:151], %[rd0] offset:20608\n\tds_read_b64_tr_b16 a[80:81], %[rd1] offset:16384\n\tds_read_b64_tr_b16 a[82:83], %[rd1] offset:16512\n\tds_read_b64_tr_b16 a[84:85], %[rd1] offset:20480\n\tds_read_b64_tr_b16 a[86:87], %[rd1] offset:20608\n\tds_read_b64_tr_b16 a[160:161], %[rd2] offset:16384\n\tds_read_b64_tr_b16 a[162:163], %[rd2] offset:16512\n\tds_read_b64_tr_b16 a[164:165], %[rd2] offset:20480\n\tds_read_b64_tr_b16 a[166:167], %[rd2] offset:20608\n\tds_read_b64_tr_b16 a[168:169], %[rd3] offset:16384\n\tds_read_b64_tr_b16 a[170:171], %[rd3] offset:16512\n\tds_read_b64_tr_b16 a[172:173], %[rd3] offset:20480\n\tds_read_b64_tr_b16 a[174:175], %[rd3] offset:20608\n\ts_waitcnt lgkmcnt(0)\n\t" \
          "s_add_u32 m0, %[lw], 16384\n\ts_xor_b32 %[t], %[kb], 0x380000\n\tbuffer_load_dwordx4 %[go], %[rk], %[t] offen" QK_NTk " lds\n\ts_add_u32 m0, %[lw], 17408\n\ts_xor_b32 %[t], %[kb], 0x390000\n\tbuffer_load_dwordx4 %[go], %[rk], %[t] offen" QK_NTk " lds\n\ts_add_u32 m0, %[lw], 18432\n\ts_xor_b32 %[t], %[kb], 0x3a0000\n\tbuffer_load_dwordx4 %[go], %[rk], %[t] offen" QK_NTk " lds\n\ts_add_u32 m0, %[lw], 19456\n\ts_xor_b32 %[t], %[kb], 0x3b0000\n\tbuffer_load_dwordx4 %[go], %[rk], %[t] offen" QK_NTk " lds\n\ts_add_u32 m0, %[lw], 20480\n\ts_xor_b32 %[t], %[kb], 0x3c0000\n\tbuffer_load_dwordx4 %[go], %[rk], %[t] offen" QK_NTk " lds\n\ts_add_u32 m0, %[lw], 21504\n\ts_xor_b32 %[t], %[kb], 0x3d0000\n\tbuffer_load_dwordx4 %[go], %[rk], %[t] offen" QK_NTk " lds\n\ts_add_u32 m0, %[lw], 22528\n\ts_xor_b32 %[t], %[kb], 0x3e0000\n\tbuffer_load_dwordx4 %[go], %[rk], %[t] offen" QK_NTk " lds\n\ts_add_u32 m0, %[lw], 23552\n\ts_xor_b32 %[t], %[kb], 0x3f0000\n\tbuffer_load_dwordx4 %[go], %[rk], %[t] offen" QK_NTk " lds\n\t" \
          "s_waitcnt vmcnt(32)\n\tds_read_b64_tr_b16 v[80:81], %[rd0] offset:24576\n\tds_read_b64_tr_b16 v[82:83], %[rd0] offset:24704\n\tds_read_b64_tr_b16 v[84:85], %[rd0] offset:28672\n\tds_read_b64_tr_b16 v[86:87], %[rd0] offset:28800\n\tds_read_b64_tr_b16 a[88:89], %[rd1] offset:24576\n\tds_read_b64_tr_b16 a[90:91], %[rd1] offset:24704\n\tds_read_b64_tr_b16 a[92:93], %[rd1] offset:28672\n\tds_read_b64_tr_b16 a[94:95], %[rd1] offset:28800\n\tds_read_b64_tr_b16 a[176:177], %[rd2] offset:24576\n\tds_read_b64_tr_b16 a[178:179], %[rd2] offset:24704\n\tds_read_b64_tr_b16 a[180:181], %[rd2] offset:28672\n\tds_read_b64_tr_b16 a[182:183], %[rd2] offset:28800\n\tds_read_b64_tr_b16 a[184:185], %[rd3] offset:24576\n\tds_read_b64_tr_b16 a[186:187], %[rd3] offset:24704\n\tds_read_b64_tr_b16 a[188:189], %[rd3] offset:28672\n\tds_read_b64_tr_b16 a[190:191], %[rd3] offset:28800\n\ts_waitcnt lgkmcnt(0)\n\t" \
          "s_add_u32 m0, %[lw], 24576\n\ts_xor_b32 %[t], %[kb], 0x380000\n\tbuffer_load_dwordx4 %[go], %[rq], %[t] offen" QK_NTq " lds\n\ts_add_u32 m0, %[lw], 25600\n\ts_xor_b32 %[t], %[kb], 0x390000\n\tbuffer_load_dwordx4 %[go], %[rq], %[t] offen" QK_NTq " lds\n\ts_add_u32 m0, %[lw], 26624\n\ts_xor_b32 %[t], %[kb], 0x3a0000\n\tbuffer_load_dwordx4 %[go], %[rq], %[t] offen" QK_NTq " lds\n\ts_add_u32 m0, %[lw], 27648\n\ts_xor_b32 %[t], %[kb], 0x3b0000\n\tbuffer_load_dwordx4 %[go], %[rq], %[t] offen" QK_NTq " lds\n\ts_add_u32 m0, %[lw], 28672\n\ts_xor_b32 %[t], %[kb], 0x3c0000\n\tbuffer_load_dwordx4 %[go], %[rq], %[t] offen" QK_NTq " lds\n\ts_add_u32 m0, %[lw], 29696\n\ts_xor_b32 %[t], %[kb], 0x3d0000\n\tbuffer_load_dwordx4 %[go], %[rq], %[t] offen" QK_NTq " lds\n\ts_add_u32 m0, %[lw], 30720\n\ts_xor_b32 %[t], %[kb], 0x3e0000\n\tbuffer_load_dwordx4 %[go], %[rq], %[t] offen" QK_NTq " lds\n\ts_add_u32 m0, %[lw], 31744\n\ts_xor_b32 %[t], %[kb], 0x3f0000\n\tbuffer_load_dwordx4 %[go], %[rq], %[t] offen" QK_NTq " lds\n\t" \
          "s_waitcnt vmcnt(32)\n\tds_read_b64_tr_b16 v[152:153], %[rd0] offset:32768\n\tds_read_b64_tr_b16 v[154:155], %[rd0] offset:32896\n\tds_read_b64_tr_b16 v[156:157], %[rd0] offset:36864\n\tds_read_b64_tr_b16 v[158:159], %[rd0] offset:36992\n\tds_read_b64_tr_b16 a[96:97], %[rd1] offset:32768\n\tds_read_b64_tr_b16 a[98:99], %[rd1] offset:32896\n\tds_read_b64_tr_b16 a[100:101], %[rd1] offset:36864\n\tds_read_b64_tr_b16 a[102:103], %[rd1] offset:36992\n\tds_read_b64_tr_b16 a[192:193], %[rd2] offset:32768\n\tds_read_b64_tr_b16 a[194:195], %[rd2] offset:32896\n\tds_read_b64_tr_b16 a[196:197], %[rd2] offset:36864\n\tds_read_b64_tr_b16 a[198:199], %[rd2] offset:36992\n\tds_read_b64_tr_b16 a[200:201], %[rd3] offset:32768\n\tds_read_b64_tr_b16 a[202:203], %[rd3] offset:32896\n\tds_read_b64_tr_b16 a[204:205], %[rd3] offset:36864\n\tds_read_b64_tr_b16 a[206:207], %[rd3] offset:36992\n\ts_waitcnt lgkmcnt(0)\n\t" \
          "s_add_u32 m0, %[lw], 32768\n\ts_xor_b32 %[t], %[kb], 0x300000\n\tbuffer_load_dwordx4 %[go], %[rq], %[t] offen" QK_NTq " lds\n\ts_add_u32 m0, %[lw], 33792\n\ts_xor_b32 %[t], %[kb], 0x310000\n\tbuffer_load_dwordx4 %[go], %[rq], %[t] offen" QK_NTq " lds\n\ts_add_u32 m0, %[lw], 34816\n\ts_xor_b32 %[t], %[kb], 0x320000\n\tbuffer_load_dwordx4 %[go], %[rq], %[t] offen" QK_NTq " lds\n\ts_add_u32 m0, %[lw], 35840\n\ts_xor_b32 %[t], %[kb], 0x330000\n\tbuffer_load_dwordx4 %[go], %[rq], %[t] offen" QK_NTq " lds\n\ts_add_u32 m0, %[lw], 36864\n\ts_xor_b32 %[t], %[kb], 0x340000\n\tbuffer_load_dwordx4 %[go], %[rq], %[t] offen" QK_NTq " lds\n\ts_add_u32 m0, %[lw], 37888\n\ts_xor_b32 %[t], %[kb], 0x350000\n\tbuffer_load_dwordx4 %[go], %[rq], %[t] offen" QK_NTq " lds\n\ts_add_u32 m0, %[lw], 38912\n\ts_xor_b32 %[t], %[kb], 0x360000\n\tbuffer_load_dwordx4 %[go], %[rq], %[t] offen" QK_NTq " lds\n\ts_add_u32 m0, %[lw], 39936\n\ts_xor_b32 %[t], %[kb], 0x370000\n\tbuffer_load_dwordx4 %[go], %[rq], %[t] offen" QK_NTq " lds\n\t" \
          "s_waitcnt vmcnt(32)\n\tds_read_b64_tr_b16 v[120:121], %[rd0] offset:0\n\tds_read_b64_tr_b16 v[122:123], %[rd0] offset:128\n\tds_read_b64_tr_b16 v[124:125], %[rd0] offset:4096\n\tds_read_b64_tr_b16 v[126:127], %[rd0] offset:4224\n\tds_read_b64_tr_b16 a[104:105], %[rd1] offset:0\n\tds_read_b64_tr_b16 a[106:107], %[rd1] offset:128\n\tds_read_b64_tr_b16 a[108:109], %[rd1] offset:4096\n\tds_read_b64_tr_b16 a[110:111], %[rd1] offset:4224\n\tds_read_b64_tr_b16 a[208:209], %[rd2] offset:0\n\tds_read_b64_tr_b16 a[210:211], %[rd2] offset:128\n\tds_read_b64_tr_b16 a[212:213], %[rd2] offset:4096\n\tds_read_b64_tr_b16 a[214:215], %[rd2] offset:4224\n\tds_read_b64_tr_b16 a[216:217], %[rd3] offset:0\n\tds_read_b64_tr_b16 a[218:219], %[rd3] offset:128\n\tds_read_b64_tr_b16 a[220:221], %[rd3] offset:4096\n\tds_read_b64_tr_b16 a[222:223], %[rd3] offset:4224\n\ts_waitcnt lgkmcnt(0)\n\t" \
          "s_add_u32 m0, %[lw], 0\n\ts_xor_b32 %[t], %[kb], 0x280000\n\tbuffer_load_dwordx4 %[go], %[rq], %[t] offen" QK_NTq " lds\n\ts_add_u32 m0, %[lw], 1024\n\ts_xor_b32 %[t], %[kb], 0x290000\n\tbuffer_load_dwordx4 %[go], %[rq], %[t] offen" QK_NTq " lds\n\ts_add_u32 m0, %[lw], 2048\n\ts_xor_b32 %[t], %[kb], 0x2a0000\n\tbuffer_load_dwordx4 %[go], %[rq], %[t] offen" QK_NTq " lds\n\ts_add_u32 m0, %[lw], 3072\n\ts_xor_b32 %[t], %[kb], 0x2b0000\n\tbuffer_load_dwordx4 %[go], %[rq], %[t] offen" QK_NTq " lds\n\ts_add_u32 m0, %[lw], 4096\n\ts_xor_b32 %[t], %[kb], 0x2c0000\n\tbuffer_load_dwordx4 %[go], %[rq], %[t] offen" QK_NTq " lds\n\ts_add_u32 m0, %[lw], 5120\n\ts_xor_b32 %[t], %[kb], 0x2d0000\n\tbuffer_load_dwordx4 %[go], %[rq], %[t] offen" QK_NTq " lds\n\ts_add_u32 m0, %[lw], 6144\n\ts_xor_b32 %[t], %[kb], 0x2e0000\n\tbuffer_load_dwordx4 %[go], %[rq], %[t] offen" QK_NTq " lds\n\ts_add_u32 m0, %[lw], 7168\n\ts_xor_b32 %[t], %[kb], 0x2f0000\n\tbuffer_load_dwordx4 %[go], %[rq], %[t] offen" QK_NTq " lds\n\t" \
          "s_waitcnt vmcnt(32)\n\tds_read_b64_tr_b16 a[16:17], %[rd0] offset:8192\n\tds_read_b64_tr_b16 a[18:19], %[rd0] offset:8320\n\tds_read_b64_tr_b16 a[20:21], %[rd0] offset:12288\n\tds_read_b64_tr_b16 a[22:23], %[rd0] offset:12416\n\tds_read_b64_tr_b16 a[112:113], %[rd1] offset:8192\n\tds_read_b64_tr_b16 a[114:115], %[rd1] offset:8320\n\tds_read_b64_tr_b16 a[116:117], %[rd1] offset:12288\n\tds_read_b64_tr_b16 a[118:119], %[rd1] offset:12416\n\tds_read_b64_tr_b16 a[224:225], %[rd2] offset:8192\n\tds_read_b64_tr_b16 a[226:227], %[rd2] offset:8320\n\tds_read_b64_tr_b16 a[228:229], %[rd2] offset:12288\n\tds_read_b64_tr_b16 a[230:231], %[rd2] offset:12416\n\tds_read_b64_tr_b16 a[232:233], %[rd3] offset:8192\n\tds_read_b64_tr_b16 a[234:235], %[rd3] offset:8320\n\tds_read_b64_tr_b16 a[236:237], %[rd3] offset:12288\n\tds_read_b64_tr_b16 a[238:239], %[rd3] offset:12416\n\ts_waitcnt lgkmcnt(0)\n\t" \
          "s_add_u32 m0, %[lw], 8192\n\ts_xor_b32 %[t], %[kb], 0x200000\n\tbuffer_load_dwordx4 %[go], %[rq], %[t] offen" QK_NTq " lds\n\ts_add_u32 m0, %[lw], 9216\n\ts_xor_b32 %[t], %[kb], 0x210000\n\tbuffer_load_dwordx4 %[go], %[rq], %[t] offen" QK_NTq " lds\n\ts_add_u32 m0, %[lw], 10240\n\ts_xor_b32 %[t], %[kb], 0x220000\n\tbuffer_load_dwordx4 %[go], %[rq], %[t] offen" QK_NTq " lds\n\ts_add_u32 m0, %[lw], 11264\n\ts_xor_b32 %[t], %[kb], 0x230000\n\tbuffer_load_dwordx4 %[go], %[rq], %[t] offen" QK_NTq " lds\n\ts_add_u32 m0, %[lw], 12288\n\ts_xor_b32 %[t], %[kb], 0x240000\n\tbuffer_load_dwordx4 %[go], %[rq], %[t] offen" QK_NTq " lds\n\ts_add_u32 m0, %[lw], 13312\n\ts_xor_b32 %[t], %[kb], 0x250000\n\tbuffer_load_dwordx4 %[go], %[rq], %[t] offen" QK_NTq " lds\n\ts_add_u32 m0, %[lw], 14336\n\ts_xor_b32 %[t], %[kb], 0x260000\n\tbuffer_load_dwordx4 %[go], %[rq], %[t] offen" QK_NTq " lds\n\ts_add_u32 m0, %[lw], 15360\n\ts_xor_b32 %[t], %[kb], 0x270000\n\tbuffer_load_dwordx4 %[go], %[rq], %[t] offen" QK_NTq " lds\n\t" \
          "s_waitcnt vmcnt(32)\n\tds_read_b64_tr_b16 a[56:57], %[rd0] offset:16384\n\tds_read_b64_tr_b16 a[58:59], %[rd0] offset:16512\n\tds_read_b64_tr_b16 a[60:61], %[rd0] offset:20480\n\tds_read_b64_tr_b16 a[62:63], %[rd0] offset:20608\n\tds_read_b64_tr_b16 a[120:121], %[rd1] offset:16384\n\tds_read_b64_tr_b16 a[122:123], %[rd1] offset:16512\n\tds_read_b64_tr_b16 a[124:125], %[rd1] offset:20480\n\tds_read_b64_tr_b16 a[126:127], %[rd1] offset:20608\n\tds_read_b64_tr_b16 a[240:241], %[rd2] offset:16384\n\tds_read_b64_tr_b16 a[242:243], %[rd2] offset:16512\n\tds_read_b64_tr_b16 a[244:245], %[rd2] offset:20480\n\tds_read_b64_tr_b16 a[246:247], %[rd2] offset:20608\n\tds_read_b64_tr_b16 a[248:249], %[rd3] offset:16384\n\tds_read_b64_tr_b16 a[250:251], %[rd3] offset:16512\n\tds_read_b64_tr_b16 a[252:253], %[rd3] offset:20480\n\tds_read_b64_tr_b16 a[254:255], %[rd3] offset:20608\n\ts_waitcnt lgkmcnt(0)\n\t" \
          "s_add_u32 m0, %[lw], 16384\n\ts_xor_b32 %[t], %[kb], 0x180000\n\tbuffer_load_dwordx4 %[go], %[rq], %[t] offen" QK_NTq " lds\n\ts_add_u32 m0, %[lw], 17408\n\ts_xor_b32 %[t], %[kb], 0x190000\n\tbuffer_load_dwordx4 %[go], %[rq], %[t] offen" QK_NTq " lds\n\ts_add_u32 m0, %[lw], 18432\n\ts_xor_b32 %[t], %[kb], 0x1a0000\n\tbuffer_load_dwordx4 %[go], %[rq], %[t] offen" QK_NTq " lds\n\ts_add_u32 m0, %[lw], 19456\n\ts_xor_b32 %[t], %[kb], 0x1b0000\n\tbuffer_load_dwordx4 %[go], %[rq], %[t] offen" QK_NTq " lds\n\ts_add_u32 m0, %[lw], 20480\n\ts_xor_b32 %[t], %[kb], 0x1c0000\n\tbuffer_load_dwordx4 %[go], %[rq], %[t] offen" QK_NTq " lds\n\ts_add_u32 m0, %[lw], 21504\n\ts_xor_b32 %[t], %[kb], 0x1d0000\n\tbuffer_load_dwordx4 %[go], %[rq], %[t] offen" QK_NTq " lds\n\ts_add_u32 m0, %[lw], 22528\n\ts_xor_b32 %[t], %[kb], 0x1e0000\n\tbuffer_load_dwordx4 %[go], %[rq], %[t] offen" QK_NTq " lds\n\ts_add_u32 m0, %[lw], 23552\n\ts_xor_b32 %[t], %[kb], 0x1f0000\n\tbuffer_load_dwordx4 %[go], %[rq], %[t] offen" QK_NTq " lds\n\t" \
          "s_waitcnt vmcnt(32)\n\tds_read_b64_tr_b16 a[24:25], %[rd0] offset:24576\n\tds_read_b64_tr_b16 a[26:27], %[rd0] offset:24704\n\tds_read_b64_tr_b16 a[28:29], %[rd0] offset:28672\n\tds_read_b64_tr_b16 a[30:31], %[rd0] offset:28800\n\tds_read_b64_tr_b16 a[32:33], %[rd1] offset:24576\n\tds_read_b64_tr_b16 a[34:35], %[rd1] offset:24704\n\tds_read_b64_tr_b16 a[36:37], %[rd1] offset:28672\n\tds_read_b64_tr_b16 a[38:39], %[rd1] offset:28800\n\tds_read_b64_tr_b16 a[40:41], %[rd2] offset:24576\n\tds_read_b64_tr_b16 a[42:43], %[rd2] offset:24704\n\tds_read_b64_tr_b16 a[44:45], %[rd2] offset:28672\n\tds_read_b64_tr_b16 a[46:47], %[rd2] offset:28800\n\tds_read_b64_tr_b16 a[48:49], %[rd3] offset:24576\n\tds_read_b64_tr_b16 a[50:51], %[rd3] offset:24704\n\tds_read_b64_tr_b16 a[52:53], %[rd3] offset:28672\n\tds_read_b64_tr_b16 a[54:55], %[rd3] offset:28800\n\ts_waitcnt lgkmcnt(0)\n\t" \
          "s_add_u32 m0, %[lw], 24576\n\ts_xor_b32 %[t], %[kb], 0x100000\n\tbuffer_load_dwordx4 %[go], %[rq], %[t] offen" QK_NTq " lds\n\ts_add_u32 m0, %[lw], 25600\n\ts_xor_b32 %[t], %[kb], 0x110000\n\tbuffer_load_dwordx4 %[go], %[rq], %[t] offen" QK_NTq " lds\n\ts_add_u32 m0, %[lw], 26624\n\ts_xor_b32 %[t], %[kb], 0x120000\n\tbuffer_load_dwordx4 %[go], %[rq], %[t] offen" QK_NTq " lds\n\ts_add_u32 m0, %[lw], 27648\n\ts_xor_b32 %[t], %[kb], 0x130000\n\tbuffer_load_dwordx4 %[go], %[rq], %[t] offen" QK_NTq " lds\n\ts_add_u32 m0, %[lw], 28672\n\ts_xor_b32 %[t], %[kb], 0x140000\n\tbuffer_load_dwordx4 %[go], %[rq], %[t] offen" QK_NTq " lds\n\ts_add_u32 m0, %[lw], 29696\n\ts_xor_b32 %[t], %[kb], 0x150000\n\tbuffer_load_dwordx4 %[go], %[rq], %[t] offen" QK_NTq " lds\n\ts_add_u32 m0, %[lw], 30720\n\ts_xor_b32 %[t], %[kb], 0x160000\n\tbuffer_load_dwordx4 %[go], %[rq], %[t] offen" QK_NTq " lds\n\ts_add_u32 m0, %[lw], 31744\n\ts_xor_b32 %[t], %[kb], 0x170000\n\tbuffer_load_dwordx4 %[go], %[rq], %[t] offen" QK_NTq " lds\n\t" \
          "s_waitcnt vmcnt(32)\n\tds_read_b64_tr_b16 v[160:161], %[rd0] offset:32768\n\tds_read_b64_tr_b16 v[162:163], %[rd0] offset:32896\n\tds_read_b64_tr_b16 v[164:165], %[rd0] offset:36864\n\tds_read_b64_tr_b16 v[166:167], %[rd0] offset:36992\n\tds_read_b64_tr_b16 v[128:129], %[rd1] offset:32768\n\tds_read_b64_tr_b16 v[130:131], %[rd1] offset:32896\n\tds_read_b64_tr_b16 v[132:133], %[rd1] offset:36864\n\tds_read_b64_tr_b16 v[134:135], %[rd1] offset:36992\n\tds_read_b64_tr_b16 a[0:1], %[rd2] offset:32768\n\tds_read_b64_tr_b16 a[2:3], %[rd2] offset:32896\n\tds_read_b64_tr_b16 a[4:5], %[rd2] offset:36864\n\tds_read_b64_tr_b16 a[6:7], %[rd2] offset:36992\n\tds_read_b64_tr_b16 a[8:9], %[rd3] offset:32768\n\tds_read_b64_tr_b16 a[10:11], %[rd3] offset:32896\n\tds_read_b64_tr_b16 a[12:13], %[rd3] offset:36864\n\tds_read_b64_tr_b16 a[14:15], %[rd3] offset:36992\n\ts_waitcnt lgkmcnt(0)\n\t" \
          "s_add_u32 m0, %[lw], 32768\n\ts_xor_b32 %[t], %[kb], 0x80000\n\tbuffer_load_dwordx4 %[go], %[rq], %[t] offen" QK_NTq " lds\n\ts_add_u32 m0, %[lw], 33792\n\ts_xor_b32 %[t], %[kb], 0x90000\n\tbuffer_load_dwordx4 %[go], %[rq], %[t] offen" QK_NTq " lds\n\ts_add_u32 m0, %[lw], 34816\n\ts_xor_b32 %[t], %[kb], 0xa0000\n\tbuffer_load_dwordx4 %[go], %[rq], %[t] offen" QK_NTq " lds\n\ts_add_u32 m0, %[lw], 35840\n\ts_xor_b32 %[t], %[kb], 0xb0000\n\tbuffer_load_dwordx4 %[go], %[rq], %[t] offen" QK_NTq " lds\n\ts_add_u32 m0, %[lw], 36864\n\ts_xor_b32 %[t], %[kb], 0xc0000\n\tbuffer_load_dwordx4 %[go], %[rq], %[t] offen" QK_NTq " lds\n\ts_add_u32 m0, %[lw], 37888\n\ts_xor_b32 %[t], %[kb], 0xd0000\n\tbuffer_load_dwordx4 %[go], %[rq], %[t] offen" QK_NTq " lds\n\ts_add_u32 m0, %[lw], 38912\n\ts_xor_b32 %[t], %[kb], 0xe0000\n\tbuffer_load_dwordx4 %[go], %[rq], %[t] offen" QK_NTq " lds\n\ts_add_u32 m0, %[lw], 39936\n\ts_xor_b32 %[t], %[kb], 0xf0000\n\tbuffer_load_dwordx4 %[go], %[rq], %[t] offen" QK_NTq " lds\n\t" \
          "s_waitcnt vmcnt(32)\n\tds_read_b64_tr_b16 v[104:105], %[rd0] offset:0\n\tds_read_b64_tr_b16 v[106:107], %[rd0] offset:128\n\tds_read_b64_tr_b16 v[108:109], %[rd0] offset:4096\n\tds_read_b64_tr_b16 v[110:111], %[rd0] offset:4224\n\tds_read_b64_tr_b16 v[168:169], %[rd1] offset:0\n\tds_read_b64_tr_b16 v[170:171], %[rd1] offset:128\n\tds_read_b64_tr_b16 v[172:173], %[rd1] offset:4096\n\tds_read_b64_tr_b16 v[174:175], %[rd1] offset:4224\n\tds_read_b64_tr_b16 v[112:113], %[rd2] offset:0\n\tds_read_b64_tr_b16 v[114:115], %[rd2] offset:128\n\tds_read_b64_tr_b16 v[116:117], %[rd2] offset:4096\n\tds_read_b64_tr_b16 v[118:119], %[rd2] offset:4224\n\tds_read_b64_tr_b16 v[176:177], %[rd3] offset:0\n\tds_read_b64_tr_b16 v[178:179], %[rd3] offset:128\n\tds_read_b64_tr_b16 v[180:181], %[rd3] offset:4096\n\tds_read_b64_tr_b16 v[182:183], %[rd3] offset:4224\n\ts_waitcnt lgkmcnt(0)\n\t" \
          "buffer_load_dwordx4 %[rec], %[ro], %[rr], 0 offen sc1\n\t" \
          "s_add_u32 m0, %[lw], 0\n\ts_xor_b32 %[t], %[kb], 0x0\n\tbuffer_load_dwordx4 %[go], %[rq], %[t] offen" QK_NTq " lds\n\ts_add_u32 m0, %[lw], 1024\n\ts_xor_b32 %[t], %[kb], 0x10000\n\tbuffer_load_dwordx4 %[go], %[rq], %[t] offen" QK_NTq " lds\n\ts_add_u32 m0, %[lw], 2048\n\ts_xor_b32 %[t], %[kb], 0x20000\n\tbuffer_load_dwordx4 %[go], %[rq], %[t] offen" QK_NTq " lds\n\ts_add_u32 m0, %[lw], 3072\n\ts_xor_b32 %[t], %[kb], 0x30000\n\tbuffer_load_dwordx4 %[go], %[rq], %[t] offen" QK_NTq " lds\n\ts_add_u32 m0, %[lw], 4096\n\ts_xor_b32 %[t], %[kb], 0x40000\n\tbuffer_load_dwordx4 %[go], %[rq], %[t] offen" QK_NTq " lds\n\ts_add_u32 m0, %[lw], 5120\n\ts_xor_b32 %[t], %[kb], 0x50000\n\tbuffer_load_dwordx4 %[go], %[rq], %[t] offen" QK_NTq " lds\n\ts_add_u32 m0, %[lw], 6144\n\ts_xor_b32 %[t], %[kb], 0x60000\n\tbuffer_load_dwordx4 %[go], %[rq], %[t] offen" QK_NTq " lds\n\ts_add_u32 m0, %[lw], 7168\n\ts_xor_b32 %[t], %[kb], 0x70000\n\tbuffer_load_dwordx4 %[go], %[rq], %[t] offen" QK_NTq " lds\n\t" \
          "s_waitcnt vmcnt(33)\n\tds_read_b64_tr_b16 v[184:185], %[rd0] offset:8192\n\tds_read_b64_tr_b16 v[186:187], %[rd0] offset:8320\n\tds_read_b64_tr_b16 v[188:189], %[rd0] offset:12288\n\tds_read_b64_tr_b16 v[190:191], %[rd0] offset:12416\n\tds_read_b64_tr_b16 v[88:89], %[rd1] offset:8192\n\tds_read_b64_tr_b16 v[90:91], %[rd1] offset:8320\n\tds_read_b64_tr_b16 v[92:93], %[rd1] offset:12288\n\tds_read_b64_tr_b16 v[94:95], %[rd1] offset:12416\n\tds_read_b64_tr_b16 v[192:193], %[rd2] offset:8192\n\tds_read_b64_tr_b16 v[194:195], %[rd2] offset:8320\n\tds_read_b64_tr_b16 v[196:197], %[rd2] offset:12288\n\tds_read_b64_tr_b16 v[198:199], %[rd2] offset:12416\n\tds_read_b64_tr_b16 v[96:97], %[rd3] offset:8192\n\tds_read_b64_tr_b16 v[98:99], %[rd3] offset:8320\n\tds_read_b64_tr_b16 v[100:101], %[rd3] offset:12288\n\tds_read_b64_tr_b16 v[102:103], %[rd3] offset:12416\n\ts_waitcnt lgkmcnt(0)\n\tds_write_b128 %[own], v[136:139] offset:8192\n\tds_write_b128 %[own], v[140:143] offset:9216\n\tds_write_b128 %[own], v[144:147] offset:10240\n\tds_write_b128 %[own], v[148:151] offset:11264\n\tds_write_b128 %[own], v[152:155] offset:12288\n\tds_write_b128 %[own], v[156:159] offset:13312\n\tds_write_b128 %[own], v[160:163] offset:14336\n\tds_write_b128 %[own], v[164:167] offset:15360\n\ts_waitcnt lgkmcnt(0)\n\t" \
          "s_waitcnt vmcnt(25)\n\tds_read_b64_tr_b16 v[64:65], %[rd0] offset:16384\n\tds_read_b64_tr_b16 v[66:67], %[rd0] offset:16512\n\tds_read_b64_tr_b16 v[68:69], %[rd0] offset:20480\n\tds_read_b64_tr_b16 v[70:71], %[rd0] offset:20608\n\tds_read_b64_tr_b16 v[200:201], %[rd1] offset:16384\n\tds_read_b64_tr_b16 v[202:203], %[rd1] offset:16512\n\tds_read_b64_tr_b16 v[204:205], %[rd1] offset:20480\n\tds_read_b64_tr_b16 v[206:207], %[rd1] offset:20608\n\tds_read_b64_tr_b16 v[72:73], %[rd2] offset:16384\n\tds_read_b64_tr_b16 v[74:75], %[rd2] offset:16512\n\tds_read_b64_tr_b16 v[76:77], %[rd2] offset:20480\n\tds_read_b64_tr_b16 v[78:79], %[rd2] offset:20608\n\tds_read_b64_tr_b16 v[208:209], %[rd3] offset:16384\n\tds_read_b64_tr_b16 v[210:211], %[rd3] offset:16512\n\tds_read_b64_tr_b16 v[212:213], %[rd3] offset:20480\n\tds_read_b64_tr_b16 v[214:215], %[rd3] offset:20608\n\ts_waitcnt lgkmcnt(0)\n\tds_write_b128 %[own], v[168:171] offset:16384\n\tds_write_b128 %[own], v[172:175] offset:17408\n\tds_write_b128 %[own], v[176:179] offset:18432\n\tds_write_b128 %[own], v[180:183] offset:19456\n\tds_write_b128 %[own], v[184:187] offset:20480\n\tds_write_b128 %[own], v[188:191] offset:21504\n\tds_write_b128 %[own], v[192:195] offset:22528\n\tds_write_b128 %[own], v[196:199] offset:23552\n\ts_waitcnt lgkmcnt(0)\n\t" \
          "s_waitcnt vmcnt(17)\n\tds_read_b64_tr_b16 v[216:217], %[rd0] offset:24576\n\tds_read_b64_tr_b16 v[218:219], %[rd0] offset:24704\n\tds_read_b64_tr_b16 v[220:221], %[rd0] offset:28672\n\tds_read_b64_tr_b16 v[222:223], %[rd0] offset:28800\n\tds_read_b64_tr_b16 v[48:49], %[rd1] offset:24576\n\tds_read_b64_tr_b16 v[50:51], %[rd1] offset:24704\n\tds_read_b64_tr_b16 v[52:53], %[rd1] offset:28672\n\tds_read_b64_tr_b16 v[54:55], %[rd1] offset:28800\n\tds_read_b64_tr_b16 v[224:225], %[rd2] offset:24576\n\tds_read_b64_tr_b16 v[226:227], %[rd2] offset:24704\n\tds_read_b64_tr_b16 v[228:229], %[rd2] offset:28672\n\tds_read_b64_tr_b16 v[230:231], %[rd2] offset:28800\n\tds_read_b64_tr_b16 v[56:57], %[rd3] offset:24576\n\tds_read_b64_tr_b16 v[58:59], %[rd3] offset:24704\n\tds_read_b64_tr_b16 v[60:61], %[rd3] offset:28672\n\tds_read_b64_tr_b16 v[62:63], %[rd3] offset:28800\n\ts_waitcnt lgkmcnt(0)\n\tds_write_b128 %[own], v[200:203] offset:24576\n\tds_write_b128 %[own], v[204:207] offset:25600\n\tds_write_b128 %[own], v[208:211] offset:26624\n\tds_write_b128 %[own], v[212:215] offset:27648\n\tds_write_b128 %[own], v[216:219] offset:28672\n\tds_write_b128 %[own], v[220:223] offset:29696\n\tds_write_b128 %[own], v[224:227] offset:30720\n\tds_write_b128 %[own], v[228:231] offset:31744\n\ts_waitcnt lgkmcnt(0)\n\t" \
          "s_waitcnt vmcnt(8)\n\tds_read_b64_tr_b16 v[24:25], %[rd0] offset:32768\n\tds_read_b64_tr_b16 v[26:27], %[rd0] offset:32896\n\tds_read_b64_tr_b16 v[28:29], %[rd0] offset:36864\n\tds_read_b64_tr_b16 v[30:31], %[rd0] offset:36992\n\tds_read_b64_tr_b16 v[136:137], %[rd1] offset:32768\n\tds_read_b64_tr_b16 v[138:139], %[rd1] offset:32896\n\tds_read_b64_tr_b16 v[140:141], %[rd1] offset:36864\n\tds_read_b64_tr_b16 v[142:143], %[rd1] offset:36992\n\tds_read_b64_tr_b16 v[32:33], %[rd2] offset:32768\n\tds_read_b64_tr_b16 v[34:35], %[rd2] offset:32896\n\tds_read_b64_tr_b16 v[36:37], %[rd2] offset:36864\n\tds_read_b64_tr_b16 v[38:39], %[rd2] offset:36992\n\tds_read_b64_tr_b16 v[144:145], %[rd3] offset:32768\n\tds_read_b64_tr_b16 v[146:147], %[rd3] offset:32896\n\tds_read_b64_tr_b16 v[148:149], %[rd3] offset:36864\n\tds_read_b64_tr_b16 v[150:151], %[rd3] offset:36992\n\ts_waitcnt lgkmcnt(0)\n\t" \
          "buffer_load_dword %[kf], %[ko], %[rc], 0 offen sc1\n\t" \
          "s_waitcnt vmcnt(0)\n\tds_read_b64_tr_b16 v[152:153], %[rd0] offset:0\n\tds_read_b64_tr_b16 v[154:155], %[rd0] offset:128\n\tds_read_b64_tr_b16 v[156:157], %[rd0] offset:4096\n\tds_read_b64_tr_b16 v[158:159], %[rd0] offset:4224\n\tds_read_b64_tr_b16 v[8:9], %[rd1] offset:0\n\tds_read_b64_tr_b16 v[10:11], %[rd1] offset:128\n\tds_read_b64_tr_b16 v[12:13], %[rd1] offset:4096\n\tds_read_b64_tr_b16 v[14:15], %[rd1] offset:4224\n\tds_read_b64_tr_b16 v[160:161], %[rd2] offset:0\n\tds_read_b64_tr_b16 v[162:163], %[rd2] offset:128\n\tds_read_b64_tr_b16 v[164:165], %[rd2] offset:4096\n\tds_read_b64_tr_b16 v[166:167], %[rd2] offset:4224\n\tds_read_b64_tr_b16 v[16:17], %[rd3] offset:0\n\tds_read_b64_tr_b16 v[18:19], %[rd3] offset:128\n\tds_read_b64_tr_b16 v[20:21], %[rd3] offset:4096\n\tds_read_b64_tr_b16 v[22:23], %[rd3] offset:4224\n\ts_waitcnt lgkmcnt(0)\n\tds_write_b128 %[own], v[136:139] offset:0\n\tds_write_b128 %[own], v[140:143] offset:1024\n\tds_write_b128 %[own], v[144:147] offset:2048\n\tds_write_b128 %[own], v[148:151] offset:3072\n\tds_write_b128 %[own], v[152:155] offset:4096\n\tds_write_b128 %[own], v[156:159] offset:5120\n\tds_write_b128 %[own], v[160:163] offset:6144\n\tds_write_b128 %[own], v[164:167] offset:7168\n\ts_waitcnt lgkmcnt(0)\n\t"
#define QK_DEFW "=&{a[0:31]}"(wa[0]), "=&{a[32:63]}"(wa[1]), "=&{a[64:95]}"(wa[2]), "=&{a[96:127]}"(wa[3]), "=&{a[128:159]}"(wa[4]), "=&{a[160:191]}"(wa[5]), "=&{a[192:223]}"(wa[6]), "=&{a[224:255]}"(wa[7]), "=&{v[8:39]}"(wv_[0]), "=&{v[40:71]}"(wv_[1]), "=&{v[72:103]}"(wv_[2]), "=&{v[104:135]}"(wv_[3]), "=&{v[136:167]}"(wv_[4]), "=&{v[168:199]}"(wv_[5]), "=&{v[200:231]}"(wv_[6])
#define QK_ALW 1
#define QK_STEP \
          "ds_read_b128 v[200:203], %[own] offset:4096\n\t" \
          "ds_read_b128 v[204:207], %[own] offset:5120\n\t" \
          "ds_read_b128 v[208:211], %[own] offset:6144\n\t" \
          "ds_read_b128 v[212:215], %[own] offset:7168\n\t" \
          "ds_read_b128 v[216:219], %[own] offset:8192\n\t" \
          "ds_read_b128 v[220:223], %[own] offset:9216\n\t" \
          "ds_read_b128 v[224:227], %[own] offset:0\n\t" \
          "ds_read_b128 v[228:231], %[own] offset:1024\n\t" \
          "v_mov_b32 %[al], -1\n\t" \
          "v_mov_b64 v[168:169], 0\n\t" \
          "v_mov_b64 v[170:171], 0\n\t" \
          "v_mov_b64 v[172:173], 0\n\t" \
          "v_mov_b64 v[174:175], 0\n\t" \
          "v_mov_b64 v[176:177], 0\n\t" \
          "v_mov_b64 v[178:179], 0\n\t" \
          "v_mov_b64 v[180:181], 0\n\t" \
          "v_mov_b64 v[182:183], 0\n\t" \
          "v_mov_b64 v[184:185], 0\n\t" \
          "v_mov_b64 v[186:187], 0\n\t" \
          "v_mov_b64 v[188:189], 0\n\t" \
          "v_mov_b64 v[190:191], 0\n\t" \
          "v_mov_b64 v[192:193], 0\n\t" \
          "v_mov_b64 v[194:195], 0\n\t" \
          "v_mov_b64 v[196:197], 0\n\t" \
          "v_mov_b64 v[198:199], 0\n\t" \
          "s_mov_b32 %[ann], %[t]\n\t" \
          "s_mov_b32 %[fl], 0\n\t" \
          "s_mov_b64 %[en], -1\n\t" \
          "s_waitcnt lgkmcnt(0)\n\t" \
          "s_cmp_eq_u32 %[w], 3\n\t" \
          "s_cbranch_scc0 Lqk_z%=\n\t" \
          "s_branch Lqk_c%=\n\t" \
          "Lqk_fail%=:\n\t" \
          "s_memrealtime vcc\n\t" \
          "s_waitcnt lgkmcnt(0)\n\t" \
          "s_add_u32 %[ann], vcc_lo, %[ald0]\n\t" \
          "v_mbcnt_lo_u32_b32 %[al], -1, 0\n\t" \
          "v_lshlrev_b32 %[al], 2, %[al]\n\t" \
          "s_and_b32 vcc_lo, %[ann], 0xffff\n\t" \
          "s_or_b32 vcc_lo, vcc_lo, 0x10000\n\t" \
          "v_mov_b32 %[m], vcc_lo\n\t" \
          "s_mov_b64 exec, %[alx]\n\t" \
          "buffer_store_dword %[m], %[al], %[rsm], %[als] offen sc1\n\t" \
          "s_mov_b64 exec, -1\n\t" \
          "buffer_load_dwordx4 v[136:139], %[ml0], %[rsm], %[mb0] offen sc1\n\t" \
          "buffer_load_dwordx4 v[140:143], %[ml1], %[rsm], %[mb0] offen sc1\n\t" \
          "buffer_load_dwordx4 v[144:147], %[ml0], %[rsm], %[mb1] offen sc1\n\t" \
          "buffer_load_dwordx4 v[148:151], %[ml1], %[rsm], %[mb1] offen sc1\n\t" \
          "buffer_load_dwordx4 v[152:155], %[ml0], %[rsm], %[mb2] offen sc1\n\t" \
          "buffer_load_dwordx4 v[156:159], %[ml1], %[rsm], %[mb2] offen sc1\n\t" \
          "buffer_load_dwordx4 v[160:163], %[ml0], %[rsm], %[mb3] offen sc1\n\t" \
          "buffer_load_dwordx4 v[164:167], %[ml1], %[rsm], %[mb3] offen sc1\n\t" \
          "ds_read_b128 v[200:203], %[own] offset:4096\n\t" \
          "ds_read_b128 v[204:207], %[own] offset:5120\n\t" \
          "ds_read_b128 v[208:211], %[own] offset:6144\n\t" \
          "ds_read_b128 v[212:215], %[own] offset:7168\n\t" \
          "ds_read_b128 v[216:219], %[own] offset:8192\n\t" \
          "ds_read_b128 v[220:223], %[own] offset:9216\n\t" \
          "ds_read_b128 v[224:227], %[own] offset:0\n\t" \
          "ds_read_b128 v[228:231], %[own] offset:1024\n\t" \
          "v_mov_b32 %[al], -1\n\t" \
          "v_mov_b64 v[168:169], 0\n\t" \
          "v_mov_b64 v[170:171], 0\n\t" \
          "v_mov_b64 v[172:173], 0\n\t" \
          "v_mov_b64 v[174:175], 0\n\t" \
          "v_mov_b64 v[176:177], 0\n\t" \
          "v_mov_b64 v[178:179], 0\n\t" \
          "v_mov_b64 v[180:181], 0\n\t" \
          "v_mov_b64 v[182:183], 0\n\t" \
          "v_mov_b64 v[184:185], 0\n\t" \
          "v_mov_b64 v[186:187], 0\n\t" \
          "v_mov_b64 v[188:189], 0\n\t" \
          "v_mov_b64 v[190:191], 0\n\t" \
          "v_mov_b64 v[192:193], 0\n\t" \
          "v_mov_b64 v[194:195], 0\n\t" \
          "v_mov_b64 v[196:197], 0\n\t" \
          "v_mov_b64 v[198:199], 0\n\t" \
          "s_mov_b32 %[fl], 1\n\t" \
          "s_mov_b64 %[en], 0\n\t" \
          "s_mov_b32 %[cnt], 0\n\t" \
          "s_waitcnt vmcnt(7)\n\t" \
          "v_max3_u32 %[m], v136, v137, v138\n\t" \
          "v_max_u32 %[m], %[m], v139\n\t" \
          "v_cmp_eq_u32 vcc, -1, %[m]\n\t" \
          "s_cbranch_vccnz Lqk_p0_%=\n\t" \
          "Lqk_k0_%=:\n\t" \
          "s_waitcnt vmcnt(6)\n\t" \
          "v_max3_u32 %[m], v140, v141, v142\n\t" \
          "v_max_u32 %[m], %[m], v143\n\t" \
          "v_cmp_eq_u32 vcc, -1, %[m]\n\t" \
          "s_cbranch_vccnz Lqk_p1_%=\n\t" \
          "Lqk_k1_%=:\n\t" \
          "s_waitcnt vmcnt(5)\n\t" \
          "v_max3_u32 %[m], v144, v145, v146\n\t" \
          "v_max_u32 %[m], %[m], v147\n\t" \
          "v_cmp_eq_u32 vcc, -1, %[m]\n\t" \
          "s_cbranch_vccnz Lqk_p2_%=\n\t" \
          "Lqk_k2_%=:\n\t" \
          "s_waitcnt vmcnt(4)\n\t" \
          "v_max3_u32 %[m], v148, v149, v150\n\t" \
          "v_max_u32 %[m], %[m], v151\n\t" \
          "v_cmp_eq_u32 vcc, -1, %[m]\n\t" \
          "s_cbranch_vccnz Lqk_p3_%=\n\t" \
          "Lqk_k3_%=:\n\t" \
          "s_waitcnt vmcnt(3)\n\t" \
          "v_max3_u32 %[m], v152, v153, v154\n\t" \
          "v_max_u32 %[m], %[m], v155\n\t" \
          "v_cmp_eq_u32 vcc, -1, %[m]\n\t" \
          "s_cbranch_vccnz Lqk_p4_%=\n\t" \
          "Lqk_k4_%=:\n\t" \
          "s_waitcnt vmcnt(2)\n\t" \
          "v_max3_u32 %[m], v156, v157, v158\n\t" \
          "v_max_u32 %[m], %[m], v159\n\t" \
          "v_cmp_eq_u32 vcc, -1, %[m]\n\t" \
          "s_cbranch_vccnz Lqk_p5_%=\n\t" \
          "Lqk_k5_%=:\n\t" \
          "s_waitcnt vmcnt(1)\n\t" \
          "v_max3_u32 %[m], v160, v161, v162\n\t" \
          "v_max_u32 %[m], %[m], v163\n\t" \
          "v_cmp_eq_u32 vcc, -1, %[m]\n\t" \
          "s_cbranch_vccnz Lqk_p6_%=\n\t" \
          "Lqk_k6_%=:\n\t" \
          "s_waitcnt vmcnt(0)\n\t" \
          "v_max3_u32 %[m], v164, v165, v166\n\t" \
          "v_max_u32 %[m], %[m], v167\n\t" \
          "v_cmp_eq_u32 vcc, -1, %[m]\n\t" \
          "s_cbranch_vccnz Lqk_p7_%=\n\t" \
          "Lqk_k7_%=:\n\t" \
          "s_memrealtime vcc\n\t" \
          "s_waitcnt lgkmcnt(0)\n\t" \
          "s_add_u32 vcc_lo, vcc_lo, %[ald]\n\t" \
          "s_sub_i32 vcc_hi, vcc_lo, %[ann]\n\t" \
          "s_cmp_gt_i32 vcc_hi, 0\n\t" \
          "s_cbranch_scc0 Lqk_h%=\n\t" \
          "s_mov_b32 %[ann], vcc_lo\n\t" \
          "v_mbcnt_lo_u32_b32 %[al], -1, 0\n\t" \
          "v_lshlrev_b32 %[al], 2, %[al]\n\t" \
          "s_and_b32 vcc_lo, %[ann], 0xffff\n\t" \
          "s_or_b32 vcc_lo, vcc_lo, 0x10000\n\t" \
          "v_mov_b32 %[m], vcc_lo\n\t" \
          "s_mov_b64 exec, %[alx]\n\t" \
          "buffer_store_dword %[m], %[al], %[rsm], %[als] offen sc1\n\t" \
          "s_mov_b64 exec, -1\n\t" \
          "Lqk_h%=:\n\t" \
          "v_mov_b32 %[al], -1\n\t" \
          "s_branch Lqk_go%=\n\t" \
          "Lqk_p0_%=:\n\t" \
          "s_cmp_lt_i32 %[cnt], 0\n\t" \
          "s_cbranch_scc1 Lqk_k0_%=\n\t" \
          "s_add_u32 %[cnt], %[cnt], 1\n\t" \
          "s_and_b32 vcc_lo, %[cnt], 3\n\t" \
          "s_cbranch_scc1 Lqk_q0_%=\n\t" \
          "s_getpc_b64 s[48:49]\n\t" \
          "s_branch Lqk_spin%=\n\t" \
          "Lqk_q0_%=:\n\t" \
          "buffer_load_dwordx4 v[136:139], %[ml0], %[rsm], %[mb0] offen sc1\n\t" \
          "s_waitcnt vmcnt(0)\n\t" \
          "v_max3_u32 %[m], v136, v137, v138\n\t" \
          "v_max_u32 %[m], %[m], v139\n\t" \
          "v_cmp_eq_u32 vcc, -1, %[m]\n\t" \
          "s_cbranch_vccnz Lqk_p0_%=\n\t" \
          "buffer_load_dwordx4 v[140:143], %[ml1], %[rsm], %[mb0] offen sc1\n\t" \
          "buffer_load_dwordx4 v[144:147], %[ml0], %[rsm], %[mb1] offen sc1\n\t" \
          "buffer_load_dwordx4 v[148:151], %[ml1], %[rsm], %[mb1] offen sc1\n\t" \
          "buffer_load_dwordx4 v[152:155], %[ml0], %[rsm], %[mb2] offen sc1\n\t" \
          "buffer_load_dwordx4 v[156:159], %[ml1], %[rsm], %[mb2] offen sc1\n\t" \
          "buffer_load_dwordx4 v[160:163], %[ml0], %[rsm], %[mb3] offen sc1\n\t" \
          "buffer_load_dwordx4 v[164:167], %[ml1], %[rsm], %[mb3] offen sc1\n\t" \
          "s_branch Lqk_k0_%=\n\t" \
          "Lqk_p1_%=:\n\t" \
          "s_cmp_lt_i32 %[cnt], 0\n\t" \
          "s_cbranch_scc1 Lqk_k1_%=\n\t" \
          "s_add_u32 %[cnt], %[cnt], 1\n\t" \
          "s_and_b32 vcc_lo, %[cnt], 3\n\t" \
          "s_cbranch_scc1 Lqk_q1_%=\n\t" \
          "s_getpc_b64 s[48:49]\n\t" \
          "s_branch Lqk_spin%=\n\t" \
          "Lqk_q1_%=:\n\t" \
          "buffer_load_dwordx4 v[140:143], %[ml1], %[rsm], %[mb0] offen sc1\n\t" \
          "s_waitcnt vmcnt(0)\n\t" \
          "v_max3_u32 %[m], v140, v141, v142\n\t" \
          "v_max_u32 %[m], %[m], v143\n\t" \
          "v_cmp_eq_u32 vcc, -1, %[m]\n\t" \
          "s_cbranch_vccnz Lqk_p1_%=\n\t" \
          "buffer_load_dwordx4 v[144:147], %[ml0], %[rsm], %[mb1] offen sc1\n\t" \
          "buffer_load_dwordx4 v[148:151], %[ml1], %[rsm], %[mb1] offen sc1\n\t" \
          "buffer_load_dwordx4 v[152:155], %[ml0], %[rsm], %[mb2] offen sc1\n\t" \
          "buffer_load_dwordx4 v[156:159], %[ml1], %[rsm], %[mb2] offen sc1\n\t" \
          "buffer_load_dwordx4 v[160:163], %[ml0], %[rsm], %[mb3] offen sc1\n\t" \
          "buffer_load_dwordx4 v[164:167], %[ml1], %[rsm], %[mb3] offen sc1\n\t" \
          "s_branch Lqk_k1_%=\n\t" \
          "Lqk_p2_%=:\n\t" \
          "s_cmp_lt_i32 %[cnt], 0\n\t" \
          "s_cbranch_scc1 Lqk_k2_%=\n\t" \
          "s_add_u32 %[cnt], %[cnt], 1\n\t" \
          "s_and_b32 vcc_lo, %[cnt], 3\n\t" \
          "s_cbranch_scc1 Lqk_q2_%=\n\t" \
          "s_getpc_b64 s[48:49]\n\t" \
          "s_branch Lqk_spin%=\n\t" \
          "Lqk_q2_%=:\n\t" \
          "buffer_load_dwordx4 v[144:147], %[ml0], %[rsm], %[mb1] offen sc1\n\t" \
          "s_waitcnt vmcnt(0)\n\t" \
          "v_max3_u32 %[m], v144, v145, v146\n\t" \
          "v_max_u32 %[m], %[m], v147\n\t" \
          "v_cmp_eq_u32 vcc, -1, %[m]\n\t" \
          "s_cbranch_vccnz Lqk_p2_%=\n\t" \
          "buffer_load_dwordx4 v[148:151], %[ml1], %[rsm], %[mb1] offen sc1\n\t" \
          "buffer_load_dwordx4 v[152:155], %[ml0], %[rsm], %[mb2] offen sc1\n\t" \
          "buffer_load_dwordx4 v[156:159], %[ml1], %[rsm], %[mb2] offen sc1\n\t" \
          "buffer_load_dwordx4 v[160:163], %[ml0], %[rsm], %[mb3] offen sc1\n\t" \
          "buffer_load_dwordx4 v[164:167], %[ml1], %[rsm], %[mb3] offen sc1\n\t" \
          "s_branch Lqk_k2_%=\n\t" \
          "Lqk_p3_%=:\n\t" \
          "s_cmp_lt_i32 %[cnt], 0\n\t" \
          "s_cbranch_scc1 Lqk_k3_%=\n\t" \
          "s_add_u32 %[cnt], %[cnt], 1\n\t" \
          "s_and_b32 vcc_lo, %[cnt], 3\n\t" \
          "s_cbranch_scc1 Lqk_q3_%=\n\t" \
          "s_getpc_b64 s[48:49]\n\t" \
          "s_branch Lqk_spin%=\n\t" \
          "Lqk_q3_%=:\n\t" \
          "buffer_load_dwordx4 v[148:151], %[ml1], %[rsm], %[mb1] offen sc1\n\t" \
          "s_waitcnt vmcnt(0)\n\t" \
          "v_max3_u32 %[m], v148, v149, v150\n\t" \
          "v_max_u32 %[m], %[m], v151\n\t" \
          "v_cmp_eq_u32 vcc, -1, %[m]\n\t" \
          "s_cbranch_vccnz Lqk_p3_%=\n\t" \
          "buffer_load_dwordx4 v[152:155], %[ml0], %[rsm], %[mb2] offen sc1\n\t" \
          "buffer_load_dwordx4 v[156:159], %[ml1], %[rsm], %[mb2] offen sc1\n\t" \
          "buffer_load_dwordx4 v[160:163], %[ml0], %[rsm], %[mb3] offen sc1\n\t" \
          "buffer_load_dwordx4 v[164:167], %[ml1], %[rsm], %[mb3] offen sc1\n\t" \
          "s_branch Lqk_k3_%=\n\t" \
          "Lqk_p4_%=:\n\t" \
          "s_cmp_lt_i32 %[cnt], 0\n\t" \
          "s_cbranch_scc1 Lqk_k4_%=\n\t" \
          "s_add_u32 %[cnt], %[cnt], 1\n\t" \
          "s_and_b32 vcc_lo, %[cnt], 3\n\t" \
          "s_cbranch_scc1 Lqk_q4_%=\n\t" \
          "s_getpc_b64 s[48:49]\n\t" \
          "s_branch Lqk_spin%=\n\t" \
          "Lqk_q4_%=:\n\t" \
          "buffer_load_dwordx4 v[152:155], %[ml0], %[rsm], %[mb2] offen sc1\n\t" \
          "s_waitcnt vmcnt(0)\n\t" \
          "v_max3_u32 %[m], v152, v153, v154\n\t" \
          "v_max_u32 %[m], %[m], v155\n\t" \
          "v_cmp_eq_u32 vcc, -1, %[m]\n\t" \
          "s_cbranch_vccnz Lqk_p4_%=\n\t" \
          "buffer_load_dwordx4 v[156:159], %[ml1], %[rsm], %[mb2] offen sc1\n\t" \
          "buffer_load_dwordx4 v[160:163], %[ml0], %[rsm], %[mb3] offen sc1\n\t" \
          "buffer_load_dwordx4 v[164:167], %[ml1], %[rsm], %[mb3] offen sc1\n\t" \
          "s_branch Lqk_k4_%=\n\t" \
          "Lqk_p5_%=:\n\t" \
          "s_cmp_lt_i32 %[cnt], 0\n\t" \
          "s_cbranch_scc1 Lqk_k5_%=\n\t" \
          "s_add_u32 %[cnt], %[cnt], 1\n\t" \
          "s_and_b32 vcc_lo, %[cnt], 3\n\t" \
          "s_cbranch_scc1 Lqk_q5_%=\n\t" \
          "s_getpc_b64 s[48:49]\n\t" \
          "s_branch Lqk_spin%=\n\t" \
          "Lqk_q5_%=:\n\t" \
          "buffer_load_dwordx4 v[156:159], %[ml1], %[rsm], %[mb2] offen sc1\n\t" \
          "s_waitcnt vmcnt(0)\n\t" \
          "v_max3_u32 %[m], v156, v157, v158\n\t" \
          "v_max_u32 %[m], %[m], v159\n\t" \
          "v_cmp_eq_u32 vcc, -1, %[m]\n\t" \
          "s_cbranch_vccnz Lqk_p5_%=\n\t" \
          "buffer_load_dwordx4 v[160:163], %[ml0], %[rsm], %[mb3] offen sc1\n\t" \
          "buffer_load_dwordx4 v[164:167], %[ml1], %[rsm], %[mb3] offen sc1\n\t" \
          "s_branch Lqk_k5_%=\n\t" \
          "Lqk_p6_%=:\n\t" \
          "s_cmp_lt_i32 %[cnt], 0\n\t" \
          "s_cbranch_scc1 Lqk_k6_%=\n\t" \
          "s_add_u32 %[cnt], %[cnt], 1\n\t" \
          "s_and_b32 vcc_lo, %[cnt], 3\n\t" \
          "s_cbranch_scc1 Lqk_q6_%=\n\t" \
          "s_getpc_b64 s[48:49]\n\t" \
          "s_branch Lqk_spin%=\n\t" \
          "Lqk_q6_%=:\n\t" \
          "buffer_load_dwordx4 v[160:163], %[ml0], %[rsm], %[mb3] offen sc1\n\t" \
          "s_waitcnt vmcnt(0)\n\t" \
          "v_max3_u32 %[m], v160, v161, v162\n\t" \
          "v_max_u32 %[m], %[m], v163\n\t" \
          "v_cmp_eq_u32 vcc, -1, %[m]\n\t" \
          "s_cbranch_vccnz Lqk_p6_%=\n\t" \
          "buffer_load_dwordx4 v[164:167], %[ml1], %[rsm], %[mb3] offen sc1\n\t" \
          "s_branch Lqk_k6_%=\n\t" \
          "Lqk_p7_%=:\n\t" \
          "s_cmp_lt_i32 %[cnt], 0\n\t" \
          "s_cbranch_scc1 Lqk_k7_%=\n\t" \
          "s_add_u32 %[cnt], %[cnt], 1\n\t" \
          "s_and_b32 vcc_lo, %[cnt], 3\n\t" \
          "s_cbranch_scc1 Lqk_q7_%=\n\t" \
          "s_getpc_b64 s[48:49]\n\t" \
          "s_branch Lqk_spin%=\n\t" \
          "Lqk_q7_%=:\n\t" \
          "buffer_load_dwordx4 v[164:167], %[ml1], %[rsm], %[mb3] offen sc1\n\t" \
          "s_waitcnt vmcnt(0)\n\t" \
          "v_max3_u32 %[m], v164, v165, v166\n\t" \
          "v_max_u32 %[m], %[m], v167\n\t" \
          "v_cmp_eq_u32 vcc, -1, %[m]\n\t" \
          "s_cbranch_vccnz Lqk_p7_%=\n\t" \
          "s_branch Lqk_k7_%=\n\t" \
          "Lqk_spin%=:\n\t" \
          "s_memrealtime vcc\n\t" \
          "s_waitcnt lgkmcnt(0)\n\t" \
          "s_add_u32 %[ann], vcc_lo, %[ald0]\n\t" \
          "v_mbcnt_lo_u32_b32 %[al], -1, 0\n\t" \
          "v_lshlrev_b32 %[al], 2, %[al]\n\t" \
          "s_and_b32 vcc_lo, %[ann], 0xffff\n\t" \
          "s_or_b32 vcc_lo, vcc_lo, 0x10000\n\t" \
          "v_mov_b32 %[m], vcc_lo\n\t" \
          "s_mov_b64 exec, %[alx]\n\t" \
          "buffer_store_dword %[m], %[al], %[rsm], %[als] offen sc1\n\t" \
          "s_mov_b64 exec, -1\n\t" \
          "buffer_load_dword %[al], off, %[rsc], %[tmo] sc1\n\t" \
          "s_add_u32 s48, s48, 4\n\t" \
          "s_addc_u32 s49, s49, 0\n\t" \
          "s_waitcnt vmcnt(0)\n\t" \
          "v_readfirstlane_b32 vcc_lo, %[al]\n\t" \
          "s_lshr_b32 vcc_hi, %[cnt], 16\n\t" \
          "s_or_b32 vcc_lo, vcc_lo, vcc_hi\n\t" \
          "s_cbranch_scc0 Lqk_back%=\n\t" \
          "v_mov_b32 %[al], 1\n\t" \
          "buffer_store_dword %[al], off, %[rsc], %[tmo] sc1\n\t" \
          "s_mov_b32 %[cnt], -1\n\t" \
          "Lqk_back%=:\n\t" \
          "s_setpc_b64 s[48:49]\n\t" \
          ".p2align 6\n\t" \
          "s_nop 0\n\t" \
          "s_nop 0\n\t" \
          "Lqk_s%=:\n\t" \
          "s_sleep 2\n\t" \
          "Lqk_c%=:\n\t" \
          "s_memrealtime vcc\n\t" \
          "s_waitcnt lgkmcnt(0)\n\t" \
          "s_sub_i32 vcc_lo, %[t], vcc_lo\n\t" \
          "s_cmp_gt_i32 vcc_lo, 16\n\t" \
          "s_cbranch_scc1 Lqk_s%=\n\t" \
          "Lqk_w%=:\n\t" \
          "s_memrealtime s[50:51]\n\t" \
          "s_getpc_b64 s[48:49]\n\t" \
          "Lqk_y%=:\n\t" \
          "s_add_u32 s48, s48, Lqk_z%=-Lqk_y%=\n\t" \
          "s_addc_u32 s49, s49, 0\n\t" \
          "Lqk_v%=:\n\t" \
          "s_memrealtime s[52:53]\n\t" \
          "s_waitcnt lgkmcnt(1)\n\t" \
          "s_sub_i32 vcc_lo, %[t], s50\n\t" \
          "s_cmp_gt_i32 vcc_lo, 3\n\t" \
          "s_cbranch_scc0 Lqk_x%=\n\t" \
          "s_memrealtime s[50:51]\n\t" \
          "s_waitcnt lgkmcnt(1)\n\t" \
          "s_sub_i32 vcc_lo, %[t], s52\n\t" \
          "s_cmp_gt_i32 vcc_lo, 3\n\t" \
          "s_cbranch_scc0 Lqk_x%=\n\t" \
          "s_memrealtime s[52:53]\n\t" \
          "s_waitcnt lgkmcnt(1)\n\t" \
          "s_sub_i32 vcc_lo, %[t], s50\n\t" \
          "s_cmp_gt_i32 vcc_lo, 3\n\t" \
          "s_cbranch_scc0 Lqk_x%=\n\t" \
          "s_memrealtime s[50:51]\n\t" \
          "s_waitcnt lgkmcnt(1)\n\t" \
          "s_sub_i32 vcc_lo, %[t], s52\n\t" \
          "s_cmp_gt_i32 vcc_lo, 3\n\t" \
          "s_cbranch_scc0 Lqk_x%=\n\t" \
          "s_memrealtime s[52:53]\n\t" \
          "s_waitcnt lgkmcnt(1)\n\t" \
          "s_sub_i32 vcc_lo, %[t], s50\n\t" \
          "s_cmp_gt_i32 vcc_lo, 3\n\t" \
          "s_cbranch_scc0 Lqk_x%=\n\t" \
          "s_memrealtime s[50:51]\n\t" \
          "s_waitcnt lgkmcnt(1)\n\t" \
          "s_sub_i32 vcc_lo, %[t], s52\n\t" \
          "s_cmp_gt_i32 vcc_lo, 3\n\t" \
          "s_cbranch_scc0 Lqk_x%=\n\t" \
          "s_memrealtime s[52:53]\n\t" \
          "s_waitcnt lgkmcnt(1)\n\t" \
          "s_sub_i32 vcc_lo, %[t], s50\n\t" \
          "s_cmp_gt_i32 vcc_lo, 3\n\t" \
          "s_cbranch_scc0 Lqk_x%=\n\t" \
          "s_memrealtime s[50:51]\n\t" \
          "s_waitcnt lgkmcnt(1)\n\t" \
          "s_sub_i32 vcc_lo, %[t], s52\n\t" \
          "s_cmp_gt_i32 vcc_lo, 3\n\t" \
          "s_cbranch_scc0 Lqk_x%=\n\t" \
          "s_branch Lqk_v%=\n\t" \
          "Lqk_x%=:\n\t" \
          "s_max_i32 vcc_lo, vcc_lo, 0\n\t" \
          "s_lshl_b32 vcc_lo, vcc_lo, 2\n\t" \
          "s_sub_u32 s48, s48, vcc_lo\n\t" \
          "s_subb_u32 s49, s49, 0\n\t" \
          "s_setpc_b64 s[48:49]\n\t" \
          "s_nop 5\n\t" \
          "s_nop 5\n\t" \
          "s_nop 5\n\t" \
          "Lqk_z%=:\n\t" \
          "s_barrier\n\t" \
          "buffer_load_dwordx4 v[136:139], %[ml0], %[rsm], %[mb0] offen sc1\n\t" \
          "s_nop 9\n\t" \
          "buffer_load_dwordx4 v[140:143], %[ml1], %[rsm], %[mb0] offen sc1\n\t" \
          "s_nop 9\n\t" \
          "buffer_load_dwordx4 v[144:147], %[ml0], %[rsm], %[mb1] offen sc1\n\t" \
          "s_nop 9\n\t" \
          "buffer_load_dwordx4 v[148:151], %[ml1], %[rsm], %[mb1] offen sc1\n\t" \
          "s_nop 9\n\t" \
          "buffer_load_dwordx4 v[152:155], %[ml0], %[rsm], %[mb2] offen sc1\n\t" \
          "s_nop 9\n\t" \
          "buffer_load_dwordx4 v[156:159], %[ml1], %[rsm], %[mb2] offen sc1\n\t" \
          "s_nop 9\n\t" \
          "buffer_load_dwordx4 v[160:163], %[ml0], %[rsm], %[mb3] offen sc1\n\t" \
          "s_nop 9\n\t" \
          "buffer_load_dwordx4 v[164:167], %[ml1], %[rsm], %[mb3] offen sc1\n\t" \
          "s_waitcnt vmcnt(7)\n\t" \
          "Lqk_go%=:\n\t" \
          "v_smfmac_f32_16x16x64_bf16 v[168:171], v[136:139], v[200:207], %[idx]\n\t" \
          "ds_read_b128 v[200:203], %[own] offset:2048\n\t" \
          "v_smfmac_f32_16x16x64_bf16 v[172:175], v[136:139], v[8:15], %[idx]\n\t" \
          "ds_read_b128 v[204:207], %[own] offset:3072\n\t" \
          "v_smfmac_f32_16x16x64_bf16 v[176:179], v[136:139], v[208:215], %[idx]\n\t" \
          "ds_read_b128 v[208:211], %[own] offset:28672\n\t" \
          "v_smfmac_f32_16x16x64_bf16 v[180:183], v[136:139], v[16:23], %[idx]\n\t" \
          "ds_read_b128 v[212:215], %[own] offset:29696\n\t" \
          "s_waitcnt vmcnt(6)\n\t" \
          "v_smfmac_f32_16x16x64_bf16 v[184:187], v[136:139], v[216:223], %[idx]\n\t" \
          "ds_read_b128 v[216:219], %[own] offset:30720\n\t" \
          "v_smfmac_f32_16x16x64_bf16 v[168:171], v[140:143], v[24:31], %[idx]\n\t" \
          "ds_read_b128 v[220:223], %[own] offset:31744\n\t" \
          "v_smfmac_f32_16x16x64_bf16 v[172:175], v[140:143], v[224:231], %[idx]\n\t" \
          "ds_read_b128 v[224:227], %[own] offset:10240\n\t" \
          "v_smfmac_f32_16x16x64_bf16 v[176:179], v[140:143], v[32:39], %[idx]\n\t" \
          "ds_read_b128 v[228:231], %[own] offset:11264\n\t" \
          "s_waitcnt lgkmcnt(6)\n\t" \
          "v_smfmac_f32_16x16x64_bf16 v[180:183], v[140:143], v[200:207], %[idx]\n\t" \
          "ds_read_b128 v[200:203], %[own] offset:24576\n\t" \
          "v_max3_u32 %[m], v136, v137, v138\n\t" \
          "v_smfmac_f32_16x16x64_bf16 v[184:187], v[140:143], v[40:47], %[idx]\n\t" \
          "ds_read_b128 v[204:207], %[own] offset:25600\n\t" \
          "s_waitcnt vmcnt(5) lgkmcnt(6)\n\t" \
          "v_smfmac_f32_16x16x64_bf16 v[168:171], v[144:147], v[208:215], %[idx]\n\t" \
          "ds_read_b128 v[208:211], %[own] offset:26624\n\t" \
          "v_max3_u32 %[m], v138, v139, %[m]\n\t" \
          "v_smfmac_f32_16x16x64_bf16 v[172:175], v[144:147], v[48:55], %[idx]\n\t" \
          "ds_read_b128 v[212:215], %[own] offset:27648\n\t" \
          "s_waitcnt lgkmcnt(6)\n\t" \
          "v_smfmac_f32_16x16x64_bf16 v[176:179], v[144:147], v[216:223], %[idx]\n\t" \
          "ds_read_b128 v[216:219], %[own] offset:20480\n\t" \
          "v_cmp_eq_u32 %[bad1], -1, %[m]\n\t" \
          "v_smfmac_f32_16x16x64_bf16 v[180:183], v[144:147], v[56:63], %[idx]\n\t" \
          "ds_read_b128 v[220:223], %[own] offset:21504\n\t" \
          "s_waitcnt vmcnt(4) lgkmcnt(6)\n\t" \
          "v_smfmac_f32_16x16x64_bf16 v[184:187], v[144:147], v[224:231], %[idx]\n\t" \
          "ds_read_b128 v[224:227], %[own] offset:22528\n\t" \
          "s_and_b64 %[bad1], %[bad1], %[en]\n\t" \
          "v_smfmac_f32_16x16x64_bf16 v[168:171], v[148:151], v[64:71], %[idx]\n\t" \
          "ds_read_b128 v[228:231], %[own] offset:23552\n\t" \
          "s_waitcnt lgkmcnt(6)\n\t" \
          "v_smfmac_f32_16x16x64_bf16 v[172:175], v[148:151], v[200:207], %[idx]\n\t" \
          "ds_read_b128 v[200:203], %[own] offset:12288\n\t" \
          "s_cbranch_scc1 Lqk_fail%=\n\t" \
          "v_smfmac_f32_16x16x64_bf16 v[176:179], v[148:151], v[72:79], %[idx]\n\t" \
          "ds_read_b128 v[204:207], %[own] offset:13312\n\t" \
          "s_waitcnt lgkmcnt(6)\n\t" \
          "v_smfmac_f32_16x16x64_bf16 v[180:183], v[148:151], v[208:215], %[idx]\n\t" \
          "ds_read_b128 v[208:211], %[own] offset:16384\n\t" \
          "v_max3_u32 %[m], v140, v141, %[m]\n\t" \
          "v_smfmac_f32_16x16x64_bf16 v[184:187], v[148:151], v[80:87], %[idx]\n\t" \
          "ds_read_b128 v[212:215], %[own] offset:17408\n\t" \
          "s_waitcnt vmcnt(3) lgkmcnt(6)\n\t" \
          "v_smfmac_f32_16x16x64_bf16 v[168:171], v[152:155], v[216:223], %[idx]\n\t" \
          "ds_read_b128 v[216:219], %[own] offset:18432\n\t" \
          "v_max3_u32 %[m], v142, v143, %[m]\n\t" \
          "v_smfmac_f32_16x16x64_bf16 v[172:175], v[152:155], v[88:95], %[idx]\n\t" \
          "ds_read_b128 v[220:223], %[own] offset:19456\n\t" \
          "s_waitcnt lgkmcnt(6)\n\t" \
          "v_smfmac_f32_16x16x64_bf16 v[176:179], v[152:155], v[224:231], %[idx]\n\t" \
          "ds_read_b128 v[224:227], %[own] offset:14336\n\t" \
          "v_max3_u32 %[m], v144, v145, %[m]\n\t" \
          "v_smfmac_f32_16x16x64_bf16 v[180:183], v[152:155], v[96:103], %[idx]\n\t" \
          "ds_read_b128 v[228:231], %[own] offset:15360\n\t" \
          "s_waitcnt vmcnt(2) lgkmcnt(6)\n\t" \
          "v_smfmac_f32_16x16x64_bf16 v[184:187], v[152:155], v[200:207], %[idx]\n\t" \
          "v_max3_u32 %[m], v146, v147, %[m]\n\t" \
          "v_smfmac_f32_16x16x64_bf16 v[168:171], v[156:159], v[104:111], %[idx]\n\t" \
          "s_waitcnt lgkmcnt(4)\n\t" \
          "v_max3_u32 %[m], v148, v149, %[m]\n\t" \
          "v_smfmac_f32_16x16x64_bf16 v[172:175], v[156:159], v[208:215], %[idx]\n\t" \
          "v_max3_u32 %[m], v150, v151, %[m]\n\t" \
          "v_smfmac_f32_16x16x64_bf16 v[176:179], v[156:159], v[112:119], %[idx]\n\t" \
          "s_waitcnt lgkmcnt(2)\n\t" \
          "v_max3_u32 %[m], v152, v153, %[m]\n\t" \
          "v_smfmac_f32_16x16x64_bf16 v[180:183], v[156:159], v[216:223], %[idx]\n\t" \
          "ds_read_b128 v[216:219], %[own] offset:32768\n\t" \
          "v_max3_u32 %[m], v154, v155, %[m]\n\t" \
          "v_smfmac_f32_16x16x64_bf16 v[184:187], v[156:159], v[120:127], %[idx]\n\t" \
          "ds_read_b128 v[220:223], %[own] offset:33792\n\t" \
          "s_waitcnt vmcnt(1) lgkmcnt(2)\n\t" \
          "v_smfmac_f32_16x16x64_bf16 v[168:171], v[160:163], v[224:231], %[idx]\n\t" \
          "ds_read_b64_tr_b16 v[224:225], %[vxp]\n\t" \
          "v_max3_u32 %[m], v156, v157, %[m]\n\t" \
          "v_smfmac_f32_16x16x64_bf16 v[172:175], v[160:163], v[128:135], %[idx]\n\t" \
          "ds_read_b64_tr_b16 v[226:227], %[vxp] offset:1024\n\t" \
          "v_max3_u32 %[m], v158, v159, %[m]\n\t" \
          "v_smfmac_f32_16x16x64_bf16 v[176:179], v[160:163], a[0:7], %[idx]\n\t" \
          "ds_read_b64_tr_b16 v[228:229], %[vxp] offset:8\n\t" \
          "v_max3_u32 %[m], v160, v161, %[m]\n\t" \
          "v_smfmac_f32_16x16x64_bf16 v[180:183], v[160:163], a[8:15], %[idx]\n\t" \
          "ds_read_b64_tr_b16 v[230:231], %[vxp] offset:1032\n\t" \
          "v_smfmac_f32_16x16x64_bf16 v[184:187], v[160:163], a[16:23], %[idx]\n\t" \
          "s_waitcnt vmcnt(0)\n\t" \
          "v_max3_u32 %[m], v162, v163, %[m]\n\t" \
          "v_smfmac_f32_16x16x64_bf16 v[168:171], v[164:167], a[24:31], %[idx]\n\t" \
          "v_max3_u32 %[m], v164, v165, %[m]\n\t" \
          "v_smfmac_f32_16x16x64_bf16 v[172:175], v[164:167], a[32:39], %[idx]\n\t" \
          "v_max3_u32 %[m], v166, v167, %[m]\n\t" \
          "v_smfmac_f32_16x16x64_bf16 v[176:179], v[164:167], a[40:47], %[idx]\n\t" \
          "v_cvt_pk_bf16_f32 v200, v168, v169\n\t" \
          "v_cvt_pk_bf16_f32 v201, v170, v171\n\t" \
          "v_smfmac_f32_16x16x64_bf16 v[180:183], v[164:167], a[48:55], %[idx]\n\t" \
          "ds_write_b64 %[own], v[200:201] offset:36864\n\t" \
          "v_cvt_pk_bf16_f32 v202, v172, v173\n\t" \
          "v_cvt_pk_bf16_f32 v203, v174, v175\n\t" \
          "v_smfmac_f32_16x16x64_bf16 v[184:187], v[164:167], a[56:63], %[idx]\n\t" \
          "ds_write_b64 %[own], v[202:203] offset:36872\n\t" \
          "v_smfmac_f32_16x16x64_bf16 v[188:191], v[136:139], a[64:71], %[idx]\n\t" \
          "v_cmp_eq_u32 %[bad1], -1, %[m]\n\t" \
          "v_smfmac_f32_16x16x64_bf16 v[188:191], v[140:143], a[72:79], %[idx]\n\t" \
          "buffer_store_dword %[al], %[ho], %[rsm], %[rb] offen sc1\n\t" \
          "v_cvt_pk_bf16_f32 v208, v176, v177\n\t" \
          "v_smfmac_f32_16x16x64_bf16 v[188:191], v[144:147], a[80:87], %[idx]\n\t" \
          "s_and_b64 %[bad1], %[bad1], %[en]\n\t" \
          "s_cbranch_scc1 Lqk_fail%=\n\t" \
          "s_waitcnt lgkmcnt(0)\n\t" \
          "s_barrier\n\t" \
          "v_cvt_pk_bf16_f32 v209, v178, v179\n\t" \
          "v_smfmac_f32_16x16x64_bf16 v[188:191], v[148:151], a[88:95], %[idx]\n\t" \
          "ds_read_b128 v[210:213], %[p1] offset:36864\n\t" \
          "v_cvt_pk_bf16_f32 v214, v180, v181\n\t" \
          "v_smfmac_f32_16x16x64_bf16 v[188:191], v[152:155], a[96:103], %[idx]\n\t" \
          "buffer_store_dword %[al], %[ho], %[rsm], %[rb] offen offset:256 sc1\n\t" \
          "v_cvt_pk_bf16_f32 v215, v182, v183\n\t" \
          "v_smfmac_f32_16x16x64_bf16 v[188:191], v[156:159], a[104:111], %[idx]\n\t" \
          "v_cvt_pk_bf16_f32 v204, v184, v185\n\t" \
          "v_smfmac_f32_16x16x64_bf16 v[188:191], v[160:163], a[112:119], %[idx]\n\t" \
          "v_cvt_pk_bf16_f32 v205, v186, v187\n\t" \
          "v_smfmac_f32_16x16x64_bf16 v[188:191], v[164:167], a[120:127], %[idx]\n\t" \
          "s_waitcnt lgkmcnt(0)\n\t" \
          "v_mfma_f32_16x16x32_bf16 v[168:171], v[208:211], %[jk], 0\n\t" \
          "v_smfmac_f32_16x16x64_bf16 v[192:195], v[136:139], a[128:135], %[idx]\n\t" \
          "v_mfma_f32_16x16x32_bf16 v[172:175], v[212:215], %[jk], 0\n\t" \
          "v_cvt_pk_bf16_f32 v176, v168, v169\n\t" \
          "v_cvt_pk_bf16_f32 v177, v170, v171\n\t" \
          "v_smfmac_f32_16x16x64_bf16 v[196:199], v[136:139], a[136:143], %[idx]\n\t" \
          "ds_write_b64 %[own], v[204:205] offset:37888\n\t" \
          "v_cvt_pk_bf16_f32 v206, v188, v189\n\t" \
          "v_smfmac_f32_16x16x64_bf16 v[192:195], v[140:143], a[144:151], %[idx]\n\t" \
          "v_cvt_pk_bf16_f32 v178, v172, v173\n\t" \
          "v_cvt_pk_bf16_f32 v179, v174, v175\n\t" \
          "v_smfmac_f32_16x16x64_bf16 v[196:199], v[140:143], a[152:159], %[idx]\n\t" \
          "v_cvt_pk_bf16_f32 v207, v190, v191\n\t" \
          "v_mfma_f32_16x16x32_bf16 v[208:211], v[216:219], v[176:179], 0\n\t" \
          "v_mov_b32 v168, %[one]\n\t" \
          "v_mov_b32 v169, %[one]\n\t" \
          "v_mfma_f32_16x16x32_bf16 v[212:215], v[220:223], v[176:179], 0\n\t" \
          "v_mov_b32 v170, %[one]\n\t" \
          "v_mov_b32 v171, %[one]\n\t" \
          "v_smfmac_f32_16x16x64_bf16 v[192:195], v[144:147], a[160:167], %[idx]\n\t" \
          "ds_write_b64 %[own], v[206:207] offset:37896\n\t" \
          "v_smfmac_f32_16x16x64_bf16 v[196:199], v[144:147], a[168:175], %[idx]\n\t" \
          "v_cndmask_b32 v208, v208, v212, %[twin]\n\t" \
          "v_cndmask_b32 v209, v209, v213, %[twin]\n\t" \
          "v_smfmac_f32_16x16x64_bf16 v[192:195], v[148:151], a[176:183], %[idx]\n\t" \
          "ds_write_b64 %[own], v[208:209] offset:38912\n\t" \
          "v_cndmask_b32 v210, v210, v214, %[twin]\n\t" \
          "v_cndmask_b32 v211, v211, v215, %[twin]\n\t" \
          "v_smfmac_f32_16x16x64_bf16 v[196:199], v[148:151], a[184:191], %[idx]\n\t" \
          "ds_write_b64 %[own], v[210:211] offset:38920\n\t" \
          "v_smfmac_f32_16x16x64_bf16 v[192:195], v[152:155], a[192:199], %[idx]\n\t" \
          "v_mul_f32 v212, %[qs], v208\n\t" \
          "v_smfmac_f32_16x16x64_bf16 v[196:199], v[152:155], a[200:207], %[idx]\n\t" \
          "buffer_load_dword v232, %[ho], %[rsm], %[vb] offen sc1\n\t" \
          "v_mul_f32 v213, %[qs], v209\n\t" \
          "v_smfmac_f32_16x16x64_bf16 v[192:195], v[156:159], a[208:215], %[idx]\n\t" \
          "buffer_load_dword v234, %[ho], %[rsm], %[vb] offen offset:256 sc1\n\t" \
          "v_mul_f32 v214, %[qs], v210\n\t" \
          "v_smfmac_f32_16x16x64_bf16 v[196:199], v[156:159], a[216:223], %[idx]\n\t" \
          "s_waitcnt lgkmcnt(0)\n\t" \
          "s_barrier\n\t" \
          "v_mul_f32 v215, %[qs], v211\n\t" \
          "v_smfmac_f32_16x16x64_bf16 v[192:195], v[160:163], a[224:231], %[idx]\n\t" \
          "ds_read_b128 v[138:141], %[p1] offset:37888\n\t" \
          "v_smfmac_f32_16x16x64_bf16 v[196:199], v[160:163], a[232:239], %[idx]\n\t" \
          "ds_read_b128 v[216:219], %[p1] offset:38912\n\t" \
          "v_smfmac_f32_16x16x64_bf16 v[192:195], v[164:167], a[240:247], %[idx]\n\t" \
          "ds_read_b128 v[220:223], %[p2] offset:38912\n\t" \
          "v_smfmac_f32_16x16x64_bf16 v[196:199], v[164:167], a[248:255], %[idx]\n\t" \
          "ds_read_b128 v[200:203], %[p3] offset:38912\n\t" \
          "s_nop 0\n\t" \
          "s_waitcnt lgkmcnt(2)\n\t" \
          "v_cvt_pk_bf16_f32 v136, v192, v193\n\t" \
          "v_cvt_pk_bf16_f32 v137, v194, v195\n\t" \
          "v_fmac_f32 v212, %[qs], v216\n\t" \
          "v_fmac_f32 v213, %[qs], v217\n\t" \
          "v_cvt_pk_bf16_f32 v142, v196, v197\n\t" \
          "v_cvt_pk_bf16_f32 v143, v198, v199\n\t" \
          "v_mfma_f32_16x16x32_bf16 v[144:147], v[136:139], %[jk], 0\n\t" \
          "v_fmac_f32 v214, %[qs], v218\n\t" \
          "v_mfma_f32_16x16x32_bf16 v[148:151], v[140:143], %[jk], 0\n\t" \
          "v_fmac_f32 v215, %[qs], v219\n\t" \
          "s_waitcnt lgkmcnt(0)\n\t" \
          "v_fmac_f32 v212, %[qs], v220\n\t" \
          "v_fmac_f32 v213, %[qs], v221\n\t" \
          "v_cvt_pk_bf16_f32 v152, v144, v145\n\t" \
          "v_cvt_pk_bf16_f32 v153, v146, v147\n\t" \
          "v_fmac_f32 v214, %[qs], v222\n\t" \
          "v_fmac_f32 v215, %[qs], v223\n\t" \
          "v_cvt_pk_bf16_f32 v154, v148, v149\n\t" \
          "v_cvt_pk_bf16_f32 v155, v150, v151\n\t" \
          "v_pk_fma_f32 v[212:213], v[200:201], %[qs2], v[212:213] op_sel_hi:[1,0,1]\n\t" \
          "v_pk_fma_f32 v[214:215], v[202:203], %[qs2], v[214:215] op_sel_hi:[1,0,1]\n\t" \
          "v_mfma_f32_16x16x32_bf16 v[156:159], v[152:155], v[176:179], 0\n\t" \
          "v_exp_f32 v180, v212\n\t" \
          "v_mfma_f32_16x16x32_bf16 v[196:199], v[168:171], v[168:171], 0\n\t" \
          "v_exp_f32 v181, v213\n\t" \
          "v_mfma_f32_16x16x32_bf16 v[196:199], v[168:171], v[168:171], 0\n\t" \
          "ds_write_b64 %[own], v[156:157] offset:39936\n\t" \
          "v_exp_f32 v182, v214\n\t" \
          "v_mov_b32 v137, 0\n\t" \
          "v_exp_f32 v183, v215\n\t" \
          "v_cvt_pk_bf16_f32 v172, v180, v181\n\t" \
          "v_cvt_pk_bf16_f32 v173, v182, v183\n\t" \
          "v_pk_mul_f32 v[160:161], v[156:157], %[qs2] op_sel_hi:[1,0]\n\t" \
          "v_mov_b32_dpp v174, v172 quad_perm:[2,3,0,1] row_mask:0xf bank_mask:0xf\n\t" \
          "v_mov_b32_dpp v175, v173 quad_perm:[2,3,0,1] row_mask:0xf bank_mask:0xf\n\t" \
          "s_nop 0\n\t" \
          "s_waitcnt lgkmcnt(0)\n\t" \
          "v_mfma_f32_16x16x32_bf16 v[184:187], v[172:175], v[168:171], 0\n\t" \
          "s_barrier\n\t" \
          "ds_read_b64 v[144:145], %[p1] offset:39936\n\t" \
          "ds_read_b64 v[146:147], %[p2] offset:39936\n\t" \
          "v_mfma_f32_16x16x32_bf16 v[188:191], v[172:175], v[224:227], 0\n\t" \
          "ds_read_b64 v[148:149], %[p3] offset:39936\n\t" \
          "v_mfma_f32_16x16x32_bf16 v[192:195], v[172:175], v[228:231], 0\n\t" \
          "s_waitcnt lgkmcnt(2)\n\t" \
          "v_fmac_f32 v160, %[qs], v144\n\t" \
          "v_fmac_f32 v161, %[qs], v145\n\t" \
          "s_waitcnt lgkmcnt(1)\n\t" \
          "v_fmac_f32 v160, %[qs], v146\n\t" \
          "v_fmac_f32 v161, %[qs], v147\n\t" \
          "s_waitcnt lgkmcnt(0)\n\t" \
          "v_fmac_f32 v160, %[qs], v148\n\t" \
          "v_fmac_f32 v161, %[qs], v149\n\t" \
          "v_exp_f32 v162, v160\n\t" \
          "v_exp_f32 v163, v161\n\t" \
          "v_max_f32 v182, v160, v161\n\t" \
          "v_cvt_pk_bf16_f32 v136, v162, v163\n\t" \
          "v_cmp_nge_f32 vcc, %[th], v182\n\t" \
          "s_waitcnt vmcnt(0)\n\t" \
          "v_mfma_f32_16x16x16_bf16 v[164:167], v[136:137], v[168:169], v[184:187]\n\t" \
          "v_max_f32 v180, v184, v185\n\t" \
          "v_min_f32 v181, v184, v185\n\t" \
          "v_mfma_f32_16x16x16_bf16 v[188:191], v[136:137], v[232:233], v[188:191]\n\t" \
          "v_cmp_nge_f32 %[b2], %[hi], v180\n\t" \
          "v_cmp_nle_f32 %[b3], %[lo], v181\n\t" \
          "v_mfma_f32_16x16x16_bf16 v[192:195], v[136:137], v[234:235], v[192:195]\n\t" \
          "v_max_u32 v183, v232, v234\n\t" \
          "v_cmp_eq_u32 %[bad], -1, v183\n\t" \
          "v_rcp_f32 v162, v164\n\t" \
          "s_or_b64 %[b2], %[b2], %[b3]\n\t" \
          "v_rcp_f32 v163, v165\n\t" \
          "s_or_b64 %[b2], %[b2], vcc\n\t" \
          "v_pk_mul_f32 v[156:157], v[188:189], v[162:163]\n\t" \
          "s_or_b64 %[bad], %[bad], %[b2]\n\t" \
          "s_cselect_b64 exec, 0, -1\n\t" \
          "v_pk_mul_f32 v[158:159], v[192:193], v[162:163]\n\t" \
          "s_nop 0\n\t" \
          "v_cvt_pk_bf16_f32 v156, v156, v158\n\t" \
          "v_cvt_pk_bf16_f32 v157, v157, v159\n\t" \
          "v_min_u32 v156, -2, v156\n\t" \
          "v_min_u32 v157, -2, v157\n\t" \
          "buffer_store_dword v156, %[ho], %[rsm], %[ob] offen sc1\n\t" \
          "buffer_store_dword v157, %[ho], %[rsm], %[ob] offen offset:256 sc1\n\t" \
          "s_memrealtime %[po]\n\t" \
          "s_mov_b64 exec, -1\n\t" \
          "s_sleep 1\n\t" \
          "buffer_load_dword %[al], %[alo], %[rsm], %[mb] offen sc1\n\t" \
          "s_waitcnt lgkmcnt(0)\n\t"
constexpr int QK_SX = 12, QK_SM = 24, QK_KM = 16, QK_VX = 24, QK_KV = 32768, QK_EX = 36864;
#define VO_BODY \
          "s_mov_b64 %[en], -1\n\t" \
          "s_cmp_eq_u32 %[pw], 0\n\t" \
          "s_cbranch_scc1 Lo_c%=\n\t" \
          "v_mov_b64 v[224:225], 0\n\t" \
          "v_mov_b64 v[226:227], 0\n\t" \
          "v_mov_b64 v[228:229], 0\n\t" \
          "v_mov_b64 v[230:231], 0\n\t" \
          "v_mov_b64 v[232:233], 0\n\t" \
          "v_mov_b64 v[234:235], 0\n\t" \
          "v_mov_b64 v[236:237], 0\n\t" \
          "v_mov_b64 v[238:239], 0\n\t" \
          "v_mov_b64 v[176:177], 0\n\t" \
          "v_mov_b64 v[178:179], 0\n\t" \
          "v_mov_b64 v[180:181], 0\n\t" \
          "v_mov_b64 v[182:183], 0\n\t" \
          "v_mov_b64 v[184:185], 0\n\t" \
          "v_mov_b64 v[186:187], 0\n\t" \
          "v_mov_b64 v[188:189], 0\n\t" \
          "v_mov_b64 v[190:191], 0\n\t" \
          "s_cmp_eq_u32 %[s1], 0\n\t" \
          "s_cbranch_scc1 Lv_n%=\n\t" \
          "s_branch Lo_z%=\n\t" \
          "Lo_f0_%=:\n\t" \
          "buffer_load_dwordx4 v[192:195], %[oo0], %[rsm], %[ob] offen sc1\n\t" \
          "Lo_f1_%=:\n\t" \
          "buffer_load_dwordx4 v[196:199], %[oo1], %[rsm], %[ob] offen sc1\n\t" \
          "Lo_f2_%=:\n\t" \
          "buffer_load_dwordx4 v[200:203], %[oo2], %[rsm], %[ob] offen sc1\n\t" \
          "Lo_f3_%=:\n\t" \
          "buffer_load_dwordx4 v[204:207], %[oo3], %[rsm], %[ob] offen sc1\n\t" \
          "Lo_f4_%=:\n\t" \
          "buffer_load_dwordx4 v[208:211], %[oo4], %[rsm], %[ob] offen sc1\n\t" \
          "Lo_f5_%=:\n\t" \
          "buffer_load_dwordx4 v[212:215], %[oo5], %[rsm], %[ob] offen sc1\n\t" \
          "Lo_f6_%=:\n\t" \
          "buffer_load_dwordx4 v[216:219], %[oo6], %[rsm], %[ob] offen sc1\n\t" \
          "Lo_f7_%=:\n\t" \
          "buffer_load_dwordx4 v[220:223], %[oo7], %[rsm], %[ob] offen sc1\n\t" \
          "v_mov_b32 v242, 0x10000\n\t" \
          "s_mov_b64 exec, %[alx]\n\t" \
          "buffer_store_dword v242, %[l4], %[rsm], %[als] offen sc1\n\t" \
          "s_mov_b64 exec, -1\n\t" \
          "v_mov_b32 v243, %[s1]\n\t" \
          "ds_write_b32 %[fa], v243\n\t" \
          "v_mov_b64 v[224:225], 0\n\t" \
          "v_mov_b64 v[226:227], 0\n\t" \
          "v_mov_b64 v[228:229], 0\n\t" \
          "v_mov_b64 v[230:231], 0\n\t" \
          "v_mov_b64 v[232:233], 0\n\t" \
          "v_mov_b64 v[234:235], 0\n\t" \
          "v_mov_b64 v[236:237], 0\n\t" \
          "v_mov_b64 v[238:239], 0\n\t" \
          "s_nop 1\n\t" \
          "s_mov_b32 %[cnt], 0\n\t" \
          "s_waitcnt vmcnt(7)\n\t" \
          "v_max3_u32 v240, v192, v193, v194\n\t" \
          "v_max_u32 v240, v240, v195\n\t" \
          "v_cmp_eq_u32 vcc, -1, v240\n\t" \
          "s_cbranch_vccnz Lo_p0_%=\n\t" \
          "Lo_k0_%=:\n\t" \
          "v_smfmac_f32_16x16x64_bf16 v[224:227], v[192:195], a[0:7], %[idx]\n\t" \
          "v_smfmac_f32_16x16x64_bf16 v[228:231], v[192:195], a[8:15], %[idx]\n\t" \
          "v_smfmac_f32_16x16x64_bf16 v[232:235], v[192:195], a[16:23], %[idx]\n\t" \
          "v_smfmac_f32_16x16x64_bf16 v[236:239], v[192:195], a[24:31], %[idx]\n\t" \
          "s_waitcnt vmcnt(6)\n\t" \
          "v_max3_u32 v240, v196, v197, v198\n\t" \
          "v_max_u32 v240, v240, v199\n\t" \
          "v_cmp_eq_u32 vcc, -1, v240\n\t" \
          "s_cbranch_vccnz Lo_p1_%=\n\t" \
          "Lo_k1_%=:\n\t" \
          "v_smfmac_f32_16x16x64_bf16 v[224:227], v[196:199], a[32:39], %[idx]\n\t" \
          "v_smfmac_f32_16x16x64_bf16 v[228:231], v[196:199], a[40:47], %[idx]\n\t" \
          "v_smfmac_f32_16x16x64_bf16 v[232:235], v[196:199], a[48:55], %[idx]\n\t" \
          "v_smfmac_f32_16x16x64_bf16 v[236:239], v[196:199], a[56:63], %[idx]\n\t" \
          "s_waitcnt vmcnt(5)\n\t" \
          "v_max3_u32 v240, v200, v201, v202\n\t" \
          "v_max_u32 v240, v240, v203\n\t" \
          "v_cmp_eq_u32 vcc, -1, v240\n\t" \
          "s_cbranch_vccnz Lo_p2_%=\n\t" \
          "Lo_k2_%=:\n\t" \
          "v_smfmac_f32_16x16x64_bf16 v[224:227], v[200:203], a[64:71], %[idx]\n\t" \
          "v_smfmac_f32_16x16x64_bf16 v[228:231], v[200:203], a[72:79], %[idx]\n\t" \
          "v_smfmac_f32_16x16x64_bf16 v[232:235], v[200:203], a[80:87], %[idx]\n\t" \
          "v_smfmac_f32_16x16x64_bf16 v[236:239], v[200:203], a[88:95], %[idx]\n\t" \
          "s_waitcnt vmcnt(4)\n\t" \
          "v_max3_u32 v240, v204, v205, v206\n\t" \
          "v_max_u32 v240, v240, v207\n\t" \
          "v_cmp_eq_u32 vcc, -1, v240\n\t" \
          "s_cbranch_vccnz Lo_p3_%=\n\t" \
          "Lo_k3_%=:\n\t" \
          "v_smfmac_f32_16x16x64_bf16 v[224:227], v[204:207], a[96:103], %[idx]\n\t" \
          "v_smfmac_f32_16x16x64_bf16 v[228:231], v[204:207], a[104:111], %[idx]\n\t" \
          "v_smfmac_f32_16x16x64_bf16 v[232:235], v[204:207], a[112:119], %[idx]\n\t" \
          "v_smfmac_f32_16x16x64_bf16 v[236:239], v[204:207], a[120:127], %[idx]\n\t" \
          "s_waitcnt vmcnt(3)\n\t" \
          "v_max3_u32 v240, v208, v209, v210\n\t" \
          "v_max_u32 v240, v240, v211\n\t" \
          "v_cmp_eq_u32 vcc, -1, v240\n\t" \
          "s_cbranch_vccnz Lo_p4_%=\n\t" \
          "Lo_k4_%=:\n\t" \
          "v_smfmac_f32_16x16x64_bf16 v[224:227], v[208:211], a[128:135], %[idx]\n\t" \
          "v_smfmac_f32_16x16x64_bf16 v[228:231], v[208:211], a[136:143], %[idx]\n\t" \
          "v_smfmac_f32_16x16x64_bf16 v[232:235], v[208:211], a[144:151], %[idx]\n\t" \
          "v_smfmac_f32_16x16x64_bf16 v[236:239], v[208:211], a[152:159], %[idx]\n\t" \
          "s_waitcnt vmcnt(2)\n\t" \
          "v_max3_u32 v240, v212, v213, v214\n\t" \
          "v_max_u32 v240, v240, v215\n\t" \
          "v_cmp_eq_u32 vcc, -1, v240\n\t" \
          "s_cbranch_vccnz Lo_p5_%=\n\t" \
          "Lo_k5_%=:\n\t" \
          "v_smfmac_f32_16x16x64_bf16 v[224:227], v[212:215], a[160:167], %[idx]\n\t" \
          "v_smfmac_f32_16x16x64_bf16 v[228:231], v[212:215], a[168:175], %[idx]\n\t" \
          "v_smfmac_f32_16x16x64_bf16 v[232:235], v[212:215], a[176:183], %[idx]\n\t" \
          "v_smfmac_f32_16x16x64_bf16 v[236:239], v[212:215], a[184:191], %[idx]\n\t" \
          "s_waitcnt vmcnt(1)\n\t" \
          "v_max3_u32 v240, v216, v217, v218\n\t" \
          "v_max_u32 v240, v240, v219\n\t" \
          "v_cmp_eq_u32 vcc, -1, v240\n\t" \
          "s_cbranch_vccnz Lo_p6_%=\n\t" \
          "Lo_k6_%=:\n\t" \
          "v_smfmac_f32_16x16x64_bf16 v[224:227], v[216:219], a[192:199], %[idx]\n\t" \
          "v_smfmac_f32_16x16x64_bf16 v[228:231], v[216:219], a[200:207], %[idx]\n\t" \
          "v_smfmac_f32_16x16x64_bf16 v[232:235], v[216:219], a[208:215], %[idx]\n\t" \
          "v_smfmac_f32_16x16x64_bf16 v[236:239], v[216:219], a[216:223], %[idx]\n\t" \
          "s_waitcnt vmcnt(0)\n\t" \
          "v_max3_u32 v240, v220, v221, v222\n\t" \
          "v_max_u32 v240, v240, v223\n\t" \
          "v_cmp_eq_u32 vcc, -1, v240\n\t" \
          "s_cbranch_vccnz Lo_p7_%=\n\t" \
          "Lo_k7_%=:\n\t" \
          "v_smfmac_f32_16x16x64_bf16 v[224:227], v[220:223], a[224:231], %[idx]\n\t" \
          "v_smfmac_f32_16x16x64_bf16 v[228:231], v[220:223], a[232:239], %[idx]\n\t" \
          "v_smfmac_f32_16x16x64_bf16 v[232:235], v[220:223], a[240:247], %[idx]\n\t" \
          "v_smfmac_f32_16x16x64_bf16 v[236:239], v[220:223], a[248:255], %[idx]\n\t" \
          "s_nop 7\n\t" \
          "v_add_f32 v242, v224, v225\n\t" \
          "v_add_f32 v244, v234, v235\n\t" \
          "s_cmp_lg_u32 %[w], 0\n\t" \
          "s_waitcnt lgkmcnt(0)\n\t" \
          "s_branch Lo_tail%=\n\t" \
          "Lo_p0_%=:\n\t" \
          "s_cmp_lt_i32 %[cnt], 0\n\t" \
          "s_cbranch_scc1 Lo_k0_%=\n\t" \
          "s_add_u32 %[cnt], %[cnt], 1\n\t" \
          "s_and_b32 vcc_lo, %[cnt], 3\n\t" \
          "s_cbranch_scc1 Lo_q0_%=\n\t" \
          "s_getpc_b64 s[48:49]\n\t" \
          "s_branch Lo_spin%=\n\t" \
          "Lo_q0_%=:\n\t" \
          "buffer_load_dwordx4 v[192:195], %[oo0], %[rsm], %[ob] offen sc1\n\t" \
          "s_waitcnt vmcnt(0)\n\t" \
          "v_max3_u32 v240, v192, v193, v194\n\t" \
          "v_max_u32 v240, v240, v195\n\t" \
          "v_cmp_eq_u32 vcc, -1, v240\n\t" \
          "s_cbranch_vccnz Lo_p0_%=\n\t" \
          "buffer_load_dwordx4 v[196:199], %[oo1], %[rsm], %[ob] offen sc1\n\t" \
          "buffer_load_dwordx4 v[200:203], %[oo2], %[rsm], %[ob] offen sc1\n\t" \
          "buffer_load_dwordx4 v[204:207], %[oo3], %[rsm], %[ob] offen sc1\n\t" \
          "buffer_load_dwordx4 v[208:211], %[oo4], %[rsm], %[ob] offen sc1\n\t" \
          "buffer_load_dwordx4 v[212:215], %[oo5], %[rsm], %[ob] offen sc1\n\t" \
          "buffer_load_dwordx4 v[216:219], %[oo6], %[rsm], %[ob] offen sc1\n\t" \
          "buffer_load_dwordx4 v[220:223], %[oo7], %[rsm], %[ob] offen sc1\n\t" \
          "s_branch Lo_k0_%=\n\t" \
          "Lo_p1_%=:\n\t" \
          "s_cmp_lt_i32 %[cnt], 0\n\t" \
          "s_cbranch_scc1 Lo_k1_%=\n\t" \
          "s_add_u32 %[cnt], %[cnt], 1\n\t" \
          "s_and_b32 vcc_lo, %[cnt], 3\n\t" \
          "s_cbranch_scc1 Lo_q1_%=\n\t" \
          "s_getpc_b64 s[48:49]\n\t" \
          "s_branch Lo_spin%=\n\t" \
          "Lo_q1_%=:\n\t" \
          "buffer_load_dwordx4 v[196:199], %[oo1], %[rsm], %[ob] offen sc1\n\t" \
          "s_waitcnt vmcnt(0)\n\t" \
          "v_max3_u32 v240, v196, v197, v198\n\t" \
          "v_max_u32 v240, v240, v199\n\t" \
          "v_cmp_eq_u32 vcc, -1, v240\n\t" \
          "s_cbranch_vccnz Lo_p1_%=\n\t" \
          "buffer_load_dwordx4 v[200:203], %[oo2], %[rsm], %[ob] offen sc1\n\t" \
          "buffer_load_dwordx4 v[204:207], %[oo3], %[rsm], %[ob] offen sc1\n\t" \
          "buffer_load_dwordx4 v[208:211], %[oo4], %[rsm], %[ob] offen sc1\n\t" \
          "buffer_load_dwordx4 v[212:215], %[oo5], %[rsm], %[ob] offen sc1\n\t" \
          "buffer_load_dwordx4 v[216:219], %[oo6], %[rsm], %[ob] offen sc1\n\t" \
          "buffer_load_dwordx4 v[220:223], %[oo7], %[rsm], %[ob] offen sc1\n\t" \
          "s_branch Lo_k1_%=\n\t" \
          "Lo_p2_%=:\n\t" \
          "s_cmp_lt_i32 %[cnt], 0\n\t" \
          "s_cbranch_scc1 Lo_k2_%=\n\t" \
          "s_add_u32 %[cnt], %[cnt], 1\n\t" \
          "s_and_b32 vcc_lo, %[cnt], 3\n\t" \
          "s_cbranch_scc1 Lo_q2_%=\n\t" \
          "s_getpc_b64 s[48:49]\n\t" \
          "s_branch Lo_spin%=\n\t" \
          "Lo_q2_%=:\n\t" \
          "buffer_load_dwordx4 v[200:203], %[oo2], %[rsm], %[ob] offen sc1\n\t" \
          "s_waitcnt vmcnt(0)\n\t" \
          "v_max3_u32 v240, v200, v201, v202\n\t" \
          "v_max_u32 v240, v240, v203\n\t" \
          "v_cmp_eq_u32 vcc, -1, v240\n\t" \
          "s_cbranch_vccnz Lo_p2_%=\n\t" \
          "buffer_load_dwordx4 v[204:207], %[oo3], %[rsm], %[ob] offen sc1\n\t" \
          "buffer_load_dwordx4 v[208:211], %[oo4], %[rsm], %[ob] offen sc1\n\t" \
          "buffer_load_dwordx4 v[212:215], %[oo5], %[rsm], %[ob] offen sc1\n\t" \
          "buffer_load_dwordx4 v[216:219], %[oo6], %[rsm], %[ob] offen sc1\n\t" \
          "buffer_load_dwordx4 v[220:223], %[oo7], %[rsm], %[ob] offen sc1\n\t" \
          "s_branch Lo_k2_%=\n\t" \
          "Lo_p3_%=:\n\t" \
          "s_cmp_lt_i32 %[cnt], 0\n\t" \
          "s_cbranch_scc1 Lo_k3_%=\n\t" \
          "s_add_u32 %[cnt], %[cnt], 1\n\t" \
          "s_and_b32 vcc_lo, %[cnt], 3\n\t" \
          "s_cbranch_scc1 Lo_q3_%=\n\t" \
          "s_getpc_b64 s[48:49]\n\t" \
          "s_branch Lo_spin%=\n\t" \
          "Lo_q3_%=:\n\t" \
          "buffer_load_dwordx4 v[204:207], %[oo3], %[rsm], %[ob] offen sc1\n\t" \
          "s_waitcnt vmcnt(0)\n\t" \
          "v_max3_u32 v240, v204, v205, v206\n\t" \
          "v_max_u32 v240, v240, v207\n\t" \
          "v_cmp_eq_u32 vcc, -1, v240\n\t" \
          "s_cbranch_vccnz Lo_p3_%=\n\t" \
          "buffer_load_dwordx4 v[208:211], %[oo4], %[rsm], %[ob] offen sc1\n\t" \
          "buffer_load_dwordx4 v[212:215], %[oo5], %[rsm], %[ob] offen sc1\n\t" \
          "buffer_load_dwordx4 v[216:219], %[oo6], %[rsm], %[ob] offen sc1\n\t" \
          "buffer_load_dwordx4 v[220:223], %[oo7], %[rsm], %[ob] offen sc1\n\t" \
          "s_branch Lo_k3_%=\n\t" \
          "Lo_p4_%=:\n\t" \
          "s_cmp_lt_i32 %[cnt], 0\n\t" \
          "s_cbranch_scc1 Lo_k4_%=\n\t" \
          "s_add_u32 %[cnt], %[cnt], 1\n\t" \
          "s_and_b32 vcc_lo, %[cnt], 3\n\t" \
          "s_cbranch_scc1 Lo_q4_%=\n\t" \
          "s_getpc_b64 s[48:49]\n\t" \
          "s_branch Lo_spin%=\n\t" \
          "Lo_q4_%=:\n\t" \
          "buffer_load_dwordx4 v[208:211], %[oo4], %[rsm], %[ob] offen sc1\n\t" \
          "s_waitcnt vmcnt(0)\n\t" \
          "v_max3_u32 v240, v208, v209, v210\n\t" \
          "v_max_u32 v240, v240, v211\n\t" \
          "v_cmp_eq_u32 vcc, -1, v240\n\t" \
          "s_cbranch_vccnz Lo_p4_%=\n\t" \
          "buffer_load_dwordx4 v[212:215], %[oo5], %[rsm], %[ob] offen sc1\n\t" \
          "buffer_load_dwordx4 v[216:219], %[oo6], %[rsm], %[ob] offen sc1\n\t" \
          "buffer_load_dwordx4 v[220:223], %[oo7], %[rsm], %[ob] offen sc1\n\t" \
          "s_branch Lo_k4_%=\n\t" \
          "Lo_p5_%=:\n\t" \
          "s_cmp_lt_i32 %[cnt], 0\n\t" \
          "s_cbranch_scc1 Lo_k5_%=\n\t" \
          "s_add_u32 %[cnt], %[cnt], 1\n\t" \
          "s_and_b32 vcc_lo, %[cnt], 3\n\t" \
          "s_cbranch_scc1 Lo_q5_%=\n\t" \
          "s_getpc_b64 s[48:49]\n\t" \
          "s_branch Lo_spin%=\n\t" \
          "Lo_q5_%=:\n\t" \
          "buffer_load_dwordx4 v[212:215], %[oo5], %[rsm], %[ob] offen sc1\n\t" \
          "s_waitcnt vmcnt(0)\n\t" \
          "v_max3_u32 v240, v212, v213, v214\n\t" \
          "v_max_u32 v240, v240, v215\n\t" \
          "v_cmp_eq_u32 vcc, -1, v240\n\t" \
          "s_cbranch_vccnz Lo_p5_%=\n\t" \
          "buffer_load_dwordx4 v[216:219], %[oo6], %[rsm], %[ob] offen sc1\n\t" \
          "buffer_load_dwordx4 v[220:223], %[oo7], %[rsm], %[ob] offen sc1\n\t" \
          "s_branch Lo_k5_%=\n\t" \
          "Lo_p6_%=:\n\t" \
          "s_cmp_lt_i32 %[cnt], 0\n\t" \
          "s_cbranch_scc1 Lo_k6_%=\n\t" \
          "s_add_u32 %[cnt], %[cnt], 1\n\t" \
          "s_and_b32 vcc_lo, %[cnt], 3\n\t" \
          "s_cbranch_scc1 Lo_q6_%=\n\t" \
          "s_getpc_b64 s[48:49]\n\t" \
          "s_branch Lo_spin%=\n\t" \
          "Lo_q6_%=:\n\t" \
          "buffer_load_dwordx4 v[216:219], %[oo6], %[rsm], %[ob] offen sc1\n\t" \
          "s_waitcnt vmcnt(0)\n\t" \
          "v_max3_u32 v240, v216, v217, v218\n\t" \
          "v_max_u32 v240, v240, v219\n\t" \
          "v_cmp_eq_u32 vcc, -1, v240\n\t" \
          "s_cbranch_vccnz Lo_p6_%=\n\t" \
          "buffer_load_dwordx4 v[220:223], %[oo7], %[rsm], %[ob] offen sc1\n\t" \
          "s_branch Lo_k6_%=\n\t" \
          "Lo_p7_%=:\n\t" \
          "s_cmp_lt_i32 %[cnt], 0\n\t" \
          "s_cbranch_scc1 Lo_k7_%=\n\t" \
          "s_add_u32 %[cnt], %[cnt], 1\n\t" \
          "s_and_b32 vcc_lo, %[cnt], 3\n\t" \
          "s_cbranch_scc1 Lo_q7_%=\n\t" \
          "s_getpc_b64 s[48:49]\n\t" \
          "s_branch Lo_spin%=\n\t" \
          "Lo_q7_%=:\n\t" \
          "buffer_load_dwordx4 v[220:223], %[oo7], %[rsm], %[ob] offen sc1\n\t" \
          "s_waitcnt vmcnt(0)\n\t" \
          "v_max3_u32 v240, v220, v221, v222\n\t" \
          "v_max_u32 v240, v240, v223\n\t" \
          "v_cmp_eq_u32 vcc, -1, v240\n\t" \
          "s_cbranch_vccnz Lo_p7_%=\n\t" \
          "s_branch Lo_k7_%=\n\t" \
          "Lo_spin%=:\n\t" \
          "buffer_load_dword v241, off, %[rsc], %[tmo] sc1\n\t" \
          "s_add_u32 s48, s48, 4\n\t" \
          "s_addc_u32 s49, s49, 0\n\t" \
          "s_waitcnt vmcnt(0)\n\t" \
          "v_readfirstlane_b32 vcc_lo, v241\n\t" \
          "s_lshr_b32 vcc_hi, %[cnt], 16\n\t" \
          "s_or_b32 vcc_lo, vcc_lo, vcc_hi\n\t" \
          "s_cbranch_scc0 Lo_back%=\n\t" \
          "v_mov_b32 v241, 1\n\t" \
          "buffer_store_dword v241, off, %[rsc], %[tmo] sc1\n\t" \
          "s_mov_b32 %[cnt], -1\n\t" \
          "Lo_back%=:\n\t" \
          "s_setpc_b64 s[48:49]\n\t" \
          "Lo_fa%=:\n\t" \
          "s_add_u32 %[ta], %[pm], %[shf]\n\t" \
          "s_branch Lo_fb%=\n\t" \
          "Lo_ka%=:\n\t" \
          "v_add_u32 v252, %[ta], v252\n\t" \
          "v_min_u32 v252, -2, v252\n\t" \
          "s_mov_b64 exec, 1\n\t" \
          "buffer_store_dwordx2 v[252:253], off, %[rsm], %[rco] sc1\n\t" \
          "v_mov_b32 v248, %[pm]\n\t" \
          "v_mov_b32 v249, %[t]\n\t" \
          "v_mov_b32 v250, %[fw]\n\t" \
          "v_mov_b32 v251, %[ta]\n\t" \
          "ds_write_b128 %[fa], v[248:251] offset:144\n\t" \
          "s_mov_b64 exec, -1\n\t" \
          "s_branch Lo_kb%=\n\t" \
          "Lv_fail%=:\n\t" \
          "buffer_load_dwordx4 v[192:195], %[ga0], %[rsm], %[mbn] offen sc1\n\t" \
          "buffer_load_dwordx4 v[196:199], %[ga1], %[rsm], %[mbn] offen sc1\n\t" \
          "buffer_load_dwordx4 v[200:203], %[ga2], %[rsm], %[mbn] offen sc1\n\t" \
          "buffer_load_dwordx4 v[204:207], %[ga3], %[rsm], %[mbn] offen sc1\n\t" \
          "s_nop 3\n\t" \
          "v_mov_b64 v[176:177], 0\n\t" \
          "v_mov_b64 v[178:179], 0\n\t" \
          "v_mov_b64 v[180:181], 0\n\t" \
          "v_mov_b64 v[182:183], 0\n\t" \
          "v_mov_b64 v[184:185], 0\n\t" \
          "v_mov_b64 v[186:187], 0\n\t" \
          "v_mov_b64 v[188:189], 0\n\t" \
          "v_mov_b64 v[190:191], 0\n\t" \
          "s_mov_b64 %[en], 0\n\t" \
          "s_mov_b32 %[cnt], 0\n\t" \
          "s_waitcnt vmcnt(3)\n\t" \
          "v_max3_u32 v240, v192, v193, v194\n\t" \
          "v_max_u32 v240, v240, v195\n\t" \
          "v_cmp_eq_u32 vcc, -1, v240\n\t" \
          "s_cbranch_vccnz Lv_p0_%=\n\t" \
          "Lv_k0_%=:\n\t" \
          "s_waitcnt vmcnt(2)\n\t" \
          "v_max3_u32 v240, v196, v197, v198\n\t" \
          "v_max_u32 v240, v240, v199\n\t" \
          "v_cmp_eq_u32 vcc, -1, v240\n\t" \
          "s_cbranch_vccnz Lv_p1_%=\n\t" \
          "Lv_k1_%=:\n\t" \
          "s_waitcnt vmcnt(1)\n\t" \
          "v_max3_u32 v240, v200, v201, v202\n\t" \
          "v_max_u32 v240, v240, v203\n\t" \
          "v_cmp_eq_u32 vcc, -1, v240\n\t" \
          "s_cbranch_vccnz Lv_p2_%=\n\t" \
          "Lv_k2_%=:\n\t" \
          "s_waitcnt vmcnt(0)\n\t" \
          "v_max3_u32 v240, v204, v205, v206\n\t" \
          "v_max_u32 v240, v240, v207\n\t" \
          "v_cmp_eq_u32 vcc, -1, v240\n\t" \
          "s_cbranch_vccnz Lv_p3_%=\n\t" \
          "Lv_k3_%=:\n\t" \
          "s_branch Lv_go%=\n\t" \
          "Lv_p0_%=:\n\t" \
          "s_cmp_lt_i32 %[cnt], 0\n\t" \
          "s_cbranch_scc1 Lv_k0_%=\n\t" \
          "s_add_u32 %[cnt], %[cnt], 1\n\t" \
          "s_and_b32 vcc_lo, %[cnt], 3\n\t" \
          "s_cbranch_scc1 Lv_q0_%=\n\t" \
          "s_getpc_b64 s[48:49]\n\t" \
          "s_branch Lv_spin%=\n\t" \
          "Lv_q0_%=:\n\t" \
          "buffer_load_dwordx4 v[192:195], %[ga0], %[rsm], %[mbn] offen sc1\n\t" \
          "s_waitcnt vmcnt(0)\n\t" \
          "v_max3_u32 v240, v192, v193, v194\n\t" \
          "v_max_u32 v240, v240, v195\n\t" \
          "v_cmp_eq_u32 vcc, -1, v240\n\t" \
          "s_cbranch_vccnz Lv_p0_%=\n\t" \
          "buffer_load_dwordx4 v[196:199], %[ga1], %[rsm], %[mbn] offen sc1\n\t" \
          "buffer_load_dwordx4 v[200:203], %[ga2], %[rsm], %[mbn] offen sc1\n\t" \
          "buffer_load_dwordx4 v[204:207], %[ga3], %[rsm], %[mbn] offen sc1\n\t" \
          "s_branch Lv_k0_%=\n\t" \
          "Lv_p1_%=:\n\t" \
          "s_cmp_lt_i32 %[cnt], 0\n\t" \
          "s_cbranch_scc1 Lv_k1_%=\n\t" \
          "s_add_u32 %[cnt], %[cnt], 1\n\t" \
          "s_and_b32 vcc_lo, %[cnt], 3\n\t" \
          "s_cbranch_scc1 Lv_q1_%=\n\t" \
          "s_getpc_b64 s[48:49]\n\t" \
          "s_branch Lv_spin%=\n\t" \
          "Lv_q1_%=:\n\t" \
          "buffer_load_dwordx4 v[196:199], %[ga1], %[rsm], %[mbn] offen sc1\n\t" \
          "s_waitcnt vmcnt(0)\n\t" \
          "v_max3_u32 v240, v196, v197, v198\n\t" \
          "v_max_u32 v240, v240, v199\n\t" \
          "v_cmp_eq_u32 vcc, -1, v240\n\t" \
          "s_cbranch_vccnz Lv_p1_%=\n\t" \
          "buffer_load_dwordx4 v[200:203], %[ga2], %[rsm], %[mbn] offen sc1\n\t" \
          "buffer_load_dwordx4 v[204:207], %[ga3], %[rsm], %[mbn] offen sc1\n\t" \
          "s_branch Lv_k1_%=\n\t" \
          "Lv_p2_%=:\n\t" \
          "s_cmp_lt_i32 %[cnt], 0\n\t" \
          "s_cbranch_scc1 Lv_k2_%=\n\t" \
          "s_add_u32 %[cnt], %[cnt], 1\n\t" \
          "s_and_b32 vcc_lo, %[cnt], 3\n\t" \
          "s_cbranch_scc1 Lv_q2_%=\n\t" \
          "s_getpc_b64 s[48:49]\n\t" \
          "s_branch Lv_spin%=\n\t" \
          "Lv_q2_%=:\n\t" \
          "buffer_load_dwordx4 v[200:203], %[ga2], %[rsm], %[mbn] offen sc1\n\t" \
          "s_waitcnt vmcnt(0)\n\t" \
          "v_max3_u32 v240, v200, v201, v202\n\t" \
          "v_max_u32 v240, v240, v203\n\t" \
          "v_cmp_eq_u32 vcc, -1, v240\n\t" \
          "s_cbranch_vccnz Lv_p2_%=\n\t" \
          "buffer_load_dwordx4 v[204:207], %[ga3], %[rsm], %[mbn] offen sc1\n\t" \
          "s_branch Lv_k2_%=\n\t" \
          "Lv_p3_%=:\n\t" \
          "s_cmp_lt_i32 %[cnt], 0\n\t" \
          "s_cbranch_scc1 Lv_k3_%=\n\t" \
          "s_add_u32 %[cnt], %[cnt], 1\n\t" \
          "s_and_b32 vcc_lo, %[cnt], 3\n\t" \
          "s_cbranch_scc1 Lv_q3_%=\n\t" \
          "s_getpc_b64 s[48:49]\n\t" \
          "s_branch Lv_spin%=\n\t" \
          "Lv_q3_%=:\n\t" \
          "buffer_load_dwordx4 v[204:207], %[ga3], %[rsm], %[mbn] offen sc1\n\t" \
          "s_waitcnt vmcnt(0)\n\t" \
          "v_max3_u32 v240, v204, v205, v206\n\t" \
          "v_max_u32 v240, v240, v207\n\t" \
          "v_cmp_eq_u32 vcc, -1, v240\n\t" \
          "s_cbranch_vccnz Lv_p3_%=\n\t" \
          "s_branch Lv_k3_%=\n\t" \
          "Lv_spin%=:\n\t" \
          "buffer_load_dword v241, off, %[rsc], %[tmo] sc1\n\t" \
          "s_add_u32 s48, s48, 4\n\t" \
          "s_addc_u32 s49, s49, 0\n\t" \
          "s_waitcnt vmcnt(0)\n\t" \
          "v_readfirstlane_b32 vcc_lo, v241\n\t" \
          "s_lshr_b32 vcc_hi, %[cnt], 16\n\t" \
          "s_or_b32 vcc_lo, vcc_lo, vcc_hi\n\t" \
          "s_cbranch_scc0 Lv_back%=\n\t" \
          "v_mov_b32 v241, 1\n\t" \
          "buffer_store_dword v241, off, %[rsc], %[tmo] sc1\n\t" \
          "s_mov_b32 %[cnt], -1\n\t" \
          "Lv_back%=:\n\t" \
          "s_setpc_b64 s[48:49]\n\t" \
          ".p2align 6\n\t" \
          "s_nop 0\n\t" \
          "s_nop 0\n\t" \
          "s_nop 0\n\t" \
          "Lo_al%=:\n\t" \
          "v_subrev_u32 v252, %[t], v255\n\t" \
          "v_bfe_i32 v252, v252, 0, 16\n\t" \
          "v_cndmask_b32 v252, 0, v252, vcc\n\t" \
          "v_cmp_gt_u32 vcc, 1000, v252\n\t" \
          "s_nop 1\n\t" \
          "v_cndmask_b32 v252, 0, v252, vcc\n\t" \
          "s_nop 1\n\t" \
          "v_max_i32_dpp v252, v252, v252 row_ror:8 row_mask:0xf bank_mask:0xf\n\t" \
          "s_nop 1\n\t" \
          "v_max_i32_dpp v252, v252, v252 row_ror:4 row_mask:0xf bank_mask:0xf\n\t" \
          "s_nop 1\n\t" \
          "v_max_i32_dpp v252, v252, v252 row_ror:2 row_mask:0xf bank_mask:0xf\n\t" \
          "s_nop 1\n\t" \
          "v_max_i32_dpp v252, v252, v252 row_ror:1 row_mask:0xf bank_mask:0xf\n\t" \
          "s_nop 0\n\t" \
          "v_readfirstlane_b32 vcc_lo, v252\n\t" \
          "s_add_u32 %[t], %[t], vcc_lo\n\t" \
          "s_memrealtime vcc\n\t" \
          "s_waitcnt lgkmcnt(0)\n\t" \
          "s_sub_i32 vcc_lo, %[t], vcc_lo\n\t" \
          "s_cmp_gt_i32 vcc_lo, %[lk]\n\t" \
          "s_cbranch_scc0 Lo_w%=\n\t" \
          "Lo_s%=:\n\t" \
          "s_sleep 1\n\t" \
          "Lo_c%=:\n\t" \
          "s_memrealtime vcc\n\t" \
          "s_waitcnt lgkmcnt(0)\n\t" \
          "s_sub_i32 vcc_lo, %[t], vcc_lo\n\t" \
          "s_cmp_gt_i32 vcc_lo, %[lk]\n\t" \
          "s_cbranch_scc1 Lo_s%=\n\t" \
          "buffer_load_dword v255, %[al1], %[rsm], %[mb] offen sc1\n\t" \
          "v_mov_b64 v[224:225], 0\n\t" \
          "v_mov_b64 v[226:227], 0\n\t" \
          "v_mov_b64 v[228:229], 0\n\t" \
          "v_mov_b64 v[230:231], 0\n\t" \
          "v_mov_b64 v[232:233], 0\n\t" \
          "v_mov_b64 v[234:235], 0\n\t" \
          "v_mov_b64 v[236:237], 0\n\t" \
          "v_mov_b64 v[238:239], 0\n\t" \
          "v_mov_b64 v[176:177], 0\n\t" \
          "v_mov_b64 v[178:179], 0\n\t" \
          "v_mov_b64 v[180:181], 0\n\t" \
          "v_mov_b64 v[182:183], 0\n\t" \
          "v_mov_b64 v[184:185], 0\n\t" \
          "v_mov_b64 v[186:187], 0\n\t" \
          "v_mov_b64 v[188:189], 0\n\t" \
          "v_mov_b64 v[190:191], 0\n\t" \
          "s_waitcnt vmcnt(0)\n\t" \
          "v_lshrrev_b32 v252, 16, v255\n\t" \
          "v_cmp_eq_u32 vcc, 1, v252\n\t" \
          "s_cbranch_vccnz Lo_al%=\n\t" \
          "Lo_w%=:\n\t" \
          "s_memrealtime s[50:51]\n\t" \
          "s_getpc_b64 s[48:49]\n\t" \
          "Lo_y%=:\n\t" \
          "s_add_u32 s48, s48, Lo_z%=-Lo_y%=\n\t" \
          "s_addc_u32 s49, s49, 0\n\t" \
          "Lo_v%=:\n\t" \
          "s_memrealtime s[52:53]\n\t" \
          "s_waitcnt lgkmcnt(1)\n\t" \
          "s_sub_i32 vcc_lo, %[t], s50\n\t" \
          "s_cmp_gt_i32 vcc_lo, 3\n\t" \
          "s_cbranch_scc0 Lo_x%=\n\t" \
          "s_memrealtime s[50:51]\n\t" \
          "s_waitcnt lgkmcnt(1)\n\t" \
          "s_sub_i32 vcc_lo, %[t], s52\n\t" \
          "s_cmp_gt_i32 vcc_lo, 3\n\t" \
          "s_cbranch_scc0 Lo_x%=\n\t" \
          "s_memrealtime s[52:53]\n\t" \
          "s_waitcnt lgkmcnt(1)\n\t" \
          "s_sub_i32 vcc_lo, %[t], s50\n\t" \
          "s_cmp_gt_i32 vcc_lo, 3\n\t" \
          "s_cbranch_scc0 Lo_x%=\n\t" \
          "s_memrealtime s[50:51]\n\t" \
          "s_waitcnt lgkmcnt(1)\n\t" \
          "s_sub_i32 vcc_lo, %[t], s52\n\t" \
          "s_cmp_gt_i32 vcc_lo, 3\n\t" \
          "s_cbranch_scc0 Lo_x%=\n\t" \
          "s_memrealtime s[52:53]\n\t" \
          "s_waitcnt lgkmcnt(1)\n\t" \
          "s_sub_i32 vcc_lo, %[t], s50\n\t" \
          "s_cmp_gt_i32 vcc_lo, 3\n\t" \
          "s_cbranch_scc0 Lo_x%=\n\t" \
          "s_memrealtime s[50:51]\n\t" \
          "s_waitcnt lgkmcnt(1)\n\t" \
          "s_sub_i32 vcc_lo, %[t], s52\n\t" \
          "s_cmp_gt_i32 vcc_lo, 3\n\t" \
          "s_cbranch_scc0 Lo_x%=\n\t" \
          "s_memrealtime s[52:53]\n\t" \
          "s_waitcnt lgkmcnt(1)\n\t" \
          "s_sub_i32 vcc_lo, %[t], s50\n\t" \
          "s_cmp_gt_i32 vcc_lo, 3\n\t" \
          "s_cbranch_scc0 Lo_x%=\n\t" \
          "s_memrealtime s[50:51]\n\t" \
          "s_waitcnt lgkmcnt(1)\n\t" \
          "s_sub_i32 vcc_lo, %[t], s52\n\t" \
          "s_cmp_gt_i32 vcc_lo, 3\n\t" \
          "s_cbranch_scc0 Lo_x%=\n\t" \
          "s_branch Lo_v%=\n\t" \
          "Lo_x%=:\n\t" \
          "s_max_i32 vcc_lo, vcc_lo, 0\n\t" \
          "s_lshl_b32 vcc_lo, vcc_lo, 2\n\t" \
          "s_sub_u32 s48, s48, vcc_lo\n\t" \
          "s_subb_u32 s49, s49, 0\n\t" \
          "s_setpc_b64 s[48:49]\n\t" \
          "s_nop 5\n\t" \
          "s_nop 5\n\t" \
          "s_nop 5\n\t" \
          "Lo_z%=:\n\t" \
          "s_barrier\n\t" \
          VO_ST0 \
          "buffer_load_dwordx4 v[192:195], %[oo0], %[rsm], %[ob] offen sc1\n\t" \
          "s_nop 9\n\t" \
          "buffer_load_dwordx4 v[196:199], %[oo1], %[rsm], %[ob] offen sc1\n\t" \
          "s_nop 9\n\t" \
          "buffer_load_dwordx4 v[200:203], %[oo2], %[rsm], %[ob] offen sc1\n\t" \
          "s_nop 9\n\t" \
          "buffer_load_dwordx4 v[204:207], %[oo3], %[rsm], %[ob] offen sc1\n\t" \
          "s_nop 9\n\t" \
          "buffer_load_dwordx4 v[208:211], %[oo4], %[rsm], %[ob] offen sc1\n\t" \
          "s_nop 9\n\t" \
          "buffer_load_dwordx4 v[212:215], %[oo5], %[rsm], %[ob] offen sc1\n\t" \
          "s_nop 9\n\t" \
          "buffer_load_dwordx4 v[216:219], %[oo6], %[rsm], %[ob] offen sc1\n\t" \
          "s_nop 9\n\t" \
          "buffer_load_dwordx4 v[220:223], %[oo7], %[rsm], %[ob] offen sc1\n\t" \
          "s_waitcnt vmcnt(7)\n\t" \
          "v_smfmac_f32_16x16x64_bf16 v[224:227], v[192:195], a[0:7], %[idx]\n\t" \
          "v_max3_u32 v240, v192, v193, v194\n\t" \
          "v_smfmac_f32_16x16x64_bf16 v[228:231], v[192:195], a[8:15], %[idx]\n\t" \
          "v_max3_u32 v240, v194, v195, v240\n\t" \
          "v_smfmac_f32_16x16x64_bf16 v[232:235], v[192:195], a[16:23], %[idx]\n\t" \
          "v_cmp_eq_u32 vcc, -1, v240\n\t" \
          "v_smfmac_f32_16x16x64_bf16 v[236:239], v[192:195], a[24:31], %[idx]\n\t" \
          "s_waitcnt vmcnt(6)\n\t" \
          "v_smfmac_f32_16x16x64_bf16 v[224:227], v[196:199], a[32:39], %[idx]\n\t" \
          "s_cbranch_vccnz Lo_f0_%=\n\t" \
          "v_max3_u32 v240, v196, v197, v198\n\t" \
          "v_smfmac_f32_16x16x64_bf16 v[228:231], v[196:199], a[40:47], %[idx]\n\t" \
          "v_max3_u32 v240, v198, v199, v240\n\t" \
          "v_smfmac_f32_16x16x64_bf16 v[232:235], v[196:199], a[48:55], %[idx]\n\t" \
          "v_cmp_eq_u32 vcc, -1, v240\n\t" \
          "v_smfmac_f32_16x16x64_bf16 v[236:239], v[196:199], a[56:63], %[idx]\n\t" \
          "s_waitcnt vmcnt(5)\n\t" \
          "v_smfmac_f32_16x16x64_bf16 v[224:227], v[200:203], a[64:71], %[idx]\n\t" \
          "s_cbranch_vccnz Lo_f1_%=\n\t" \
          "v_max3_u32 v240, v200, v201, v202\n\t" \
          "v_smfmac_f32_16x16x64_bf16 v[228:231], v[200:203], a[72:79], %[idx]\n\t" \
          "v_max3_u32 v240, v202, v203, v240\n\t" \
          "v_smfmac_f32_16x16x64_bf16 v[232:235], v[200:203], a[80:87], %[idx]\n\t" \
          "v_cmp_eq_u32 vcc, -1, v240\n\t" \
          "v_smfmac_f32_16x16x64_bf16 v[236:239], v[200:203], a[88:95], %[idx]\n\t" \
          "s_waitcnt vmcnt(4)\n\t" \
          "v_smfmac_f32_16x16x64_bf16 v[224:227], v[204:207], a[96:103], %[idx]\n\t" \
          "s_cbranch_vccnz Lo_f2_%=\n\t" \
          "v_max3_u32 v240, v204, v205, v206\n\t" \
          "v_smfmac_f32_16x16x64_bf16 v[228:231], v[204:207], a[104:111], %[idx]\n\t" \
          "v_max3_u32 v240, v206, v207, v240\n\t" \
          "v_smfmac_f32_16x16x64_bf16 v[232:235], v[204:207], a[112:119], %[idx]\n\t" \
          "v_cmp_eq_u32 vcc, -1, v240\n\t" \
          "v_smfmac_f32_16x16x64_bf16 v[236:239], v[204:207], a[120:127], %[idx]\n\t" \
          "s_waitcnt vmcnt(3)\n\t" \
          "v_smfmac_f32_16x16x64_bf16 v[224:227], v[208:211], a[128:135], %[idx]\n\t" \
          "s_cbranch_vccnz Lo_f3_%=\n\t" \
          "v_max3_u32 v240, v208, v209, v210\n\t" \
          "v_smfmac_f32_16x16x64_bf16 v[228:231], v[208:211], a[136:143], %[idx]\n\t" \
          "v_max3_u32 v240, v210, v211, v240\n\t" \
          "v_smfmac_f32_16x16x64_bf16 v[232:235], v[208:211], a[144:151], %[idx]\n\t" \
          "v_cmp_eq_u32 vcc, -1, v240\n\t" \
          "v_smfmac_f32_16x16x64_bf16 v[236:239], v[208:211], a[152:159], %[idx]\n\t" \
          "s_waitcnt vmcnt(2)\n\t" \
          "v_smfmac_f32_16x16x64_bf16 v[224:227], v[212:215], a[160:167], %[idx]\n\t" \
          "s_cbranch_vccnz Lo_f4_%=\n\t" \
          "v_max3_u32 v240, v212, v213, v214\n\t" \
          "v_smfmac_f32_16x16x64_bf16 v[228:231], v[212:215], a[168:175], %[idx]\n\t" \
          "v_max3_u32 v240, v214, v215, v240\n\t" \
          "v_smfmac_f32_16x16x64_bf16 v[232:235], v[212:215], a[176:183], %[idx]\n\t" \
          "v_cmp_eq_u32 vcc, -1, v240\n\t" \
          "v_smfmac_f32_16x16x64_bf16 v[236:239], v[212:215], a[184:191], %[idx]\n\t" \
          "s_waitcnt vmcnt(1)\n\t" \
          "v_smfmac_f32_16x16x64_bf16 v[224:227], v[216:219], a[192:199], %[idx]\n\t" \
          "s_cbranch_vccnz Lo_f5_%=\n\t" \
          "v_max3_u32 v240, v216, v217, v218\n\t" \
          "v_smfmac_f32_16x16x64_bf16 v[228:231], v[216:219], a[200:207], %[idx]\n\t" \
          "v_max3_u32 v240, v218, v219, v240\n\t" \
          "v_smfmac_f32_16x16x64_bf16 v[232:235], v[216:219], a[208:215], %[idx]\n\t" \
          "v_cmp_eq_u32 vcc, -1, v240\n\t" \
          "v_smfmac_f32_16x16x64_bf16 v[236:239], v[216:219], a[216:223], %[idx]\n\t" \
          "s_waitcnt vmcnt(0)\n\t" \
          "v_smfmac_f32_16x16x64_bf16 v[224:227], v[220:223], a[224:231], %[idx]\n\t" \
          "s_cbranch_vccnz Lo_f6_%=\n\t" \
          "v_max3_u32 v240, v220, v221, v222\n\t" \
          "v_smfmac_f32_16x16x64_bf16 v[232:235], v[220:223], a[240:247], %[idx]\n\t" \
          "v_max3_u32 v240, v222, v223, v240\n\t" \
          "v_smfmac_f32_16x16x64_bf16 v[228:231], v[220:223], a[232:239], %[idx]\n\t" \
          "v_cmp_eq_u32 vcc, -1, v240\n\t" \
          "v_smfmac_f32_16x16x64_bf16 v[236:239], v[220:223], a[248:255], %[idx]\n\t" \
          VO_ST1 \
          "s_cmp_lg_u32 %[w], 0\n\t" \
          "v_add_f32 v242, v224, v225\n\t" \
          "v_add_f32 v244, v234, v235\n\t" \
          "s_cbranch_vccnz Lo_f7_%=\n\t" \
          "Lo_tail%=:\n\t" \
          "v_add_f32 v243, v228, v229\n\t" \
          "v_add_f32 v242, v242, v244\n\t" \
          "s_nop 0\n\t" \
          "v_add_f32 v243, v243, v238\n\t" \
          "s_cbranch_scc0 Lo_w0%=\n\t" \
          "v_add_f32 v243, v243, v239\n\t" \
          "ds_write_b64 %[rw], v[242:243]\n\t" \
          "s_waitcnt lgkmcnt(0)\n\t" \
          "s_barrier\n\t" \
          "s_cmp_eq_u32 %[w], 3\n\t" \
          "s_cbranch_scc0 Lv_z%=\n\t" \
          "s_sleep 1\n\t" \
          "s_branch Lo_end%=\n\t" \
          "Lo_w0%=:\n\t" \
          "v_add_f32 v243, v243, v239\n\t" \
          "s_barrier\n\t" \
          "ds_read_b64 v[244:245], %[rw] offset:512\n\t" \
          "ds_read_b64 v[246:247], %[rw] offset:1024\n\t" \
          "ds_read_b64 v[248:249], %[rw] offset:1536\n\t" \
          "s_waitcnt lgkmcnt(2)\n\t" \
          "v_pk_add_f32 v[242:243], v[242:243], v[244:245]\n\t" \
          "s_waitcnt lgkmcnt(1)\n\t" \
          "v_pk_add_f32 v[242:243], v[242:243], v[246:247]\n\t" \
          "s_waitcnt lgkmcnt(0)\n\t" \
          "v_pk_add_f32 v[242:243], v[242:243], v[248:249]\n\t" \
          "s_nop 0\n\t" \
          "v_cvt_pk_bf16_f32 v242, v242, v243\n\t" \
          "v_min_u32 v242, -2, v242\n\t" \
          "buffer_store_dword v242, %[mo], %[rsm], %[mbn] offen sc1\n\t" \
          VO_ST3 \
          "s_branch Lv_z%=\n\t" \
          "Lo_end%=:\n\t" \
          VO_ST3 \
          "s_memrealtime vcc\n\t" \
          "ds_read_b32 v241, %[fa]\n\t" \
          "ds_read_b64 v[252:253], %[fa] offset:128\n\t" \
          "s_add_u32 %[ta], %[t], %[d2]\n\t" \
          "s_waitcnt lgkmcnt(0)\n\t" \
          "s_mov_b32 %[pm], vcc_lo\n\t" \
          "v_readfirstlane_b32 %[fw], v241\n\t" \
          "s_add_u32 vcc_lo, vcc_lo, %[mn]\n\t" \
          "s_sub_i32 vcc_hi, vcc_lo, %[ta]\n\t" \
          "s_cmp_gt_i32 vcc_hi, 0\n\t" \
          "s_cselect_b32 %[ta], vcc_lo, %[ta]\n\t" \
          "s_cmp_eq_u32 %[fw], %[s1]\n\t" \
          "s_cbranch_scc1 Lo_fa%=\n\t" \
          "Lo_fb%=:\n\t" \
          "s_cmp_eq_u32 %[c], 0\n\t" \
          "s_cbranch_scc1 Lo_ka%=\n\t" \
          "Lo_kb%=:\n\t" \
          "s_add_u32 %[cnt], %[ta], %[dv]\n\t" \
          "Lv_w%=:\n\t" \
          "s_memrealtime s[50:51]\n\t" \
          "s_getpc_b64 s[48:49]\n\t" \
          "Lv_y%=:\n\t" \
          "s_add_u32 s48, s48, Lv_z%=-Lv_y%=\n\t" \
          "s_addc_u32 s49, s49, 0\n\t" \
          "Lv_v%=:\n\t" \
          "s_memrealtime s[52:53]\n\t" \
          "s_waitcnt lgkmcnt(1)\n\t" \
          "s_sub_i32 vcc_lo, %[cnt], s50\n\t" \
          "s_cmp_gt_i32 vcc_lo, 3\n\t" \
          "s_cbranch_scc0 Lv_x%=\n\t" \
          "s_memrealtime s[50:51]\n\t" \
          "s_waitcnt lgkmcnt(1)\n\t" \
          "s_sub_i32 vcc_lo, %[cnt], s52\n\t" \
          "s_cmp_gt_i32 vcc_lo, 3\n\t" \
          "s_cbranch_scc0 Lv_x%=\n\t" \
          "s_memrealtime s[52:53]\n\t" \
          "s_waitcnt lgkmcnt(1)\n\t" \
          "s_sub_i32 vcc_lo, %[cnt], s50\n\t" \
          "s_cmp_gt_i32 vcc_lo, 3\n\t" \
          "s_cbranch_scc0 Lv_x%=\n\t" \
          "s_memrealtime s[50:51]\n\t" \
          "s_waitcnt lgkmcnt(1)\n\t" \
          "s_sub_i32 vcc_lo, %[cnt], s52\n\t" \
          "s_cmp_gt_i32 vcc_lo, 3\n\t" \
          "s_cbranch_scc0 Lv_x%=\n\t" \
          "s_memrealtime s[52:53]\n\t" \
          "s_waitcnt lgkmcnt(1)\n\t" \
          "s_sub_i32 vcc_lo, %[cnt], s50\n\t" \
          "s_cmp_gt_i32 vcc_lo, 3\n\t" \
          "s_cbranch_scc0 Lv_x%=\n\t" \
          "s_memrealtime s[50:51]\n\t" \
          "s_waitcnt lgkmcnt(1)\n\t" \
          "s_sub_i32 vcc_lo, %[cnt], s52\n\t" \
          "s_cmp_gt_i32 vcc_lo, 3\n\t" \
          "s_cbranch_scc0 Lv_x%=\n\t" \
          "s_memrealtime s[52:53]\n\t" \
          "s_waitcnt lgkmcnt(1)\n\t" \
          "s_sub_i32 vcc_lo, %[cnt], s50\n\t" \
          "s_cmp_gt_i32 vcc_lo, 3\n\t" \
          "s_cbranch_scc0 Lv_x%=\n\t" \
          "s_memrealtime s[50:51]\n\t" \
          "s_waitcnt lgkmcnt(1)\n\t" \
          "s_sub_i32 vcc_lo, %[cnt], s52\n\t" \
          "s_cmp_gt_i32 vcc_lo, 3\n\t" \
          "s_cbranch_scc0 Lv_x%=\n\t" \
          "s_branch Lv_v%=\n\t" \
          "Lv_x%=:\n\t" \
          "s_max_i32 vcc_lo, vcc_lo, 0\n\t" \
          "s_lshl_b32 vcc_lo, vcc_lo, 2\n\t" \
          "s_sub_u32 s48, s48, vcc_lo\n\t" \
          "s_subb_u32 s49, s49, 0\n\t" \
          "s_setpc_b64 s[48:49]\n\t" \
          "s_nop 5\n\t" \
          "s_nop 5\n\t" \
          "s_nop 5\n\t" \
          "Lv_z%=:\n\t" \
          "s_barrier\n\t" \
          "Lv_n%=:\n\t" \
          "buffer_load_dwordx4 v[192:195], %[ga0], %[rsm], %[mbn] offen sc1\n\t" \
          "s_nop 15\n\t" \
          "s_nop 5\n\t" \
          "buffer_load_dwordx4 v[196:199], %[ga1], %[rsm], %[mbn] offen sc1\n\t" \
          "s_nop 15\n\t" \
          "s_nop 5\n\t" \
          "buffer_load_dwordx4 v[200:203], %[ga2], %[rsm], %[mbn] offen sc1\n\t" \
          "s_nop 15\n\t" \
          "s_nop 5\n\t" \
          "buffer_load_dwordx4 v[204:207], %[ga3], %[rsm], %[mbn] offen sc1\n\t" \
          "buffer_load_dword %[al], %[alo], %[rsm], %[mb] offen sc1\n\t" \
          "Lv_go%=:\n\t" \
          "s_waitcnt vmcnt(4)\n\t" \
          "v_smfmac_f32_16x16x64_bf16 v[176:179], v[192:195], v[8:15], %[idx]\n\t" \
          "v_max3_u32 v240, v192, v193, v194\n\t" \
          "v_smfmac_f32_16x16x64_bf16 v[180:183], v[192:195], v[16:23], %[idx]\n\t" \
          "v_max3_u32 v240, v194, v195, v240\n\t" \
          "v_smfmac_f32_16x16x64_bf16 v[184:187], v[192:195], v[24:31], %[idx]\n\t" \
          "buffer_load_dwordx2 %[rec], off, %[rsm], %[rco] sc1\n\t" \
          "v_smfmac_f32_16x16x64_bf16 v[188:191], v[192:195], v[32:39], %[idx]\n\t" \
          "s_waitcnt vmcnt(4)\n\t" \
          "v_smfmac_f32_16x16x64_bf16 v[176:179], v[196:199], v[40:47], %[idx]\n\t" \
          "v_max3_u32 v240, v196, v197, v240\n\t" \
          "v_smfmac_f32_16x16x64_bf16 v[180:183], v[196:199], v[48:55], %[idx]\n\t" \
          "v_max3_u32 v240, v198, v199, v240\n\t" \
          "v_smfmac_f32_16x16x64_bf16 v[184:187], v[196:199], v[56:63], %[idx]\n\t" \
          "v_smfmac_f32_16x16x64_bf16 v[188:191], v[196:199], v[64:71], %[idx]\n\t" \
          "s_waitcnt vmcnt(3)\n\t" \
          "v_smfmac_f32_16x16x64_bf16 v[176:179], v[200:203], v[72:79], %[idx]\n\t" \
          "v_max3_u32 v240, v200, v201, v240\n\t" \
          "v_smfmac_f32_16x16x64_bf16 v[180:183], v[200:203], v[80:87], %[idx]\n\t" \
          "v_max3_u32 v240, v202, v203, v240\n\t" \
          "v_smfmac_f32_16x16x64_bf16 v[184:187], v[200:203], v[88:95], %[idx]\n\t" \
          "v_smfmac_f32_16x16x64_bf16 v[188:191], v[200:203], v[96:103], %[idx]\n\t" \
          "s_waitcnt vmcnt(2)\n\t" \
          "v_smfmac_f32_16x16x64_bf16 v[176:179], v[204:207], v[104:111], %[idx]\n\t" \
          "v_max3_u32 v240, v204, v205, v240\n\t" \
          "v_smfmac_f32_16x16x64_bf16 v[180:183], v[204:207], v[112:119], %[idx]\n\t" \
          "v_max3_u32 v240, v206, v207, v240\n\t" \
          "v_smfmac_f32_16x16x64_bf16 v[184:187], v[204:207], v[120:127], %[idx]\n\t" \
          "v_cmp_eq_u32 vcc, -1, v240\n\t" \
          "v_smfmac_f32_16x16x64_bf16 v[188:191], v[204:207], v[128:135], %[idx]\n\t" \
          "s_and_b64 vcc, vcc, %[en]\n\t" \
          "v_add_f32 v242, v176, v177\n\t" \
          "v_add_f32 v243, v178, v179\n\t" \
          "v_add_f32 v244, v180, v181\n\t" \
          "v_add_f32 v245, v182, v183\n\t" \
          "s_cbranch_scc1 Lv_fail%=\n\t" \
          "v_add_f32 v246, v184, v185\n\t" \
          "v_add_f32 v247, v186, v187\n\t" \
          "ds_write_b64 %[vw0], v[242:243]\n\t" \
          "ds_write_b64 %[vw1], v[244:245]\n\t" \
          "ds_write_b64 %[vw2], v[246:247]\n\t" \
          "v_add_f32 v248, v188, v189\n\t" \
          "v_add_f32 v249, v190, v191\n\t" \
          "v_mfma_f32_16x16x32_bf16 v[208:211], v[212:215], v[212:215], 0\n\t" \
          "v_mfma_f32_16x16x32_bf16 v[208:211], v[212:215], v[212:215], 0\n\t" \
          "s_waitcnt lgkmcnt(0)\n\t" \
          "s_barrier\n\t" \
          "ds_read_b64 v[242:243], %[vr] offset:0\n\t" \
          "v_mfma_f32_16x16x32_bf16 v[208:211], v[212:215], v[212:215], 0\n\t" \
          "ds_read_b64 v[244:245], %[vr] offset:2064\n\t" \
          "ds_read_b64 v[246:247], %[vr] offset:4128\n\t" \
          "v_mfma_f32_16x16x32_bf16 v[208:211], v[212:215], v[212:215], 0\n\t" \
          "s_waitcnt lgkmcnt(2)\n\t" \
          "v_add_f32 v248, v248, v242\n\t" \
          "v_add_f32 v249, v249, v243\n\t" \
          "s_waitcnt lgkmcnt(1)\n\t" \
          "v_add_f32 v248, v248, v244\n\t" \
          "v_add_f32 v249, v249, v245\n\t" \
          "s_waitcnt lgkmcnt(0)\n\t" \
          "v_add_f32 v248, v248, v246\n\t" \
          "v_add_f32 v249, v249, v247\n\t" \
          "v_cvt_pk_bf16_f32 v242, v248, v249\n\t" \
          "v_min_u32 v242, -2, v242\n\t" \
          "buffer_store_dword v242, %[vo], %[rsm], %[vb] offen sc1\n\t" \
          "s_memrealtime %[te]\n\t" \
          "v_mov_b64 v[252:253], -1\n\t" \
          "v_mov_b64 v[254:255], -1\n\t" \
          "s_sleep 1\n\t" \
          "buffer_store_dwordx4 v[252:255], %[rso], %[rsm], %[rb] offen sc1\n\t" \
          "s_waitcnt vmcnt(2) lgkmcnt(0)\n\t"
#define VO_W "{a[0:31]}"(wa[0]), "{a[32:63]}"(wa[1]), "{a[64:95]}"(wa[2]), "{a[96:127]}"(wa[3]), "{a[128:159]}"(wa[4]), "{a[160:191]}"(wa[5]), "{a[192:223]}"(wa[6]), "{a[224:255]}"(wa[7]), "{v[8:39]}"(wv_[0]), "{v[40:71]}"(wv_[1]), "{v[72:103]}"(wv_[2]), "{v[104:135]}"(wv_[3])
#define VO_DEFW "=&{a[0:31]}"(wa[0]), "=&{a[32:63]}"(wa[1]), "=&{a[64:95]}"(wa[2]), "=&{a[96:127]}"(wa[3]), "=&{a[128:159]}"(wa[4]), "=&{a[160:191]}"(wa[5]), "=&{a[192:223]}"(wa[6]), "=&{a[224:255]}"(wa[7]), "=&{v[8:39]}"(wv_[0]), "=&{v[40:71]}"(wv_[1]), "=&{v[72:103]}"(wv_[2]), "=&{v[104:135]}"(wv_[3])
#define VO_LOADER \
          "s_mov_b64 s[48:49], %[wo]\n\ts_mov_b32 s50, %[wsz]\n\ts_mov_b32 s51, 0x20000\n\ts_nop 4\n\ts_add_u32 m0, %[stg], 0\n\ts_xor_b32 %[t], %[vk], 0x0\n\t" \
          "buffer_load_dwordx4 %[sv], %[rv], %[t] offen lds\n\ts_add_u32 m0, %[stg], 1024\n\ts_xor_b32 %[t], %[vk], 0x10000\n\tbuffer_load_dwordx4 %[sv], %[rv], %[t] offen lds\n\ts_add_u32 m0, %[stg], 2048\n\ts_xor_b32 %[t], %[vk], 0x20000\n\t" \
          "buffer_load_dwordx4 %[sv], %[rv], %[t] offen lds\n\ts_add_u32 m0, %[stg], 3072\n\ts_xor_b32 %[t], %[vk], 0x30000\n\tbuffer_load_dwordx4 %[sv], %[rv], %[t] offen lds\n\ts_add_u32 m0, %[stg], 4096\n\ts_xor_b32 %[t], %[vk], 0x40000\n\t" \
          "buffer_load_dwordx4 %[sv], %[rv], %[t] offen lds\n\ts_add_u32 m0, %[stg], 5120\n\ts_xor_b32 %[t], %[vk], 0x50000\n\tbuffer_load_dwordx4 %[sv], %[rv], %[t] offen lds\n\ts_add_u32 m0, %[stg], 6144\n\ts_xor_b32 %[t], %[vk], 0x60000\n\t" \
          "buffer_load_dwordx4 %[sv], %[rv], %[t] offen lds\n\ts_add_u32 m0, %[stg], 7168\n\ts_xor_b32 %[t], %[vk], 0x70000\n\tbuffer_load_dwordx4 %[sv], %[rv], %[t] offen lds\n\ts_add_u32 m0, %[stg], 8192\n\ts_xor_b32 %[t], %[vk], 0x100000\n\t" \
          "buffer_load_dwordx4 %[sv], %[rv], %[t] offen lds\n\ts_add_u32 m0, %[stg], 9216\n\ts_xor_b32 %[t], %[vk], 0x110000\n\tbuffer_load_dwordx4 %[sv], %[rv], %[t] offen lds\n\ts_add_u32 m0, %[stg], 10240\n\ts_xor_b32 %[t], %[vk], 0x120000\n\t" \
          "buffer_load_dwordx4 %[sv], %[rv], %[t] offen lds\n\ts_add_u32 m0, %[stg], 11264\n\ts_xor_b32 %[t], %[vk], 0x130000\n\tbuffer_load_dwordx4 %[sv], %[rv], %[t] offen lds\n\ts_add_u32 m0, %[stg], 12288\n\ts_xor_b32 %[t], %[vk], 0x140000\n\t" \
          "buffer_load_dwordx4 %[sv], %[rv], %[t] offen lds\n\ts_add_u32 m0, %[stg], 13312\n\ts_xor_b32 %[t], %[vk], 0x150000\n\tbuffer_load_dwordx4 %[sv], %[rv], %[t] offen lds\n\ts_add_u32 m0, %[stg], 14336\n\ts_xor_b32 %[t], %[vk], 0x160000\n\t" \
          "buffer_load_dwordx4 %[sv], %[rv], %[t] offen lds\n\ts_add_u32 m0, %[stg], 15360\n\ts_xor_b32 %[t], %[vk], 0x170000\n\tbuffer_load_dwordx4 %[sv], %[rv], %[t] offen lds\n\ts_add_u32 m0, %[stg], 16384\n\ts_xor_b32 %[t], %[vk], 0x200000\n\t" \
          "buffer_load_dwordx4 %[sv], %[rv], %[t] offen lds\n\ts_add_u32 m0, %[stg], 17408\n\ts_xor_b32 %[t], %[vk], 0x210000\n\tbuffer_load_dwordx4 %[sv], %[rv], %[t] offen lds\n\ts_add_u32 m0, %[stg], 18432\n\ts_xor_b32 %[t], %[vk], 0x220000\n\t" \
          "buffer_load_dwordx4 %[sv], %[rv], %[t] offen lds\n\ts_add_u32 m0, %[stg], 19456\n\ts_xor_b32 %[t], %[vk], 0x230000\n\tbuffer_load_dwordx4 %[sv], %[rv], %[t] offen lds\n\ts_add_u32 m0, %[stg], 20480\n\ts_xor_b32 %[t], %[vk], 0x240000\n\t" \
          "buffer_load_dwordx4 %[sv], %[rv], %[t] offen lds\n\ts_add_u32 m0, %[stg], 21504\n\ts_xor_b32 %[t], %[vk], 0x250000\n\tbuffer_load_dwordx4 %[sv], %[rv], %[t] offen lds\n\ts_add_u32 m0, %[stg], 22528\n\ts_xor_b32 %[t], %[vk], 0x260000\n\t" \
          "buffer_load_dwordx4 %[sv], %[rv], %[t] offen lds\n\ts_add_u32 m0, %[stg], 23552\n\ts_xor_b32 %[t], %[vk], 0x270000\n\tbuffer_load_dwordx4 %[sv], %[rv], %[t] offen lds\n\ts_add_u32 m0, %[stg], 24576\n\ts_xor_b32 %[t], %[vk], 0x300000\n\t" \
          "buffer_load_dwordx4 %[sv], %[rv], %[t] offen lds\n\ts_add_u32 m0, %[stg], 25600\n\ts_xor_b32 %[t], %[vk], 0x310000\n\tbuffer_load_dwordx4 %[sv], %[rv], %[t] offen lds\n\ts_add_u32 m0, %[stg], 26624\n\ts_xor_b32 %[t], %[vk], 0x320000\n\t" \
          "buffer_load_dwordx4 %[sv], %[rv], %[t] offen lds\n\ts_add_u32 m0, %[stg], 27648\n\ts_xor_b32 %[t], %[vk], 0x330000\n\tbuffer_load_dwordx4 %[sv], %[rv], %[t] offen lds\n\ts_add_u32 m0, %[stg], 28672\n\ts_xor_b32 %[t], %[vk], 0x340000\n\t" \
          "buffer_load_dwordx4 %[sv], %[rv], %[t] offen lds\n\ts_add_u32 m0, %[stg], 29696\n\ts_xor_b32 %[t], %[vk], 0x350000\n\tbuffer_load_dwordx4 %[sv], %[rv], %[t] offen lds\n\ts_add_u32 m0, %[stg], 30720\n\ts_xor_b32 %[t], %[vk], 0x360000\n\t" \
          "buffer_load_dwordx4 %[sv], %[rv], %[t] offen lds\n\ts_add_u32 m0, %[stg], 31744\n\ts_xor_b32 %[t], %[vk], 0x370000\n\tbuffer_load_dwordx4 %[sv], %[rv], %[t] offen lds\n\ts_waitcnt vmcnt(16)\n\tds_read_b64_tr_b16 v[8:9], %[vb0] offset:0\n\t" \
          "ds_read_b64_tr_b16 v[10:11], %[vb0] offset:128\n\tds_read_b64_tr_b16 v[12:13], %[vb0] offset:4096\n\tds_read_b64_tr_b16 v[14:15], %[vb0] offset:4224\n\tds_read_b64_tr_b16 v[16:17], %[vb1] offset:0\n\tds_read_b64_tr_b16 v[18:19], %[vb1] offset:128\n\tds_read_b64_tr_b16 v[20:21], %[vb1] offset:4096\n\t" \
          "ds_read_b64_tr_b16 v[22:23], %[vb1] offset:4224\n\tds_read_b64_tr_b16 v[24:25], %[vb2] offset:0\n\tds_read_b64_tr_b16 v[26:27], %[vb2] offset:128\n\tds_read_b64_tr_b16 v[28:29], %[vb2] offset:4096\n\tds_read_b64_tr_b16 v[30:31], %[vb2] offset:4224\n\tds_read_b64_tr_b16 v[32:33], %[vb3] offset:0\n\t" \
          "ds_read_b64_tr_b16 v[34:35], %[vb3] offset:128\n\tds_read_b64_tr_b16 v[36:37], %[vb3] offset:4096\n\tds_read_b64_tr_b16 v[38:39], %[vb3] offset:4224\n\tds_read_b64_tr_b16 v[40:41], %[vb0] offset:8192\n\tds_read_b64_tr_b16 v[42:43], %[vb0] offset:8320\n\tds_read_b64_tr_b16 v[44:45], %[vb0] offset:12288\n\t" \
          "ds_read_b64_tr_b16 v[46:47], %[vb0] offset:12416\n\tds_read_b64_tr_b16 v[48:49], %[vb1] offset:8192\n\tds_read_b64_tr_b16 v[50:51], %[vb1] offset:8320\n\tds_read_b64_tr_b16 v[52:53], %[vb1] offset:12288\n\tds_read_b64_tr_b16 v[54:55], %[vb1] offset:12416\n\tds_read_b64_tr_b16 v[56:57], %[vb2] offset:8192\n\t" \
          "ds_read_b64_tr_b16 v[58:59], %[vb2] offset:8320\n\tds_read_b64_tr_b16 v[60:61], %[vb2] offset:12288\n\tds_read_b64_tr_b16 v[62:63], %[vb2] offset:12416\n\tds_read_b64_tr_b16 v[64:65], %[vb3] offset:8192\n\tds_read_b64_tr_b16 v[66:67], %[vb3] offset:8320\n\tds_read_b64_tr_b16 v[68:69], %[vb3] offset:12288\n\t" \
          "ds_read_b64_tr_b16 v[70:71], %[vb3] offset:12416\n\ts_waitcnt lgkmcnt(0)\n\ts_add_u32 %[ho], %[k0], 1\n\ts_and_b32 %[ho], %[ho], 2\n\ts_or_b32 %[ho], %[ho], 0\n\ts_lshl_b32 %[ho], %[ho], 20\n\t" \
          "s_xor_b32 %[ho], %[ho], %[hx]\n\ts_add_u32 m0, %[stg], 0\n\ts_add_u32 %[t], %[ho], 0\n\tbuffer_load_dwordx4 %[so], s[48:51], %[t] offen lds\n\ts_add_u32 m0, %[stg], 1024\n\ts_add_u32 %[t], %[ho], 16384\n\t" \
          "buffer_load_dwordx4 %[so], s[48:51], %[t] offen lds\n\ts_add_u32 m0, %[stg], 2048\n\ts_add_u32 %[t], %[ho], 32768\n\tbuffer_load_dwordx4 %[so], s[48:51], %[t] offen lds\n\ts_add_u32 m0, %[stg], 3072\n\ts_add_u32 %[t], %[ho], 49152\n\t" \
          "buffer_load_dwordx4 %[so], s[48:51], %[t] offen lds\n\ts_add_u32 m0, %[stg], 4096\n\ts_add_u32 %[t], %[ho], 65536\n\tbuffer_load_dwordx4 %[so], s[48:51], %[t] offen lds\n\ts_add_u32 m0, %[stg], 5120\n\ts_add_u32 %[t], %[ho], 81920\n\t" \
          "buffer_load_dwordx4 %[so], s[48:51], %[t] offen lds\n\ts_add_u32 m0, %[stg], 6144\n\ts_add_u32 %[t], %[ho], 98304\n\tbuffer_load_dwordx4 %[so], s[48:51], %[t] offen lds\n\ts_add_u32 m0, %[stg], 7168\n\ts_add_u32 %[t], %[ho], 114688\n\t" \
          "buffer_load_dwordx4 %[so], s[48:51], %[t] offen lds\n\ts_add_u32 m0, %[stg], 8192\n\ts_add_u32 %[t], %[ho], 131072\n\tbuffer_load_dwordx4 %[so], s[48:51], %[t] offen lds\n\ts_add_u32 m0, %[stg], 9216\n\ts_add_u32 %[t], %[ho], 147456\n\t" \
          "buffer_load_dwordx4 %[so], s[48:51], %[t] offen lds\n\ts_add_u32 m0, %[stg], 10240\n\ts_add_u32 %[t], %[ho], 163840\n\tbuffer_load_dwordx4 %[so], s[48:51], %[t] offen lds\n\ts_add_u32 m0, %[stg], 11264\n\ts_add_u32 %[t], %[ho], 180224\n\t" \
          "buffer_load_dwordx4 %[so], s[48:51], %[t] offen lds\n\ts_add_u32 m0, %[stg], 12288\n\ts_add_u32 %[t], %[ho], 196608\n\tbuffer_load_dwordx4 %[so], s[48:51], %[t] offen lds\n\ts_add_u32 m0, %[stg], 13312\n\ts_add_u32 %[t], %[ho], 212992\n\t" \
          "buffer_load_dwordx4 %[so], s[48:51], %[t] offen lds\n\ts_add_u32 m0, %[stg], 14336\n\ts_add_u32 %[t], %[ho], 229376\n\tbuffer_load_dwordx4 %[so], s[48:51], %[t] offen lds\n\ts_add_u32 m0, %[stg], 15360\n\ts_add_u32 %[t], %[ho], 245760\n\t" \
          "buffer_load_dwordx4 %[so], s[48:51], %[t] offen lds\n\ts_waitcnt vmcnt(16)\n\tds_read_b64_tr_b16 v[72:73], %[vb0] offset:16384\n\tds_read_b64_tr_b16 v[74:75], %[vb0] offset:16512\n\tds_read_b64_tr_b16 v[76:77], %[vb0] offset:20480\n\tds_read_b64_tr_b16 v[78:79], %[vb0] offset:20608\n\t" \
          "ds_read_b64_tr_b16 v[80:81], %[vb1] offset:16384\n\tds_read_b64_tr_b16 v[82:83], %[vb1] offset:16512\n\tds_read_b64_tr_b16 v[84:85], %[vb1] offset:20480\n\tds_read_b64_tr_b16 v[86:87], %[vb1] offset:20608\n\tds_read_b64_tr_b16 v[88:89], %[vb2] offset:16384\n\tds_read_b64_tr_b16 v[90:91], %[vb2] offset:16512\n\t" \
          "ds_read_b64_tr_b16 v[92:93], %[vb2] offset:20480\n\tds_read_b64_tr_b16 v[94:95], %[vb2] offset:20608\n\tds_read_b64_tr_b16 v[96:97], %[vb3] offset:16384\n\tds_read_b64_tr_b16 v[98:99], %[vb3] offset:16512\n\tds_read_b64_tr_b16 v[100:101], %[vb3] offset:20480\n\tds_read_b64_tr_b16 v[102:103], %[vb3] offset:20608\n\t" \
          "ds_read_b64_tr_b16 v[104:105], %[vb0] offset:24576\n\tds_read_b64_tr_b16 v[106:107], %[vb0] offset:24704\n\tds_read_b64_tr_b16 v[108:109], %[vb0] offset:28672\n\tds_read_b64_tr_b16 v[110:111], %[vb0] offset:28800\n\tds_read_b64_tr_b16 v[112:113], %[vb1] offset:24576\n\tds_read_b64_tr_b16 v[114:115], %[vb1] offset:24704\n\t" \
          "ds_read_b64_tr_b16 v[116:117], %[vb1] offset:28672\n\tds_read_b64_tr_b16 v[118:119], %[vb1] offset:28800\n\tds_read_b64_tr_b16 v[120:121], %[vb2] offset:24576\n\tds_read_b64_tr_b16 v[122:123], %[vb2] offset:24704\n\tds_read_b64_tr_b16 v[124:125], %[vb2] offset:28672\n\tds_read_b64_tr_b16 v[126:127], %[vb2] offset:28800\n\t" \
          "ds_read_b64_tr_b16 v[128:129], %[vb3] offset:24576\n\tds_read_b64_tr_b16 v[130:131], %[vb3] offset:24704\n\tds_read_b64_tr_b16 v[132:133], %[vb3] offset:28672\n\tds_read_b64_tr_b16 v[134:135], %[vb3] offset:28800\n\ts_waitcnt lgkmcnt(0)\n\ts_add_u32 %[ho], %[k0], 0\n\t" \
          "s_and_b32 %[ho], %[ho], 2\n\ts_or_b32 %[ho], %[ho], 1\n\ts_lshl_b32 %[ho], %[ho], 20\n\ts_xor_b32 %[ho], %[ho], %[hx]\n\ts_add_u32 m0, %[stg], 16384\n\ts_add_u32 %[t], %[ho], 0\n\t" \
          "buffer_load_dwordx4 %[so], s[48:51], %[t] offen lds\n\ts_add_u32 m0, %[stg], 17408\n\ts_add_u32 %[t], %[ho], 16384\n\tbuffer_load_dwordx4 %[so], s[48:51], %[t] offen lds\n\ts_add_u32 m0, %[stg], 18432\n\ts_add_u32 %[t], %[ho], 32768\n\t" \
          "buffer_load_dwordx4 %[so], s[48:51], %[t] offen lds\n\ts_add_u32 m0, %[stg], 19456\n\ts_add_u32 %[t], %[ho], 49152\n\tbuffer_load_dwordx4 %[so], s[48:51], %[t] offen lds\n\ts_add_u32 m0, %[stg], 20480\n\ts_add_u32 %[t], %[ho], 65536\n\t" \
          "buffer_load_dwordx4 %[so], s[48:51], %[t] offen lds\n\ts_add_u32 m0, %[stg], 21504\n\ts_add_u32 %[t], %[ho], 81920\n\tbuffer_load_dwordx4 %[so], s[48:51], %[t] offen lds\n\ts_add_u32 m0, %[stg], 22528\n\ts_add_u32 %[t], %[ho], 98304\n\t" \
          "buffer_load_dwordx4 %[so], s[48:51], %[t] offen lds\n\ts_add_u32 m0, %[stg], 23552\n\ts_add_u32 %[t], %[ho], 114688\n\tbuffer_load_dwordx4 %[so], s[48:51], %[t] offen lds\n\ts_add_u32 m0, %[stg], 24576\n\ts_add_u32 %[t], %[ho], 131072\n\t" \
          "buffer_load_dwordx4 %[so], s[48:51], %[t] offen lds\n\ts_add_u32 m0, %[stg], 25600\n\ts_add_u32 %[t], %[ho], 147456\n\tbuffer_load_dwordx4 %[so], s[48:51], %[t] offen lds\n\ts_add_u32 m0, %[stg], 26624\n\ts_add_u32 %[t], %[ho], 163840\n\t" \
          "buffer_load_dwordx4 %[so], s[48:51], %[t] offen lds\n\ts_add_u32 m0, %[stg], 27648\n\ts_add_u32 %[t], %[ho], 180224\n\tbuffer_load_dwordx4 %[so], s[48:51], %[t] offen lds\n\ts_add_u32 m0, %[stg], 28672\n\ts_add_u32 %[t], %[ho], 196608\n\t" \
          "buffer_load_dwordx4 %[so], s[48:51], %[t] offen lds\n\ts_add_u32 m0, %[stg], 29696\n\ts_add_u32 %[t], %[ho], 212992\n\tbuffer_load_dwordx4 %[so], s[48:51], %[t] offen lds\n\ts_add_u32 m0, %[stg], 30720\n\ts_add_u32 %[t], %[ho], 229376\n\t" \
          "buffer_load_dwordx4 %[so], s[48:51], %[t] offen lds\n\ts_add_u32 m0, %[stg], 31744\n\ts_add_u32 %[t], %[ho], 245760\n\tbuffer_load_dwordx4 %[so], s[48:51], %[t] offen lds\n\ts_mov_b32 %[n], 8\n\ts_cmp_eq_u32 %[k0], 2\n\t" \
          "s_cbranch_scc1 Lw_2_%=\n\ts_cmp_eq_u32 %[k0], 0\n\ts_cbranch_scc1 Lw_0_%=\n\ts_waitcnt vmcnt(0)\n\ts_cmp_eq_u32 %[k0], 1\n\ts_cbranch_scc1 Lw_1_%=\n\t" \
          "s_branch Lw_3_%=\n\tLw_0_%=:\n\ts_waitcnt vmcnt(16)\n\tds_read_b64_tr_b16 a[0:1], %[ob] offset:0\n\tds_read_b64_tr_b16 a[2:3], %[ob] offset:128\n\tds_read_b64_tr_b16 a[4:5], %[ob] offset:8192\n\t" \
          "ds_read_b64_tr_b16 a[6:7], %[ob] offset:8320\n\tds_read_b64_tr_b16 a[8:9], %[ob] offset:32\n\tds_read_b64_tr_b16 a[10:11], %[ob] offset:160\n\tds_read_b64_tr_b16 a[12:13], %[ob] offset:8224\n\tds_read_b64_tr_b16 a[14:15], %[ob] offset:8352\n\tds_read_b64_tr_b16 a[16:17], %[ob] offset:1024\n\t" \
          "ds_read_b64_tr_b16 a[18:19], %[ob] offset:1152\n\tds_read_b64_tr_b16 a[20:21], %[ob] offset:9216\n\tds_read_b64_tr_b16 a[22:23], %[ob] offset:9344\n\tds_read_b64_tr_b16 a[24:25], %[ob] offset:1056\n\tds_read_b64_tr_b16 a[26:27], %[ob] offset:1184\n\tds_read_b64_tr_b16 a[28:29], %[ob] offset:9248\n\t" \
          "ds_read_b64_tr_b16 a[30:31], %[ob] offset:9376\n\ts_cmp_gt_u32 %[n], 2\n\ts_cselect_b32 s50, s50, 0\n\ts_cmp_gt_u32 %[n], 6\n\ts_cselect_b32 %[ho], 0x2, 0x6\n\ts_lshl_b32 %[ho], %[ho], 20\n\t" \
          "s_xor_b32 %[ho], %[ho], %[hx]\n\ts_waitcnt lgkmcnt(0)\n\ts_add_u32 m0, %[stg], 0\n\ts_add_u32 %[t], %[ho], 0\n\tbuffer_load_dwordx4 %[so], s[48:51], %[t] offen lds\n\ts_add_u32 m0, %[stg], 1024\n\t" \
          "s_add_u32 %[t], %[ho], 16384\n\tbuffer_load_dwordx4 %[so], s[48:51], %[t] offen lds\n\ts_add_u32 m0, %[stg], 2048\n\ts_add_u32 %[t], %[ho], 32768\n\tbuffer_load_dwordx4 %[so], s[48:51], %[t] offen lds\n\ts_add_u32 m0, %[stg], 3072\n\t" \
          "s_add_u32 %[t], %[ho], 49152\n\tbuffer_load_dwordx4 %[so], s[48:51], %[t] offen lds\n\ts_add_u32 m0, %[stg], 4096\n\ts_add_u32 %[t], %[ho], 65536\n\tbuffer_load_dwordx4 %[so], s[48:51], %[t] offen lds\n\ts_add_u32 m0, %[stg], 5120\n\t" \
          "s_add_u32 %[t], %[ho], 81920\n\tbuffer_load_dwordx4 %[so], s[48:51], %[t] offen lds\n\ts_add_u32 m0, %[stg], 6144\n\ts_add_u32 %[t], %[ho], 98304\n\tbuffer_load_dwordx4 %[so], s[48:51], %[t] offen lds\n\ts_add_u32 m0, %[stg], 7168\n\t" \
          "s_add_u32 %[t], %[ho], 114688\n\tbuffer_load_dwordx4 %[so], s[48:51], %[t] offen lds\n\ts_add_u32 m0, %[stg], 8192\n\ts_add_u32 %[t], %[ho], 131072\n\tbuffer_load_dwordx4 %[so], s[48:51], %[t] offen lds\n\ts_add_u32 m0, %[stg], 9216\n\t" \
          "s_add_u32 %[t], %[ho], 147456\n\tbuffer_load_dwordx4 %[so], s[48:51], %[t] offen lds\n\ts_add_u32 m0, %[stg], 10240\n\ts_add_u32 %[t], %[ho], 163840\n\tbuffer_load_dwordx4 %[so], s[48:51], %[t] offen lds\n\ts_add_u32 m0, %[stg], 11264\n\t" \
          "s_add_u32 %[t], %[ho], 180224\n\tbuffer_load_dwordx4 %[so], s[48:51], %[t] offen lds\n\ts_add_u32 m0, %[stg], 12288\n\ts_add_u32 %[t], %[ho], 196608\n\tbuffer_load_dwordx4 %[so], s[48:51], %[t] offen lds\n\ts_add_u32 m0, %[stg], 13312\n\t" \
          "s_add_u32 %[t], %[ho], 212992\n\tbuffer_load_dwordx4 %[so], s[48:51], %[t] offen lds\n\ts_add_u32 m0, %[stg], 14336\n\ts_add_u32 %[t], %[ho], 229376\n\tbuffer_load_dwordx4 %[so], s[48:51], %[t] offen lds\n\ts_add_u32 m0, %[stg], 15360\n\t" \
          "s_add_u32 %[t], %[ho], 245760\n\tbuffer_load_dwordx4 %[so], s[48:51], %[t] offen lds\n\ts_sub_u32 %[n], %[n], 1\n\ts_cmp_eq_u32 %[n], 4\n\ts_cbranch_scc1 Lw_5_%=\n\tLw_1_%=:\n\t" \
          "s_waitcnt vmcnt(16)\n\tds_read_b64_tr_b16 a[32:33], %[ob] offset:16384\n\tds_read_b64_tr_b16 a[34:35], %[ob] offset:16512\n\tds_read_b64_tr_b16 a[36:37], %[ob] offset:24576\n\tds_read_b64_tr_b16 a[38:39], %[ob] offset:24704\n\tds_read_b64_tr_b16 a[40:41], %[ob] offset:16416\n\t" \
          "ds_read_b64_tr_b16 a[42:43], %[ob] offset:16544\n\tds_read_b64_tr_b16 a[44:45], %[ob] offset:24608\n\tds_read_b64_tr_b16 a[46:47], %[ob] offset:24736\n\tds_read_b64_tr_b16 a[48:49], %[ob] offset:17408\n\tds_read_b64_tr_b16 a[50:51], %[ob] offset:17536\n\tds_read_b64_tr_b16 a[52:53], %[ob] offset:25600\n\t" \
          "ds_read_b64_tr_b16 a[54:55], %[ob] offset:25728\n\tds_read_b64_tr_b16 a[56:57], %[ob] offset:17440\n\tds_read_b64_tr_b16 a[58:59], %[ob] offset:17568\n\tds_read_b64_tr_b16 a[60:61], %[ob] offset:25632\n\tds_read_b64_tr_b16 a[62:63], %[ob] offset:25760\n\ts_cmp_gt_u32 %[n], 2\n\t" \
          "s_cselect_b32 s50, s50, 0\n\ts_cmp_gt_u32 %[n], 6\n\ts_cselect_b32 %[ho], 0x3, 0x7\n\ts_lshl_b32 %[ho], %[ho], 20\n\ts_xor_b32 %[ho], %[ho], %[hx]\n\ts_waitcnt lgkmcnt(0)\n\t" \
          "s_add_u32 m0, %[stg], 16384\n\ts_add_u32 %[t], %[ho], 0\n\tbuffer_load_dwordx4 %[so], s[48:51], %[t] offen lds\n\ts_add_u32 m0, %[stg], 17408\n\ts_add_u32 %[t], %[ho], 16384\n\tbuffer_load_dwordx4 %[so], s[48:51], %[t] offen lds\n\t" \
          "s_add_u32 m0, %[stg], 18432\n\ts_add_u32 %[t], %[ho], 32768\n\tbuffer_load_dwordx4 %[so], s[48:51], %[t] offen lds\n\ts_add_u32 m0, %[stg], 19456\n\ts_add_u32 %[t], %[ho], 49152\n\tbuffer_load_dwordx4 %[so], s[48:51], %[t] offen lds\n\t" \
          "s_add_u32 m0, %[stg], 20480\n\ts_add_u32 %[t], %[ho], 65536\n\tbuffer_load_dwordx4 %[so], s[48:51], %[t] offen lds\n\ts_add_u32 m0, %[stg], 21504\n\ts_add_u32 %[t], %[ho], 81920\n\tbuffer_load_dwordx4 %[so], s[48:51], %[t] offen lds\n\t" \
          "s_add_u32 m0, %[stg], 22528\n\ts_add_u32 %[t], %[ho], 98304\n\tbuffer_load_dwordx4 %[so], s[48:51], %[t] offen lds\n\ts_add_u32 m0, %[stg], 23552\n\ts_add_u32 %[t], %[ho], 114688\n\tbuffer_load_dwordx4 %[so], s[48:51], %[t] offen lds\n\t" \
          "s_add_u32 m0, %[stg], 24576\n\ts_add_u32 %[t], %[ho], 131072\n\tbuffer_load_dwordx4 %[so], s[48:51], %[t] offen lds\n\ts_add_u32 m0, %[stg], 25600\n\ts_add_u32 %[t], %[ho], 147456\n\tbuffer_load_dwordx4 %[so], s[48:51], %[t] offen lds\n\t" \
          "s_add_u32 m0, %[stg], 26624\n\ts_add_u32 %[t], %[ho], 163840\n\tbuffer_load_dwordx4 %[so], s[48:51], %[t] offen lds\n\ts_add_u32 m0, %[stg], 27648\n\ts_add_u32 %[t], %[ho], 180224\n\tbuffer_load_dwordx4 %[so], s[48:51], %[t] offen lds\n\t" \
          "s_add_u32 m0, %[stg], 28672\n\ts_add_u32 %[t], %[ho], 196608\n\tbuffer_load_dwordx4 %[so], s[48:51], %[t] offen lds\n\ts_add_u32 m0, %[stg], 29696\n\ts_add_u32 %[t], %[ho], 212992\n\tbuffer_load_dwordx4 %[so], s[48:51], %[t] offen lds\n\t" \
          "s_add_u32 m0, %[stg], 30720\n\ts_add_u32 %[t], %[ho], 229376\n\tbuffer_load_dwordx4 %[so], s[48:51], %[t] offen lds\n\ts_add_u32 m0, %[stg], 31744\n\ts_add_u32 %[t], %[ho], 245760\n\tbuffer_load_dwordx4 %[so], s[48:51], %[t] offen lds\n\t" \
          "s_sub_u32 %[n], %[n], 1\n\ts_cmp_eq_u32 %[n], 4\n\ts_cbranch_scc1 Lw_6_%=\n\tLw_2_%=:\n\ts_waitcnt vmcnt(16)\n\tds_read_b64_tr_b16 a[64:65], %[ob] offset:0\n\t" \
          "ds_read_b64_tr_b16 a[66:67], %[ob] offset:128\n\tds_read_b64_tr_b16 a[68:69], %[ob] offset:8192\n\tds_read_b64_tr_b16 a[70:71], %[ob] offset:8320\n\tds_read_b64_tr_b16 a[72:73], %[ob] offset:32\n\tds_read_b64_tr_b16 a[74:75], %[ob] offset:160\n\tds_read_b64_tr_b16 a[76:77], %[ob] offset:8224\n\t" \
          "ds_read_b64_tr_b16 a[78:79], %[ob] offset:8352\n\tds_read_b64_tr_b16 a[80:81], %[ob] offset:1024\n\tds_read_b64_tr_b16 a[82:83], %[ob] offset:1152\n\tds_read_b64_tr_b16 a[84:85], %[ob] offset:9216\n\tds_read_b64_tr_b16 a[86:87], %[ob] offset:9344\n\tds_read_b64_tr_b16 a[88:89], %[ob] offset:1056\n\t" \
          "ds_read_b64_tr_b16 a[90:91], %[ob] offset:1184\n\tds_read_b64_tr_b16 a[92:93], %[ob] offset:9248\n\tds_read_b64_tr_b16 a[94:95], %[ob] offset:9376\n\ts_cmp_gt_u32 %[n], 2\n\ts_cselect_b32 s50, s50, 0\n\ts_cmp_gt_u32 %[n], 6\n\t" \
          "s_cselect_b32 %[ho], 0x0, 0x4\n\ts_lshl_b32 %[ho], %[ho], 20\n\ts_xor_b32 %[ho], %[ho], %[hx]\n\ts_waitcnt lgkmcnt(0)\n\ts_add_u32 m0, %[stg], 0\n\ts_add_u32 %[t], %[ho], 0\n\t" \
          "buffer_load_dwordx4 %[so], s[48:51], %[t] offen lds\n\ts_add_u32 m0, %[stg], 1024\n\ts_add_u32 %[t], %[ho], 16384\n\tbuffer_load_dwordx4 %[so], s[48:51], %[t] offen lds\n\ts_add_u32 m0, %[stg], 2048\n\ts_add_u32 %[t], %[ho], 32768\n\t" \
          "buffer_load_dwordx4 %[so], s[48:51], %[t] offen lds\n\ts_add_u32 m0, %[stg], 3072\n\ts_add_u32 %[t], %[ho], 49152\n\tbuffer_load_dwordx4 %[so], s[48:51], %[t] offen lds\n\ts_add_u32 m0, %[stg], 4096\n\ts_add_u32 %[t], %[ho], 65536\n\t" \
          "buffer_load_dwordx4 %[so], s[48:51], %[t] offen lds\n\ts_add_u32 m0, %[stg], 5120\n\ts_add_u32 %[t], %[ho], 81920\n\tbuffer_load_dwordx4 %[so], s[48:51], %[t] offen lds\n\ts_add_u32 m0, %[stg], 6144\n\ts_add_u32 %[t], %[ho], 98304\n\t" \
          "buffer_load_dwordx4 %[so], s[48:51], %[t] offen lds\n\ts_add_u32 m0, %[stg], 7168\n\ts_add_u32 %[t], %[ho], 114688\n\tbuffer_load_dwordx4 %[so], s[48:51], %[t] offen lds\n\ts_add_u32 m0, %[stg], 8192\n\ts_add_u32 %[t], %[ho], 131072\n\t" \
          "buffer_load_dwordx4 %[so], s[48:51], %[t] offen lds\n\ts_add_u32 m0, %[stg], 9216\n\ts_add_u32 %[t], %[ho], 147456\n\tbuffer_load_dwordx4 %[so], s[48:51], %[t] offen lds\n\ts_add_u32 m0, %[stg], 10240\n\ts_add_u32 %[t], %[ho], 163840\n\t" \
          "buffer_load_dwordx4 %[so], s[48:51], %[t] offen lds\n\ts_add_u32 m0, %[stg], 11264\n\ts_add_u32 %[t], %[ho], 180224\n\tbuffer_load_dwordx4 %[so], s[48:51], %[t] offen lds\n\ts_add_u32 m0, %[stg], 12288\n\ts_add_u32 %[t], %[ho], 196608\n\t" \
          "buffer_load_dwordx4 %[so], s[48:51], %[t] offen lds\n\ts_add_u32 m0, %[stg], 13312\n\ts_add_u32 %[t], %[ho], 212992\n\tbuffer_load_dwordx4 %[so], s[48:51], %[t] offen lds\n\ts_add_u32 m0, %[stg], 14336\n\ts_add_u32 %[t], %[ho], 229376\n\t" \
          "buffer_load_dwordx4 %[so], s[48:51], %[t] offen lds\n\ts_add_u32 m0, %[stg], 15360\n\ts_add_u32 %[t], %[ho], 245760\n\tbuffer_load_dwordx4 %[so], s[48:51], %[t] offen lds\n\ts_sub_u32 %[n], %[n], 1\n\ts_cmp_eq_u32 %[n], 4\n\t" \
          "s_cbranch_scc1 Lw_7_%=\n\tLw_3_%=:\n\ts_waitcnt vmcnt(16)\n\tds_read_b64_tr_b16 a[96:97], %[ob] offset:16384\n\tds_read_b64_tr_b16 a[98:99], %[ob] offset:16512\n\tds_read_b64_tr_b16 a[100:101], %[ob] offset:24576\n\t" \
          "ds_read_b64_tr_b16 a[102:103], %[ob] offset:24704\n\tds_read_b64_tr_b16 a[104:105], %[ob] offset:16416\n\tds_read_b64_tr_b16 a[106:107], %[ob] offset:16544\n\tds_read_b64_tr_b16 a[108:109], %[ob] offset:24608\n\tds_read_b64_tr_b16 a[110:111], %[ob] offset:24736\n\tds_read_b64_tr_b16 a[112:113], %[ob] offset:17408\n\t" \
          "ds_read_b64_tr_b16 a[114:115], %[ob] offset:17536\n\tds_read_b64_tr_b16 a[116:117], %[ob] offset:25600\n\tds_read_b64_tr_b16 a[118:119], %[ob] offset:25728\n\tds_read_b64_tr_b16 a[120:121], %[ob] offset:17440\n\tds_read_b64_tr_b16 a[122:123], %[ob] offset:17568\n\tds_read_b64_tr_b16 a[124:125], %[ob] offset:25632\n\t" \
          "ds_read_b64_tr_b16 a[126:127], %[ob] offset:25760\n\ts_cmp_gt_u32 %[n], 2\n\ts_cselect_b32 s50, s50, 0\n\ts_cmp_gt_u32 %[n], 6\n\ts_cselect_b32 %[ho], 0x1, 0x5\n\ts_lshl_b32 %[ho], %[ho], 20\n\t" \
          "s_xor_b32 %[ho], %[ho], %[hx]\n\ts_waitcnt lgkmcnt(0)\n\ts_add_u32 m0, %[stg], 16384\n\ts_add_u32 %[t], %[ho], 0\n\tbuffer_load_dwordx4 %[so], s[48:51], %[t] offen lds\n\ts_add_u32 m0, %[stg], 17408\n\t" \
          "s_add_u32 %[t], %[ho], 16384\n\tbuffer_load_dwordx4 %[so], s[48:51], %[t] offen lds\n\ts_add_u32 m0, %[stg], 18432\n\ts_add_u32 %[t], %[ho], 32768\n\tbuffer_load_dwordx4 %[so], s[48:51], %[t] offen lds\n\ts_add_u32 m0, %[stg], 19456\n\t" \
          "s_add_u32 %[t], %[ho], 49152\n\tbuffer_load_dwordx4 %[so], s[48:51], %[t] offen lds\n\ts_add_u32 m0, %[stg], 20480\n\ts_add_u32 %[t], %[ho], 65536\n\tbuffer_load_dwordx4 %[so], s[48:51], %[t] offen lds\n\ts_add_u32 m0, %[stg], 21504\n\t" \
          "s_add_u32 %[t], %[ho], 81920\n\tbuffer_load_dwordx4 %[so], s[48:51], %[t] offen lds\n\ts_add_u32 m0, %[stg], 22528\n\ts_add_u32 %[t], %[ho], 98304\n\tbuffer_load_dwordx4 %[so], s[48:51], %[t] offen lds\n\ts_add_u32 m0, %[stg], 23552\n\t" \
          "s_add_u32 %[t], %[ho], 114688\n\tbuffer_load_dwordx4 %[so], s[48:51], %[t] offen lds\n\ts_add_u32 m0, %[stg], 24576\n\ts_add_u32 %[t], %[ho], 131072\n\tbuffer_load_dwordx4 %[so], s[48:51], %[t] offen lds\n\ts_add_u32 m0, %[stg], 25600\n\t" \
          "s_add_u32 %[t], %[ho], 147456\n\tbuffer_load_dwordx4 %[so], s[48:51], %[t] offen lds\n\ts_add_u32 m0, %[stg], 26624\n\ts_add_u32 %[t], %[ho], 163840\n\tbuffer_load_dwordx4 %[so], s[48:51], %[t] offen lds\n\ts_add_u32 m0, %[stg], 27648\n\t" \
          "s_add_u32 %[t], %[ho], 180224\n\tbuffer_load_dwordx4 %[so], s[48:51], %[t] offen lds\n\ts_add_u32 m0, %[stg], 28672\n\ts_add_u32 %[t], %[ho], 196608\n\tbuffer_load_dwordx4 %[so], s[48:51], %[t] offen lds\n\ts_add_u32 m0, %[stg], 29696\n\t" \
          "s_add_u32 %[t], %[ho], 212992\n\tbuffer_load_dwordx4 %[so], s[48:51], %[t] offen lds\n\ts_add_u32 m0, %[stg], 30720\n\ts_add_u32 %[t], %[ho], 229376\n\tbuffer_load_dwordx4 %[so], s[48:51], %[t] offen lds\n\ts_add_u32 m0, %[stg], 31744\n\t" \
          "s_add_u32 %[t], %[ho], 245760\n\tbuffer_load_dwordx4 %[so], s[48:51], %[t] offen lds\n\ts_sub_u32 %[n], %[n], 1\n\ts_cmp_eq_u32 %[n], 4\n\ts_cbranch_scc1 Lw_4_%=\n\ts_branch Lw_0_%=\n\t" \
          "Lw_4_%=:\n\ts_waitcnt vmcnt(16)\n\tds_read_b64_tr_b16 a[128:129], %[ob] offset:0\n\tds_read_b64_tr_b16 a[130:131], %[ob] offset:128\n\tds_read_b64_tr_b16 a[132:133], %[ob] offset:8192\n\tds_read_b64_tr_b16 a[134:135], %[ob] offset:8320\n\t" \
          "ds_read_b64_tr_b16 a[136:137], %[ob] offset:32\n\tds_read_b64_tr_b16 a[138:139], %[ob] offset:160\n\tds_read_b64_tr_b16 a[140:141], %[ob] offset:8224\n\tds_read_b64_tr_b16 a[142:143], %[ob] offset:8352\n\tds_read_b64_tr_b16 a[144:145], %[ob] offset:1024\n\tds_read_b64_tr_b16 a[146:147], %[ob] offset:1152\n\t" \
          "ds_read_b64_tr_b16 a[148:149], %[ob] offset:9216\n\tds_read_b64_tr_b16 a[150:151], %[ob] offset:9344\n\tds_read_b64_tr_b16 a[152:153], %[ob] offset:1056\n\tds_read_b64_tr_b16 a[154:155], %[ob] offset:1184\n\tds_read_b64_tr_b16 a[156:157], %[ob] offset:9248\n\tds_read_b64_tr_b16 a[158:159], %[ob] offset:9376\n\t" \
          "s_cmp_gt_u32 %[n], 2\n\ts_cselect_b32 s50, s50, 0\n\ts_cmp_gt_u32 %[n], 6\n\ts_cselect_b32 %[ho], 0x2, 0x6\n\ts_lshl_b32 %[ho], %[ho], 20\n\ts_xor_b32 %[ho], %[ho], %[hx]\n\t" \
          "s_waitcnt lgkmcnt(0)\n\ts_add_u32 m0, %[stg], 0\n\ts_add_u32 %[t], %[ho], 0\n\tbuffer_load_dwordx4 %[so], s[48:51], %[t] offen lds\n\ts_add_u32 m0, %[stg], 1024\n\ts_add_u32 %[t], %[ho], 16384\n\t" \
          "buffer_load_dwordx4 %[so], s[48:51], %[t] offen lds\n\ts_add_u32 m0, %[stg], 2048\n\ts_add_u32 %[t], %[ho], 32768\n\tbuffer_load_dwordx4 %[so], s[48:51], %[t] offen lds\n\ts_add_u32 m0, %[stg], 3072\n\ts_add_u32 %[t], %[ho], 49152\n\t" \
          "buffer_load_dwordx4 %[so], s[48:51], %[t] offen lds\n\ts_add_u32 m0, %[stg], 4096\n\ts_add_u32 %[t], %[ho], 65536\n\tbuffer_load_dwordx4 %[so], s[48:51], %[t] offen lds\n\ts_add_u32 m0, %[stg], 5120\n\ts_add_u32 %[t], %[ho], 81920\n\t" \
          "buffer_load_dwordx4 %[so], s[48:51], %[t] offen lds\n\ts_add_u32 m0, %[stg], 6144\n\ts_add_u32 %[t], %[ho], 98304\n\tbuffer_load_dwordx4 %[so], s[48:51], %[t] offen lds\n\ts_add_u32 m0, %[stg], 7168\n\ts_add_u32 %[t], %[ho], 114688\n\t" \
          "buffer_load_dwordx4 %[so], s[48:51], %[t] offen lds\n\ts_add_u32 m0, %[stg], 8192\n\ts_add_u32 %[t], %[ho], 131072\n\tbuffer_load_dwordx4 %[so], s[48:51], %[t] offen lds\n\ts_add_u32 m0, %[stg], 9216\n\ts_add_u32 %[t], %[ho], 147456\n\t" \
          "buffer_load_dwordx4 %[so], s[48:51], %[t] offen lds\n\ts_add_u32 m0, %[stg], 10240\n\ts_add_u32 %[t], %[ho], 163840\n\tbuffer_load_dwordx4 %[so], s[48:51], %[t] offen lds\n\ts_add_u32 m0, %[stg], 11264\n\ts_add_u32 %[t], %[ho], 180224\n\t" \
          "buffer_load_dwordx4 %[so], s[48:51], %[t] offen lds\n\ts_add_u32 m0, %[stg], 12288\n\ts_add_u32 %[t], %[ho], 196608\n\tbuffer_load_dwordx4 %[so], s[48:51], %[t] offen lds\n\ts_add_u32 m0, %[stg], 13312\n\ts_add_u32 %[t], %[ho], 212992\n\t" \
          "buffer_load_dwordx4 %[so], s[48:51], %[t] offen lds\n\ts_add_u32 m0, %[stg], 14336\n\ts_add_u32 %[t], %[ho], 229376\n\tbuffer_load_dwordx4 %[so], s[48:51], %[t] offen lds\n\ts_add_u32 m0, %[stg], 15360\n\ts_add_u32 %[t], %[ho], 245760\n\t" \
          "buffer_load_dwordx4 %[so], s[48:51], %[t] offen lds\n\ts_sub_u32 %[n], %[n], 1\n\ts_cmp_eq_u32 %[n], 0\n\ts_cbranch_scc1 Lw_end%=\n\tLw_5_%=:\n\ts_waitcnt vmcnt(16)\n\t" \
          "ds_read_b64_tr_b16 a[160:161], %[ob] offset:16384\n\tds_read_b64_tr_b16 a[162:163], %[ob] offset:16512\n\tds_read_b64_tr_b16 a[164:165], %[ob] offset:24576\n\tds_read_b64_tr_b16 a[166:167], %[ob] offset:24704\n\tds_read_b64_tr_b16 a[168:169], %[ob] offset:16416\n\tds_read_b64_tr_b16 a[170:171], %[ob] offset:16544\n\t" \
          "ds_read_b64_tr_b16 a[172:173], %[ob] offset:24608\n\tds_read_b64_tr_b16 a[174:175], %[ob] offset:24736\n\tds_read_b64_tr_b16 a[176:177], %[ob] offset:17408\n\tds_read_b64_tr_b16 a[178:179], %[ob] offset:17536\n\tds_read_b64_tr_b16 a[180:181], %[ob] offset:25600\n\tds_read_b64_tr_b16 a[182:183], %[ob] offset:25728\n\t" \
          "ds_read_b64_tr_b16 a[184:185], %[ob] offset:17440\n\tds_read_b64_tr_b16 a[186:187], %[ob] offset:17568\n\tds_read_b64_tr_b16 a[188:189], %[ob] offset:25632\n\tds_read_b64_tr_b16 a[190:191], %[ob] offset:25760\n\ts_cmp_gt_u32 %[n], 2\n\ts_cselect_b32 s50, s50, 0\n\t" \
          "s_cmp_gt_u32 %[n], 6\n\ts_cselect_b32 %[ho], 0x3, 0x7\n\ts_lshl_b32 %[ho], %[ho], 20\n\ts_xor_b32 %[ho], %[ho], %[hx]\n\ts_waitcnt lgkmcnt(0)\n\ts_add_u32 m0, %[stg], 16384\n\t" \
          "s_add_u32 %[t], %[ho], 0\n\tbuffer_load_dwordx4 %[so], s[48:51], %[t] offen lds\n\ts_add_u32 m0, %[stg], 17408\n\ts_add_u32 %[t], %[ho], 16384\n\tbuffer_load_dwordx4 %[so], s[48:51], %[t] offen lds\n\ts_add_u32 m0, %[stg], 18432\n\t" \
          "s_add_u32 %[t], %[ho], 32768\n\tbuffer_load_dwordx4 %[so], s[48:51], %[t] offen lds\n\ts_add_u32 m0, %[stg], 19456\n\ts_add_u32 %[t], %[ho], 49152\n\tbuffer_load_dwordx4 %[so], s[48:51], %[t] offen lds\n\ts_add_u32 m0, %[stg], 20480\n\t" \
          "s_add_u32 %[t], %[ho], 65536\n\tbuffer_load_dwordx4 %[so], s[48:51], %[t] offen lds\n\ts_add_u32 m0, %[stg], 21504\n\ts_add_u32 %[t], %[ho], 81920\n\tbuffer_load_dwordx4 %[so], s[48:51], %[t] offen lds\n\ts_add_u32 m0, %[stg], 22528\n\t" \
          "s_add_u32 %[t], %[ho], 98304\n\tbuffer_load_dwordx4 %[so], s[48:51], %[t] offen lds\n\ts_add_u32 m0, %[stg], 23552\n\ts_add_u32 %[t], %[ho], 114688\n\tbuffer_load_dwordx4 %[so], s[48:51], %[t] offen lds\n\ts_add_u32 m0, %[stg], 24576\n\t" \
          "s_add_u32 %[t], %[ho], 131072\n\tbuffer_load_dwordx4 %[so], s[48:51], %[t] offen lds\n\ts_add_u32 m0, %[stg], 25600\n\ts_add_u32 %[t], %[ho], 147456\n\tbuffer_load_dwordx4 %[so], s[48:51], %[t] offen lds\n\ts_add_u32 m0, %[stg], 26624\n\t" \
          "s_add_u32 %[t], %[ho], 163840\n\tbuffer_load_dwordx4 %[so], s[48:51], %[t] offen lds\n\ts_add_u32 m0, %[stg], 27648\n\ts_add_u32 %[t], %[ho], 180224\n\tbuffer_load_dwordx4 %[so], s[48:51], %[t] offen lds\n\ts_add_u32 m0, %[stg], 28672\n\t" \
          "s_add_u32 %[t], %[ho], 196608\n\tbuffer_load_dwordx4 %[so], s[48:51], %[t] offen lds\n\ts_add_u32 m0, %[stg], 29696\n\ts_add_u32 %[t], %[ho], 212992\n\tbuffer_load_dwordx4 %[so], s[48:51], %[t] offen lds\n\ts_add_u32 m0, %[stg], 30720\n\t" \
          "s_add_u32 %[t], %[ho], 229376\n\tbuffer_load_dwordx4 %[so], s[48:51], %[t] offen lds\n\ts_add_u32 m0, %[stg], 31744\n\ts_add_u32 %[t], %[ho], 245760\n\tbuffer_load_dwordx4 %[so], s[48:51], %[t] offen lds\n\ts_sub_u32 %[n], %[n], 1\n\t" \
          "s_cmp_eq_u32 %[n], 0\n\ts_cbranch_scc1 Lw_end%=\n\tLw_6_%=:\n\ts_waitcnt vmcnt(16)\n\tbuffer_load_dwordx4 %[rec], %[ro], %[rr], 0 offen sc1\n\tds_read_b64_tr_b16 a[192:193], %[ob] offset:0\n\t" \
          "ds_read_b64_tr_b16 a[194:195], %[ob] offset:128\n\tds_read_b64_tr_b16 a[196:197], %[ob] offset:8192\n\tds_read_b64_tr_b16 a[198:199], %[ob] offset:8320\n\tds_read_b64_tr_b16 a[200:201], %[ob] offset:32\n\tds_read_b64_tr_b16 a[202:203], %[ob] offset:160\n\tds_read_b64_tr_b16 a[204:205], %[ob] offset:8224\n\t" \
          "ds_read_b64_tr_b16 a[206:207], %[ob] offset:8352\n\tds_read_b64_tr_b16 a[208:209], %[ob] offset:1024\n\tds_read_b64_tr_b16 a[210:211], %[ob] offset:1152\n\tds_read_b64_tr_b16 a[212:213], %[ob] offset:9216\n\tds_read_b64_tr_b16 a[214:215], %[ob] offset:9344\n\tds_read_b64_tr_b16 a[216:217], %[ob] offset:1056\n\t" \
          "ds_read_b64_tr_b16 a[218:219], %[ob] offset:1184\n\tds_read_b64_tr_b16 a[220:221], %[ob] offset:9248\n\tds_read_b64_tr_b16 a[222:223], %[ob] offset:9376\n\ts_cmp_gt_u32 %[n], 2\n\ts_cselect_b32 s50, s50, 0\n\ts_cmp_gt_u32 %[n], 6\n\t" \
          "s_cselect_b32 %[ho], 0x0, 0x4\n\ts_lshl_b32 %[ho], %[ho], 20\n\ts_xor_b32 %[ho], %[ho], %[hx]\n\ts_waitcnt lgkmcnt(0)\n\ts_add_u32 m0, %[stg], 0\n\ts_add_u32 %[t], %[ho], 0\n\t" \
          "buffer_load_dwordx4 %[so], s[48:51], %[t] offen lds\n\ts_add_u32 m0, %[stg], 1024\n\ts_add_u32 %[t], %[ho], 16384\n\tbuffer_load_dwordx4 %[so], s[48:51], %[t] offen lds\n\ts_add_u32 m0, %[stg], 2048\n\ts_add_u32 %[t], %[ho], 32768\n\t" \
          "buffer_load_dwordx4 %[so], s[48:51], %[t] offen lds\n\ts_add_u32 m0, %[stg], 3072\n\ts_add_u32 %[t], %[ho], 49152\n\tbuffer_load_dwordx4 %[so], s[48:51], %[t] offen lds\n\ts_add_u32 m0, %[stg], 4096\n\ts_add_u32 %[t], %[ho], 65536\n\t" \
          "buffer_load_dwordx4 %[so], s[48:51], %[t] offen lds\n\ts_add_u32 m0, %[stg], 5120\n\ts_add_u32 %[t], %[ho], 81920\n\tbuffer_load_dwordx4 %[so], s[48:51], %[t] offen lds\n\ts_add_u32 m0, %[stg], 6144\n\ts_add_u32 %[t], %[ho], 98304\n\t" \
          "buffer_load_dwordx4 %[so], s[48:51], %[t] offen lds\n\ts_add_u32 m0, %[stg], 7168\n\ts_add_u32 %[t], %[ho], 114688\n\tbuffer_load_dwordx4 %[so], s[48:51], %[t] offen lds\n\ts_add_u32 m0, %[stg], 8192\n\ts_add_u32 %[t], %[ho], 131072\n\t" \
          "buffer_load_dwordx4 %[so], s[48:51], %[t] offen lds\n\ts_add_u32 m0, %[stg], 9216\n\ts_add_u32 %[t], %[ho], 147456\n\tbuffer_load_dwordx4 %[so], s[48:51], %[t] offen lds\n\ts_add_u32 m0, %[stg], 10240\n\ts_add_u32 %[t], %[ho], 163840\n\t" \
          "buffer_load_dwordx4 %[so], s[48:51], %[t] offen lds\n\ts_add_u32 m0, %[stg], 11264\n\ts_add_u32 %[t], %[ho], 180224\n\tbuffer_load_dwordx4 %[so], s[48:51], %[t] offen lds\n\ts_add_u32 m0, %[stg], 12288\n\ts_add_u32 %[t], %[ho], 196608\n\t" \
          "buffer_load_dwordx4 %[so], s[48:51], %[t] offen lds\n\ts_add_u32 m0, %[stg], 13312\n\ts_add_u32 %[t], %[ho], 212992\n\tbuffer_load_dwordx4 %[so], s[48:51], %[t] offen lds\n\ts_add_u32 m0, %[stg], 14336\n\ts_add_u32 %[t], %[ho], 229376\n\t" \
          "buffer_load_dwordx4 %[so], s[48:51], %[t] offen lds\n\ts_add_u32 m0, %[stg], 15360\n\ts_add_u32 %[t], %[ho], 245760\n\tbuffer_load_dwordx4 %[so], s[48:51], %[t] offen lds\n\ts_sub_u32 %[n], %[n], 1\n\ts_cmp_eq_u32 %[n], 0\n\t" \
          "s_cbranch_scc1 Lw_end%=\n\tLw_7_%=:\n\ts_waitcnt vmcnt(16)\n\tds_read_b64_tr_b16 a[224:225], %[ob] offset:16384\n\tds_read_b64_tr_b16 a[226:227], %[ob] offset:16512\n\tds_read_b64_tr_b16 a[228:229], %[ob] offset:24576\n\t" \
          "ds_read_b64_tr_b16 a[230:231], %[ob] offset:24704\n\tds_read_b64_tr_b16 a[232:233], %[ob] offset:16416\n\tds_read_b64_tr_b16 a[234:235], %[ob] offset:16544\n\tds_read_b64_tr_b16 a[236:237], %[ob] offset:24608\n\tds_read_b64_tr_b16 a[238:239], %[ob] offset:24736\n\tds_read_b64_tr_b16 a[240:241], %[ob] offset:17408\n\t" \
          "ds_read_b64_tr_b16 a[242:243], %[ob] offset:17536\n\tds_read_b64_tr_b16 a[244:245], %[ob] offset:25600\n\tds_read_b64_tr_b16 a[246:247], %[ob] offset:25728\n\tds_read_b64_tr_b16 a[248:249], %[ob] offset:17440\n\tds_read_b64_tr_b16 a[250:251], %[ob] offset:17568\n\tds_read_b64_tr_b16 a[252:253], %[ob] offset:25632\n\t" \
          "ds_read_b64_tr_b16 a[254:255], %[ob] offset:25760\n\ts_cmp_gt_u32 %[n], 2\n\ts_cselect_b32 s50, s50, 0\n\ts_cmp_gt_u32 %[n], 6\n\ts_cselect_b32 %[ho], 0x1, 0x5\n\ts_lshl_b32 %[ho], %[ho], 20\n\t" \
          "s_xor_b32 %[ho], %[ho], %[hx]\n\ts_waitcnt lgkmcnt(0)\n\ts_add_u32 m0, %[stg], 16384\n\ts_add_u32 %[t], %[ho], 0\n\tbuffer_load_dwordx4 %[so], s[48:51], %[t] offen lds\n\ts_add_u32 m0, %[stg], 17408\n\t" \
          "s_add_u32 %[t], %[ho], 16384\n\tbuffer_load_dwordx4 %[so], s[48:51], %[t] offen lds\n\ts_add_u32 m0, %[stg], 18432\n\ts_add_u32 %[t], %[ho], 32768\n\tbuffer_load_dwordx4 %[so], s[48:51], %[t] offen lds\n\ts_add_u32 m0, %[stg], 19456\n\t" \
          "s_add_u32 %[t], %[ho], 49152\n\tbuffer_load_dwordx4 %[so], s[48:51], %[t] offen lds\n\ts_add_u32 m0, %[stg], 20480\n\ts_add_u32 %[t], %[ho], 65536\n\tbuffer_load_dwordx4 %[so], s[48:51], %[t] offen lds\n\ts_add_u32 m0, %[stg], 21504\n\t" \
          "s_add_u32 %[t], %[ho], 81920\n\tbuffer_load_dwordx4 %[so], s[48:51], %[t] offen lds\n\ts_add_u32 m0, %[stg], 22528\n\ts_add_u32 %[t], %[ho], 98304\n\tbuffer_load_dwordx4 %[so], s[48:51], %[t] offen lds\n\ts_add_u32 m0, %[stg], 23552\n\t" \
          "s_add_u32 %[t], %[ho], 114688\n\tbuffer_load_dwordx4 %[so], s[48:51], %[t] offen lds\n\ts_add_u32 m0, %[stg], 24576\n\ts_add_u32 %[t], %[ho], 131072\n\tbuffer_load_dwordx4 %[so], s[48:51], %[t] offen lds\n\ts_add_u32 m0, %[stg], 25600\n\t" \
          "s_add_u32 %[t], %[ho], 147456\n\tbuffer_load_dwordx4 %[so], s[48:51], %[t] offen lds\n\ts_add_u32 m0, %[stg], 26624\n\ts_add_u32 %[t], %[ho], 163840\n\tbuffer_load_dwordx4 %[so], s[48:51], %[t] offen lds\n\ts_add_u32 m0, %[stg], 27648\n\t" \
          "s_add_u32 %[t], %[ho], 180224\n\tbuffer_load_dwordx4 %[so], s[48:51], %[t] offen lds\n\ts_add_u32 m0, %[stg], 28672\n\ts_add_u32 %[t], %[ho], 196608\n\tbuffer_load_dwordx4 %[so], s[48:51], %[t] offen lds\n\ts_add_u32 m0, %[stg], 29696\n\t" \
          "s_add_u32 %[t], %[ho], 212992\n\tbuffer_load_dwordx4 %[so], s[48:51], %[t] offen lds\n\ts_add_u32 m0, %[stg], 30720\n\ts_add_u32 %[t], %[ho], 229376\n\tbuffer_load_dwordx4 %[so], s[48:51], %[t] offen lds\n\ts_add_u32 m0, %[stg], 31744\n\t" \
          "s_add_u32 %[t], %[ho], 245760\n\tbuffer_load_dwordx4 %[so], s[48:51], %[t] offen lds\n\ts_sub_u32 %[n], %[n], 1\n\ts_cmp_eq_u32 %[n], 0\n\ts_cbranch_scc1 Lw_end%=\n\ts_branch Lw_4_%=\n\t" \
          "Lw_end%=:\n\ts_waitcnt vmcnt(0)\n\ts_barrier\n\t"
#define VO_CLOB "v176", "v177", "v178", "v179", "v180", "v181", "v182", "v183", "v184", "v185", "v186", "v187", "v188", "v189", "v190", "v191", "v192", "v193", "v194", "v195", "v196", "v197", "v198", "v199", "v200", "v201", "v202", "v203", "v204", "v205", "v206", "v207", "v208", "v209", "v210", "v211", "v212", "v213", "v214", "v215", "v216", "v217", "v218", "v219", "v220", "v221", "v222", "v223", "v224", "v225", "v226", "v227", "v228", "v229", "v230", "v231", "v232", "v233", "v234", "v235", "v236", "v237", "v238", "v239", "v240", "v241", "v242", "v243", "v244", "v245", "v246", "v247", "v248", "v249", "v250", "v251", "v252", "v253", "v254", "v255", "s48", "s49", "s50", "s51", "s52", "s53"
__global__ void __launch_bounds__(256, 1) k_fused(FParams P) {
  __shared__ __attribute__((aligned(16))) char Lraw[LDS_ALL];
  unsigned long long pc0;
  asm volatile("s_getpc_b64 %0\nLfused_pc0:" : "=s"(pc0));
  const int w = __builtin_amdgcn_readfirstlane(threadIdx.x >> 6);
  unsigned cz; unsigned long long tag;
  {
    int clen; asm volatile("s_mov_b32 %0, Lfused_end-Lfused_pc0" : "=s"(clen));
    const int o = ((blockIdx.x >> 3) * 4 + w) * 1024 + (threadIdx.x & 7) * 128;
    asm volatile("s_nop 4\n\tbuffer_load_dword %0, %1, %2, 0 offen" : "=v"(cz) : "v"(o), "s"(__builtin_amdgcn_make_buffer_rsrc((void*)pc0, 0, clen, 0x00020000))); }
  if ((blockIdx.x & 7) >= 3) {
    asm volatile("Lfused_worker:");
    worker(P, cz, Lraw, w);
    asm volatile("Lfused_end:");
    return;
  }
  unsigned* tmo = &P.ctl->tmo[0]; unsigned long long* prof = P.prof; (void)prof;
  const int xq = blockIdx.x & 7, slot = blockIdx.x >> 3, nq = xq < 2 ? 12 : 8;
  const int b = slot < nq ? 12 * xq + slot : NQK + 20 * xq + (slot - nq);
  const int alw = b < NQK ? (b % 9 == 0 ? (b / 9) * 4 + w : -1) : (b == 33 ? 16 + w : (b == 95 ? 20 + w : -1));
  const int xr = xq;
  const unsigned long long alx = (unsigned)__builtin_amdgcn_readfirstlane(alw >= 0 ? -1 : 0);
  const __amdgpu_buffer_rsrc_t rs_m = __builtin_amdgcn_make_buffer_rsrc(P.ring, 0, RSIZE, 0x00020000), rs_r = __builtin_amdgcn_make_buffer_rsrc(P.rdy, 0, RDY_SIZE, 0x00020000);
  const __amdgpu_buffer_rsrc_t rs_c = __builtin_amdgcn_make_buffer_rsrc(P.ctl, 0, sizeof(Ctl), 0x00020000);
  v4u rdy;
#define LANE_CONSTS const int lane = opaque_lane(), tid = 64 * w + lane, l15 = lane & 15, lq = lane >> 4, idx = (lane & 1) ? 0xEEEE : 0x4444; \
  const int mem_lane_off = (((lq >> 1) * 2 + (l15 >> 3)) * 16 + (lq & 1) * 8 + (l15 & 7)) * 16 + dbl(w * 4096); \
  const int wrow = 8 * (lq >> 1) + 2 * (lq & 1) + 4 * (l15 >> 3) + 16 * ((l15 >> 2) & 1), wsw = (wrow >> 1) & 3
  unsigned tA = 0;
  if (b < NQK) {
    const int h = b;
    LANE_CONSTS;
    v32u wa[8], wv_[7];
    const unsigned lb0 = (unsigned)(size_t)Lraw;
    const unsigned own = lb0 + w * 40960 + lane * 16;
    unsigned kfl;
    {
      const __amdgpu_buffer_rsrc_t rq = __builtin_amdgcn_make_buffer_rsrc((void*)(P.Wq + h * DHA + 64 * (w >> 1)), 0, 0x7fffffff, 0x00020000), rk = __builtin_amdgcn_make_buffer_rsrc((void*)(P.Wk + h * DHA + 64 * (w >> 1)), 0, 0x7fffffff, 0x00020000);
      const int go = (lane >> 3) * HDA * 2 + (((((lane & 7) >> 1) ^ ((lane >> 4) & 3)) << 1) | (lane & 1)) * 16;
      unsigned rd[4];
#pragma unroll
      for (int t = 0; t < 4; ++t) rd[t] = lb0 + w * 40960 + wrow * 128 + (l15 & 3) * 8 + ((t ^ (2 * (~w & 1)) ^ wsw) << 5);
#define QK_NTq ""
#define QK_NTk ""
      int t_;
      asm volatile(QK_LOADER : QK_DEFW, [t] "=&s"(t_), [rec] "=&v"(rdy), [kf] "=&v"(kfl)
                   : [ko] "v"(lane < 2 ? (int)offsetof(Ctl, prg) + lane * 4096 + h * 128 : 0x7ffffff0), [rc] "s"(rs_c), [rd0] "v"(rd[0]), [rd1] "v"(rd[1]), [rd2] "v"(rd[2]), [rd3] "v"(rd[3]), [own] "v"(own), [go] "v"(go), [ro] "v"(lane * 128), [rr] "s"(rs_r), [rq] "s"(rq), [rk] "s"(rk), [lw] "s"(lb0 + w * 40960), [kb] "s"((8 * (w & 1) + 2 * xr + (w >> 1)) * 0x80000) : "memory", "scc");
    }
    asm volatile("" :: "v"(cz));
    { const unsigned long long t0 = rdy_tag(rdy); if ((~__ballot(rdy.x == (unsigned)t0 && rdy.y == (unsigned)(t0 >> 32)) << 4) != 0) kfl = 0; }
    const unsigned long long parq = rdy_wait(P, rdy, tag);
    const int rpl = (int)(parq >> 63) * 8192;
    const __amdgpu_buffer_rsrc_t rs_x = __builtin_amdgcn_make_buffer_rsrc(P.qkvA, 0, QSIZE, 0x00020000);
    const int xk = qoff(parq, 16 + (h >> 1)) + (h & 1) * 4096, xv = qoff(parq, 32 + (h >> 1)) + (h & 1) * 4096;
    const __amdgpu_buffer_rsrc_t rs_k = __builtin_amdgcn_make_buffer_rsrc(P.kvm, 0, NSEG * KVSEG, 0x00020000);
#define PF_ADDR const int tz = opaque_lane()
#define KV_SEGS(v) (min(__builtin_amdgcn_readlane(v, 0), __builtin_amdgcn_readlane(v, 1)) >> 1)
#define WAIT_KV(sg) do { for (unsigned spins = 0;; ++spins) { const unsigned v_ = ld_agent(&P.ctl->prg[(tz & 1) * 32 + h][0]); kvok = KV_SEGS(v_); if (kvok > (sg)) break; \
        if (spin_fail(spins, tmo, 3u)) break; __builtin_amdgcn_s_sleep(8); } } while (0)
    int kvok = 0;
#define LOAD_KV(sg) do { const int so_ = (sg) * 2 * QBLK + w * 1024; \
        _Pragma("unroll") for (int q = 0; q < 4; ++q) \
          asm volatile("s_mov_b32 m0, %0\n\ts_nop 4\n\tbuffer_load_dwordx4 %1, %2, %3 offen sc1 lds" :: "s"(lb0 + w * 40960 + QK_KV + q * 1024), "v"((q < 2 ? tz : tz ^ ((tz >> 1) & 8)) * 16), "s"(rs_x), \
                       "s"(so_ + (q & 1) * QBLK + ((q >> 1) ? xv : xk)) : "memory"); } while (0)
    unsigned pz = 0;
#define LD_ASYNC(dst, voff, rs, soff) asm volatile("s_nop 4\n\tbuffer_load_dword %0, %1, %2, %3 offen sc1" : "=v"(dst) : "v"(voff), "s"(rs), "s"(soff) : "memory")
    {
      PF_ADDR;
      kvok = KV_SEGS(kfl);
      if (kvok <= 0) WAIT_KV(0);
      LOAD_KV(0);
      LD_ASYNC(pz, kvok > 1 && tz < 32 ? tz * 128 : 0x7ffffff0, rs_x, (2 + (w & 1)) * QBLK + ((w >> 1) ? xv : xk));
    }
    const float QS = 0.08838834764831845f * 1.4426950408889634f;
    const unsigned j0 = lq == (l15 >> 2) && !(l15 & 1) ? 0x3f803f80u : 0u, j1 = lq == (l15 >> 2) && (l15 & 1) ? 0x3f803f80u : 0u;
    const v4u JK = {j0, j1, j0, j1};
    const unsigned vxp = lb0 + w * 40960 + QK_KV + 2048 + ((l15 & 3) * 16 + ((4 * lq + (l15 >> 2)) ^ ((l15 & 1) << 3))) * 16;
    const unsigned p1 = lb0 + (w ^ 1) * 40960 + lane * 16, p2 = lb0 + (w ^ 2) * 40960 + lane * 16, p3 = lb0 + (w ^ 3) * 40960 + lane * 16;
    unsigned pot = 0, d1p = 140;
    const int mlo = mem_lane_off - dbl(w * 4096) + (w & 1) * 16384 + (w >> 1) * 1024, hoff = dbl(h * 2048) + w * 512 + lane * 4;
    v4u vm = {0u, 0u, 0u, 0u};
    const int alo = lane < 16 && w == 3 ? ALM + lane * 128 : (lane >= 62 ? RECO + (lane - 62) * 4 : 0x7ffffff0);
    for (int s = 0; s < NSEG; ++s) {
      int mb = RBASE(s) + ROFF_M, vb = RBASE(s) + ROFF_V, ob = RBASE(s) + ROFF_O, mb0 = mb + xr * 2048;
      unsigned al, ann, fl; unsigned long long bad, bad1, b2, b3, po_;
      if (__builtin_expect(s == 1 && w == 3, 0)) {
        unsigned recD; rec_get(tA, recD, rs_m, mb + RECO, tmo); tA -= (recD & 0xffffu) + (recD >> 16);
      }
      { int rem = (int)(tA - rtnow());
        if (s == 0 || rem > 20000) tA -= rem; }
      tA = __builtin_amdgcn_readfirstlane(tA); d1p = __builtin_amdgcn_readfirstlane(d1p);
#define QK_STO
      { unsigned cnt; unsigned long long en;
        asm volatile(QK_STEP
          : "=&{v[136:167]}"(wv_[4]), "=&{v[168:199]}"(wv_[5]), "=&{v[200:231]}"(wv_[6]), "+{v[232:235]}"(vm), [m] "+v"(pz), "+v"(kfl), [al] "=&v"(al),
            [bad] "=&s"(bad), [bad1] "=&s"(bad1), [b2] "=&s"(b2), [b3] "=&s"(b3), [po] "=&s"(po_), [cnt] "=&s"(cnt), [en] "=&s"(en), [ann] "=&s"(ann), [fl] "=&s"(fl) QK_STO
          : "{a[0:31]}"(wa[0]), "{a[32:63]}"(wa[1]), "{a[64:95]}"(wa[2]), "{a[96:127]}"(wa[3]), "{a[128:159]}"(wa[4]), "{a[160:191]}"(wa[5]), "{a[192:223]}"(wa[6]), "{a[224:255]}"(wa[7]),
            "{v[8:39]}"(wv_[0]), "{v[40:71]}"(wv_[1]), "{v[72:103]}"(wv_[2]), "{v[104:135]}"(wv_[3]),
            [own] "v"(own), [p1] "v"(p1), [p2] "v"(p2), [p3] "v"(p3), [vxp] "v"(vxp), [ml0] "v"(mlo), [ml1] "v"(mlo ^ 1024), [jk] "v"(JK), [ho] "v"(hoff), [idx] "v"(idx), [alo] "v"(alo),
            [rsm] "s"(rs_m), [rsc] "s"(rs_c), [mb] "s"(mb), [vb] "s"(vb), [ob] "s"(ob), [rb] "s"(RBASE(s + 2) + ROFF_O), [twin] "s"(0xccccccccccccccccull),
            [one] "s"(0x3f803f80u), [qs] "s"(QS), [qs2] "s"((unsigned long long)__float_as_uint(QS)), [hi] "s"(0x1p40f), [lo] "s"(0x1p-40f), [th] "s"(40.f), [t] "s"(tA), [w] "s"(w), [ald0] "s"(d1p + TP(P.sh0, SH0)),
            [tmo] "s"((int)offsetof(Ctl, tmo)), [mb0] "s"(mb0), [mb1] "s"(mb0 ^ 2048), [mb2] "s"(mb0 ^ 4096), [mb3] "s"(mb0 ^ 6144), [ald] "s"(d1p - TP(P.sh1, SH1)), [als] "s"(mb + ALM + (alw & 15) * 128), [alx] "s"(alx)
          : "vcc", "scc", "s48", "s49", "s50", "s51", "s52", "s53", "memory"); }
      {
        const v4u Km = {wv_[4][QK_KM], wv_[4][QK_KM + 1], wv_[4][QK_KM + 2], wv_[4][QK_KM + 3]};
        if (__builtin_expect(bad != 0, 0)) {
#pragma unroll 1
          for (unsigned fails = 0; !__all((vm.x > vm.z ? vm.x : vm.z) != 0xffffffffu); ++fails) {
            __builtin_amdgcn_s_sleep(2);
            vm.x = __builtin_amdgcn_raw_buffer_load_b32(rs_m, hoff, vb, 16); vm.z = __builtin_amdgcn_raw_buffer_load_b32(rs_m, hoff, vb + 256, 16);
            if (spin_fail(fails, tmo, 1u)) break;
          }
          float sx[4], sm[2];
#pragma unroll
          for (int e = 0; e < 4; ++e) sx[e] = __uint_as_float(wv_[6][QK_SX + e]);
          sm[0] = __uint_as_float(wv_[4][QK_SM]); sm[1] = __uint_as_float(wv_[4][QK_SM + 1]);
          float mx = fmaxf(fmaxf(sx[0], sx[1]), fmaxf(sx[2], sx[3]));
          mx = fmaxf(mx, __int_as_float(__builtin_amdgcn_update_dpp(0, __float_as_int(mx), 0x4E, 0xf, 0xf, false)));
          mx = xmax32(xmax16(fmaxf(mx, fmaxf(sm[0], sm[1]))));
          if (b2 == 0) mx = 0.f;
          v4u px; px.x = pk2(__builtin_amdgcn_exp2f(sx[0] - mx), __builtin_amdgcn_exp2f(sx[1] - mx)); px.y = pk2(__builtin_amdgcn_exp2f(sx[2] - mx), __builtin_amdgcn_exp2f(sx[3] - mx));
          px.z = __builtin_amdgcn_update_dpp(0, px.x, 0x4E, 0xf, 0xf, false); px.w = __builtin_amdgcn_update_dpp(0, px.y, 0x4E, 0xf, 0xf, false);
          const v2u pm = {pk2(__builtin_amdgcn_exp2f(sm[0] - mx), __builtin_amdgcn_exp2f(sm[1] - mx)), 0u};
          const v4u on = {0x3f803f80u, 0x3f803f80u, 0x3f803f80u, 0x3f803f80u};
          const v4u vx0 = {wv_[6][QK_VX], wv_[6][QK_VX + 1], wv_[6][QK_VX + 2], wv_[6][QK_VX + 3]}, vx1 = {wv_[6][QK_VX + 4], wv_[6][QK_VX + 5], wv_[6][QK_VX + 6], wv_[6][QK_VX + 7]};
          f32x4 c0, c1, ls;
          asm volatile("s_nop 1\n\t"
                       "v_mfma_f32_16x16x32_bf16 %0, %3, %4, 0\n\t"
                       "v_mfma_f32_16x16x32_bf16 %1, %3, %5, 0\n\t"
                       "v_mfma_f32_16x16x32_bf16 %2, %3, %6, 0\n\t"
                       "s_nop 7\n\t"
                       "v_mfma_f32_16x16x16_bf16 %0, %7, v[232:233], %0\n\t"
                       "v_mfma_f32_16x16x16_bf16 %1, %7, v[234:235], %1\n\t"
                       "v_mfma_f32_16x16x16_bf16 %2, %7, v[168:169], %2\n\t"
                       "s_nop 7"
                       : "=&v"(c0), "=&v"(c1), "=&v"(ls) : "v"(px), "v"(vx0), "v"(vx1), "{v[168:171]}"(on), "v"(pm), "{v[232:235]}"(vm));
          const float i0 = __builtin_amdgcn_rcpf(ls[0]), i1 = __builtin_amdgcn_rcpf(ls[1]);
          __builtin_amdgcn_raw_buffer_store_b32(nosent(pk2(c0[0] * i0, c1[0] * i0)), rs_m, hoff, ob, 16);
          __builtin_amdgcn_raw_buffer_store_b32(nosent(pk2(c0[1] * i1, c1[1] * i1)), rs_m, hoff + 256, ob, 16);
          po_ = rtnow();
        }
        SB0;
        const unsigned po = __builtin_amdgcn_readfirstlane((unsigned)po_);
        fl = __builtin_amdgcn_readfirstlane(fl | (bad != 0));
        if (QGAP) __builtin_amdgcn_s_sleep(QGAP);
        SB0;
        __builtin_amdgcn_raw_buffer_store_b32(po - tA, rs_m, (h | (w ^ 3) | opaque_lane() | fl) == 0 ? RECO + 128 : 0x7ffffff0, mb, 16);
      { const int lz = opaque_lane(), kb = (KVFAKE && s < SSEG0 ? s & 1 : s) * KVSEG + h * 4096 + w * 512, rb = RBASE(s + 2);
        __builtin_amdgcn_raw_buffer_store_b32(vm.x, rs_k, lz * 4 + 2048, kb, KVAUX); __builtin_amdgcn_raw_buffer_store_b32(vm.z, rs_k, lz * 4 + 2304, kb, KVAUX);
        __builtin_amdgcn_raw_buffer_store_b128((v4u){nosent(Km.x), nosent(Km.y), nosent(Km.z), nosent(Km.w)}, rs_k, (lz & 2) ? 0x7ffffff0 : (2 * (lz >> 2) + (lz & 1)) * 16, kb, KVAUX);
        __builtin_amdgcn_raw_buffer_store_b128((v4u){~0u, ~0u, ~0u, ~0u}, rs_m, (alw & 3) != 0 ? 0x7ffffff0 : lz < 32 ? ROFF_M + ALM + alw * 128 + lz * 16 : (lz == 32 && b == 0 ? ROFF_M + RECO + 128 : 0x7ffffff0), rb, 16); }
        asm volatile("s_waitcnt vmcnt(%1)" : "+v"(al) : "i"(QK_ALW + 4) : "memory");
        pot = po;
      if (s < NSEG - 1) {
        unsigned recD = __builtin_amdgcn_readlane(al, 63);
        tA = __builtin_amdgcn_readlane(al, 62);
        if (s > 0) {
          if (__builtin_expect(tA == 0xffffffffu || recD == 0xffffffffu, 0)) rec_get(tA, recD, rs_m, mb + RECO, tmo);
          d1p = recD & 0xffffu;
          unsigned tB = tA - (recD >> 16);
          al_eval(al, tB);
          if (__builtin_expect((int)(pot + TP(P.sh3, SH3) - tB) > 0, 0)) tB = pot + TP(P.sh3, SH3);
          tA = tB + (recD >> 16);
        } else tA = pot;
        PF_ADDR;
        kvok = max(kvok, KV_SEGS(kfl));
        if (__builtin_expect(kvok <= s + 1, 0)) WAIT_KV(s + 1);
        LOAD_KV(s + 1);
        LD_ASYNC(kfl, kvok < s + 5 && kvok < NSEG && tz < 2 ? tz * 4096 : 0x7ffffff0, rs_c, (int)offsetof(Ctl, prg) + h * 128);
        const int po2 = s + 2 < kvok && !(tz & 32) ? tz * 128 : 0x7ffffff0;
        LD_ASYNC(pz, po2, rs_x, (2 * (s + 2) + (w & 1)) * QBLK + ((w >> 1) ? xv : xk));
      }
      }
    }
    check_in(P, tag, w);
  } else {
    LANE_CONSTS;
    const unsigned c00 = (unsigned)now(), t00 = rtnow();
    const int c = b - NQK, cg = c >> 1, rh = c & 1;
    v32u wa[8], wv_[4];
#define VCH(j) (8 * (w & 1) + 2 * ((j) ^ xq) + (w >> 1))
#define OHEAD(i) (4 * ((i) ^ (2 * xq)) + w)
    {
      const unsigned stg = (unsigned)(size_t)Lraw + w * 32768;
      const unsigned vb = stg + wrow * 128 + (l15 & 3) * 8 + ((w ^ 3 ^ wsw) << 5);
      const __amdgpu_buffer_rsrc_t rv = __builtin_amdgcn_make_buffer_rsrc((void*)(P.Wv + 64 * c), 0, DM * HDA * 2 - 128 * c, 0x00020000);
      int t_, ho, n_;
      asm volatile(VO_LOADER : VO_DEFW, [t] "=&s"(t_), [ho] "=&s"(ho), [n] "=&s"(n_), [rec] "=&v"(rdy)
        : [sv] "v"((lane >> 3) * HDA * 2 + (((((lane & 7) >> 1) ^ ((lane >> 4) & 3)) << 1) | (lane & 1)) * 16), [so] "v"((lane >> 3) * DM * 2 + (lane & 7) * 16), [vb0] "v"(vb), [vb1] "v"(vb ^ 32), [vb2] "v"(vb ^ 64), [vb3] "v"(vb ^ 96),
          [ob] "v"(stg + (32 * (lq >> 1) + 2 * (lq & 1) + 4 * (l15 >> 3) + 16 * ((l15 >> 2) & 1)) * 128 + (cg & 1) * 64 + (l15 & 3) * 8),
          [ro] "v"(lane * 128), [rr] "s"(rs_r), [stg] "s"(stg), [rv] "s"(rv), [vk] "s"(VCH(0) * 0x80000), [wo] "s"(P.Wo + 64 * (cg >> 1)), [wsz] "s"(HDA * DM * 2 - 128 * (cg >> 1)), [hx] "s"(OHEAD(0) * 0x40000), [k0] "s"(c & 3)
        : "s48", "s49", "s50", "s51", "s52", "s53", "scc", "memory");
    }
    asm volatile("" :: "v"(cz));
    const int rpl = (int)(rdy_wait(P, rdy, tag) >> 63) * 8192;
    const unsigned lb0 = (unsigned)(size_t)Lraw;
    unsigned vw[3];
#pragma unroll
    for (int t = 0; t < 3; ++t) vw[t] = lb0 + t * 2064 + ((t ^ w ^ 3) * 64 + lane) * 8;
    const unsigned vr = lb0 + tid * 8, rw = lb0 + 8192 + tid * 8, fa = lb0 + 16384;
    int oo[8];
#pragma unroll
    for (int i = 0; i < 8; ++i) oo[i] = dbl(OHEAD(i) * 2048) + ((lq * 2 + rh) * 16 + l15) * 16;
    int ga[4];
#pragma unroll
    for (int j = 0; j < 4; ++j) ga[j] = mem_lane_off - dbl(w * 4096) + dbl(VCH(j) * 1024);
    const int vo = dbl(c * 1024) + tid * 4, mo = dbl(c * 256) + (32 * (l15 >> 3) + 8 * lq + (l15 & 7)) * 4;
    const bool keeper = (c == 0 && w == 0);
    int d1 = TP(P.dmv, DMV) * 16, d2 = TP(P.dov, DOV) * 16, m1 = TP(P.im, IMARG) * 16, m2 = TP(P.im, IMARG) * 16;
    int LV = 70;
    bool prev_clean = false; int nbad = 0;
    unsigned recD = 140;
    const float tpc = 16.f * (float)(rtnow() - t00) / (float)((unsigned)now() - c00);
    int e1 = __builtin_amdgcn_readfirstlane((int)(TP(P.dmv, DMV) * tpc)), e2 = __builtin_amdgcn_readfirstlane((int)(TP(P.dov, DOV) * tpc));
    d1 = e1 + m1 + TP(P.dq2, DQ2) * 16; d2 = e2 + m2 + TP(P.dq, DQ) * 16;
    if (tid < 32) ((int*)(Lraw + 16384))[tid] = 0;
    const int alk = !keeper ? 0x7ffffff0 : (lane < 24 ? ALM + lane * 128 : (lane == 63 ? RECO + 128 : 0x7ffffff0));
    const int al1 = lane < 16 ? ALM + lane * 128 : 0x7ffffff0;
    const int rso = w == 1 ? ROFF_V + dbl(c * 1024) + lane * 16 : (w != 2 ? 0x7ffffff0 : (lane < 16 ? ROFF_M + dbl(c * 256) + lane * 16 :
                    (lane >= 32 && alw >= 0 ? ROFF_M + ALM + (alw - 6) * 128 + lane * 16 : (lane == 16 && c == 0 ? ROFF_M + RECO : 0x7ffffff0))));
    const int boot = c == 0 ? TP(P.pad, BOOT) & 255 : TP(P.pad, BOOT) >> 8;
    unsigned tB = 0; int mb = RBASE(0) + ROFF_M, ob = 0;
    for (int s = 0; s < NSEG; ++s) {
      unsigned alr, pm, fw; unsigned long long te; v2u rec;
      const int mbn = RBASE(s) + ROFF_M, d2r = recD >> 16;
#define VO_ST0
#define VO_ST1
#define VO_ST3
#define VO_STO
      { unsigned cnt; unsigned long long en;
        asm volatile(VO_BODY : [al] "=&v"(alr), [rec] "=&v"(rec), [te] "=&s"(te), [cnt] "=&s"(cnt), [en] "=&s"(en), [t] "+s"(tB), [ta] "=&s"(tA), [pm] "=&s"(pm), [fw] "=&s"(fw) VO_STO
          : VO_W, [oo0] "v"(oo[0]), [oo1] "v"(oo[1]), [oo2] "v"(oo[2]), [oo3] "v"(oo[3]), [oo4] "v"(oo[4]), [oo5] "v"(oo[5]), [oo6] "v"(oo[6]), [oo7] "v"(oo[7]), [idx] "v"(idx), [rw] "v"(rw), [mo] "v"(mo), [fa] "v"(fa), [l4] "v"(lane * 4), [al1] "v"(al1),
            [ga0] "v"(ga[0]), [ga1] "v"(ga[1]), [ga2] "v"(ga[2]), [ga3] "v"(ga[3]), [alo] "v"(alk), [vw0] "v"(vw[0]), [vw1] "v"(vw[1]), [vw2] "v"(vw[2]), [vr] "v"(vr), [vo] "v"(vo), [rso] "v"(rso), [rb] "s"(RBASE(s + 2)),
            [w] "s"(w), [rsm] "s"(rs_m), [rsc] "s"(rs_c), [ob] "s"(ob), [mb] "s"(mb), [mbn] "s"(mbn), [vb] "s"(RBASE(s) + ROFF_V), [rco] "s"(mbn + RECO), [tmo] "s"((int)offsetof(Ctl, tmo)), [s1] "s"(s),
            [lk] "s"(TP(P.lk1, LK1)), [als] "s"(mb + ALM + (alw & 31) * 128), [alx] "s"(alx), [dv] "s"(TP(P.dv, DVV)),
            [d2] "s"(d2r), [mn] "s"(s == 1 ? boot : 10), [shf] "s"(s == 1 ? boot : TP(P.sh4, SH4)), [c] "s"(c), [pw] "s"(__builtin_amdgcn_readfirstlane(s == 0 ? 1 : w ^ 3))
          : VO_CLOB, "vcc", "scc", "memory"); }
      const unsigned tv = __builtin_amdgcn_readfirstlane((unsigned)te);
      if (c == 2 && tid == 0 && s > 0) __hip_atomic_store(&P.ctl->prog[0], (unsigned)(s - 1), RLX_AGENT);
      if (keeper && s > 0) {
        const v4u kz = *(const v4u*)(Lraw + 16384 + 144); pm = kz.x; tB = kz.y; fw = kz.z; tA = kz.w;
        const unsigned long long f = __ballot((alr >> 16) == 1u);
        const bool kclean = __builtin_amdgcn_readfirstlane(fw) != s, okA = (f & 0xffff) == 0, okB = kclean && ((f >> 16) & 255) == 0;
        const int kx1 = __builtin_amdgcn_readlane(alr, 63), kx2 = __builtin_amdgcn_readfirstlane(pm - tB);
        {
          const int gain = s < 2 ? 0 : (s < 5 ? TP(P.gain0, GAIN0) : TP(P.gain, GAIN)), inc = s < 2 ? 0 : TP(P.inc, MINC);
          e1 += okA && kx1 != -1 ? (min(kx1 * 16 - e1, 320) * gain) >> 4 : 0;
          e2 += kclean ? (min(kx2 * 16 - e2, 320) * gain) >> 4 : 0;
          nbad = okA && okB ? 0 : nbad + 1;
          const int far = nbad >= 4 ? 2 * inc : 0;
          m1 = min(max(m1 + far + (!okA ? 0 : (okB ? -TP(P.dec, MDEC) - (m1 >> 3) : inc)), 0), 800);
          m2 = min(max(m2 + far + (okA ? -TP(P.dec, MDEC) - (m2 >> 3) : (prev_clean ? inc : 0)), 0), 800);
          d1 = e1 + m1 + TP(P.dq2, DQ2) * 16; d2 = e2 + m2 + TP(P.dq, DQ) * 16;
        }
        prev_clean = okB;
      }
      if (keeper) {
        const unsigned r1 = (d1 >> 4) + (s < 2 ? (TP(P.boot2, BOOT2) >> (8 * s)) & 255 : 0), r2 = (d2 >> 4) + (s < 2 ? (TP(P.boot3, BOOT3) >> (8 * s)) & 255 : 0);
        *(v2u*)(Lraw + 16384 + 128) = (v2u){r1 + r2, r1 | (r2 << 16)};
      }
      tB = tv; recD = 0;
      if (s > 0 && w == 3) {
        unsigned recA = __builtin_amdgcn_readfirstlane(rec.x); recD = __builtin_amdgcn_readfirstlane(rec.y);
        if (__builtin_expect(recA == 0xffffffffu || recD == 0xffffffffu, 0)) rec_get(recA, recD, rs_m, mbn + RECO, tmo);
        tB = recA - (recD >> 16);
        if ((int)(tB - tv) > 20000) tB = tv;
      }
      tB = __builtin_amdgcn_readfirstlane(tB); recD = __builtin_amdgcn_readfirstlane(recD);
      mb = mbn; ob = RBASE(s) + ROFF_O;
    }
    check_in(P, tag, w);
  }
}

extern "C" void kernel_launch(void* const* d_in, const int* in_sizes, int n_in,
                              void* d_out, int out_size, void* d_ws, size_t ws_size,
                              hipStream_t stream) {
  const u16* A_x = (const u16*)d_in[0];
  const u16* A_mem0 = (const u16*)d_in[1];
  const u16* A_Wq = (const u16*)d_in[2];
  const u16* A_Wk = (const u16*)d_in[3];
  const u16* A_Wv = (const u16*)d_in[4];
  const u16* A_Wo = (const u16*)d_in[5];
  const u16* B_x = (const u16*)d_in[7];
  const u16* B_wqkv = (const u16*)d_in[8];
  const u16* B_wo = (const u16*)d_in[9];
  u16* out = (u16*)d_out;

  char* ws = (char*)d_ws;
  size_t off = 0;
  auto alloc = [&](size_t bytes) { char* p = ws + off; off += (bytes + 255) & ~(size_t)255; return p; };
  char* rdy = alloc(RDY_SIZE);
  Ctl* ctl = (Ctl*)alloc(sizeof(Ctl));
  unsigned long long* prof = (unsigned long long*)alloc(96 * 4 * 72 * 8 + 8 * 256 * 16 + 64 * 384 * 8);
  char* kvm = alloc((size_t)NSEG * KVSEG);
  off += (size_t)(-(long long)(size_t)(ws + off)) & (QPAGE - 1);
  char* ring = alloc(QPAGE);
  u16* qkvA = (u16*)alloc(QSIZE);

  FParams cp;
  cp.Ax = A_x; cp.mem0 = A_mem0; cp.Bx = B_x; cp.Wq = A_Wq; cp.Wk = A_Wk; cp.Wv = A_Wv; cp.Wo = A_Wo; cp.wqkvB = B_wqkv; cp.woB = B_wo;
  cp.qkvA = qkvA; cp.out = out; cp.kvm = kvm; cp.ring = ring; cp.rdy = rdy;
  cp.ctl = ctl; cp.prof = prof;
  k_fused<<<256, 256, 0, stream>>>(cp);
}
```

```cpp
#include <hip/hip_runtime.h>
#include <hip/hip_bf16.h>
#include <stdint.h>
#include <stddef.h>

#define DQ 12
#define DQ2 11
#define DMV 2440
#define DOV 1410
#define GAIN 4
#define GAIN0 8
#define MINC 32
#define MDEC 3
#define STAG 600
#define SH1 14
#define PTH 620
#define SH3 6
#define DVV 0
#define BOOT 0x2850
#define PXV (600 * 65536 + 32)
#define PBV (1800 * 65536 + 3)
#define PROBES 6
#define JB 80
#define BOOT2 0x0505
#define BOOT3 0x0a14
#define IMARG 3
#define PFR 0
#define OJ1 8
#define LK1 34
#define LK4 38
#define SH0 10
#define SH4 24
#define XA1 26
#define XA2 14
#define VSLOT 60
#define TP(run, fixed) (fixed)
typedef unsigned short u16;
typedef __attribute__((ext_vector_type(4))) float f32x4;
typedef unsigned v4u __attribute__((ext_vector_type(4)));

constexpr int S_ = 2048, DM = 1024, HA = 32, DHA = 128, HDA = 4096, MEM = 8, SEG = 32, NSEG = 64;
constexpr int QPAGE = 2 << 20, QBLK = 16384, NPAGE = 60, QSIZE = (NPAGE + 8) * QPAGE;
__device__ __forceinline__ int qoff(unsigned long long parq, int n) { return n * QPAGE + (int)((parq >> n) & 1) * 8192; }
__device__ __forceinline__ int kvoff(unsigned pk, int h) { return (NPAGE + (h >> 2)) * QPAGE + (int)((pk >> (h >> 2)) & 1) * 8192 + ((h >> 1) & 1) * QBLK + (h & 1) * 4096; }
constexpr int DB = 1024, HB = 32, DHB = 32, NQKVB = 3 * DB;

constexpr int ROFF_O = 0, ROFF_V = 131072, ROFF_M = 262144, RSLOT = 327680, RSIZE = 4 * RSLOT;
typedef __attribute__((ext_vector_type(8))) __bf16 bfx8;
typedef __attribute__((ext_vector_type(2))) __bf16 bfx2;
typedef __attribute__((ext_vector_type(2))) float f32x2;
typedef unsigned v2u __attribute__((ext_vector_type(2)));
typedef unsigned v8u __attribute__((ext_vector_type(8)));
typedef unsigned v32u __attribute__((ext_vector_type(32)));
constexpr int NQK = 32, NVO = 64;
constexpr int VXLD = 144, LDS_ALL = 163840;
template <int I> struct IC { static constexpr int v = I; };
template <int I, int N, class F> __device__ __forceinline__ void sfor(F&& f) { if constexpr (I < N) { f(IC<I>{}); sfor<I + 1, N>(f); } }
#define SFOR(u, a, b) sfor<a, b>([&](auto u##_) __attribute__((always_inline)) { constexpr int u = decltype(u##_)::v;
#define SEND });

__device__ __forceinline__ void lds_barrier() { asm volatile("s_waitcnt lgkmcnt(0)" ::: "memory"); __builtin_amdgcn_s_barrier(); asm volatile("" ::: "memory"); }
template <int N> __device__ __forceinline__ float dpp_ror(float v) {
  return __int_as_float(__builtin_amdgcn_update_dpp(0, __float_as_int(v), 0x120 + N, 0xf, 0xf, false));
}
__device__ __forceinline__ unsigned pk2(float a, float b) { f32x2 t = {a, b}; return __builtin_bit_cast(unsigned, __builtin_convertvector(t, bfx2)); }
__device__ __forceinline__ unsigned nosent(unsigned x) { return x < 0xfffffffeu ? x : 0xfffffffeu; }
__device__ __forceinline__ unsigned umax3(unsigned a, unsigned b, unsigned c) { unsigned m = a > b ? a : b; return m > c ? m : c; }
__device__ __forceinline__ unsigned chk4(unsigned m, v4u v) { return umax3(umax3(m, v.x, v.y), v.z, v.w); }
__device__ __forceinline__ unsigned long long now() { return __builtin_amdgcn_s_memtime(); }
__device__ __forceinline__ f32x4 mf(v4u a, v4u b, f32x4 c) {
  return __builtin_amdgcn_mfma_f32_16x16x32_bf16(__builtin_bit_cast(bfx8, a), __builtin_bit_cast(bfx8, b), c, 0, 0, 0);
}
__device__ __forceinline__ v2u trrd(const u16* p) {
  typedef __attribute__((ext_vector_type(4))) short v4i16_t;
  return __builtin_bit_cast(v2u, __builtin_amdgcn_ds_read_tr16_b64_v4i16((__attribute__((address_space(3))) v4i16_t*)p));
}
__device__ __forceinline__ v8u cat8(v4u a, v4u b) { return __builtin_shufflevector(a, b, 0, 1, 2, 3, 4, 5, 6, 7); }
__device__ __forceinline__ float xmax16(float x) { auto r = __builtin_amdgcn_permlane16_swap(__float_as_uint(x), __float_as_uint(x), false, false); return fmaxf(__uint_as_float(r[0]), __uint_as_float(r[1])); }
__device__ __forceinline__ float xmax32(float x) { auto r = __builtin_amdgcn_permlane32_swap(__float_as_uint(x), __float_as_uint(x), false, false); return fmaxf(__uint_as_float(r[0]), __uint_as_float(r[1])); }
__device__ __forceinline__ float xsum16(float x) { auto r = __builtin_amdgcn_permlane16_swap(__float_as_uint(x), __float_as_uint(x), false, false); return __uint_as_float(r[0]) + __uint_as_float(r[1]); }
__device__ __forceinline__ float xsum32(float x) { auto r = __builtin_amdgcn_permlane32_swap(__float_as_uint(x), __float_as_uint(x), false, false); return __uint_as_float(r[0]) + __uint_as_float(r[1]); }
__device__ __forceinline__ int dbl(int off) { return ((off >> 13) << 14) | (off & 8191); }
__device__ __forceinline__ bool spin_fail(unsigned spins, unsigned* tmo, unsigned code) {
  if ((spins & 31u) != 31u) return false;
  if (spins > (1u << 15)) { __hip_atomic_store(tmo, code, __ATOMIC_RELAXED, __HIP_MEMORY_SCOPE_AGENT); return true; }
  return __builtin_amdgcn_readfirstlane(__hip_atomic_load(tmo, __ATOMIC_RELAXED, __HIP_MEMORY_SCOPE_AGENT)) != 0u;
}
#define SB0 __builtin_amdgcn_sched_barrier(0)
#define NTQ 0
#define QGAP 0
#define PSL 48
#define TSL 2
__device__ __forceinline__ int opaque_lane() { int l; asm volatile("v_mbcnt_lo_u32_b32 %0, -1, 0\n\tv_mbcnt_hi_u32_b32 %0, -1, %0" : "=v"(l)); return l & 63; }

#define OUTAUX 16
namespace wg {
constexpr int LDS_USE = 131072;
__device__ __forceinline__ void* uni(const void* p) { const unsigned long long x = (unsigned long long)p; return (void*)(((unsigned long long)(unsigned)__builtin_amdgcn_readfirstlane(x >> 32) << 32) | (unsigned)__builtin_amdgcn_readfirstlane(x)); }
template <int MT>
__device__ __forceinline__ void gemm_tile(const u16* A, int lda, const u16* B, int ldb, int nst  , void* C, int ldc, unsigned long long parq  , char* lds, int w) {
  constexpr bool apack = true, cpack = false;
  constexpr int MI = MT / 32, PA = MT / 64, NP = PA + 4;
  constexpr int A_B = MT * 64, ST = A_B + 16384;
  const int lane = opaque_lane();
  const int l15 = lane & 15, lq = lane >> 4, wm = w >> 1, wn = w & 1;
  lda = __builtin_amdgcn_readfirstlane(lda); ldb = __builtin_amdgcn_readfirstlane(ldb); ldc = __builtin_amdgcn_readfirstlane(ldc); nst = __builtin_amdgcn_readfirstlane(nst);
  f32x4 acc[8][MI];
#pragma unroll
  for (int i = 0; i < 8; ++i)
#pragma unroll
    for (int j = 0; j < MI; ++j) acc[i][j] = (f32x4){0.f, 0.f, 0.f, 0.f};
  const unsigned lb = (unsigned)(size_t)lds;
  auto fsw = [](int x) { return ((x ^ (x >> 1)) & 1) << 1 | (x >> 1); };
  unsigned ao[2], bo[2][8];
  ao[0] = lb + wm * (MT / 2) * 64 + (apack ? lane * 16 : l15 * 64 + ((lq ^ fsw(l15 >> 2)) << 4)); ao[1] = ao[0] + 2 * ST;
  { const int hh = (l15 >> 2) | ((lq & 1) << 2);
#pragma unroll
    for (int i = 0; i < 8; ++i) { bo[0][i] = lb + A_B + (8 * lq + (l15 >> 2)) * 512 + ((16 * wn + ((2 * i) ^ (2 * hh)) + ((l15 >> 1) & 1)) << 4) + (l15 & 1) * 8; bo[1][i] = bo[0][i] + 2 * ST; } }
  __amdgpu_buffer_rsrc_t ra = __builtin_amdgcn_make_buffer_rsrc(uni(A), 0, 0x7fffffff, 0x00020000), rb = __builtin_amdgcn_make_buffer_rsrc(uni(B), 0, 0x7fffffff, 0x00020000);
  const int va = (16 * PA * w * lda + (apack ? lane * 8 : (lane >> 2) * lda + (((lane & 3) ^ fsw(lane >> 4)) << 3))) * 2, ska = __builtin_amdgcn_readfirstlane(apack ? 1024 : 64);
  const int vb0 = ((8 * w + (lane >> 5)) * ldb + (((lane & 31) ^ ((lane >> 5) << 1) ^ ((w & 1) << 3)) << 3)) * 2, vb1 = vb0 ^ 64;
  const int sa = 32 * lda, sb = 4 * ldb, skb = 64 * ldb;
  const unsigned ma = lb + PA * w * 1024, mb = lb + A_B + w * 4096;
#define PIECE1(NT, base, imm, vo, rs, so) do { if constexpr (NT) asm volatile("s_add_u32 m0, %0, %1\n\ts_nop 0\n\tbuffer_load_dwordx4 %2, %3, %4 offen nt lds" :: "s"(base), "i"(imm), "v"(vo), "s"(rs), "s"(so) : "memory", "scc"); \
    else asm volatile("s_add_u32 m0, %0, %1\n\ts_nop 0\n\tbuffer_load_dwordx4 %2, %3, %4 offen lds" :: "s"(base), "i"(imm), "v"(vo), "s"(rs), "s"(so) : "memory", "scc"); } while (0)
#define PIECE(q, st) do { \
    if ((q) < PA) PIECE1(false, ma, (st) * ST + (q) * 1024, va, ra, ka + (q) * sa); \
    else PIECE1(false, mb, (st) * ST + ((q) - PA) * 1024, (((q) - PA) & 1) ? vb1 : vb0, rb, kb + ((q) - PA) * sb); } while (0)
#define RDA(f, j, st) asm volatile("ds_read_b128 %0, %1 offset:%2" : "=v"(fa[f][j]) : "v"(ao[(st) >> 1]), "i"(((st) & 1) * ST + (j) * 1024) : "memory")
#define RDB(f, i, h, st) asm volatile("ds_read_b64_tr_b16 %0, %1 offset:%2" : "=v"(fb[f][i][h]) : "v"(bo[(st) >> 1][i]), "i"(((st) & 1) * ST + (h) * 2048) : "memory")
#define TIE(f) do { _Pragma("unroll") for (int j = 0; j < MI; ++j) asm volatile("" : "+v"(fa[f][j])); \
    _Pragma("unroll") for (int i = 0; i < 8; ++i) asm volatile("" : "+v"(fb[f][i][0]), "+v"(fb[f][i][1])); } while (0)
  v4u fa[2][MI]; v2u fb[2][8][2];
  int ka = qoff(parq, 0), kb = 0;
  asm volatile("s_nop 4" :: "s"(ra), "s"(rb), "s"(sa), "s"(sb), "s"(skb), "s"(ska));
#pragma unroll
  for (int st = 0; st < 4; ++st) {
#pragma unroll
    for (int q = 0; q < NP; ++q) PIECE(q, st);
    ka += ska; kb += skb;
  }
  asm volatile("s_waitcnt vmcnt(%0)\n\ts_barrier" :: "i"(3 * NP) : "memory");
#pragma unroll
  for (int i = 0; i < 8; ++i) { RDB(0, i, 0, 0); RDB(0, i, 1, 0); }
#pragma unroll
  for (int j = 0; j < MI; ++j) RDA(0, j, 0);
  int k4 = 0;
  do {
    if (apack && (k4 & 4)) ka = qoff(parq, (k4 + 4) >> 3);
    if (k4 + 4 >= nst) { ra = __builtin_amdgcn_make_buffer_rsrc(uni(C), 0, 0, 0x00020000); rb = ra; }
#pragma unroll
    for (int st = 0; st < 4; ++st) {
      constexpr int NM = 8 * MI, NR = 16 + MI;
      const int f = st & 1, sn = (st + 1) & 3;
      asm volatile("s_waitcnt vmcnt(%0) lgkmcnt(0)\n\ts_barrier" :: "i"(2 * NP) : "memory");
      TIE(f);
      SB0;
#pragma unroll
      for (int u = 0; u < NM; ++u) {
        const int j = u >> 3, i = u & 7;
        asm volatile("v_mfma_f32_16x16x32_bf16 %0, %1, %2, %0" : "+a"(acc[i][j]) : "v"((v4u){fb[f][i][0].x, fb[f][i][0].y, fb[f][i][1].x, fb[f][i][1].y}), "v"(fa[f][j]));
        SB0;
        constexpr int RS = MT == 256 ? 2 : 1;
        if (u % RS == 0 && u / RS < NR) {
          const int r = u / RS;
          if (r < 16) RDB(f ^ 1, r >> 1, r & 1, sn); else RDA(f ^ 1, r - 16, sn);
          if (MT == 64 && (u == 0 || u == 2)) RDA(f ^ 1, u >> 1, sn);
          SB0;
        }
        constexpr int PS = NM / 8;
        if (u % PS == PS - 1 && u / PS < NP) { PIECE(u / PS, st); SB0; }
      }
      ka += ska; kb += skb;
    }
    k4 += 4;
  } while (k4 < nst);
#undef PIECE
#undef PIECE1
#undef RDA
#undef RDB
  asm volatile("s_nop 7\n\ts_waitcnt lgkmcnt(0)" ::: "memory");
  SB0;
  TIE(0);
#undef TIE
  char* st = lds + LDS_USE + 2048 + w * 4096;
  const __amdgpu_buffer_rsrc_t rs = __builtin_amdgcn_make_buffer_rsrc(uni(C), 0, MT * ldc * 2 - 1536 * 2, 0x00020000);
  const int so = wm * (MT / 2) * ldc * 2 + (cpack ? wn * 4096 + lane * 16 : (lq * ldc + wn * 128 + l15 * 8) * 2), sq = cpack ? 1024 : 8 * ldc;
#pragma unroll
  for (int j = 0; j < MI; ++j) {
    v2u p[8]; v4u v[4];
#pragma unroll
    for (int i = 0; i < 8; ++i) { asm volatile("" : "+a"(acc[i][j])); p[i] = (v2u){pk2(acc[i][j][0], acc[i][j][1]), pk2(acc[i][j][2], acc[i][j][3])}; }
#pragma unroll
    for (int q = 0; q < 4; ++q) { v[q] = (v4u){p[2 * q].x, p[2 * q].y, p[2 * q + 1].x, p[2 * q + 1].y}; asm volatile("" : "+v"(v[q])); }
    if (!cpack) {
#pragma unroll
      for (int i = 0; i < 8; ++i) *(v2u*)(st + l15 * 256 + (((2 * i + (lq >> 1)) ^ l15) << 4) + (lq & 1) * 8) = (i & 1) ? (v2u){v[i >> 1].z, v[i >> 1].w} : (v2u){v[i >> 1].x, v[i >> 1].y};
      asm volatile("" ::: "memory");
#pragma unroll
      for (int q = 0; q < 4; ++q) v[q] = *(const v4u*)(st + (4 * q + lq) * 256 + ((l15 ^ (4 * q + lq)) << 4));
      asm volatile("" ::: "memory");
    }
#pragma unroll
    for (int q = 0; q < 4; ++q) {
      __builtin_amdgcn_raw_buffer_store_b128(v[q], rs, so, 32 * j * ldc + q * sq, OUTAUX);
    }
    SB0;
  }
  asm volatile("s_waitcnt vmcnt(0)" ::: "memory");
  __builtin_amdgcn_s_barrier();
}
}

struct Ctl {
  unsigned qhead[32];
  unsigned prg[192][32];
  unsigned cntAB[16][32];
  unsigned flagO[72][8][32];
  alignas(8) unsigned prog[32];
  unsigned tmo[32];
  unsigned chk[32];
};
struct FParams {
  const u16 *Ax, *mem0, *Bx, *Wq, *Wk, *Wv, *Wo, *wqkvB, *woB;
  u16 *qkvA, *out; char *ring, *rdy;
  Ctl* ctl; unsigned long long* prof;
};
#define RLX_AGENT __ATOMIC_RELAXED, __HIP_MEMORY_SCOPE_AGENT
__device__ __forceinline__ unsigned ld_agent(const unsigned* p) { return __hip_atomic_load(p, RLX_AGENT); }
constexpr int RDY_SIZE = 64 * 128;
__device__ __forceinline__ v4u rdy_load(const FParams& F) { const int lz = opaque_lane(); return __builtin_amdgcn_raw_buffer_load_b128(__builtin_amdgcn_make_buffer_rsrc(F.rdy, 0, RDY_SIZE, 0x00020000), lz * 128, 0, 16); }
__device__ __forceinline__ unsigned long long rdy_tag(v4u r) { return (((unsigned long long)(unsigned)__builtin_amdgcn_readlane(r.y, 63) << 32) | (unsigned)__builtin_amdgcn_readlane(r.x, 63)) + 1; }
__device__ __forceinline__ unsigned long long rdy_wait(const FParams& F, v4u r, unsigned long long& tag, unsigned& pk) {
  tag = rdy_tag(r);
  for (unsigned spins = 0; (~__ballot(r.x == (unsigned)tag && r.y == (unsigned)(tag >> 32)) << 4) != 0 && spins < (1u << 16); ++spins) { __builtin_amdgcn_s_sleep(8); r = rdy_load(F); }
  asm volatile("" ::: "memory");
  const unsigned long long b4 = __ballot(r.z & 4);
  pk = (unsigned)b4 & 255u;
  return (__ballot(r.z & 1) & ~(15ull << 60)) | (__builtin_popcountll(b4 << 4 >> 12) > 26 ? 1ull << 63 : 0ull);
}
__device__ __forceinline__ void check_in(const FParams& F, unsigned long long tag, int w) {
  __syncthreads();
  if (w == 0 && opaque_lane() == 0 && __hip_atomic_fetch_add(&F.ctl->chk[0], 1u, RLX_AGENT) == 255u) __hip_atomic_store((unsigned long long*)(F.rdy + 63 * 128), tag, RLX_AGENT);
}
__device__ __forceinline__ void wg_wait(const unsigned* p, unsigned need, Ctl* ctl, int w) {
  if (w == 0) {
    for (unsigned spins = 0;; ++spins) {
      const bool ok = (p == nullptr) || ld_agent(p) >= need;
      if (__all(ok)) break;
      if (spin_fail(spins, &ctl->tmo[0], 2u)) break;
      __builtin_amdgcn_s_sleep(16);
    }
    __builtin_amdgcn_fence(__ATOMIC_ACQUIRE, "agent");
    asm volatile("s_waitcnt vmcnt(0)" ::: "memory");
  }
  __syncthreads();
}

#define PN_BODY(PN_NT) \
          "s_mov_b64 s[40:41], %[xa]\n\ts_mov_b32 s42, 0x400000\n\ts_mov_b32 s43, 0x20000\n\ts_nop 4\n\ts_add_u32 m0, %[wl], 65536\n\ts_mul_i32 %[t], %[ldb], 0\n\t" \
          "buffer_load_dwordx4 %[vw0], %[rw], %[t] offen" PN_NT " lds\n\ts_add_u32 m0, %[wl], 66560\n\ts_mul_i32 %[t], %[ldb], 4\n\tbuffer_load_dwordx4 %[vw0], %[rw], %[t] offen" PN_NT " lds\n\ts_add_u32 m0, %[wl], 67584\n\ts_mul_i32 %[t], %[ldb], 8\n\t" \
          "buffer_load_dwordx4 %[vw1], %[rw], %[t] offen" PN_NT " lds\n\ts_add_u32 m0, %[wl], 68608\n\ts_mul_i32 %[t], %[ldb], 12\n\tbuffer_load_dwordx4 %[vw1], %[rw], %[t] offen" PN_NT " lds\n\ts_add_u32 m0, %[wl], 81920\n\ts_mul_i32 %[t], %[ldb], 64\n\t" \
          "buffer_load_dwordx4 %[vw0], %[rw], %[t] offen" PN_NT " lds\n\ts_add_u32 m0, %[wl], 82944\n\ts_mul_i32 %[t], %[ldb], 68\n\tbuffer_load_dwordx4 %[vw0], %[rw], %[t] offen" PN_NT " lds\n\ts_add_u32 m0, %[wl], 83968\n\ts_mul_i32 %[t], %[ldb], 72\n\t" \
          "buffer_load_dwordx4 %[vw1], %[rw], %[t] offen" PN_NT " lds\n\ts_add_u32 m0, %[wl], 84992\n\ts_mul_i32 %[t], %[ldb], 76\n\tbuffer_load_dwordx4 %[vw1], %[rw], %[t] offen" PN_NT " lds\n\ts_add_u32 m0, %[sl], 0\n\ts_nop 0\n\t" \
          "buffer_load_dwordx4 %[vd0], s[40:43], %[xo] offen lds\n\tbuffer_load_dwordx4 %[vd0], s[40:43], %[xo] offen offset:1024 lds\n\tbuffer_load_dwordx4 %[vd1], s[40:43], %[xo] offen offset:2048 lds\n\tbuffer_load_dwordx4 %[vd1], s[40:43], %[xo] offen offset:3072 lds\n\ts_add_u32 m0, %[sl], 4096\n\ts_nop 0\n\t" \
          "buffer_load_dwordx4 %[vd2], s[40:43], %[xo] offen lds\n\tbuffer_load_dwordx4 %[vd2], s[40:43], %[xo] offen offset:1024 lds\n\tbuffer_load_dwordx4 %[vd3], s[40:43], %[xo] offen offset:2048 lds\n\tbuffer_load_dwordx4 %[vd3], s[40:43], %[xo] offen offset:3072 lds\n\ts_add_u32 %[xo], %[xo], 0x8000\n\ts_add_u32 %[sl], %[sl], 0x8000\n\t" \
          "s_bitset0_b32 %[sl], 17\n\ts_add_u32 m0, %[sl], 0\n\ts_nop 0\n\tbuffer_load_dwordx4 %[vd0], s[40:43], %[xo] offen lds\n\tbuffer_load_dwordx4 %[vd0], s[40:43], %[xo] offen offset:1024 lds\n\tbuffer_load_dwordx4 %[vd1], s[40:43], %[xo] offen offset:2048 lds\n\t" \
          "buffer_load_dwordx4 %[vd1], s[40:43], %[xo] offen offset:3072 lds\n\ts_add_u32 m0, %[sl], 4096\n\ts_nop 0\n\tbuffer_load_dwordx4 %[vd2], s[40:43], %[xo] offen lds\n\tbuffer_load_dwordx4 %[vd2], s[40:43], %[xo] offen offset:1024 lds\n\tbuffer_load_dwordx4 %[vd3], s[40:43], %[xo] offen offset:2048 lds\n\t" \
          "buffer_load_dwordx4 %[vd3], s[40:43], %[xo] offen offset:3072 lds\n\ts_add_u32 %[xo], %[xo], 0x8000\n\ts_add_u32 %[sl], %[sl], 0x8000\n\ts_bitset0_b32 %[sl], 17\n\ts_add_u32 m0, %[wl], 98304\n\ts_mul_i32 %[t], %[ldb], 128\n\t" \
          "buffer_load_dwordx4 %[vw0], %[rw], %[t] offen" PN_NT " lds\n\ts_add_u32 m0, %[wl], 99328\n\ts_mul_i32 %[t], %[ldb], 132\n\tbuffer_load_dwordx4 %[vw0], %[rw], %[t] offen" PN_NT " lds\n\ts_add_u32 m0, %[wl], 100352\n\ts_mul_i32 %[t], %[ldb], 136\n\t" \
          "buffer_load_dwordx4 %[vw1], %[rw], %[t] offen" PN_NT " lds\n\ts_add_u32 m0, %[wl], 101376\n\ts_mul_i32 %[t], %[ldb], 140\n\tbuffer_load_dwordx4 %[vw1], %[rw], %[t] offen" PN_NT " lds\n\ts_add_u32 m0, %[wl], 114688\n\ts_mul_i32 %[t], %[ldb], 192\n\t" \
          "buffer_load_dwordx4 %[vw0], %[rw], %[t] offen" PN_NT " lds\n\ts_add_u32 m0, %[wl], 115712\n\ts_mul_i32 %[t], %[ldb], 196\n\tbuffer_load_dwordx4 %[vw0], %[rw], %[t] offen" PN_NT " lds\n\ts_add_u32 m0, %[wl], 116736\n\ts_mul_i32 %[t], %[ldb], 200\n\t" \
          "buffer_load_dwordx4 %[vw1], %[rw], %[t] offen" PN_NT " lds\n\ts_add_u32 m0, %[wl], 117760\n\ts_mul_i32 %[t], %[ldb], 204\n\tbuffer_load_dwordx4 %[vw1], %[rw], %[t] offen" PN_NT " lds\n\ts_add_u32 m0, %[wl], 131072\n\ts_mul_i32 %[t], %[ldb], 256\n\t" \
          "buffer_load_dwordx4 %[vw0], %[rw], %[t] offen" PN_NT " lds\n\ts_add_u32 m0, %[wl], 132096\n\ts_mul_i32 %[t], %[ldb], 260\n\tbuffer_load_dwordx4 %[vw0], %[rw], %[t] offen" PN_NT " lds\n\ts_add_u32 m0, %[wl], 133120\n\ts_mul_i32 %[t], %[ldb], 264\n\t" \
          "buffer_load_dwordx4 %[vw1], %[rw], %[t] offen" PN_NT " lds\n\ts_add_u32 m0, %[wl], 134144\n\ts_mul_i32 %[t], %[ldb], 268\n\tbuffer_load_dwordx4 %[vw1], %[rw], %[t] offen" PN_NT " lds\n\ts_waitcnt vmcnt(32) lgkmcnt(0)\n\ts_barrier\n\t" \
          "s_add_u32 m0, %[wl], 147456\n\ts_mul_i32 %[t], %[ldb], 320\n\tbuffer_load_dwordx4 %[vw0], %[rw], %[t] offen" PN_NT " lds\n\ts_add_u32 m0, %[wl], 148480\n\ts_mul_i32 %[t], %[ldb], 324\n\tbuffer_load_dwordx4 %[vw0], %[rw], %[t] offen" PN_NT " lds\n\t" \
          "s_add_u32 m0, %[wl], 149504\n\ts_mul_i32 %[t], %[ldb], 328\n\tbuffer_load_dwordx4 %[vw1], %[rw], %[t] offen" PN_NT " lds\n\ts_add_u32 m0, %[wl], 150528\n\ts_mul_i32 %[t], %[ldb], 332\n\tbuffer_load_dwordx4 %[vw1], %[rw], %[t] offen" PN_NT " lds\n\t" \
          "ds_read_b64_tr_b16 a[0:1], %[rt0] offset:0\n\tds_read_b64_tr_b16 a[2:3], %[rt0] offset:1024\n\tds_read_b64_tr_b16 a[4:5], %[rt1] offset:0\n\tds_read_b64_tr_b16 a[6:7], %[rt1] offset:1024\n\tds_read_b64_tr_b16 a[8:9], %[rt0] offset:8192\n\tds_read_b64_tr_b16 a[10:11], %[rt0] offset:9216\n\t" \
          "ds_read_b64_tr_b16 a[12:13], %[rt1] offset:8192\n\tds_read_b64_tr_b16 a[14:15], %[rt1] offset:9216\n\ts_waitcnt vmcnt(32) lgkmcnt(0)\n\ts_barrier\n\ts_add_u32 m0, %[wl], 65536\n\ts_mul_i32 %[t], %[ldb], 384\n\t" \
          "buffer_load_dwordx4 %[vw0], %[rw], %[t] offen" PN_NT " lds\n\ts_add_u32 m0, %[wl], 66560\n\ts_mul_i32 %[t], %[ldb], 388\n\tbuffer_load_dwordx4 %[vw0], %[rw], %[t] offen" PN_NT " lds\n\ts_add_u32 m0, %[wl], 67584\n\ts_mul_i32 %[t], %[ldb], 392\n\t" \
          "buffer_load_dwordx4 %[vw1], %[rw], %[t] offen" PN_NT " lds\n\ts_add_u32 m0, %[wl], 68608\n\ts_mul_i32 %[t], %[ldb], 396\n\tbuffer_load_dwordx4 %[vw1], %[rw], %[t] offen" PN_NT " lds\n\tds_read_b64_tr_b16 a[16:17], %[rt0] offset:16384\n\tds_read_b64_tr_b16 a[18:19], %[rt0] offset:17408\n\t" \
          "ds_read_b64_tr_b16 a[20:21], %[rt1] offset:16384\n\tds_read_b64_tr_b16 a[22:23], %[rt1] offset:17408\n\tds_read_b64_tr_b16 a[24:25], %[rt0] offset:24576\n\tds_read_b64_tr_b16 a[26:27], %[rt0] offset:25600\n\tds_read_b64_tr_b16 a[28:29], %[rt1] offset:24576\n\tds_read_b64_tr_b16 a[30:31], %[rt1] offset:25600\n\t" \
          "s_waitcnt vmcnt(16) lgkmcnt(0)\n\ts_barrier\n\ts_add_u32 m0, %[wl], 81920\n\ts_mul_i32 %[t], %[ldb], 448\n\tbuffer_load_dwordx4 %[vw0], %[rw], %[t] offen" PN_NT " lds\n\ts_add_u32 m0, %[wl], 82944\n\t" \
          "s_mul_i32 %[t], %[ldb], 452\n\tbuffer_load_dwordx4 %[vw0], %[rw], %[t] offen" PN_NT " lds\n\ts_add_u32 m0, %[wl], 83968\n\ts_mul_i32 %[t], %[ldb], 456\n\tbuffer_load_dwordx4 %[vw1], %[rw], %[t] offen" PN_NT " lds\n\ts_add_u32 m0, %[wl], 84992\n\t" \
          "s_mul_i32 %[t], %[ldb], 460\n\tbuffer_load_dwordx4 %[vw1], %[rw], %[t] offen" PN_NT " lds\n\tds_read_b64_tr_b16 a[32:33], %[rt0] offset:32768\n\tds_read_b64_tr_b16 a[34:35], %[rt0] offset:33792\n\tds_read_b64_tr_b16 a[36:37], %[rt1] offset:32768\n\tds_read_b64_tr_b16 a[38:39], %[rt1] offset:33792\n\t" \
          "ds_read_b64_tr_b16 a[40:41], %[rt0] offset:40960\n\tds_read_b64_tr_b16 a[42:43], %[rt0] offset:41984\n\tds_read_b64_tr_b16 a[44:45], %[rt1] offset:40960\n\tds_read_b64_tr_b16 a[46:47], %[rt1] offset:41984\n\ts_waitcnt vmcnt(16) lgkmcnt(0)\n\ts_barrier\n\t" \
          "s_add_u32 m0, %[wl], 98304\n\ts_mul_i32 %[t], %[ldb], 512\n\tbuffer_load_dwordx4 %[vw0], %[rw], %[t] offen" PN_NT " lds\n\ts_add_u32 m0, %[wl], 99328\n\ts_mul_i32 %[t], %[ldb], 516\n\tbuffer_load_dwordx4 %[vw0], %[rw], %[t] offen" PN_NT " lds\n\t" \
          "s_add_u32 m0, %[wl], 100352\n\ts_mul_i32 %[t], %[ldb], 520\n\tbuffer_load_dwordx4 %[vw1], %[rw], %[t] offen" PN_NT " lds\n\ts_add_u32 m0, %[wl], 101376\n\ts_mul_i32 %[t], %[ldb], 524\n\tbuffer_load_dwordx4 %[vw1], %[rw], %[t] offen" PN_NT " lds\n\t" \
          "ds_read_b64_tr_b16 a[48:49], %[rt2] offset:0\n\tds_read_b64_tr_b16 a[50:51], %[rt2] offset:1024\n\tds_read_b64_tr_b16 a[52:53], %[rt3] offset:0\n\tds_read_b64_tr_b16 a[54:55], %[rt3] offset:1024\n\tds_read_b64_tr_b16 a[56:57], %[rt2] offset:8192\n\tds_read_b64_tr_b16 a[58:59], %[rt2] offset:9216\n\t" \
          "ds_read_b64_tr_b16 a[60:61], %[rt3] offset:8192\n\tds_read_b64_tr_b16 a[62:63], %[rt3] offset:9216\n\ts_waitcnt vmcnt(16) lgkmcnt(0)\n\ts_barrier\n\ts_add_u32 m0, %[wl], 114688\n\ts_mul_i32 %[t], %[ldb], 576\n\t" \
          "buffer_load_dwordx4 %[vw0], %[rw], %[t] offen" PN_NT " lds\n\ts_add_u32 m0, %[wl], 115712\n\ts_mul_i32 %[t], %[ldb], 580\n\tbuffer_load_dwordx4 %[vw0], %[rw], %[t] offen" PN_NT " lds\n\ts_add_u32 m0, %[wl], 116736\n\ts_mul_i32 %[t], %[ldb], 584\n\t" \
          "buffer_load_dwordx4 %[vw1], %[rw], %[t] offen" PN_NT " lds\n\ts_add_u32 m0, %[wl], 117760\n\ts_mul_i32 %[t], %[ldb], 588\n\tbuffer_load_dwordx4 %[vw1], %[rw], %[t] offen" PN_NT " lds\n\tds_read_b64_tr_b16 a[64:65], %[rt2] offset:16384\n\tds_read_b64_tr_b16 a[66:67], %[rt2] offset:17408\n\t" \
          "ds_read_b64_tr_b16 a[68:69], %[rt3] offset:16384\n\tds_read_b64_tr_b16 a[70:71], %[rt3] offset:17408\n\tds_read_b64_tr_b16 a[72:73], %[rt2] offset:24576\n\tds_read_b64_tr_b16 a[74:75], %[rt2] offset:25600\n\tds_read_b64_tr_b16 a[76:77], %[rt3] offset:24576\n\tds_read_b64_tr_b16 a[78:79], %[rt3] offset:25600\n\t" \
          "s_waitcnt vmcnt(16) lgkmcnt(0)\n\ts_barrier\n\ts_add_u32 m0, %[wl], 131072\n\ts_mul_i32 %[t], %[ldb], 640\n\tbuffer_load_dwordx4 %[vw0], %[rw], %[t] offen" PN_NT " lds\n\ts_add_u32 m0, %[wl], 132096\n\t" \
          "s_mul_i32 %[t], %[ldb], 644\n\tbuffer_load_dwordx4 %[vw0], %[rw], %[t] offen" PN_NT " lds\n\ts_add_u32 m0, %[wl], 133120\n\ts_mul_i32 %[t], %[ldb], 648\n\tbuffer_load_dwordx4 %[vw1], %[rw], %[t] offen" PN_NT " lds\n\ts_add_u32 m0, %[wl], 134144\n\t" \
          "s_mul_i32 %[t], %[ldb], 652\n\tbuffer_load_dwordx4 %[vw1], %[rw], %[t] offen" PN_NT " lds\n\tds_read_b64_tr_b16 a[80:81], %[rt2] offset:32768\n\tds_read_b64_tr_b16 a[82:83], %[rt2] offset:33792\n\tds_read_b64_tr_b16 a[84:85], %[rt3] offset:32768\n\tds_read_b64_tr_b16 a[86:87], %[rt3] offset:33792\n\t" \
          "ds_read_b64_tr_b16 a[88:89], %[rt2] offset:40960\n\tds_read_b64_tr_b16 a[90:91], %[rt2] offset:41984\n\tds_read_b64_tr_b16 a[92:93], %[rt3] offset:40960\n\tds_read_b64_tr_b16 a[94:95], %[rt3] offset:41984\n\ts_waitcnt vmcnt(16) lgkmcnt(0)\n\ts_barrier\n\t" \
          "s_add_u32 m0, %[wl], 147456\n\ts_mul_i32 %[t], %[ldb], 704\n\tbuffer_load_dwordx4 %[vw0], %[rw], %[t] offen" PN_NT " lds\n\ts_add_u32 m0, %[wl], 148480\n\ts_mul_i32 %[t], %[ldb], 708\n\tbuffer_load_dwordx4 %[vw0], %[rw], %[t] offen" PN_NT " lds\n\t" \
          "s_add_u32 m0, %[wl], 149504\n\ts_mul_i32 %[t], %[ldb], 712\n\tbuffer_load_dwordx4 %[vw1], %[rw], %[t] offen" PN_NT " lds\n\ts_add_u32 m0, %[wl], 150528\n\ts_mul_i32 %[t], %[ldb], 716\n\tbuffer_load_dwordx4 %[vw1], %[rw], %[t] offen" PN_NT " lds\n\t" \
          "ds_read_b64_tr_b16 a[96:97], %[rt0] offset:0\n\tds_read_b64_tr_b16 a[98:99], %[rt0] offset:1024\n\tds_read_b64_tr_b16 a[100:101], %[rt1] offset:0\n\tds_read_b64_tr_b16 a[102:103], %[rt1] offset:1024\n\tds_read_b64_tr_b16 a[104:105], %[rt0] offset:8192\n\tds_read_b64_tr_b16 a[106:107], %[rt0] offset:9216\n\t" \
          "ds_read_b64_tr_b16 a[108:109], %[rt1] offset:8192\n\tds_read_b64_tr_b16 a[110:111], %[rt1] offset:9216\n\ts_waitcnt vmcnt(16) lgkmcnt(0)\n\ts_barrier\n\ts_add_u32 m0, %[wl], 65536\n\ts_mul_i32 %[t], %[ldb], 768\n\t" \
          "buffer_load_dwordx4 %[vw0], %[rw], %[t] offen" PN_NT " lds\n\ts_add_u32 m0, %[wl], 66560\n\ts_mul_i32 %[t], %[ldb], 772\n\tbuffer_load_dwordx4 %[vw0], %[rw], %[t] offen" PN_NT " lds\n\ts_add_u32 m0, %[wl], 67584\n\ts_mul_i32 %[t], %[ldb], 776\n\t" \
          "buffer_load_dwordx4 %[vw1], %[rw], %[t] offen" PN_NT " lds\n\ts_add_u32 m0, %[wl], 68608\n\ts_mul_i32 %[t], %[ldb], 780\n\tbuffer_load_dwordx4 %[vw1], %[rw], %[t] offen" PN_NT " lds\n\tds_read_b64_tr_b16 a[112:113], %[rt0] offset:16384\n\tds_read_b64_tr_b16 a[114:115], %[rt0] offset:17408\n\t" \
          "ds_read_b64_tr_b16 a[116:117], %[rt1] offset:16384\n\tds_read_b64_tr_b16 a[118:119], %[rt1] offset:17408\n\tds_read_b64_tr_b16 a[120:121], %[rt0] offset:24576\n\tds_read_b64_tr_b16 a[122:123], %[rt0] offset:25600\n\tds_read_b64_tr_b16 a[124:125], %[rt1] offset:24576\n\tds_read_b64_tr_b16 a[126:127], %[rt1] offset:25600\n\t" \
          "s_waitcnt vmcnt(16) lgkmcnt(0)\n\ts_barrier\n\ts_add_u32 m0, %[wl], 81920\n\ts_mul_i32 %[t], %[ldb], 832\n\tbuffer_load_dwordx4 %[vw0], %[rw], %[t] offen" PN_NT " lds\n\ts_add_u32 m0, %[wl], 82944\n\t" \
          "s_mul_i32 %[t], %[ldb], 836\n\tbuffer_load_dwordx4 %[vw0], %[rw], %[t] offen" PN_NT " lds\n\ts_add_u32 m0, %[wl], 83968\n\ts_mul_i32 %[t], %[ldb], 840\n\tbuffer_load_dwordx4 %[vw1], %[rw], %[t] offen" PN_NT " lds\n\ts_add_u32 m0, %[wl], 84992\n\t" \
          "s_mul_i32 %[t], %[ldb], 844\n\tbuffer_load_dwordx4 %[vw1], %[rw], %[t] offen" PN_NT " lds\n\tds_read_b64_tr_b16 a[128:129], %[rt0] offset:32768\n\tds_read_b64_tr_b16 a[130:131], %[rt0] offset:33792\n\tds_read_b64_tr_b16 a[132:133], %[rt1] offset:32768\n\tds_read_b64_tr_b16 a[134:135], %[rt1] offset:33792\n\t" \
          "ds_read_b64_tr_b16 a[136:137], %[rt0] offset:40960\n\tds_read_b64_tr_b16 a[138:139], %[rt0] offset:41984\n\tds_read_b64_tr_b16 a[140:141], %[rt1] offset:40960\n\tds_read_b64_tr_b16 a[142:143], %[rt1] offset:41984\n\ts_waitcnt vmcnt(16) lgkmcnt(0)\n\ts_barrier\n\t" \
          "s_add_u32 m0, %[wl], 98304\n\ts_mul_i32 %[t], %[ldb], 896\n\tbuffer_load_dwordx4 %[vw0], %[rw], %[t] offen" PN_NT " lds\n\ts_add_u32 m0, %[wl], 99328\n\ts_mul_i32 %[t], %[ldb], 900\n\tbuffer_load_dwordx4 %[vw0], %[rw], %[t] offen" PN_NT " lds\n\t" \
          "s_add_u32 m0, %[wl], 100352\n\ts_mul_i32 %[t], %[ldb], 904\n\tbuffer_load_dwordx4 %[vw1], %[rw], %[t] offen" PN_NT " lds\n\ts_add_u32 m0, %[wl], 101376\n\ts_mul_i32 %[t], %[ldb], 908\n\tbuffer_load_dwordx4 %[vw1], %[rw], %[t] offen" PN_NT " lds\n\t" \
          "ds_read_b64_tr_b16 a[144:145], %[rt2] offset:0\n\tds_read_b64_tr_b16 a[146:147], %[rt2] offset:1024\n\tds_read_b64_tr_b16 a[148:149], %[rt3] offset:0\n\tds_read_b64_tr_b16 a[150:151], %[rt3] offset:1024\n\tds_read_b64_tr_b16 a[152:153], %[rt2] offset:8192\n\tds_read_b64_tr_b16 a[154:155], %[rt2] offset:9216\n\t" \
          "ds_read_b64_tr_b16 a[156:157], %[rt3] offset:8192\n\tds_read_b64_tr_b16 a[158:159], %[rt3] offset:9216\n\ts_waitcnt vmcnt(16) lgkmcnt(0)\n\ts_barrier\n\ts_add_u32 m0, %[wl], 114688\n\ts_mul_i32 %[t], %[ldb], 960\n\t" \
          "buffer_load_dwordx4 %[vw0], %[rw], %[t] offen" PN_NT " lds\n\ts_add_u32 m0, %[wl], 115712\n\ts_mul_i32 %[t], %[ldb], 964\n\tbuffer_load_dwordx4 %[vw0], %[rw], %[t] offen" PN_NT " lds\n\ts_add_u32 m0, %[wl], 116736\n\ts_mul_i32 %[t], %[ldb], 968\n\t" \
          "buffer_load_dwordx4 %[vw1], %[rw], %[t] offen" PN_NT " lds\n\ts_add_u32 m0, %[wl], 117760\n\ts_mul_i32 %[t], %[ldb], 972\n\tbuffer_load_dwordx4 %[vw1], %[rw], %[t] offen" PN_NT " lds\n\tds_read_b64_tr_b16 a[160:161], %[rt2] offset:16384\n\tds_read_b64_tr_b16 a[162:163], %[rt2] offset:17408\n\t" \
          "ds_read_b64_tr_b16 a[164:165], %[rt3] offset:16384\n\tds_read_b64_tr_b16 a[166:167], %[rt3] offset:17408\n\tds_read_b64_tr_b16 a[168:169], %[rt2] offset:24576\n\tds_read_b64_tr_b16 a[170:171], %[rt2] offset:25600\n\tds_read_b64_tr_b16 a[172:173], %[rt3] offset:24576\n\tds_read_b64_tr_b16 a[174:175], %[rt3] offset:25600\n\t" \
          "s_waitcnt vmcnt(16) lgkmcnt(0)\n\ts_barrier\n\tbuffer_load_dwordx4 v[40:43], %[ro], %[rr], 0 offen sc1\n\tds_read_b64_tr_b16 a[176:177], %[rt2] offset:32768\n\tds_read_b64_tr_b16 a[178:179], %[rt2] offset:33792\n\tds_read_b64_tr_b16 a[180:181], %[rt3] offset:32768\n\t" \
          "ds_read_b64_tr_b16 a[182:183], %[rt3] offset:33792\n\tds_read_b64_tr_b16 a[184:185], %[rt2] offset:40960\n\tds_read_b64_tr_b16 a[186:187], %[rt2] offset:41984\n\tds_read_b64_tr_b16 a[188:189], %[rt3] offset:40960\n\tds_read_b64_tr_b16 a[190:191], %[rt3] offset:41984\n\ts_waitcnt vmcnt(13) lgkmcnt(0)\n\t" \
          "s_barrier\n\tds_read_b64_tr_b16 a[192:193], %[rt0] offset:0\n\tds_read_b64_tr_b16 a[194:195], %[rt0] offset:1024\n\tds_read_b64_tr_b16 a[196:197], %[rt1] offset:0\n\tds_read_b64_tr_b16 a[198:199], %[rt1] offset:1024\n\tds_read_b64_tr_b16 a[200:201], %[rt0] offset:8192\n\t" \
          "ds_read_b64_tr_b16 a[202:203], %[rt0] offset:9216\n\tds_read_b64_tr_b16 a[204:205], %[rt1] offset:8192\n\tds_read_b64_tr_b16 a[206:207], %[rt1] offset:9216\n\ts_waitcnt vmcnt(9) lgkmcnt(0)\n\ts_barrier\n\tds_read_b64_tr_b16 a[208:209], %[rt0] offset:16384\n\t" \
          "ds_read_b64_tr_b16 a[210:211], %[rt0] offset:17408\n\tds_read_b64_tr_b16 a[212:213], %[rt1] offset:16384\n\tds_read_b64_tr_b16 a[214:215], %[rt1] offset:17408\n\tds_read_b64_tr_b16 a[216:217], %[rt0] offset:24576\n\tds_read_b64_tr_b16 a[218:219], %[rt0] offset:25600\n\tds_read_b64_tr_b16 a[220:221], %[rt1] offset:24576\n\t" \
          "ds_read_b64_tr_b16 a[222:223], %[rt1] offset:25600\n\ts_waitcnt vmcnt(5) lgkmcnt(0)\n\ts_barrier\n\tds_read_b64_tr_b16 a[224:225], %[rt0] offset:32768\n\tds_read_b64_tr_b16 a[226:227], %[rt0] offset:33792\n\tds_read_b64_tr_b16 a[228:229], %[rt1] offset:32768\n\t" \
          "ds_read_b64_tr_b16 a[230:231], %[rt1] offset:33792\n\tds_read_b64_tr_b16 a[232:233], %[rt0] offset:40960\n\tds_read_b64_tr_b16 a[234:235], %[rt0] offset:41984\n\tds_read_b64_tr_b16 a[236:237], %[rt1] offset:40960\n\tds_read_b64_tr_b16 a[238:239], %[rt1] offset:41984\n\ts_waitcnt vmcnt(0) lgkmcnt(0)\n\t" \
          "s_barrier\n\tds_read_b64_tr_b16 a[240:241], %[rt2] offset:0\n\tds_read_b64_tr_b16 a[242:243], %[rt2] offset:1024\n\tds_read_b64_tr_b16 a[244:245], %[rt3] offset:0\n\tds_read_b64_tr_b16 a[246:247], %[rt3] offset:1024\n\tds_read_b64_tr_b16 a[248:249], %[rt2] offset:8192\n\t" \
          "ds_read_b64_tr_b16 a[250:251], %[rt2] offset:9216\n\tds_read_b64_tr_b16 a[252:253], %[rt3] offset:8192\n\tds_read_b64_tr_b16 a[254:255], %[rt3] offset:9216\n\ts_waitcnt lgkmcnt(0)\n\ts_barrier\n\ts_add_u32 m0, %[sl], 0\n\t" \
          "s_nop 0\n\tbuffer_load_dwordx4 %[vd0], s[40:43], %[xo] offen lds\n\tbuffer_load_dwordx4 %[vd0], s[40:43], %[xo] offen offset:1024 lds\n\tbuffer_load_dwordx4 %[vd1], s[40:43], %[xo] offen offset:2048 lds\n\tbuffer_load_dwordx4 %[vd1], s[40:43], %[xo] offen offset:3072 lds\n\ts_add_u32 m0, %[sl], 4096\n\t" \
          "s_nop 0\n\tbuffer_load_dwordx4 %[vd2], s[40:43], %[xo] offen lds\n\tbuffer_load_dwordx4 %[vd2], s[40:43], %[xo] offen offset:1024 lds\n\tbuffer_load_dwordx4 %[vd3], s[40:43], %[xo] offen offset:2048 lds\n\tbuffer_load_dwordx4 %[vd3], s[40:43], %[xo] offen offset:3072 lds\n\ts_add_u32 %[xo], %[xo], 0x8000\n\t" \
          "s_add_u32 %[sl], %[sl], 0x8000\n\ts_bitset0_b32 %[sl], 17\n\ts_mov_b32 %[t], 0\n\tv_readlane_b32 s44, v40, 63\n\tv_readlane_b32 s45, v41, 63\n\ts_add_u32 s44, s44, 1\n\t" \
          "s_addc_u32 s45, s45, 0\n\tLpn_r%=:\n\tv_cmp_eq_u64 vcc, s[44:45], v[40:41]\n\ts_add_u32 %[t], %[t], 0x10000\n\ts_cbranch_scc1 Lpn_g%=\n\ts_or_b32 vcc_hi, vcc_hi, 0xf0000000\n\t" \
          "s_and_b32 vcc_lo, vcc_lo, vcc_hi\n\ts_cmp_eq_u32 vcc_lo, -1\n\ts_cbranch_scc1 Lpn_g%=\n\ts_sleep 8\n\tbuffer_load_dwordx4 v[40:43], %[ro], %[rr], 0 offen sc1\n\ts_waitcnt vmcnt(0)\n\t" \
          "s_branch Lpn_r%=\n\tLpn_g%=:\n\tv_readlane_b32 %[t], v42, %[pl]\n\ts_and_b32 %[t], %[t], 1\n\ts_lshl_b32 %[t], %[t], 13\n\ts_add_u32 %[co], %[co], %[t]\n\t" \
          "v_readlane_b32 %[ko], v42, %[kl]\n\ts_and_b32 %[ko], %[ko], 4\n\ts_lshl_b32 %[ko], %[ko], 11\n\tv_mov_b64 v[36:37], -1\n\tv_mov_b64 v[38:39], -1\n\tds_read_b128 v[64:67], %[ra0] offset:0\n\t" \
          "ds_read_b128 v[68:71], %[ra1] offset:0\n\tds_read_b128 v[72:75], %[ra2] offset:0\n\tds_read_b128 v[76:79], %[ra3] offset:0\n\tds_read_b128 v[80:83], %[ra0] offset:256\n\tds_read_b128 v[84:87], %[ra1] offset:256\n\tds_read_b128 v[88:91], %[ra2] offset:256\n\t" \
          "ds_read_b128 v[92:95], %[ra3] offset:256\n\ts_mov_b64 %[sx], 0\n\tLpn_loop%=:\n\ts_waitcnt lgkmcnt(6)\n\tv_mfma_f32_16x16x32_bf16 v[48:51], a[0:3], v[64:67], 0\n\tds_read_b128 v[96:99], %[ra0] offset:512\n\t" \
          "v_mfma_f32_16x16x32_bf16 v[52:55], a[4:7], v[64:67], 0\n\ts_add_u32 %[t], %[j], 3\n\ts_cmp_lt_u32 %[t], %[je]\n\ts_cselect_b32 s42, s42, 0\n\tv_mfma_f32_16x16x32_bf16 v[48:51], a[8:11], v[68:71], v[48:51]\n\tds_read_b128 v[100:103], %[ra1] offset:512\n\t" \
          "v_mfma_f32_16x16x32_bf16 v[52:55], a[12:15], v[68:71], v[52:55]\n\tv_cvt_pk_bf16_f32 v44, v56, v57\n\tv_cvt_pk_bf16_f32 v45, v58, v59\n\ts_waitcnt lgkmcnt(6)\n\tv_mfma_f32_16x16x32_bf16 v[48:51], a[16:19], v[72:75], v[48:51]\n\tds_read_b128 v[104:107], %[ra2] offset:512\n\t" \
          "v_mfma_f32_16x16x32_bf16 v[52:55], a[20:23], v[72:75], v[52:55]\n\tv_cvt_pk_bf16_f32 v46, v60, v61\n\tv_cvt_pk_bf16_f32 v47, v62, v63\n\tv_mfma_f32_16x16x32_bf16 v[48:51], a[24:27], v[76:79], v[48:51]\n\tds_read_b128 v[108:111], %[ra3] offset:512\n\tv_mfma_f32_16x16x32_bf16 v[52:55], a[28:31], v[76:79], v[52:55]\n\t" \
          "s_mov_b64 exec, %[sx]\n\tbuffer_store_dwordx4 v[44:47], %[so], %[rc], %[co] offen sc1\n\ts_mov_b64 exec, -1\n\ts_waitcnt lgkmcnt(6)\n\tv_mfma_f32_16x16x32_bf16 v[48:51], a[32:35], v[80:83], v[48:51]\n\tds_read_b128 v[112:115], %[ra0] offset:768\n\t" \
          "v_mfma_f32_16x16x32_bf16 v[52:55], a[36:39], v[80:83], v[52:55]\n\ts_mov_b64 exec, %[fx]\n\tbuffer_store_dword %[fv], off, %[rf], %[fo] sc1\n\ts_mov_b64 exec, -1\n\tv_mfma_f32_16x16x32_bf16 v[48:51], a[40:43], v[84:87], v[48:51]\n\tds_read_b128 v[116:119], %[ra1] offset:768\n\t" \
          "v_mfma_f32_16x16x32_bf16 v[52:55], a[44:47], v[84:87], v[52:55]\n\ts_add_u32 %[co], %[co], %[cs]\n\ts_mov_b32 %[cs], 0x4000\n\ts_mov_b64 %[sx], -1\n\ts_waitcnt lgkmcnt(6)\n\tv_mfma_f32_16x16x32_bf16 v[48:51], a[48:51], v[88:91], v[48:51]\n\t" \
          "ds_read_b128 v[120:123], %[ra2] offset:768\n\ts_add_u32 m0, %[sl], 0\n\tv_mfma_f32_16x16x32_bf16 v[52:55], a[52:55], v[88:91], v[52:55]\n\tbuffer_load_dwordx4 %[vd0], s[40:43], %[xo] offen lds\n\tv_mfma_f32_16x16x32_bf16 v[48:51], a[56:59], v[92:95], v[48:51]\n\tds_read_b128 v[124:127], %[ra3] offset:768\n\t" \
          "v_mfma_f32_16x16x32_bf16 v[52:55], a[60:63], v[92:95], v[52:55]\n\ts_waitcnt lgkmcnt(6)\n\tv_mfma_f32_16x16x32_bf16 v[48:51], a[64:67], v[96:99], v[48:51]\n\tds_read_b128 v[64:67], %[ra0] offset:1024\n\tv_mfma_f32_16x16x32_bf16 v[52:55], a[68:71], v[96:99], v[52:55]\n\tbuffer_load_dwordx4 %[vd0], s[40:43], %[xo] offen offset:1024 lds\n\t" \
          "v_mfma_f32_16x16x32_bf16 v[48:51], a[72:75], v[100:103], v[48:51]\n\tds_read_b128 v[68:71], %[ra1] offset:1024\n\tv_mfma_f32_16x16x32_bf16 v[52:55], a[76:79], v[100:103], v[52:55]\n\ts_waitcnt lgkmcnt(6)\n\tv_mfma_f32_16x16x32_bf16 v[48:51], a[80:83], v[104:107], v[48:51]\n\tds_read_b128 v[72:75], %[ra2] offset:1024\n\t" \
          "v_mfma_f32_16x16x32_bf16 v[52:55], a[84:87], v[104:107], v[52:55]\n\tbuffer_load_dwordx4 %[vd1], s[40:43], %[xo] offen offset:2048 lds\n\tv_mfma_f32_16x16x32_bf16 v[48:51], a[88:91], v[108:111], v[48:51]\n\tds_read_b128 v[76:79], %[ra3] offset:1024\n\tv_mfma_f32_16x16x32_bf16 v[52:55], a[92:95], v[108:111], v[52:55]\n\ts_waitcnt lgkmcnt(6)\n\t" \
          "v_mfma_f32_16x16x32_bf16 v[48:51], a[96:99], v[112:115], v[48:51]\n\tds_read_b128 v[80:83], %[ra0] offset:1280\n\tv_mfma_f32_16x16x32_bf16 v[52:55], a[100:103], v[112:115], v[52:55]\n\tbuffer_load_dwordx4 %[vd1], s[40:43], %[xo] offen offset:3072 lds\n\tv_mfma_f32_16x16x32_bf16 v[48:51], a[104:107], v[116:119], v[48:51]\n\tds_read_b128 v[84:87], %[ra1] offset:1280\n\t" \
          "v_mfma_f32_16x16x32_bf16 v[52:55], a[108:111], v[116:119], v[52:55]\n\ts_waitcnt lgkmcnt(6)\n\tv_mfma_f32_16x16x32_bf16 v[48:51], a[112:115], v[120:123], v[48:51]\n\tds_read_b128 v[88:91], %[ra2] offset:1280\n\ts_add_u32 m0, %[sl], 4096\n\tv_mfma_f32_16x16x32_bf16 v[52:55], a[116:119], v[120:123], v[52:55]\n\t" \
          "buffer_load_dwordx4 %[vd2], s[40:43], %[xo] offen lds\n\tv_mfma_f32_16x16x32_bf16 v[48:51], a[120:123], v[124:127], v[48:51]\n\tds_read_b128 v[92:95], %[ra3] offset:1280\n\tv_mfma_f32_16x16x32_bf16 v[52:55], a[124:127], v[124:127], v[52:55]\n\ts_waitcnt lgkmcnt(6)\n\tv_mfma_f32_16x16x32_bf16 v[48:51], a[128:131], v[64:67], v[48:51]\n\t" \
          "ds_read_b128 v[96:99], %[ra0] offset:1536\n\tv_mfma_f32_16x16x32_bf16 v[52:55], a[132:135], v[64:67], v[52:55]\n\tbuffer_load_dwordx4 %[vd2], s[40:43], %[xo] offen offset:1024 lds\n\tv_mfma_f32_16x16x32_bf16 v[48:51], a[136:139], v[68:71], v[48:51]\n\tds_read_b128 v[100:103], %[ra1] offset:1536\n\tv_mfma_f32_16x16x32_bf16 v[52:55], a[140:143], v[68:71], v[52:55]\n\t" \
          "s_waitcnt lgkmcnt(6)\n\tv_mfma_f32_16x16x32_bf16 v[48:51], a[144:147], v[72:75], v[48:51]\n\tds_read_b128 v[104:107], %[ra2] offset:1536\n\tv_mfma_f32_16x16x32_bf16 v[52:55], a[148:151], v[72:75], v[52:55]\n\tbuffer_load_dwordx4 %[vd3], s[40:43], %[xo] offen offset:2048 lds\n\tv_mfma_f32_16x16x32_bf16 v[48:51], a[152:155], v[76:79], v[48:51]\n\t" \
          "ds_read_b128 v[108:111], %[ra3] offset:1536\n\tv_mfma_f32_16x16x32_bf16 v[52:55], a[156:159], v[76:79], v[52:55]\n\ts_waitcnt lgkmcnt(6)\n\tv_mfma_f32_16x16x32_bf16 v[48:51], a[160:163], v[80:83], v[48:51]\n\tds_read_b128 v[112:115], %[ra0] offset:1792\n\tv_mfma_f32_16x16x32_bf16 v[52:55], a[164:167], v[80:83], v[52:55]\n\t" \
          "buffer_load_dwordx4 %[vd3], s[40:43], %[xo] offen offset:3072 lds\n\tv_mfma_f32_16x16x32_bf16 v[48:51], a[168:171], v[84:87], v[48:51]\n\tds_read_b128 v[116:119], %[ra1] offset:1792\n\tv_mfma_f32_16x16x32_bf16 v[52:55], a[172:175], v[84:87], v[52:55]\n\ts_waitcnt lgkmcnt(6)\n\tv_mfma_f32_16x16x32_bf16 v[48:51], a[176:179], v[88:91], v[48:51]\n\t" \
          "ds_read_b128 v[120:123], %[ra2] offset:1792\n\tv_mfma_f32_16x16x32_bf16 v[52:55], a[180:183], v[88:91], v[52:55]\n\tv_mfma_f32_16x16x32_bf16 v[48:51], a[184:187], v[92:95], v[48:51]\n\tds_read_b128 v[124:127], %[ra3] offset:1792\n\tv_mfma_f32_16x16x32_bf16 v[52:55], a[188:191], v[92:95], v[52:55]\n\ts_add_u32 %[xo], %[xo], 0x8000\n\t" \
          "s_add_u32 %[sl], %[sl], 0x8000\n\ts_bitset0_b32 %[sl], 17\n\ts_waitcnt lgkmcnt(6)\n\tv_mfma_f32_16x16x32_bf16 v[48:51], a[192:195], v[96:99], v[48:51]\n\tv_mfma_f32_16x16x32_bf16 v[52:55], a[196:199], v[96:99], v[52:55]\n\tv_mfma_f32_16x16x32_bf16 v[48:51], a[200:203], v[100:103], v[48:51]\n\t" \
          "v_mfma_f32_16x16x32_bf16 v[52:55], a[204:207], v[100:103], v[52:55]\n\ts_waitcnt lgkmcnt(4)\n\tv_mfma_f32_16x16x32_bf16 v[48:51], a[208:211], v[104:107], v[48:51]\n\tv_mfma_f32_16x16x32_bf16 v[52:55], a[212:215], v[104:107], v[52:55]\n\tv_mfma_f32_16x16x32_bf16 v[48:51], a[216:219], v[108:111], v[48:51]\n\tv_mfma_f32_16x16x32_bf16 v[52:55], a[220:223], v[108:111], v[52:55]\n\t" \
          "s_waitcnt vmcnt(18) lgkmcnt(0)\n\ts_barrier\n\tv_mfma_f32_16x16x32_bf16 v[48:51], a[224:227], v[112:115], v[48:51]\n\tds_read_b128 v[64:67], %[ra0] offset:32768\n\tv_mfma_f32_16x16x32_bf16 v[52:55], a[228:231], v[112:115], v[52:55]\n\tds_read_b128 v[68:71], %[ra1] offset:32768\n\t" \
          "v_mfma_f32_16x16x32_bf16 v[48:51], a[232:235], v[116:119], v[48:51]\n\tds_read_b128 v[72:75], %[ra2] offset:32768\n\tv_mfma_f32_16x16x32_bf16 v[52:55], a[236:239], v[116:119], v[52:55]\n\tds_read_b128 v[76:79], %[ra3] offset:32768\n\tv_mfma_f32_16x16x32_bf16 v[48:51], a[240:243], v[120:123], v[48:51]\n\tds_read_b128 v[80:83], %[ra0] offset:33024\n\t" \
          "v_mfma_f32_16x16x32_bf16 v[52:55], a[244:247], v[120:123], v[52:55]\n\tds_read_b128 v[84:87], %[ra1] offset:33024\n\tv_mfma_f32_16x16x32_bf16 v[48:51], a[248:251], v[124:127], v[48:51]\n\tds_read_b128 v[88:91], %[ra2] offset:33024\n\tv_mfma_f32_16x16x32_bf16 v[52:55], a[252:255], v[124:127], v[52:55]\n\tds_read_b128 v[92:95], %[ra3] offset:33024\n\t" \
          "s_waitcnt lgkmcnt(6)\n\tv_mfma_f32_16x16x32_bf16 v[56:59], a[0:3], v[64:67], 0\n\tds_read_b128 v[96:99], %[ra0] offset:33280\n\tv_mfma_f32_16x16x32_bf16 v[60:63], a[4:7], v[64:67], 0\n\ts_add_u32 %[t], %[j], 4\n\ts_cmp_lt_u32 %[t], %[je]\n\t" \
          "s_cselect_b32 s42, s42, 0\n\tv_mfma_f32_16x16x32_bf16 v[56:59], a[8:11], v[68:71], v[56:59]\n\tds_read_b128 v[100:103], %[ra1] offset:33280\n\tv_mfma_f32_16x16x32_bf16 v[60:63], a[12:15], v[68:71], v[60:63]\n\tv_cvt_pk_bf16_f32 v44, v48, v49\n\tv_cvt_pk_bf16_f32 v45, v50, v51\n\t" \
          "s_waitcnt lgkmcnt(6)\n\tv_mfma_f32_16x16x32_bf16 v[56:59], a[16:19], v[72:75], v[56:59]\n\tds_read_b128 v[104:107], %[ra2] offset:33280\n\tv_mfma_f32_16x16x32_bf16 v[60:63], a[20:23], v[72:75], v[60:63]\n\tv_cvt_pk_bf16_f32 v46, v52, v53\n\tv_cvt_pk_bf16_f32 v47, v54, v55\n\t" \
          "v_mfma_f32_16x16x32_bf16 v[56:59], a[24:27], v[76:79], v[56:59]\n\tds_read_b128 v[108:111], %[ra3] offset:33280\n\tv_mfma_f32_16x16x32_bf16 v[60:63], a[28:31], v[76:79], v[60:63]\n\tbuffer_store_dwordx4 v[44:47], %[so], %[rc], %[co] offen sc1\n\ts_waitcnt lgkmcnt(6)\n\tv_mfma_f32_16x16x32_bf16 v[56:59], a[32:35], v[80:83], v[56:59]\n\t" \
          "ds_read_b128 v[112:115], %[ra0] offset:33536\n\tv_mfma_f32_16x16x32_bf16 v[60:63], a[36:39], v[80:83], v[60:63]\n\tbuffer_store_dwordx4 v[36:39], %[sk], %[rk], %[ko] offen sc1\n\tv_mov_b32 %[fv], %[j]\n\tv_mfma_f32_16x16x32_bf16 v[56:59], a[40:43], v[84:87], v[56:59]\n\tds_read_b128 v[116:119], %[ra1] offset:33536\n\t" \
          "v_mfma_f32_16x16x32_bf16 v[60:63], a[44:47], v[84:87], v[60:63]\n\ts_add_u32 %[co], %[co], 0x4000\n\ts_add_u32 %[ko], %[ko], 0x8000\n\ts_waitcnt lgkmcnt(6)\n\tv_mfma_f32_16x16x32_bf16 v[56:59], a[48:51], v[88:91], v[56:59]\n\tds_read_b128 v[120:123], %[ra2] offset:33536\n\t" \
          "s_add_u32 m0, %[sl], 0\n\tv_mfma_f32_16x16x32_bf16 v[60:63], a[52:55], v[88:91], v[60:63]\n\tbuffer_load_dwordx4 %[vd0], s[40:43], %[xo] offen lds\n\tv_mfma_f32_16x16x32_bf16 v[56:59], a[56:59], v[92:95], v[56:59]\n\tds_read_b128 v[124:127], %[ra3] offset:33536\n\tv_mfma_f32_16x16x32_bf16 v[60:63], a[60:63], v[92:95], v[60:63]\n\t" \
          "s_waitcnt lgkmcnt(6)\n\tv_mfma_f32_16x16x32_bf16 v[56:59], a[64:67], v[96:99], v[56:59]\n\tds_read_b128 v[64:67], %[ra0] offset:33792\n\tv_mfma_f32_16x16x32_bf16 v[60:63], a[68:71], v[96:99], v[60:63]\n\tbuffer_load_dwordx4 %[vd0], s[40:43], %[xo] offen offset:1024 lds\n\tv_mfma_f32_16x16x32_bf16 v[56:59], a[72:75], v[100:103], v[56:59]\n\t" \
          "ds_read_b128 v[68:71], %[ra1] offset:33792\n\tv_mfma_f32_16x16x32_bf16 v[60:63], a[76:79], v[100:103], v[60:63]\n\ts_waitcnt lgkmcnt(6)\n\tv_mfma_f32_16x16x32_bf16 v[56:59], a[80:83], v[104:107], v[56:59]\n\tds_read_b128 v[72:75], %[ra2] offset:33792\n\tv_mfma_f32_16x16x32_bf16 v[60:63], a[84:87], v[104:107], v[60:63]\n\t" \
          "buffer_load_dwordx4 %[vd1], s[40:43], %[xo] offen offset:2048 lds\n\tv_mfma_f32_16x16x32_bf16 v[56:59], a[88:91], v[108:111], v[56:59]\n\tds_read_b128 v[76:79], %[ra3] offset:33792\n\tv_mfma_f32_16x16x32_bf16 v[60:63], a[92:95], v[108:111], v[60:63]\n\ts_waitcnt lgkmcnt(6)\n\tv_mfma_f32_16x16x32_bf16 v[56:59], a[96:99], v[112:115], v[56:59]\n\t" \
          "ds_read_b128 v[80:83], %[ra0] offset:34048\n\tv_mfma_f32_16x16x32_bf16 v[60:63], a[100:103], v[112:115], v[60:63]\n\tbuffer_load_dwordx4 %[vd1], s[40:43], %[xo] offen offset:3072 lds\n\tv_mfma_f32_16x16x32_bf16 v[56:59], a[104:107], v[116:119], v[56:59]\n\tds_read_b128 v[84:87], %[ra1] offset:34048\n\tv_mfma_f32_16x16x32_bf16 v[60:63], a[108:111], v[116:119], v[60:63]\n\t" \
          "s_waitcnt lgkmcnt(6)\n\tv_mfma_f32_16x16x32_bf16 v[56:59], a[112:115], v[120:123], v[56:59]\n\tds_read_b128 v[88:91], %[ra2] offset:34048\n\ts_add_u32 m0, %[sl], 4096\n\tv_mfma_f32_16x16x32_bf16 v[60:63], a[116:119], v[120:123], v[60:63]\n\tbuffer_load_dwordx4 %[vd2], s[40:43], %[xo] offen lds\n\t" \
          "v_mfma_f32_16x16x32_bf16 v[56:59], a[120:123], v[124:127], v[56:59]\n\tds_read_b128 v[92:95], %[ra3] offset:34048\n\tv_mfma_f32_16x16x32_bf16 v[60:63], a[124:127], v[124:127], v[60:63]\n\ts_waitcnt lgkmcnt(6)\n\tv_mfma_f32_16x16x32_bf16 v[56:59], a[128:131], v[64:67], v[56:59]\n\tds_read_b128 v[96:99], %[ra0] offset:34304\n\t" \
          "v_mfma_f32_16x16x32_bf16 v[60:63], a[132:135], v[64:67], v[60:63]\n\tbuffer_load_dwordx4 %[vd2], s[40:43], %[xo] offen offset:1024 lds\n\tv_mfma_f32_16x16x32_bf16 v[56:59], a[136:139], v[68:71], v[56:59]\n\tds_read_b128 v[100:103], %[ra1] offset:34304\n\tv_mfma_f32_16x16x32_bf16 v[60:63], a[140:143], v[68:71], v[60:63]\n\ts_waitcnt lgkmcnt(6)\n\t" \
          "v_mfma_f32_16x16x32_bf16 v[56:59], a[144:147], v[72:75], v[56:59]\n\tds_read_b128 v[104:107], %[ra2] offset:34304\n\tv_mfma_f32_16x16x32_bf16 v[60:63], a[148:151], v[72:75], v[60:63]\n\tbuffer_load_dwordx4 %[vd3], s[40:43], %[xo] offen offset:2048 lds\n\tv_mfma_f32_16x16x32_bf16 v[56:59], a[152:155], v[76:79], v[56:59]\n\tds_read_b128 v[108:111], %[ra3] offset:34304\n\t" \
          "v_mfma_f32_16x16x32_bf16 v[60:63], a[156:159], v[76:79], v[60:63]\n\ts_waitcnt lgkmcnt(6)\n\tv_mfma_f32_16x16x32_bf16 v[56:59], a[160:163], v[80:83], v[56:59]\n\tds_read_b128 v[112:115], %[ra0] offset:34560\n\tv_mfma_f32_16x16x32_bf16 v[60:63], a[164:167], v[80:83], v[60:63]\n\tbuffer_load_dwordx4 %[vd3], s[40:43], %[xo] offen offset:3072 lds\n\t" \
          "v_mfma_f32_16x16x32_bf16 v[56:59], a[168:171], v[84:87], v[56:59]\n\tds_read_b128 v[116:119], %[ra1] offset:34560\n\tv_mfma_f32_16x16x32_bf16 v[60:63], a[172:175], v[84:87], v[60:63]\n\ts_waitcnt lgkmcnt(6)\n\tv_mfma_f32_16x16x32_bf16 v[56:59], a[176:179], v[88:91], v[56:59]\n\tds_read_b128 v[120:123], %[ra2] offset:34560\n\t" \
          "v_mfma_f32_16x16x32_bf16 v[60:63], a[180:183], v[88:91], v[60:63]\n\tv_mfma_f32_16x16x32_bf16 v[56:59], a[184:187], v[92:95], v[56:59]\n\tds_read_b128 v[124:127], %[ra3] offset:34560\n\tv_mfma_f32_16x16x32_bf16 v[60:63], a[188:191], v[92:95], v[60:63]\n\ts_add_u32 %[xo], %[xo], 0x8000\n\ts_add_u32 %[sl], %[sl], 0x8000\n\t" \
          "s_bitset0_b32 %[sl], 17\n\ts_waitcnt lgkmcnt(6)\n\tv_mfma_f32_16x16x32_bf16 v[56:59], a[192:195], v[96:99], v[56:59]\n\tv_mfma_f32_16x16x32_bf16 v[60:63], a[196:199], v[96:99], v[60:63]\n\tv_xor_b32 %[ra0], 0x10000, %[ra0]\n\tv_mfma_f32_16x16x32_bf16 v[56:59], a[200:203], v[100:103], v[56:59]\n\t" \
          "v_mfma_f32_16x16x32_bf16 v[60:63], a[204:207], v[100:103], v[60:63]\n\tv_xor_b32 %[ra1], 0x10000, %[ra1]\n\ts_waitcnt lgkmcnt(4)\n\tv_mfma_f32_16x16x32_bf16 v[56:59], a[208:211], v[104:107], v[56:59]\n\tv_mfma_f32_16x16x32_bf16 v[60:63], a[212:215], v[104:107], v[60:63]\n\tv_xor_b32 %[ra2], 0x10000, %[ra2]\n\t" \
          "v_mfma_f32_16x16x32_bf16 v[56:59], a[216:219], v[108:111], v[56:59]\n\ts_add_u32 %[j], %[j], 2\n\ts_cmp_lt_u32 %[j], %[je]\n\tv_mfma_f32_16x16x32_bf16 v[60:63], a[220:223], v[108:111], v[60:63]\n\tv_xor_b32 %[ra3], 0x10000, %[ra3]\n\ts_waitcnt vmcnt(18) lgkmcnt(0)\n\t" \
          "s_barrier\n\tv_mfma_f32_16x16x32_bf16 v[56:59], a[224:227], v[112:115], v[56:59]\n\tds_read_b128 v[64:67], %[ra0] offset:0\n\tv_mfma_f32_16x16x32_bf16 v[60:63], a[228:231], v[112:115], v[60:63]\n\tds_read_b128 v[68:71], %[ra1] offset:0\n\tv_mfma_f32_16x16x32_bf16 v[56:59], a[232:235], v[116:119], v[56:59]\n\t" \
          "ds_read_b128 v[72:75], %[ra2] offset:0\n\tv_mfma_f32_16x16x32_bf16 v[60:63], a[236:239], v[116:119], v[60:63]\n\tds_read_b128 v[76:79], %[ra3] offset:0\n\tv_mfma_f32_16x16x32_bf16 v[56:59], a[240:243], v[120:123], v[56:59]\n\tds_read_b128 v[80:83], %[ra0] offset:256\n\tv_mfma_f32_16x16x32_bf16 v[60:63], a[244:247], v[120:123], v[60:63]\n\t" \
          "ds_read_b128 v[84:87], %[ra1] offset:256\n\tv_mfma_f32_16x16x32_bf16 v[56:59], a[248:251], v[124:127], v[56:59]\n\tds_read_b128 v[88:91], %[ra2] offset:256\n\tv_mfma_f32_16x16x32_bf16 v[60:63], a[252:255], v[124:127], v[60:63]\n\tds_read_b128 v[92:95], %[ra3] offset:256\n\ts_cbranch_scc1 Lpn_loop%=\n\t" \
          "s_nop 7\n\tv_mov_b32 %[fv], %[je]\n\tv_cvt_pk_bf16_f32 v44, v56, v57\n\tv_cvt_pk_bf16_f32 v45, v58, v59\n\tv_cvt_pk_bf16_f32 v46, v60, v61\n\tv_cvt_pk_bf16_f32 v47, v62, v63\n\t" \
          "buffer_store_dwordx4 v[44:47], %[so], %[rc], %[co] offen sc1\n\ts_waitcnt vmcnt(0) lgkmcnt(0)\n\ts_barrier\n\ts_mov_b64 exec, %[fx]\n\tbuffer_store_dword %[fv], off, %[rf], %[fo] sc1\n\ts_mov_b64 exec, -1\n\t"
#define PN_CLOB "v36", "v37", "v38", "v39", "v44", "v45", "v46", "v47", "v48", "v49", "v50", "v51", "v52", "v53", "v54", "v55", "v56", "v57", "v58", "v59", "v60", "v61", "v62", "v63", "v64", "v65", "v66", "v67", "v68", "v69", "v70", "v71", "v72", "v73", "v74", "v75", "v76", "v77", "v78", "v79", "v80", "v81", "v82", "v83", "v84", "v85", "v86", "v87", "v88", "v89", "v90", "v91", "v92", "v93", "v94", "v95", "v96", "v97", "v98", "v99", "v100", "v101", "v102", "v103", "v104", "v105", "v106", "v107", "v108", "v109", "v110", "v111", "v112", "v113", "v114", "v115", "v116", "v117", "v118", "v119", "v120", "v121", "v122", "v123", "v124", "v125", "v126", "v127", "a0", "a1", "a2", "a3", "a4", "a5", "a6", "a7", "a8", "a9", "a10", "a11", "a12", "a13", "a14", "a15", "a16", "a17", "a18", "a19", "a20", "a21", "a22", "a23", "a24", "a25", "a26", "a27", "a28", "a29", "a30", "a31", "a32", "a33", "a34", "a35", "a36", "a37", "a38", "a39", "a40", "a41", "a42", "a43", "a44", "a45", "a46", "a47", "a48", "a49", "a50", "a51", "a52", "a53", "a54", "a55", "a56", "a57", "a58", "a59", "a60", "a61", "a62", "a63", "a64", "a65", "a66", "a67", "a68", "a69", "a70", "a71", "a72", "a73", "a74", "a75", "a76", "a77", "a78", "a79", "a80", "a81", "a82", "a83", "a84", "a85", "a86", "a87", "a88", "a89", "a90", "a91", "a92", "a93", "a94", "a95", "a96", "a97", "a98", "a99", "a100", "a101", "a102", "a103", "a104", "a105", "a106", "a107", "a108", "a109", "a110", "a111", "a112", "a113", "a114", "a115", "a116", "a117", "a118", "a119", "a120", "a121", "a122", "a123", "a124", "a125", "a126", "a127", "a128", "a129", "a130", "a131", "a132", "a133", "a134", "a135", "a136", "a137", "a138", "a139", "a140", "a141", "a142", "a143", "a144", "a145", "a146", "a147", "a148", "a149", "a150", "a151", "a152", "a153", "a154", "a155", "a156", "a157", "a158", "a159", "a160", "a161", "a162", "a163", "a164", "a165", "a166", "a167", "a168", "a169", "a170", "a171", "a172", "a173", "a174", "a175", "a176", "a177", "a178", "a179", "a180", "a181", "a182", "a183", "a184", "a185", "a186", "a187", "a188", "a189", "a190", "a191", "a192", "a193", "a194", "a195", "a196", "a197", "a198", "a199", "a200", "a201", "a202", "a203", "a204", "a205", "a206", "a207", "a208", "a209", "a210", "a211", "a212", "a213", "a214", "a215", "a216", "a217", "a218", "a219", "a220", "a221", "a222", "a223", "a224", "a225", "a226", "a227", "a228", "a229", "a230", "a231", "a232", "a233", "a234", "a235", "a236", "a237", "a238", "a239", "a240", "a241", "a242", "a243", "a244", "a245", "a246", "a247", "a248", "a249", "a250", "a251", "a252", "a253", "a254", "a255", "s40", "s41", "s42", "s43", "s44", "s45"
#define NTP 0
template <bool NT>
__device__ __forceinline__ v4u panel_task(const FParams& F, int p, int fi, int j, int je, int w) {
  const int lane = opaque_lane(), l15 = lane & 15, lq = lane >> 4, m = p >> 5, ldb = __builtin_amdgcn_readfirstlane(m < 3 ? HDA * 2 : NQKVB * 2);
  const __amdgpu_buffer_rsrc_t rw = __builtin_amdgcn_make_buffer_rsrc(wg::uni((m == 0 ? F.Wq : (m == 1 ? F.Wk : (m == 2 ? F.Wv : F.wqkvB))) + (p & 31) * 128), 0, DM * ldb - (p & 31) * 256, 0x00020000);
  const __amdgpu_buffer_rsrc_t rf = __builtin_amdgcn_make_buffer_rsrc(F.ctl, 0, sizeof(Ctl), 0x00020000), rr = __builtin_amdgcn_make_buffer_rsrc(F.rdy, 0, RDY_SIZE, 0x00020000);
  void* const xa = wg::uni(m < 3 ? F.Ax : F.Bx);
  int ra[4], vd[4], rt[4], vw[2], fv = 0, xo = w * 8192 + j * 32768, sl = w * 8192, co = j * QBLK, cs = 0, t; unsigned long long sx;
  v4u rec;
#pragma unroll
  for (int i = 0; i < 4; ++i) {
    ra[i] = l15 * 2048 + (((4 * i + lq) ^ l15) << 4); vd[i] = ((lane ^ (4 * w + i)) << 4) + (i >> 1) * 4096;
    rt[i] = 65536 + (i >> 1) * 49152 + (8 * lq + (l15 >> 2)) * 256 + (((2 * w + (i & 1)) ^ ((l15 >> 2) | ((lq & 1) << 2))) << 5) + (l15 & 3) * 8;
  }
  vw[0] = (16 * w + lq) * ldb + ((l15 ^ (lq << 1)) << 4); vw[1] = vw[0] ^ 128;
  const __amdgpu_buffer_rsrc_t rc = __builtin_amdgcn_make_buffer_rsrc((char*)F.qkvA + __builtin_amdgcn_readfirstlane((p >> 1) * QPAGE + (p & 1) * 4096 + w * 1024), 0, QPAGE - 8192 + 1024, 0x00020000);
  const bool kv = m == 1 || m == 2;
  const __amdgpu_buffer_rsrc_t rk = __builtin_amdgcn_make_buffer_rsrc((char*)F.qkvA + __builtin_amdgcn_readfirstlane(kvoff(0, p & 31) + (m & 2) * 1024 + w * 512), 0, QPAGE - 2 * QBLK + 8192 + 512, 0x00020000);
  int ko;
#define PN_OPS : "=&{v[40:43]}"(rec), [ra0] "+v"(ra[0]), [ra1] "+v"(ra[1]), [ra2] "+v"(ra[2]), [ra3] "+v"(ra[3]), [fv] "+v"(fv), [xo] "+s"(xo), [sl] "+s"(sl), [co] "+s"(co), [cs] "+s"(cs), [t] "=&s"(t), [j] "+s"(j), [sx] "=&s"(sx), [ko] "=&s"(ko) \
    : [vd0] "v"(vd[0]), [vd1] "v"(vd[1]), [vd2] "v"(vd[2]), [vd3] "v"(vd[3]), [rt0] "v"(rt[0]), [rt1] "v"(rt[1]), [rt2] "v"(rt[2]), [rt3] "v"(rt[3]), [vw0] "v"(vw[0]), [vw1] "v"(vw[1]), [ro] "v"(lane * 128), [so] "v"(lane * 16), [sk] "v"(lane < 32 && kv ? lane * 16 : 0x7ffffff0), \
      [xa] "s"(xa), [rw] "s"(rw), [rr] "s"(rr), [rc] "s"(rc), [rf] "s"(rf), [wl] "s"(w * 4096), [ldb] "s"(ldb), [fo] "s"((int)offsetof(Ctl, prg) + fi * 128), [je] "s"(je), \
      [fx] "s"((unsigned long long)(unsigned)__builtin_amdgcn_readfirstlane(w == 0)), [pl] "s"(p >> 1), [rk] "s"(rk), [kl] "s"((p & 31) >> 2) : PN_CLOB, "vcc", "scc", "memory"
  if constexpr (NT) asm volatile(PN_BODY(" nt") PN_OPS); else asm volatile(PN_BODY("") PN_OPS);
  return rec;
}

struct AttnX { v4u q[2][4], kx[2][4], vr[8]; };
template <int AUX = 0>
__device__ __forceinline__ void attnA_load_x(const FParams& F, unsigned long long parq, int seg, int h, int lane, AttnX& X) {
  const __amdgpu_buffer_rsrc_t rs = __builtin_amdgcn_make_buffer_rsrc(F.qkvA, 0, QSIZE, 0x00020000);
  const int so = seg * 2 * QBLK + (h & 1) * 4096, oq = so + qoff(parq, h >> 1), ok = so + qoff(parq, 16 + (h >> 1)), ov = so + qoff(parq, 32 + (h >> 1));
#pragma unroll
  for (int i = 0; i < 8; ++i) X.vr[i] = __builtin_amdgcn_raw_buffer_load_b128(rs, lane * 16 + (i & 3) * 1024, ov + (i >> 2) * QBLK, AUX);
#pragma unroll
  for (int t = 0; t < 2; ++t)
#pragma unroll
    for (int j = 0; j < 4; ++j) {
      X.q[t][j] = __builtin_amdgcn_raw_buffer_load_b128(rs, lane * 16 + j * 1024, oq + t * QBLK, AUX);
      X.kx[t][j] = __builtin_amdgcn_raw_buffer_load_b128(rs, lane * 16 + j * 1024, ok + t * QBLK, AUX);
    }
}
__device__ __forceinline__ void attnA_stage_v(const AttnX& X, u16* vs  , int lane) {
#pragma unroll
  for (int i = 0; i < 8; ++i) *(v4u*)&vs[(16 * (i >> 2) + (lane & 15)) * VXLD + 32 * (i & 3) + 8 * (lane >> 4)] = X.vr[i];
}
template <int WHAT = 3>
__device__ __forceinline__ unsigned attnA_load_m(const FParams& F, int seg, int ko, int lane, v4u (&km)[4], unsigned (&vm)[8]) {
  const int l15 = lane & 15, lq = lane >> 4;
  const __amdgpu_buffer_rsrc_t rk = __builtin_amdgcn_make_buffer_rsrc((char*)F.qkvA + ko + seg * 2 * QBLK, 0, 4096, 0x00020000);
#pragma unroll
  for (int j = 0; j < 4; ++j)
    if (WHAT & 1) km[j] = __builtin_amdgcn_raw_buffer_load_b128(rk, (2 * (lane >> 2) + (lane & 1)) * 16 + 512 * j, 0, 16);
#pragma unroll
  for (int t = 0; t < 8; ++t)
    if (WHAT & 2) vm[t] = __builtin_amdgcn_raw_buffer_load_b32(rk, 2048 + (((l15 >> 3) * 4 + lq) * 16 + 8 * ((l15 >> 2) & 1) + (l15 & 3)) * 4 + 512 * (t >> 1) + 16 * (t & 1), 0, 16);
  unsigned m = 0;
#pragma unroll
  for (int j = 0; j < 4; ++j) if (WHAT & 1) m = chk4(m, km[j]);
#pragma unroll
  for (int t = 0; t < 8; t += 2) if (WHAT & 2) m = umax3(m, vm[t], vm[t + 1]);
  return m;
}
__device__ __forceinline__ void attnA_compute(const AttnX& X, const v4u (&km)[4], const unsigned (&vm)[8], const u16* vs, int lane, v4u (&ov)[2][4]) {
  const int l15 = lane & 15, lq = lane >> 4;
  const float scale = 0.08838834764831845f, L2E = 1.4426950408889634f;
  v4u px[2], pm[2];
#pragma unroll
  for (int t = 0; t < 2; ++t) {
    f32x4 s0 = {0.f,0.f,0.f,0.f}, s1 = s0, s2 = s0;
#pragma unroll
    for (int j = 0; j < 4; ++j) { s0 = mf(km[j], X.q[t][j], s0); s1 = mf(X.kx[0][j], X.q[t][j], s1); if (t == 1) s2 = mf(X.kx[1][j], X.q[t][j], s2); }
    float sv[12];
#pragma unroll
    for (int e = 0; e < 4; ++e) { sv[e] = s0[e]; sv[4 + e] = s1[e]; sv[8 + e] = s2[e]; }
#pragma unroll
    for (int e = 0; e < 12; e += 2) {
      unsigned p = pk2(sv[e], sv[e + 1]);
      p = pk2(__uint_as_float(p << 16) * scale, __uint_as_float(p & 0xffff0000u) * scale);
      sv[e] = __uint_as_float(p << 16); sv[e + 1] = __uint_as_float(p & 0xffff0000u);
    }
    bool ok[12];
#pragma unroll
    for (int e = 0; e < 4; ++e) { ok[e] = e < 2; ok[4 + e] = (t == 1) || (4 * lq + e <= l15); ok[8 + e] = (t == 1) && (4 * lq + e <= l15); }
    float mx = -INFINITY;
#pragma unroll
    for (int e = 0; e < 12; ++e) mx = ok[e] ? fmaxf(mx, sv[e]) : mx;
    mx = xmax32(xmax16(mx));
    float sum = 0.f;
#pragma unroll
    for (int e = 0; e < 12; ++e) { sv[e] = ok[e] ? __builtin_amdgcn_exp2f((sv[e] - mx) * L2E) : 0.f; sum += sv[e]; }
    sum = xsum32(xsum16(sum));
    const float inv = __builtin_amdgcn_rcpf(sum);
#pragma unroll
    for (int e = 0; e < 12; ++e) sv[e] = ok[e] ? sv[e] * inv : 0.f;
    px[t] = (v4u){pk2(sv[4], sv[5]), pk2(sv[6], sv[7]), pk2(sv[8], sv[9]), pk2(sv[10], sv[11])};
    pm[t] = (v4u){pk2(sv[0], sv[1]), 0u, 0u, 0u};
  }
  const u16* vp = &vs[(4 * lq + (l15 >> 2)) * VXLD + 16 * (l15 & 1) + 2 * (l15 & 2)];
#pragma unroll
  for (int u = 0; u < 4; ++u) {
    f32x4 o[2][2];
#pragma unroll
    for (int ab = 0; ab < 2; ++ab) {
      v2u lo = trrd(vp + 32 * u + 8 * ab), hi = trrd(vp + 16 * VXLD + 32 * u + 8 * ab);
      const v4u va = {lo.x, lo.y, hi.x, hi.y}, vb = {vm[2 * u + ab], 0u, 0u, 0u};
#pragma unroll
      for (int t = 0; t < 2; ++t) { o[t][ab] = mf(va, px[t], (f32x4){0.f,0.f,0.f,0.f}); o[t][ab] = mf(vb, pm[t], o[t][ab]); }
    }
#pragma unroll
    for (int t = 0; t < 2; ++t)
      ov[t][u] = (v4u){pk2(o[t][0][0], o[t][0][1]), pk2(o[t][0][2], o[t][0][3]), pk2(o[t][1][0], o[t][1][1]), pk2(o[t][1][2], o[t][1][3])};
  }
}
__device__ __forceinline__ void attnA_block(const FParams& F, unsigned long long parq, unsigned pk, int r, int h, u16* vs, int lane, int w, unsigned* shw) {
  AttnX X[2]; v4u km[2][4]; unsigned vm[2][8];
  unsigned seen = 0;
  attnA_load_x(F, parq, 4 * r, h, lane, X[0]);
  bool have = false;
#pragma unroll
  for (int i = 0; i < 4; ++i) {
    const int seg = 4 * r + i, c = i & 1;
    v4u ov[2][4];
    if (!have) {
      if (seen < (unsigned)seg + 1) {
        if (w == 0) {
          for (unsigned spins = 0;; ++spins) {
            seen = __builtin_amdgcn_readfirstlane(ld_agent(&F.ctl->prog[0]));
            if (seen >= (unsigned)seg + 1 || spin_fail(spins, &F.ctl->tmo[0], 6u)) break;
            for (unsigned z = seen; z < (unsigned)seg + 1; ++z) __builtin_amdgcn_s_sleep(PSL);
          }
          *shw = seen;
        }
        __syncthreads();
        seen = __builtin_amdgcn_readfirstlane(*shw);
        __syncthreads();
      }
      asm volatile("" ::: "memory");
      (void)attnA_load_m(F, seg, kvoff(pk, h), lane, km[c], vm[c]);
    }
    if (i < 3) {
      attnA_load_x(F, parq, seg + 1, h, lane, X[c ^ 1]);
      have = seen >= (unsigned)seg + 2;
      (void)attnA_load_m(F, have ? seg + 1 : seg, kvoff(pk, h), lane, km[c ^ 1], vm[c ^ 1]);
    }
    SB0;
    attnA_stage_v(X[c], vs, lane);
    attnA_compute(X[c], km[c], vm[c], vs, lane, ov);
    const __amdgpu_buffer_rsrc_t rs = __builtin_amdgcn_make_buffer_rsrc(F.qkvA, 0, QSIZE, 0x00020000);
#pragma unroll
    for (int u = 0; u < 4; ++u)
#pragma unroll
      for (int t = 0; t < 2; ++t)
        __builtin_amdgcn_raw_buffer_store_b128(ov[t][u], rs, lane * 16 + u * 1024, qoff(parq, h >> 1) + (seg * 2 + t) * QBLK + (h & 1) * 4096, 16);
    SB0;
  }
}

constexpr int TSEG0 = 32, OSEG0 = 16, NSET = 4;
__device__ __forceinline__ void attn_stream(const FParams& F, unsigned long long parq, unsigned pk, int set, int hg, char* lds, int w) {
  const int lane = opaque_lane(), l15 = lane & 15, lq = lane >> 4, h = 4 * hg + w;
  const float L2E = 1.4426950408889634f;
  auto rnd2 = [](float& a, float& b) { const float scale = 0.08838834764831845f; unsigned p = pk2(a, b); p = pk2(__uint_as_float(p << 16) * scale, __uint_as_float(p & 0xffff0000u) * scale); a = __uint_as_float(p << 16); b = __uint_as_float(p & 0xffff0000u); };
  Ctl* ctl = F.ctl;
  unsigned have = 0;
  auto need = [&](int sg) {
    for (unsigned spins = 0; !__all(have >= 2u * sg + 2u); ++spins) {
      if (spins) __builtin_amdgcn_s_sleep(16);
      have = ld_agent(&ctl->prg[(lane < 2 ? lane : 3) * 32 + h][0]);
      if (spin_fail(spins, &ctl->tmo[0], 2u)) break;
    }
    asm volatile("" ::: "memory");
  };
  static_assert(TSEG0 >= 32, "upper q panel");
  need(TSEG0 + set);
  u16* vs = (u16*)(lds + w * 16384);
  AttnX X; int seen = 0; unsigned dur = 158;
  attnA_load_x<16>(F, parq, TSEG0 + set, h, lane, X);
#pragma unroll 1
  for (int seg = TSEG0 + set; seg < NSEG; seg += NSET) {
    v4u km[4]; unsigned vm[8]; f32x4 o[2][8]; float mx[2], ls[2];
    attnA_stage_v(X, vs, lane);
    { v4u px[2];
#pragma unroll
      for (int t = 0; t < 2; ++t) {
        f32x4 s1 = {0.f, 0.f, 0.f, 0.f}, s2 = s1;
#pragma unroll
        for (int j = 0; j < 4; ++j) { s1 = mf(X.kx[0][j], X.q[t][j], s1); if (t == 1) s2 = mf(X.kx[1][j], X.q[t][j], s2); }
        float sv[8]; bool ok[8];
#pragma unroll
        for (int e = 0; e < 4; ++e) { sv[e] = s1[e]; sv[4 + e] = s2[e]; ok[e] = (t == 1) || (4 * lq + e <= l15); ok[4 + e] = (t == 1) && (4 * lq + e <= l15); }
#pragma unroll
        for (int e = 0; e < 8; e += 2) rnd2(sv[e], sv[e + 1]);
        float m = -INFINITY, sum = 0.f;
#pragma unroll
        for (int e = 0; e < 8; ++e) m = ok[e] ? fmaxf(m, sv[e]) : m;
        m = xmax32(xmax16(m));
#pragma unroll
        for (int e = 0; e < 8; ++e) { sv[e] = ok[e] ? __builtin_amdgcn_exp2f((sv[e] - m) * L2E) : 0.f; sum += sv[e]; }
        mx[t] = m; ls[t] = sum;
        px[t] = (v4u){pk2(sv[0], sv[1]), pk2(sv[2], sv[3]), pk2(sv[4], sv[5]), pk2(sv[6], sv[7])};
      }
      const u16* vp = &vs[(4 * lq + (l15 >> 2)) * VXLD + 16 * (l15 & 1) + 2 * (l15 & 2)];
#pragma unroll
      for (int i = 0; i < 8; ++i) {
        const v2u lo = trrd(vp + 32 * (i >> 1) + 8 * (i & 1)), hi = trrd(vp + 16 * VXLD + 32 * (i >> 1) + 8 * (i & 1));
#pragma unroll
        for (int t = 0; t < 2; ++t) { o[t][i] = mf((v4u){lo.x, lo.y, hi.x, hi.y}, px[t], (f32x4){0.f, 0.f, 0.f, 0.f}); asm volatile("" : "+v"(o[t][i])); }
      } }
    const __amdgpu_buffer_rsrc_t rs = __builtin_amdgcn_make_buffer_rsrc(F.qkvA, 0, QSIZE, 0x00020000);
    const int oo = qoff(parq, h >> 1) + seg * 2 * QBLK + (h & 1) * 4096;
    if (seg >= NSEG - 2) {
#pragma unroll
      for (int u = 0; u < 8; ++u) __builtin_amdgcn_raw_buffer_store_b128((v4u){~0u, ~0u, ~0u, ~0u}, rs, lane * 16 + (u >> 1) * 1024, oo + (u & 1) * QBLK, 16);
    }
    if (seg + NSET < NSEG) need(seg + NSET);
    unsigned due = 0;
    for (unsigned spins = 0; seen <= seg - 3; ++spins) {
      const unsigned long long pv = __hip_atomic_load((unsigned long long*)&ctl->prog[0], RLX_AGENT);
      seen = __builtin_amdgcn_readfirstlane((unsigned)pv); due = __builtin_amdgcn_readfirstlane((unsigned)(pv >> 32)) + TP(F.x1, XA1);
      if (seen > seg - 3 || spin_fail(spins, &ctl->tmo[0], 6u)) break;
      __builtin_amdgcn_s_sleep(24);
    }
    if (seen != seg - 2 || (unsigned)(due - (unsigned)__builtin_amdgcn_s_memrealtime()) > 1000u) due = 0;
    if (seg >= NSEG - 2) {
      asm volatile("s_waitcnt vmcnt(0)\n\ts_barrier" ::: "memory");
      if (w == 0 && lane == 0 && due) __hip_atomic_store(&ctl->flagO[seg][hg][1], (due + dur) | 1u, RLX_AGENT);
    }
    if (due) while ((int)(due - (unsigned)__builtin_amdgcn_s_memrealtime()) > 0) __builtin_amdgcn_s_sleep(1);
    asm volatile("" ::: "memory");
    unsigned vc;
    for (unsigned spins = 0;; ++spins) {
      const unsigned kc = attnA_load_m<1>(F, seg, kvoff(pk, h), lane, km, vm);
      vc = attnA_load_m<2>(F, seg, kvoff(pk, h), lane, km, vm);
      if (__all(kc != 0xffffffffu) || spin_fail(spins, &ctl->tmo[0], 4u)) break;
      __builtin_amdgcn_s_sleep(TSL);
    }
#define ELOG(i)
    v4u pm[2];
#pragma unroll
    for (int t = 0; t < 2; ++t) {
      f32x4 s0 = {0.f, 0.f, 0.f, 0.f};
#pragma unroll
      for (int j = 0; j < 4; ++j) s0 = mf(km[j], X.q[t][j], s0);
      float a = s0[0], c = s0[1];
      rnd2(a, c);
      const float m = fmaxf(mx[t], xmax32(xmax16(fmaxf(a, c)))), al = __builtin_amdgcn_exp2f((mx[t] - m) * L2E);
      a = __builtin_amdgcn_exp2f((a - m) * L2E); c = __builtin_amdgcn_exp2f((c - m) * L2E);
      const float inv = __builtin_amdgcn_rcpf(xsum32(xsum16(ls[t] * al + a + c))), sc = al * inv;
      pm[t] = (v4u){pk2(a * inv, c * inv), 0u, 0u, 0u};
#pragma unroll
      for (int i = 0; i < 8; ++i) { o[t][i] *= sc; asm volatile("" : "+v"(o[t][i])); }
    }
    SB0;
    for (unsigned spins = 0; !__all(vc != 0xffffffffu) && !spin_fail(spins, &ctl->tmo[0], 4u); ++spins) vc = attnA_load_m<2>(F, seg, kvoff(pk, h), lane, km, vm);
#pragma unroll
    for (int u = 0; u < 4; ++u)
#pragma unroll
      for (int t = 0; t < 2; ++t) {
        const f32x4 y0 = mf((v4u){vm[2 * u], 0u, 0u, 0u}, pm[t], o[t][2 * u]), y1 = mf((v4u){vm[2 * u + 1], 0u, 0u, 0u}, pm[t], o[t][2 * u + 1]);
        __builtin_amdgcn_raw_buffer_store_b128((v4u){pk2(y0[0], y0[1]), pk2(y0[2], y0[3]), pk2(y1[0], y1[1]), pk2(y1[2], y1[3])}, rs, lane * 16 + u * 1024, oo + t * QBLK, 16);
      }
    SB0;
    if (seg + 2 * NSET < NSEG) { attnA_load_x<16>(F, parq, seg + NSET, h, lane, X); SB0; asm volatile("s_waitcnt vmcnt(24)" ::: "memory"); }
    else asm volatile("s_waitcnt vmcnt(0)" ::: "memory");
    asm volatile("s_barrier" ::: "memory");
    if (w == 0 && lane == 0) __hip_atomic_store(&ctl->flagO[seg][hg][0], 1u, RLX_AGENT);
    if (due) dur = min((unsigned)__builtin_amdgcn_s_memrealtime() - due - TP(F.x2, XA2), 158u);
    SB0;
    if (seg + NSET < NSEG && seg + 2 * NSET >= NSEG) attnA_load_x<16>(F, parq, seg + NSET, h, lane, X);
    ELOG(2);
  }
}
#define OU_AUX ""
#define OU_ST0
#define OU_ST1
#define OU_STO
#define OU_BODY \
          "s_mov_b64 s[40:41], %[qa]\n\ts_mov_b32 s42, %[qs]\n\ts_mov_b32 s43, 0x20000\n\ts_mov_b64 s[44:45], %[qa]\n\ts_mov_b32 s47, 0x20000\n\ts_nop 4\n\t" \
          "s_add_u32 m0, %[wl], 0\n\ts_xor_b32 %[t], %[wo], 0x0\n\tbuffer_load_dwordx4 %[vw0], %[rw], %[t] offen lds\n\ts_add_u32 m0, %[wl], 1024\n\ts_xor_b32 %[t], %[wo], 0x4000\n\tbuffer_load_dwordx4 %[vw1], %[rw], %[t] offen lds\n\t" \
          "s_add_u32 m0, %[wl], 2048\n\ts_xor_b32 %[t], %[wo], 0x8000\n\tbuffer_load_dwordx4 %[vw0], %[rw], %[t] offen lds\n\ts_add_u32 m0, %[wl], 3072\n\ts_xor_b32 %[t], %[wo], 0xc000\n\tbuffer_load_dwordx4 %[vw1], %[rw], %[t] offen lds\n\t" \
          "s_add_u32 m0, %[wl], 4096\n\ts_xor_b32 %[t], %[wo], 0x10000\n\tbuffer_load_dwordx4 %[vw0], %[rw], %[t] offen lds\n\ts_add_u32 m0, %[wl], 5120\n\ts_xor_b32 %[t], %[wo], 0x14000\n\tbuffer_load_dwordx4 %[vw1], %[rw], %[t] offen lds\n\t" \
          "s_add_u32 m0, %[wl], 6144\n\ts_xor_b32 %[t], %[wo], 0x18000\n\tbuffer_load_dwordx4 %[vw0], %[rw], %[t] offen lds\n\ts_add_u32 m0, %[wl], 7168\n\ts_xor_b32 %[t], %[wo], 0x1c000\n\tbuffer_load_dwordx4 %[vw1], %[rw], %[t] offen lds\n\t" \
          "s_add_u32 m0, %[wl], 8192\n\ts_xor_b32 %[t], %[wo], 0x20000\n\tbuffer_load_dwordx4 %[vw0], %[rw], %[t] offen lds\n\ts_add_u32 m0, %[wl], 9216\n\ts_xor_b32 %[t], %[wo], 0x24000\n\tbuffer_load_dwordx4 %[vw1], %[rw], %[t] offen lds\n\t" \
          "s_add_u32 m0, %[wl], 10240\n\ts_xor_b32 %[t], %[wo], 0x28000\n\tbuffer_load_dwordx4 %[vw0], %[rw], %[t] offen lds\n\ts_add_u32 m0, %[wl], 11264\n\ts_xor_b32 %[t], %[wo], 0x2c000\n\tbuffer_load_dwordx4 %[vw1], %[rw], %[t] offen lds\n\t" \
          "s_add_u32 m0, %[wl], 12288\n\ts_xor_b32 %[t], %[wo], 0x30000\n\tbuffer_load_dwordx4 %[vw0], %[rw], %[t] offen lds\n\ts_add_u32 m0, %[wl], 13312\n\ts_xor_b32 %[t], %[wo], 0x34000\n\tbuffer_load_dwordx4 %[vw1], %[rw], %[t] offen lds\n\t" \
          "s_add_u32 m0, %[wl], 14336\n\ts_xor_b32 %[t], %[wo], 0x38000\n\tbuffer_load_dwordx4 %[vw0], %[rw], %[t] offen lds\n\ts_add_u32 m0, %[wl], 15360\n\ts_xor_b32 %[t], %[wo], 0x3c000\n\tbuffer_load_dwordx4 %[vw1], %[rw], %[t] offen lds\n\t" \
          "s_add_u32 m0, %[wl], 16384\n\ts_xor_b32 %[t], %[wo], 0x40000\n\tbuffer_load_dwordx4 %[vw0], %[rw], %[t] offen lds\n\ts_add_u32 m0, %[wl], 17408\n\ts_xor_b32 %[t], %[wo], 0x44000\n\tbuffer_load_dwordx4 %[vw1], %[rw], %[t] offen lds\n\t" \
          "s_add_u32 m0, %[wl], 18432\n\ts_xor_b32 %[t], %[wo], 0x48000\n\tbuffer_load_dwordx4 %[vw0], %[rw], %[t] offen lds\n\ts_add_u32 m0, %[wl], 19456\n\ts_xor_b32 %[t], %[wo], 0x4c000\n\tbuffer_load_dwordx4 %[vw1], %[rw], %[t] offen lds\n\t" \
          "s_add_u32 m0, %[wl], 20480\n\ts_xor_b32 %[t], %[wo], 0x50000\n\tbuffer_load_dwordx4 %[vw0], %[rw], %[t] offen lds\n\ts_add_u32 m0, %[wl], 21504\n\ts_xor_b32 %[t], %[wo], 0x54000\n\tbuffer_load_dwordx4 %[vw1], %[rw], %[t] offen lds\n\t" \
          "s_add_u32 m0, %[wl], 22528\n\ts_xor_b32 %[t], %[wo], 0x58000\n\tbuffer_load_dwordx4 %[vw0], %[rw], %[t] offen lds\n\ts_add_u32 m0, %[wl], 23552\n\ts_xor_b32 %[t], %[wo], 0x5c000\n\tbuffer_load_dwordx4 %[vw1], %[rw], %[t] offen lds\n\t" \
          "s_add_u32 m0, %[wl], 24576\n\ts_xor_b32 %[t], %[wo], 0x60000\n\tbuffer_load_dwordx4 %[vw0], %[rw], %[t] offen lds\n\ts_add_u32 m0, %[wl], 25600\n\ts_xor_b32 %[t], %[wo], 0x64000\n\tbuffer_load_dwordx4 %[vw1], %[rw], %[t] offen lds\n\t" \
          "s_add_u32 m0, %[wl], 26624\n\ts_xor_b32 %[t], %[wo], 0x68000\n\tbuffer_load_dwordx4 %[vw0], %[rw], %[t] offen lds\n\ts_add_u32 m0, %[wl], 27648\n\ts_xor_b32 %[t], %[wo], 0x6c000\n\tbuffer_load_dwordx4 %[vw1], %[rw], %[t] offen lds\n\t" \
          "s_add_u32 m0, %[wl], 28672\n\ts_xor_b32 %[t], %[wo], 0x70000\n\tbuffer_load_dwordx4 %[vw0], %[rw], %[t] offen lds\n\ts_add_u32 m0, %[wl], 29696\n\ts_xor_b32 %[t], %[wo], 0x74000\n\tbuffer_load_dwordx4 %[vw1], %[rw], %[t] offen lds\n\t" \
          "s_add_u32 m0, %[wl], 30720\n\ts_xor_b32 %[t], %[wo], 0x78000\n\tbuffer_load_dwordx4 %[vw0], %[rw], %[t] offen lds\n\ts_add_u32 m0, %[wl], 31744\n\ts_xor_b32 %[t], %[wo], 0x7c000\n\tbuffer_load_dwordx4 %[vw1], %[rw], %[t] offen lds\n\t" \
          "s_add_u32 m0, %[wl], 32768\n\ts_xor_b32 %[t], %[wo], 0x80000\n\tbuffer_load_dwordx4 %[vw0], %[rw], %[t] offen lds\n\ts_add_u32 m0, %[wl], 33792\n\ts_xor_b32 %[t], %[wo], 0x84000\n\tbuffer_load_dwordx4 %[vw1], %[rw], %[t] offen lds\n\t" \
          "s_add_u32 m0, %[wl], 34816\n\ts_xor_b32 %[t], %[wo], 0x88000\n\tbuffer_load_dwordx4 %[vw0], %[rw], %[t] offen lds\n\ts_add_u32 m0, %[wl], 35840\n\ts_xor_b32 %[t], %[wo], 0x8c000\n\tbuffer_load_dwordx4 %[vw1], %[rw], %[t] offen lds\n\t" \
          "s_add_u32 m0, %[wl], 36864\n\ts_xor_b32 %[t], %[wo], 0x90000\n\tbuffer_load_dwordx4 %[vw0], %[rw], %[t] offen lds\n\ts_add_u32 m0, %[wl], 37888\n\ts_xor_b32 %[t], %[wo], 0x94000\n\tbuffer_load_dwordx4 %[vw1], %[rw], %[t] offen lds\n\t" \
          "s_add_u32 m0, %[wl], 38912\n\ts_xor_b32 %[t], %[wo], 0x98000\n\tbuffer_load_dwordx4 %[vw0], %[rw], %[t] offen lds\n\ts_add_u32 m0, %[wl], 39936\n\ts_xor_b32 %[t], %[wo], 0x9c000\n\tbuffer_load_dwordx4 %[vw1], %[rw], %[t] offen lds\n\t" \
          "s_waitcnt vmcnt(32)\n\tds_read_b64_tr_b16 a[0:1], %[rt0] offset:0\n\tds_read_b64_tr_b16 a[2:3], %[rt0] offset:512\n\tds_read_b64_tr_b16 a[4:5], %[rt1] offset:0\n\tds_read_b64_tr_b16 a[6:7], %[rt1] offset:512\n\tds_read_b64_tr_b16 v[128:129], %[rt2] offset:0\n\t" \
          "ds_read_b64_tr_b16 v[130:131], %[rt2] offset:512\n\tds_read_b64_tr_b16 v[32:33], %[rt3] offset:0\n\tds_read_b64_tr_b16 v[34:35], %[rt3] offset:512\n\tds_read_b64_tr_b16 a[8:9], %[rt0] offset:4096\n\tds_read_b64_tr_b16 a[10:11], %[rt0] offset:4608\n\tds_read_b64_tr_b16 a[12:13], %[rt1] offset:4096\n\t" \
          "ds_read_b64_tr_b16 a[14:15], %[rt1] offset:4608\n\tds_read_b64_tr_b16 v[132:133], %[rt2] offset:4096\n\tds_read_b64_tr_b16 v[134:135], %[rt2] offset:4608\n\tds_read_b64_tr_b16 v[36:37], %[rt3] offset:4096\n\tds_read_b64_tr_b16 v[38:39], %[rt3] offset:4608\n\ts_waitcnt lgkmcnt(0)\n\t" \
          "s_add_u32 m0, %[wl], 0\n\ts_xor_b32 %[t], %[wo], 0xa0000\n\tbuffer_load_dwordx4 %[vw0], %[rw], %[t] offen lds\n\ts_add_u32 m0, %[wl], 1024\n\ts_xor_b32 %[t], %[wo], 0xa4000\n\tbuffer_load_dwordx4 %[vw1], %[rw], %[t] offen lds\n\t" \
          "s_add_u32 m0, %[wl], 2048\n\ts_xor_b32 %[t], %[wo], 0xa8000\n\tbuffer_load_dwordx4 %[vw0], %[rw], %[t] offen lds\n\ts_add_u32 m0, %[wl], 3072\n\ts_xor_b32 %[t], %[wo], 0xac000\n\tbuffer_load_dwordx4 %[vw1], %[rw], %[t] offen lds\n\t" \
          "s_add_u32 m0, %[wl], 4096\n\ts_xor_b32 %[t], %[wo], 0xb0000\n\tbuffer_load_dwordx4 %[vw0], %[rw], %[t] offen lds\n\ts_add_u32 m0, %[wl], 5120\n\ts_xor_b32 %[t], %[wo], 0xb4000\n\tbuffer_load_dwordx4 %[vw1], %[rw], %[t] offen lds\n\t" \
          "s_add_u32 m0, %[wl], 6144\n\ts_xor_b32 %[t], %[wo], 0xb8000\n\tbuffer_load_dwordx4 %[vw0], %[rw], %[t] offen lds\n\ts_add_u32 m0, %[wl], 7168\n\ts_xor_b32 %[t], %[wo], 0xbc000\n\tbuffer_load_dwordx4 %[vw1], %[rw], %[t] offen lds\n\t" \
          "s_waitcnt vmcnt(32)\n\tds_read_b64_tr_b16 a[16:17], %[rt0] offset:8192\n\tds_read_b64_tr_b16 a[18:19], %[rt0] offset:8704\n\tds_read_b64_tr_b16 a[20:21], %[rt1] offset:8192\n\tds_read_b64_tr_b16 a[22:23], %[rt1] offset:8704\n\tds_read_b64_tr_b16 v[136:137], %[rt2] offset:8192\n\t" \
          "ds_read_b64_tr_b16 v[138:139], %[rt2] offset:8704\n\tds_read_b64_tr_b16 v[40:41], %[rt3] offset:8192\n\tds_read_b64_tr_b16 v[42:43], %[rt3] offset:8704\n\tds_read_b64_tr_b16 a[24:25], %[rt0] offset:12288\n\tds_read_b64_tr_b16 a[26:27], %[rt0] offset:12800\n\tds_read_b64_tr_b16 a[28:29], %[rt1] offset:12288\n\t" \
          "ds_read_b64_tr_b16 a[30:31], %[rt1] offset:12800\n\tds_read_b64_tr_b16 v[140:141], %[rt2] offset:12288\n\tds_read_b64_tr_b16 v[142:143], %[rt2] offset:12800\n\tds_read_b64_tr_b16 v[44:45], %[rt3] offset:12288\n\tds_read_b64_tr_b16 v[46:47], %[rt3] offset:12800\n\ts_waitcnt lgkmcnt(0)\n\t" \
          "s_add_u32 m0, %[wl], 8192\n\ts_xor_b32 %[t], %[wo], 0xc0000\n\tbuffer_load_dwordx4 %[vw0], %[rw], %[t] offen lds\n\ts_add_u32 m0, %[wl], 9216\n\ts_xor_b32 %[t], %[wo], 0xc4000\n\tbuffer_load_dwordx4 %[vw1], %[rw], %[t] offen lds\n\t" \
          "s_add_u32 m0, %[wl], 10240\n\ts_xor_b32 %[t], %[wo], 0xc8000\n\tbuffer_load_dwordx4 %[vw0], %[rw], %[t] offen lds\n\ts_add_u32 m0, %[wl], 11264\n\ts_xor_b32 %[t], %[wo], 0xcc000\n\tbuffer_load_dwordx4 %[vw1], %[rw], %[t] offen lds\n\t" \
          "s_add_u32 m0, %[wl], 12288\n\ts_xor_b32 %[t], %[wo], 0xd0000\n\tbuffer_load_dwordx4 %[vw0], %[rw], %[t] offen lds\n\ts_add_u32 m0, %[wl], 13312\n\ts_xor_b32 %[t], %[wo], 0xd4000\n\tbuffer_load_dwordx4 %[vw1], %[rw], %[t] offen lds\n\t" \
          "s_add_u32 m0, %[wl], 14336\n\ts_xor_b32 %[t], %[wo], 0xd8000\n\tbuffer_load_dwordx4 %[vw0], %[rw], %[t] offen lds\n\ts_add_u32 m0, %[wl], 15360\n\ts_xor_b32 %[t], %[wo], 0xdc000\n\tbuffer_load_dwordx4 %[vw1], %[rw], %[t] offen lds\n\t" \
          "s_waitcnt vmcnt(32)\n\tds_read_b64_tr_b16 a[32:33], %[rt0] offset:16384\n\tds_read_b64_tr_b16 a[34:35], %[rt0] offset:16896\n\tds_read_b64_tr_b16 a[36:37], %[rt1] offset:16384\n\tds_read_b64_tr_b16 a[38:39], %[rt1] offset:16896\n\tds_read_b64_tr_b16 v[144:145], %[rt2] offset:16384\n\t" \
          "ds_read_b64_tr_b16 v[146:147], %[rt2] offset:16896\n\tds_read_b64_tr_b16 v[48:49], %[rt3] offset:16384\n\tds_read_b64_tr_b16 v[50:51], %[rt3] offset:16896\n\tds_read_b64_tr_b16 a[40:41], %[rt0] offset:20480\n\tds_read_b64_tr_b16 a[42:43], %[rt0] offset:20992\n\tds_read_b64_tr_b16 a[44:45], %[rt1] offset:20480\n\t" \
          "ds_read_b64_tr_b16 a[46:47], %[rt1] offset:20992\n\tds_read_b64_tr_b16 v[148:149], %[rt2] offset:20480\n\tds_read_b64_tr_b16 v[150:151], %[rt2] offset:20992\n\tds_read_b64_tr_b16 v[52:53], %[rt3] offset:20480\n\tds_read_b64_tr_b16 v[54:55], %[rt3] offset:20992\n\ts_waitcnt lgkmcnt(0)\n\t" \
          "s_add_u32 m0, %[wl], 16384\n\ts_xor_b32 %[t], %[wo], 0xe0000\n\tbuffer_load_dwordx4 %[vw0], %[rw], %[t] offen lds\n\ts_add_u32 m0, %[wl], 17408\n\ts_xor_b32 %[t], %[wo], 0xe4000\n\tbuffer_load_dwordx4 %[vw1], %[rw], %[t] offen lds\n\t" \
          "s_add_u32 m0, %[wl], 18432\n\ts_xor_b32 %[t], %[wo], 0xe8000\n\tbuffer_load_dwordx4 %[vw0], %[rw], %[t] offen lds\n\ts_add_u32 m0, %[wl], 19456\n\ts_xor_b32 %[t], %[wo], 0xec000\n\tbuffer_load_dwordx4 %[vw1], %[rw], %[t] offen lds\n\t" \
          "s_add_u32 m0, %[wl], 20480\n\ts_xor_b32 %[t], %[wo], 0xf0000\n\tbuffer_load_dwordx4 %[vw0], %[rw], %[t] offen lds\n\ts_add_u32 m0, %[wl], 21504\n\ts_xor_b32 %[t], %[wo], 0xf4000\n\tbuffer_load_dwordx4 %[vw1], %[rw], %[t] offen lds\n\t" \
          "s_add_u32 m0, %[wl], 22528\n\ts_xor_b32 %[t], %[wo], 0xf8000\n\tbuffer_load_dwordx4 %[vw0], %[rw], %[t] offen lds\n\ts_add_u32 m0, %[wl], 23552\n\ts_xor_b32 %[t], %[wo], 0xfc000\n\tbuffer_load_dwordx4 %[vw1], %[rw], %[t] offen lds\n\t" \
          "s_waitcnt vmcnt(32)\n\tds_read_b64_tr_b16 a[48:49], %[rt0] offset:24576\n\tds_read_b64_tr_b16 a[50:51], %[rt0] offset:25088\n\tds_read_b64_tr_b16 a[52:53], %[rt1] offset:24576\n\tds_read_b64_tr_b16 a[54:55], %[rt1] offset:25088\n\tds_read_b64_tr_b16 v[152:153], %[rt2] offset:24576\n\t" \
          "ds_read_b64_tr_b16 v[154:155], %[rt2] offset:25088\n\tds_read_b64_tr_b16 v[56:57], %[rt3] offset:24576\n\tds_read_b64_tr_b16 v[58:59], %[rt3] offset:25088\n\tds_read_b64_tr_b16 a[56:57], %[rt0] offset:28672\n\tds_read_b64_tr_b16 a[58:59], %[rt0] offset:29184\n\tds_read_b64_tr_b16 a[60:61], %[rt1] offset:28672\n\t" \
          "ds_read_b64_tr_b16 a[62:63], %[rt1] offset:29184\n\tds_read_b64_tr_b16 v[156:157], %[rt2] offset:28672\n\tds_read_b64_tr_b16 v[158:159], %[rt2] offset:29184\n\tds_read_b64_tr_b16 v[60:61], %[rt3] offset:28672\n\tds_read_b64_tr_b16 v[62:63], %[rt3] offset:29184\n\ts_waitcnt lgkmcnt(0)\n\t" \
          "s_add_u32 m0, %[wl], 24576\n\ts_xor_b32 %[t], %[wo], 0x100000\n\tbuffer_load_dwordx4 %[vw0], %[rw], %[t] offen lds\n\ts_add_u32 m0, %[wl], 25600\n\ts_xor_b32 %[t], %[wo], 0x104000\n\tbuffer_load_dwordx4 %[vw1], %[rw], %[t] offen lds\n\t" \
          "s_add_u32 m0, %[wl], 26624\n\ts_xor_b32 %[t], %[wo], 0x108000\n\tbuffer_load_dwordx4 %[vw0], %[rw], %[t] offen lds\n\ts_add_u32 m0, %[wl], 27648\n\ts_xor_b32 %[t], %[wo], 0x10c000\n\tbuffer_load_dwordx4 %[vw1], %[rw], %[t] offen lds\n\t" \
          "s_add_u32 m0, %[wl], 28672\n\ts_xor_b32 %[t], %[wo], 0x110000\n\tbuffer_load_dwordx4 %[vw0], %[rw], %[t] offen lds\n\ts_add_u32 m0, %[wl], 29696\n\ts_xor_b32 %[t], %[wo], 0x114000\n\tbuffer_load_dwordx4 %[vw1], %[rw], %[t] offen lds\n\t" \
          "s_add_u32 m0, %[wl], 30720\n\ts_xor_b32 %[t], %[wo], 0x118000\n\tbuffer_load_dwordx4 %[vw0], %[rw], %[t] offen lds\n\ts_add_u32 m0, %[wl], 31744\n\ts_xor_b32 %[t], %[wo], 0x11c000\n\tbuffer_load_dwordx4 %[vw1], %[rw], %[t] offen lds\n\t" \
          "s_waitcnt vmcnt(32)\n\tds_read_b64_tr_b16 a[64:65], %[rt0] offset:32768\n\tds_read_b64_tr_b16 a[66:67], %[rt0] offset:33280\n\tds_read_b64_tr_b16 a[68:69], %[rt1] offset:32768\n\tds_read_b64_tr_b16 a[70:71], %[rt1] offset:33280\n\tds_read_b64_tr_b16 v[160:161], %[rt2] offset:32768\n\t" \
          "ds_read_b64_tr_b16 v[162:163], %[rt2] offset:33280\n\tds_read_b64_tr_b16 v[64:65], %[rt3] offset:32768\n\tds_read_b64_tr_b16 v[66:67], %[rt3] offset:33280\n\tds_read_b64_tr_b16 a[72:73], %[rt0] offset:36864\n\tds_read_b64_tr_b16 a[74:75], %[rt0] offset:37376\n\tds_read_b64_tr_b16 a[76:77], %[rt1] offset:36864\n\t" \
          "ds_read_b64_tr_b16 a[78:79], %[rt1] offset:37376\n\tds_read_b64_tr_b16 v[164:165], %[rt2] offset:36864\n\tds_read_b64_tr_b16 v[166:167], %[rt2] offset:37376\n\tds_read_b64_tr_b16 v[68:69], %[rt3] offset:36864\n\tds_read_b64_tr_b16 v[70:71], %[rt3] offset:37376\n\ts_waitcnt lgkmcnt(0)\n\t" \
          "s_add_u32 m0, %[wl], 32768\n\ts_xor_b32 %[t], %[wo], 0x120000\n\tbuffer_load_dwordx4 %[vw0], %[rw], %[t] offen lds\n\ts_add_u32 m0, %[wl], 33792\n\ts_xor_b32 %[t], %[wo], 0x124000\n\tbuffer_load_dwordx4 %[vw1], %[rw], %[t] offen lds\n\t" \
          "s_add_u32 m0, %[wl], 34816\n\ts_xor_b32 %[t], %[wo], 0x128000\n\tbuffer_load_dwordx4 %[vw0], %[rw], %[t] offen lds\n\ts_add_u32 m0, %[wl], 35840\n\ts_xor_b32 %[t], %[wo], 0x12c000\n\tbuffer_load_dwordx4 %[vw1], %[rw], %[t] offen lds\n\t" \
          "s_add_u32 m0, %[wl], 36864\n\ts_xor_b32 %[t], %[wo], 0x130000\n\tbuffer_load_dwordx4 %[vw0], %[rw], %[t] offen lds\n\ts_add_u32 m0, %[wl], 37888\n\ts_xor_b32 %[t], %[wo], 0x134000\n\tbuffer_load_dwordx4 %[vw1], %[rw], %[t] offen lds\n\t" \
          "s_add_u32 m0, %[wl], 38912\n\ts_xor_b32 %[t], %[wo], 0x138000\n\tbuffer_load_dwordx4 %[vw0], %[rw], %[t] offen lds\n\ts_add_u32 m0, %[wl], 39936\n\ts_xor_b32 %[t], %[wo], 0x13c000\n\tbuffer_load_dwordx4 %[vw1], %[rw], %[t] offen lds\n\t" \
          "s_waitcnt vmcnt(32)\n\tds_read_b64_tr_b16 a[80:81], %[rt0] offset:0\n\tds_read_b64_tr_b16 a[82:83], %[rt0] offset:512\n\tds_read_b64_tr_b16 a[84:85], %[rt1] offset:0\n\tds_read_b64_tr_b16 a[86:87], %[rt1] offset:512\n\tds_read_b64_tr_b16 v[168:169], %[rt2] offset:0\n\t" \
          "ds_read_b64_tr_b16 v[170:171], %[rt2] offset:512\n\tds_read_b64_tr_b16 v[72:73], %[rt3] offset:0\n\tds_read_b64_tr_b16 v[74:75], %[rt3] offset:512\n\tds_read_b64_tr_b16 a[88:89], %[rt0] offset:4096\n\tds_read_b64_tr_b16 a[90:91], %[rt0] offset:4608\n\tds_read_b64_tr_b16 a[92:93], %[rt1] offset:4096\n\t" \
          "ds_read_b64_tr_b16 a[94:95], %[rt1] offset:4608\n\tds_read_b64_tr_b16 v[172:173], %[rt2] offset:4096\n\tds_read_b64_tr_b16 v[174:175], %[rt2] offset:4608\n\tds_read_b64_tr_b16 v[76:77], %[rt3] offset:4096\n\tds_read_b64_tr_b16 v[78:79], %[rt3] offset:4608\n\ts_waitcnt lgkmcnt(0)\n\t" \
          "s_add_u32 m0, %[wl], 0\n\ts_xor_b32 %[t], %[wo], 0x140000\n\tbuffer_load_dwordx4 %[vw0], %[rw], %[t] offen lds\n\ts_add_u32 m0, %[wl], 1024\n\ts_xor_b32 %[t], %[wo], 0x144000\n\tbuffer_load_dwordx4 %[vw1], %[rw], %[t] offen lds\n\t" \
          "s_add_u32 m0, %[wl], 2048\n\ts_xor_b32 %[t], %[wo], 0x148000\n\tbuffer_load_dwordx4 %[vw0], %[rw], %[t] offen lds\n\ts_add_u32 m0, %[wl], 3072\n\ts_xor_b32 %[t], %[wo], 0x14c000\n\tbuffer_load_dwordx4 %[vw1], %[rw], %[t] offen lds\n\t" \
          "s_add_u32 m0, %[wl], 4096\n\ts_xor_b32 %[t], %[wo], 0x150000\n\tbuffer_load_dwordx4 %[vw0], %[rw], %[t] offen lds\n\ts_add_u32 m0, %[wl], 5120\n\ts_xor_b32 %[t], %[wo], 0x154000\n\tbuffer_load_dwordx4 %[vw1], %[rw], %[t] offen lds\n\t" \
          "s_add_u32 m0, %[wl], 6144\n\ts_xor_b32 %[t], %[wo], 0x158000\n\tbuffer_load_dwordx4 %[vw0], %[rw], %[t] offen lds\n\ts_add_u32 m0, %[wl], 7168\n\ts_xor_b32 %[t], %[wo], 0x15c000\n\tbuffer_load_dwordx4 %[vw1], %[rw], %[t] offen lds\n\t" \
          "s_waitcnt vmcnt(32)\n\tds_read_b64_tr_b16 a[96:97], %[rt0] offset:8192\n\tds_read_b64_tr_b16 a[98:99], %[rt0] offset:8704\n\tds_read_b64_tr_b16 a[100:101], %[rt1] offset:8192\n\tds_read_b64_tr_b16 a[102:103], %[rt1] offset:8704\n\tds_read_b64_tr_b16 v[176:177], %[rt2] offset:8192\n\t" \
          "ds_read_b64_tr_b16 v[178:179], %[rt2] offset:8704\n\tds_read_b64_tr_b16 v[80:81], %[rt3] offset:8192\n\tds_read_b64_tr_b16 v[82:83], %[rt3] offset:8704\n\tds_read_b64_tr_b16 a[104:105], %[rt0] offset:12288\n\tds_read_b64_tr_b16 a[106:107], %[rt0] offset:12800\n\tds_read_b64_tr_b16 a[108:109], %[rt1] offset:12288\n\t" \
          "ds_read_b64_tr_b16 a[110:111], %[rt1] offset:12800\n\tds_read_b64_tr_b16 v[180:181], %[rt2] offset:12288\n\tds_read_b64_tr_b16 v[182:183], %[rt2] offset:12800\n\tds_read_b64_tr_b16 v[84:85], %[rt3] offset:12288\n\tds_read_b64_tr_b16 v[86:87], %[rt3] offset:12800\n\ts_waitcnt lgkmcnt(0)\n\t" \
          "s_add_u32 m0, %[wl], 8192\n\ts_xor_b32 %[t], %[wo], 0x160000\n\tbuffer_load_dwordx4 %[vw0], %[rw], %[t] offen lds\n\ts_add_u32 m0, %[wl], 9216\n\ts_xor_b32 %[t], %[wo], 0x164000\n\tbuffer_load_dwordx4 %[vw1], %[rw], %[t] offen lds\n\t" \
          "s_add_u32 m0, %[wl], 10240\n\ts_xor_b32 %[t], %[wo], 0x168000\n\tbuffer_load_dwordx4 %[vw0], %[rw], %[t] offen lds\n\ts_add_u32 m0, %[wl], 11264\n\ts_xor_b32 %[t], %[wo], 0x16c000\n\tbuffer_load_dwordx4 %[vw1], %[rw], %[t] offen lds\n\t" \
          "s_add_u32 m0, %[wl], 12288\n\ts_xor_b32 %[t], %[wo], 0x170000\n\tbuffer_load_dwordx4 %[vw0], %[rw], %[t] offen lds\n\ts_add_u32 m0, %[wl], 13312\n\ts_xor_b32 %[t], %[wo], 0x174000\n\tbuffer_load_dwordx4 %[vw1], %[rw], %[t] offen lds\n\t" \
          "s_add_u32 m0, %[wl], 14336\n\ts_xor_b32 %[t], %[wo], 0x178000\n\tbuffer_load_dwordx4 %[vw0], %[rw], %[t] offen lds\n\ts_add_u32 m0, %[wl], 15360\n\ts_xor_b32 %[t], %[wo], 0x17c000\n\tbuffer_load_dwordx4 %[vw1], %[rw], %[t] offen lds\n\t" \
          "s_waitcnt vmcnt(32)\n\tds_read_b64_tr_b16 a[112:113], %[rt0] offset:16384\n\tds_read_b64_tr_b16 a[114:115], %[rt0] offset:16896\n\tds_read_b64_tr_b16 a[116:117], %[rt1] offset:16384\n\tds_read_b64_tr_b16 a[118:119], %[rt1] offset:16896\n\tds_read_b64_tr_b16 v[184:185], %[rt2] offset:16384\n\t" \
          "ds_read_b64_tr_b16 v[186:187], %[rt2] offset:16896\n\tds_read_b64_tr_b16 v[88:89], %[rt3] offset:16384\n\tds_read_b64_tr_b16 v[90:91], %[rt3] offset:16896\n\tds_read_b64_tr_b16 a[120:121], %[rt0] offset:20480\n\tds_read_b64_tr_b16 a[122:123], %[rt0] offset:20992\n\tds_read_b64_tr_b16 a[124:125], %[rt1] offset:20480\n\t" \
          "ds_read_b64_tr_b16 a[126:127], %[rt1] offset:20992\n\tds_read_b64_tr_b16 v[188:189], %[rt2] offset:20480\n\tds_read_b64_tr_b16 v[190:191], %[rt2] offset:20992\n\tds_read_b64_tr_b16 v[92:93], %[rt3] offset:20480\n\tds_read_b64_tr_b16 v[94:95], %[rt3] offset:20992\n\ts_waitcnt lgkmcnt(0)\n\t" \
          "s_add_u32 m0, %[wl], 16384\n\ts_xor_b32 %[t], %[wo], 0x180000\n\tbuffer_load_dwordx4 %[vw0], %[rw], %[t] offen lds\n\ts_add_u32 m0, %[wl], 17408\n\ts_xor_b32 %[t], %[wo], 0x184000\n\tbuffer_load_dwordx4 %[vw1], %[rw], %[t] offen lds\n\t" \
          "s_add_u32 m0, %[wl], 18432\n\ts_xor_b32 %[t], %[wo], 0x188000\n\tbuffer_load_dwordx4 %[vw0], %[rw], %[t] offen lds\n\ts_add_u32 m0, %[wl], 19456\n\ts_xor_b32 %[t], %[wo], 0x18c000\n\tbuffer_load_dwordx4 %[vw1], %[rw], %[t] offen lds\n\t" \
          "s_add_u32 m0, %[wl], 20480\n\ts_xor_b32 %[t], %[wo], 0x190000\n\tbuffer_load_dwordx4 %[vw0], %[rw], %[t] offen lds\n\ts_add_u32 m0, %[wl], 21504\n\ts_xor_b32 %[t], %[wo], 0x194000\n\tbuffer_load_dwordx4 %[vw1], %[rw], %[t] offen lds\n\t" \
          "s_add_u32 m0, %[wl], 22528\n\ts_xor_b32 %[t], %[wo], 0x198000\n\tbuffer_load_dwordx4 %[vw0], %[rw], %[t] offen lds\n\ts_add_u32 m0, %[wl], 23552\n\ts_xor_b32 %[t], %[wo], 0x19c000\n\tbuffer_load_dwordx4 %[vw1], %[rw], %[t] offen lds\n\t" \
          "s_waitcnt vmcnt(32)\n\tds_read_b64_tr_b16 a[128:129], %[rt0] offset:24576\n\tds_read_b64_tr_b16 a[130:131], %[rt0] offset:25088\n\tds_read_b64_tr_b16 a[132:133], %[rt1] offset:24576\n\tds_read_b64_tr_b16 a[134:135], %[rt1] offset:25088\n\tds_read_b64_tr_b16 v[192:193], %[rt2] offset:24576\n\t" \
          "ds_read_b64_tr_b16 v[194:195], %[rt2] offset:25088\n\tds_read_b64_tr_b16 v[96:97], %[rt3] offset:24576\n\tds_read_b64_tr_b16 v[98:99], %[rt3] offset:25088\n\tds_read_b64_tr_b16 a[136:137], %[rt0] offset:28672\n\tds_read_b64_tr_b16 a[138:139], %[rt0] offset:29184\n\tds_read_b64_tr_b16 a[140:141], %[rt1] offset:28672\n\t" \
          "ds_read_b64_tr_b16 a[142:143], %[rt1] offset:29184\n\tds_read_b64_tr_b16 v[196:197], %[rt2] offset:28672\n\tds_read_b64_tr_b16 v[198:199], %[rt2] offset:29184\n\tds_read_b64_tr_b16 v[100:101], %[rt3] offset:28672\n\tds_read_b64_tr_b16 v[102:103], %[rt3] offset:29184\n\ts_waitcnt lgkmcnt(0)\n\t" \
          "s_add_u32 m0, %[wl], 24576\n\ts_xor_b32 %[t], %[wo], 0x1a0000\n\tbuffer_load_dwordx4 %[vw0], %[rw], %[t] offen lds\n\ts_add_u32 m0, %[wl], 25600\n\ts_xor_b32 %[t], %[wo], 0x1a4000\n\tbuffer_load_dwordx4 %[vw1], %[rw], %[t] offen lds\n\t" \
          "s_add_u32 m0, %[wl], 26624\n\ts_xor_b32 %[t], %[wo], 0x1a8000\n\tbuffer_load_dwordx4 %[vw0], %[rw], %[t] offen lds\n\ts_add_u32 m0, %[wl], 27648\n\ts_xor_b32 %[t], %[wo], 0x1ac000\n\tbuffer_load_dwordx4 %[vw1], %[rw], %[t] offen lds\n\t" \
          "s_add_u32 m0, %[wl], 28672\n\ts_xor_b32 %[t], %[wo], 0x1b0000\n\tbuffer_load_dwordx4 %[vw0], %[rw], %[t] offen lds\n\ts_add_u32 m0, %[wl], 29696\n\ts_xor_b32 %[t], %[wo], 0x1b4000\n\tbuffer_load_dwordx4 %[vw1], %[rw], %[t] offen lds\n\t" \
          "s_add_u32 m0, %[wl], 30720\n\ts_xor_b32 %[t], %[wo], 0x1b8000\n\tbuffer_load_dwordx4 %[vw0], %[rw], %[t] offen lds\n\ts_add_u32 m0, %[wl], 31744\n\ts_xor_b32 %[t], %[wo], 0x1bc000\n\tbuffer_load_dwordx4 %[vw1], %[rw], %[t] offen lds\n\t" \
          "s_waitcnt vmcnt(32)\n\tds_read_b64_tr_b16 a[144:145], %[rt0] offset:32768\n\tds_read_b64_tr_b16 a[146:147], %[rt0] offset:33280\n\tds_read_b64_tr_b16 a[148:149], %[rt1] offset:32768\n\tds_read_b64_tr_b16 a[150:151], %[rt1] offset:33280\n\tds_read_b64_tr_b16 v[200:201], %[rt2] offset:32768\n\t" \
          "ds_read_b64_tr_b16 v[202:203], %[rt2] offset:33280\n\tds_read_b64_tr_b16 v[104:105], %[rt3] offset:32768\n\tds_read_b64_tr_b16 v[106:107], %[rt3] offset:33280\n\tds_read_b64_tr_b16 a[152:153], %[rt0] offset:36864\n\tds_read_b64_tr_b16 a[154:155], %[rt0] offset:37376\n\tds_read_b64_tr_b16 a[156:157], %[rt1] offset:36864\n\t" \
          "ds_read_b64_tr_b16 a[158:159], %[rt1] offset:37376\n\tds_read_b64_tr_b16 v[204:205], %[rt2] offset:36864\n\tds_read_b64_tr_b16 v[206:207], %[rt2] offset:37376\n\tds_read_b64_tr_b16 v[108:109], %[rt3] offset:36864\n\tds_read_b64_tr_b16 v[110:111], %[rt3] offset:37376\n\ts_waitcnt lgkmcnt(0)\n\t" \
          "s_add_u32 m0, %[wl], 32768\n\ts_xor_b32 %[t], %[wo], 0x1c0000\n\tbuffer_load_dwordx4 %[vw0], %[rw], %[t] offen lds\n\ts_add_u32 m0, %[wl], 33792\n\ts_xor_b32 %[t], %[wo], 0x1c4000\n\tbuffer_load_dwordx4 %[vw1], %[rw], %[t] offen lds\n\t" \
          "s_add_u32 m0, %[wl], 34816\n\ts_xor_b32 %[t], %[wo], 0x1c8000\n\tbuffer_load_dwordx4 %[vw0], %[rw], %[t] offen lds\n\ts_add_u32 m0, %[wl], 35840\n\ts_xor_b32 %[t], %[wo], 0x1cc000\n\tbuffer_load_dwordx4 %[vw1], %[rw], %[t] offen lds\n\t" \
          "s_add_u32 m0, %[wl], 36864\n\ts_xor_b32 %[t], %[wo], 0x1d0000\n\tbuffer_load_dwordx4 %[vw0], %[rw], %[t] offen lds\n\ts_add_u32 m0, %[wl], 37888\n\ts_xor_b32 %[t], %[wo], 0x1d4000\n\tbuffer_load_dwordx4 %[vw1], %[rw], %[t] offen lds\n\t" \
          "s_add_u32 m0, %[wl], 38912\n\ts_xor_b32 %[t], %[wo], 0x1d8000\n\tbuffer_load_dwordx4 %[vw0], %[rw], %[t] offen lds\n\ts_add_u32 m0, %[wl], 39936\n\ts_xor_b32 %[t], %[wo], 0x1dc000\n\tbuffer_load_dwordx4 %[vw1], %[rw], %[t] offen lds\n\t" \
          "s_waitcnt vmcnt(32)\n\tds_read_b64_tr_b16 a[160:161], %[rt0] offset:0\n\tds_read_b64_tr_b16 a[162:163], %[rt0] offset:512\n\tds_read_b64_tr_b16 a[164:165], %[rt1] offset:0\n\tds_read_b64_tr_b16 a[166:167], %[rt1] offset:512\n\tds_read_b64_tr_b16 v[208:209], %[rt2] offset:0\n\t" \
          "ds_read_b64_tr_b16 v[210:211], %[rt2] offset:512\n\tds_read_b64_tr_b16 v[112:113], %[rt3] offset:0\n\tds_read_b64_tr_b16 v[114:115], %[rt3] offset:512\n\tds_read_b64_tr_b16 a[168:169], %[rt0] offset:4096\n\tds_read_b64_tr_b16 a[170:171], %[rt0] offset:4608\n\tds_read_b64_tr_b16 a[172:173], %[rt1] offset:4096\n\t" \
          "ds_read_b64_tr_b16 a[174:175], %[rt1] offset:4608\n\tds_read_b64_tr_b16 v[212:213], %[rt2] offset:4096\n\tds_read_b64_tr_b16 v[214:215], %[rt2] offset:4608\n\tds_read_b64_tr_b16 v[116:117], %[rt3] offset:4096\n\tds_read_b64_tr_b16 v[118:119], %[rt3] offset:4608\n\ts_waitcnt lgkmcnt(0)\n\t" \
          "s_add_u32 m0, %[wl], 0\n\ts_xor_b32 %[t], %[wo], 0x1e0000\n\tbuffer_load_dwordx4 %[vw0], %[rw], %[t] offen lds\n\ts_add_u32 m0, %[wl], 1024\n\ts_xor_b32 %[t], %[wo], 0x1e4000\n\tbuffer_load_dwordx4 %[vw1], %[rw], %[t] offen lds\n\t" \
          "s_add_u32 m0, %[wl], 2048\n\ts_xor_b32 %[t], %[wo], 0x1e8000\n\tbuffer_load_dwordx4 %[vw0], %[rw], %[t] offen lds\n\ts_add_u32 m0, %[wl], 3072\n\ts_xor_b32 %[t], %[wo], 0x1ec000\n\tbuffer_load_dwordx4 %[vw1], %[rw], %[t] offen lds\n\t" \
          "s_add_u32 m0, %[wl], 4096\n\ts_xor_b32 %[t], %[wo], 0x1f0000\n\tbuffer_load_dwordx4 %[vw0], %[rw], %[t] offen lds\n\ts_add_u32 m0, %[wl], 5120\n\ts_xor_b32 %[t], %[wo], 0x1f4000\n\tbuffer_load_dwordx4 %[vw1], %[rw], %[t] offen lds\n\t" \
          "s_add_u32 m0, %[wl], 6144\n\ts_xor_b32 %[t], %[wo], 0x1f8000\n\tbuffer_load_dwordx4 %[vw0], %[rw], %[t] offen lds\n\ts_add_u32 m0, %[wl], 7168\n\ts_xor_b32 %[t], %[wo], 0x1fc000\n\tbuffer_load_dwordx4 %[vw1], %[rw], %[t] offen lds\n\t" \
          "s_waitcnt vmcnt(32)\n\tds_read_b64_tr_b16 a[176:177], %[rt0] offset:8192\n\tds_read_b64_tr_b16 a[178:179], %[rt0] offset:8704\n\tds_read_b64_tr_b16 a[180:181], %[rt1] offset:8192\n\tds_read_b64_tr_b16 a[182:183], %[rt1] offset:8704\n\tds_read_b64_tr_b16 v[216:217], %[rt2] offset:8192\n\t" \
          "ds_read_b64_tr_b16 v[218:219], %[rt2] offset:8704\n\tds_read_b64_tr_b16 v[120:121], %[rt3] offset:8192\n\tds_read_b64_tr_b16 v[122:123], %[rt3] offset:8704\n\tds_read_b64_tr_b16 a[184:185], %[rt0] offset:12288\n\tds_read_b64_tr_b16 a[186:187], %[rt0] offset:12800\n\tds_read_b64_tr_b16 a[188:189], %[rt1] offset:12288\n\t" \
          "ds_read_b64_tr_b16 a[190:191], %[rt1] offset:12800\n\tds_read_b64_tr_b16 v[220:221], %[rt2] offset:12288\n\tds_read_b64_tr_b16 v[222:223], %[rt2] offset:12800\n\tds_read_b64_tr_b16 v[124:125], %[rt3] offset:12288\n\tds_read_b64_tr_b16 v[126:127], %[rt3] offset:12800\n\ts_waitcnt lgkmcnt(0)\n\t" \
          "ds_write_b128 %[own], v[32:35] offset:8192\n\tds_write_b128 %[own], v[36:39] offset:9216\n\tds_write_b128 %[own], v[40:43] offset:10240\n\tds_write_b128 %[own], v[44:47] offset:11264\n\tds_write_b128 %[own], v[48:51] offset:12288\n\tds_write_b128 %[own], v[52:55] offset:13312\n\t" \
          "ds_write_b128 %[own], v[56:59] offset:14336\n\tds_write_b128 %[own], v[60:63] offset:15360\n\ts_waitcnt lgkmcnt(0)\n\ts_waitcnt vmcnt(24)\n\tds_read_b64_tr_b16 a[192:193], %[rt0] offset:16384\n\tds_read_b64_tr_b16 a[194:195], %[rt0] offset:16896\n\t" \
          "ds_read_b64_tr_b16 a[196:197], %[rt1] offset:16384\n\tds_read_b64_tr_b16 a[198:199], %[rt1] offset:16896\n\tds_read_b64_tr_b16 v[224:225], %[rt2] offset:16384\n\tds_read_b64_tr_b16 v[226:227], %[rt2] offset:16896\n\tds_read_b64_tr_b16 v[32:33], %[rt3] offset:16384\n\tds_read_b64_tr_b16 v[34:35], %[rt3] offset:16896\n\t" \
          "ds_read_b64_tr_b16 a[200:201], %[rt0] offset:20480\n\tds_read_b64_tr_b16 a[202:203], %[rt0] offset:20992\n\tds_read_b64_tr_b16 a[204:205], %[rt1] offset:20480\n\tds_read_b64_tr_b16 a[206:207], %[rt1] offset:20992\n\tds_read_b64_tr_b16 v[228:229], %[rt2] offset:20480\n\tds_read_b64_tr_b16 v[230:231], %[rt2] offset:20992\n\t" \
          "ds_read_b64_tr_b16 v[36:37], %[rt3] offset:20480\n\tds_read_b64_tr_b16 v[38:39], %[rt3] offset:20992\n\ts_waitcnt lgkmcnt(0)\n\tds_write_b128 %[own], v[64:67] offset:16384\n\tds_write_b128 %[own], v[68:71] offset:17408\n\tds_write_b128 %[own], v[72:75] offset:18432\n\t" \
          "ds_write_b128 %[own], v[76:79] offset:19456\n\tds_write_b128 %[own], v[80:83] offset:20480\n\tds_write_b128 %[own], v[84:87] offset:21504\n\tds_write_b128 %[own], v[88:91] offset:22528\n\tds_write_b128 %[own], v[92:95] offset:23552\n\ts_waitcnt lgkmcnt(0)\n\t" \
          "s_waitcnt vmcnt(16)\n\tds_read_b64_tr_b16 a[208:209], %[rt0] offset:24576\n\tds_read_b64_tr_b16 a[210:211], %[rt0] offset:25088\n\tds_read_b64_tr_b16 a[212:213], %[rt1] offset:24576\n\tds_read_b64_tr_b16 a[214:215], %[rt1] offset:25088\n\tds_read_b64_tr_b16 v[232:233], %[rt2] offset:24576\n\t" \
          "ds_read_b64_tr_b16 v[234:235], %[rt2] offset:25088\n\tds_read_b64_tr_b16 v[40:41], %[rt3] offset:24576\n\tds_read_b64_tr_b16 v[42:43], %[rt3] offset:25088\n\tds_read_b64_tr_b16 a[216:217], %[rt0] offset:28672\n\tds_read_b64_tr_b16 a[218:219], %[rt0] offset:29184\n\tds_read_b64_tr_b16 a[220:221], %[rt1] offset:28672\n\t" \
          "ds_read_b64_tr_b16 a[222:223], %[rt1] offset:29184\n\tds_read_b64_tr_b16 v[236:237], %[rt2] offset:28672\n\tds_read_b64_tr_b16 v[238:239], %[rt2] offset:29184\n\tds_read_b64_tr_b16 v[44:45], %[rt3] offset:28672\n\tds_read_b64_tr_b16 v[46:47], %[rt3] offset:29184\n\ts_waitcnt lgkmcnt(0)\n\t" \
          "ds_write_b128 %[own], v[96:99] offset:24576\n\tds_write_b128 %[own], v[100:103] offset:25600\n\tds_write_b128 %[own], v[104:107] offset:26624\n\tds_write_b128 %[own], v[108:111] offset:27648\n\tds_write_b128 %[own], v[112:115] offset:28672\n\tds_write_b128 %[own], v[116:119] offset:29696\n\t" \
          "ds_write_b128 %[own], v[120:123] offset:30720\n\tds_write_b128 %[own], v[124:127] offset:31744\n\ts_waitcnt lgkmcnt(0)\n\ts_waitcnt vmcnt(8)\n\tds_read_b64_tr_b16 a[224:225], %[rt0] offset:32768\n\tds_read_b64_tr_b16 a[226:227], %[rt0] offset:33280\n\t" \
          "ds_read_b64_tr_b16 a[228:229], %[rt1] offset:32768\n\tds_read_b64_tr_b16 a[230:231], %[rt1] offset:33280\n\tds_read_b64_tr_b16 v[240:241], %[rt2] offset:32768\n\tds_read_b64_tr_b16 v[242:243], %[rt2] offset:33280\n\tds_read_b64_tr_b16 v[48:49], %[rt3] offset:32768\n\tds_read_b64_tr_b16 v[50:51], %[rt3] offset:33280\n\t" \
          "ds_read_b64_tr_b16 a[232:233], %[rt0] offset:36864\n\tds_read_b64_tr_b16 a[234:235], %[rt0] offset:37376\n\tds_read_b64_tr_b16 a[236:237], %[rt1] offset:36864\n\tds_read_b64_tr_b16 a[238:239], %[rt1] offset:37376\n\tds_read_b64_tr_b16 v[244:245], %[rt2] offset:36864\n\tds_read_b64_tr_b16 v[246:247], %[rt2] offset:37376\n\t" \
          "ds_read_b64_tr_b16 v[52:53], %[rt3] offset:36864\n\tds_read_b64_tr_b16 v[54:55], %[rt3] offset:37376\n\ts_waitcnt lgkmcnt(0)\n\ts_waitcnt vmcnt(0)\n\tds_read_b64_tr_b16 a[240:241], %[rt0] offset:0\n\tds_read_b64_tr_b16 a[242:243], %[rt0] offset:512\n\t" \
          "ds_read_b64_tr_b16 a[244:245], %[rt1] offset:0\n\tds_read_b64_tr_b16 a[246:247], %[rt1] offset:512\n\tds_read_b64_tr_b16 v[248:249], %[rt2] offset:0\n\tds_read_b64_tr_b16 v[250:251], %[rt2] offset:512\n\tds_read_b64_tr_b16 v[56:57], %[rt3] offset:0\n\tds_read_b64_tr_b16 v[58:59], %[rt3] offset:512\n\t" \
          "ds_read_b64_tr_b16 a[248:249], %[rt0] offset:4096\n\tds_read_b64_tr_b16 a[250:251], %[rt0] offset:4608\n\tds_read_b64_tr_b16 a[252:253], %[rt1] offset:4096\n\tds_read_b64_tr_b16 a[254:255], %[rt1] offset:4608\n\tds_read_b64_tr_b16 v[252:253], %[rt2] offset:4096\n\tds_read_b64_tr_b16 v[254:255], %[rt2] offset:4608\n\t" \
          "ds_read_b64_tr_b16 v[60:61], %[rt3] offset:4096\n\tds_read_b64_tr_b16 v[62:63], %[rt3] offset:4608\n\ts_waitcnt lgkmcnt(0)\n\tds_write_b128 %[own], v[32:35] offset:0\n\tds_write_b128 %[own], v[36:39] offset:1024\n\tds_write_b128 %[own], v[40:43] offset:2048\n\t" \
          "ds_write_b128 %[own], v[44:47] offset:3072\n\tds_write_b128 %[own], v[48:51] offset:4096\n\tds_write_b128 %[own], v[52:55] offset:5120\n\tds_write_b128 %[own], v[56:59] offset:6144\n\tds_write_b128 %[own], v[60:63] offset:7168\n\ts_waitcnt lgkmcnt(0)\n\t" \
          "Lou_wait%=:\n\ts_mov_b32 %[cnt], 0\n\tLou_poll%=:\n\ts_lshr_b32 %[t], %[j], 1\n\ts_cmp_lt_u32 %[t], %[av]\n\ts_cbranch_scc1 Lou_load%=\n\t" \
          "s_sleep 2\n\ts_lshl_b32 %[t], %[av], 10\n\ts_add_u32 %[t], %[t], %[fo]\n\tbuffer_load_dwordx2 v[30:31], %[vf], %[rf], %[t] offen sc1\n\tbuffer_load_dword v48, off, %[rf], %[tmo] sc1\n\ts_waitcnt vmcnt(0)\n\t" \
          "s_lshr_b32 %[stp], %[j], 1\n\ts_cmp_eq_u32 %[stp], %[av]\n\ts_cselect_b32 %[stp], %[sp], 1\n\tv_cmp_eq_u32 vcc, 0, v30\n\ts_nop 0\n\ts_ff1_i32_b64 %[t], vcc\n\t" \
          "s_lshr_b32 %[t], %[t], 1\n\ts_add_u32 %[av], %[av], %[t]\n\ts_or_b32 %[stp], %[stp], %[t]\n\ts_cbranch_scc1 Lou_ns%=\n\tv_readlane_b32 %[t], v31, 0\n\tv_readlane_b32 %[stp], v31, 1\n\t" \
          "s_and_b32 vcc_lo, %[t], %[stp]\n\ts_cbranch_scc0 Lou_ns%=\n\ts_sub_i32 vcc_lo, %[t], %[stp]\n\ts_cmp_gt_i32 vcc_lo, 0\n\ts_cselect_b32 %[t], %[t], %[stp]\n\ts_mov_b32 %[sp], 1\n\t" \
          "Lou_tw%=:\n\ts_memrealtime vcc\n\ts_waitcnt lgkmcnt(0)\n\ts_sub_i32 vcc_lo, %[t], vcc_lo\n\ts_sub_u32 vcc_lo, vcc_lo, 1\n\ts_cmp_lt_u32 vcc_lo, 2000\n\t" \
          "s_cbranch_scc1 Lou_tw%=\n\ts_branch Lou_load%=\n\tLou_ns%=:\n\tv_readfirstlane_b32 vcc_lo, v48\n\ts_add_u32 %[cnt], %[cnt], 1\n\ts_lshr_b32 vcc_hi, %[cnt], 15\n\t" \
          "s_or_b32 vcc_lo, vcc_lo, vcc_hi\n\ts_cbranch_scc0 Lou_poll%=\n\tv_mov_b32 v48, 7\n\tbuffer_store_dword v48, off, %[rf], %[tmo] sc1\n\ts_mov_b32 %[av], 64\n\tLou_load%=:\n\t" \
          "" OU_ST0 "s_nop 0\n\tbuffer_load_dwordx4 v[64:67], %[vo0], s[40:43], %[pg0] offen sc1\n\tbuffer_load_dwordx4 v[68:71], %[vo0], s[40:43], %[pg0] offen offset:1024 sc1\n\tbuffer_load_dwordx4 v[72:75], %[vo0], s[40:43], %[pg0] offen offset:2048 sc1\n\tbuffer_load_dwordx4 v[76:79], %[vo0], s[40:43], %[pg0] offen offset:3072 sc1\n\tbuffer_load_dwordx4 v[80:83], %[vo1], s[40:43], %[pg0] offen sc1\n\t" \
          "buffer_load_dwordx4 v[84:87], %[vo1], s[40:43], %[pg0] offen offset:1024 sc1\n\tbuffer_load_dwordx4 v[88:91], %[vo1], s[40:43], %[pg0] offen offset:2048 sc1\n\tbuffer_load_dwordx4 v[92:95], %[vo1], s[40:43], %[pg0] offen offset:3072 sc1\n\tbuffer_load_dwordx4 v[96:99], %[vo0], s[40:43], %[pg1] offen sc1\n\tbuffer_load_dwordx4 v[100:103], %[vo0], s[40:43], %[pg1] offen offset:1024 sc1\n\tbuffer_load_dwordx4 v[104:107], %[vo0], s[40:43], %[pg1] offen offset:2048 sc1\n\t" \
          "buffer_load_dwordx4 v[108:111], %[vo0], s[40:43], %[pg1] offen offset:3072 sc1\n\tbuffer_load_dwordx4 v[112:115], %[vo1], s[40:43], %[pg1] offen sc1\n\tbuffer_load_dwordx4 v[116:119], %[vo1], s[40:43], %[pg1] offen offset:1024 sc1\n\tbuffer_load_dwordx4 v[120:123], %[vo1], s[40:43], %[pg1] offen offset:2048 sc1\n\tbuffer_load_dwordx4 v[124:127], %[vo1], s[40:43], %[pg1] offen offset:3072 sc1\n\tLou_top%=:\n\t" \
          "s_lshl_b32 %[t], %[av], 10\n\ts_add_u32 %[t], %[t], %[fo]\n\tbuffer_load_dwordx2 v[30:31], %[vf], %[rf], %[t] offen sc1\n\tds_read_b128 v[32:35], %[own] offset:8192\n\tds_read_b128 v[36:39], %[own] offset:9216\n\tds_read_b128 v[40:43], %[own] offset:10240\n\t" \
          "s_waitcnt vmcnt(16) lgkmcnt(2)\n\tv_mfma_f32_16x16x32_bf16 v[48:51], a[0:3], v[64:67], 0\n\tds_read_b128 v[44:47], %[own] offset:11264\n\tv_mfma_f32_16x16x32_bf16 v[52:55], a[4:7], v[64:67], 0\n\tv_mfma_f32_16x16x32_bf16 v[56:59], v[128:131], v[64:67], 0\n\tv_mfma_f32_16x16x32_bf16 v[60:63], v[32:35], v[64:67], 0\n\t" \
          "buffer_load_dwordx4 v[64:67], %[vo0], s[40:43], %[pg2] offen sc1\n\ts_waitcnt vmcnt(16) lgkmcnt(2)\n\tv_mfma_f32_16x16x32_bf16 v[48:51], a[8:11], v[68:71], v[48:51]\n\tds_read_b128 v[32:35], %[own] offset:12288\n\tv_mfma_f32_16x16x32_bf16 v[52:55], a[12:15], v[68:71], v[52:55]\n\tv_mfma_f32_16x16x32_bf16 v[56:59], v[132:135], v[68:71], v[56:59]\n\t" \
          "v_mfma_f32_16x16x32_bf16 v[60:63], v[36:39], v[68:71], v[60:63]\n\tbuffer_load_dwordx4 v[68:71], %[vo0], s[40:43], %[pg2] offen offset:1024 sc1\n\ts_waitcnt vmcnt(16) lgkmcnt(2)\n\tv_mfma_f32_16x16x32_bf16 v[48:51], a[16:19], v[72:75], v[48:51]\n\tds_read_b128 v[36:39], %[own] offset:13312\n\tv_mfma_f32_16x16x32_bf16 v[52:55], a[20:23], v[72:75], v[52:55]\n\t" \
          "v_mfma_f32_16x16x32_bf16 v[56:59], v[136:139], v[72:75], v[56:59]\n\tv_mfma_f32_16x16x32_bf16 v[60:63], v[40:43], v[72:75], v[60:63]\n\tbuffer_load_dwordx4 v[72:75], %[vo0], s[40:43], %[pg2] offen offset:2048 sc1\n\ts_waitcnt vmcnt(16) lgkmcnt(2)\n\tv_mfma_f32_16x16x32_bf16 v[48:51], a[24:27], v[76:79], v[48:51]\n\tds_read_b128 v[40:43], %[own] offset:14336\n\t" \
          "v_mfma_f32_16x16x32_bf16 v[52:55], a[28:31], v[76:79], v[52:55]\n\tv_mfma_f32_16x16x32_bf16 v[56:59], v[140:143], v[76:79], v[56:59]\n\tv_mfma_f32_16x16x32_bf16 v[60:63], v[44:47], v[76:79], v[60:63]\n\tbuffer_load_dwordx4 v[76:79], %[vo0], s[40:43], %[pg2] offen offset:3072 sc1\n\ts_waitcnt vmcnt(16) lgkmcnt(2)\n\tv_mfma_f32_16x16x32_bf16 v[48:51], a[32:35], v[80:83], v[48:51]\n\t" \
          "ds_read_b128 v[44:47], %[own] offset:15360\n\tv_mfma_f32_16x16x32_bf16 v[52:55], a[36:39], v[80:83], v[52:55]\n\tv_mfma_f32_16x16x32_bf16 v[56:59], v[144:147], v[80:83], v[56:59]\n\tv_mfma_f32_16x16x32_bf16 v[60:63], v[32:35], v[80:83], v[60:63]\n\tbuffer_load_dwordx4 v[80:83], %[vo1], s[40:43], %[pg2] offen sc1\n\ts_waitcnt vmcnt(16) lgkmcnt(2)\n\t" \
          "v_mfma_f32_16x16x32_bf16 v[48:51], a[40:43], v[84:87], v[48:51]\n\tds_read_b128 v[32:35], %[own] offset:16384\n\tv_mfma_f32_16x16x32_bf16 v[52:55], a[44:47], v[84:87], v[52:55]\n\tv_mfma_f32_16x16x32_bf16 v[56:59], v[148:151], v[84:87], v[56:59]\n\tv_mfma_f32_16x16x32_bf16 v[60:63], v[36:39], v[84:87], v[60:63]\n\tbuffer_load_dwordx4 v[84:87], %[vo1], s[40:43], %[pg2] offen offset:1024 sc1\n\t" \
          "s_waitcnt vmcnt(16) lgkmcnt(2)\n\tv_mfma_f32_16x16x32_bf16 v[48:51], a[48:51], v[88:91], v[48:51]\n\tds_read_b128 v[36:39], %[own] offset:17408\n\tv_mfma_f32_16x16x32_bf16 v[52:55], a[52:55], v[88:91], v[52:55]\n\tv_mfma_f32_16x16x32_bf16 v[56:59], v[152:155], v[88:91], v[56:59]\n\tv_mfma_f32_16x16x32_bf16 v[60:63], v[40:43], v[88:91], v[60:63]\n\t" \
          "buffer_load_dwordx4 v[88:91], %[vo1], s[40:43], %[pg2] offen offset:2048 sc1\n\ts_waitcnt vmcnt(16) lgkmcnt(2)\n\tv_mfma_f32_16x16x32_bf16 v[48:51], a[56:59], v[92:95], v[48:51]\n\tds_read_b128 v[40:43], %[own] offset:18432\n\tv_mfma_f32_16x16x32_bf16 v[52:55], a[60:63], v[92:95], v[52:55]\n\tv_mfma_f32_16x16x32_bf16 v[56:59], v[156:159], v[92:95], v[56:59]\n\t" \
          "v_mfma_f32_16x16x32_bf16 v[60:63], v[44:47], v[92:95], v[60:63]\n\tbuffer_load_dwordx4 v[92:95], %[vo1], s[40:43], %[pg2] offen offset:3072 sc1\n\ts_waitcnt vmcnt(16) lgkmcnt(2)\n\tv_mfma_f32_16x16x32_bf16 v[48:51], a[64:67], v[96:99], v[48:51]\n\tds_read_b128 v[44:47], %[own] offset:19456\n\tv_mfma_f32_16x16x32_bf16 v[52:55], a[68:71], v[96:99], v[52:55]\n\t" \
          "v_mfma_f32_16x16x32_bf16 v[56:59], v[160:163], v[96:99], v[56:59]\n\tv_mfma_f32_16x16x32_bf16 v[60:63], v[32:35], v[96:99], v[60:63]\n\tbuffer_load_dwordx4 v[96:99], %[vo0], s[40:43], %[pg3] offen sc1\n\ts_waitcnt vmcnt(16) lgkmcnt(2)\n\tv_mfma_f32_16x16x32_bf16 v[48:51], a[72:75], v[100:103], v[48:51]\n\tds_read_b128 v[32:35], %[own] offset:20480\n\t" \
          "v_mfma_f32_16x16x32_bf16 v[52:55], a[76:79], v[100:103], v[52:55]\n\tv_mfma_f32_16x16x32_bf16 v[56:59], v[164:167], v[100:103], v[56:59]\n\tv_mfma_f32_16x16x32_bf16 v[60:63], v[36:39], v[100:103], v[60:63]\n\tbuffer_load_dwordx4 v[100:103], %[vo0], s[40:43], %[pg3] offen offset:1024 sc1\n\ts_waitcnt vmcnt(16) lgkmcnt(2)\n\tv_mfma_f32_16x16x32_bf16 v[48:51], a[80:83], v[104:107], v[48:51]\n\t" \
          "ds_read_b128 v[36:39], %[own] offset:21504\n\tv_mfma_f32_16x16x32_bf16 v[52:55], a[84:87], v[104:107], v[52:55]\n\tv_mfma_f32_16x16x32_bf16 v[56:59], v[168:171], v[104:107], v[56:59]\n\tv_mfma_f32_16x16x32_bf16 v[60:63], v[40:43], v[104:107], v[60:63]\n\tbuffer_load_dwordx4 v[104:107], %[vo0], s[40:43], %[pg3] offen offset:2048 sc1\n\ts_waitcnt vmcnt(16) lgkmcnt(2)\n\t" \
          "v_mfma_f32_16x16x32_bf16 v[48:51], a[88:91], v[108:111], v[48:51]\n\tds_read_b128 v[40:43], %[own] offset:22528\n\tv_mfma_f32_16x16x32_bf16 v[52:55], a[92:95], v[108:111], v[52:55]\n\tv_mfma_f32_16x16x32_bf16 v[56:59], v[172:175], v[108:111], v[56:59]\n\tv_mfma_f32_16x16x32_bf16 v[60:63], v[44:47], v[108:111], v[60:63]\n\tbuffer_load_dwordx4 v[108:111], %[vo0], s[40:43], %[pg3] offen offset:3072 sc1\n\t" \
          "s_waitcnt vmcnt(16) lgkmcnt(2)\n\tv_mfma_f32_16x16x32_bf16 v[48:51], a[96:99], v[112:115], v[48:51]\n\tds_read_b128 v[44:47], %[own] offset:23552\n\tv_mfma_f32_16x16x32_bf16 v[52:55], a[100:103], v[112:115], v[52:55]\n\tv_mfma_f32_16x16x32_bf16 v[56:59], v[176:179], v[112:115], v[56:59]\n\tv_mfma_f32_16x16x32_bf16 v[60:63], v[32:35], v[112:115], v[60:63]\n\t" \
          "buffer_load_dwordx4 v[112:115], %[vo1], s[40:43], %[pg3] offen sc1\n\ts_waitcnt vmcnt(16) lgkmcnt(2)\n\tv_mfma_f32_16x16x32_bf16 v[48:51], a[104:107], v[116:119], v[48:51]\n\tds_read_b128 v[32:35], %[own] offset:24576\n\tv_mfma_f32_16x16x32_bf16 v[52:55], a[108:111], v[116:119], v[52:55]\n\tv_mfma_f32_16x16x32_bf16 v[56:59], v[180:183], v[116:119], v[56:59]\n\t" \
          "v_mfma_f32_16x16x32_bf16 v[60:63], v[36:39], v[116:119], v[60:63]\n\tbuffer_load_dwordx4 v[116:119], %[vo1], s[40:43], %[pg3] offen offset:1024 sc1\n\ts_waitcnt vmcnt(16) lgkmcnt(2)\n\tv_mfma_f32_16x16x32_bf16 v[48:51], a[112:115], v[120:123], v[48:51]\n\tds_read_b128 v[36:39], %[own] offset:25600\n\tv_mfma_f32_16x16x32_bf16 v[52:55], a[116:119], v[120:123], v[52:55]\n\t" \
          "v_mfma_f32_16x16x32_bf16 v[56:59], v[184:187], v[120:123], v[56:59]\n\tv_mfma_f32_16x16x32_bf16 v[60:63], v[40:43], v[120:123], v[60:63]\n\tbuffer_load_dwordx4 v[120:123], %[vo1], s[40:43], %[pg3] offen offset:2048 sc1\n\ts_waitcnt vmcnt(16) lgkmcnt(2)\n\tv_mfma_f32_16x16x32_bf16 v[48:51], a[120:123], v[124:127], v[48:51]\n\tds_read_b128 v[40:43], %[own] offset:26624\n\t" \
          "v_mfma_f32_16x16x32_bf16 v[52:55], a[124:127], v[124:127], v[52:55]\n\tv_mfma_f32_16x16x32_bf16 v[56:59], v[188:191], v[124:127], v[56:59]\n\tv_mfma_f32_16x16x32_bf16 v[60:63], v[44:47], v[124:127], v[60:63]\n\tbuffer_load_dwordx4 v[124:127], %[vo1], s[40:43], %[pg3] offen offset:3072 sc1\n\ts_waitcnt vmcnt(15) lgkmcnt(2)\n\tv_mfma_f32_16x16x32_bf16 v[48:51], a[128:131], v[64:67], v[48:51]\n\t" \
          "ds_read_b128 v[44:47], %[own] offset:27648\n\tv_mfma_f32_16x16x32_bf16 v[52:55], a[132:135], v[64:67], v[52:55]\n\tv_cmp_eq_u32 vcc, 0, v30\n\ts_nop 0\n\ts_ff1_i32_b64 %[t], vcc\n\ts_lshr_b32 %[t], %[t], 1\n\t" \
          "s_add_u32 %[av], %[av], %[t]\n\tv_mfma_f32_16x16x32_bf16 v[56:59], v[192:195], v[64:67], v[56:59]\n\ts_lshl_b32 %[oo], %[j], 16\n\ts_cmp_lt_u32 %[j], %[j1]\n\ts_cselect_b32 %[stp], 2, 4\n\ts_add_u32 %[j], %[j], %[stp]\n\t" \
          "s_lshl_b32 %[t], %[stp], 14\n\ts_add_u32 %[pg0], %[pg0], %[t]\n\ts_add_u32 %[pg1], %[pg1], %[t]\n\ts_add_u32 %[pg2], %[pg2], %[t]\n\ts_add_u32 %[pg3], %[pg3], %[t]\n\ts_lshr_b32 %[t], %[j], 1\n\t" \
          "s_cmp_lt_u32 %[t], %[av]\n\ts_cselect_b32 s46, s42, 0\n\tv_mfma_f32_16x16x32_bf16 v[60:63], v[32:35], v[64:67], v[60:63]\n\tbuffer_load_dwordx4 v[64:67], %[vo0], s[44:47], %[pg0] offen sc1\n\ts_waitcnt vmcnt(15) lgkmcnt(2)\n\tv_mfma_f32_16x16x32_bf16 v[48:51], a[136:139], v[68:71], v[48:51]\n\t" \
          "ds_read_b128 v[32:35], %[own] offset:28672\n\tv_mfma_f32_16x16x32_bf16 v[52:55], a[140:143], v[68:71], v[52:55]\n\tv_mfma_f32_16x16x32_bf16 v[56:59], v[196:199], v[68:71], v[56:59]\n\tv_mfma_f32_16x16x32_bf16 v[60:63], v[36:39], v[68:71], v[60:63]\n\tbuffer_load_dwordx4 v[68:71], %[vo0], s[44:47], %[pg0] offen offset:1024 sc1\n\ts_waitcnt vmcnt(15) lgkmcnt(2)\n\t" \
          "v_mfma_f32_16x16x32_bf16 v[48:51], a[144:147], v[72:75], v[48:51]\n\tds_read_b128 v[36:39], %[own] offset:29696\n\tv_mfma_f32_16x16x32_bf16 v[52:55], a[148:151], v[72:75], v[52:55]\n\tv_mfma_f32_16x16x32_bf16 v[56:59], v[200:203], v[72:75], v[56:59]\n\tv_mfma_f32_16x16x32_bf16 v[60:63], v[40:43], v[72:75], v[60:63]\n\tbuffer_load_dwordx4 v[72:75], %[vo0], s[44:47], %[pg0] offen offset:2048 sc1\n\t" \
          "s_waitcnt vmcnt(15) lgkmcnt(2)\n\tv_mfma_f32_16x16x32_bf16 v[48:51], a[152:155], v[76:79], v[48:51]\n\tds_read_b128 v[40:43], %[own] offset:30720\n\tv_mfma_f32_16x16x32_bf16 v[52:55], a[156:159], v[76:79], v[52:55]\n\tv_mfma_f32_16x16x32_bf16 v[56:59], v[204:207], v[76:79], v[56:59]\n\tv_mfma_f32_16x16x32_bf16 v[60:63], v[44:47], v[76:79], v[60:63]\n\t" \
          "buffer_load_dwordx4 v[76:79], %[vo0], s[44:47], %[pg0] offen offset:3072 sc1\n\ts_waitcnt vmcnt(15) lgkmcnt(2)\n\tv_mfma_f32_16x16x32_bf16 v[48:51], a[160:163], v[80:83], v[48:51]\n\tds_read_b128 v[44:47], %[own] offset:31744\n\tv_mfma_f32_16x16x32_bf16 v[52:55], a[164:167], v[80:83], v[52:55]\n\tv_mfma_f32_16x16x32_bf16 v[56:59], v[208:211], v[80:83], v[56:59]\n\t" \
          "v_mfma_f32_16x16x32_bf16 v[60:63], v[32:35], v[80:83], v[60:63]\n\tbuffer_load_dwordx4 v[80:83], %[vo1], s[44:47], %[pg0] offen sc1\n\ts_waitcnt vmcnt(15) lgkmcnt(2)\n\tv_mfma_f32_16x16x32_bf16 v[48:51], a[168:171], v[84:87], v[48:51]\n\tds_read_b128 v[32:35], %[own] offset:0\n\tv_mfma_f32_16x16x32_bf16 v[52:55], a[172:175], v[84:87], v[52:55]\n\t" \
          "v_mfma_f32_16x16x32_bf16 v[56:59], v[212:215], v[84:87], v[56:59]\n\tv_mfma_f32_16x16x32_bf16 v[60:63], v[36:39], v[84:87], v[60:63]\n\tbuffer_load_dwordx4 v[84:87], %[vo1], s[44:47], %[pg0] offen offset:1024 sc1\n\ts_waitcnt vmcnt(15) lgkmcnt(2)\n\tv_mfma_f32_16x16x32_bf16 v[48:51], a[176:179], v[88:91], v[48:51]\n\tds_read_b128 v[36:39], %[own] offset:1024\n\t" \
          "v_mfma_f32_16x16x32_bf16 v[52:55], a[180:183], v[88:91], v[52:55]\n\tv_mfma_f32_16x16x32_bf16 v[56:59], v[216:219], v[88:91], v[56:59]\n\tv_mfma_f32_16x16x32_bf16 v[60:63], v[40:43], v[88:91], v[60:63]\n\tbuffer_load_dwordx4 v[88:91], %[vo1], s[44:47], %[pg0] offen offset:2048 sc1\n\ts_waitcnt vmcnt(15) lgkmcnt(2)\n\tv_mfma_f32_16x16x32_bf16 v[48:51], a[184:187], v[92:95], v[48:51]\n\t" \
          "ds_read_b128 v[40:43], %[own] offset:2048\n\tv_mfma_f32_16x16x32_bf16 v[52:55], a[188:191], v[92:95], v[52:55]\n\tv_mfma_f32_16x16x32_bf16 v[56:59], v[220:223], v[92:95], v[56:59]\n\tv_mfma_f32_16x16x32_bf16 v[60:63], v[44:47], v[92:95], v[60:63]\n\tbuffer_load_dwordx4 v[92:95], %[vo1], s[44:47], %[pg0] offen offset:3072 sc1\n\ts_waitcnt vmcnt(15) lgkmcnt(2)\n\t" \
          "v_mfma_f32_16x16x32_bf16 v[48:51], a[192:195], v[96:99], v[48:51]\n\tds_read_b128 v[44:47], %[own] offset:3072\n\tv_mfma_f32_16x16x32_bf16 v[52:55], a[196:199], v[96:99], v[52:55]\n\tv_mfma_f32_16x16x32_bf16 v[56:59], v[224:227], v[96:99], v[56:59]\n\tv_mfma_f32_16x16x32_bf16 v[60:63], v[32:35], v[96:99], v[60:63]\n\tbuffer_load_dwordx4 v[96:99], %[vo0], s[44:47], %[pg1] offen sc1\n\t" \
          "s_waitcnt vmcnt(15) lgkmcnt(2)\n\tv_mfma_f32_16x16x32_bf16 v[48:51], a[200:203], v[100:103], v[48:51]\n\tds_read_b128 v[32:35], %[own] offset:4096\n\tv_mfma_f32_16x16x32_bf16 v[52:55], a[204:207], v[100:103], v[52:55]\n\tv_mfma_f32_16x16x32_bf16 v[56:59], v[228:231], v[100:103], v[56:59]\n\tv_mfma_f32_16x16x32_bf16 v[60:63], v[36:39], v[100:103], v[60:63]\n\t" \
          "buffer_load_dwordx4 v[100:103], %[vo0], s[44:47], %[pg1] offen offset:1024 sc1\n\ts_waitcnt vmcnt(15) lgkmcnt(2)\n\tv_mfma_f32_16x16x32_bf16 v[48:51], a[208:211], v[104:107], v[48:51]\n\tds_read_b128 v[36:39], %[own] offset:5120\n\tv_mfma_f32_16x16x32_bf16 v[52:55], a[212:215], v[104:107], v[52:55]\n\tv_mfma_f32_16x16x32_bf16 v[56:59], v[232:235], v[104:107], v[56:59]\n\t" \
          "v_mfma_f32_16x16x32_bf16 v[60:63], v[40:43], v[104:107], v[60:63]\n\tbuffer_load_dwordx4 v[104:107], %[vo0], s[44:47], %[pg1] offen offset:2048 sc1\n\ts_waitcnt vmcnt(15) lgkmcnt(2)\n\tv_mfma_f32_16x16x32_bf16 v[48:51], a[216:219], v[108:111], v[48:51]\n\tds_read_b128 v[40:43], %[own] offset:6144\n\tv_mfma_f32_16x16x32_bf16 v[52:55], a[220:223], v[108:111], v[52:55]\n\t" \
          "v_mfma_f32_16x16x32_bf16 v[56:59], v[236:239], v[108:111], v[56:59]\n\tv_mfma_f32_16x16x32_bf16 v[60:63], v[44:47], v[108:111], v[60:63]\n\tbuffer_load_dwordx4 v[108:111], %[vo0], s[44:47], %[pg1] offen offset:3072 sc1\n\ts_waitcnt vmcnt(15) lgkmcnt(2)\n\tv_mfma_f32_16x16x32_bf16 v[48:51], a[224:227], v[112:115], v[48:51]\n\tds_read_b128 v[44:47], %[own] offset:7168\n\t" \
          "v_mfma_f32_16x16x32_bf16 v[52:55], a[228:231], v[112:115], v[52:55]\n\tv_mfma_f32_16x16x32_bf16 v[56:59], v[240:243], v[112:115], v[56:59]\n\tv_mfma_f32_16x16x32_bf16 v[60:63], v[32:35], v[112:115], v[60:63]\n\tbuffer_load_dwordx4 v[112:115], %[vo1], s[44:47], %[pg1] offen sc1\n\ts_waitcnt vmcnt(15) lgkmcnt(2)\n\tv_mfma_f32_16x16x32_bf16 v[48:51], a[232:235], v[116:119], v[48:51]\n\t" \
          "v_mfma_f32_16x16x32_bf16 v[52:55], a[236:239], v[116:119], v[52:55]\n\tv_mfma_f32_16x16x32_bf16 v[56:59], v[244:247], v[116:119], v[56:59]\n\tv_mfma_f32_16x16x32_bf16 v[60:63], v[36:39], v[116:119], v[60:63]\n\tbuffer_load_dwordx4 v[116:119], %[vo1], s[44:47], %[pg1] offen offset:1024 sc1\n\ts_waitcnt vmcnt(15) lgkmcnt(1)\n\tv_mfma_f32_16x16x32_bf16 v[48:51], a[240:243], v[120:123], v[48:51]\n\t" \
          "v_mfma_f32_16x16x32_bf16 v[52:55], a[244:247], v[120:123], v[52:55]\n\tv_mfma_f32_16x16x32_bf16 v[56:59], v[248:251], v[120:123], v[56:59]\n\tv_mfma_f32_16x16x32_bf16 v[60:63], v[40:43], v[120:123], v[60:63]\n\tbuffer_load_dwordx4 v[120:123], %[vo1], s[44:47], %[pg1] offen offset:2048 sc1\n\ts_waitcnt vmcnt(15) lgkmcnt(0)\n\tv_mfma_f32_16x16x32_bf16 v[48:51], a[248:251], v[124:127], v[48:51]\n\t" \
          "v_mfma_f32_16x16x32_bf16 v[52:55], a[252:255], v[124:127], v[52:55]\n\tv_mfma_f32_16x16x32_bf16 v[56:59], v[252:255], v[124:127], v[56:59]\n\tv_mfma_f32_16x16x32_bf16 v[60:63], v[44:47], v[124:127], v[60:63]\n\tbuffer_load_dwordx4 v[124:127], %[vo1], s[44:47], %[pg1] offen offset:3072 sc1\n\t" OU_ST1 "s_nop 6\n\ts_cmp_eq_u32 %[sp], 1\n\t" \
          "s_cbranch_scc0 Lou_red%=\n\tv_cmp_u_f32 vcc, v48, v48\n\ts_mov_b32 %[sp], 2\n\ts_cbranch_vccz Lou_red%=\n\ts_sub_u32 %[j], %[j], %[stp]\n\ts_lshl_b32 %[t], %[stp], 14\n\t" \
          "s_sub_u32 %[pg0], %[pg0], %[t]\n\ts_sub_u32 %[pg1], %[pg1], %[t]\n\ts_sub_u32 %[pg2], %[pg2], %[t]\n\ts_sub_u32 %[pg3], %[pg3], %[t]\n\ts_branch Lou_wait%=\n\tLou_red%=:\n\t" \
          "ds_write_b128 %[rw_], v[48:51] offset:0\n\tds_write_b128 %[rw_], v[52:55] offset:1024\n\tds_write_b128 %[rw_], v[56:59] offset:2048\n\tds_write_b128 %[rw_], v[60:63] offset:3072\n\ts_waitcnt lgkmcnt(0)\n\ts_barrier\n\t" \
          "ds_read_b128 v[32:35], %[rr0] offset:0\n\tds_read_b128 v[36:39], %[rr0] offset:40960\n\tds_read_b128 v[40:43], %[rr1] offset:0\n\tds_read_b128 v[44:47], %[rr1] offset:40960\n\tv_xor_b32 %[rw_], 0x1000, %[rw_]\n\tv_xor_b32 %[rr0], 0x1000, %[rr0]\n\t" \
          "v_xor_b32 %[rr1], 0x1000, %[rr1]\n\ts_waitcnt lgkmcnt(0)\n\tv_pk_add_f32 v[32:33], v[32:33], v[36:37]\n\tv_pk_add_f32 v[34:35], v[34:35], v[38:39]\n\tv_pk_add_f32 v[40:41], v[40:41], v[44:45]\n\tv_pk_add_f32 v[42:43], v[42:43], v[46:47]\n\t" \
          "v_pk_add_f32 v[32:33], v[32:33], v[40:41]\n\tv_pk_add_f32 v[34:35], v[34:35], v[42:43]\n\ts_nop 0\n\tv_cvt_pk_bf16_f32 v44, v32, v33\n\tv_cvt_pk_bf16_f32 v45, v34, v35\n\tbuffer_store_dwordx2 v[44:45], %[so], %[rc], %[oo] offen" OU_AUX "\n\t" \
          "s_cmp_lg_u32 s46, 0\n\ts_cbranch_scc1 Lou_top%=\n\ts_cmp_lt_u32 %[j], 128\n\ts_cbranch_scc1 Lou_wait%=\n\ts_waitcnt vmcnt(0)\n\t"
#define OU_CLOB "v30", "v31", "v32", "v33", "v34", "v35", "v36", "v37", "v38", "v39", "v40", "v41", "v42", "v43", "v44", "v45", "v46", "v47", "v48", "v49", "v50", "v51", "v52", "v53", "v54", "v55", "v56", "v57", "v58", "v59", "v60", "v61", "v62", "v63", "v64", "v65", "v66", "v67", "v68", "v69", "v70", "v71", "v72", "v73", "v74", "v75", "v76", "v77", "v78", "v79", "v80", "v81", "v82", "v83", "v84", "v85", "v86", "v87", "v88", "v89", "v90", "v91", "v92", "v93", "v94", "v95", "v96", "v97", "v98", "v99", "v100", "v101", "v102", "v103", "v104", "v105", "v106", "v107", "v108", "v109", "v110", "v111", "v112", "v113", "v114", "v115", "v116", "v117", "v118", "v119", "v120", "v121", "v122", "v123", "v124", "v125", "v126", "v127", "v128", "v129", "v130", "v131", "v132", "v133", "v134", "v135", "v136", "v137", "v138", "v139", "v140", "v141", "v142", "v143", "v144", "v145", "v146", "v147", "v148", "v149", "v150", "v151", "v152", "v153", "v154", "v155", "v156", "v157", "v158", "v159", "v160", "v161", "v162", "v163", "v164", "v165", "v166", "v167", "v168", "v169", "v170", "v171", "v172", "v173", "v174", "v175", "v176", "v177", "v178", "v179", "v180", "v181", "v182", "v183", "v184", "v185", "v186", "v187", "v188", "v189", "v190", "v191", "v192", "v193", "v194", "v195", "v196", "v197", "v198", "v199", "v200", "v201", "v202", "v203", "v204", "v205", "v206", "v207", "v208", "v209", "v210", "v211", "v212", "v213", "v214", "v215", "v216", "v217", "v218", "v219", "v220", "v221", "v222", "v223", "v224", "v225", "v226", "v227", "v228", "v229", "v230", "v231", "v232", "v233", "v234", "v235", "v236", "v237", "v238", "v239", "v240", "v241", "v242", "v243", "v244", "v245", "v246", "v247", "v248", "v249", "v250", "v251", "v252", "v253", "v254", "v255", "a0", "a1", "a2", "a3", "a4", "a5", "a6", "a7", "a8", "a9", "a10", "a11", "a12", "a13", "a14", "a15", "a16", "a17", "a18", "a19", "a20", "a21", "a22", "a23", "a24", "a25", "a26", "a27", "a28", "a29", "a30", "a31", "a32", "a33", "a34", "a35", "a36", "a37", "a38", "a39", "a40", "a41", "a42", "a43", "a44", "a45", "a46", "a47", "a48", "a49", "a50", "a51", "a52", "a53", "a54", "a55", "a56", "a57", "a58", "a59", "a60", "a61", "a62", "a63", "a64", "a65", "a66", "a67", "a68", "a69", "a70", "a71", "a72", "a73", "a74", "a75", "a76", "a77", "a78", "a79", "a80", "a81", "a82", "a83", "a84", "a85", "a86", "a87", "a88", "a89", "a90", "a91", "a92", "a93", "a94", "a95", "a96", "a97", "a98", "a99", "a100", "a101", "a102", "a103", "a104", "a105", "a106", "a107", "a108", "a109", "a110", "a111", "a112", "a113", "a114", "a115", "a116", "a117", "a118", "a119", "a120", "a121", "a122", "a123", "a124", "a125", "a126", "a127", "a128", "a129", "a130", "a131", "a132", "a133", "a134", "a135", "a136", "a137", "a138", "a139", "a140", "a141", "a142", "a143", "a144", "a145", "a146", "a147", "a148", "a149", "a150", "a151", "a152", "a153", "a154", "a155", "a156", "a157", "a158", "a159", "a160", "a161", "a162", "a163", "a164", "a165", "a166", "a167", "a168", "a169", "a170", "a171", "a172", "a173", "a174", "a175", "a176", "a177", "a178", "a179", "a180", "a181", "a182", "a183", "a184", "a185", "a186", "a187", "a188", "a189", "a190", "a191", "a192", "a193", "a194", "a195", "a196", "a197", "a198", "a199", "a200", "a201", "a202", "a203", "a204", "a205", "a206", "a207", "a208", "a209", "a210", "a211", "a212", "a213", "a214", "a215", "a216", "a217", "a218", "a219", "a220", "a221", "a222", "a223", "a224", "a225", "a226", "a227", "a228", "a229", "a230", "a231", "a232", "a233", "a234", "a235", "a236", "a237", "a238", "a239", "a240", "a241", "a242", "a243", "a244", "a245", "a246", "a247", "a248", "a249", "a250", "a251", "a252", "a253", "a254", "a255", "s40", "s41", "s42", "s43", "s44", "s45", "s46", "s47"
__device__ __forceinline__ void out_unit(const FParams& F, unsigned long long parq, int set, int un, int w) {
  const int lane = opaque_lane(), l15 = lane & 15, lq = lane >> 4, r = un & 7;
  const __amdgpu_buffer_rsrc_t rw = __builtin_amdgcn_make_buffer_rsrc(wg::uni(F.Wo + 64 * un), 0, HDA * DM * 2 - 128 * un, 0x00020000);
  const __amdgpu_buffer_rsrc_t rc = __builtin_amdgcn_make_buffer_rsrc(wg::uni(F.out + 64 * un), 0, S_ * 2 * DM * 2 - 128 * un, 0x00020000);
  const __amdgpu_buffer_rsrc_t rf = __builtin_amdgcn_make_buffer_rsrc(F.ctl, 0, sizeof(Ctl), 0x00020000);
  const int own = w * 40960 + lane * 16;
  int j = (2 * OSEG0 + (set < 2 ? 0 : TP(F.oj1, OJ1))) + set, av = OSEG0, pg[4], t, cnt, oo, sp = 0, stp, rw_ = own + 32768, rr0 = 32768 + (l15 >> 2) * 1024 + ((l15 & 3) * 16 + 4 * w + lq) * 16, rr1 = rr0 + 81920;
#pragma unroll
  for (int i = 0; i < 4; ++i) pg[i] = __builtin_amdgcn_readfirstlane(qoff(parq, 4 * w + (i ^ (r >> 1))) + j * QBLK);
  const int rt = w * 40960 + (8 * lq + (l15 >> 2)) * 128 + (((l15 >> 3) | ((lq & 1) << 1)) << 5) + (l15 & 3) * 8, vw = (lane >> 3) * 2048 + (((lane & 7) ^ ((lq & 1) << 1)) << 4);
  asm volatile(OU_BODY
    : [j] "+s"(j), [av] "+s"(av), [pg0] "+s"(pg[0]), [pg1] "+s"(pg[1]), [pg2] "+s"(pg[2]), [pg3] "+s"(pg[3]), [t] "=&s"(t), [cnt] "=&s"(cnt), [oo] "=&s"(oo), [sp] "+s"(sp), [stp] "=&s"(stp), [rw_] "+v"(rw_), [rr0] "+v"(rr0), [rr1] "+v"(rr1) OU_STO
    : [vo0] "v"(lane * 16 + (r & 1) * 4096), [vo1] "v"(lane * 16 + (~r & 1) * 4096), [vw0] "v"(vw), [vw1] "v"(vw ^ 64), [rt0] "v"(rt), [rt1] "v"(rt ^ 32), [rt2] "v"(rt ^ 64), [rt3] "v"(rt ^ 96), [own] "v"(own),
      [vf] "v"(lane < 8 ? (lane >> 1) * 1024 + (2 * w + (lane & 1)) * 128 : 0x7ffffff0), [so] "v"((4 * w + lq) * 4096 + l15 * 8),
      [qa] "s"(F.qkvA), [qs] "s"(QSIZE), [rw] "s"(rw), [rc] "s"(rc), [rf] "s"(rf), [wl] "s"(w * 40960), [wo] "s"((1024 * w + 128 * r) * 2048), [fo] "s"((int)offsetof(Ctl, flagO)), [tmo] "s"((int)offsetof(Ctl, tmo)), [j1] "s"(2 * OSEG0 + TP(F.oj1, OJ1))
    : OU_CLOB, "vcc", "scc", "memory");
}

__device__ __forceinline__ void attnB_task(const FParams& F, unsigned long long parq, int r, int pg, u16* vs  , int w) {
  const int lane = opaque_lane(), l15 = lane & 15, lq = lane >> 4;
  const __amdgpu_buffer_rsrc_t rs = __builtin_amdgcn_make_buffer_rsrc(F.qkvA, 0, QSIZE, 0x00020000);
  const int so = 8 * r * QBLK + w * 2048, oq = so + qoff(parq, 48 + pg), ok = so + qoff(parq, 52 + pg), ov = so + qoff(parq, 56 + pg), p0 = r ? -QBLK : 0;
  v4u q[2][8], k[2][9], v[2][9];
#pragma unroll
  for (int hh = 0; hh < 2; ++hh) {
#pragma unroll
    for (int i = 0; i < 9; ++i) v[hh][i] = __builtin_amdgcn_raw_buffer_load_b128(rs, lane * 16 + hh * 1024, ov + (i ? (i - 1) * QBLK : p0), 0);
#pragma unroll
    for (int i = 0; i < 9; ++i) { k[hh][i] = __builtin_amdgcn_raw_buffer_load_b128(rs, lane * 16 + hh * 1024, ok + (i ? (i - 1) * QBLK : p0), 0); if (i < 8) q[hh][i] = __builtin_amdgcn_raw_buffer_load_b128(rs, lane * 16 + hh * 1024, oq + i * QBLK, 0); }
  }
  const float scale = 0.17677669529663687f, L2E = 1.4426950408889634f;
  const bool par = l15 & 1, ownA = l15 - 4 * lq - par >= 0, ownB = l15 - 4 * lq - par - 2 >= 0;
  const unsigned sa = par ? 0x01000c0cu : 0x0c0c0100u, sb = par ? 0x03020c0cu : 0x0c0c0302u, sz = 0x0c0c0c0cu;
  const unsigned sel[4] = {ownA ? sz : sa, ownB ? sz : sb, ownA ? sa : sz, ownB ? sb : sz};
  const u16* vp = &vs[(4 * lq + (l15 >> 2)) * 40 + 16 * (l15 & 1) + 2 * (l15 & 2)];
#pragma unroll
  for (int hh = 0; hh < 2; ++hh) {
    SB0;
#pragma unroll
    for (int i = 0; i < 9; ++i) *(v4u*)&vs[(16 * i + l15) * 40 + 8 * lq] = v[hh][i];
#pragma unroll
    for (int i = 0; i < 8; ++i) {
      const f32x4 z = {0.f, 0.f, 0.f, 0.f}, s0 = mf(k[hh][i], q[hh][i], z), s1 = mf(k[hh][i + 1], q[hh][i], z);
      float a = ownA ? (par ? s1[1] : s1[0]) : (par ? s0[1] : s0[0]), b = ownB ? (par ? s1[3] : s1[2]) : (par ? s0[3] : s0[2]);
      unsigned p = pk2(a, b);
      p = pk2(__uint_as_float(p << 16) * scale, __uint_as_float(p & 0xffff0000u) * scale);
      a = __uint_as_float(p << 16); b = __uint_as_float(p & 0xffff0000u);
      const bool va = i > 0 || r > 0 || ownA, vb = i > 0 || r > 0 || ownB;
      const float mx = xmax32(xmax16(fmaxf(va ? a : -INFINITY, vb ? b : -INFINITY)));
      a = va ? __builtin_amdgcn_exp2f((a - mx) * L2E) : 0.f; b = vb ? __builtin_amdgcn_exp2f((b - mx) * L2E) : 0.f;
      const float inv = __builtin_amdgcn_rcpf(xsum32(xsum16(a + b)));
      p = pk2(a * inv, b * inv);
      const v4u px = {__builtin_amdgcn_perm(0u, p, sel[0]), __builtin_amdgcn_perm(0u, p, sel[1]), __builtin_amdgcn_perm(0u, p, sel[2]), __builtin_amdgcn_perm(0u, p, sel[3])};
      f32x4 o[2];
#pragma unroll
      for (int ab = 0; ab < 2; ++ab) { const v2u lo = trrd(vp + 16 * i * 40 + 8 * ab), hi = trrd(vp + 16 * (i + 1) * 40 + 8 * ab); o[ab] = mf((v4u){lo.x, lo.y, hi.x, hi.y}, px, z); }
      __builtin_amdgcn_raw_buffer_store_b128((v4u){pk2(o[0][0], o[0][1]), pk2(o[0][2], o[0][3]), pk2(o[1][0], o[1][1]), pk2(o[1][2], o[1][3])}, rs, lane * 16 + hh * 1024, oq + i * QBLK, 16);
    }
  }
}

constexpr int NTASK = 240;
__constant__ unsigned short TASKS[NTASK] = {
  5120, 5121, 5122, 5123, 5124, 5125, 5126, 5127, 5128, 5129, 5130, 5131, 5132, 5133, 5134, 5135, 5136, 5137, 5138, 5139, 5140, 5141, 5142, 5143,
  3072, 3073, 3074, 3075, 3076, 3077, 3078, 3079, 3080, 3081, 3082, 3083, 3084, 3085, 3086, 3087, 3088, 3089, 3090, 3091, 3092, 3093, 3094, 3095,
  6144, 6145, 6146, 6147, 6148, 6149, 6150, 6151, 5144, 5145, 5146, 5147, 5148, 5149, 5150, 5151, 5152, 5153, 5154, 5155, 5156, 5157, 5158, 5159,
  6152, 6153, 6154, 6155, 4096, 4097, 4098, 4099, 4100, 4101, 4102, 4103, 4104, 4105, 4106, 4107, 5160, 5161, 5162, 5163, 5164, 5165, 5166, 5167,
  6156, 6157, 6158, 6159, 5168, 5169, 5170, 5171, 5172, 5173, 5174, 5175, 5176, 5177, 5178, 5179, 5180, 5181, 5182, 5183, 7168, 7169, 7170, 7171,
  7172, 7173, 7174, 7175, 7176, 7177, 7178, 7179, 7180, 7181, 7182, 7183, 7184, 7185, 7186, 7187, 7188, 7189, 7190, 7191, 7192, 7193, 7194, 7195,
  7196, 7197, 7198, 7199, 4108, 4109, 4110, 4111, 4112, 4113, 4114, 4115, 4116, 4117, 4118, 4119, 3096, 3097, 3098, 3099, 3100, 3101, 3102, 3103,
  3104, 3105, 3106, 3107, 3108, 3109, 3110, 3111, 4120, 4121, 4122, 4123, 4124, 4125, 4126, 4127, 4128, 4129, 4130, 4131, 4132, 4133, 4134, 4135,
  3112, 3113, 3114, 3115, 3116, 3117, 3118, 3119, 3120, 3121, 3122, 3123, 3124, 3125, 3126, 3127, 3128, 3129, 3130, 3131, 3132, 3133, 3134, 3135,
  4136, 4137, 4138, 4139, 4140, 4141, 4142, 4143, 4144, 4145, 4146, 4147, 4148, 4149, 4150, 4151, 4152, 4153, 4154, 4155, 4156, 4157, 4158, 4159,
};
__device__ __forceinline__ const FParams& args() {
  typedef const __attribute__((address_space(4))) FParams* KP;
  KP p = (KP)__builtin_amdgcn_kernarg_segment_ptr(); asm volatile("" : "+s"(p)); return *(const FParams*)p;
}
__device__ __forceinline__ void wait_until(unsigned t) { while ((int)(t - (unsigned)__builtin_amdgcn_s_memrealtime()) > 0) __builtin_amdgcn_s_sleep(8); }
template <int AUX = 0>
__device__ __forceinline__ void touch(const void* base, int bytes, int voff, int soff, char* lds) {
  __builtin_amdgcn_raw_ptr_buffer_load_lds(__builtin_amdgcn_make_buffer_rsrc((void*)base, 0, bytes, 0x00020000), (__attribute__((address_space(3))) void*)(lds + wg::LDS_USE + 1024), 4, voff, soff, 0, AUX);
}
constexpr int RECO = 32768;
__device__ __forceinline__ void setup(const FParams& F, int n, int w) {
  const int lane = opaque_lane(), tid = 64 * w + lane;
  const v4u ff = {0xffffffffu, 0xffffffffu, 0xffffffffu, 0xffffffffu}, ep = rdy_load(F);
  const __amdgpu_buffer_rsrc_t rs = __builtin_amdgcn_make_buffer_rsrc(F.ring, 0, QPAGE + QSIZE, 0x00020000);
  const int o = ((lane & 16) ? (n < 8 ? (1 + NPAGE + n) * QPAGE : RSIZE + n * 128) : (1 + n) * QPAGE) + ((lane >> 3) & 1) * 8192 + (lane & 7) * 16;
  __builtin_amdgcn_raw_buffer_store_b128(ff, rs, tid < 32 ? o : 0x7ffffff0, 0, 16);
#pragma unroll 2
  for (int i = n * 256 + tid; i < RSIZE / 16; i += NPAGE * 256) {
    v4u v = ff;
    const int m = i - ROFF_M / 16;
    if ((unsigned)m < 2048u) {
      const int q = (m >> 10) * 512 + (m & 511);
      const u16* src = F.mem0 + (((q >> 4) & 1) * 4 + ((q >> 1) & 3)) * DM + 32 * (q >> 5) + 8 * ((q >> 3) & 1) + 4 * (q & 1);
#pragma unroll
      for (int p = 0; p < 4; ++p) v[p] = nosent(src[p] | ((unsigned)src[p + 16] << 16));
    }
    __builtin_amdgcn_raw_buffer_store_b128(v, rs, i * 16, 0, 16);
  }
  __builtin_amdgcn_raw_buffer_store_b128((v4u){0u, 0u, 0u, 0u}, __builtin_amdgcn_make_buffer_rsrc(F.ctl, 0, sizeof(Ctl), 0x00020000), w >= 2 ? (n * 128 + tid - 128) * 16 : 0x7ffffff0, 0, 16);
  static_assert(sizeof(Ctl) <= NPAGE * 128 * 16, "");
  const unsigned long long tag = rdy_tag(ep);
  asm volatile("s_waitcnt vmcnt(0)" ::: "memory");
  __syncthreads();
  if (w == 0) {
    unsigned best[4] = {~0u, ~0u, ~0u, ~0u};
#pragma unroll
    for (int g = 0; g < 4; g += 2)
#pragma unroll 1
      for (int r = 0; r < TP(F.probes, PROBES); ++r) {
#pragma unroll
        for (int t = g; t < g + 2; ++t) {
          const unsigned long long t0 = __builtin_amdgcn_s_memtime();
          __builtin_amdgcn_raw_buffer_store_b128(ff, rs, (lane >> 3) == t ? o : 0x7ffffff0, 0, 16);
          asm volatile("s_waitcnt vmcnt(0)" ::: "memory");
          best[t] = min(best[t], (unsigned)(__builtin_amdgcn_s_memtime() - t0));
        }
        if (min(best[g], best[g + 1]) < TP(F.sh2, PTH) && max(best[g], best[g + 1]) > min(best[g], best[g + 1]) + 120) break;
      }
    unsigned xcc; asm volatile("s_getreg_b32 %0, hwreg(HW_REG_XCC_ID)" : "=s"(xcc));
    const bool flip = ((xcc ^ (xcc - blockIdx.x + 1)) >> 2) & 1;
    const unsigned bits = ((best[1] < best[0]) == flip ? 1 : 0) | (((best[3] < best[2]) != flip) != (n < 8) ? 4 : 0);
    __builtin_amdgcn_raw_buffer_store_b128((v4u){(unsigned)tag, (unsigned)(tag >> 32), bits, 0u}, __builtin_amdgcn_make_buffer_rsrc(F.rdy, 0, RDY_SIZE, 0x00020000), lane < 8 ? n * 128 + lane * 16 : 0x7ffffff0, 0, 16);
  }
}
__device__ __forceinline__ void worker(const FParams& F, unsigned cz, char* lds, const int w0) {
#define lane opaque_lane()
#define tid (64 * w + opaque_lane())
  const int grp = (blockIdx.x & 7) - 3, slot = blockIdx.x >> 3;
#define TLOG(t, e)
  unsigned long long par, tag; unsigned pk;
  { int w = w0; v4u rec;
    const unsigned t0 = (unsigned)__builtin_amdgcn_s_memrealtime();
    const int id = slot * 4 + w, tz = opaque_lane();
    if (grp != 2) {
      if (grp > 2) { if (slot < 30) setup(F, (grp - 3) * 30 + slot, w); TLOG(1024 + grp * 32 + slot, 0); wait_until(t0 + TP(F.stag, STAG)); }
      rec = panel_task<NTP>(F, (grp < 2 ? grp + 1 : 0) * 32 + slot, (grp - (grp > 2)) * 32 + slot, grp == 4 ? 64 : 0, grp == 3 ? 64 : 128, w);
    } else {
#pragma unroll
      for (int q = 0; q < 4; ++q) {
        const u16* base = q == 0 ? F.Wk : (q == 1 ? F.Wo : (q == 2 ? F.Wq : F.Wv));
        const int o3 = (384 - (int)(size_t)base) & 896, o63 = (8064 - (int)(size_t)base) & 8064;
        touch(base, DM * HDA * 2, (tz * 128 + id) * 1024 + o3, 0, lds);
        if (!(q & 1)) { touch(base, DM * HDA * 2, (tz * 128 + id) * 1024 + (o3 ^ 128), 0, lds); touch(base, DM * HDA * 2, tz < 16 ? ((tz >> 1) * 128 + id) * 8192 + (o63 ^ ((tz & 1) << 7)) : 0x7ffffff0, 0, lds); }
      }
#pragma unroll 1
      for (int q = id; q < 768; q += 128) {
        wait_until(t0 + (TP(F.px, PXV) >> 16) + ((q * (TP(F.px, PXV) & 0xffff)) >> 6));
        if (q < 512) touch(F.Ax, S_ * DM * 2, tz * 128, ((q & 1) * 256 + (q >> 1)) * 8192, lds); else touch(F.woB, DB * DB * 2, tz * 128, (q - 512) * 8192, lds);
      }
      if (slot < 28) wait_until(t0 + (TP(F.pb, PBV) >> 16));
      rec = rdy_load(F);
      asm volatile("s_waitcnt vmcnt(0)" ::: "memory");
      __syncthreads();
    }
    asm volatile("" :: "v"(cz));
    par = rdy_wait(F, rec, tag, pk);
    if (grp == 2 || grp == 3) {
      const FParams& F = args();
      asm volatile("" : "+s"(w));
      const int j0 = grp == 2 ? 0 : TP(F.jb, JB), j1 = grp == 2 ? TP(F.jb, JB) : 128;
      if (slot < 24) (void)panel_task<true>(F, 96 + slot, 64 + grp * 32 + slot, j0, j1, w);
      else if (slot < 28) {
        const int id = (slot - 24) * 4 + w, tz = opaque_lane();
        int seen = j0;
#pragma unroll 1
        for (int j = j0 + (id >> 2); j < j1; j += 4) {
          for (unsigned spins = 0; j > seen + 16; ++spins) {
            seen = max(seen, __builtin_amdgcn_readfirstlane(ld_agent(&F.ctl->prg[64 + grp * 32 + id][0])));
            if (spin_fail(spins, &F.ctl->tmo[0], 2u)) break;
            __builtin_amdgcn_s_sleep(32);
          }
          touch<2>(F.Bx, S_ * DB * 2, tz * 128, (j * 4 + (id & 3)) * 8192, lds);
          touch(F.Bx, S_ * DB * 2, tz * 128 + 64, (j * 4 + (id & 3)) * 8192, lds);
        }
        asm volatile("s_waitcnt vmcnt(0)" ::: "memory");
      }
    }
    check_in(F, tag, w);
    TLOG(1024 + grp * 32 + slot, 1);
    if (grp < 2) {
      const FParams& F = args();
      asm volatile("" : "+s"(w));
      out_unit(F, par, 2 * grp + (slot >> 4), slot & 15, w);
      TLOG(1280 + grp * 32 + slot, 1);
      return;
    } }
  Ctl* ctl = F.ctl;
  int* sh = (int*)(lds + wg::LDS_USE);
  int next = -1;
  for (;;) {
    const FParams& F = args();
    int w = w0; asm volatile("" : "+s"(w));
    if (next < 0) {
      if (tid == 0) sh[0] = (int)__hip_atomic_fetch_add(&ctl->qhead[0], 1u, RLX_AGENT);
      __syncthreads();
      const int t = __builtin_amdgcn_readfirstlane(sh[0]);
      __syncthreads();
      { if (t >= NTASK) return; next = TASKS[t]; }
    }
    const int code = __builtin_amdgcn_readfirstlane(next); next = -1;
    const int tk = code >> 10, u = code & 1023;
    const u16 *A = nullptr, *Bt = nullptr; void* C = nullptr; int lda = 0, ldb = 0, ldc = 0, nk = 32, r = u >> 2, n = u & 3, nsig = 1;
    const unsigned* wp = nullptr; unsigned need = 0; unsigned* sig = nullptr;
    if (tk == 3) {
      const int lz = opaque_lane(); const bool hi = 8 * r >= TP(F.jb, JB);
      need = lz < 8 ? 8 * r + 8 : TP(F.jb, JB); sig = &ctl->cntAB[r][0];
      if ((lz & 7) < 6 && lz < (hi ? 16 : 8)) wp = &ctl->prg[(lz < 8 && hi ? 160 : 128) + ((lz & 7) >> 1) * 8 + 2 * n + (lz & 1)][0];
    } else if (tk == 4) {
      A = F.qkvA + r * 4 * QBLK + 24 * QPAGE; lda = QBLK / 32; Bt = F.woB + n * 256; ldb = DB;
      C = F.out + (size_t)r * 128 * 2 * DM + DM + n * 256; ldc = 2 * DM; need = 4; if (lane == 0) wp = &ctl->cntAB[r][0];
    } else if (tk == 5) {
      r = u >> 3; n = u & 7; sig = &ctl->flagO[4 * r][n][0]; nsig = 4; need = 8 * r + 8;
      const int lz = opaque_lane(); if (lz < 12) wp = &ctl->prg[((lz >> 2) + (lz >= 8 && r >= 8)) * 32 + 4 * n + (lz & 3)][0];
    } else if (tk == 6) {
      A = F.qkvA + r * 4 * QBLK; lda = QBLK / 32; Bt = F.Wo + n * 256; ldb = DM; nk = HDA / 32;
      C = F.out + (size_t)r * 128 * 2 * DM + n * 256; ldc = 2 * DM; need = 1; { const int lz = opaque_lane(); if (lz < 8) wp = &ctl->flagO[4 * r][lz][0]; }
    } else {
      attn_stream(F, par, pk, u >> 3, u & 7, lds, w);
      TLOG(lastt, 1);
      return;
    }
    wg_wait(wp, need, ctl, w);
    if (tk == 5) {
      attnA_block(F, par, pk, r, 4 * n + w, (u16*)(lds + w * 16384), opaque_lane(), w, (unsigned*)sh + 4);
      asm volatile("s_waitcnt vmcnt(0)" ::: "memory");
      __syncthreads();
    } else if (tk == 3) {
      attnB_task(F, par, r, n, (u16*)(lds + w * 16384), w);
      asm volatile("s_waitcnt vmcnt(0)" ::: "memory");
      __syncthreads();
    } else wg::gemm_tile<128>(A, lda, Bt, ldb, nk, C, ldc, par >> (tk == 4 ? 48 : 0), lds, w);
    if (sig && tid < nsig) __hip_atomic_fetch_add(sig + tid * 256, 1u, RLX_AGENT);
  }
}

#undef lane
#undef tid
__device__ __forceinline__ unsigned rtnow() { return (unsigned)__builtin_amdgcn_s_memrealtime(); }
#define RBASE(s) (((s) & 3) * RSLOT + rpl)
constexpr int ALM = RECO + 1024;
__device__ __forceinline__ float rowmax16(float f) { f = fmaxf(f, dpp_ror<8>(f)); f = fmaxf(f, dpp_ror<4>(f)); f = fmaxf(f, dpp_ror<2>(f)); return fmaxf(f, dpp_ror<1>(f)); }
__device__ __forceinline__ void al_eval(unsigned v, unsigned& t) {
  const bool set = (v >> 16) == 1u;
  if (__builtin_expect(((unsigned)__ballot(set) & 0xffffu) == 0, 1)) return;
  const int d = (short)(v - t);
  t += __builtin_amdgcn_readfirstlane((int)rowmax16(set && d > 0 && d < 1000 ? (float)d : 0.f));
}
__device__ __forceinline__ void rec_get(unsigned& recA, unsigned& recD, __amdgpu_buffer_rsrc_t rs, int off, unsigned* tmo) {
#pragma unroll 1
  for (unsigned fails = 0;; ++fails) {
    const v2u rec = __builtin_amdgcn_raw_buffer_load_b64(rs, off, 0, 16);
    recA = __builtin_amdgcn_readfirstlane(rec.x); recD = __builtin_amdgcn_readfirstlane(rec.y);
    if ((recA != 0xffffffffu && recD != 0xffffffffu) || spin_fail(fails, tmo, 1u)) break;
    __builtin_amdgcn_s_sleep(1);
  }
}

#define QK_LOADER \
          "s_nop 4\n\t" \
          "s_add_u32 m0, %[lw], 0\n\ts_xor_b32 %[t], %[kb], 0x0\n\tbuffer_load_dwordx4 %[go], %[rk], %[t] offen" QK_NTk " lds\n\ts_add_u32 m0, %[lw], 1024\n\ts_xor_b32 %[t], %[kb], 0x10000\n\tbuffer_load_dwordx4 %[go], %[rk], %[t] offen" QK_NTk " lds\n\ts_add_u32 m0, %[lw], 2048\n\ts_xor_b32 %[t], %[kb], 0x20000\n\tbuffer_load_dwordx4 %[go], %[rk], %[t] offen" QK_NTk " lds\n\ts_add_u32 m0, %[lw], 3072\n\ts_xor_b32 %[t], %[kb], 0x30000\n\tbuffer_load_dwordx4 %[go], %[rk], %[t] offen" QK_NTk " lds\n\ts_add_u32 m0, %[lw], 4096\n\ts_xor_b32 %[t], %[kb], 0x40000\n\tbuffer_load_dwordx4 %[go], %[rk], %[t] offen" QK_NTk " lds\n\ts_add_u32 m0, %[lw], 5120\n\ts_xor_b32 %[t], %[kb], 0x50000\n\tbuffer_load_dwordx4 %[go], %[rk], %[t] offen" QK_NTk " lds\n\ts_add_u32 m0, %[lw], 6144\n\ts_xor_b32 %[t], %[kb], 0x60000\n\tbuffer_load_dwordx4 %[go], %[rk], %[t] offen" QK_NTk " lds\n\ts_add_u32 m0, %[lw], 7168\n\ts_xor_b32 %[t], %[kb], 0x70000\n\tbuffer_load_dwordx4 %[go], %[rk], %[t] offen" QK_NTk " lds\n\t" \
          "s_add_u32 m0, %[lw], 8192\n\ts_xor_b32 %[t], %[kb], 0x80000\n\tbuffer_load_dwordx4 %[go], %[rk], %[t] offen" QK_NTk " lds\n\ts_add_u32 m0, %[lw], 9216\n\ts_xor_b32 %[t], %[kb], 0x90000\n\tbuffer_load_dwordx4 %[go], %[rk], %[t] offen" QK_NTk " lds\n\ts_add_u32 m0, %[lw], 10240\n\ts_xor_b32 %[t], %[kb], 0xa0000\n\tbuffer_load_dwordx4 %[go], %[rk], %[t] offen" QK_NTk " lds\n\ts_add_u32 m0, %[lw], 11264\n\ts_xor_b32 %[t], %[kb], 0xb0000\n\tbuffer_load_dwordx4 %[go], %[rk], %[t] offen" QK_NTk " lds\n\ts_add_u32 m0, %[lw], 12288\n\ts_xor_b32 %[t], %[kb], 0xc0000\n\tbuffer_load_dwordx4 %[go], %[rk], %[t] offen" QK_NTk " lds\n\ts_add_u32 m0, %[lw], 13312\n\ts_xor_b32 %[t], %[kb], 0xd0000\n\tbuffer_load_dwordx4 %[go], %[rk], %[t] offen" QK_NTk " lds\n\ts_add_u32 m0, %[lw], 14336\n\ts_xor_b32 %[t], %[kb], 0xe0000\n\tbuffer_load_dwordx4 %[go], %[rk], %[t] offen" QK_NTk " lds\n\ts_add_u32 m0, %[lw], 15360\n\ts_xor_b32 %[t], %[kb], 0xf0000\n\tbuffer_load_dwordx4 %[go], %[rk], %[t] offen" QK_NTk " lds\n\t" \
          "s_add_u32 m0, %[lw], 16384\n\ts_xor_b32 %[t], %[kb], 0x100000\n\tbuffer_load_dwordx4 %[go], %[rk], %[t] offen" QK_NTk " lds\n\ts_add_u32 m0, %[lw], 17408\n\ts_xor_b32 %[t], %[kb], 0x110000\n\tbuffer_load_dwordx4 %[go], %[rk], %[t] offen" QK_NTk " lds\n\ts_add_u32 m0, %[lw], 18432\n\ts_xor_b32 %[t], %[kb], 0x120000\n\tbuffer_load_dwordx4 %[go], %[rk], %[t] offen" QK_NTk " lds\n\ts_add_u32 m0, %[lw], 19456\n\ts_xor_b32 %[t], %[kb], 0x130000\n\tbuffer_load_dwordx4 %[go], %[rk], %[t] offen" QK_NTk " lds\n\ts_add_u32 m0, %[lw], 20480\n\ts_xor_b32 %[t], %[kb], 0x140000\n\tbuffer_load_dwordx4 %[go], %[rk], %[t] offen" QK_NTk " lds\n\ts_add_u32 m0, %[lw], 21504\n\ts_xor_b32 %[t], %[kb], 0x150000\n\tbuffer_load_dwordx4 %[go], %[rk], %[t] offen" QK_NTk " lds\n\ts_add_u32 m0, %[lw], 22528\n\ts_xor_b32 %[t], %[kb], 0x160000\n\tbuffer_load_dwordx4 %[go], %[rk], %[t] offen" QK_NTk " lds\n\ts_add_u32 m0, %[lw], 23552\n\ts_xor_b32 %[t], %[kb], 0x170000\n\tbuffer_load_dwordx4 %[go], %[rk], %[t] offen" QK_NTk " lds\n\t" \
          "s_add_u32 m0, %[lw], 24576\n\ts_xor_b32 %[t], %[kb], 0x180000\n\tbuffer_load_dwordx4 %[go], %[rk], %[t] offen" QK_NTk " lds\n\ts_add_u32 m0, %[lw], 25600\n\ts_xor_b32 %[t], %[kb], 0x190000\n\tbuffer_load_dwordx4 %[go], %[rk], %[t] offen" QK_NTk " lds\n\ts_add_u32 m0, %[lw], 26624\n\ts_xor_b32 %[t], %[kb], 0x1a0000\n\tbuffer_load_dwordx4 %[go], %[rk], %[t] offen" QK_NTk " lds\n\ts_add_u32 m0, %[lw], 27648\n\ts_xor_b32 %[t], %[kb], 0x1b0000\n\tbuffer_load_dwordx4 %[go], %[rk], %[t] offen" QK_NTk " lds\n\ts_add_u32 m0, %[lw], 28672\n\ts_xor_b32 %[t], %[kb], 0x1c0000\n\tbuffer_load_dwordx4 %[go], %[rk], %[t] offen" QK_NTk " lds\n\ts_add_u32 m0, %[lw], 29696\n\ts_xor_b32 %[t], %[kb], 0x1d0000\n\tbuffer_load_dwordx4 %[go], %[rk], %[t] offen" QK_NTk " lds\n\ts_add_u32 m0, %[lw], 30720\n\ts_xor_b32 %[t], %[kb], 0x1e0000\n\tbuffer_load_dwordx4 %[go], %[rk], %[t] offen" QK_NTk " lds\n\ts_add_u32 m0, %[lw], 31744\n\ts_xor_b32 %[t], %[kb], 0x1f0000\n\tbuffer_load_dwordx4 %[go], %[rk], %[t] offen" QK_NTk " lds\n\t" \
          "s_add_u32 m0, %[lw], 32768\n\ts_xor_b32 %[t], %[kb], 0x200000\n\tbuffer_load_dwordx4 %[go], %[rk], %[t] offen" QK_NTk " lds\n\ts_add_u32 m0, %[lw], 33792\n\ts_xor_b32 %[t], %[kb], 0x210000\n\tbuffer_load_dwordx4 %[go], %[rk], %[t] offen" QK_NTk " lds\n\ts_add_u32 m0, %[lw], 34816\n\ts_xor_b32 %[t], %[kb], 0x220000\n\tbuffer_load_dwordx4 %[go], %[rk], %[t] offen" QK_NTk " lds\n\ts_add_u32 m0, %[lw], 35840\n\ts_xor_b32 %[t], %[kb], 0x230000\n\tbuffer_load_dwordx4 %[go], %[rk], %[t] offen" QK_NTk " lds\n\ts_add_u32 m0, %[lw], 36864\n\ts_xor_b32 %[t], %[kb], 0x240000\n\tbuffer_load_dwordx4 %[go], %[rk], %[t] offen" QK_NTk " lds\n\ts_add_u32 m0, %[lw], 37888\n\ts_xor_b32 %[t], %[kb], 0x250000\n\tbuffer_load_dwordx4 %[go], %[rk], %[t] offen" QK_NTk " lds\n\ts_add_u32 m0, %[lw], 38912\n\ts_xor_b32 %[t], %[kb], 0x260000\n\tbuffer_load_dwordx4 %[go], %[rk], %[t] offen" QK_NTk " lds\n\ts_add_u32 m0, %[lw], 39936\n\ts_xor_b32 %[t], %[kb], 0x270000\n\tbuffer_load_dwordx4 %[go], %[rk], %[t] offen" QK_NTk " lds\n\t" \
          "s_waitcnt vmcnt(32)\n\tds_read_b64_tr_b16 v[136:137], %[rd0] offset:0\n\tds_read_b64_tr_b16 v[138:139], %[rd0] offset:128\n\tds_read_b64_tr_b16 v[140:141], %[rd0] offset:4096\n\tds_read_b64_tr_b16 v[142:143], %[rd0] offset:4224\n\tds_read_b64_tr_b16 a[64:65], %[rd1] offset:0\n\tds_read_b64_tr_b16 a[66:67], %[rd1] offset:128\n\tds_read_b64_tr_b16 a[68:69], %[rd1] offset:4096\n\tds_read_b64_tr_b16 a[70:71], %[rd1] offset:4224\n\tds_read_b64_tr_b16 a[128:129], %[rd2] offset:0\n\tds_read_b64_tr_b16 a[130:131], %[rd2] offset:128\n\tds_read_b64_tr_b16 a[132:133], %[rd2] offset:4096\n\tds_read_b64_tr_b16 a[134:135], %[rd2] offset:4224\n\tds_read_b64_tr_b16 a[136:137], %[rd3] offset:0\n\tds_read_b64_tr_b16 a[138:139], %[rd3] offset:128\n\tds_read_b64_tr_b16 a[140:141], %[rd3] offset:4096\n\tds_read_b64_tr_b16 a[142:143], %[rd3] offset:4224\n\ts_waitcnt lgkmcnt(0)\n\t" \
          "s_add_u32 m0, %[lw], 0\n\ts_xor_b32 %[t], %[kb], 0x280000\n\tbuffer_load_dwordx4 %[go], %[rk], %[t] offen" QK_NTk " lds\n\ts_add_u32 m0, %[lw], 1024\n\ts_xor_b32 %[t], %[kb], 0x290000\n\tbuffer_load_dwordx4 %[go], %[rk], %[t] offen" QK_NTk " lds\n\ts_add_u32 m0, %[lw], 2048\n\ts_xor_b32 %[t], %[kb], 0x2a0000\n\tbuffer_load_dwordx4 %[go], %[rk], %[t] offen" QK_NTk " lds\n\ts_add_u32 m0, %[lw], 3072\n\ts_xor_b32 %[t], %[kb], 0x2b0000\n\tbuffer_load_dwordx4 %[go], %[rk], %[t] offen" QK_NTk " lds\n\ts_add_u32 m0, %[lw], 4096\n\ts_xor_b32 %[t], %[kb], 0x2c0000\n\tbuffer_load_dwordx4 %[go], %[rk], %[t] offen" QK_NTk " lds\n\ts_add_u32 m0, %[lw], 5120\n\ts_xor_b32 %[t], %[kb], 0x2d0000\n\tbuffer_load_dwordx4 %[go], %[rk], %[t] offen" QK_NTk " lds\n\ts_add_u32 m0, %[lw], 6144\n\ts_xor_b32 %[t], %[kb], 0x2e0000\n\tbuffer_load_dwordx4 %[go], %[rk], %[t] offen" QK_NTk " lds\n\ts_add_u32 m0, %[lw], 7168\n\ts_xor_b32 %[t], %[kb], 0x2f0000\n\tbuffer_load_dwordx4 %[go], %[rk], %[t] offen" QK_NTk " lds\n\t" \
          "s_waitcnt vmcnt(32)\n\tds_read_b64_tr_b16 v[40:41], %[rd0] offset:8192\n\tds_read_b64_tr_b16 v[42:43], %[rd0] offset:8320\n\tds_read_b64_tr_b16 v[44:45], %[rd0] offset:12288\n\tds_read_b64_tr_b16 v[46:47], %[rd0] offset:12416\n\tds_read_b64_tr_b16 a[72:73], %[rd1] offset:8192\n\tds_read_b64_tr_b16 a[74:75], %[rd1] offset:8320\n\tds_read_b64_tr_b16 a[76:77], %[rd1] offset:12288\n\tds_read_b64_tr_b16 a[78:79], %[rd1] offset:12416\n\tds_read_b64_tr_b16 a[144:145], %[rd2] offset:8192\n\tds_read_b64_tr_b16 a[146:147], %[rd2] offset:8320\n\tds_read_b64_tr_b16 a[148:149], %[rd2] offset:12288\n\tds_read_b64_tr_b16 a[150:151], %[rd2] offset:12416\n\tds_read_b64_tr_b16 a[152:153], %[rd3] offset:8192\n\tds_read_b64_tr_b16 a[154:155], %[rd3] offset:8320\n\tds_read_b64_tr_b16 a[156:157], %[rd3] offset:12288\n\tds_read_b64_tr_b16 a[158:159], %[rd3] offset:12416\n\ts_waitcnt lgkmcnt(0)\n\t" \
          "s_add_u32 m0, %[lw], 8192\n\ts_xor_b32 %[t], %[kb], 0x300000\n\tbuffer_load_dwordx4 %[go], %[rk], %[t] offen" QK_NTk " lds\n\ts_add_u32 m0, %[lw], 9216\n\ts_xor_b32 %[t], %[kb], 0x310000\n\tbuffer_load_dwordx4 %[go], %[rk], %[t] offen" QK_NTk " lds\n\ts_add_u32 m0, %[lw], 10240\n\ts_xor_b32 %[t], %[kb], 0x320000\n\tbuffer_load_dwordx4 %[go], %[rk], %[t] offen" QK_NTk " lds\n\ts_add_u32 m0, %[lw], 11264\n\ts_xor_b32 %[t], %[kb], 0x330000\n\tbuffer_load_dwordx4 %[go], %[rk], %[t] offen" QK_NTk " lds\n\ts_add_u32 m0, %[lw], 12288\n\ts_xor_b32 %[t], %[kb], 0x340000\n\tbuffer_load_dwordx4 %[go], %[rk], %[t] offen" QK_NTk " lds\n\ts_add_u32 m0, %[lw], 13312\n\ts_xor_b32 %[t], %[kb], 0x350000\n\tbuffer_load_dwordx4 %[go], %[rk], %[t] offen" QK_NTk " lds\n\ts_add_u32 m0, %[lw], 14336\n\ts_xor_b32 %[t], %[kb], 0x360000\n\tbuffer_load_dwordx4 %[go], %[rk], %[t] offen" QK_NTk " lds\n\ts_add_u32 m0, %[lw], 15360\n\ts_xor_b32 %[t], %[kb], 0x370000\n\tbuffer_load_dwordx4 %[go], %[rk], %[t] offen" QK_NTk " lds\n\t" \
          "s_waitcnt vmcnt(32)\n\tds_read_b64_tr_b16 v[144:145], %[rd0] offset:16384\n\tds_read_b64_tr_b16 v[146:147], %[rd0] offset:16512\n\tds_read_b64_tr_b16 v[148:149], %[rd0] offset:20480\n\tds_read_b64_tr_b16 v[150:151], %[rd0] offset:20608\n\tds_read_b64_tr_b16 a[80:81], %[rd1] offset:16384\n\tds_read_b64_tr_b16 a[82:83], %[rd1] offset:16512\n\tds_read_b64_tr_b16 a[84:85], %[rd1] offset:20480\n\tds_read_b64_tr_b16 a[86:87], %[rd1] offset:20608\n\tds_read_b64_tr_b16 a[160:161], %[rd2] offset:16384\n\tds_read_b64_tr_b16 a[162:163], %[rd2] offset:16512\n\tds_read_b64_tr_b16 a[164:165], %[rd2] offset:20480\n\tds_read_b64_tr_b16 a[166:167], %[rd2] offset:20608\n\tds_read_b64_tr_b16 a[168:169], %[rd3] offset:16384\n\tds_read_b64_tr_b16 a[170:171], %[rd3] offset:16512\n\tds_read_b64_tr_b16 a[172:173], %[rd3] offset:20480\n\tds_read_b64_tr_b16 a[174:175], %[rd3] offset:20608\n\ts_waitcnt lgkmcnt(0)\n\t" \
          "s_add_u32 m0, %[lw], 16384\n\ts_xor_b32 %[t], %[kb], 0x380000\n\tbuffer_load_dwordx4 %[go], %[rk], %[t] offen" QK_NTk " lds\n\ts_add_u32 m0, %[lw], 17408\n\ts_xor_b32 %[t], %[kb], 0x390000\n\tbuffer_load_dwordx4 %[go], %[rk], %[t] offen" QK_NTk " lds\n\ts_add_u32 m0, %[lw], 18432\n\ts_xor_b32 %[t], %[kb], 0x3a0000\n\tbuffer_load_dwordx4 %[go], %[rk], %[t] offen" QK_NTk " lds\n\ts_add_u32 m0, %[lw], 19456\n\ts_xor_b32 %[t], %[kb], 0x3b0000\n\tbuffer_load_dwordx4 %[go], %[rk], %[t] offen" QK_NTk " lds\n\ts_add_u32 m0, %[lw], 20480\n\ts_xor_b32 %[t], %[kb], 0x3c0000\n\tbuffer_load_dwordx4 %[go], %[rk], %[t] offen" QK_NTk " lds\n\ts_add_u32 m0, %[lw], 21504\n\ts_xor_b32 %[t], %[kb], 0x3d0000\n\tbuffer_load_dwordx4 %[go], %[rk], %[t] offen" QK_NTk " lds\n\ts_add_u32 m0, %[lw], 22528\n\ts_xor_b32 %[t], %[kb], 0x3e0000\n\tbuffer_load_dwordx4 %[go], %[rk], %[t] offen" QK_NTk " lds\n\ts_add_u32 m0, %[lw], 23552\n\ts_xor_b32 %[t], %[kb], 0x3f0000\n\tbuffer_load_dwordx4 %[go], %[rk], %[t] offen" QK_NTk " lds\n\t" \
          "s_waitcnt vmcnt(32)\n\tds_read_b64_tr_b16 v[80:81], %[rd0] offset:24576\n\tds_read_b64_tr_b16 v[82:83], %[rd0] offset:24704\n\tds_read_b64_tr_b16 v[84:85], %[rd0] offset:28672\n\tds_read_b64_tr_b16 v[86:87], %[rd0] offset:28800\n\tds_read_b64_tr_b16 a[88:89], %[rd1] offset:24576\n\tds_read_b64_tr_b16 a[90:91], %[rd1] offset:24704\n\tds_read_b64_tr_b16 a[92:93], %[rd1] offset:28672\n\tds_read_b64_tr_b16 a[94:95], %[rd1] offset:28800\n\tds_read_b64_tr_b16 a[176:177], %[rd2] offset:24576\n\tds_read_b64_tr_b16 a[178:179], %[rd2] offset:24704\n\tds_read_b64_tr_b16 a[180:181], %[rd2] offset:28672\n\tds_read_b64_tr_b16 a[182:183], %[rd2] offset:28800\n\tds_read_b64_tr_b16 a[184:185], %[rd3] offset:24576\n\tds_read_b64_tr_b16 a[186:187], %[rd3] offset:24704\n\tds_read_b64_tr_b16 a[188:189], %[rd3] offset:28672\n\tds_read_b64_tr_b16 a[190:191], %[rd3] offset:28800\n\ts_waitcnt lgkmcnt(0)\n\t" \
          "s_add_u32 m0, %[lw], 24576\n\ts_xor_b32 %[t], %[kb], 0x380000\n\tbuffer_load_dwordx4 %[go], %[rq], %[t] offen" QK_NTq " lds\n\ts_add_u32 m0, %[lw], 25600\n\ts_xor_b32 %[t], %[kb], 0x390000\n\tbuffer_load_dwordx4 %[go], %[rq], %[t] offen" QK_NTq " lds\n\ts_add_u32 m0, %[lw], 26624\n\ts_xor_b32 %[t], %[kb], 0x3a0000\n\tbuffer_load_dwordx4 %[go], %[rq], %[t] offen" QK_NTq " lds\n\ts_add_u32 m0, %[lw], 27648\n\ts_xor_b32 %[t], %[kb], 0x3b0000\n\tbuffer_load_dwordx4 %[go], %[rq], %[t] offen" QK_NTq " lds\n\ts_add_u32 m0, %[lw], 28672\n\ts_xor_b32 %[t], %[kb], 0x3c0000\n\tbuffer_load_dwordx4 %[go], %[rq], %[t] offen" QK_NTq " lds\n\ts_add_u32 m0, %[lw], 29696\n\ts_xor_b32 %[t], %[kb], 0x3d0000\n\tbuffer_load_dwordx4 %[go], %[rq], %[t] offen" QK_NTq " lds\n\ts_add_u32 m0, %[lw], 30720\n\ts_xor_b32 %[t], %[kb], 0x3e0000\n\tbuffer_load_dwordx4 %[go], %[rq], %[t] offen" QK_NTq " lds\n\ts_add_u32 m0, %[lw], 31744\n\ts_xor_b32 %[t], %[kb], 0x3f0000\n\tbuffer_load_dwordx4 %[go], %[rq], %[t] offen" QK_NTq " lds\n\t" \
          "s_waitcnt vmcnt(32)\n\tds_read_b64_tr_b16 v[152:153], %[rd0] offset:32768\n\tds_read_b64_tr_b16 v[154:155], %[rd0] offset:32896\n\tds_read_b64_tr_b16 v[156:157], %[rd0] offset:36864\n\tds_read_b64_tr_b16 v[158:159], %[rd0] offset:36992\n\tds_read_b64_tr_b16 a[96:97], %[rd1] offset:32768\n\tds_read_b64_tr_b16 a[98:99], %[rd1] offset:32896\n\tds_read_b64_tr_b16 a[100:101], %[rd1] offset:36864\n\tds_read_b64_tr_b16 a[102:103], %[rd1] offset:36992\n\tds_read_b64_tr_b16 a[192:193], %[rd2] offset:32768\n\tds_read_b64_tr_b16 a[194:195], %[rd2] offset:32896\n\tds_read_b64_tr_b16 a[196:197], %[rd2] offset:36864\n\tds_read_b64_tr_b16 a[198:199], %[rd2] offset:36992\n\tds_read_b64_tr_b16 a[200:201], %[rd3] offset:32768\n\tds_read_b64_tr_b16 a[202:203], %[rd3] offset:32896\n\tds_read_b64_tr_b16 a[204:205], %[rd3] offset:36864\n\tds_read_b64_tr_b16 a[206:207], %[rd3] offset:36992\n\ts_waitcnt lgkmcnt(0)\n\t" \
          "s_add_u32 m0, %[lw], 32768\n\ts_xor_b32 %[t], %[kb], 0x300000\n\tbuffer_load_dwordx4 %[go], %[rq], %[t] offen" QK_NTq " lds\n\ts_add_u32 m0, %[lw], 33792\n\ts_xor_b32 %[t], %[kb], 0x310000\n\tbuffer_load_dwordx4 %[go], %[rq], %[t] offen" QK_NTq " lds\n\ts_add_u32 m0, %[lw], 34816\n\ts_xor_b32 %[t], %[kb], 0x320000\n\tbuffer_load_dwordx4 %[go], %[rq], %[t] offen" QK_NTq " lds\n\ts_add_u32 m0, %[lw], 35840\n\ts_xor_b32 %[t], %[kb], 0x330000\n\tbuffer_load_dwordx4 %[go], %[rq], %[t] offen" QK_NTq " lds\n\ts_add_u32 m0, %[lw], 36864\n\ts_xor_b32 %[t], %[kb], 0x340000\n\tbuffer_load_dwordx4 %[go], %[rq], %[t] offen" QK_NTq " lds\n\ts_add_u32 m0, %[lw], 37888\n\ts_xor_b32 %[t], %[kb], 0x350000\n\tbuffer_load_dwordx4 %[go], %[rq], %[t] offen" QK_NTq " lds\n\ts_add_u32 m0, %[lw], 38912\n\ts_xor_b32 %[t], %[kb], 0x360000\n\tbuffer_load_dwordx4 %[go], %[rq], %[t] offen" QK_NTq " lds\n\ts_add_u32 m0, %[lw], 39936\n\ts_xor_b32 %[t], %[kb], 0x370000\n\tbuffer_load_dwordx4 %[go], %[rq], %[t] offen" QK_NTq " lds\n\t" \
          "s_waitcnt vmcnt(32)\n\tds_read_b64_tr_b16 v[120:121], %[rd0] offset:0\n\tds_read_b64_tr_b16 v[122:123], %[rd0] offset:128\n\tds_read_b64_tr_b16 v[124:125], %[rd0] offset:4096\n\tds_read_b64_tr_b16 v[126:127], %[rd0] offset:4224\n\tds_read_b64_tr_b16 a[104:105], %[rd1] offset:0\n\tds_read_b64_tr_b16 a[106:107], %[rd1] offset:128\n\tds_read_b64_tr_b16 a[108:109], %[rd1] offset:4096\n\tds_read_b64_tr_b16 a[110:111], %[rd1] offset:4224\n\tds_read_b64_tr_b16 a[208:209], %[rd2] offset:0\n\tds_read_b64_tr_b16 a[210:211], %[rd2] offset:128\n\tds_read_b64_tr_b16 a[212:213], %[rd2] offset:4096\n\tds_read_b64_tr_b16 a[214:215], %[rd2] offset:4224\n\tds_read_b64_tr_b16 a[216:217], %[rd3] offset:0\n\tds_read_b64_tr_b16 a[218:219], %[rd3] offset:128\n\tds_read_b64_tr_b16 a[220:221], %[rd3] offset:4096\n\tds_read_b64_tr_b16 a[222:223], %[rd3] offset:4224\n\ts_waitcnt lgkmcnt(0)\n\t" \
          "s_add_u32 m0, %[lw], 0\n\ts_xor_b32 %[t], %[kb], 0x280000\n\tbuffer_load_dwordx4 %[go], %[rq], %[t] offen" QK_NTq " lds\n\ts_add_u32 m0, %[lw], 1024\n\ts_xor_b32 %[t], %[kb], 0x290000\n\tbuffer_load_dwordx4 %[go], %[rq], %[t] offen" QK_NTq " lds\n\ts_add_u32 m0, %[lw], 2048\n\ts_xor_b32 %[t], %[kb], 0x2a0000\n\tbuffer_load_dwordx4 %[go], %[rq], %[t] offen" QK_NTq " lds\n\ts_add_u32 m0, %[lw], 3072\n\ts_xor_b32 %[t], %[kb], 0x2b0000\n\tbuffer_load_dwordx4 %[go], %[rq], %[t] offen" QK_NTq " lds\n\ts_add_u32 m0, %[lw], 4096\n\ts_xor_b32 %[t], %[kb], 0x2c0000\n\tbuffer_load_dwordx4 %[go], %[rq], %[t] offen" QK_NTq " lds\n\ts_add_u32 m0, %[lw], 5120\n\ts_xor_b32 %[t], %[kb], 0x2d0000\n\tbuffer_load_dwordx4 %[go], %[rq], %[t] offen" QK_NTq " lds\n\ts_add_u32 m0, %[lw], 6144\n\ts_xor_b32 %[t], %[kb], 0x2e0000\n\tbuffer_load_dwordx4 %[go], %[rq], %[t] offen" QK_NTq " lds\n\ts_add_u32 m0, %[lw], 7168\n\ts_xor_b32 %[t], %[kb], 0x2f0000\n\tbuffer_load_dwordx4 %[go], %[rq], %[t] offen" QK_NTq " lds\n\t" \
          "s_waitcnt vmcnt(32)\n\tds_read_b64_tr_b16 a[16:17], %[rd0] offset:8192\n\tds_read_b64_tr_b16 a[18:19], %[rd0] offset:8320\n\tds_read_b64_tr_b16 a[20:21], %[rd0] offset:12288\n\tds_read_b64_tr_b16 a[22:23], %[rd0] offset:12416\n\tds_read_b64_tr_b16 a[112:113], %[rd1] offset:8192\n\tds_read_b64_tr_b16 a[114:115], %[rd1] offset:8320\n\tds_read_b64_tr_b16 a[116:117], %[rd1] offset:12288\n\tds_read_b64_tr_b16 a[118:119], %[rd1] offset:12416\n\tds_read_b64_tr_b16 a[224:225], %[rd2] offset:8192\n\tds_read_b64_tr_b16 a[226:227], %[rd2] offset:8320\n\tds_read_b64_tr_b16 a[228:229], %[rd2] offset:12288\n\tds_read_b64_tr_b16 a[230:231], %[rd2] offset:12416\n\tds_read_b64_tr_b16 a[232:233], %[rd3] offset:8192\n\tds_read_b64_tr_b16 a[234:235], %[rd3] offset:8320\n\tds_read_b64_tr_b16 a[236:237], %[rd3] offset:12288\n\tds_read_b64_tr_b16 a[238:239], %[rd3] offset:12416\n\ts_waitcnt lgkmcnt(0)\n\t" \
          "s_add_u32 m0, %[lw], 8192\n\ts_xor_b32 %[t], %[kb], 0x200000\n\tbuffer_load_dwordx4 %[go], %[rq], %[t] offen" QK_NTq " lds\n\ts_add_u32 m0, %[lw], 9216\n\ts_xor_b32 %[t], %[kb], 0x210000\n\tbuffer_load_dwordx4 %[go], %[rq], %[t] offen" QK_NTq " lds\n\ts_add_u32 m0, %[lw], 10240\n\ts_xor_b32 %[t], %[kb], 0x220000\n\tbuffer_load_dwordx4 %[go], %[rq], %[t] offen" QK_NTq " lds\n\ts_add_u32 m0, %[lw], 11264\n\ts_xor_b32 %[t], %[kb], 0x230000\n\tbuffer_load_dwordx4 %[go], %[rq], %[t] offen" QK_NTq " lds\n\ts_add_u32 m0, %[lw], 12288\n\ts_xor_b32 %[t], %[kb], 0x240000\n\tbuffer_load_dwordx4 %[go], %[rq], %[t] offen" QK_NTq " lds\n\ts_add_u32 m0, %[lw], 13312\n\ts_xor_b32 %[t], %[kb], 0x250000\n\tbuffer_load_dwordx4 %[go], %[rq], %[t] offen" QK_NTq " lds\n\ts_add_u32 m0, %[lw], 14336\n\ts_xor_b32 %[t], %[kb], 0x260000\n\tbuffer_load_dwordx4 %[go], %[rq], %[t] offen" QK_NTq " lds\n\ts_add_u32 m0, %[lw], 15360\n\ts_xor_b32 %[t], %[kb], 0x270000\n\tbuffer_load_dwordx4 %[go], %[rq], %[t] offen" QK_NTq " lds\n\t" \
          "s_waitcnt vmcnt(32)\n\tds_read_b64_tr_b16 a[56:57], %[rd0] offset:16384\n\tds_read_b64_tr_b16 a[58:59], %[rd0] offset:16512\n\tds_read_b64_tr_b16 a[60:61], %[rd0] offset:20480\n\tds_read_b64_tr_b16 a[62:63], %[rd0] offset:20608\n\tds_read_b64_tr_b16 a[120:121], %[rd1] offset:16384\n\tds_read_b64_tr_b16 a[122:123], %[rd1] offset:16512\n\tds_read_b64_tr_b16 a[124:125], %[rd1] offset:20480\n\tds_read_b64_tr_b16 a[126:127], %[rd1] offset:20608\n\tds_read_b64_tr_b16 a[240:241], %[rd2] offset:16384\n\tds_read_b64_tr_b16 a[242:243], %[rd2] offset:16512\n\tds_read_b64_tr_b16 a[244:245], %[rd2] offset:20480\n\tds_read_b64_tr_b16 a[246:247], %[rd2] offset:20608\n\tds_read_b64_tr_b16 a[248:249], %[rd3] offset:16384\n\tds_read_b64_tr_b16 a[250:251], %[rd3] offset:16512\n\tds_read_b64_tr_b16 a[252:253], %[rd3] offset:20480\n\tds_read_b64_tr_b16 a[254:255], %[rd3] offset:20608\n\ts_waitcnt lgkmcnt(0)\n\t" \
          "s_add_u32 m0, %[lw], 16384\n\ts_xor_b32 %[t], %[kb], 0x180000\n\tbuffer_load_dwordx4 %[go], %[rq], %[t] offen" QK_NTq " lds\n\ts_add_u32 m0, %[lw], 17408\n\ts_xor_b32 %[t], %[kb], 0x190000\n\tbuffer_load_dwordx4 %[go], %[rq], %[t] offen" QK_NTq " lds\n\ts_add_u32 m0, %[lw], 18432\n\ts_xor_b32 %[t], %[kb], 0x1a0000\n\tbuffer_load_dwordx4 %[go], %[rq], %[t] offen" QK_NTq " lds\n\ts_add_u32 m0, %[lw], 19456\n\ts_xor_b32 %[t], %[kb], 0x1b0000\n\tbuffer_load_dwordx4 %[go], %[rq], %[t] offen" QK_NTq " lds\n\ts_add_u32 m0, %[lw], 20480\n\ts_xor_b32 %[t], %[kb], 0x1c0000\n\tbuffer_load_dwordx4 %[go], %[rq], %[t] offen" QK_NTq " lds\n\ts_add_u32 m0, %[lw], 21504\n\ts_xor_b32 %[t], %[kb], 0x1d0000\n\tbuffer_load_dwordx4 %[go], %[rq], %[t] offen" QK_NTq " lds\n\ts_add_u32 m0, %[lw], 22528\n\ts_xor_b32 %[t], %[kb], 0x1e0000\n\tbuffer_load_dwordx4 %[go], %[rq], %[t] offen" QK_NTq " lds\n\ts_add_u32 m0, %[lw], 23552\n\ts_xor_b32 %[t], %[kb], 0x1f0000\n\tbuffer_load_dwordx4 %[go], %[rq], %[t] offen" QK_NTq " lds\n\t" \
          "s_waitcnt vmcnt(32)\n\tds_read_b64_tr_b16 a[24:25], %[rd0] offset:24576\n\tds_read_b64_tr_b16 a[26:27], %[rd0] offset:24704\n\tds_read_b64_tr_b16 a[28:29], %[rd0] offset:28672\n\tds_read_b64_tr_b16 a[30:31], %[rd0] offset:28800\n\tds_read_b64_tr_b16 a[32:33], %[rd1] offset:24576\n\tds_read_b64_tr_b16 a[34:35], %[rd1] offset:24704\n\tds_read_b64_tr_b16 a[36:37], %[rd1] offset:28672\n\tds_read_b64_tr_b16 a[38:39], %[rd1] offset:28800\n\tds_read_b64_tr_b16 a[40:41], %[rd2] offset:24576\n\tds_read_b64_tr_b16 a[42:43], %[rd2] offset:24704\n\tds_read_b64_tr_b16 a[44:45], %[rd2] offset:28672\n\tds_read_b64_tr_b16 a[46:47], %[rd2] offset:28800\n\tds_read_b64_tr_b16 a[48:49], %[rd3] offset:24576\n\tds_read_b64_tr_b16 a[50:51], %[rd3] offset:24704\n\tds_read_b64_tr_b16 a[52:53], %[rd3] offset:28672\n\tds_read_b64_tr_b16 a[54:55], %[rd3] offset:28800\n\ts_waitcnt lgkmcnt(0)\n\t" \
          "s_add_u32 m0, %[lw], 24576\n\ts_xor_b32 %[t], %[kb], 0x100000\n\tbuffer_load_dwordx4 %[go], %[rq], %[t] offen" QK_NTq " lds\n\ts_add_u32 m0, %[lw], 25600\n\ts_xor_b32 %[t], %[kb], 0x110000\n\tbuffer_load_dwordx4 %[go], %[rq], %[t] offen" QK_NTq " lds\n\ts_add_u32 m0, %[lw], 26624\n\ts_xor_b32 %[t], %[kb], 0x120000\n\tbuffer_load_dwordx4 %[go], %[rq], %[t] offen" QK_NTq " lds\n\ts_add_u32 m0, %[lw], 27648\n\ts_xor_b32 %[t], %[kb], 0x130000\n\tbuffer_load_dwordx4 %[go], %[rq], %[t] offen" QK_NTq " lds\n\ts_add_u32 m0, %[lw], 28672\n\ts_xor_b32 %[t], %[kb], 0x140000\n\tbuffer_load_dwordx4 %[go], %[rq], %[t] offen" QK_NTq " lds\n\ts_add_u32 m0, %[lw], 29696\n\ts_xor_b32 %[t], %[kb], 0x150000\n\tbuffer_load_dwordx4 %[go], %[rq], %[t] offen" QK_NTq " lds\n\ts_add_u32 m0, %[lw], 30720\n\ts_xor_b32 %[t], %[kb], 0x160000\n\tbuffer_load_dwordx4 %[go], %[rq], %[t] offen" QK_NTq " lds\n\ts_add_u32 m0, %[lw], 31744\n\ts_xor_b32 %[t], %[kb], 0x170000\n\tbuffer_load_dwordx4 %[go], %[rq], %[t] offen" QK_NTq " lds\n\t" \
          "s_waitcnt vmcnt(32)\n\tds_read_b64_tr_b16 v[160:161], %[rd0] offset:32768\n\tds_read_b64_tr_b16 v[162:163], %[rd0] offset:32896\n\tds_read_b64_tr_b16 v[164:165], %[rd0] offset:36864\n\tds_read_b64_tr_b16 v[166:167], %[rd0] offset:36992\n\tds_read_b64_tr_b16 v[128:129], %[rd1] offset:32768\n\tds_read_b64_tr_b16 v[130:131], %[rd1] offset:32896\n\tds_read_b64_tr_b16 v[132:133], %[rd1] offset:36864\n\tds_read_b64_tr_b16 v[134:135], %[rd1] offset:36992\n\tds_read_b64_tr_b16 a[0:1], %[rd2] offset:32768\n\tds_read_b64_tr_b16 a[2:3], %[rd2] offset:32896\n\tds_read_b64_tr_b16 a[4:5], %[rd2] offset:36864\n\tds_read_b64_tr_b16 a[6:7], %[rd2] offset:36992\n\tds_read_b64_tr_b16 a[8:9], %[rd3] offset:32768\n\tds_read_b64_tr_b16 a[10:11], %[rd3] offset:32896\n\tds_read_b64_tr_b16 a[12:13], %[rd3] offset:36864\n\tds_read_b64_tr_b16 a[14:15], %[rd3] offset:36992\n\ts_waitcnt lgkmcnt(0)\n\t" \
          "s_add_u32 m0, %[lw], 32768\n\ts_xor_b32 %[t], %[kb], 0x80000\n\tbuffer_load_dwordx4 %[go], %[rq], %[t] offen" QK_NTq " lds\n\ts_add_u32 m0, %[lw], 33792\n\ts_xor_b32 %[t], %[kb], 0x90000\n\tbuffer_load_dwordx4 %[go], %[rq], %[t] offen" QK_NTq " lds\n\ts_add_u32 m0, %[lw], 34816\n\ts_xor_b32 %[t], %[kb], 0xa0000\n\tbuffer_load_dwordx4 %[go], %[rq], %[t] offen" QK_NTq " lds\n\ts_add_u32 m0, %[lw], 35840\n\ts_xor_b32 %[t], %[kb], 0xb0000\n\tbuffer_load_dwordx4 %[go], %[rq], %[t] offen" QK_NTq " lds\n\ts_add_u32 m0, %[lw], 36864\n\ts_xor_b32 %[t], %[kb], 0xc0000\n\tbuffer_load_dwordx4 %[go], %[rq], %[t] offen" QK_NTq " lds\n\ts_add_u32 m0, %[lw], 37888\n\ts_xor_b32 %[t], %[kb], 0xd0000\n\tbuffer_load_dwordx4 %[go], %[rq], %[t] offen" QK_NTq " lds\n\ts_add_u32 m0, %[lw], 38912\n\ts_xor_b32 %[t], %[kb], 0xe0000\n\tbuffer_load_dwordx4 %[go], %[rq], %[t] offen" QK_NTq " lds\n\ts_add_u32 m0, %[lw], 39936\n\ts_xor_b32 %[t], %[kb], 0xf0000\n\tbuffer_load_dwordx4 %[go], %[rq], %[t] offen" QK_NTq " lds\n\t" \
          "s_waitcnt vmcnt(32)\n\tds_read_b64_tr_b16 v[104:105], %[rd0] offset:0\n\tds_read_b64_tr_b16 v[106:107], %[rd0] offset:128\n\tds_read_b64_tr_b16 v[108:109], %[rd0] offset:4096\n\tds_read_b64_tr_b16 v[110:111], %[rd0] offset:4224\n\tds_read_b64_tr_b16 v[168:169], %[rd1] offset:0\n\tds_read_b64_tr_b16 v[170:171], %[rd1] offset:128\n\tds_read_b64_tr_b16 v[172:173], %[rd1] offset:4096\n\tds_read_b64_tr_b16 v[174:175], %[rd1] offset:4224\n\tds_read_b64_tr_b16 v[112:113], %[rd2] offset:0\n\tds_read_b64_tr_b16 v[114:115], %[rd2] offset:128\n\tds_read_b64_tr_b16 v[116:117], %[rd2] offset:4096\n\tds_read_b64_tr_b16 v[118:119], %[rd2] offset:4224\n\tds_read_b64_tr_b16 v[176:177], %[rd3] offset:0\n\tds_read_b64_tr_b16 v[178:179], %[rd3] offset:128\n\tds_read_b64_tr_b16 v[180:181], %[rd3] offset:4096\n\tds_read_b64_tr_b16 v[182:183], %[rd3] offset:4224\n\ts_waitcnt lgkmcnt(0)\n\t" \
          "buffer_load_dwordx4 %[rec], %[ro], %[rr], 0 offen sc1\n\t" \
          "s_add_u32 m0, %[lw], 0\n\ts_xor_b32 %[t], %[kb], 0x0\n\tbuffer_load_dwordx4 %[go], %[rq], %[t] offen" QK_NTq " lds\n\ts_add_u32 m0, %[lw], 1024\n\ts_xor_b32 %[t], %[kb], 0x10000\n\tbuffer_load_dwordx4 %[go], %[rq], %[t] offen" QK_NTq " lds\n\ts_add_u32 m0, %[lw], 2048\n\ts_xor_b32 %[t], %[kb], 0x20000\n\tbuffer_load_dwordx4 %[go], %[rq], %[t] offen" QK_NTq " lds\n\ts_add_u32 m0, %[lw], 3072\n\ts_xor_b32 %[t], %[kb], 0x30000\n\tbuffer_load_dwordx4 %[go], %[rq], %[t] offen" QK_NTq " lds\n\ts_add_u32 m0, %[lw], 4096\n\ts_xor_b32 %[t], %[kb], 0x40000\n\tbuffer_load_dwordx4 %[go], %[rq], %[t] offen" QK_NTq " lds\n\ts_add_u32 m0, %[lw], 5120\n\ts_xor_b32 %[t], %[kb], 0x50000\n\tbuffer_load_dwordx4 %[go], %[rq], %[t] offen" QK_NTq " lds\n\ts_add_u32 m0, %[lw], 6144\n\ts_xor_b32 %[t], %[kb], 0x60000\n\tbuffer_load_dwordx4 %[go], %[rq], %[t] offen" QK_NTq " lds\n\ts_add_u32 m0, %[lw], 7168\n\ts_xor_b32 %[t], %[kb], 0x70000\n\tbuffer_load_dwordx4 %[go], %[rq], %[t] offen" QK_NTq " lds\n\t" \
          "s_waitcnt vmcnt(33)\n\tds_read_b64_tr_b16 v[184:185], %[rd0] offset:8192\n\tds_read_b64_tr_b16 v[186:187], %[rd0] offset:8320\n\tds_read_b64_tr_b16 v[188:189], %[rd0] offset:12288\n\tds_read_b64_tr_b16 v[190:191], %[rd0] offset:12416\n\tds_read_b64_tr_b16 v[88:89], %[rd1] offset:8192\n\tds_read_b64_tr_b16 v[90:91], %[rd1] offset:8320\n\tds_read_b64_tr_b16 v[92:93], %[rd1] offset:12288\n\tds_read_b64_tr_b16 v[94:95], %[rd1] offset:12416\n\tds_read_b64_tr_b16 v[192:193], %[rd2] offset:8192\n\tds_read_b64_tr_b16 v[194:195], %[rd2] offset:8320\n\tds_read_b64_tr_b16 v[196:197], %[rd2] offset:12288\n\tds_read_b64_tr_b16 v[198:199], %[rd2] offset:12416\n\tds_read_b64_tr_b16 v[96:97], %[rd3] offset:8192\n\tds_read_b64_tr_b16 v[98:99], %[rd3] offset:8320\n\tds_read_b64_tr_b16 v[100:101], %[rd3] offset:12288\n\tds_read_b64_tr_b16 v[102:103], %[rd3] offset:12416\n\ts_waitcnt lgkmcnt(0)\n\tds_write_b128 %[own], v[136:139] offset:8192\n\tds_write_b128 %[own], v[140:143] offset:9216\n\tds_write_b128 %[own], v[144:147] offset:10240\n\tds_write_b128 %[own], v[148:151] offset:11264\n\tds_write_b128 %[own], v[152:155] offset:12288\n\tds_write_b128 %[own], v[156:159] offset:13312\n\tds_write_b128 %[own], v[160:163] offset:14336\n\tds_write_b128 %[own], v[164:167] offset:15360\n\ts_waitcnt lgkmcnt(0)\n\t" \
          "s_waitcnt vmcnt(25)\n\tds_read_b64_tr_b16 v[64:65], %[rd0] offset:16384\n\tds_read_b64_tr_b16 v[66:67], %[rd0] offset:16512\n\tds_read_b64_tr_b16 v[68:69], %[rd0] offset:20480\n\tds_read_b64_tr_b16 v[70:71], %[rd0] offset:20608\n\tds_read_b64_tr_b16 v[200:201], %[rd1] offset:16384\n\tds_read_b64_tr_b16 v[202:203], %[rd1] offset:16512\n\tds_read_b64_tr_b16 v[204:205], %[rd1] offset:20480\n\tds_read_b64_tr_b16 v[206:207], %[rd1] offset:20608\n\tds_read_b64_tr_b16 v[72:73], %[rd2] offset:16384\n\tds_read_b64_tr_b16 v[74:75], %[rd2] offset:16512\n\tds_read_b64_tr_b16 v[76:77], %[rd2] offset:20480\n\tds_read_b64_tr_b16 v[78:79], %[rd2] offset:20608\n\tds_read_b64_tr_b16 v[208:209], %[rd3] offset:16384\n\tds_read_b64_tr_b16 v[210:211], %[rd3] offset:16512\n\tds_read_b64_tr_b16 v[212:213], %[rd3] offset:20480\n\tds_read_b64_tr_b16 v[214:215], %[rd3] offset:20608\n\ts_waitcnt lgkmcnt(0)\n\tds_write_b128 %[own], v[168:171] offset:16384\n\tds_write_b128 %[own], v[172:175] offset:17408\n\tds_write_b128 %[own], v[176:179] offset:18432\n\tds_write_b128 %[own], v[180:183] offset:19456\n\tds_write_b128 %[own], v[184:187] offset:20480\n\tds_write_b128 %[own], v[188:191] offset:21504\n\tds_write_b128 %[own], v[192:195] offset:22528\n\tds_write_b128 %[own], v[196:199] offset:23552\n\ts_waitcnt lgkmcnt(0)\n\t" \
          "s_waitcnt vmcnt(17)\n\tds_read_b64_tr_b16 v[216:217], %[rd0] offset:24576\n\tds_read_b64_tr_b16 v[218:219], %[rd0] offset:24704\n\tds_read_b64_tr_b16 v[220:221], %[rd0] offset:28672\n\tds_read_b64_tr_b16 v[222:223], %[rd0] offset:28800\n\tds_read_b64_tr_b16 v[48:49], %[rd1] offset:24576\n\tds_read_b64_tr_b16 v[50:51], %[rd1] offset:24704\n\tds_read_b64_tr_b16 v[52:53], %[rd1] offset:28672\n\tds_read_b64_tr_b16 v[54:55], %[rd1] offset:28800\n\tds_read_b64_tr_b16 v[224:225], %[rd2] offset:24576\n\tds_read_b64_tr_b16 v[226:227], %[rd2] offset:24704\n\tds_read_b64_tr_b16 v[228:229], %[rd2] offset:28672\n\tds_read_b64_tr_b16 v[230:231], %[rd2] offset:28800\n\tds_read_b64_tr_b16 v[56:57], %[rd3] offset:24576\n\tds_read_b64_tr_b16 v[58:59], %[rd3] offset:24704\n\tds_read_b64_tr_b16 v[60:61], %[rd3] offset:28672\n\tds_read_b64_tr_b16 v[62:63], %[rd3] offset:28800\n\ts_waitcnt lgkmcnt(0)\n\tds_write_b128 %[own], v[200:203] offset:24576\n\tds_write_b128 %[own], v[204:207] offset:25600\n\tds_write_b128 %[own], v[208:211] offset:26624\n\tds_write_b128 %[own], v[212:215] offset:27648\n\tds_write_b128 %[own], v[216:219] offset:28672\n\tds_write_b128 %[own], v[220:223] offset:29696\n\tds_write_b128 %[own], v[224:227] offset:30720\n\tds_write_b128 %[own], v[228:231] offset:31744\n\ts_waitcnt lgkmcnt(0)\n\t" \
          "s_waitcnt vmcnt(8)\n\tds_read_b64_tr_b16 v[24:25], %[rd0] offset:32768\n\tds_read_b64_tr_b16 v[26:27], %[rd0] offset:32896\n\tds_read_b64_tr_b16 v[28:29], %[rd0] offset:36864\n\tds_read_b64_tr_b16 v[30:31], %[rd0] offset:36992\n\tds_read_b64_tr_b16 v[136:137], %[rd1] offset:32768\n\tds_read_b64_tr_b16 v[138:139], %[rd1] offset:32896\n\tds_read_b64_tr_b16 v[140:141], %[rd1] offset:36864\n\tds_read_b64_tr_b16 v[142:143], %[rd1] offset:36992\n\tds_read_b64_tr_b16 v[32:33], %[rd2] offset:32768\n\tds_read_b64_tr_b16 v[34:35], %[rd2] offset:32896\n\tds_read_b64_tr_b16 v[36:37], %[rd2] offset:36864\n\tds_read_b64_tr_b16 v[38:39], %[rd2] offset:36992\n\tds_read_b64_tr_b16 v[144:145], %[rd3] offset:32768\n\tds_read_b64_tr_b16 v[146:147], %[rd3] offset:32896\n\tds_read_b64_tr_b16 v[148:149], %[rd3] offset:36864\n\tds_read_b64_tr_b16 v[150:151], %[rd3] offset:36992\n\ts_waitcnt lgkmcnt(0)\n\t" \
          "buffer_load_dword %[kf], %[ko], %[rc], 0 offen sc1\n\t" \
          "s_waitcnt vmcnt(0)\n\tds_read_b64_tr_b16 v[152:153], %[rd0] offset:0\n\tds_read_b64_tr_b16 v[154:155], %[rd0] offset:128\n\tds_read_b64_tr_b16 v[156:157], %[rd0] offset:4096\n\tds_read_b64_tr_b16 v[158:159], %[rd0] offset:4224\n\tds_read_b64_tr_b16 v[8:9], %[rd1] offset:0\n\tds_read_b64_tr_b16 v[10:11], %[rd1] offset:128\n\tds_read_b64_tr_b16 v[12:13], %[rd1] offset:4096\n\tds_read_b64_tr_b16 v[14:15], %[rd1] offset:4224\n\tds_read_b64_tr_b16 v[160:161], %[rd2] offset:0\n\tds_read_b64_tr_b16 v[162:163], %[rd2] offset:128\n\tds_read_b64_tr_b16 v[164:165], %[rd2] offset:4096\n\tds_read_b64_tr_b16 v[166:167], %[rd2] offset:4224\n\tds_read_b64_tr_b16 v[16:17], %[rd3] offset:0\n\tds_read_b64_tr_b16 v[18:19], %[rd3] offset:128\n\tds_read_b64_tr_b16 v[20:21], %[rd3] offset:4096\n\tds_read_b64_tr_b16 v[22:23], %[rd3] offset:4224\n\ts_waitcnt lgkmcnt(0)\n\tds_write_b128 %[own], v[136:139] offset:0\n\tds_write_b128 %[own], v[140:143] offset:1024\n\tds_write_b128 %[own], v[144:147] offset:2048\n\tds_write_b128 %[own], v[148:151] offset:3072\n\tds_write_b128 %[own], v[152:155] offset:4096\n\tds_write_b128 %[own], v[156:159] offset:5120\n\tds_write_b128 %[own], v[160:163] offset:6144\n\tds_write_b128 %[own], v[164:167] offset:7168\n\ts_waitcnt lgkmcnt(0)\n\t"
#define QK_DEFW "=&{a[0:31]}"(wa[0]), "=&{a[32:63]}"(wa[1]), "=&{a[64:95]}"(wa[2]), "=&{a[96:127]}"(wa[3]), "=&{a[128:159]}"(wa[4]), "=&{a[160:191]}"(wa[5]), "=&{a[192:223]}"(wa[6]), "=&{a[224:255]}"(wa[7]), "=&{v[8:39]}"(wv_[0]), "=&{v[40:71]}"(wv_[1]), "=&{v[72:103]}"(wv_[2]), "=&{v[104:135]}"(wv_[3]), "=&{v[136:167]}"(wv_[4]), "=&{v[168:199]}"(wv_[5]), "=&{v[200:231]}"(wv_[6])
#define QK_ALW 1
#define QK_STEP \
          "ds_read_b128 v[200:203], %[own] offset:4096\n\t" \
          "ds_read_b128 v[204:207], %[own] offset:5120\n\t" \
          "ds_read_b128 v[208:211], %[own] offset:6144\n\t" \
          "ds_read_b128 v[212:215], %[own] offset:7168\n\t" \
          "ds_read_b128 v[216:219], %[own] offset:8192\n\t" \
          "ds_read_b128 v[220:223], %[own] offset:9216\n\t" \
          "ds_read_b128 v[224:227], %[own] offset:0\n\t" \
          "ds_read_b128 v[228:231], %[own] offset:1024\n\t" \
          "v_mov_b32 %[al], -1\n\t" \
          "v_mov_b64 v[168:169], 0\n\t" \
          "v_mov_b64 v[170:171], 0\n\t" \
          "v_mov_b64 v[172:173], 0\n\t" \
          "v_mov_b64 v[174:175], 0\n\t" \
          "v_mov_b64 v[176:177], 0\n\t" \
          "v_mov_b64 v[178:179], 0\n\t" \
          "v_mov_b64 v[180:181], 0\n\t" \
          "v_mov_b64 v[182:183], 0\n\t" \
          "v_mov_b64 v[184:185], 0\n\t" \
          "v_mov_b64 v[186:187], 0\n\t" \
          "v_mov_b64 v[188:189], 0\n\t" \
          "v_mov_b64 v[190:191], 0\n\t" \
          "v_mov_b64 v[192:193], 0\n\t" \
          "v_mov_b64 v[194:195], 0\n\t" \
          "v_mov_b64 v[196:197], 0\n\t" \
          "v_mov_b64 v[198:199], 0\n\t" \
          "s_mov_b32 %[ann], %[t]\n\t" \
          "s_mov_b32 %[fl], 0\n\t" \
          "s_mov_b64 %[en], -1\n\t" \
          "s_waitcnt lgkmcnt(0)\n\t" \
          "s_cmp_eq_u32 %[w], 3\n\t" \
          "s_cbranch_scc0 Lqk_z%=\n\t" \
          "s_branch Lqk_c%=\n\t" \
          "Lqk_fail%=:\n\t" \
          "s_memrealtime vcc\n\t" \
          "s_waitcnt lgkmcnt(0)\n\t" \
          "s_add_u32 %[ann], vcc_lo, %[ald0]\n\t" \
          "v_mbcnt_lo_u32_b32 %[al], -1, 0\n\t" \
          "v_lshlrev_b32 %[al], 2, %[al]\n\t" \
          "s_and_b32 vcc_lo, %[ann], 0xffff\n\t" \
          "s_or_b32 vcc_lo, vcc_lo, 0x10000\n\t" \
          "v_mov_b32 %[m], vcc_lo\n\t" \
          "s_mov_b64 exec, %[alx]\n\t" \
          "buffer_store_dword %[m], %[al], %[rsm], %[als] offen sc1\n\t" \
          "s_mov_b64 exec, -1\n\t" \
          "buffer_load_dwordx4 v[136:139], %[ml0], %[rsm], %[mb0] offen sc1\n\t" \
          "buffer_load_dwordx4 v[140:143], %[ml1], %[rsm], %[mb0] offen sc1\n\t" \
          "buffer_load_dwordx4 v[144:147], %[ml0], %[rsm], %[mb1] offen sc1\n\t" \
          "buffer_load_dwordx4 v[148:151], %[ml1], %[rsm], %[mb1] offen sc1\n\t" \
          "buffer_load_dwordx4 v[152:155], %[ml0], %[rsm], %[mb2] offen sc1\n\t" \
          "buffer_load_dwordx4 v[156:159], %[ml1], %[rsm], %[mb2] offen sc1\n\t" \
          "buffer_load_dwordx4 v[160:163], %[ml0], %[rsm], %[mb3] offen sc1\n\t" \
          "buffer_load_dwordx4 v[164:167], %[ml1], %[rsm], %[mb3] offen sc1\n\t" \
          "ds_read_b128 v[200:203], %[own] offset:4096\n\t" \
          "ds_read_b128 v[204:207], %[own] offset:5120\n\t" \
          "ds_read_b128 v[208:211], %[own] offset:6144\n\t" \
          "ds_read_b128 v[212:215], %[own] offset:7168\n\t" \
          "ds_read_b128 v[216:219], %[own] offset:8192\n\t" \
          "ds_read_b128 v[220:223], %[own] offset:9216\n\t" \
          "ds_read_b128 v[224:227], %[own] offset:0\n\t" \
          "ds_read_b128 v[228:231], %[own] offset:1024\n\t" \
          "v_mov_b32 %[al], -1\n\t" \
          "v_mov_b64 v[168:169], 0\n\t" \
          "v_mov_b64 v[170:171], 0\n\t" \
          "v_mov_b64 v[172:173], 0\n\t" \
          "v_mov_b64 v[174:175], 0\n\t" \
          "v_mov_b64 v[176:177], 0\n\t" \
          "v_mov_b64 v[178:179], 0\n\t" \
          "v_mov_b64 v[180:181], 0\n\t" \
          "v_mov_b64 v[182:183], 0\n\t" \
          "v_mov_b64 v[184:185], 0\n\t" \
          "v_mov_b64 v[186:187], 0\n\t" \
          "v_mov_b64 v[188:189], 0\n\t" \
          "v_mov_b64 v[190:191], 0\n\t" \
          "v_mov_b64 v[192:193], 0\n\t" \
          "v_mov_b64 v[194:195], 0\n\t" \
          "v_mov_b64 v[196:197], 0\n\t" \
          "v_mov_b64 v[198:199], 0\n\t" \
          "s_mov_b32 %[fl], 1\n\t" \
          "s_mov_b64 %[en], 0\n\t" \
          "s_mov_b32 %[cnt], 0\n\t" \
          "s_waitcnt vmcnt(7)\n\t" \
          "v_max3_u32 %[m], v136, v137, v138\n\t" \
          "v_max_u32 %[m], %[m], v139\n\t" \
          "v_cmp_eq_u32 vcc, -1, %[m]\n\t" \
          "s_cbranch_vccnz Lqk_p0_%=\n\t" \
          "Lqk_k0_%=:\n\t" \
          "s_waitcnt vmcnt(6)\n\t" \
          "v_max3_u32 %[m], v140, v141, v142\n\t" \
          "v_max_u32 %[m], %[m], v143\n\t" \
          "v_cmp_eq_u32 vcc, -1, %[m]\n\t" \
          "s_cbranch_vccnz Lqk_p1_%=\n\t" \
          "Lqk_k1_%=:\n\t" \
          "s_waitcnt vmcnt(5)\n\t" \
          "v_max3_u32 %[m], v144, v145, v146\n\t" \
          "v_max_u32 %[m], %[m], v147\n\t" \
          "v_cmp_eq_u32 vcc, -1, %[m]\n\t" \
          "s_cbranch_vccnz Lqk_p2_%=\n\t" \
          "Lqk_k2_%=:\n\t" \
          "s_waitcnt vmcnt(4)\n\t" \
          "v_max3_u32 %[m], v148, v149, v150\n\t" \
          "v_max_u32 %[m], %[m], v151\n\t" \
          "v_cmp_eq_u32 vcc, -1, %[m]\n\t" \
          "s_cbranch_vccnz Lqk_p3_%=\n\t" \
          "Lqk_k3_%=:\n\t" \
          "s_waitcnt vmcnt(3)\n\t" \
          "v_max3_u32 %[m], v152, v153, v154\n\t" \
          "v_max_u32 %[m], %[m], v155\n\t" \
          "v_cmp_eq_u32 vcc, -1, %[m]\n\t" \
          "s_cbranch_vccnz Lqk_p4_%=\n\t" \
          "Lqk_k4_%=:\n\t" \
          "s_waitcnt vmcnt(2)\n\t" \
          "v_max3_u32 %[m], v156, v157, v158\n\t" \
          "v_max_u32 %[m], %[m], v159\n\t" \
          "v_cmp_eq_u32 vcc, -1, %[m]\n\t" \
          "s_cbranch_vccnz Lqk_p5_%=\n\t" \
          "Lqk_k5_%=:\n\t" \
          "s_waitcnt vmcnt(1)\n\t" \
          "v_max3_u32 %[m], v160, v161, v162\n\t" \
          "v_max_u32 %[m], %[m], v163\n\t" \
          "v_cmp_eq_u32 vcc, -1, %[m]\n\t" \
          "s_cbranch_vccnz Lqk_p6_%=\n\t" \
          "Lqk_k6_%=:\n\t" \
          "s_waitcnt vmcnt(0)\n\t" \
          "v_max3_u32 %[m], v164, v165, v166\n\t" \
          "v_max_u32 %[m], %[m], v167\n\t" \
          "v_cmp_eq_u32 vcc, -1, %[m]\n\t" \
          "s_cbranch_vccnz Lqk_p7_%=\n\t" \
          "Lqk_k7_%=:\n\t" \
          "s_memrealtime vcc\n\t" \
          "s_waitcnt lgkmcnt(0)\n\t" \
          "s_add_u32 vcc_lo, vcc_lo, %[ald]\n\t" \
          "s_sub_i32 vcc_hi, vcc_lo, %[ann]\n\t" \
          "s_cmp_gt_i32 vcc_hi, 0\n\t" \
          "s_cbranch_scc0 Lqk_h%=\n\t" \
          "s_mov_b32 %[ann], vcc_lo\n\t" \
          "v_mbcnt_lo_u32_b32 %[al], -1, 0\n\t" \
          "v_lshlrev_b32 %[al], 2, %[al]\n\t" \
          "s_and_b32 vcc_lo, %[ann], 0xffff\n\t" \
          "s_or_b32 vcc_lo, vcc_lo, 0x10000\n\t" \
          "v_mov_b32 %[m], vcc_lo\n\t" \
          "s_mov_b64 exec, %[alx]\n\t" \
          "buffer_store_dword %[m], %[al], %[rsm], %[als] offen sc1\n\t" \
          "s_mov_b64 exec, -1\n\t" \
          "Lqk_h%=:\n\t" \
          "v_mov_b32 %[al], -1\n\t" \
          "s_branch Lqk_go%=\n\t" \
          "Lqk_p0_%=:\n\t" \
          "s_cmp_lt_i32 %[cnt], 0\n\t" \
          "s_cbranch_scc1 Lqk_k0_%=\n\t" \
          "s_add_u32 %[cnt], %[cnt], 1\n\t" \
          "s_and_b32 vcc_lo, %[cnt], 3\n\t" \
          "s_cbranch_scc1 Lqk_q0_%=\n\t" \
          "s_getpc_b64 s[48:49]\n\t" \
          "s_branch Lqk_spin%=\n\t" \
          "Lqk_q0_%=:\n\t" \
          "buffer_load_dwordx4 v[136:139], %[ml0], %[rsm], %[mb0] offen sc1\n\t" \
          "s_waitcnt vmcnt(0)\n\t" \
          "v_max3_u32 %[m], v136, v137, v138\n\t" \
          "v_max_u32 %[m], %[m], v139\n\t" \
          "v_cmp_eq_u32 vcc, -1, %[m]\n\t" \
          "s_cbranch_vccnz Lqk_p0_%=\n\t" \
          "buffer_load_dwordx4 v[140:143], %[ml1], %[rsm], %[mb0] offen sc1\n\t" \
          "buffer_load_dwordx4 v[144:147], %[ml0], %[rsm], %[mb1] offen sc1\n\t" \
          "buffer_load_dwordx4 v[148:151], %[ml1], %[rsm], %[mb1] offen sc1\n\t" \
          "buffer_load_dwordx4 v[152:155], %[ml0], %[rsm], %[mb2] offen sc1\n\t" \
          "buffer_load_dwordx4 v[156:159], %[ml1], %[rsm], %[mb2] offen sc1\n\t" \
          "buffer_load_dwordx4 v[160:163], %[ml0], %[rsm], %[mb3] offen sc1\n\t" \
          "buffer_load_dwordx4 v[164:167], %[ml1], %[rsm], %[mb3] offen sc1\n\t" \
          "s_branch Lqk_k0_%=\n\t" \
          "Lqk_p1_%=:\n\t" \
          "s_cmp_lt_i32 %[cnt], 0\n\t" \
          "s_cbranch_scc1 Lqk_k1_%=\n\t" \
          "s_add_u32 %[cnt], %[cnt], 1\n\t" \
          "s_and_b32 vcc_lo, %[cnt], 3\n\t" \
          "s_cbranch_scc1 Lqk_q1_%=\n\t" \
          "s_getpc_b64 s[48:49]\n\t" \
          "s_branch Lqk_spin%=\n\t" \
          "Lqk_q1_%=:\n\t" \
          "buffer_load_dwordx4 v[140:143], %[ml1], %[rsm], %[mb0] offen sc1\n\t" \
          "s_waitcnt vmcnt(0)\n\t" \
          "v_max3_u32 %[m], v140, v141, v142\n\t" \
          "v_max_u32 %[m], %[m], v143\n\t" \
          "v_cmp_eq_u32 vcc, -1, %[m]\n\t" \
          "s_cbranch_vccnz Lqk_p1_%=\n\t" \
          "buffer_load_dwordx4 v[144:147], %[ml0], %[rsm], %[mb1] offen sc1\n\t" \
          "buffer_load_dwordx4 v[148:151], %[ml1], %[rsm], %[mb1] offen sc1\n\t" \
          "buffer_load_dwordx4 v[152:155], %[ml0], %[rsm], %[mb2] offen sc1\n\t" \
          "buffer_load_dwordx4 v[156:159], %[ml1], %[rsm], %[mb2] offen sc1\n\t" \
          "buffer_load_dwordx4 v[160:163], %[ml0], %[rsm], %[mb3] offen sc1\n\t" \
          "buffer_load_dwordx4 v[164:167], %[ml1], %[rsm], %[mb3] offen sc1\n\t" \
          "s_branch Lqk_k1_%=\n\t" \
          "Lqk_p2_%=:\n\t" \
          "s_cmp_lt_i32 %[cnt], 0\n\t" \
          "s_cbranch_scc1 Lqk_k2_%=\n\t" \
          "s_add_u32 %[cnt], %[cnt], 1\n\t" \
          "s_and_b32 vcc_lo, %[cnt], 3\n\t" \
          "s_cbranch_scc1 Lqk_q2_%=\n\t" \
          "s_getpc_b64 s[48:49]\n\t" \
          "s_branch Lqk_spin%=\n\t" \
          "Lqk_q2_%=:\n\t" \
          "buffer_load_dwordx4 v[144:147], %[ml0], %[rsm], %[mb1] offen sc1\n\t" \
          "s_waitcnt vmcnt(0)\n\t" \
          "v_max3_u32 %[m], v144, v145, v146\n\t" \
          "v_max_u32 %[m], %[m], v147\n\t" \
          "v_cmp_eq_u32 vcc, -1, %[m]\n\t" \
          "s_cbranch_vccnz Lqk_p2_%=\n\t" \
          "buffer_load_dwordx4 v[148:151], %[ml1], %[rsm], %[mb1] offen sc1\n\t" \
          "buffer_load_dwordx4 v[152:155], %[ml0], %[rsm], %[mb2] offen sc1\n\t" \
          "buffer_load_dwordx4 v[156:159], %[ml1], %[rsm], %[mb2] offen sc1\n\t" \
          "buffer_load_dwordx4 v[160:163], %[ml0], %[rsm], %[mb3] offen sc1\n\t" \
          "buffer_load_dwordx4 v[164:167], %[ml1], %[rsm], %[mb3] offen sc1\n\t" \
          "s_branch Lqk_k2_%=\n\t" \
          "Lqk_p3_%=:\n\t" \
          "s_cmp_lt_i32 %[cnt], 0\n\t" \
          "s_cbranch_scc1 Lqk_k3_%=\n\t" \
          "s_add_u32 %[cnt], %[cnt], 1\n\t" \
          "s_and_b32 vcc_lo, %[cnt], 3\n\t" \
          "s_cbranch_scc1 Lqk_q3_%=\n\t" \
          "s_getpc_b64 s[48:49]\n\t" \
          "s_branch Lqk_spin%=\n\t" \
          "Lqk_q3_%=:\n\t" \
          "buffer_load_dwordx4 v[148:151], %[ml1], %[rsm], %[mb1] offen sc1\n\t" \
          "s_waitcnt vmcnt(0)\n\t" \
          "v_max3_u32 %[m], v148, v149, v150\n\t" \
          "v_max_u32 %[m], %[m], v151\n\t" \
          "v_cmp_eq_u32 vcc, -1, %[m]\n\t" \
          "s_cbranch_vccnz Lqk_p3_%=\n\t" \
          "buffer_load_dwordx4 v[152:155], %[ml0], %[rsm], %[mb2] offen sc1\n\t" \
          "buffer_load_dwordx4 v[156:159], %[ml1], %[rsm], %[mb2] offen sc1\n\t" \
          "buffer_load_dwordx4 v[160:163], %[ml0], %[rsm], %[mb3] offen sc1\n\t" \
          "buffer_load_dwordx4 v[164:167], %[ml1], %[rsm], %[mb3] offen sc1\n\t" \
          "s_branch Lqk_k3_%=\n\t" \
          "Lqk_p4_%=:\n\t" \
          "s_cmp_lt_i32 %[cnt], 0\n\t" \
          "s_cbranch_scc1 Lqk_k4_%=\n\t" \
          "s_add_u32 %[cnt], %[cnt], 1\n\t" \
          "s_and_b32 vcc_lo, %[cnt], 3\n\t" \
          "s_cbranch_scc1 Lqk_q4_%=\n\t" \
          "s_getpc_b64 s[48:49]\n\t" \
          "s_branch Lqk_spin%=\n\t" \
          "Lqk_q4_%=:\n\t" \
          "buffer_load_dwordx4 v[152:155], %[ml0], %[rsm], %[mb2] offen sc1\n\t" \
          "s_waitcnt vmcnt(0)\n\t" \
          "v_max3_u32 %[m], v152, v153, v154\n\t" \
          "v_max_u32 %[m], %[m], v155\n\t" \
          "v_cmp_eq_u32 vcc, -1, %[m]\n\t" \
          "s_cbranch_vccnz Lqk_p4_%=\n\t" \
          "buffer_load_dwordx4 v[156:159], %[ml1], %[rsm], %[mb2] offen sc1\n\t" \
          "buffer_load_dwordx4 v[160:163], %[ml0], %[rsm], %[mb3] offen sc1\n\t" \
          "buffer_load_dwordx4 v[164:167], %[ml1], %[rsm], %[mb3] offen sc1\n\t" \
          "s_branch Lqk_k4_%=\n\t" \
          "Lqk_p5_%=:\n\t" \
          "s_cmp_lt_i32 %[cnt], 0\n\t" \
          "s_cbranch_scc1 Lqk_k5_%=\n\t" \
          "s_add_u32 %[cnt], %[cnt], 1\n\t" \
          "s_and_b32 vcc_lo, %[cnt], 3\n\t" \
          "s_cbranch_scc1 Lqk_q5_%=\n\t" \
          "s_getpc_b64 s[48:49]\n\t" \
          "s_branch Lqk_spin%=\n\t" \
          "Lqk_q5_%=:\n\t" \
          "buffer_load_dwordx4 v[156:159], %[ml1], %[rsm], %[mb2] offen sc1\n\t" \
          "s_waitcnt vmcnt(0)\n\t" \
          "v_max3_u32 %[m], v156, v157, v158\n\t" \
          "v_max_u32 %[m], %[m], v159\n\t" \
          "v_cmp_eq_u32 vcc, -1, %[m]\n\t" \
          "s_cbranch_vccnz Lqk_p5_%=\n\t" \
          "buffer_load_dwordx4 v[160:163], %[ml0], %[rsm], %[mb3] offen sc1\n\t" \
          "buffer_load_dwordx4 v[164:167], %[ml1], %[rsm], %[mb3] offen sc1\n\t" \
          "s_branch Lqk_k5_%=\n\t" \
          "Lqk_p6_%=:\n\t" \
          "s_cmp_lt_i32 %[cnt], 0\n\t" \
          "s_cbranch_scc1 Lqk_k6_%=\n\t" \
          "s_add_u32 %[cnt], %[cnt], 1\n\t" \
          "s_and_b32 vcc_lo, %[cnt], 3\n\t" \
          "s_cbranch_scc1 Lqk_q6_%=\n\t" \
          "s_getpc_b64 s[48:49]\n\t" \
          "s_branch Lqk_spin%=\n\t" \
          "Lqk_q6_%=:\n\t" \
          "buffer_load_dwordx4 v[160:163], %[ml0], %[rsm], %[mb3] offen sc1\n\t" \
          "s_waitcnt vmcnt(0)\n\t" \
          "v_max3_u32 %[m], v160, v161, v162\n\t" \
          "v_max_u32 %[m], %[m], v163\n\t" \
          "v_cmp_eq_u32 vcc, -1, %[m]\n\t" \
          "s_cbranch_vccnz Lqk_p6_%=\n\t" \
          "buffer_load_dwordx4 v[164:167], %[ml1], %[rsm], %[mb3] offen sc1\n\t" \
          "s_branch Lqk_k6_%=\n\t" \
          "Lqk_p7_%=:\n\t" \
          "s_cmp_lt_i32 %[cnt], 0\n\t" \
          "s_cbranch_scc1 Lqk_k7_%=\n\t" \
          "s_add_u32 %[cnt], %[cnt], 1\n\t" \
          "s_and_b32 vcc_lo, %[cnt], 3\n\t" \
          "s_cbranch_scc1 Lqk_q7_%=\n\t" \
          "s_getpc_b64 s[48:49]\n\t" \
          "s_branch Lqk_spin%=\n\t" \
          "Lqk_q7_%=:\n\t" \
          "buffer_load_dwordx4 v[164:167], %[ml1], %[rsm], %[mb3] offen sc1\n\t" \
          "s_waitcnt vmcnt(0)\n\t" \
          "v_max3_u32 %[m], v164, v165, v166\n\t" \
          "v_max_u32 %[m], %[m], v167\n\t" \
          "v_cmp_eq_u32 vcc, -1, %[m]\n\t" \
          "s_cbranch_vccnz Lqk_p7_%=\n\t" \
          "s_branch Lqk_k7_%=\n\t" \
          "Lqk_spin%=:\n\t" \
          "s_memrealtime vcc\n\t" \
          "s_waitcnt lgkmcnt(0)\n\t" \
          "s_add_u32 %[ann], vcc_lo, %[ald0]\n\t" \
          "v_mbcnt_lo_u32_b32 %[al], -1, 0\n\t" \
          "v_lshlrev_b32 %[al], 2, %[al]\n\t" \
          "s_and_b32 vcc_lo, %[ann], 0xffff\n\t" \
          "s_or_b32 vcc_lo, vcc_lo, 0x10000\n\t" \
          "v_mov_b32 %[m], vcc_lo\n\t" \
          "s_mov_b64 exec, %[alx]\n\t" \
          "buffer_store_dword %[m], %[al], %[rsm], %[als] offen sc1\n\t" \
          "s_mov_b64 exec, -1\n\t" \
          "buffer_load_dword %[al], off, %[rsc], %[tmo] sc1\n\t" \
          "s_add_u32 s48, s48, 4\n\t" \
          "s_addc_u32 s49, s49, 0\n\t" \
          "s_waitcnt vmcnt(0)\n\t" \
          "v_readfirstlane_b32 vcc_lo, %[al]\n\t" \
          "s_lshr_b32 vcc_hi, %[cnt], 16\n\t" \
          "s_or_b32 vcc_lo, vcc_lo, vcc_hi\n\t" \
          "s_cbranch_scc0 Lqk_back%=\n\t" \
          "v_mov_b32 %[al], 1\n\t" \
          "buffer_store_dword %[al], off, %[rsc], %[tmo] sc1\n\t" \
          "s_mov_b32 %[cnt], -1\n\t" \
          "Lqk_back%=:\n\t" \
          "s_setpc_b64 s[48:49]\n\t" \
          ".p2align 6\n\t" \
          "s_nop 0\n\t" \
          "s_nop 0\n\t" \
          "Lqk_s%=:\n\t" \
          "s_sleep 2\n\t" \
          "Lqk_c%=:\n\t" \
          "s_memrealtime vcc\n\t" \
          "s_waitcnt lgkmcnt(0)\n\t" \
          "s_sub_i32 vcc_lo, %[t], vcc_lo\n\t" \
          "s_cmp_gt_i32 vcc_lo, 16\n\t" \
          "s_cbranch_scc1 Lqk_s%=\n\t" \
          "Lqk_w%=:\n\t" \
          "s_memrealtime s[50:51]\n\t" \
          "s_getpc_b64 s[48:49]\n\t" \
          "Lqk_y%=:\n\t" \
          "s_add_u32 s48, s48, Lqk_z%=-Lqk_y%=\n\t" \
          "s_addc_u32 s49, s49, 0\n\t" \
          "Lqk_v%=:\n\t" \
          "s_memrealtime s[52:53]\n\t" \
          "s_waitcnt lgkmcnt(1)\n\t" \
          "s_sub_i32 vcc_lo, %[t], s50\n\t" \
          "s_cmp_gt_i32 vcc_lo, 3\n\t" \
          "s_cbranch_scc0 Lqk_x%=\n\t" \
          "s_memrealtime s[50:51]\n\t" \
          "s_waitcnt lgkmcnt(1)\n\t" \
          "s_sub_i32 vcc_lo, %[t], s52\n\t" \
          "s_cmp_gt_i32 vcc_lo, 3\n\t" \
          "s_cbranch_scc0 Lqk_x%=\n\t" \
          "s_memrealtime s[52:53]\n\t" \
          "s_waitcnt lgkmcnt(1)\n\t" \
          "s_sub_i32 vcc_lo, %[t], s50\n\t" \
          "s_cmp_gt_i32 vcc_lo, 3\n\t" \
          "s_cbranch_scc0 Lqk_x%=\n\t" \
          "s_memrealtime s[50:51]\n\t" \
          "s_waitcnt lgkmcnt(1)\n\t" \
          "s_sub_i32 vcc_lo, %[t], s52\n\t" \
          "s_cmp_gt_i32 vcc_lo, 3\n\t" \
          "s_cbranch_scc0 Lqk_x%=\n\t" \
          "s_memrealtime s[52:53]\n\t" \
          "s_waitcnt lgkmcnt(1)\n\t" \
          "s_sub_i32 vcc_lo, %[t], s50\n\t" \
          "s_cmp_gt_i32 vcc_lo, 3\n\t" \
          "s_cbranch_scc0 Lqk_x%=\n\t" \
          "s_memrealtime s[50:51]\n\t" \
          "s_waitcnt lgkmcnt(1)\n\t" \
          "s_sub_i32 vcc_lo, %[t], s52\n\t" \
          "s_cmp_gt_i32 vcc_lo, 3\n\t" \
          "s_cbranch_scc0 Lqk_x%=\n\t" \
          "s_memrealtime s[52:53]\n\t" \
          "s_waitcnt lgkmcnt(1)\n\t" \
          "s_sub_i32 vcc_lo, %[t], s50\n\t" \
          "s_cmp_gt_i32 vcc_lo, 3\n\t" \
          "s_cbranch_scc0 Lqk_x%=\n\t" \
          "s_memrealtime s[50:51]\n\t" \
          "s_waitcnt lgkmcnt(1)\n\t" \
          "s_sub_i32 vcc_lo, %[t], s52\n\t" \
          "s_cmp_gt_i32 vcc_lo, 3\n\t" \
          "s_cbranch_scc0 Lqk_x%=\n\t" \
          "s_branch Lqk_v%=\n\t" \
          "Lqk_x%=:\n\t" \
          "s_max_i32 vcc_lo, vcc_lo, 0\n\t" \
          "s_lshl_b32 vcc_lo, vcc_lo, 2\n\t" \
          "s_sub_u32 s48, s48, vcc_lo\n\t" \
          "s_subb_u32 s49, s49, 0\n\t" \
          "s_setpc_b64 s[48:49]\n\t" \
          "s_nop 5\n\t" \
          "s_nop 5\n\t" \
          "s_nop 5\n\t" \
          "Lqk_z%=:\n\t" \
          "s_barrier\n\t" \
          "buffer_load_dwordx4 v[136:139], %[ml0], %[rsm], %[mb0] offen sc1\n\t" \
          "s_nop 9\n\t" \
          "buffer_load_dwordx4 v[140:143], %[ml1], %[rsm], %[mb0] offen sc1\n\t" \
          "s_nop 9\n\t" \
          "buffer_load_dwordx4 v[144:147], %[ml0], %[rsm], %[mb1] offen sc1\n\t" \
          "s_nop 9\n\t" \
          "buffer_load_dwordx4 v[148:151], %[ml1], %[rsm], %[mb1] offen sc1\n\t" \
          "s_nop 9\n\t" \
          "buffer_load_dwordx4 v[152:155], %[ml0], %[rsm], %[mb2] offen sc1\n\t" \
          "s_nop 9\n\t" \
          "buffer_load_dwordx4 v[156:159], %[ml1], %[rsm], %[mb2] offen sc1\n\t" \
          "s_nop 9\n\t" \
          "buffer_load_dwordx4 v[160:163], %[ml0], %[rsm], %[mb3] offen sc1\n\t" \
          "s_nop 9\n\t" \
          "buffer_load_dwordx4 v[164:167], %[ml1], %[rsm], %[mb3] offen sc1\n\t" \
          "s_waitcnt vmcnt(7)\n\t" \
          "Lqk_go%=:\n\t" \
          "v_smfmac_f32_16x16x64_bf16 v[168:171], v[136:139], v[200:207], %[idx]\n\t" \
          "ds_read_b128 v[200:203], %[own] offset:2048\n\t" \
          "v_smfmac_f32_16x16x64_bf16 v[172:175], v[136:139], v[8:15], %[idx]\n\t" \
          "ds_read_b128 v[204:207], %[own] offset:3072\n\t" \
          "v_smfmac_f32_16x16x64_bf16 v[176:179], v[136:139], v[208:215], %[idx]\n\t" \
          "ds_read_b128 v[208:211], %[own] offset:28672\n\t" \
          "v_smfmac_f32_16x16x64_bf16 v[180:183], v[136:139], v[16:23], %[idx]\n\t" \
          "ds_read_b128 v[212:215], %[own] offset:29696\n\t" \
          "s_waitcnt vmcnt(6)\n\t" \
          "v_smfmac_f32_16x16x64_bf16 v[184:187], v[136:139], v[216:223], %[idx]\n\t" \
          "ds_read_b128 v[216:219], %[own] offset:30720\n\t" \
          "v_smfmac_f32_16x16x64_bf16 v[168:171], v[140:143], v[24:31], %[idx]\n\t" \
          "ds_read_b128 v[220:223], %[own] offset:31744\n\t" \
          "v_smfmac_f32_16x16x64_bf16 v[172:175], v[140:143], v[224:231], %[idx]\n\t" \
          "ds_read_b128 v[224:227], %[own] offset:10240\n\t" \
          "v_smfmac_f32_16x16x64_bf16 v[176:179], v[140:143], v[32:39], %[idx]\n\t" \
          "ds_read_b128 v[228:231], %[own] offset:11264\n\t" \
          "s_waitcnt lgkmcnt(6)\n\t" \
          "v_smfmac_f32_16x16x64_bf16 v[180:183], v[140:143], v[200:207], %[idx]\n\t" \
          "ds_read_b128 v[200:203], %[own] offset:24576\n\t" \
          "v_max3_u32 %[m], v136, v137, v138\n\t" \
          "v_smfmac_f32_16x16x64_bf16 v[184:187], v[140:143], v[40:47], %[idx]\n\t" \
          "ds_read_b128 v[204:207], %[own] offset:25600\n\t" \
          "s_waitcnt vmcnt(5) lgkmcnt(6)\n\t" \
          "v_smfmac_f32_16x16x64_bf16 v[168:171], v[144:147], v[208:215], %[idx]\n\t" \
          "ds_read_b128 v[208:211], %[own] offset:26624\n\t" \
          "v_max3_u32 %[m], v138, v139, %[m]\n\t" \
          "v_smfmac_f32_16x16x64_bf16 v[172:175], v[144:147], v[48:55], %[idx]\n\t" \
          "ds_read_b128 v[212:215], %[own] offset:27648\n\t" \
          "s_waitcnt lgkmcnt(6)\n\t" \
          "v_smfmac_f32_16x16x64_bf16 v[176:179], v[144:147], v[216:223], %[idx]\n\t" \
          "ds_read_b128 v[216:219], %[own] offset:20480\n\t" \
          "v_cmp_eq_u32 %[bad1], -1, %[m]\n\t" \
          "v_smfmac_f32_16x16x64_bf16 v[180:183], v[144:147], v[56:63], %[idx]\n\t" \
          "ds_read_b128 v[220:223], %[own] offset:21504\n\t" \
          "s_waitcnt vmcnt(4) lgkmcnt(6)\n\t" \
          "v_smfmac_f32_16x16x64_bf16 v[184:187], v[144:147], v[224:231], %[idx]\n\t" \
          "ds_read_b128 v[224:227], %[own] offset:22528\n\t" \
          "s_and_b64 %[bad1], %[bad1], %[en]\n\t" \
          "v_smfmac_f32_16x16x64_bf16 v[168:171], v[148:151], v[64:71], %[idx]\n\t" \
          "ds_read_b128 v[228:231], %[own] offset:23552\n\t" \
          "s_waitcnt lgkmcnt(6)\n\t" \
          "v_smfmac_f32_16x16x64_bf16 v[172:175], v[148:151], v[200:207], %[idx]\n\t" \
          "ds_read_b128 v[200:203], %[own] offset:12288\n\t" \
          "s_cbranch_scc1 Lqk_fail%=\n\t" \
          "v_smfmac_f32_16x16x64_bf16 v[176:179], v[148:151], v[72:79], %[idx]\n\t" \
          "ds_read_b128 v[204:207], %[own] offset:13312\n\t" \
          "s_waitcnt lgkmcnt(6)\n\t" \
          "v_smfmac_f32_16x16x64_bf16 v[180:183], v[148:151], v[208:215], %[idx]\n\t" \
          "ds_read_b128 v[208:211], %[own] offset:16384\n\t" \
          "v_max3_u32 %[m], v140, v141, %[m]\n\t" \
          "v_smfmac_f32_16x16x64_bf16 v[184:187], v[148:151], v[80:87], %[idx]\n\t" \
          "ds_read_b128 v[212:215], %[own] offset:17408\n\t" \
          "s_waitcnt vmcnt(3) lgkmcnt(6)\n\t" \
          "v_smfmac_f32_16x16x64_bf16 v[168:171], v[152:155], v[216:223], %[idx]\n\t" \
          "ds_read_b128 v[216:219], %[own] offset:18432\n\t" \
          "v_max3_u32 %[m], v142, v143, %[m]\n\t" \
          "v_smfmac_f32_16x16x64_bf16 v[172:175], v[152:155], v[88:95], %[idx]\n\t" \
          "ds_read_b128 v[220:223], %[own] offset:19456\n\t" \
          "s_waitcnt lgkmcnt(6)\n\t" \
          "v_smfmac_f32_16x16x64_bf16 v[176:179], v[152:155], v[224:231], %[idx]\n\t" \
          "ds_read_b128 v[224:227], %[own] offset:14336\n\t" \
          "v_max3_u32 %[m], v144, v145, %[m]\n\t" \
          "v_smfmac_f32_16x16x64_bf16 v[180:183], v[152:155], v[96:103], %[idx]\n\t" \
          "ds_read_b128 v[228:231], %[own] offset:15360\n\t" \
          "s_waitcnt vmcnt(2) lgkmcnt(6)\n\t" \
          "v_smfmac_f32_16x16x64_bf16 v[184:187], v[152:155], v[200:207], %[idx]\n\t" \
          "v_max3_u32 %[m], v146, v147, %[m]\n\t" \
          "v_smfmac_f32_16x16x64_bf16 v[168:171], v[156:159], v[104:111], %[idx]\n\t" \
          "s_waitcnt lgkmcnt(4)\n\t" \
          "v_max3_u32 %[m], v148, v149, %[m]\n\t" \
          "v_smfmac_f32_16x16x64_bf16 v[172:175], v[156:159], v[208:215], %[idx]\n\t" \
          "v_max3_u32 %[m], v150, v151, %[m]\n\t" \
          "v_smfmac_f32_16x16x64_bf16 v[176:179], v[156:159], v[112:119], %[idx]\n\t" \
          "s_waitcnt lgkmcnt(2)\n\t" \
          "v_max3_u32 %[m], v152, v153, %[m]\n\t" \
          "v_smfmac_f32_16x16x64_bf16 v[180:183], v[156:159], v[216:223], %[idx]\n\t" \
          "ds_read_b128 v[216:219], %[own] offset:32768\n\t" \
          "v_max3_u32 %[m], v154, v155, %[m]\n\t" \
          "v_smfmac_f32_16x16x64_bf16 v[184:187], v[156:159], v[120:127], %[idx]\n\t" \
          "ds_read_b128 v[220:223], %[own] offset:33792\n\t" \
          "s_waitcnt vmcnt(1) lgkmcnt(2)\n\t" \
          "v_smfmac_f32_16x16x64_bf16 v[168:171], v[160:163], v[224:231], %[idx]\n\t" \
          "ds_read_b64_tr_b16 v[224:225], %[vxp]\n\t" \
          "v_max3_u32 %[m], v156, v157, %[m]\n\t" \
          "v_smfmac_f32_16x16x64_bf16 v[172:175], v[160:163], v[128:135], %[idx]\n\t" \
          "ds_read_b64_tr_b16 v[226:227], %[vxp] offset:1024\n\t" \
          "v_max3_u32 %[m], v158, v159, %[m]\n\t" \
          "v_smfmac_f32_16x16x64_bf16 v[176:179], v[160:163], a[0:7], %[idx]\n\t" \
          "ds_read_b64_tr_b16 v[228:229], %[vxp] offset:8\n\t" \
          "v_max3_u32 %[m], v160, v161, %[m]\n\t" \
          "v_smfmac_f32_16x16x64_bf16 v[180:183], v[160:163], a[8:15], %[idx]\n\t" \
          "ds_read_b64_tr_b16 v[230:231], %[vxp] offset:1032\n\t" \
          "v_smfmac_f32_16x16x64_bf16 v[184:187], v[160:163], a[16:23], %[idx]\n\t" \
          "s_waitcnt vmcnt(0)\n\t" \
          "v_max3_u32 %[m], v162, v163, %[m]\n\t" \
          "v_smfmac_f32_16x16x64_bf16 v[168:171], v[164:167], a[24:31], %[idx]\n\t" \
          "v_max3_u32 %[m], v164, v165, %[m]\n\t" \
          "v_smfmac_f32_16x16x64_bf16 v[172:175], v[164:167], a[32:39], %[idx]\n\t" \
          "v_max3_u32 %[m], v166, v167, %[m]\n\t" \
          "v_smfmac_f32_16x16x64_bf16 v[176:179], v[164:167], a[40:47], %[idx]\n\t" \
          "v_cvt_pk_bf16_f32 v200, v168, v169\n\t" \
          "v_cvt_pk_bf16_f32 v201, v170, v171\n\t" \
          "v_smfmac_f32_16x16x64_bf16 v[180:183], v[164:167], a[48:55], %[idx]\n\t" \
          "ds_write_b64 %[own], v[200:201] offset:36864\n\t" \
          "v_cvt_pk_bf16_f32 v202, v172, v173\n\t" \
          "v_cvt_pk_bf16_f32 v203, v174, v175\n\t" \
          "v_smfmac_f32_16x16x64_bf16 v[184:187], v[164:167], a[56:63], %[idx]\n\t" \
          "ds_write_b64 %[own], v[202:203] offset:36872\n\t" \
          "v_smfmac_f32_16x16x64_bf16 v[188:191], v[136:139], a[64:71], %[idx]\n\t" \
          "v_cmp_eq_u32 %[bad1], -1, %[m]\n\t" \
          "v_smfmac_f32_16x16x64_bf16 v[188:191], v[140:143], a[72:79], %[idx]\n\t" \
          "buffer_store_dword %[al], %[ho], %[rsm], %[rb] offen sc1\n\t" \
          "v_cvt_pk_bf16_f32 v208, v176, v177\n\t" \
          "v_smfmac_f32_16x16x64_bf16 v[188:191], v[144:147], a[80:87], %[idx]\n\t" \
          "s_and_b64 %[bad1], %[bad1], %[en]\n\t" \
          "s_cbranch_scc1 Lqk_fail%=\n\t" \
          "s_waitcnt lgkmcnt(0)\n\t" \
          "s_barrier\n\t" \
          "v_cvt_pk_bf16_f32 v209, v178, v179\n\t" \
          "v_smfmac_f32_16x16x64_bf16 v[188:191], v[148:151], a[88:95], %[idx]\n\t" \
          "ds_read_b128 v[210:213], %[p1] offset:36864\n\t" \
          "v_cvt_pk_bf16_f32 v214, v180, v181\n\t" \
          "v_smfmac_f32_16x16x64_bf16 v[188:191], v[152:155], a[96:103], %[idx]\n\t" \
          "buffer_store_dword %[al], %[ho], %[rsm], %[rb] offen offset:256 sc1\n\t" \
          "v_cvt_pk_bf16_f32 v215, v182, v183\n\t" \
          "v_smfmac_f32_16x16x64_bf16 v[188:191], v[156:159], a[104:111], %[idx]\n\t" \
          "v_cvt_pk_bf16_f32 v204, v184, v185\n\t" \
          "v_smfmac_f32_16x16x64_bf16 v[188:191], v[160:163], a[112:119], %[idx]\n\t" \
          "v_cvt_pk_bf16_f32 v205, v186, v187\n\t" \
          "v_smfmac_f32_16x16x64_bf16 v[188:191], v[164:167], a[120:127], %[idx]\n\t" \
          "s_waitcnt lgkmcnt(0)\n\t" \
          "v_mfma_f32_16x16x32_bf16 v[168:171], v[208:211], %[jk], 0\n\t" \
          "v_smfmac_f32_16x16x64_bf16 v[192:195], v[136:139], a[128:135], %[idx]\n\t" \
          "v_mfma_f32_16x16x32_bf16 v[172:175], v[212:215], %[jk], 0\n\t" \
          "v_cvt_pk_bf16_f32 v176, v168, v169\n\t" \
          "v_cvt_pk_bf16_f32 v177, v170, v171\n\t" \
          "v_smfmac_f32_16x16x64_bf16 v[196:199], v[136:139], a[136:143], %[idx]\n\t" \
          "ds_write_b64 %[own], v[204:205] offset:37888\n\t" \
          "v_cvt_pk_bf16_f32 v206, v188, v189\n\t" \
          "v_smfmac_f32_16x16x64_bf16 v[192:195], v[140:143], a[144:151], %[idx]\n\t" \
          "v_cvt_pk_bf16_f32 v178, v172, v173\n\t" \
          "v_cvt_pk_bf16_f32 v179, v174, v175\n\t" \
          "v_smfmac_f32_16x16x64_bf16 v[196:199], v[140:143], a[152:159], %[idx]\n\t" \
          "v_cvt_pk_bf16_f32 v207, v190, v191\n\t" \
          "v_mfma_f32_16x16x32_bf16 v[208:211], v[216:219], v[176:179], 0\n\t" \
          "v_mov_b32 v168, %[one]\n\t" \
          "v_mov_b32 v169, %[one]\n\t" \
          "v_mfma_f32_16x16x32_bf16 v[212:215], v[220:223], v[176:179], 0\n\t" \
          "v_mov_b32 v170, %[one]\n\t" \
          "v_mov_b32 v171, %[one]\n\t" \
          "v_smfmac_f32_16x16x64_bf16 v[192:195], v[144:147], a[160:167], %[idx]\n\t" \
          "ds_write_b64 %[own], v[206:207] offset:37896\n\t" \
          "v_smfmac_f32_16x16x64_bf16 v[196:199], v[144:147], a[168:175], %[idx]\n\t" \
          "v_cndmask_b32 v208, v208, v212, %[twin]\n\t" \
          "v_cndmask_b32 v209, v209, v213, %[twin]\n\t" \
          "v_smfmac_f32_16x16x64_bf16 v[192:195], v[148:151], a[176:183], %[idx]\n\t" \
          "ds_write_b64 %[own], v[208:209] offset:38912\n\t" \
          "v_cndmask_b32 v210, v210, v214, %[twin]\n\t" \
          "v_cndmask_b32 v211, v211, v215, %[twin]\n\t" \
          "v_smfmac_f32_16x16x64_bf16 v[196:199], v[148:151], a[184:191], %[idx]\n\t" \
          "ds_write_b64 %[own], v[210:211] offset:38920\n\t" \
          "v_smfmac_f32_16x16x64_bf16 v[192:195], v[152:155], a[192:199], %[idx]\n\t" \
          "v_mul_f32 v212, %[qs], v208\n\t" \
          "v_smfmac_f32_16x16x64_bf16 v[196:199], v[152:155], a[200:207], %[idx]\n\t" \
          "buffer_load_dword v232, %[ho], %[rsm], %[vb] offen sc1\n\t" \
          "v_mul_f32 v213, %[qs], v209\n\t" \
          "v_smfmac_f32_16x16x64_bf16 v[192:195], v[156:159], a[208:215], %[idx]\n\t" \
          "buffer_load_dword v234, %[ho], %[rsm], %[vb] offen offset:256 sc1\n\t" \
          "v_mul_f32 v214, %[qs], v210\n\t" \
          "v_smfmac_f32_16x16x64_bf16 v[196:199], v[156:159], a[216:223], %[idx]\n\t" \
          "s_waitcnt lgkmcnt(0)\n\t" \
          "s_barrier\n\t" \
          "v_mul_f32 v215, %[qs], v211\n\t" \
          "v_smfmac_f32_16x16x64_bf16 v[192:195], v[160:163], a[224:231], %[idx]\n\t" \
          "ds_read_b128 v[138:141], %[p1] offset:37888\n\t" \
          "v_smfmac_f32_16x16x64_bf16 v[196:199], v[160:163], a[232:239], %[idx]\n\t" \
          "ds_read_b128 v[216:219], %[p1] offset:38912\n\t" \
          "v_smfmac_f32_16x16x64_bf16 v[192:195], v[164:167], a[240:247], %[idx]\n\t" \
          "ds_read_b128 v[220:223], %[p2] offset:38912\n\t" \
          "v_smfmac_f32_16x16x64_bf16 v[196:199], v[164:167], a[248:255], %[idx]\n\t" \
          "ds_read_b128 v[200:203], %[p3] offset:38912\n\t" \
          "s_nop 0\n\t" \
          "s_waitcnt lgkmcnt(2)\n\t" \
          "v_cvt_pk_bf16_f32 v136, v192, v193\n\t" \
          "v_cvt_pk_bf16_f32 v137, v194, v195\n\t" \
          "v_fmac_f32 v212, %[qs], v216\n\t" \
          "v_fmac_f32 v213, %[qs], v217\n\t" \
          "v_cvt_pk_bf16_f32 v142, v196, v197\n\t" \
          "v_cvt_pk_bf16_f32 v143, v198, v199\n\t" \
          "v_mfma_f32_16x16x32_bf16 v[144:147], v[136:139], %[jk], 0\n\t" \
          "v_fmac_f32 v214, %[qs], v218\n\t" \
          "v_mfma_f32_16x16x32_bf16 v[148:151], v[140:143], %[jk], 0\n\t" \
          "v_fmac_f32 v215, %[qs], v219\n\t" \
          "s_waitcnt lgkmcnt(0)\n\t" \
          "v_fmac_f32 v212, %[qs], v220\n\t" \
          "v_fmac_f32 v213, %[qs], v221\n\t" \
          "v_cvt_pk_bf16_f32 v152, v144, v145\n\t" \
          "v_cvt_pk_bf16_f32 v153, v146, v147\n\t" \
          "v_fmac_f32 v214, %[qs], v222\n\t" \
          "v_fmac_f32 v215, %[qs], v223\n\t" \
          "v_cvt_pk_bf16_f32 v154, v148, v149\n\t" \
          "v_cvt_pk_bf16_f32 v155, v150, v151\n\t" \
          "v_pk_fma_f32 v[212:213], v[200:201], %[qs2], v[212:213] op_sel_hi:[1,0,1]\n\t" \
          "v_pk_fma_f32 v[214:215], v[202:203], %[qs2], v[214:215] op_sel_hi:[1,0,1]\n\t" \
          "v_mfma_f32_16x16x32_bf16 v[156:159], v[152:155], v[176:179], 0\n\t" \
          "v_exp_f32 v180, v212\n\t" \
          "v_mfma_f32_16x16x32_bf16 v[196:199], v[168:171], v[168:171], 0\n\t" \
          "v_exp_f32 v181, v213\n\t" \
          "v_mfma_f32_16x16x32_bf16 v[196:199], v[168:171], v[168:171], 0\n\t" \
          "ds_write_b64 %[own], v[156:157] offset:39936\n\t" \
          "v_exp_f32 v182, v214\n\t" \
          "v_mov_b32 v137, 0\n\t" \
          "v_exp_f32 v183, v215\n\t" \
          "v_cvt_pk_bf16_f32 v172, v180, v181\n\t" \
          "v_cvt_pk_bf16_f32 v173, v182, v183\n\t" \
          "v_pk_mul_f32 v[160:161], v[156:157], %[qs2] op_sel_hi:[1,0]\n\t" \
          "v_mov_b32_dpp v174, v172 quad_perm:[2,3,0,1] row_mask:0xf bank_mask:0xf\n\t" \
          "v_mov_b32_dpp v175, v173 quad_perm:[2,3,0,1] row_mask:0xf bank_mask:0xf\n\t" \
          "s_nop 0\n\t" \
          "s_waitcnt lgkmcnt(0)\n\t" \
          "v_mfma_f32_16x16x32_bf16 v[184:187], v[172:175], v[168:171], 0\n\t" \
          "s_barrier\n\t" \
          "ds_read_b64 v[144:145], %[p1] offset:39936\n\t" \
          "ds_read_b64 v[146:147], %[p2] offset:39936\n\t" \
          "v_mfma_f32_16x16x32_bf16 v[188:191], v[172:175], v[224:227], 0\n\t" \
          "ds_read_b64 v[148:149], %[p3] offset:39936\n\t" \
          "v_mfma_f32_16x16x32_bf16 v[192:195], v[172:175], v[228:231], 0\n\t" \
          "s_waitcnt lgkmcnt(2)\n\t" \
          "v_fmac_f32 v160, %[qs], v144\n\t" \
          "v_fmac_f32 v161, %[qs], v145\n\t" \
          "s_waitcnt lgkmcnt(1)\n\t" \
          "v_fmac_f32 v160, %[qs], v146\n\t" \
          "v_fmac_f32 v161, %[qs], v147\n\t" \
          "s_waitcnt lgkmcnt(0)\n\t" \
          "v_fmac_f32 v160, %[qs], v148\n\t" \
          "v_fmac_f32 v161, %[qs], v149\n\t" \
          "v_exp_f32 v162, v160\n\t" \
          "v_exp_f32 v163, v161\n\t" \
          "v_max_f32 v182, v160, v161\n\t" \
          "v_cvt_pk_bf16_f32 v136, v162, v163\n\t" \
          "v_cmp_nge_f32 vcc, %[th], v182\n\t" \
          "s_waitcnt vmcnt(0)\n\t" \
          "v_mfma_f32_16x16x16_bf16 v[164:167], v[136:137], v[168:169], v[184:187]\n\t" \
          "v_max_f32 v180, v184, v185\n\t" \
          "v_min_f32 v181, v184, v185\n\t" \
          "v_mfma_f32_16x16x16_bf16 v[188:191], v[136:137], v[232:233], v[188:191]\n\t" \
          "v_cmp_nge_f32 %[b2], %[hi], v180\n\t" \
          "v_cmp_nle_f32 %[b3], %[lo], v181\n\t" \
          "v_mfma_f32_16x16x16_bf16 v[192:195], v[136:137], v[234:235], v[192:195]\n\t" \
          "v_max_u32 v183, v232, v234\n\t" \
          "v_cmp_eq_u32 %[bad], -1, v183\n\t" \
          "v_rcp_f32 v162, v164\n\t" \
          "s_or_b64 %[b2], %[b2], %[b3]\n\t" \
          "v_rcp_f32 v163, v165\n\t" \
          "s_or_b64 %[b2], %[b2], vcc\n\t" \
          "v_pk_mul_f32 v[156:157], v[188:189], v[162:163]\n\t" \
          "s_or_b64 %[bad], %[bad], %[b2]\n\t" \
          "s_cselect_b64 exec, 0, -1\n\t" \
          "v_pk_mul_f32 v[158:159], v[192:193], v[162:163]\n\t" \
          "s_nop 0\n\t" \
          "v_cvt_pk_bf16_f32 v156, v156, v158\n\t" \
          "v_cvt_pk_bf16_f32 v157, v157, v159\n\t" \
          "v_min_u32 v156, -2, v156\n\t" \
          "v_min_u32 v157, -2, v157\n\t" \
          "buffer_store_dword v156, %[ho], %[rsm], %[ob] offen sc1\n\t" \
          "buffer_store_dword v157, %[ho], %[rsm], %[ob] offen offset:256 sc1\n\t" \
          "s_memrealtime %[po]\n\t" \
          "s_mov_b64 exec, -1\n\t" \
          "s_sleep 1\n\t" \
          "buffer_load_dword %[al], %[alo], %[rsm], %[mb] offen sc1\n\t" \
          "s_waitcnt lgkmcnt(0)\n\t"
constexpr int QK_SX = 12, QK_SM = 24, QK_KM = 16, QK_VX = 24, QK_KV = 32768, QK_EX = 36864;
#define VO_BODY \
          "s_mov_b64 %[en], -1\n\t" \
          "s_cmp_eq_u32 %[pw], 0\n\t" \
          "s_cbranch_scc1 Lo_c%=\n\t" \
          "v_mov_b64 v[224:225], 0\n\t" \
          "v_mov_b64 v[226:227], 0\n\t" \
          "v_mov_b64 v[228:229], 0\n\t" \
          "v_mov_b64 v[230:231], 0\n\t" \
          "v_mov_b64 v[232:233], 0\n\t" \
          "v_mov_b64 v[234:235], 0\n\t" \
          "v_mov_b64 v[236:237], 0\n\t" \
          "v_mov_b64 v[238:239], 0\n\t" \
          "v_mov_b64 v[176:177], 0\n\t" \
          "v_mov_b64 v[178:179], 0\n\t" \
          "v_mov_b64 v[180:181], 0\n\t" \
          "v_mov_b64 v[182:183], 0\n\t" \
          "v_mov_b64 v[184:185], 0\n\t" \
          "v_mov_b64 v[186:187], 0\n\t" \
          "v_mov_b64 v[188:189], 0\n\t" \
          "v_mov_b64 v[190:191], 0\n\t" \
          "s_cmp_eq_u32 %[s1], 0\n\t" \
          "s_cbranch_scc1 Lv_n%=\n\t" \
          "s_branch Lo_z%=\n\t" \
          "Lo_f0_%=:\n\t" \
          "buffer_load_dwordx4 v[192:195], %[oo0], %[rsm], %[ob] offen sc1\n\t" \
          "Lo_f1_%=:\n\t" \
          "buffer_load_dwordx4 v[196:199], %[oo1], %[rsm], %[ob] offen sc1\n\t" \
          "Lo_f2_%=:\n\t" \
          "buffer_load_dwordx4 v[200:203], %[oo2], %[rsm], %[ob] offen sc1\n\t" \
          "Lo_f3_%=:\n\t" \
          "buffer_load_dwordx4 v[204:207], %[oo3], %[rsm], %[ob] offen sc1\n\t" \
          "Lo_f4_%=:\n\t" \
          "buffer_load_dwordx4 v[208:211], %[oo4], %[rsm], %[ob] offen sc1\n\t" \
          "Lo_f5_%=:\n\t" \
          "buffer_load_dwordx4 v[212:215], %[oo5], %[rsm], %[ob] offen sc1\n\t" \
          "Lo_f6_%=:\n\t" \
          "buffer_load_dwordx4 v[216:219], %[oo6], %[rsm], %[ob] offen sc1\n\t" \
          "Lo_f7_%=:\n\t" \
          "buffer_load_dwordx4 v[220:223], %[oo7], %[rsm], %[ob] offen sc1\n\t" \
          "v_mov_b32 v242, 0x10000\n\t" \
          "s_mov_b64 exec, %[alx]\n\t" \
          "buffer_store_dword v242, %[l4], %[rsm], %[als] offen sc1\n\t" \
          "s_mov_b64 exec, -1\n\t" \
          "v_mov_b32 v243, %[s1]\n\t" \
          "ds_write_b32 %[fa], v243\n\t" \
          "v_mov_b64 v[224:225], 0\n\t" \
          "v_mov_b64 v[226:227], 0\n\t" \
          "v_mov_b64 v[228:229], 0\n\t" \
          "v_mov_b64 v[230:231], 0\n\t" \
          "v_mov_b64 v[232:233], 0\n\t" \
          "v_mov_b64 v[234:235], 0\n\t" \
          "v_mov_b64 v[236:237], 0\n\t" \
          "v_mov_b64 v[238:239], 0\n\t" \
          "s_nop 1\n\t" \
          "s_mov_b32 %[cnt], 0\n\t" \
          "s_waitcnt vmcnt(7)\n\t" \
          "v_max3_u32 v240, v192, v193, v194\n\t" \
          "v_max_u32 v240, v240, v195\n\t" \
          "v_cmp_eq_u32 vcc, -1, v240\n\t" \
          "s_cbranch_vccnz Lo_p0_%=\n\t" \
          "Lo_k0_%=:\n\t" \
          "v_smfmac_f32_16x16x64_bf16 v[224:227], v[192:195], a[0:7], %[idx]\n\t" \
          "v_smfmac_f32_16x16x64_bf16 v[228:231], v[192:195], a[8:15], %[idx]\n\t" \
          "v_smfmac_f32_16x16x64_bf16 v[232:235], v[192:195], a[16:23], %[idx]\n\t" \
          "v_smfmac_f32_16x16x64_bf16 v[236:239], v[192:195], a[24:31], %[idx]\n\t" \
          "s_waitcnt vmcnt(6)\n\t" \
          "v_max3_u32 v240, v196, v197, v198\n\t" \
          "v_max_u32 v240, v240, v199\n\t" \
          "v_cmp_eq_u32 vcc, -1, v240\n\t" \
          "s_cbranch_vccnz Lo_p1_%=\n\t" \
          "Lo_k1_%=:\n\t" \
          "v_smfmac_f32_16x16x64_bf16 v[224:227], v[196:199], a[32:39], %[idx]\n\t" \
          "v_smfmac_f32_16x16x64_bf16 v[228:231], v[196:199], a[40:47], %[idx]\n\t" \
          "v_smfmac_f32_16x16x64_bf16 v[232:235], v[196:199], a[48:55], %[idx]\n\t" \
          "v_smfmac_f32_16x16x64_bf16 v[236:239], v[196:199], a[56:63], %[idx]\n\t" \
          "s_waitcnt vmcnt(5)\n\t" \
          "v_max3_u32 v240, v200, v201, v202\n\t" \
          "v_max_u32 v240, v240, v203\n\t" \
          "v_cmp_eq_u32 vcc, -1, v240\n\t" \
          "s_cbranch_vccnz Lo_p2_%=\n\t" \
          "Lo_k2_%=:\n\t" \
          "v_smfmac_f32_16x16x64_bf16 v[224:227], v[200:203], a[64:71], %[idx]\n\t" \
          "v_smfmac_f32_16x16x64_bf16 v[228:231], v[200:203], a[72:79], %[idx]\n\t" \
          "v_smfmac_f32_16x16x64_bf16 v[232:235], v[200:203], a[80:87], %[idx]\n\t" \
          "v_smfmac_f32_16x16x64_bf16 v[236:239], v[200:203], a[88:95], %[idx]\n\t" \
          "s_waitcnt vmcnt(4)\n\t" \
          "v_max3_u32 v240, v204, v205, v206\n\t" \
          "v_max_u32 v240, v240, v207\n\t" \
          "v_cmp_eq_u32 vcc, -1, v240\n\t" \
          "s_cbranch_vccnz Lo_p3_%=\n\t" \
          "Lo_k3_%=:\n\t" \
          "v_smfmac_f32_16x16x64_bf16 v[224:227], v[204:207], a[96:103], %[idx]\n\t" \
          "v_smfmac_f32_16x16x64_bf16 v[228:231], v[204:207], a[104:111], %[idx]\n\t" \
          "v_smfmac_f32_16x16x64_bf16 v[232:235], v[204:207], a[112:119], %[idx]\n\t" \
          "v_smfmac_f32_16x16x64_bf16 v[236:239], v[204:207], a[120:127], %[idx]\n\t" \
          "s_waitcnt vmcnt(3)\n\t" \
          "v_max3_u32 v240, v208, v209, v210\n\t" \
          "v_max_u32 v240, v240, v211\n\t" \
          "v_cmp_eq_u32 vcc, -1, v240\n\t" \
          "s_cbranch_vccnz Lo_p4_%=\n\t" \
          "Lo_k4_%=:\n\t" \
          "v_smfmac_f32_16x16x64_bf16 v[224:227], v[208:211], a[128:135], %[idx]\n\t" \
          "v_smfmac_f32_16x16x64_bf16 v[228:231], v[208:211], a[136:143], %[idx]\n\t" \
          "v_smfmac_f32_16x16x64_bf16 v[232:235], v[208:211], a[144:151], %[idx]\n\t" \
          "v_smfmac_f32_16x16x64_bf16 v[236:239], v[208:211], a[152:159], %[idx]\n\t" \
          "s_waitcnt vmcnt(2)\n\t" \
          "v_max3_u32 v240, v212, v213, v214\n\t" \
          "v_max_u32 v240, v240, v215\n\t" \
          "v_cmp_eq_u32 vcc, -1, v240\n\t" \
          "s_cbranch_vccnz Lo_p5_%=\n\t" \
          "Lo_k5_%=:\n\t" \
          "v_smfmac_f32_16x16x64_bf16 v[224:227], v[212:215], a[160:167], %[idx]\n\t" \
          "v_smfmac_f32_16x16x64_bf16 v[228:231], v[212:215], a[168:175], %[idx]\n\t" \
          "v_smfmac_f32_16x16x64_bf16 v[232:235], v[212:215], a[176:183], %[idx]\n\t" \
          "v_smfmac_f32_16x16x64_bf16 v[236:239], v[212:215], a[184:191], %[idx]\n\t" \
          "s_waitcnt vmcnt(1)\n\t" \
          "v_max3_u32 v240, v216, v217, v218\n\t" \
          "v_max_u32 v240, v240, v219\n\t" \
          "v_cmp_eq_u32 vcc, -1, v240\n\t" \
          "s_cbranch_vccnz Lo_p6_%=\n\t" \
          "Lo_k6_%=:\n\t" \
          "v_smfmac_f32_16x16x64_bf16 v[224:227], v[216:219], a[192:199], %[idx]\n\t" \
          "v_smfmac_f32_16x16x64_bf16 v[228:231], v[216:219], a[200:207], %[idx]\n\t" \
          "v_smfmac_f32_16x16x64_bf16 v[232:235], v[216:219], a[208:215], %[idx]\n\t" \
          "v_smfmac_f32_16x16x64_bf16 v[236:239], v[216:219], a[216:223], %[idx]\n\t" \
          "s_waitcnt vmcnt(0)\n\t" \
          "v_max3_u32 v240, v220, v221, v222\n\t" \
          "v_max_u32 v240, v240, v223\n\t" \
          "v_cmp_eq_u32 vcc, -1, v240\n\t" \
          "s_cbranch_vccnz Lo_p7_%=\n\t" \
          "Lo_k7_%=:\n\t" \
          "v_smfmac_f32_16x16x64_bf16 v[224:227], v[220:223], a[224:231], %[idx]\n\t" \
          "v_smfmac_f32_16x16x64_bf16 v[228:231], v[220:223], a[232:239], %[idx]\n\t" \
          "v_smfmac_f32_16x16x64_bf16 v[232:235], v[220:223], a[240:247], %[idx]\n\t" \
          "v_smfmac_f32_16x16x64_bf16 v[236:239], v[220:223], a[248:255], %[idx]\n\t" \
          "s_nop 7\n\t" \
          "v_add_f32 v242, v224, v225\n\t" \
          "v_add_f32 v244, v234, v235\n\t" \
          "s_cmp_lg_u32 %[w], 0\n\t" \
          "s_waitcnt lgkmcnt(0)\n\t" \
          "s_branch Lo_tail%=\n\t" \
          "Lo_p0_%=:\n\t" \
          "s_cmp_lt_i32 %[cnt], 0\n\t" \
          "s_cbranch_scc1 Lo_k0_%=\n\t" \
          "s_add_u32 %[cnt], %[cnt], 1\n\t" \
          "s_and_b32 vcc_lo, %[cnt], 3\n\t" \
          "s_cbranch_scc1 Lo_q0_%=\n\t" \
          "s_getpc_b64 s[48:49]\n\t" \
          "s_branch Lo_spin%=\n\t" \
          "Lo_q0_%=:\n\t" \
          "buffer_load_dwordx4 v[192:195], %[oo0], %[rsm], %[ob] offen sc1\n\t" \
          "s_waitcnt vmcnt(0)\n\t" \
          "v_max3_u32 v240, v192, v193, v194\n\t" \
          "v_max_u32 v240, v240, v195\n\t" \
          "v_cmp_eq_u32 vcc, -1, v240\n\t" \
          "s_cbranch_vccnz Lo_p0_%=\n\t" \
          "buffer_load_dwordx4 v[196:199], %[oo1], %[rsm], %[ob] offen sc1\n\t" \
          "buffer_load_dwordx4 v[200:203], %[oo2], %[rsm], %[ob] offen sc1\n\t" \
          "buffer_load_dwordx4 v[204:207], %[oo3], %[rsm], %[ob] offen sc1\n\t" \
          "buffer_load_dwordx4 v[208:211], %[oo4], %[rsm], %[ob] offen sc1\n\t" \
          "buffer_load_dwordx4 v[212:215], %[oo5], %[rsm], %[ob] offen sc1\n\t" \
          "buffer_load_dwordx4 v[216:219], %[oo6], %[rsm], %[ob] offen sc1\n\t" \
          "buffer_load_dwordx4 v[220:223], %[oo7], %[rsm], %[ob] offen sc1\n\t" \
          "s_branch Lo_k0_%=\n\t" \
          "Lo_p1_%=:\n\t" \
          "s_cmp_lt_i32 %[cnt], 0\n\t" \
          "s_cbranch_scc1 Lo_k1_%=\n\t" \
          "s_add_u32 %[cnt], %[cnt], 1\n\t" \
          "s_and_b32 vcc_lo, %[cnt], 3\n\t" \
          "s_cbranch_scc1 Lo_q1_%=\n\t" \
          "s_getpc_b64 s[48:49]\n\t" \
          "s_branch Lo_spin%=\n\t" \
          "Lo_q1_%=:\n\t" \
          "buffer_load_dwordx4 v[196:199], %[oo1], %[rsm], %[ob] offen sc1\n\t" \
          "s_waitcnt vmcnt(0)\n\t" \
          "v_max3_u32 v240, v196, v197, v198\n\t" \
          "v_max_u32 v240, v240, v199\n\t" \
          "v_cmp_eq_u32 vcc, -1, v240\n\t" \
          "s_cbranch_vccnz Lo_p1_%=\n\t" \
          "buffer_load_dwordx4 v[200:203], %[oo2], %[rsm], %[ob] offen sc1\n\t" \
          "buffer_load_dwordx4 v[204:207], %[oo3], %[rsm], %[ob] offen sc1\n\t" \
          "buffer_load_dwordx4 v[208:211], %[oo4], %[rsm], %[ob] offen sc1\n\t" \
          "buffer_load_dwordx4 v[212:215], %[oo5], %[rsm], %[ob] offen sc1\n\t" \
          "buffer_load_dwordx4 v[216:219], %[oo6], %[rsm], %[ob] offen sc1\n\t" \
          "buffer_load_dwordx4 v[220:223], %[oo7], %[rsm], %[ob] offen sc1\n\t" \
          "s_branch Lo_k1_%=\n\t" \
          "Lo_p2_%=:\n\t" \
          "s_cmp_lt_i32 %[cnt], 0\n\t" \
          "s_cbranch_scc1 Lo_k2_%=\n\t" \
          "s_add_u32 %[cnt], %[cnt], 1\n\t" \
          "s_and_b32 vcc_lo, %[cnt], 3\n\t" \
          "s_cbranch_scc1 Lo_q2_%=\n\t" \
          "s_getpc_b64 s[48:49]\n\t" \
          "s_branch Lo_spin%=\n\t" \
          "Lo_q2_%=:\n\t" \
          "buffer_load_dwordx4 v[200:203], %[oo2], %[rsm], %[ob] offen sc1\n\t" \
          "s_waitcnt vmcnt(0)\n\t" \
          "v_max3_u32 v240, v200, v201, v202\n\t" \
          "v_max_u32 v240, v240, v203\n\t" \
          "v_cmp_eq_u32 vcc, -1, v240\n\t" \
          "s_cbranch_vccnz Lo_p2_%=\n\t" \
          "buffer_load_dwordx4 v[204:207], %[oo3], %[rsm], %[ob] offen sc1\n\t" \
          "buffer_load_dwordx4 v[208:211], %[oo4], %[rsm], %[ob] offen sc1\n\t" \
          "buffer_load_dwordx4 v[212:215], %[oo5], %[rsm], %[ob] offen sc1\n\t" \
          "buffer_load_dwordx4 v[216:219], %[oo6], %[rsm], %[ob] offen sc1\n\t" \
          "buffer_load_dwordx4 v[220:223], %[oo7], %[rsm], %[ob] offen sc1\n\t" \
          "s_branch Lo_k2_%=\n\t" \
          "Lo_p3_%=:\n\t" \
          "s_cmp_lt_i32 %[cnt], 0\n\t" \
          "s_cbranch_scc1 Lo_k3_%=\n\t" \
          "s_add_u32 %[cnt], %[cnt], 1\n\t" \
          "s_and_b32 vcc_lo, %[cnt], 3\n\t" \
          "s_cbranch_scc1 Lo_q3_%=\n\t" \
          "s_getpc_b64 s[48:49]\n\t" \
          "s_branch Lo_spin%=\n\t" \
          "Lo_q3_%=:\n\t" \
          "buffer_load_dwordx4 v[204:207], %[oo3], %[rsm], %[ob] offen sc1\n\t" \
          "s_waitcnt vmcnt(0)\n\t" \
          "v_max3_u32 v240, v204, v205, v206\n\t" \
          "v_max_u32 v240, v240, v207\n\t" \
          "v_cmp_eq_u32 vcc, -1, v240\n\t" \
          "s_cbranch_vccnz Lo_p3_%=\n\t" \
          "buffer_load_dwordx4 v[208:211], %[oo4], %[rsm], %[ob] offen sc1\n\t" \
          "buffer_load_dwordx4 v[212:215], %[oo5], %[rsm], %[ob] offen sc1\n\t" \
          "buffer_load_dwordx4 v[216:219], %[oo6], %[rsm], %[ob] offen sc1\n\t" \
          "buffer_load_dwordx4 v[220:223], %[oo7], %[rsm], %[ob] offen sc1\n\t" \
          "s_branch Lo_k3_%=\n\t" \
          "Lo_p4_%=:\n\t" \
          "s_cmp_lt_i32 %[cnt], 0\n\t" \
          "s_cbranch_scc1 Lo_k4_%=\n\t" \
          "s_add_u32 %[cnt], %[cnt], 1\n\t" \
          "s_and_b32 vcc_lo, %[cnt], 3\n\t" \
          "s_cbranch_scc1 Lo_q4_%=\n\t" \
          "s_getpc_b64 s[48:49]\n\t" \
          "s_branch Lo_spin%=\n\t" \
          "Lo_q4_%=:\n\t" \
          "buffer_load_dwordx4 v[208:211], %[oo4], %[rsm], %[ob] offen sc1\n\t" \
          "s_waitcnt vmcnt(0)\n\t" \
          "v_max3_u32 v240, v208, v209, v210\n\t" \
          "v_max_u32 v240, v240, v211\n\t" \
          "v_cmp_eq_u32 vcc, -1, v240\n\t" \
          "s_cbranch_vccnz Lo_p4_%=\n\t" \
          "buffer_load_dwordx4 v[212:215], %[oo5], %[rsm], %[ob] offen sc1\n\t" \
          "buffer_load_dwordx4 v[216:219], %[oo6], %[rsm], %[ob] offen sc1\n\t" \
          "buffer_load_dwordx4 v[220:223], %[oo7], %[rsm], %[ob] offen sc1\n\t" \
          "s_branch Lo_k4_%=\n\t" \
          "Lo_p5_%=:\n\t" \
          "s_cmp_lt_i32 %[cnt], 0\n\t" \
          "s_cbranch_scc1 Lo_k5_%=\n\t" \
          "s_add_u32 %[cnt], %[cnt], 1\n\t" \
          "s_and_b32 vcc_lo, %[cnt], 3\n\t" \
          "s_cbranch_scc1 Lo_q5_%=\n\t" \
          "s_getpc_b64 s[48:49]\n\t" \
          "s_branch Lo_spin%=\n\t" \
          "Lo_q5_%=:\n\t" \
          "buffer_load_dwordx4 v[212:215], %[oo5], %[rsm], %[ob] offen sc1\n\t" \
          "s_waitcnt vmcnt(0)\n\t" \
          "v_max3_u32 v240, v212, v213, v214\n\t" \
          "v_max_u32 v240, v240, v215\n\t" \
          "v_cmp_eq_u32 vcc, -1, v240\n\t" \
          "s_cbranch_vccnz Lo_p5_%=\n\t" \
          "buffer_load_dwordx4 v[216:219], %[oo6], %[rsm], %[ob] offen sc1\n\t" \
          "buffer_load_dwordx4 v[220:223], %[oo7], %[rsm], %[ob] offen sc1\n\t" \
          "s_branch Lo_k5_%=\n\t" \
          "Lo_p6_%=:\n\t" \
          "s_cmp_lt_i32 %[cnt], 0\n\t" \
          "s_cbranch_scc1 Lo_k6_%=\n\t" \
          "s_add_u32 %[cnt], %[cnt], 1\n\t" \
          "s_and_b32 vcc_lo, %[cnt], 3\n\t" \
          "s_cbranch_scc1 Lo_q6_%=\n\t" \
          "s_getpc_b64 s[48:49]\n\t" \
          "s_branch Lo_spin%=\n\t" \
          "Lo_q6_%=:\n\t" \
          "buffer_load_dwordx4 v[216:219], %[oo6], %[rsm], %[ob] offen sc1\n\t" \
          "s_waitcnt vmcnt(0)\n\t" \
          "v_max3_u32 v240, v216, v217, v218\n\t" \
          "v_max_u32 v240, v240, v219\n\t" \
          "v_cmp_eq_u32 vcc, -1, v240\n\t" \
          "s_cbranch_vccnz Lo_p6_%=\n\t" \
          "buffer_load_dwordx4 v[220:223], %[oo7], %[rsm], %[ob] offen sc1\n\t" \
          "s_branch Lo_k6_%=\n\t" \
          "Lo_p7_%=:\n\t" \
          "s_cmp_lt_i32 %[cnt], 0\n\t" \
          "s_cbranch_scc1 Lo_k7_%=\n\t" \
          "s_add_u32 %[cnt], %[cnt], 1\n\t" \
          "s_and_b32 vcc_lo, %[cnt], 3\n\t" \
          "s_cbranch_scc1 Lo_q7_%=\n\t" \
          "s_getpc_b64 s[48:49]\n\t" \
          "s_branch Lo_spin%=\n\t" \
          "Lo_q7_%=:\n\t" \
          "buffer_load_dwordx4 v[220:223], %[oo7], %[rsm], %[ob] offen sc1\n\t" \
          "s_waitcnt vmcnt(0)\n\t" \
          "v_max3_u32 v240, v220, v221, v222\n\t" \
          "v_max_u32 v240, v240, v223\n\t" \
          "v_cmp_eq_u32 vcc, -1, v240\n\t" \
          "s_cbranch_vccnz Lo_p7_%=\n\t" \
          "s_branch Lo_k7_%=\n\t" \
          "Lo_spin%=:\n\t" \
          "buffer_load_dword v241, off, %[rsc], %[tmo] sc1\n\t" \
          "s_add_u32 s48, s48, 4\n\t" \
          "s_addc_u32 s49, s49, 0\n\t" \
          "s_waitcnt vmcnt(0)\n\t" \
          "v_readfirstlane_b32 vcc_lo, v241\n\t" \
          "s_lshr_b32 vcc_hi, %[cnt], 16\n\t" \
          "s_or_b32 vcc_lo, vcc_lo, vcc_hi\n\t" \
          "s_cbranch_scc0 Lo_back%=\n\t" \
          "v_mov_b32 v241, 1\n\t" \
          "buffer_store_dword v241, off, %[rsc], %[tmo] sc1\n\t" \
          "s_mov_b32 %[cnt], -1\n\t" \
          "Lo_back%=:\n\t" \
          "s_setpc_b64 s[48:49]\n\t" \
          "Lo_fa%=:\n\t" \
          "s_add_u32 %[ta], %[pm], %[shf]\n\t" \
          "s_branch Lo_fb%=\n\t" \
          "Lo_ka%=:\n\t" \
          "v_add_u32 v252, %[ta], v252\n\t" \
          "v_min_u32 v252, -2, v252\n\t" \
          "s_mov_b64 exec, 1\n\t" \
          "buffer_store_dwordx2 v[252:253], off, %[rsm], %[rco] sc1\n\t" \
          "v_mov_b32 v248, %[pm]\n\t" \
          "v_mov_b32 v249, %[t]\n\t" \
          "v_mov_b32 v250, %[fw]\n\t" \
          "v_mov_b32 v251, %[ta]\n\t" \
          "ds_write_b128 %[fa], v[248:251] offset:144\n\t" \
          "s_mov_b64 exec, -1\n\t" \
          "s_branch Lo_kb%=\n\t" \
          "Lv_fail%=:\n\t" \
          "buffer_load_dwordx4 v[192:195], %[ga0], %[rsm], %[mbn] offen sc1\n\t" \
          "buffer_load_dwordx4 v[196:199], %[ga1], %[rsm], %[mbn] offen sc1\n\t" \
          "buffer_load_dwordx4 v[200:203], %[ga2], %[rsm], %[mbn] offen sc1\n\t" \
          "buffer_load_dwordx4 v[204:207], %[ga3], %[rsm], %[mbn] offen sc1\n\t" \
          "s_nop 3\n\t" \
          "v_mov_b64 v[176:177], 0\n\t" \
          "v_mov_b64 v[178:179], 0\n\t" \
          "v_mov_b64 v[180:181], 0\n\t" \
          "v_mov_b64 v[182:183], 0\n\t" \
          "v_mov_b64 v[184:185], 0\n\t" \
          "v_mov_b64 v[186:187], 0\n\t" \
          "v_mov_b64 v[188:189], 0\n\t" \
          "v_mov_b64 v[190:191], 0\n\t" \
          "s_mov_b64 %[en], 0\n\t" \
          "s_mov_b32 %[cnt], 0\n\t" \
          "s_waitcnt vmcnt(3)\n\t" \
          "v_max3_u32 v240, v192, v193, v194\n\t" \
          "v_max_u32 v240, v240, v195\n\t" \
          "v_cmp_eq_u32 vcc, -1, v240\n\t" \
          "s_cbranch_vccnz Lv_p0_%=\n\t" \
          "Lv_k0_%=:\n\t" \
          "s_waitcnt vmcnt(2)\n\t" \
          "v_max3_u32 v240, v196, v197, v198\n\t" \
          "v_max_u32 v240, v240, v199\n\t" \
          "v_cmp_eq_u32 vcc, -1, v240\n\t" \
          "s_cbranch_vccnz Lv_p1_%=\n\t" \
          "Lv_k1_%=:\n\t" \
          "s_waitcnt vmcnt(1)\n\t" \
          "v_max3_u32 v240, v200, v201, v202\n\t" \
          "v_max_u32 v240, v240, v203\n\t" \
          "v_cmp_eq_u32 vcc, -1, v240\n\t" \
          "s_cbranch_vccnz Lv_p2_%=\n\t" \
          "Lv_k2_%=:\n\t" \
          "s_waitcnt vmcnt(0)\n\t" \
          "v_max3_u32 v240, v204, v205, v206\n\t" \
          "v_max_u32 v240, v240, v207\n\t" \
          "v_cmp_eq_u32 vcc, -1, v240\n\t" \
          "s_cbranch_vccnz Lv_p3_%=\n\t" \
          "Lv_k3_%=:\n\t" \
          "s_branch Lv_go%=\n\t" \
          "Lv_p0_%=:\n\t" \
          "s_cmp_lt_i32 %[cnt], 0\n\t" \
          "s_cbranch_scc1 Lv_k0_%=\n\t" \
          "s_add_u32 %[cnt], %[cnt], 1\n\t" \
          "s_and_b32 vcc_lo, %[cnt], 3\n\t" \
          "s_cbranch_scc1 Lv_q0_%=\n\t" \
          "s_getpc_b64 s[48:49]\n\t" \
          "s_branch Lv_spin%=\n\t" \
          "Lv_q0_%=:\n\t" \
          "buffer_load_dwordx4 v[192:195], %[ga0], %[rsm], %[mbn] offen sc1\n\t" \
          "s_waitcnt vmcnt(0)\n\t" \
          "v_max3_u32 v240, v192, v193, v194\n\t" \
          "v_max_u32 v240, v240, v195\n\t" \
          "v_cmp_eq_u32 vcc, -1, v240\n\t" \
          "s_cbranch_vccnz Lv_p0_%=\n\t" \
          "buffer_load_dwordx4 v[196:199], %[ga1], %[rsm], %[mbn] offen sc1\n\t" \
          "buffer_load_dwordx4 v[200:203], %[ga2], %[rsm], %[mbn] offen sc1\n\t" \
          "buffer_load_dwordx4 v[204:207], %[ga3], %[rsm], %[mbn] offen sc1\n\t" \
          "s_branch Lv_k0_%=\n\t" \
          "Lv_p1_%=:\n\t" \
          "s_cmp_lt_i32 %[cnt], 0\n\t" \
          "s_cbranch_scc1 Lv_k1_%=\n\t" \
          "s_add_u32 %[cnt], %[cnt], 1\n\t" \
          "s_and_b32 vcc_lo, %[cnt], 3\n\t" \
          "s_cbranch_scc1 Lv_q1_%=\n\t" \
          "s_getpc_b64 s[48:49]\n\t" \
          "s_branch Lv_spin%=\n\t" \
          "Lv_q1_%=:\n\t" \
          "buffer_load_dwordx4 v[196:199], %[ga1], %[rsm], %[mbn] offen sc1\n\t" \
          "s_waitcnt vmcnt(0)\n\t" \
          "v_max3_u32 v240, v196, v197, v198\n\t" \
          "v_max_u32 v240, v240, v199\n\t" \
          "v_cmp_eq_u32 vcc, -1, v240\n\t" \
          "s_cbranch_vccnz Lv_p1_%=\n\t" \
          "buffer_load_dwordx4 v[200:203], %[ga2], %[rsm], %[mbn] offen sc1\n\t" \
          "buffer_load_dwordx4 v[204:207], %[ga3], %[rsm], %[mbn] offen sc1\n\t" \
          "s_branch Lv_k1_%=\n\t" \
          "Lv_p2_%=:\n\t" \
          "s_cmp_lt_i32 %[cnt], 0\n\t" \
          "s_cbranch_scc1 Lv_k2_%=\n\t" \
          "s_add_u32 %[cnt], %[cnt], 1\n\t" \
          "s_and_b32 vcc_lo, %[cnt], 3\n\t" \
          "s_cbranch_scc1 Lv_q2_%=\n\t" \
          "s_getpc_b64 s[48:49]\n\t" \
          "s_branch Lv_spin%=\n\t" \
          "Lv_q2_%=:\n\t" \
          "buffer_load_dwordx4 v[200:203], %[ga2], %[rsm], %[mbn] offen sc1\n\t" \
          "s_waitcnt vmcnt(0)\n\t" \
          "v_max3_u32 v240, v200, v201, v202\n\t" \
          "v_max_u32 v240, v240, v203\n\t" \
          "v_cmp_eq_u32 vcc, -1, v240\n\t" \
          "s_cbranch_vccnz Lv_p2_%=\n\t" \
          "buffer_load_dwordx4 v[204:207], %[ga3], %[rsm], %[mbn] offen sc1\n\t" \
          "s_branch Lv_k2_%=\n\t" \
          "Lv_p3_%=:\n\t" \
          "s_cmp_lt_i32 %[cnt], 0\n\t" \
          "s_cbranch_scc1 Lv_k3_%=\n\t" \
          "s_add_u32 %[cnt], %[cnt], 1\n\t" \
          "s_and_b32 vcc_lo, %[cnt], 3\n\t" \
          "s_cbranch_scc1 Lv_q3_%=\n\t" \
          "s_getpc_b64 s[48:49]\n\t" \
          "s_branch Lv_spin%=\n\t" \
          "Lv_q3_%=:\n\t" \
          "buffer_load_dwordx4 v[204:207], %[ga3], %[rsm], %[mbn] offen sc1\n\t" \
          "s_waitcnt vmcnt(0)\n\t" \
          "v_max3_u32 v240, v204, v205, v206\n\t" \
          "v_max_u32 v240, v240, v207\n\t" \
          "v_cmp_eq_u32 vcc, -1, v240\n\t" \
          "s_cbranch_vccnz Lv_p3_%=\n\t" \
          "s_branch Lv_k3_%=\n\t" \
          "Lv_spin%=:\n\t" \
          "buffer_load_dword v241, off, %[rsc], %[tmo] sc1\n\t" \
          "s_add_u32 s48, s48, 4\n\t" \
          "s_addc_u32 s49, s49, 0\n\t" \
          "s_waitcnt vmcnt(0)\n\t" \
          "v_readfirstlane_b32 vcc_lo, v241\n\t" \
          "s_lshr_b32 vcc_hi, %[cnt], 16\n\t" \
          "s_or_b32 vcc_lo, vcc_lo, vcc_hi\n\t" \
          "s_cbranch_scc0 Lv_back%=\n\t" \
          "v_mov_b32 v241, 1\n\t" \
          "buffer_store_dword v241, off, %[rsc], %[tmo] sc1\n\t" \
          "s_mov_b32 %[cnt], -1\n\t" \
          "Lv_back%=:\n\t" \
          "s_setpc_b64 s[48:49]\n\t" \
          ".p2align 6\n\t" \
          "s_nop 0\n\t" \
          "s_nop 0\n\t" \
          "s_nop 0\n\t" \
          "Lo_al%=:\n\t" \
          "v_subrev_u32 v252, %[t], v255\n\t" \
          "v_bfe_i32 v252, v252, 0, 16\n\t" \
          "v_cndmask_b32 v252, 0, v252, vcc\n\t" \
          "v_cmp_gt_u32 vcc, 1000, v252\n\t" \
          "s_nop 1\n\t" \
          "v_cndmask_b32 v252, 0, v252, vcc\n\t" \
          "s_nop 1\n\t" \
          "v_max_i32_dpp v252, v252, v252 row_ror:8 row_mask:0xf bank_mask:0xf\n\t" \
          "s_nop 1\n\t" \
          "v_max_i32_dpp v252, v252, v252 row_ror:4 row_mask:0xf bank_mask:0xf\n\t" \
          "s_nop 1\n\t" \
          "v_max_i32_dpp v252, v252, v252 row_ror:2 row_mask:0xf bank_mask:0xf\n\t" \
          "s_nop 1\n\t" \
          "v_max_i32_dpp v252, v252, v252 row_ror:1 row_mask:0xf bank_mask:0xf\n\t" \
          "s_nop 0\n\t" \
          "v_readfirstlane_b32 vcc_lo, v252\n\t" \
          "s_add_u32 %[t], %[t], vcc_lo\n\t" \
          "s_memrealtime vcc\n\t" \
          "s_waitcnt lgkmcnt(0)\n\t" \
          "s_sub_i32 vcc_lo, %[t], vcc_lo\n\t" \
          "s_cmp_gt_i32 vcc_lo, %[lk]\n\t" \
          "s_cbranch_scc0 Lo_w%=\n\t" \
          "Lo_s%=:\n\t" \
          "s_sleep 1\n\t" \
          "Lo_c%=:\n\t" \
          "s_memrealtime vcc\n\t" \
          "s_waitcnt lgkmcnt(0)\n\t" \
          "s_sub_i32 vcc_lo, %[t], vcc_lo\n\t" \
          "s_cmp_gt_i32 vcc_lo, %[lk]\n\t" \
          "s_cbranch_scc1 Lo_s%=\n\t" \
          "buffer_load_dword v255, %[al1], %[rsm], %[mb] offen sc1\n\t" \
          "v_mov_b64 v[224:225], 0\n\t" \
          "v_mov_b64 v[226:227], 0\n\t" \
          "v_mov_b64 v[228:229], 0\n\t" \
          "v_mov_b64 v[230:231], 0\n\t" \
          "v_mov_b64 v[232:233], 0\n\t" \
          "v_mov_b64 v[234:235], 0\n\t" \
          "v_mov_b64 v[236:237], 0\n\t" \
          "v_mov_b64 v[238:239], 0\n\t" \
          "v_mov_b64 v[176:177], 0\n\t" \
          "v_mov_b64 v[178:179], 0\n\t" \
          "v_mov_b64 v[180:181], 0\n\t" \
          "v_mov_b64 v[182:183], 0\n\t" \
          "v_mov_b64 v[184:185], 0\n\t" \
          "v_mov_b64 v[186:187], 0\n\t" \
          "v_mov_b64 v[188:189], 0\n\t" \
          "v_mov_b64 v[190:191], 0\n\t" \
          "s_waitcnt vmcnt(0)\n\t" \
          "v_lshrrev_b32 v252, 16, v255\n\t" \
          "v_cmp_eq_u32 vcc, 1, v252\n\t" \
          "s_cbranch_vccnz Lo_al%=\n\t" \
          "Lo_w%=:\n\t" \
          "s_memrealtime s[50:51]\n\t" \
          "s_getpc_b64 s[48:49]\n\t" \
          "Lo_y%=:\n\t" \
          "s_add_u32 s48, s48, Lo_z%=-Lo_y%=\n\t" \
          "s_addc_u32 s49, s49, 0\n\t" \
          "Lo_v%=:\n\t" \
          "s_memrealtime s[52:53]\n\t" \
          "s_waitcnt lgkmcnt(1)\n\t" \
          "s_sub_i32 vcc_lo, %[t], s50\n\t" \
          "s_cmp_gt_i32 vcc_lo, 3\n\t" \
          "s_cbranch_scc0 Lo_x%=\n\t" \
          "s_memrealtime s[50:51]\n\t" \
          "s_waitcnt lgkmcnt(1)\n\t" \
          "s_sub_i32 vcc_lo, %[t], s52\n\t" \
          "s_cmp_gt_i32 vcc_lo, 3\n\t" \
          "s_cbranch_scc0 Lo_x%=\n\t" \
          "s_memrealtime s[52:53]\n\t" \
          "s_waitcnt lgkmcnt(1)\n\t" \
          "s_sub_i32 vcc_lo, %[t], s50\n\t" \
          "s_cmp_gt_i32 vcc_lo, 3\n\t" \
          "s_cbranch_scc0 Lo_x%=\n\t" \
          "s_memrealtime s[50:51]\n\t" \
          "s_waitcnt lgkmcnt(1)\n\t" \
          "s_sub_i32 vcc_lo, %[t], s52\n\t" \
          "s_cmp_gt_i32 vcc_lo, 3\n\t" \
          "s_cbranch_scc0 Lo_x%=\n\t" \
          "s_memrealtime s[52:53]\n\t" \
          "s_waitcnt lgkmcnt(1)\n\t" \
          "s_sub_i32 vcc_lo, %[t], s50\n\t" \
          "s_cmp_gt_i32 vcc_lo, 3\n\t" \
          "s_cbranch_scc0 Lo_x%=\n\t" \
          "s_memrealtime s[50:51]\n\t" \
          "s_waitcnt lgkmcnt(1)\n\t" \
          "s_sub_i32 vcc_lo, %[t], s52\n\t" \
          "s_cmp_gt_i32 vcc_lo, 3\n\t" \
          "s_cbranch_scc0 Lo_x%=\n\t" \
          "s_memrealtime s[52:53]\n\t" \
          "s_waitcnt lgkmcnt(1)\n\t" \
          "s_sub_i32 vcc_lo, %[t], s50\n\t" \
          "s_cmp_gt_i32 vcc_lo, 3\n\t" \
          "s_cbranch_scc0 Lo_x%=\n\t" \
          "s_memrealtime s[50:51]\n\t" \
          "s_waitcnt lgkmcnt(1)\n\t" \
          "s_sub_i32 vcc_lo, %[t], s52\n\t" \
          "s_cmp_gt_i32 vcc_lo, 3\n\t" \
          "s_cbranch_scc0 Lo_x%=\n\t" \
          "s_branch Lo_v%=\n\t" \
          "Lo_x%=:\n\t" \
          "s_max_i32 vcc_lo, vcc_lo, 0\n\t" \
          "s_lshl_b32 vcc_lo, vcc_lo, 2\n\t" \
          "s_sub_u32 s48, s48, vcc_lo\n\t" \
          "s_subb_u32 s49, s49, 0\n\t" \
          "s_setpc_b64 s[48:49]\n\t" \
          "s_nop 5\n\t" \
          "s_nop 5\n\t" \
          "s_nop 5\n\t" \
          "Lo_z%=:\n\t" \
          "s_barrier\n\t" \
          VO_ST0 \
          "buffer_load_dwordx4 v[192:195], %[oo0], %[rsm], %[ob] offen sc1\n\t" \
          "s_nop 9\n\t" \
          "buffer_load_dwordx4 v[196:199], %[oo1], %[rsm], %[ob] offen sc1\n\t" \
          "s_nop 9\n\t" \
          "buffer_load_dwordx4 v[200:203], %[oo2], %[rsm], %[ob] offen sc1\n\t" \
          "s_nop 9\n\t" \
          "buffer_load_dwordx4 v[204:207], %[oo3], %[rsm], %[ob] offen sc1\n\t" \
          "s_nop 9\n\t" \
          "buffer_load_dwordx4 v[208:211], %[oo4], %[rsm], %[ob] offen sc1\n\t" \
          "s_nop 9\n\t" \
          "buffer_load_dwordx4 v[212:215], %[oo5], %[rsm], %[ob] offen sc1\n\t" \
          "s_nop 9\n\t" \
          "buffer_load_dwordx4 v[216:219], %[oo6], %[rsm], %[ob] offen sc1\n\t" \
          "s_nop 9\n\t" \
          "buffer_load_dwordx4 v[220:223], %[oo7], %[rsm], %[ob] offen sc1\n\t" \
          "s_waitcnt vmcnt(7)\n\t" \
          "v_smfmac_f32_16x16x64_bf16 v[224:227], v[192:195], a[0:7], %[idx]\n\t" \
          "v_max3_u32 v240, v192, v193, v194\n\t" \
          "v_smfmac_f32_16x16x64_bf16 v[228:231], v[192:195], a[8:15], %[idx]\n\t" \
          "v_max3_u32 v240, v194, v195, v240\n\t" \
          "v_smfmac_f32_16x16x64_bf16 v[232:235], v[192:195], a[16:23], %[idx]\n\t" \
          "v_cmp_eq_u32 vcc, -1, v240\n\t" \
          "v_smfmac_f32_16x16x64_bf16 v[236:239], v[192:195], a[24:31], %[idx]\n\t" \
          "s_waitcnt vmcnt(6)\n\t" \
          "v_smfmac_f32_16x16x64_bf16 v[224:227], v[196:199], a[32:39], %[idx]\n\t" \
          "s_cbranch_vccnz Lo_f0_%=\n\t" \
          "v_max3_u32 v240, v196, v197, v198\n\t" \
          "v_smfmac_f32_16x16x64_bf16 v[228:231], v[196:199], a[40:47], %[idx]\n\t" \
          "v_max3_u32 v240, v198, v199, v240\n\t" \
          "v_smfmac_f32_16x16x64_bf16 v[232:235], v[196:199], a[48:55], %[idx]\n\t" \
          "v_cmp_eq_u32 vcc, -1, v240\n\t" \
          "v_smfmac_f32_16x16x64_bf16 v[236:239], v[196:199], a[56:63], %[idx]\n\t" \
          "s_waitcnt vmcnt(5)\n\t" \
          "v_smfmac_f32_16x16x64_bf16 v[224:227], v[200:203], a[64:71], %[idx]\n\t" \
          "s_cbranch_vccnz Lo_f1_%=\n\t" \
          "v_max3_u32 v240, v200, v201, v202\n\t" \
          "v_smfmac_f32_16x16x64_bf16 v[228:231], v[200:203], a[72:79], %[idx]\n\t" \
          "v_max3_u32 v240, v202, v203, v240\n\t" \
          "v_smfmac_f32_16x16x64_bf16 v[232:235], v[200:203], a[80:87], %[idx]\n\t" \
          "v_cmp_eq_u32 vcc, -1, v240\n\t" \
          "v_smfmac_f32_16x16x64_bf16 v[236:239], v[200:203], a[88:95], %[idx]\n\t" \
          "s_waitcnt vmcnt(4)\n\t" \
          "v_smfmac_f32_16x16x64_bf16 v[224:227], v[204:207], a[96:103], %[idx]\n\t" \
          "s_cbranch_vccnz Lo_f2_%=\n\t" \
          "v_max3_u32 v240, v204, v205, v206\n\t" \
          "v_smfmac_f32_16x16x64_bf16 v[228:231], v[204:207], a[104:111], %[idx]\n\t" \
          "v_max3_u32 v240, v206, v207, v240\n\t" \
          "v_smfmac_f32_16x16x64_bf16 v[232:235], v[204:207], a[112:119], %[idx]\n\t" \
          "v_cmp_eq_u32 vcc, -1, v240\n\t" \
          "v_smfmac_f32_16x16x64_bf16 v[236:239], v[204:207], a[120:127], %[idx]\n\t" \
          "s_waitcnt vmcnt(3)\n\t" \
          "v_smfmac_f32_16x16x64_bf16 v[224:227], v[208:211], a[128:135], %[idx]\n\t" \
          "s_cbranch_vccnz Lo_f3_%=\n\t" \
          "v_max3_u32 v240, v208, v209, v210\n\t" \
          "v_smfmac_f32_16x16x64_bf16 v[228:231], v[208:211], a[136:143], %[idx]\n\t" \
          "v_max3_u32 v240, v210, v211, v240\n\t" \
          "v_smfmac_f32_16x16x64_bf16 v[232:235], v[208:211], a[144:151], %[idx]\n\t" \
          "v_cmp_eq_u32 vcc, -1, v240\n\t" \
          "v_smfmac_f32_16x16x64_bf16 v[236:239], v[208:211], a[152:159], %[idx]\n\t" \
          "s_waitcnt vmcnt(2)\n\t" \
          "v_smfmac_f32_16x16x64_bf16 v[224:227], v[212:215], a[160:167], %[idx]\n\t" \
          "s_cbranch_vccnz Lo_f4_%=\n\t" \
          "v_max3_u32 v240, v212, v213, v214\n\t" \
          "v_smfmac_f32_16x16x64_bf16 v[228:231], v[212:215], a[168:175], %[idx]\n\t" \
          "v_max3_u32 v240, v214, v215, v240\n\t" \
          "v_smfmac_f32_16x16x64_bf16 v[232:235], v[212:215], a[176:183], %[idx]\n\t" \
          "v_cmp_eq_u32 vcc, -1, v240\n\t" \
          "v_smfmac_f32_16x16x64_bf16 v[236:239], v[212:215], a[184:191], %[idx]\n\t" \
          "s_waitcnt vmcnt(1)\n\t" \
          "v_smfmac_f32_16x16x64_bf16 v[224:227], v[216:219], a[192:199], %[idx]\n\t" \
          "s_cbranch_vccnz Lo_f5_%=\n\t" \
          "v_max3_u32 v240, v216, v217, v218\n\t" \
          "v_smfmac_f32_16x16x64_bf16 v[228:231], v[216:219], a[200:207], %[idx]\n\t" \
          "v_max3_u32 v240, v218, v219, v240\n\t" \
          "v_smfmac_f32_16x16x64_bf16 v[232:235], v[216:219], a[208:215], %[idx]\n\t" \
          "v_cmp_eq_u32 vcc, -1, v240\n\t" \
          "v_smfmac_f32_16x16x64_bf16 v[236:239], v[216:219], a[216:223], %[idx]\n\t" \
          "s_waitcnt vmcnt(0)\n\t" \
          "v_smfmac_f32_16x16x64_bf16 v[224:227], v[220:223], a[224:231], %[idx]\n\t" \
          "s_cbranch_vccnz Lo_f6_%=\n\t" \
          "v_max3_u32 v240, v220, v221, v222\n\t" \
          "v_smfmac_f32_16x16x64_bf16 v[232:235], v[220:223], a[240:247], %[idx]\n\t" \
          "v_max3_u32 v240, v222, v223, v240\n\t" \
          "v_smfmac_f32_16x16x64_bf16 v[228:231], v[220:223], a[232:239], %[idx]\n\t" \
          "v_cmp_eq_u32 vcc, -1, v240\n\t" \
          "v_smfmac_f32_16x16x64_bf16 v[236:239], v[220:223], a[248:255], %[idx]\n\t" \
          VO_ST1 \
          "s_cmp_lg_u32 %[w], 0\n\t" \
          "v_add_f32 v242, v224, v225\n\t" \
          "v_add_f32 v244, v234, v235\n\t" \
          "s_cbranch_vccnz Lo_f7_%=\n\t" \
          "Lo_tail%=:\n\t" \
          "v_add_f32 v243, v228, v229\n\t" \
          "v_add_f32 v242, v242, v244\n\t" \
          "s_nop 0\n\t" \
          "v_add_f32 v243, v243, v238\n\t" \
          "s_cbranch_scc0 Lo_w0%=\n\t" \
          "v_add_f32 v243, v243, v239\n\t" \
          "ds_write_b64 %[rw], v[242:243]\n\t" \
          "s_waitcnt lgkmcnt(0)\n\t" \
          "s_barrier\n\t" \
          "s_cmp_eq_u32 %[w], 3\n\t" \
          "s_cbranch_scc0 Lv_z%=\n\t" \
          "s_sleep 1\n\t" \
          "s_branch Lo_end%=\n\t" \
          "Lo_w0%=:\n\t" \
          "v_add_f32 v243, v243, v239\n\t" \
          "s_barrier\n\t" \
          "ds_read_b64 v[244:245], %[rw] offset:512\n\t" \
          "ds_read_b64 v[246:247], %[rw] offset:1024\n\t" \
          "ds_read_b64 v[248:249], %[rw] offset:1536\n\t" \
          "s_waitcnt lgkmcnt(2)\n\t" \
          "v_pk_add_f32 v[242:243], v[242:243], v[244:245]\n\t" \
          "s_waitcnt lgkmcnt(1)\n\t" \
          "v_pk_add_f32 v[242:243], v[242:243], v[246:247]\n\t" \
          "s_waitcnt lgkmcnt(0)\n\t" \
          "v_pk_add_f32 v[242:243], v[242:243], v[248:249]\n\t" \
          "s_nop 0\n\t" \
          "v_cvt_pk_bf16_f32 v242, v242, v243\n\t" \
          "v_min_u32 v242, -2, v242\n\t" \
          "buffer_store_dword v242, %[mo], %[rsm], %[mbn] offen sc1\n\t" \
          VO_ST3 \
          "s_branch Lv_z%=\n\t" \
          "Lo_end%=:\n\t" \
          VO_ST3 \
          "s_memrealtime vcc\n\t" \
          "ds_read_b32 v241, %[fa]\n\t" \
          "ds_read_b64 v[252:253], %[fa] offset:128\n\t" \
          "s_add_u32 %[ta], %[t], %[d2]\n\t" \
          "s_waitcnt lgkmcnt(0)\n\t" \
          "s_mov_b32 %[pm], vcc_lo\n\t" \
          "v_readfirstlane_b32 %[fw], v241\n\t" \
          "s_add_u32 vcc_lo, vcc_lo, %[mn]\n\t" \
          "s_sub_i32 vcc_hi, vcc_lo, %[ta]\n\t" \
          "s_cmp_gt_i32 vcc_hi, 0\n\t" \
          "s_cselect_b32 %[ta], vcc_lo, %[ta]\n\t" \
          "s_cmp_eq_u32 %[fw], %[s1]\n\t" \
          "s_cbranch_scc1 Lo_fa%=\n\t" \
          "Lo_fb%=:\n\t" \
          "s_cmp_eq_u32 %[c], 0\n\t" \
          "s_cbranch_scc1 Lo_ka%=\n\t" \
          "Lo_kb%=:\n\t" \
          "s_add_u32 %[cnt], %[ta], %[dv]\n\t" \
          "Lv_w%=:\n\t" \
          "s_memrealtime s[50:51]\n\t" \
          "s_getpc_b64 s[48:49]\n\t" \
          "Lv_y%=:\n\t" \
          "s_add_u32 s48, s48, Lv_z%=-Lv_y%=\n\t" \
          "s_addc_u32 s49, s49, 0\n\t" \
          "Lv_v%=:\n\t" \
          "s_memrealtime s[52:53]\n\t" \
          "s_waitcnt lgkmcnt(1)\n\t" \
          "s_sub_i32 vcc_lo, %[cnt], s50\n\t" \
          "s_cmp_gt_i32 vcc_lo, 3\n\t" \
          "s_cbranch_scc0 Lv_x%=\n\t" \
          "s_memrealtime s[50:51]\n\t" \
          "s_waitcnt lgkmcnt(1)\n\t" \
          "s_sub_i32 vcc_lo, %[cnt], s52\n\t" \
          "s_cmp_gt_i32 vcc_lo, 3\n\t" \
          "s_cbranch_scc0 Lv_x%=\n\t" \
          "s_memrealtime s[52:53]\n\t" \
          "s_waitcnt lgkmcnt(1)\n\t" \
          "s_sub_i32 vcc_lo, %[cnt], s50\n\t" \
          "s_cmp_gt_i32 vcc_lo, 3\n\t" \
          "s_cbranch_scc0 Lv_x%=\n\t" \
          "s_memrealtime s[50:51]\n\t" \
          "s_waitcnt lgkmcnt(1)\n\t" \
          "s_sub_i32 vcc_lo, %[cnt], s52\n\t" \
          "s_cmp_gt_i32 vcc_lo, 3\n\t" \
          "s_cbranch_scc0 Lv_x%=\n\t" \
          "s_memrealtime s[52:53]\n\t" \
          "s_waitcnt lgkmcnt(1)\n\t" \
          "s_sub_i32 vcc_lo, %[cnt], s50\n\t" \
          "s_cmp_gt_i32 vcc_lo, 3\n\t" \
          "s_cbranch_scc0 Lv_x%=\n\t" \
          "s_memrealtime s[50:51]\n\t" \
          "s_waitcnt lgkmcnt(1)\n\t" \
          "s_sub_i32 vcc_lo, %[cnt], s52\n\t" \
          "s_cmp_gt_i32 vcc_lo, 3\n\t" \
          "s_cbranch_scc0 Lv_x%=\n\t" \
          "s_memrealtime s[52:53]\n\t" \
          "s_waitcnt lgkmcnt(1)\n\t" \
          "s_sub_i32 vcc_lo, %[cnt], s50\n\t" \
          "s_cmp_gt_i32 vcc_lo, 3\n\t" \
          "s_cbranch_scc0 Lv_x%=\n\t" \
          "s_memrealtime s[50:51]\n\t" \
          "s_waitcnt lgkmcnt(1)\n\t" \
          "s_sub_i32 vcc_lo, %[cnt], s52\n\t" \
          "s_cmp_gt_i32 vcc_lo, 3\n\t" \
          "s_cbranch_scc0 Lv_x%=\n\t" \
          "s_branch Lv_v%=\n\t" \
          "Lv_x%=:\n\t" \
          "s_max_i32 vcc_lo, vcc_lo, 0\n\t" \
          "s_lshl_b32 vcc_lo, vcc_lo, 2\n\t" \
          "s_sub_u32 s48, s48, vcc_lo\n\t" \
          "s_subb_u32 s49, s49, 0\n\t" \
          "s_setpc_b64 s[48:49]\n\t" \
          "s_nop 5\n\t" \
          "s_nop 5\n\t" \
          "s_nop 5\n\t" \
          "Lv_z%=:\n\t" \
          "s_barrier\n\t" \
          "Lv_n%=:\n\t" \
          "buffer_load_dwordx4 v[192:195], %[ga0], %[rsm], %[mbn] offen sc1\n\t" \
          "s_nop 15\n\t" \
          "s_nop 5\n\t" \
          "buffer_load_dwordx4 v[196:199], %[ga1], %[rsm], %[mbn] offen sc1\n\t" \
          "s_nop 15\n\t" \
          "s_nop 5\n\t" \
          "buffer_load_dwordx4 v[200:203], %[ga2], %[rsm], %[mbn] offen sc1\n\t" \
          "s_nop 15\n\t" \
          "s_nop 5\n\t" \
          "buffer_load_dwordx4 v[204:207], %[ga3], %[rsm], %[mbn] offen sc1\n\t" \
          "buffer_load_dword %[al], %[alo], %[rsm], %[mb] offen sc1\n\t" \
          "Lv_go%=:\n\t" \
          "s_waitcnt vmcnt(4)\n\t" \
          "v_smfmac_f32_16x16x64_bf16 v[176:179], v[192:195], v[8:15], %[idx]\n\t" \
          "v_max3_u32 v240, v192, v193, v194\n\t" \
          "v_smfmac_f32_16x16x64_bf16 v[180:183], v[192:195], v[16:23], %[idx]\n\t" \
          "v_max3_u32 v240, v194, v195, v240\n\t" \
          "v_smfmac_f32_16x16x64_bf16 v[184:187], v[192:195], v[24:31], %[idx]\n\t" \
          "buffer_load_dwordx2 %[rec], off, %[rsm], %[rco] sc1\n\t" \
          "v_smfmac_f32_16x16x64_bf16 v[188:191], v[192:195], v[32:39], %[idx]\n\t" \
          "s_waitcnt vmcnt(4)\n\t" \
          "v_smfmac_f32_16x16x64_bf16 v[176:179], v[196:199], v[40:47], %[idx]\n\t" \
          "v_max3_u32 v240, v196, v197, v240\n\t" \
          "v_smfmac_f32_16x16x64_bf16 v[180:183], v[196:199], v[48:55], %[idx]\n\t" \
          "v_max3_u32 v240, v198, v199, v240\n\t" \
          "v_smfmac_f32_16x16x64_bf16 v[184:187], v[196:199], v[56:63], %[idx]\n\t" \
          "v_smfmac_f32_16x16x64_bf16 v[188:191], v[196:199], v[64:71], %[idx]\n\t" \
          "s_waitcnt vmcnt(3)\n\t" \
          "v_smfmac_f32_16x16x64_bf16 v[176:179], v[200:203], v[72:79], %[idx]\n\t" \
          "v_max3_u32 v240, v200, v201, v240\n\t" \
          "v_smfmac_f32_16x16x64_bf16 v[180:183], v[200:203], v[80:87], %[idx]\n\t" \
          "v_max3_u32 v240, v202, v203, v240\n\t" \
          "v_smfmac_f32_16x16x64_bf16 v[184:187], v[200:203], v[88:95], %[idx]\n\t" \
          "v_smfmac_f32_16x16x64_bf16 v[188:191], v[200:203], v[96:103], %[idx]\n\t" \
          "s_waitcnt vmcnt(2)\n\t" \
          "v_smfmac_f32_16x16x64_bf16 v[176:179], v[204:207], v[104:111], %[idx]\n\t" \
          "v_max3_u32 v240, v204, v205, v240\n\t" \
          "v_smfmac_f32_16x16x64_bf16 v[180:183], v[204:207], v[112:119], %[idx]\n\t" \
          "v_max3_u32 v240, v206, v207, v240\n\t" \
          "v_smfmac_f32_16x16x64_bf16 v[184:187], v[204:207], v[120:127], %[idx]\n\t" \
          "v_cmp_eq_u32 vcc, -1, v240\n\t" \
          "v_smfmac_f32_16x16x64_bf16 v[188:191], v[204:207], v[128:135], %[idx]\n\t" \
          "s_and_b64 vcc, vcc, %[en]\n\t" \
          "v_add_f32 v242, v176, v177\n\t" \
          "v_add_f32 v243, v178, v179\n\t" \
          "v_add_f32 v244, v180, v181\n\t" \
          "v_add_f32 v245, v182, v183\n\t" \
          "s_cbranch_scc1 Lv_fail%=\n\t" \
          "v_add_f32 v246, v184, v185\n\t" \
          "v_add_f32 v247, v186, v187\n\t" \
          "ds_write_b64 %[vw0], v[242:243]\n\t" \
          "ds_write_b64 %[vw1], v[244:245]\n\t" \
          "ds_write_b64 %[vw2], v[246:247]\n\t" \
          "v_add_f32 v248, v188, v189\n\t" \
          "v_add_f32 v249, v190, v191\n\t" \
          "v_mfma_f32_16x16x32_bf16 v[208:211], v[212:215], v[212:215], 0\n\t" \
          "v_mfma_f32_16x16x32_bf16 v[208:211], v[212:215], v[212:215], 0\n\t" \
          "s_waitcnt lgkmcnt(0)\n\t" \
          "s_barrier\n\t" \
          "ds_read_b64 v[242:243], %[vr] offset:0\n\t" \
          "v_mfma_f32_16x16x32_bf16 v[208:211], v[212:215], v[212:215], 0\n\t" \
          "ds_read_b64 v[244:245], %[vr] offset:2064\n\t" \
          "ds_read_b64 v[246:247], %[vr] offset:4128\n\t" \
          "v_mfma_f32_16x16x32_bf16 v[208:211], v[212:215], v[212:215], 0\n\t" \
          "s_waitcnt lgkmcnt(2)\n\t" \
          "v_add_f32 v248, v248, v242\n\t" \
          "v_add_f32 v249, v249, v243\n\t" \
          "s_waitcnt lgkmcnt(1)\n\t" \
          "v_add_f32 v248, v248, v244\n\t" \
          "v_add_f32 v249, v249, v245\n\t" \
          "s_waitcnt lgkmcnt(0)\n\t" \
          "v_add_f32 v248, v248, v246\n\t" \
          "v_add_f32 v249, v249, v247\n\t" \
          "v_cvt_pk_bf16_f32 v242, v248, v249\n\t" \
          "v_min_u32 v242, -2, v242\n\t" \
          "buffer_store_dword v242, %[vo], %[rsm], %[vb] offen sc1\n\t" \
          "s_memrealtime %[te]\n\t" \
          "v_mov_b64 v[252:253], -1\n\t" \
          "v_mov_b64 v[254:255], -1\n\t" \
          "s_sleep 1\n\t" \
          "buffer_store_dwordx4 v[252:255], %[rso], %[rsm], %[rb] offen sc1\n\t" \
          "s_waitcnt vmcnt(2) lgkmcnt(0)\n\t"
#define VO_W "{a[0:31]}"(wa[0]), "{a[32:63]}"(wa[1]), "{a[64:95]}"(wa[2]), "{a[96:127]}"(wa[3]), "{a[128:159]}"(wa[4]), "{a[160:191]}"(wa[5]), "{a[192:223]}"(wa[6]), "{a[224:255]}"(wa[7]), "{v[8:39]}"(wv_[0]), "{v[40:71]}"(wv_[1]), "{v[72:103]}"(wv_[2]), "{v[104:135]}"(wv_[3])
#define VO_DEFW "=&{a[0:31]}"(wa[0]), "=&{a[32:63]}"(wa[1]), "=&{a[64:95]}"(wa[2]), "=&{a[96:127]}"(wa[3]), "=&{a[128:159]}"(wa[4]), "=&{a[160:191]}"(wa[5]), "=&{a[192:223]}"(wa[6]), "=&{a[224:255]}"(wa[7]), "=&{v[8:39]}"(wv_[0]), "=&{v[40:71]}"(wv_[1]), "=&{v[72:103]}"(wv_[2]), "=&{v[104:135]}"(wv_[3])
#define VO_LOADER \
          "s_mov_b64 s[48:49], %[wo]\n\ts_mov_b32 s50, %[wsz]\n\ts_mov_b32 s51, 0x20000\n\ts_nop 4\n\ts_add_u32 m0, %[stg], 0\n\ts_xor_b32 %[t], %[vk], 0x0\n\t" \
          "buffer_load_dwordx4 %[sv], %[rv], %[t] offen lds\n\ts_add_u32 m0, %[stg], 1024\n\ts_xor_b32 %[t], %[vk], 0x10000\n\tbuffer_load_dwordx4 %[sv], %[rv], %[t] offen lds\n\ts_add_u32 m0, %[stg], 2048\n\ts_xor_b32 %[t], %[vk], 0x20000\n\t" \
          "buffer_load_dwordx4 %[sv], %[rv], %[t] offen lds\n\ts_add_u32 m0, %[stg], 3072\n\ts_xor_b32 %[t], %[vk], 0x30000\n\tbuffer_load_dwordx4 %[sv], %[rv], %[t] offen lds\n\ts_add_u32 m0, %[stg], 4096\n\ts_xor_b32 %[t], %[vk], 0x40000\n\t" \
          "buffer_load_dwordx4 %[sv], %[rv], %[t] offen lds\n\ts_add_u32 m0, %[stg], 5120\n\ts_xor_b32 %[t], %[vk], 0x50000\n\tbuffer_load_dwordx4 %[sv], %[rv], %[t] offen lds\n\ts_add_u32 m0, %[stg], 6144\n\ts_xor_b32 %[t], %[vk], 0x60000\n\t" \
          "buffer_load_dwordx4 %[sv], %[rv], %[t] offen lds\n\ts_add_u32 m0, %[stg], 7168\n\ts_xor_b32 %[t], %[vk], 0x70000\n\tbuffer_load_dwordx4 %[sv], %[rv], %[t] offen lds\n\ts_add_u32 m0, %[stg], 8192\n\ts_xor_b32 %[t], %[vk], 0x100000\n\t" \
          "buffer_load_dwordx4 %[sv], %[rv], %[t] offen lds\n\ts_add_u32 m0, %[stg], 9216\n\ts_xor_b32 %[t], %[vk], 0x110000\n\tbuffer_load_dwordx4 %[sv], %[rv], %[t] offen lds\n\ts_add_u32 m0, %[stg], 10240\n\ts_xor_b32 %[t], %[vk], 0x120000\n\t" \
          "buffer_load_dwordx4 %[sv], %[rv], %[t] offen lds\n\ts_add_u32 m0, %[stg], 11264\n\ts_xor_b32 %[t], %[vk], 0x130000\n\tbuffer_load_dwordx4 %[sv], %[rv], %[t] offen lds\n\ts_add_u32 m0, %[stg], 12288\n\ts_xor_b32 %[t], %[vk], 0x140000\n\t" \
          "buffer_load_dwordx4 %[sv], %[rv], %[t] offen lds\n\ts_add_u32 m0, %[stg], 13312\n\ts_xor_b32 %[t], %[vk], 0x150000\n\tbuffer_load_dwordx4 %[sv], %[rv], %[t] offen lds\n\ts_add_u32 m0, %[stg], 14336\n\ts_xor_b32 %[t], %[vk], 0x160000\n\t" \
          "buffer_load_dwordx4 %[sv], %[rv], %[t] offen lds\n\ts_add_u32 m0, %[stg], 15360\n\ts_xor_b32 %[t], %[vk], 0x170000\n\tbuffer_load_dwordx4 %[sv], %[rv], %[t] offen lds\n\ts_add_u32 m0, %[stg], 16384\n\ts_xor_b32 %[t], %[vk], 0x200000\n\t" \
          "buffer_load_dwordx4 %[sv], %[rv], %[t] offen lds\n\ts_add_u32 m0, %[stg], 17408\n\ts_xor_b32 %[t], %[vk], 0x210000\n\tbuffer_load_dwordx4 %[sv], %[rv], %[t] offen lds\n\ts_add_u32 m0, %[stg], 18432\n\ts_xor_b32 %[t], %[vk], 0x220000\n\t" \
          "buffer_load_dwordx4 %[sv], %[rv], %[t] offen lds\n\ts_add_u32 m0, %[stg], 19456\n\ts_xor_b32 %[t], %[vk], 0x230000\n\tbuffer_load_dwordx4 %[sv], %[rv], %[t] offen lds\n\ts_add_u32 m0, %[stg], 20480\n\ts_xor_b32 %[t], %[vk], 0x240000\n\t" \
          "buffer_load_dwordx4 %[sv], %[rv], %[t] offen lds\n\ts_add_u32 m0, %[stg], 21504\n\ts_xor_b32 %[t], %[vk], 0x250000\n\tbuffer_load_dwordx4 %[sv], %[rv], %[t] offen lds\n\ts_add_u32 m0, %[stg], 22528\n\ts_xor_b32 %[t], %[vk], 0x260000\n\t" \
          "buffer_load_dwordx4 %[sv], %[rv], %[t] offen lds\n\ts_add_u32 m0, %[stg], 23552\n\ts_xor_b32 %[t], %[vk], 0x270000\n\tbuffer_load_dwordx4 %[sv], %[rv], %[t] offen lds\n\ts_add_u32 m0, %[stg], 24576\n\ts_xor_b32 %[t], %[vk], 0x300000\n\t" \
          "buffer_load_dwordx4 %[sv], %[rv], %[t] offen lds\n\ts_add_u32 m0, %[stg], 25600\n\ts_xor_b32 %[t], %[vk], 0x310000\n\tbuffer_load_dwordx4 %[sv], %[rv], %[t] offen lds\n\ts_add_u32 m0, %[stg], 26624\n\ts_xor_b32 %[t], %[vk], 0x320000\n\t" \
          "buffer_load_dwordx4 %[sv], %[rv], %[t] offen lds\n\ts_add_u32 m0, %[stg], 27648\n\ts_xor_b32 %[t], %[vk], 0x330000\n\tbuffer_load_dwordx4 %[sv], %[rv], %[t] offen lds\n\ts_add_u32 m0, %[stg], 28672\n\ts_xor_b32 %[t], %[vk], 0x340000\n\t" \
          "buffer_load_dwordx4 %[sv], %[rv], %[t] offen lds\n\ts_add_u32 m0, %[stg], 29696\n\ts_xor_b32 %[t], %[vk], 0x350000\n\tbuffer_load_dwordx4 %[sv], %[rv], %[t] offen lds\n\ts_add_u32 m0, %[stg], 30720\n\ts_xor_b32 %[t], %[vk], 0x360000\n\t" \
          "buffer_load_dwordx4 %[sv], %[rv], %[t] offen lds\n\ts_add_u32 m0, %[stg], 31744\n\ts_xor_b32 %[t], %[vk], 0x370000\n\tbuffer_load_dwordx4 %[sv], %[rv], %[t] offen lds\n\ts_waitcnt vmcnt(16)\n\tds_read_b64_tr_b16 v[8:9], %[vb0] offset:0\n\t" \
          "ds_read_b64_tr_b16 v[10:11], %[vb0] offset:128\n\tds_read_b64_tr_b16 v[12:13], %[vb0] offset:4096\n\tds_read_b64_tr_b16 v[14:15], %[vb0] offset:4224\n\tds_read_b64_tr_b16 v[16:17], %[vb1] offset:0\n\tds_read_b64_tr_b16 v[18:19], %[vb1] offset:128\n\tds_read_b64_tr_b16 v[20:21], %[vb1] offset:4096\n\t" \
          "ds_read_b64_tr_b16 v[22:23], %[vb1] offset:4224\n\tds_read_b64_tr_b16 v[24:25], %[vb2] offset:0\n\tds_read_b64_tr_b16 v[26:27], %[vb2] offset:128\n\tds_read_b64_tr_b16 v[28:29], %[vb2] offset:4096\n\tds_read_b64_tr_b16 v[30:31], %[vb2] offset:4224\n\tds_read_b64_tr_b16 v[32:33], %[vb3] offset:0\n\t" \
          "ds_read_b64_tr_b16 v[34:35], %[vb3] offset:128\n\tds_read_b64_tr_b16 v[36:37], %[vb3] offset:4096\n\tds_read_b64_tr_b16 v[38:39], %[vb3] offset:4224\n\tds_read_b64_tr_b16 v[40:41], %[vb0] offset:8192\n\tds_read_b64_tr_b16 v[42:43], %[vb0] offset:8320\n\tds_read_b64_tr_b16 v[44:45], %[vb0] offset:12288\n\t" \
          "ds_read_b64_tr_b16 v[46:47], %[vb0] offset:12416\n\tds_read_b64_tr_b16 v[48:49], %[vb1] offset:8192\n\tds_read_b64_tr_b16 v[50:51], %[vb1] offset:8320\n\tds_read_b64_tr_b16 v[52:53], %[vb1] offset:12288\n\tds_read_b64_tr_b16 v[54:55], %[vb1] offset:12416\n\tds_read_b64_tr_b16 v[56:57], %[vb2] offset:8192\n\t" \
          "ds_read_b64_tr_b16 v[58:59], %[vb2] offset:8320\n\tds_read_b64_tr_b16 v[60:61], %[vb2] offset:12288\n\tds_read_b64_tr_b16 v[62:63], %[vb2] offset:12416\n\tds_read_b64_tr_b16 v[64:65], %[vb3] offset:8192\n\tds_read_b64_tr_b16 v[66:67], %[vb3] offset:8320\n\tds_read_b64_tr_b16 v[68:69], %[vb3] offset:12288\n\t" \
          "ds_read_b64_tr_b16 v[70:71], %[vb3] offset:12416\n\ts_waitcnt lgkmcnt(0)\n\ts_add_u32 %[ho], %[k0], 1\n\ts_and_b32 %[ho], %[ho], 2\n\ts_or_b32 %[ho], %[ho], 0\n\ts_lshl_b32 %[ho], %[ho], 20\n\t" \
          "s_xor_b32 %[ho], %[ho], %[hx]\n\ts_add_u32 m0, %[stg], 0\n\ts_add_u32 %[t], %[ho], 0\n\tbuffer_load_dwordx4 %[so], s[48:51], %[t] offen lds\n\ts_add_u32 m0, %[stg], 1024\n\ts_add_u32 %[t], %[ho], 16384\n\t" \
          "buffer_load_dwordx4 %[so], s[48:51], %[t] offen lds\n\ts_add_u32 m0, %[stg], 2048\n\ts_add_u32 %[t], %[ho], 32768\n\tbuffer_load_dwordx4 %[so], s[48:51], %[t] offen lds\n\ts_add_u32 m0, %[stg], 3072\n\ts_add_u32 %[t], %[ho], 49152\n\t" \
          "buffer_load_dwordx4 %[so], s[48:51], %[t] offen lds\n\ts_add_u32 m0, %[stg], 4096\n\ts_add_u32 %[t], %[ho], 65536\n\tbuffer_load_dwordx4 %[so], s[48:51], %[t] offen lds\n\ts_add_u32 m0, %[stg], 5120\n\ts_add_u32 %[t], %[ho], 81920\n\t" \
          "buffer_load_dwordx4 %[so], s[48:51], %[t] offen lds\n\ts_add_u32 m0, %[stg], 6144\n\ts_add_u32 %[t], %[ho], 98304\n\tbuffer_load_dwordx4 %[so], s[48:51], %[t] offen lds\n\ts_add_u32 m0, %[stg], 7168\n\ts_add_u32 %[t], %[ho], 114688\n\t" \
          "buffer_load_dwordx4 %[so], s[48:51], %[t] offen lds\n\ts_add_u32 m0, %[stg], 8192\n\ts_add_u32 %[t], %[ho], 131072\n\tbuffer_load_dwordx4 %[so], s[48:51], %[t] offen lds\n\ts_add_u32 m0, %[stg], 9216\n\ts_add_u32 %[t], %[ho], 147456\n\t" \
          "buffer_load_dwordx4 %[so], s[48:51], %[t] offen lds\n\ts_add_u32 m0, %[stg], 10240\n\ts_add_u32 %[t], %[ho], 163840\n\tbuffer_load_dwordx4 %[so], s[48:51], %[t] offen lds\n\ts_add_u32 m0, %[stg], 11264\n\ts_add_u32 %[t], %[ho], 180224\n\t" \
          "buffer_load_dwordx4 %[so], s[48:51], %[t] offen lds\n\ts_add_u32 m0, %[stg], 12288\n\ts_add_u32 %[t], %[ho], 196608\n\tbuffer_load_dwordx4 %[so], s[48:51], %[t] offen lds\n\ts_add_u32 m0, %[stg], 13312\n\ts_add_u32 %[t], %[ho], 212992\n\t" \
          "buffer_load_dwordx4 %[so], s[48:51], %[t] offen lds\n\ts_add_u32 m0, %[stg], 14336\n\ts_add_u32 %[t], %[ho], 229376\n\tbuffer_load_dwordx4 %[so], s[48:51], %[t] offen lds\n\ts_add_u32 m0, %[stg], 15360\n\ts_add_u32 %[t], %[ho], 245760\n\t" \
          "buffer_load_dwordx4 %[so], s[48:51], %[t] offen lds\n\ts_waitcnt vmcnt(16)\n\tds_read_b64_tr_b16 v[72:73], %[vb0] offset:16384\n\tds_read_b64_tr_b16 v[74:75], %[vb0] offset:16512\n\tds_read_b64_tr_b16 v[76:77], %[vb0] offset:20480\n\tds_read_b64_tr_b16 v[78:79], %[vb0] offset:20608\n\t" \
          "ds_read_b64_tr_b16 v[80:81], %[vb1] offset:16384\n\tds_read_b64_tr_b16 v[82:83], %[vb1] offset:16512\n\tds_read_b64_tr_b16 v[84:85], %[vb1] offset:20480\n\tds_read_b64_tr_b16 v[86:87], %[vb1] offset:20608\n\tds_read_b64_tr_b16 v[88:89], %[vb2] offset:16384\n\tds_read_b64_tr_b16 v[90:91], %[vb2] offset:16512\n\t" \
          "ds_read_b64_tr_b16 v[92:93], %[vb2] offset:20480\n\tds_read_b64_tr_b16 v[94:95], %[vb2] offset:20608\n\tds_read_b64_tr_b16 v[96:97], %[vb3] offset:16384\n\tds_read_b64_tr_b16 v[98:99], %[vb3] offset:16512\n\tds_read_b64_tr_b16 v[100:101], %[vb3] offset:20480\n\tds_read_b64_tr_b16 v[102:103], %[vb3] offset:20608\n\t" \
          "ds_read_b64_tr_b16 v[104:105], %[vb0] offset:24576\n\tds_read_b64_tr_b16 v[106:107], %[vb0] offset:24704\n\tds_read_b64_tr_b16 v[108:109], %[vb0] offset:28672\n\tds_read_b64_tr_b16 v[110:111], %[vb0] offset:28800\n\tds_read_b64_tr_b16 v[112:113], %[vb1] offset:24576\n\tds_read_b64_tr_b16 v[114:115], %[vb1] offset:24704\n\t" \
          "ds_read_b64_tr_b16 v[116:117], %[vb1] offset:28672\n\tds_read_b64_tr_b16 v[118:119], %[vb1] offset:28800\n\tds_read_b64_tr_b16 v[120:121], %[vb2] offset:24576\n\tds_read_b64_tr_b16 v[122:123], %[vb2] offset:24704\n\tds_read_b64_tr_b16 v[124:125], %[vb2] offset:28672\n\tds_read_b64_tr_b16 v[126:127], %[vb2] offset:28800\n\t" \
          "ds_read_b64_tr_b16 v[128:129], %[vb3] offset:24576\n\tds_read_b64_tr_b16 v[130:131], %[vb3] offset:24704\n\tds_read_b64_tr_b16 v[132:133], %[vb3] offset:28672\n\tds_read_b64_tr_b16 v[134:135], %[vb3] offset:28800\n\ts_waitcnt lgkmcnt(0)\n\ts_add_u32 %[ho], %[k0], 0\n\t" \
          "s_and_b32 %[ho], %[ho], 2\n\ts_or_b32 %[ho], %[ho], 1\n\ts_lshl_b32 %[ho], %[ho], 20\n\ts_xor_b32 %[ho], %[ho], %[hx]\n\ts_add_u32 m0, %[stg], 16384\n\ts_add_u32 %[t], %[ho], 0\n\t" \
          "buffer_load_dwordx4 %[so], s[48:51], %[t] offen lds\n\ts_add_u32 m0, %[stg], 17408\n\ts_add_u32 %[t], %[ho], 16384\n\tbuffer_load_dwordx4 %[so], s[48:51], %[t] offen lds\n\ts_add_u32 m0, %[stg], 18432\n\ts_add_u32 %[t], %[ho], 32768\n\t" \
          "buffer_load_dwordx4 %[so], s[48:51], %[t] offen lds\n\ts_add_u32 m0, %[stg], 19456\n\ts_add_u32 %[t], %[ho], 49152\n\tbuffer_load_dwordx4 %[so], s[48:51], %[t] offen lds\n\ts_add_u32 m0, %[stg], 20480\n\ts_add_u32 %[t], %[ho], 65536\n\t" \
          "buffer_load_dwordx4 %[so], s[48:51], %[t] offen lds\n\ts_add_u32 m0, %[stg], 21504\n\ts_add_u32 %[t], %[ho], 81920\n\tbuffer_load_dwordx4 %[so], s[48:51], %[t] offen lds\n\ts_add_u32 m0, %[stg], 22528\n\ts_add_u32 %[t], %[ho], 98304\n\t" \
          "buffer_load_dwordx4 %[so], s[48:51], %[t] offen lds\n\ts_add_u32 m0, %[stg], 23552\n\ts_add_u32 %[t], %[ho], 114688\n\tbuffer_load_dwordx4 %[so], s[48:51], %[t] offen lds\n\ts_add_u32 m0, %[stg], 24576\n\ts_add_u32 %[t], %[ho], 131072\n\t" \
          "buffer_load_dwordx4 %[so], s[48:51], %[t] offen lds\n\ts_add_u32 m0, %[stg], 25600\n\ts_add_u32 %[t], %[ho], 147456\n\tbuffer_load_dwordx4 %[so], s[48:51], %[t] offen lds\n\ts_add_u32 m0, %[stg], 26624\n\ts_add_u32 %[t], %[ho], 163840\n\t" \
          "buffer_load_dwordx4 %[so], s[48:51], %[t] offen lds\n\ts_add_u32 m0, %[stg], 27648\n\ts_add_u32 %[t], %[ho], 180224\n\tbuffer_load_dwordx4 %[so], s[48:51], %[t] offen lds\n\ts_add_u32 m0, %[stg], 28672\n\ts_add_u32 %[t], %[ho], 196608\n\t" \
          "buffer_load_dwordx4 %[so], s[48:51], %[t] offen lds\n\ts_add_u32 m0, %[stg], 29696\n\ts_add_u32 %[t], %[ho], 212992\n\tbuffer_load_dwordx4 %[so], s[48:51], %[t] offen lds\n\ts_add_u32 m0, %[stg], 30720\n\ts_add_u32 %[t], %[ho], 229376\n\t" \
          "buffer_load_dwordx4 %[so], s[48:51], %[t] offen lds\n\ts_add_u32 m0, %[stg], 31744\n\ts_add_u32 %[t], %[ho], 245760\n\tbuffer_load_dwordx4 %[so], s[48:51], %[t] offen lds\n\ts_mov_b32 %[n], 8\n\ts_cmp_eq_u32 %[k0], 2\n\t" \
          "s_cbranch_scc1 Lw_2_%=\n\ts_cmp_eq_u32 %[k0], 0\n\ts_cbranch_scc1 Lw_0_%=\n\ts_waitcnt vmcnt(0)\n\ts_cmp_eq_u32 %[k0], 1\n\ts_cbranch_scc1 Lw_1_%=\n\t" \
          "s_branch Lw_3_%=\n\tLw_0_%=:\n\ts_waitcnt vmcnt(16)\n\tds_read_b64_tr_b16 a[0:1], %[ob] offset:0\n\tds_read_b64_tr_b16 a[2:3], %[ob] offset:128\n\tds_read_b64_tr_b16 a[4:5], %[ob] offset:8192\n\t" \
          "ds_read_b64_tr_b16 a[6:7], %[ob] offset:8320\n\tds_read_b64_tr_b16 a[8:9], %[ob] offset:32\n\tds_read_b64_tr_b16 a[10:11], %[ob] offset:160\n\tds_read_b64_tr_b16 a[12:13], %[ob] offset:8224\n\tds_read_b64_tr_b16 a[14:15], %[ob] offset:8352\n\tds_read_b64_tr_b16 a[16:17], %[ob] offset:1024\n\t" \
          "ds_read_b64_tr_b16 a[18:19], %[ob] offset:1152\n\tds_read_b64_tr_b16 a[20:21], %[ob] offset:9216\n\tds_read_b64_tr_b16 a[22:23], %[ob] offset:9344\n\tds_read_b64_tr_b16 a[24:25], %[ob] offset:1056\n\tds_read_b64_tr_b16 a[26:27], %[ob] offset:1184\n\tds_read_b64_tr_b16 a[28:29], %[ob] offset:9248\n\t" \
          "ds_read_b64_tr_b16 a[30:31], %[ob] offset:9376\n\ts_cmp_gt_u32 %[n], 2\n\ts_cselect_b32 s50, s50, 0\n\ts_cmp_gt_u32 %[n], 6\n\ts_cselect_b32 %[ho], 0x2, 0x6\n\ts_lshl_b32 %[ho], %[ho], 20\n\t" \
          "s_xor_b32 %[ho], %[ho], %[hx]\n\ts_waitcnt lgkmcnt(0)\n\ts_add_u32 m0, %[stg], 0\n\ts_add_u32 %[t], %[ho], 0\n\tbuffer_load_dwordx4 %[so], s[48:51], %[t] offen lds\n\ts_add_u32 m0, %[stg], 1024\n\t" \
          "s_add_u32 %[t], %[ho], 16384\n\tbuffer_load_dwordx4 %[so], s[48:51], %[t] offen lds\n\ts_add_u32 m0, %[stg], 2048\n\ts_add_u32 %[t], %[ho], 32768\n\tbuffer_load_dwordx4 %[so], s[48:51], %[t] offen lds\n\ts_add_u32 m0, %[stg], 3072\n\t" \
          "s_add_u32 %[t], %[ho], 49152\n\tbuffer_load_dwordx4 %[so], s[48:51], %[t] offen lds\n\ts_add_u32 m0, %[stg], 4096\n\ts_add_u32 %[t], %[ho], 65536\n\tbuffer_load_dwordx4 %[so], s[48:51], %[t] offen lds\n\ts_add_u32 m0, %[stg], 5120\n\t" \
          "s_add_u32 %[t], %[ho], 81920\n\tbuffer_load_dwordx4 %[so], s[48:51], %[t] offen lds\n\ts_add_u32 m0, %[stg], 6144\n\ts_add_u32 %[t], %[ho], 98304\n\tbuffer_load_dwordx4 %[so], s[48:51], %[t] offen lds\n\ts_add_u32 m0, %[stg], 7168\n\t" \
          "s_add_u32 %[t], %[ho], 114688\n\tbuffer_load_dwordx4 %[so], s[48:51], %[t] offen lds\n\ts_add_u32 m0, %[stg], 8192\n\ts_add_u32 %[t], %[ho], 131072\n\tbuffer_load_dwordx4 %[so], s[48:51], %[t] offen lds\n\ts_add_u32 m0, %[stg], 9216\n\t" \
          "s_add_u32 %[t], %[ho], 147456\n\tbuffer_load_dwordx4 %[so], s[48:51], %[t] offen lds\n\ts_add_u32 m0, %[stg], 10240\n\ts_add_u32 %[t], %[ho], 163840\n\tbuffer_load_dwordx4 %[so], s[48:51], %[t] offen lds\n\ts_add_u32 m0, %[stg], 11264\n\t" \
          "s_add_u32 %[t], %[ho], 180224\n\tbuffer_load_dwordx4 %[so], s[48:51], %[t] offen lds\n\ts_add_u32 m0, %[stg], 12288\n\ts_add_u32 %[t], %[ho], 196608\n\tbuffer_load_dwordx4 %[so], s[48:51], %[t] offen lds\n\ts_add_u32 m0, %[stg], 13312\n\t" \
          "s_add_u32 %[t], %[ho], 212992\n\tbuffer_load_dwordx4 %[so], s[48:51], %[t] offen lds\n\ts_add_u32 m0, %[stg], 14336\n\ts_add_u32 %[t], %[ho], 229376\n\tbuffer_load_dwordx4 %[so], s[48:51], %[t] offen lds\n\ts_add_u32 m0, %[stg], 15360\n\t" \
          "s_add_u32 %[t], %[ho], 245760\n\tbuffer_load_dwordx4 %[so], s[48:51], %[t] offen lds\n\ts_sub_u32 %[n], %[n], 1\n\ts_cmp_eq_u32 %[n], 4\n\ts_cbranch_scc1 Lw_5_%=\n\tLw_1_%=:\n\t" \
          "s_waitcnt vmcnt(16)\n\tds_read_b64_tr_b16 a[32:33], %[ob] offset:16384\n\tds_read_b64_tr_b16 a[34:35], %[ob] offset:16512\n\tds_read_b64_tr_b16 a[36:37], %[ob] offset:24576\n\tds_read_b64_tr_b16 a[38:39], %[ob] offset:24704\n\tds_read_b64_tr_b16 a[40:41], %[ob] offset:16416\n\t" \
          "ds_read_b64_tr_b16 a[42:43], %[ob] offset:16544\n\tds_read_b64_tr_b16 a[44:45], %[ob] offset:24608\n\tds_read_b64_tr_b16 a[46:47], %[ob] offset:24736\n\tds_read_b64_tr_b16 a[48:49], %[ob] offset:17408\n\tds_read_b64_tr_b16 a[50:51], %[ob] offset:17536\n\tds_read_b64_tr_b16 a[52:53], %[ob] offset:25600\n\t" \
          "ds_read_b64_tr_b16 a[54:55], %[ob] offset:25728\n\tds_read_b64_tr_b16 a[56:57], %[ob] offset:17440\n\tds_read_b64_tr_b16 a[58:59], %[ob] offset:17568\n\tds_read_b64_tr_b16 a[60:61], %[ob] offset:25632\n\tds_read_b64_tr_b16 a[62:63], %[ob] offset:25760\n\ts_cmp_gt_u32 %[n], 2\n\t" \
          "s_cselect_b32 s50, s50, 0\n\ts_cmp_gt_u32 %[n], 6\n\ts_cselect_b32 %[ho], 0x3, 0x7\n\ts_lshl_b32 %[ho], %[ho], 20\n\ts_xor_b32 %[ho], %[ho], %[hx]\n\ts_waitcnt lgkmcnt(0)\n\t" \
          "s_add_u32 m0, %[stg], 16384\n\ts_add_u32 %[t], %[ho], 0\n\tbuffer_load_dwordx4 %[so], s[48:51], %[t] offen lds\n\ts_add_u32 m0, %[stg], 17408\n\ts_add_u32 %[t], %[ho], 16384\n\tbuffer_load_dwordx4 %[so], s[48:51], %[t] offen lds\n\t" \
          "s_add_u32 m0, %[stg], 18432\n\ts_add_u32 %[t], %[ho], 32768\n\tbuffer_load_dwordx4 %[so], s[48:51], %[t] offen lds\n\ts_add_u32 m0, %[stg], 19456\n\ts_add_u32 %[t], %[ho], 49152\n\tbuffer_load_dwordx4 %[so], s[48:51], %[t] offen lds\n\t" \
          "s_add_u32 m0, %[stg], 20480\n\ts_add_u32 %[t], %[ho], 65536\n\tbuffer_load_dwordx4 %[so], s[48:51], %[t] offen lds\n\ts_add_u32 m0, %[stg], 21504\n\ts_add_u32 %[t], %[ho], 81920\n\tbuffer_load_dwordx4 %[so], s[48:51], %[t] offen lds\n\t" \
          "s_add_u32 m0, %[stg], 22528\n\ts_add_u32 %[t], %[ho], 98304\n\tbuffer_load_dwordx4 %[so], s[48:51], %[t] offen lds\n\ts_add_u32 m0, %[stg], 23552\n\ts_add_u32 %[t], %[ho], 114688\n\tbuffer_load_dwordx4 %[so], s[48:51], %[t] offen lds\n\t" \
          "s_add_u32 m0, %[stg], 24576\n\ts_add_u32 %[t], %[ho], 131072\n\tbuffer_load_dwordx4 %[so], s[48:51], %[t] offen lds\n\ts_add_u32 m0, %[stg], 25600\n\ts_add_u32 %[t], %[ho], 147456\n\tbuffer_load_dwordx4 %[so], s[48:51], %[t] offen lds\n\t" \
          "s_add_u32 m0, %[stg], 26624\n\ts_add_u32 %[t], %[ho], 163840\n\tbuffer_load_dwordx4 %[so], s[48:51], %[t] offen lds\n\ts_add_u32 m0, %[stg], 27648\n\ts_add_u32 %[t], %[ho], 180224\n\tbuffer_load_dwordx4 %[so], s[48:51], %[t] offen lds\n\t" \
          "s_add_u32 m0, %[stg], 28672\n\ts_add_u32 %[t], %[ho], 196608\n\tbuffer_load_dwordx4 %[so], s[48:51], %[t] offen lds\n\ts_add_u32 m0, %[stg], 29696\n\ts_add_u32 %[t], %[ho], 212992\n\tbuffer_load_dwordx4 %[so], s[48:51], %[t] offen lds\n\t" \
          "s_add_u32 m0, %[stg], 30720\n\ts_add_u32 %[t], %[ho], 229376\n\tbuffer_load_dwordx4 %[so], s[48:51], %[t] offen lds\n\ts_add_u32 m0, %[stg], 31744\n\ts_add_u32 %[t], %[ho], 245760\n\tbuffer_load_dwordx4 %[so], s[48:51], %[t] offen lds\n\t" \
          "s_sub_u32 %[n], %[n], 1\n\ts_cmp_eq_u32 %[n], 4\n\ts_cbranch_scc1 Lw_6_%=\n\tLw_2_%=:\n\ts_waitcnt vmcnt(16)\n\tds_read_b64_tr_b16 a[64:65], %[ob] offset:0\n\t" \
          "ds_read_b64_tr_b16 a[66:67], %[ob] offset:128\n\tds_read_b64_tr_b16 a[68:69], %[ob] offset:8192\n\tds_read_b64_tr_b16 a[70:71], %[ob] offset:8320\n\tds_read_b64_tr_b16 a[72:73], %[ob] offset:32\n\tds_read_b64_tr_b16 a[74:75], %[ob] offset:160\n\tds_read_b64_tr_b16 a[76:77], %[ob] offset:8224\n\t" \
          "ds_read_b64_tr_b16 a[78:79], %[ob] offset:8352\n\tds_read_b64_tr_b16 a[80:81], %[ob] offset:1024\n\tds_read_b64_tr_b16 a[82:83], %[ob] offset:1152\n\tds_read_b64_tr_b16 a[84:85], %[ob] offset:9216\n\tds_read_b64_tr_b16 a[86:87], %[ob] offset:9344\n\tds_read_b64_tr_b16 a[88:89], %[ob] offset:1056\n\t" \
          "ds_read_b64_tr_b16 a[90:91], %[ob] offset:1184\n\tds_read_b64_tr_b16 a[92:93], %[ob] offset:9248\n\tds_read_b64_tr_b16 a[94:95], %[ob] offset:9376\n\ts_cmp_gt_u32 %[n], 2\n\ts_cselect_b32 s50, s50, 0\n\ts_cmp_gt_u32 %[n], 6\n\t" \
          "s_cselect_b32 %[ho], 0x0, 0x4\n\ts_lshl_b32 %[ho], %[ho], 20\n\ts_xor_b32 %[ho], %[ho], %[hx]\n\ts_waitcnt lgkmcnt(0)\n\ts_add_u32 m0, %[stg], 0\n\ts_add_u32 %[t], %[ho], 0\n\t" \
          "buffer_load_dwordx4 %[so], s[48:51], %[t] offen lds\n\ts_add_u32 m0, %[stg], 1024\n\ts_add_u32 %[t], %[ho], 16384\n\tbuffer_load_dwordx4 %[so], s[48:51], %[t] offen lds\n\ts_add_u32 m0, %[stg], 2048\n\ts_add_u32 %[t], %[ho], 32768\n\t" \
          "buffer_load_dwordx4 %[so], s[48:51], %[t] offen lds\n\ts_add_u32 m0, %[stg], 3072\n\ts_add_u32 %[t], %[ho], 49152\n\tbuffer_load_dwordx4 %[so], s[48:51], %[t] offen lds\n\ts_add_u32 m0, %[stg], 4096\n\ts_add_u32 %[t], %[ho], 65536\n\t" \
          "buffer_load_dwordx4 %[so], s[48:51], %[t] offen lds\n\ts_add_u32 m0, %[stg], 5120\n\ts_add_u32 %[t], %[ho], 81920\n\tbuffer_load_dwordx4 %[so], s[48:51], %[t] offen lds\n\ts_add_u32 m0, %[stg], 6144\n\ts_add_u32 %[t], %[ho], 98304\n\t" \
          "buffer_load_dwordx4 %[so], s[48:51], %[t] offen lds\n\ts_add_u32 m0, %[stg], 7168\n\ts_add_u32 %[t], %[ho], 114688\n\tbuffer_load_dwordx4 %[so], s[48:51], %[t] offen lds\n\ts_add_u32 m0, %[stg], 8192\n\ts_add_u32 %[t], %[ho], 131072\n\t" \
          "buffer_load_dwordx4 %[so], s[48:51], %[t] offen lds\n\ts_add_u32 m0, %[stg], 9216\n\ts_add_u32 %[t], %[ho], 147456\n\tbuffer_load_dwordx4 %[so], s[48:51], %[t] offen lds\n\ts_add_u32 m0, %[stg], 10240\n\ts_add_u32 %[t], %[ho], 163840\n\t" \
          "buffer_load_dwordx4 %[so], s[48:51], %[t] offen lds\n\ts_add_u32 m0, %[stg], 11264\n\ts_add_u32 %[t], %[ho], 180224\n\tbuffer_load_dwordx4 %[so], s[48:51], %[t] offen lds\n\ts_add_u32 m0, %[stg], 12288\n\ts_add_u32 %[t], %[ho], 196608\n\t" \
          "buffer_load_dwordx4 %[so], s[48:51], %[t] offen lds\n\ts_add_u32 m0, %[stg], 13312\n\ts_add_u32 %[t], %[ho], 212992\n\tbuffer_load_dwordx4 %[so], s[48:51], %[t] offen lds\n\ts_add_u32 m0, %[stg], 14336\n\ts_add_u32 %[t], %[ho], 229376\n\t" \
          "buffer_load_dwordx4 %[so], s[48:51], %[t] offen lds\n\ts_add_u32 m0, %[stg], 15360\n\ts_add_u32 %[t], %[ho], 245760\n\tbuffer_load_dwordx4 %[so], s[48:51], %[t] offen lds\n\ts_sub_u32 %[n], %[n], 1\n\ts_cmp_eq_u32 %[n], 4\n\t" \
          "s_cbranch_scc1 Lw_7_%=\n\tLw_3_%=:\n\ts_waitcnt vmcnt(16)\n\tds_read_b64_tr_b16 a[96:97], %[ob] offset:16384\n\tds_read_b64_tr_b16 a[98:99], %[ob] offset:16512\n\tds_read_b64_tr_b16 a[100:101], %[ob] offset:24576\n\t" \
          "ds_read_b64_tr_b16 a[102:103], %[ob] offset:24704\n\tds_read_b64_tr_b16 a[104:105], %[ob] offset:16416\n\tds_read_b64_tr_b16 a[106:107], %[ob] offset:16544\n\tds_read_b64_tr_b16 a[108:109], %[ob] offset:24608\n\tds_read_b64_tr_b16 a[110:111], %[ob] offset:24736\n\tds_read_b64_tr_b16 a[112:113], %[ob] offset:17408\n\t" \
          "ds_read_b64_tr_b16 a[114:115], %[ob] offset:17536\n\tds_read_b64_tr_b16 a[116:117], %[ob] offset:25600\n\tds_read_b64_tr_b16 a[118:119], %[ob] offset:25728\n\tds_read_b64_tr_b16 a[120:121], %[ob] offset:17440\n\tds_read_b64_tr_b16 a[122:123], %[ob] offset:17568\n\tds_read_b64_tr_b16 a[124:125], %[ob] offset:25632\n\t" \
          "ds_read_b64_tr_b16 a[126:127], %[ob] offset:25760\n\ts_cmp_gt_u32 %[n], 2\n\ts_cselect_b32 s50, s50, 0\n\ts_cmp_gt_u32 %[n], 6\n\ts_cselect_b32 %[ho], 0x1, 0x5\n\ts_lshl_b32 %[ho], %[ho], 20\n\t" \
          "s_xor_b32 %[ho], %[ho], %[hx]\n\ts_waitcnt lgkmcnt(0)\n\ts_add_u32 m0, %[stg], 16384\n\ts_add_u32 %[t], %[ho], 0\n\tbuffer_load_dwordx4 %[so], s[48:51], %[t] offen lds\n\ts_add_u32 m0, %[stg], 17408\n\t" \
          "s_add_u32 %[t], %[ho], 16384\n\tbuffer_load_dwordx4 %[so], s[48:51], %[t] offen lds\n\ts_add_u32 m0, %[stg], 18432\n\ts_add_u32 %[t], %[ho], 32768\n\tbuffer_load_dwordx4 %[so], s[48:51], %[t] offen lds\n\ts_add_u32 m0, %[stg], 19456\n\t" \
          "s_add_u32 %[t], %[ho], 49152\n\tbuffer_load_dwordx4 %[so], s[48:51], %[t] offen lds\n\ts_add_u32 m0, %[stg], 20480\n\ts_add_u32 %[t], %[ho], 65536\n\tbuffer_load_dwordx4 %[so], s[48:51], %[t] offen lds\n\ts_add_u32 m0, %[stg], 21504\n\t" \
          "s_add_u32 %[t], %[ho], 81920\n\tbuffer_load_dwordx4 %[so], s[48:51], %[t] offen lds\n\ts_add_u32 m0, %[stg], 22528\n\ts_add_u32 %[t], %[ho], 98304\n\tbuffer_load_dwordx4 %[so], s[48:51], %[t] offen lds\n\ts_add_u32 m0, %[stg], 23552\n\t" \
          "s_add_u32 %[t], %[ho], 114688\n\tbuffer_load_dwordx4 %[so], s[48:51], %[t] offen lds\n\ts_add_u32 m0, %[stg], 24576\n\ts_add_u32 %[t], %[ho], 131072\n\tbuffer_load_dwordx4 %[so], s[48:51], %[t] offen lds\n\ts_add_u32 m0, %[stg], 25600\n\t" \
          "s_add_u32 %[t], %[ho], 147456\n\tbuffer_load_dwordx4 %[so], s[48:51], %[t] offen lds\n\ts_add_u32 m0, %[stg], 26624\n\ts_add_u32 %[t], %[ho], 163840\n\tbuffer_load_dwordx4 %[so], s[48:51], %[t] offen lds\n\ts_add_u32 m0, %[stg], 27648\n\t" \
          "s_add_u32 %[t], %[ho], 180224\n\tbuffer_load_dwordx4 %[so], s[48:51], %[t] offen lds\n\ts_add_u32 m0, %[stg], 28672\n\ts_add_u32 %[t], %[ho], 196608\n\tbuffer_load_dwordx4 %[so], s[48:51], %[t] offen lds\n\ts_add_u32 m0, %[stg], 29696\n\t" \
          "s_add_u32 %[t], %[ho], 212992\n\tbuffer_load_dwordx4 %[so], s[48:51], %[t] offen lds\n\ts_add_u32 m0, %[stg], 30720\n\ts_add_u32 %[t], %[ho], 229376\n\tbuffer_load_dwordx4 %[so], s[48:51], %[t] offen lds\n\ts_add_u32 m0, %[stg], 31744\n\t" \
          "s_add_u32 %[t], %[ho], 245760\n\tbuffer_load_dwordx4 %[so], s[48:51], %[t] offen lds\n\ts_sub_u32 %[n], %[n], 1\n\ts_cmp_eq_u32 %[n], 4\n\ts_cbranch_scc1 Lw_4_%=\n\ts_branch Lw_0_%=\n\t" \
          "Lw_4_%=:\n\ts_waitcnt vmcnt(16)\n\tds_read_b64_tr_b16 a[128:129], %[ob] offset:0\n\tds_read_b64_tr_b16 a[130:131], %[ob] offset:128\n\tds_read_b64_tr_b16 a[132:133], %[ob] offset:8192\n\tds_read_b64_tr_b16 a[134:135], %[ob] offset:8320\n\t" \
          "ds_read_b64_tr_b16 a[136:137], %[ob] offset:32\n\tds_read_b64_tr_b16 a[138:139], %[ob] offset:160\n\tds_read_b64_tr_b16 a[140:141], %[ob] offset:8224\n\tds_read_b64_tr_b16 a[142:143], %[ob] offset:8352\n\tds_read_b64_tr_b16 a[144:145], %[ob] offset:1024\n\tds_read_b64_tr_b16 a[146:147], %[ob] offset:1152\n\t" \
          "ds_read_b64_tr_b16 a[148:149], %[ob] offset:9216\n\tds_read_b64_tr_b16 a[150:151], %[ob] offset:9344\n\tds_read_b64_tr_b16 a[152:153], %[ob] offset:1056\n\tds_read_b64_tr_b16 a[154:155], %[ob] offset:1184\n\tds_read_b64_tr_b16 a[156:157], %[ob] offset:9248\n\tds_read_b64_tr_b16 a[158:159], %[ob] offset:9376\n\t" \
          "s_cmp_gt_u32 %[n], 2\n\ts_cselect_b32 s50, s50, 0\n\ts_cmp_gt_u32 %[n], 6\n\ts_cselect_b32 %[ho], 0x2, 0x6\n\ts_lshl_b32 %[ho], %[ho], 20\n\ts_xor_b32 %[ho], %[ho], %[hx]\n\t" \
          "s_waitcnt lgkmcnt(0)\n\ts_add_u32 m0, %[stg], 0\n\ts_add_u32 %[t], %[ho], 0\n\tbuffer_load_dwordx4 %[so], s[48:51], %[t] offen lds\n\ts_add_u32 m0, %[stg], 1024\n\ts_add_u32 %[t], %[ho], 16384\n\t" \
          "buffer_load_dwordx4 %[so], s[48:51], %[t] offen lds\n\ts_add_u32 m0, %[stg], 2048\n\ts_add_u32 %[t], %[ho], 32768\n\tbuffer_load_dwordx4 %[so], s[48:51], %[t] offen lds\n\ts_add_u32 m0, %[stg], 3072\n\ts_add_u32 %[t], %[ho], 49152\n\t" \
          "buffer_load_dwordx4 %[so], s[48:51], %[t] offen lds\n\ts_add_u32 m0, %[stg], 4096\n\ts_add_u32 %[t], %[ho], 65536\n\tbuffer_load_dwordx4 %[so], s[48:51], %[t] offen lds\n\ts_add_u32 m0, %[stg], 5120\n\ts_add_u32 %[t], %[ho], 81920\n\t" \
          "buffer_load_dwordx4 %[so], s[48:51], %[t] offen lds\n\ts_add_u32 m0, %[stg], 6144\n\ts_add_u32 %[t], %[ho], 98304\n\tbuffer_load_dwordx4 %[so], s[48:51], %[t] offen lds\n\ts_add_u32 m0, %[stg], 7168\n\ts_add_u32 %[t], %[ho], 114688\n\t" \
          "buffer_load_dwordx4 %[so], s[48:51], %[t] offen lds\n\ts_add_u32 m0, %[stg], 8192\n\ts_add_u32 %[t], %[ho], 131072\n\tbuffer_load_dwordx4 %[so], s[48:51], %[t] offen lds\n\ts_add_u32 m0, %[stg], 9216\n\ts_add_u32 %[t], %[ho], 147456\n\t" \
          "buffer_load_dwordx4 %[so], s[48:51], %[t] offen lds\n\ts_add_u32 m0, %[stg], 10240\n\ts_add_u32 %[t], %[ho], 163840\n\tbuffer_load_dwordx4 %[so], s[48:51], %[t] offen lds\n\ts_add_u32 m0, %[stg], 11264\n\ts_add_u32 %[t], %[ho], 180224\n\t" \
          "buffer_load_dwordx4 %[so], s[48:51], %[t] offen lds\n\ts_add_u32 m0, %[stg], 12288\n\ts_add_u32 %[t], %[ho], 196608\n\tbuffer_load_dwordx4 %[so], s[48:51], %[t] offen lds\n\ts_add_u32 m0, %[stg], 13312\n\ts_add_u32 %[t], %[ho], 212992\n\t" \
          "buffer_load_dwordx4 %[so], s[48:51], %[t] offen lds\n\ts_add_u32 m0, %[stg], 14336\n\ts_add_u32 %[t], %[ho], 229376\n\tbuffer_load_dwordx4 %[so], s[48:51], %[t] offen lds\n\ts_add_u32 m0, %[stg], 15360\n\ts_add_u32 %[t], %[ho], 245760\n\t" \
          "buffer_load_dwordx4 %[so], s[48:51], %[t] offen lds\n\ts_sub_u32 %[n], %[n], 1\n\ts_cmp_eq_u32 %[n], 0\n\ts_cbranch_scc1 Lw_end%=\n\tLw_5_%=:\n\ts_waitcnt vmcnt(16)\n\t" \
          "ds_read_b64_tr_b16 a[160:161], %[ob] offset:16384\n\tds_read_b64_tr_b16 a[162:163], %[ob] offset:16512\n\tds_read_b64_tr_b16 a[164:165], %[ob] offset:24576\n\tds_read_b64_tr_b16 a[166:167], %[ob] offset:24704\n\tds_read_b64_tr_b16 a[168:169], %[ob] offset:16416\n\tds_read_b64_tr_b16 a[170:171], %[ob] offset:16544\n\t" \
          "ds_read_b64_tr_b16 a[172:173], %[ob] offset:24608\n\tds_read_b64_tr_b16 a[174:175], %[ob] offset:24736\n\tds_read_b64_tr_b16 a[176:177], %[ob] offset:17408\n\tds_read_b64_tr_b16 a[178:179], %[ob] offset:17536\n\tds_read_b64_tr_b16 a[180:181], %[ob] offset:25600\n\tds_read_b64_tr_b16 a[182:183], %[ob] offset:25728\n\t" \
          "ds_read_b64_tr_b16 a[184:185], %[ob] offset:17440\n\tds_read_b64_tr_b16 a[186:187], %[ob] offset:17568\n\tds_read_b64_tr_b16 a[188:189], %[ob] offset:25632\n\tds_read_b64_tr_b16 a[190:191], %[ob] offset:25760\n\ts_cmp_gt_u32 %[n], 2\n\ts_cselect_b32 s50, s50, 0\n\t" \
          "s_cmp_gt_u32 %[n], 6\n\ts_cselect_b32 %[ho], 0x3, 0x7\n\ts_lshl_b32 %[ho], %[ho], 20\n\ts_xor_b32 %[ho], %[ho], %[hx]\n\ts_waitcnt lgkmcnt(0)\n\ts_add_u32 m0, %[stg], 16384\n\t" \
          "s_add_u32 %[t], %[ho], 0\n\tbuffer_load_dwordx4 %[so], s[48:51], %[t] offen lds\n\ts_add_u32 m0, %[stg], 17408\n\ts_add_u32 %[t], %[ho], 16384\n\tbuffer_load_dwordx4 %[so], s[48:51], %[t] offen lds\n\ts_add_u32 m0, %[stg], 18432\n\t" \
          "s_add_u32 %[t], %[ho], 32768\n\tbuffer_load_dwordx4 %[so], s[48:51], %[t] offen lds\n\ts_add_u32 m0, %[stg], 19456\n\ts_add_u32 %[t], %[ho], 49152\n\tbuffer_load_dwordx4 %[so], s[48:51], %[t] offen lds\n\ts_add_u32 m0, %[stg], 20480\n\t" \
          "s_add_u32 %[t], %[ho], 65536\n\tbuffer_load_dwordx4 %[so], s[48:51], %[t] offen lds\n\ts_add_u32 m0, %[stg], 21504\n\ts_add_u32 %[t], %[ho], 81920\n\tbuffer_load_dwordx4 %[so], s[48:51], %[t] offen lds\n\ts_add_u32 m0, %[stg], 22528\n\t" \
          "s_add_u32 %[t], %[ho], 98304\n\tbuffer_load_dwordx4 %[so], s[48:51], %[t] offen lds\n\ts_add_u32 m0, %[stg], 23552\n\ts_add_u32 %[t], %[ho], 114688\n\tbuffer_load_dwordx4 %[so], s[48:51], %[t] offen lds\n\ts_add_u32 m0, %[stg], 24576\n\t" \
          "s_add_u32 %[t], %[ho], 131072\n\tbuffer_load_dwordx4 %[so], s[48:51], %[t] offen lds\n\ts_add_u32 m0, %[stg], 25600\n\ts_add_u32 %[t], %[ho], 147456\n\tbuffer_load_dwordx4 %[so], s[48:51], %[t] offen lds\n\ts_add_u32 m0, %[stg], 26624\n\t" \
          "s_add_u32 %[t], %[ho], 163840\n\tbuffer_load_dwordx4 %[so], s[48:51], %[t] offen lds\n\ts_add_u32 m0, %[stg], 27648\n\ts_add_u32 %[t], %[ho], 180224\n\tbuffer_load_dwordx4 %[so], s[48:51], %[t] offen lds\n\ts_add_u32 m0, %[stg], 28672\n\t" \
          "s_add_u32 %[t], %[ho], 196608\n\tbuffer_load_dwordx4 %[so], s[48:51], %[t] offen lds\n\ts_add_u32 m0, %[stg], 29696\n\ts_add_u32 %[t], %[ho], 212992\n\tbuffer_load_dwordx4 %[so], s[48:51], %[t] offen lds\n\ts_add_u32 m0, %[stg], 30720\n\t" \
          "s_add_u32 %[t], %[ho], 229376\n\tbuffer_load_dwordx4 %[so], s[48:51], %[t] offen lds\n\ts_add_u32 m0, %[stg], 31744\n\ts_add_u32 %[t], %[ho], 245760\n\tbuffer_load_dwordx4 %[so], s[48:51], %[t] offen lds\n\ts_sub_u32 %[n], %[n], 1\n\t" \
          "s_cmp_eq_u32 %[n], 0\n\ts_cbranch_scc1 Lw_end%=\n\tLw_6_%=:\n\ts_waitcnt vmcnt(16)\n\tbuffer_load_dwordx4 %[rec], %[ro], %[rr], 0 offen sc1\n\tds_read_b64_tr_b16 a[192:193], %[ob] offset:0\n\t" \
          "ds_read_b64_tr_b16 a[194:195], %[ob] offset:128\n\tds_read_b64_tr_b16 a[196:197], %[ob] offset:8192\n\tds_read_b64_tr_b16 a[198:199], %[ob] offset:8320\n\tds_read_b64_tr_b16 a[200:201], %[ob] offset:32\n\tds_read_b64_tr_b16 a[202:203], %[ob] offset:160\n\tds_read_b64_tr_b16 a[204:205], %[ob] offset:8224\n\t" \
          "ds_read_b64_tr_b16 a[206:207], %[ob] offset:8352\n\tds_read_b64_tr_b16 a[208:209], %[ob] offset:1024\n\tds_read_b64_tr_b16 a[210:211], %[ob] offset:1152\n\tds_read_b64_tr_b16 a[212:213], %[ob] offset:9216\n\tds_read_b64_tr_b16 a[214:215], %[ob] offset:9344\n\tds_read_b64_tr_b16 a[216:217], %[ob] offset:1056\n\t" \
          "ds_read_b64_tr_b16 a[218:219], %[ob] offset:1184\n\tds_read_b64_tr_b16 a[220:221], %[ob] offset:9248\n\tds_read_b64_tr_b16 a[222:223], %[ob] offset:9376\n\ts_cmp_gt_u32 %[n], 2\n\ts_cselect_b32 s50, s50, 0\n\ts_cmp_gt_u32 %[n], 6\n\t" \
          "s_cselect_b32 %[ho], 0x0, 0x4\n\ts_lshl_b32 %[ho], %[ho], 20\n\ts_xor_b32 %[ho], %[ho], %[hx]\n\ts_waitcnt lgkmcnt(0)\n\ts_add_u32 m0, %[stg], 0\n\ts_add_u32 %[t], %[ho], 0\n\t" \
          "buffer_load_dwordx4 %[so], s[48:51], %[t] offen lds\n\ts_add_u32 m0, %[stg], 1024\n\ts_add_u32 %[t], %[ho], 16384\n\tbuffer_load_dwordx4 %[so], s[48:51], %[t] offen lds\n\ts_add_u32 m0, %[stg], 2048\n\ts_add_u32 %[t], %[ho], 32768\n\t" \
          "buffer_load_dwordx4 %[so], s[48:51], %[t] offen lds\n\ts_add_u32 m0, %[stg], 3072\n\ts_add_u32 %[t], %[ho], 49152\n\tbuffer_load_dwordx4 %[so], s[48:51], %[t] offen lds\n\ts_add_u32 m0, %[stg], 4096\n\ts_add_u32 %[t], %[ho], 65536\n\t" \
          "buffer_load_dwordx4 %[so], s[48:51], %[t] offen lds\n\ts_add_u32 m0, %[stg], 5120\n\ts_add_u32 %[t], %[ho], 81920\n\tbuffer_load_dwordx4 %[so], s[48:51], %[t] offen lds\n\ts_add_u32 m0, %[stg], 6144\n\ts_add_u32 %[t], %[ho], 98304\n\t" \
          "buffer_load_dwordx4 %[so], s[48:51], %[t] offen lds\n\ts_add_u32 m0, %[stg], 7168\n\ts_add_u32 %[t], %[ho], 114688\n\tbuffer_load_dwordx4 %[so], s[48:51], %[t] offen lds\n\ts_add_u32 m0, %[stg], 8192\n\ts_add_u32 %[t], %[ho], 131072\n\t" \
          "buffer_load_dwordx4 %[so], s[48:51], %[t] offen lds\n\ts_add_u32 m0, %[stg], 9216\n\ts_add_u32 %[t], %[ho], 147456\n\tbuffer_load_dwordx4 %[so], s[48:51], %[t] offen lds\n\ts_add_u32 m0, %[stg], 10240\n\ts_add_u32 %[t], %[ho], 163840\n\t" \
          "buffer_load_dwordx4 %[so], s[48:51], %[t] offen lds\n\ts_add_u32 m0, %[stg], 11264\n\ts_add_u32 %[t], %[ho], 180224\n\tbuffer_load_dwordx4 %[so], s[48:51], %[t] offen lds\n\ts_add_u32 m0, %[stg], 12288\n\ts_add_u32 %[t], %[ho], 196608\n\t" \
          "buffer_load_dwordx4 %[so], s[48:51], %[t] offen lds\n\ts_add_u32 m0, %[stg], 13312\n\ts_add_u32 %[t], %[ho], 212992\n\tbuffer_load_dwordx4 %[so], s[48:51], %[t] offen lds\n\ts_add_u32 m0, %[stg], 14336\n\ts_add_u32 %[t], %[ho], 229376\n\t" \
          "buffer_load_dwordx4 %[so], s[48:51], %[t] offen lds\n\ts_add_u32 m0, %[stg], 15360\n\ts_add_u32 %[t], %[ho], 245760\n\tbuffer_load_dwordx4 %[so], s[48:51], %[t] offen lds\n\ts_sub_u32 %[n], %[n], 1\n\ts_cmp_eq_u32 %[n], 0\n\t" \
          "s_cbranch_scc1 Lw_end%=\n\tLw_7_%=:\n\ts_waitcnt vmcnt(16)\n\tds_read_b64_tr_b16 a[224:225], %[ob] offset:16384\n\tds_read_b64_tr_b16 a[226:227], %[ob] offset:16512\n\tds_read_b64_tr_b16 a[228:229], %[ob] offset:24576\n\t" \
          "ds_read_b64_tr_b16 a[230:231], %[ob] offset:24704\n\tds_read_b64_tr_b16 a[232:233], %[ob] offset:16416\n\tds_read_b64_tr_b16 a[234:235], %[ob] offset:16544\n\tds_read_b64_tr_b16 a[236:237], %[ob] offset:24608\n\tds_read_b64_tr_b16 a[238:239], %[ob] offset:24736\n\tds_read_b64_tr_b16 a[240:241], %[ob] offset:17408\n\t" \
          "ds_read_b64_tr_b16 a[242:243], %[ob] offset:17536\n\tds_read_b64_tr_b16 a[244:245], %[ob] offset:25600\n\tds_read_b64_tr_b16 a[246:247], %[ob] offset:25728\n\tds_read_b64_tr_b16 a[248:249], %[ob] offset:17440\n\tds_read_b64_tr_b16 a[250:251], %[ob] offset:17568\n\tds_read_b64_tr_b16 a[252:253], %[ob] offset:25632\n\t" \
          "ds_read_b64_tr_b16 a[254:255], %[ob] offset:25760\n\ts_cmp_gt_u32 %[n], 2\n\ts_cselect_b32 s50, s50, 0\n\ts_cmp_gt_u32 %[n], 6\n\ts_cselect_b32 %[ho], 0x1, 0x5\n\ts_lshl_b32 %[ho], %[ho], 20\n\t" \
          "s_xor_b32 %[ho], %[ho], %[hx]\n\ts_waitcnt lgkmcnt(0)\n\ts_add_u32 m0, %[stg], 16384\n\ts_add_u32 %[t], %[ho], 0\n\tbuffer_load_dwordx4 %[so], s[48:51], %[t] offen lds\n\ts_add_u32 m0, %[stg], 17408\n\t" \
          "s_add_u32 %[t], %[ho], 16384\n\tbuffer_load_dwordx4 %[so], s[48:51], %[t] offen lds\n\ts_add_u32 m0, %[stg], 18432\n\ts_add_u32 %[t], %[ho], 32768\n\tbuffer_load_dwordx4 %[so], s[48:51], %[t] offen lds\n\ts_add_u32 m0, %[stg], 19456\n\t" \
          "s_add_u32 %[t], %[ho], 49152\n\tbuffer_load_dwordx4 %[so], s[48:51], %[t] offen lds\n\ts_add_u32 m0, %[stg], 20480\n\ts_add_u32 %[t], %[ho], 65536\n\tbuffer_load_dwordx4 %[so], s[48:51], %[t] offen lds\n\ts_add_u32 m0, %[stg], 21504\n\t" \
          "s_add_u32 %[t], %[ho], 81920\n\tbuffer_load_dwordx4 %[so], s[48:51], %[t] offen lds\n\ts_add_u32 m0, %[stg], 22528\n\ts_add_u32 %[t], %[ho], 98304\n\tbuffer_load_dwordx4 %[so], s[48:51], %[t] offen lds\n\ts_add_u32 m0, %[stg], 23552\n\t" \
          "s_add_u32 %[t], %[ho], 114688\n\tbuffer_load_dwordx4 %[so], s[48:51], %[t] offen lds\n\ts_add_u32 m0, %[stg], 24576\n\ts_add_u32 %[t], %[ho], 131072\n\tbuffer_load_dwordx4 %[so], s[48:51], %[t] offen lds\n\ts_add_u32 m0, %[stg], 25600\n\t" \
          "s_add_u32 %[t], %[ho], 147456\n\tbuffer_load_dwordx4 %[so], s[48:51], %[t] offen lds\n\ts_add_u32 m0, %[stg], 26624\n\ts_add_u32 %[t], %[ho], 163840\n\tbuffer_load_dwordx4 %[so], s[48:51], %[t] offen lds\n\ts_add_u32 m0, %[stg], 27648\n\t" \
          "s_add_u32 %[t], %[ho], 180224\n\tbuffer_load_dwordx4 %[so], s[48:51], %[t] offen lds\n\ts_add_u32 m0, %[stg], 28672\n\ts_add_u32 %[t], %[ho], 196608\n\tbuffer_load_dwordx4 %[so], s[48:51], %[t] offen lds\n\ts_add_u32 m0, %[stg], 29696\n\t" \
          "s_add_u32 %[t], %[ho], 212992\n\tbuffer_load_dwordx4 %[so], s[48:51], %[t] offen lds\n\ts_add_u32 m0, %[stg], 30720\n\ts_add_u32 %[t], %[ho], 229376\n\tbuffer_load_dwordx4 %[so], s[48:51], %[t] offen lds\n\ts_add_u32 m0, %[stg], 31744\n\t" \
          "s_add_u32 %[t], %[ho], 245760\n\tbuffer_load_dwordx4 %[so], s[48:51], %[t] offen lds\n\ts_sub_u32 %[n], %[n], 1\n\ts_cmp_eq_u32 %[n], 0\n\ts_cbranch_scc1 Lw_end%=\n\ts_branch Lw_4_%=\n\t" \
          "Lw_end%=:\n\ts_waitcnt vmcnt(0)\n\ts_barrier\n\t"
#define VO_CLOB "v176", "v177", "v178", "v179", "v180", "v181", "v182", "v183", "v184", "v185", "v186", "v187", "v188", "v189", "v190", "v191", "v192", "v193", "v194", "v195", "v196", "v197", "v198", "v199", "v200", "v201", "v202", "v203", "v204", "v205", "v206", "v207", "v208", "v209", "v210", "v211", "v212", "v213", "v214", "v215", "v216", "v217", "v218", "v219", "v220", "v221", "v222", "v223", "v224", "v225", "v226", "v227", "v228", "v229", "v230", "v231", "v232", "v233", "v234", "v235", "v236", "v237", "v238", "v239", "v240", "v241", "v242", "v243", "v244", "v245", "v246", "v247", "v248", "v249", "v250", "v251", "v252", "v253", "v254", "v255", "s48", "s49", "s50", "s51", "s52", "s53"
__global__ void __launch_bounds__(256, 1) k_fused(FParams P) {
  __shared__ __attribute__((aligned(16))) char Lraw[LDS_ALL];
  unsigned long long pc0;
  asm volatile("s_getpc_b64 %0\nLfused_pc0:" : "=s"(pc0));
  const int w = __builtin_amdgcn_readfirstlane(threadIdx.x >> 6);
  unsigned cz, pk; unsigned long long tag;
  {
    int clen; asm volatile("s_mov_b32 %0, Lfused_end-Lfused_pc0" : "=s"(clen));
    const int o = ((blockIdx.x >> 3) * 4 + w) * 1024 + (threadIdx.x & 7) * 128;
    asm volatile("s_nop 4\n\tbuffer_load_dword %0, %1, %2, 0 offen" : "=v"(cz) : "v"(o), "s"(__builtin_amdgcn_make_buffer_rsrc((void*)pc0, 0, clen, 0x00020000))); }
  if ((blockIdx.x & 7) >= 3) {
    asm volatile("Lfused_worker:");
    worker(P, cz, Lraw, w);
    asm volatile("Lfused_end:");
    return;
  }
  unsigned* tmo = &P.ctl->tmo[0]; unsigned long long* prof = P.prof; (void)prof;
  const int xq = blockIdx.x & 7, slot = blockIdx.x >> 3, nq = xq < 2 ? 12 : 8;
  const int b = slot < nq ? 12 * xq + slot : NQK + 20 * xq + (slot - nq);
  const int alw = b < NQK ? (b % 9 == 0 ? (b / 9) * 4 + w : -1) : (b == 33 ? 16 + w : (b == 95 ? 20 + w : -1));
  const int xr = xq;
  const unsigned long long alx = (unsigned)__builtin_amdgcn_readfirstlane(alw >= 0 ? -1 : 0);
  const __amdgpu_buffer_rsrc_t rs_m = __builtin_amdgcn_make_buffer_rsrc(P.ring, 0, RSIZE, 0x00020000), rs_r = __builtin_amdgcn_make_buffer_rsrc(P.rdy, 0, RDY_SIZE, 0x00020000);
  const __amdgpu_buffer_rsrc_t rs_c = __builtin_amdgcn_make_buffer_rsrc(P.ctl, 0, sizeof(Ctl), 0x00020000);
  v4u rdy;
#define LANE_CONSTS const int lane = opaque_lane(), tid = 64 * w + lane, l15 = lane & 15, lq = lane >> 4, idx = (lane & 1) ? 0xEEEE : 0x4444; \
  const int mem_lane_off = (((lq >> 1) * 2 + (l15 >> 3)) * 16 + (lq & 1) * 8 + (l15 & 7)) * 16 + dbl(w * 4096); \
  const int wrow = 8 * (lq >> 1) + 2 * (lq & 1) + 4 * (l15 >> 3) + 16 * ((l15 >> 2) & 1), wsw = (wrow >> 1) & 3
  unsigned tA = 0;
  if (b < NQK) {
    const int h = b;
    LANE_CONSTS;
    v32u wa[8], wv_[7];
    const unsigned lb0 = (unsigned)(size_t)Lraw;
    const unsigned own = lb0 + w * 40960 + lane * 16;
    unsigned kfl;
    {
      const __amdgpu_buffer_rsrc_t rq = __builtin_amdgcn_make_buffer_rsrc((void*)(P.Wq + h * DHA + 64 * (w >> 1)), 0, 0x7fffffff, 0x00020000), rk = __builtin_amdgcn_make_buffer_rsrc((void*)(P.Wk + h * DHA + 64 * (w >> 1)), 0, 0x7fffffff, 0x00020000);
      const int go = (lane >> 3) * HDA * 2 + (((((lane & 7) >> 1) ^ ((lane >> 4) & 3)) << 1) | (lane & 1)) * 16;
      unsigned rd[4];
#pragma unroll
      for (int t = 0; t < 4; ++t) rd[t] = lb0 + w * 40960 + wrow * 128 + (l15 & 3) * 8 + ((t ^ (2 * (~w & 1)) ^ wsw) << 5);
#define QK_NTq ""
#define QK_NTk ""
      int t_;
      asm volatile(QK_LOADER : QK_DEFW, [t] "=&s"(t_), [rec] "=&v"(rdy), [kf] "=&v"(kfl)
                   : [ko] "v"(lane < 2 ? (int)offsetof(Ctl, prg) + lane * 4096 + h * 128 : 0x7ffffff0), [rc] "s"(rs_c), [rd0] "v"(rd[0]), [rd1] "v"(rd[1]), [rd2] "v"(rd[2]), [rd3] "v"(rd[3]), [own] "v"(own), [go] "v"(go), [ro] "v"(lane * 128), [rr] "s"(rs_r), [rq] "s"(rq), [rk] "s"(rk), [lw] "s"(lb0 + w * 40960), [kb] "s"((8 * (w & 1) + 2 * xr + (w >> 1)) * 0x80000) : "memory", "scc");
    }
    asm volatile("" :: "v"(cz));
    { const unsigned long long t0 = rdy_tag(rdy); if ((~__ballot(rdy.x == (unsigned)t0 && rdy.y == (unsigned)(t0 >> 32)) << 4) != 0) kfl = 0; }
    const unsigned long long parq = rdy_wait(P, rdy, tag, pk);
    const int rpl = (int)(parq >> 63) * 8192;
    const __amdgpu_buffer_rsrc_t rs_x = __builtin_amdgcn_make_buffer_rsrc(P.qkvA, 0, QSIZE, 0x00020000);
    const int xk = qoff(parq, 16 + (h >> 1)) + (h & 1) * 4096, xv = qoff(parq, 32 + (h >> 1)) + (h & 1) * 4096;
    const int xm = kvoff(pk, h) + w * 512;
#define PF_ADDR const int tz = opaque_lane()
#define KV_SEGS(v) (min(__builtin_amdgcn_readlane(v, 0), __builtin_amdgcn_readlane(v, 1)) >> 1)
#define WAIT_KV(sg) do { for (unsigned spins = 0;; ++spins) { const unsigned v_ = ld_agent(&P.ctl->prg[(tz & 1) * 32 + h][0]); kvok = KV_SEGS(v_); if (kvok > (sg)) break; \
        if (spin_fail(spins, tmo, 3u)) break; __builtin_amdgcn_s_sleep(8); } } while (0)
    int kvok = 0;
#define LOAD_KV(sg) do { const int so_ = (sg) * 2 * QBLK + w * 1024; \
        _Pragma("unroll") for (int q = 0; q < 4; ++q) \
          asm volatile("s_mov_b32 m0, %0\n\ts_nop 4\n\tbuffer_load_dwordx4 %1, %2, %3 offen sc1 lds" :: "s"(lb0 + w * 40960 + QK_KV + q * 1024), "v"((q < 2 ? tz : tz ^ ((tz >> 1) & 8)) * 16), "s"(rs_x), \
                       "s"(so_ + (q & 1) * QBLK + ((q >> 1) ? xv : xk)) : "memory"); } while (0)
    unsigned pz = 0;
#define LD_ASYNC(dst, voff, rs, soff) asm volatile("s_nop 4\n\tbuffer_load_dword %0, %1, %2, %3 offen sc1" : "=v"(dst) : "v"(voff), "s"(rs), "s"(soff) : "memory")
    {
      PF_ADDR;
      kvok = KV_SEGS(kfl);
      if (kvok <= 0) WAIT_KV(0);
      LOAD_KV(0);
      LD_ASYNC(pz, kvok > 1 && tz < 32 ? tz * 128 : 0x7ffffff0, rs_x, (2 + (w & 1)) * QBLK + ((w >> 1) ? xv : xk));
    }
    const float QS = 0.08838834764831845f * 1.4426950408889634f;
    const unsigned j0 = lq == (l15 >> 2) && !(l15 & 1) ? 0x3f803f80u : 0u, j1 = lq == (l15 >> 2) && (l15 & 1) ? 0x3f803f80u : 0u;
    const v4u JK = {j0, j1, j0, j1};
    const unsigned vxp = lb0 + w * 40960 + QK_KV + 2048 + ((l15 & 3) * 16 + ((4 * lq + (l15 >> 2)) ^ ((l15 & 1) << 3))) * 16;
    const unsigned p1 = lb0 + (w ^ 1) * 40960 + lane * 16, p2 = lb0 + (w ^ 2) * 40960 + lane * 16, p3 = lb0 + (w ^ 3) * 40960 + lane * 16;
    unsigned pot = 0, d1p = 140;
    const int mlo = mem_lane_off - dbl(w * 4096) + (w & 1) * 16384 + (w >> 1) * 1024, hoff = dbl(h * 2048) + w * 512 + lane * 4;
    v4u vm = {0u, 0u, 0u, 0u};
    const int alo = lane < 16 && w == 3 ? ALM + lane * 128 : (lane >= 62 ? RECO + (lane - 62) * 4 : 0x7ffffff0);
    for (int s = 0; s < NSEG; ++s) {
      int mb = RBASE(s) + ROFF_M, vb = RBASE(s) + ROFF_V, ob = RBASE(s) + ROFF_O, mb0 = mb + xr * 2048;
      unsigned al, ann, fl; unsigned long long bad, bad1, b2, b3, po_;
      if (__builtin_expect(s == 1 && w == 3, 0)) {
        unsigned recD; rec_get(tA, recD, rs_m, mb + RECO, tmo); tA -= (recD & 0xffffu) + (recD >> 16);
      }
      { int rem = (int)(tA - rtnow());
        if (s == 0 || rem > 20000) tA -= rem; }
      tA = __builtin_amdgcn_readfirstlane(tA); d1p = __builtin_amdgcn_readfirstlane(d1p);
#define QK_STO
      { unsigned cnt; unsigned long long en;
        asm volatile(QK_STEP
          : "=&{v[136:167]}"(wv_[4]), "=&{v[168:199]}"(wv_[5]), "=&{v[200:231]}"(wv_[6]), "+{v[232:235]}"(vm), [m] "+v"(pz), "+v"(kfl), [al] "=&v"(al),
            [bad] "=&s"(bad), [bad1] "=&s"(bad1), [b2] "=&s"(b2), [b3] "=&s"(b3), [po] "=&s"(po_), [cnt] "=&s"(cnt), [en] "=&s"(en), [ann] "=&s"(ann), [fl] "=&s"(fl) QK_STO
          : "{a[0:31]}"(wa[0]), "{a[32:63]}"(wa[1]), "{a[64:95]}"(wa[2]), "{a[96:127]}"(wa[3]), "{a[128:159]}"(wa[4]), "{a[160:191]}"(wa[5]), "{a[192:223]}"(wa[6]), "{a[224:255]}"(wa[7]),
            "{v[8:39]}"(wv_[0]), "{v[40:71]}"(wv_[1]), "{v[72:103]}"(wv_[2]), "{v[104:135]}"(wv_[3]),
            [own] "v"(own), [p1] "v"(p1), [p2] "v"(p2), [p3] "v"(p3), [vxp] "v"(vxp), [ml0] "v"(mlo), [ml1] "v"(mlo ^ 1024), [jk] "v"(JK), [ho] "v"(hoff), [idx] "v"(idx), [alo] "v"(alo),
            [rsm] "s"(rs_m), [rsc] "s"(rs_c), [mb] "s"(mb), [vb] "s"(vb), [ob] "s"(ob), [rb] "s"(RBASE(s + 2) + ROFF_O), [twin] "s"(0xccccccccccccccccull),
            [one] "s"(0x3f803f80u), [qs] "s"(QS), [qs2] "s"((unsigned long long)__float_as_uint(QS)), [hi] "s"(0x1p40f), [lo] "s"(0x1p-40f), [th] "s"(40.f), [t] "s"(tA), [w] "s"(w), [ald0] "s"(d1p + TP(P.sh0, SH0)),
            [tmo] "s"((int)offsetof(Ctl, tmo)), [mb0] "s"(mb0), [mb1] "s"(mb0 ^ 2048), [mb2] "s"(mb0 ^ 4096), [mb3] "s"(mb0 ^ 6144), [ald] "s"(d1p - TP(P.sh1, SH1)), [als] "s"(mb + ALM + (alw & 15) * 128), [alx] "s"(alx)
          : "vcc", "scc", "s48", "s49", "s50", "s51", "s52", "s53", "memory"); }
      {
        const v4u Km = {wv_[4][QK_KM], wv_[4][QK_KM + 1], wv_[4][QK_KM + 2], wv_[4][QK_KM + 3]};
        if (__builtin_expect(bad != 0, 0)) {
#pragma unroll 1
          for (unsigned fails = 0; !__all((vm.x > vm.z ? vm.x : vm.z) != 0xffffffffu); ++fails) {
            __builtin_amdgcn_s_sleep(2);
            vm.x = __builtin_amdgcn_raw_buffer_load_b32(rs_m, hoff, vb, 16); vm.z = __builtin_amdgcn_raw_buffer_load_b32(rs_m, hoff, vb + 256, 16);
            if (spin_fail(fails, tmo, 1u)) break;
          }
          float sx[4], sm[2];
#pragma unroll
          for (int e = 0; e < 4; ++e) sx[e] = __uint_as_float(wv_[6][QK_SX + e]);
          sm[0] = __uint_as_float(wv_[4][QK_SM]); sm[1] = __uint_as_float(wv_[4][QK_SM + 1]);
          float mx = fmaxf(fmaxf(sx[0], sx[1]), fmaxf(sx[2], sx[3]));
          mx = fmaxf(mx, __int_as_float(__builtin_amdgcn_update_dpp(0, __float_as_int(mx), 0x4E, 0xf, 0xf, false)));
          mx = xmax32(xmax16(fmaxf(mx, fmaxf(sm[0], sm[1]))));
          if (b2 == 0) mx = 0.f;
          v4u px; px.x = pk2(__builtin_amdgcn_exp2f(sx[0] - mx), __builtin_amdgcn_exp2f(sx[1] - mx)); px.y = pk2(__builtin_amdgcn_exp2f(sx[2] - mx), __builtin_amdgcn_exp2f(sx[3] - mx));
          px.z = __builtin_amdgcn_update_dpp(0, px.x, 0x4E, 0xf, 0xf, false); px.w = __builtin_amdgcn_update_dpp(0, px.y, 0x4E, 0xf, 0xf, false);
          const v2u pm = {pk2(__builtin_amdgcn_exp2f(sm[0] - mx), __builtin_amdgcn_exp2f(sm[1] - mx)), 0u};
          const v4u on = {0x3f803f80u, 0x3f803f80u, 0x3f803f80u, 0x3f803f80u};
          const v4u vx0 = {wv_[6][QK_VX], wv_[6][QK_VX + 1], wv_[6][QK_VX + 2], wv_[6][QK_VX + 3]}, vx1 = {wv_[6][QK_VX + 4], wv_[6][QK_VX + 5], wv_[6][QK_VX + 6], wv_[6][QK_VX + 7]};
          f32x4 c0, c1, ls;
          asm volatile("s_nop 1\n\t"
                       "v_mfma_f32_16x16x32_bf16 %0, %3, %4, 0\n\t"
                       "v_mfma_f32_16x16x32_bf16 %1, %3, %5, 0\n\t"
                       "v_mfma_f32_16x16x32_bf16 %2, %3, %6, 0\n\t"
                       "s_nop 7\n\t"
                       "v_mfma_f32_16x16x16_bf16 %0, %7, v[232:233], %0\n\t"
                       "v_mfma_f32_16x16x16_bf16 %1, %7, v[234:235], %1\n\t"
                       "v_mfma_f32_16x16x16_bf16 %2, %7, v[168:169], %2\n\t"
                       "s_nop 7"
                       : "=&v"(c0), "=&v"(c1), "=&v"(ls) : "v"(px), "v"(vx0), "v"(vx1), "{v[168:171]}"(on), "v"(pm), "{v[232:235]}"(vm));
          const float i0 = __builtin_amdgcn_rcpf(ls[0]), i1 = __builtin_amdgcn_rcpf(ls[1]);
          __builtin_amdgcn_raw_buffer_store_b32(nosent(pk2(c0[0] * i0, c1[0] * i0)), rs_m, hoff, ob, 16);
          __builtin_amdgcn_raw_buffer_store_b32(nosent(pk2(c0[1] * i1, c1[1] * i1)), rs_m, hoff + 256, ob, 16);
          po_ = rtnow();
        }
        SB0;
        const unsigned po = __builtin_amdgcn_readfirstlane((unsigned)po_);
        fl = __builtin_amdgcn_readfirstlane(fl | (bad != 0));
        if (QGAP) __builtin_amdgcn_s_sleep(QGAP);
        SB0;
        __builtin_amdgcn_raw_buffer_store_b32(po - tA, rs_m, (h | (w ^ 3) | opaque_lane() | fl) == 0 ? RECO + 128 : 0x7ffffff0, mb, 16);
      { const int lz = opaque_lane(), kb = xm + s * 2 * QBLK, rb = RBASE(s + 2);
        __builtin_amdgcn_raw_buffer_store_b32(vm.x, rs_x, lz * 4 + 2048, kb, 16); __builtin_amdgcn_raw_buffer_store_b32(vm.z, rs_x, lz * 4 + 2304, kb, 16);
        __builtin_amdgcn_raw_buffer_store_b128((v4u){nosent(Km.x), nosent(Km.y), nosent(Km.z), nosent(Km.w)}, rs_x, (lz & 2) ? 0x7ffffff0 : (2 * (lz >> 2) + (lz & 1)) * 16, kb, 16);
        __builtin_amdgcn_raw_buffer_store_b128((v4u){~0u, ~0u, ~0u, ~0u}, rs_m, (alw & 3) != 0 ? 0x7ffffff0 : lz < 32 ? ROFF_M + ALM + alw * 128 + lz * 16 : (lz == 32 && b == 0 ? ROFF_M + RECO + 128 : 0x7ffffff0), rb, 16); }
        asm volatile("s_waitcnt vmcnt(%1)" : "+v"(al) : "i"(QK_ALW + 4) : "memory");
        pot = po;
      if (s < NSEG - 1) {
        unsigned recD = __builtin_amdgcn_readlane(al, 63);
        tA = __builtin_amdgcn_readlane(al, 62);
        if (s > 0) {
          if (__builtin_expect(tA == 0xffffffffu || recD == 0xffffffffu, 0)) rec_get(tA, recD, rs_m, mb + RECO, tmo);
          d1p = recD & 0xffffu;
          unsigned tB = tA - (recD >> 16);
          al_eval(al, tB);
          if (__builtin_expect((int)(pot + TP(P.sh3, SH3) - tB) > 0, 0)) tB = pot + TP(P.sh3, SH3);
          tA = tB + (recD >> 16);
        } else tA = pot;
        PF_ADDR;
        kvok = max(kvok, KV_SEGS(kfl));
        if (__builtin_expect(kvok <= s + 1, 0)) WAIT_KV(s + 1);
        LOAD_KV(s + 1);
        LD_ASYNC(kfl, kvok < s + 5 && kvok < NSEG && tz < 2 ? tz * 4096 : 0x7ffffff0, rs_c, (int)offsetof(Ctl, prg) + h * 128);
        const int po2 = s + 2 < kvok && !(tz & 32) ? tz * 128 : 0x7ffffff0;
        LD_ASYNC(pz, po2, rs_x, (2 * (s + 2) + (w & 1)) * QBLK + ((w >> 1) ? xv : xk));
      }
      }
    }
    check_in(P, tag, w);
  } else {
    LANE_CONSTS;
    const unsigned c00 = (unsigned)now(), t00 = rtnow();
    const int c = b - NQK, cg = c >> 1, rh = c & 1;
    v32u wa[8], wv_[4];
#define VCH(j) (8 * (w & 1) + 2 * ((j) ^ xq) + (w >> 1))
#define OHEAD(i) (4 * ((i) ^ (2 * xq)) + w)
    {
      const unsigned stg = (unsigned)(size_t)Lraw + w * 32768;
      const unsigned vb = stg + wrow * 128 + (l15 & 3) * 8 + ((w ^ 3 ^ wsw) << 5);
      const __amdgpu_buffer_rsrc_t rv = __builtin_amdgcn_make_buffer_rsrc((void*)(P.Wv + 64 * c), 0, DM * HDA * 2 - 128 * c, 0x00020000);
      int t_, ho, n_;
      asm volatile(VO_LOADER : VO_DEFW, [t] "=&s"(t_), [ho] "=&s"(ho), [n] "=&s"(n_), [rec] "=&v"(rdy)
        : [sv] "v"((lane >> 3) * HDA * 2 + (((((lane & 7) >> 1) ^ ((lane >> 4) & 3)) << 1) | (lane & 1)) * 16), [so] "v"((lane >> 3) * DM * 2 + (lane & 7) * 16), [vb0] "v"(vb), [vb1] "v"(vb ^ 32), [vb2] "v"(vb ^ 64), [vb3] "v"(vb ^ 96),
          [ob] "v"(stg + (32 * (lq >> 1) + 2 * (lq & 1) + 4 * (l15 >> 3) + 16 * ((l15 >> 2) & 1)) * 128 + (cg & 1) * 64 + (l15 & 3) * 8),
          [ro] "v"(lane * 128), [rr] "s"(rs_r), [stg] "s"(stg), [rv] "s"(rv), [vk] "s"(VCH(0) * 0x80000), [wo] "s"(P.Wo + 64 * (cg >> 1)), [wsz] "s"(HDA * DM * 2 - 128 * (cg >> 1)), [hx] "s"(OHEAD(0) * 0x40000), [k0] "s"(c & 3)
        : "s48", "s49", "s50", "s51", "s52", "s53", "scc", "memory");
    }
    asm volatile("" :: "v"(cz));
    const int rpl = (int)(rdy_wait(P, rdy, tag, pk) >> 63) * 8192;
    const unsigned lb0 = (unsigned)(size_t)Lraw;
    unsigned vw[3];
#pragma unroll
    for (int t = 0; t < 3; ++t) vw[t] = lb0 + t * 2064 + ((t ^ w ^ 3) * 64 + lane) * 8;
    const unsigned vr = lb0 + tid * 8, rw = lb0 + 8192 + tid * 8, fa = lb0 + 16384;
    int oo[8];
#pragma unroll
    for (int i = 0; i < 8; ++i) oo[i] = dbl(OHEAD(i) * 2048) + ((lq * 2 + rh) * 16 + l15) * 16;
    int ga[4];
#pragma unroll
    for (int j = 0; j < 4; ++j) ga[j] = mem_lane_off - dbl(w * 4096) + dbl(VCH(j) * 1024);
    const int vo = dbl(c * 1024) + tid * 4, mo = dbl(c * 256) + (32 * (l15 >> 3) + 8 * lq + (l15 & 7)) * 4;
    const bool keeper = (c == 0 && w == 0);
    int d1 = TP(P.dmv, DMV) * 16, d2 = TP(P.dov, DOV) * 16, m1 = TP(P.im, IMARG) * 16, m2 = TP(P.im, IMARG) * 16;
    int LV = 70;
    bool prev_clean = false; int nbad = 0;
    unsigned recD = 140;
    const float tpc = 16.f * (float)(rtnow() - t00) / (float)((unsigned)now() - c00);
    int e1 = __builtin_amdgcn_readfirstlane((int)(TP(P.dmv, DMV) * tpc)), e2 = __builtin_amdgcn_readfirstlane((int)(TP(P.dov, DOV) * tpc));
    d1 = e1 + m1 + TP(P.dq2, DQ2) * 16; d2 = e2 + m2 + TP(P.dq, DQ) * 16;
    if (tid < 32) ((int*)(Lraw + 16384))[tid] = 0;
    const int alk = !keeper ? 0x7ffffff0 : (lane < 24 ? ALM + lane * 128 : (lane == 63 ? RECO + 128 : 0x7ffffff0));
    const int al1 = lane < 16 ? ALM + lane * 128 : 0x7ffffff0;
    const int rso = w == 1 ? ROFF_V + dbl(c * 1024) + lane * 16 : (w != 2 ? 0x7ffffff0 : (lane < 16 ? ROFF_M + dbl(c * 256) + lane * 16 :
                    (lane >= 32 && alw >= 0 ? ROFF_M + ALM + (alw - 6) * 128 + lane * 16 : (lane == 16 && c == 0 ? ROFF_M + RECO : 0x7ffffff0))));
    const int boot = c == 0 ? TP(P.pad, BOOT) & 255 : TP(P.pad, BOOT) >> 8;
    unsigned tB = 0; int mb = RBASE(0) + ROFF_M, ob = 0;
    for (int s = 0; s < NSEG; ++s) {
      unsigned alr, pm, fw; unsigned long long te; v2u rec;
      const int mbn = RBASE(s) + ROFF_M, d2r = recD >> 16;
#define VO_ST0
#define VO_ST1
#define VO_ST3
#define VO_STO
      { unsigned cnt; unsigned long long en;
        asm volatile(VO_BODY : [al] "=&v"(alr), [rec] "=&v"(rec), [te] "=&s"(te), [cnt] "=&s"(cnt), [en] "=&s"(en), [t] "+s"(tB), [ta] "=&s"(tA), [pm] "=&s"(pm), [fw] "=&s"(fw) VO_STO
          : VO_W, [oo0] "v"(oo[0]), [oo1] "v"(oo[1]), [oo2] "v"(oo[2]), [oo3] "v"(oo[3]), [oo4] "v"(oo[4]), [oo5] "v"(oo[5]), [oo6] "v"(oo[6]), [oo7] "v"(oo[7]), [idx] "v"(idx), [rw] "v"(rw), [mo] "v"(mo), [fa] "v"(fa), [l4] "v"(lane * 4), [al1] "v"(al1),
            [ga0] "v"(ga[0]), [ga1] "v"(ga[1]), [ga2] "v"(ga[2]), [ga3] "v"(ga[3]), [alo] "v"(alk), [vw0] "v"(vw[0]), [vw1] "v"(vw[1]), [vw2] "v"(vw[2]), [vr] "v"(vr), [vo] "v"(vo), [rso] "v"(rso), [rb] "s"(RBASE(s + 2)),
            [w] "s"(w), [rsm] "s"(rs_m), [rsc] "s"(rs_c), [ob] "s"(ob), [mb] "s"(mb), [mbn] "s"(mbn), [vb] "s"(RBASE(s) + ROFF_V), [rco] "s"(mbn + RECO), [tmo] "s"((int)offsetof(Ctl, tmo)), [s1] "s"(s),
            [lk] "s"(TP(P.lk1, LK1)), [als] "s"(mb + ALM + (alw & 31) * 128), [alx] "s"(alx), [dv] "s"(TP(P.dv, DVV)),
            [d2] "s"(d2r), [mn] "s"(s == 1 ? boot : 10), [shf] "s"(s == 1 ? boot : TP(P.sh4, SH4)), [c] "s"(c), [pw] "s"(__builtin_amdgcn_readfirstlane(s == 0 ? 1 : w ^ 3))
          : VO_CLOB, "vcc", "scc", "memory"); }
      const unsigned tv = __builtin_amdgcn_readfirstlane((unsigned)te);
      if (c == 2 && tid == 0 && s > 0) __hip_atomic_store((unsigned long long*)&P.ctl->prog[0], (unsigned)(s - 1) | ((unsigned long long)(rec.x + (rec.y & 0xffffu)) << 32), RLX_AGENT);
      if (keeper && s > 0) {
        const v4u kz = *(const v4u*)(Lraw + 16384 + 144); pm = kz.x; tB = kz.y; fw = kz.z; tA = kz.w;
        const unsigned long long f = __ballot((alr >> 16) == 1u);
        const bool kclean = __builtin_amdgcn_readfirstlane(fw) != s, okA = (f & 0xffff) == 0, okB = kclean && ((f >> 16) & 255) == 0;
        const int kx1 = __builtin_amdgcn_readlane(alr, 63), kx2 = __builtin_amdgcn_readfirstlane(pm - tB);
        {
          const int gain = s < 2 ? 0 : (s < 5 ? TP(P.gain0, GAIN0) : TP(P.gain, GAIN)), inc = s < 2 ? 0 : TP(P.inc, MINC);
          e1 += okA && kx1 != -1 ? (min(kx1 * 16 - e1, 320) * gain) >> 4 : 0;
          e2 += kclean ? (min(kx2 * 16 - e2, 320) * gain) >> 4 : 0;
          nbad = okA && okB ? 0 : nbad + 1;
          const int far = nbad >= 4 ? 2 * inc : 0;
          m1 = min(max(m1 + far + (!okA ? 0 : (okB ? -TP(P.dec, MDEC) - (m1 >> 3) : inc)), 0), 800);
          m2 = min(max(m2 + far + (okA ? -TP(P.dec, MDEC) - (m2 >> 3) : (prev_clean ? inc : 0)), 0), 800);
          d1 = e1 + m1 + TP(P.dq2, DQ2) * 16; d2 = e2 + m2 + TP(P.dq, DQ) * 16;
        }
        prev_clean = okB;
      }
      if (keeper) {
        const unsigned r1 = (d1 >> 4) + (s < 2 ? (TP(P.boot2, BOOT2) >> (8 * s)) & 255 : 0), r2 = (d2 >> 4) + (s < 2 ? (TP(P.boot3, BOOT3) >> (8 * s)) & 255 : 0);
        *(v2u*)(Lraw + 16384 + 128) = (v2u){r1 + r2, r1 | (r2 << 16)};
      }
      tB = tv; recD = 0;
      if (s > 0 && w == 3) {
        unsigned recA = __builtin_amdgcn_readfirstlane(rec.x); recD = __builtin_amdgcn_readfirstlane(rec.y);
        if (__builtin_expect(recA == 0xffffffffu || recD == 0xffffffffu, 0)) rec_get(recA, recD, rs_m, mbn + RECO, tmo);
        tB = recA - (recD >> 16);
        if ((int)(tB - tv) > 20000) tB = tv;
      }
      tB = __builtin_amdgcn_readfirstlane(tB); recD = __builtin_amdgcn_readfirstlane(recD);
      mb = mbn; ob = RBASE(s) + ROFF_O;
    }
    check_in(P, tag, w);
  }
}

extern "C" void kernel_launch(void* const* d_in, const int* in_sizes, int n_in,
                              void* d_out, int out_size, void* d_ws, size_t ws_size,
                              hipStream_t stream) {
  const u16* A_x = (const u16*)d_in[0];
  const u16* A_mem0 = (const u16*)d_in[1];
  const u16* A_Wq = (const u16*)d_in[2];
  const u16* A_Wk = (const u16*)d_in[3];
  const u16* A_Wv = (const u16*)d_in[4];
  const u16* A_Wo = (const u16*)d_in[5];
  const u16* B_x = (const u16*)d_in[7];
  const u16* B_wqkv = (const u16*)d_in[8];
  const u16* B_wo = (const u16*)d_in[9];
  u16* out = (u16*)d_out;

  char* ws = (char*)d_ws;
  size_t off = 0;
  auto alloc = [&](size_t bytes) { char* p = ws + off; off += (bytes + 255) & ~(size_t)255; return p; };
  char* rdy = alloc(RDY_SIZE);
  Ctl* ctl = (Ctl*)alloc(sizeof(Ctl));
  unsigned long long* prof = (unsigned long long*)alloc(96 * 4 * 72 * 8 + 8 * 256 * 16 + 64 * 384 * 8);
  off += (size_t)(-(long long)(size_t)(ws + off)) & (QPAGE - 1);
  char* ring = alloc(QPAGE);
  u16* qkvA = (u16*)alloc(QSIZE);

  FParams cp;
  cp.Ax = A_x; cp.mem0 = A_mem0; cp.Bx = B_x; cp.Wq = A_Wq; cp.Wk = A_Wk; cp.Wv = A_Wv; cp.Wo = A_Wo; cp.wqkvB = B_wqkv; cp.woB = B_wo;
  cp.qkvA = qkvA; cp.out = out; cp.ring = ring; cp.rdy = rdy;
  cp.ctl = ctl; cp.prof = prof;
  k_fused<<<256, 256, 0, stream>>>(cp);
}
```

```cpp
#include <hip/hip_runtime.h>
#include <hip/hip_bf16.h>
#include <stdint.h>
#include <stddef.h>

#define DQ 13
#define DQ2 12
#define DMV 2440
#define DOV 1410
#define GAIN 4
#define GAIN0 8
#define MINC 32
#define MDEC 3
#define STAG 1000
#define SH1 14
#define PTH 620
#define SH3 6
#define DVV 0
#define BOOT 0x2850
#define PXV (600 * 65536 + 32)
#define PBV (1400 * 65536)
#define PROBES 6
#define JB 80
#define BOOT2 0x0505
#define BOOT3 0x0a14
#define IMARG 3
#define PFR 0
#define OJ1 8
#define LK1 34
#define LK4 38
#define SH0 10
#define SH4 24
#define XA1 34
#define XA2 14
#define VSLOT 60
#define TP(run, fixed) (fixed)
typedef unsigned short u16;
typedef __attribute__((ext_vector_type(4))) float f32x4;
typedef unsigned v4u __attribute__((ext_vector_type(4)));

constexpr int S_ = 2048, DM = 1024, HA = 32, DHA = 128, HDA = 4096, MEM = 8, SEG = 32, NSEG = 64;
constexpr int QPAGE = 2 << 20, QBLK = 16384, NPAGE = 60, QSIZE = (NPAGE + 8) * QPAGE;
__device__ __forceinline__ int qoff(unsigned long long parq, int n) { return n * QPAGE + (int)((parq >> n) & 1) * 8192; }
__device__ __forceinline__ int kvoff(unsigned pk, int h) { return (NPAGE + (h >> 2)) * QPAGE + (int)((pk >> (h >> 2)) & 1) * 8192 + ((h >> 1) & 1) * QBLK + (h & 1) * 4096; }
constexpr int DB = 1024, HB = 32, DHB = 32, NQKVB = 3 * DB;

constexpr int ROFF_O = 0, ROFF_V = 131072, ROFF_M = 262144, RSLOT = 327680, RSIZE = 4 * RSLOT;
typedef __attribute__((ext_vector_type(8))) __bf16 bfx8;
typedef __attribute__((ext_vector_type(2))) __bf16 bfx2;
typedef __attribute__((ext_vector_type(2))) float f32x2;
typedef unsigned v2u __attribute__((ext_vector_type(2)));
typedef unsigned v8u __attribute__((ext_vector_type(8)));
typedef unsigned v32u __attribute__((ext_vector_type(32)));
constexpr int NQK = 32, NVO = 64;
constexpr int VXLD = 144, LDS_ALL = 163840;
template <int I> struct IC { static constexpr int v = I; };
template <int I, int N, class F> __device__ __forceinline__ void sfor(F&& f) { if constexpr (I < N) { f(IC<I>{}); sfor<I + 1, N>(f); } }
#define SFOR(u, a, b) sfor<a, b>([&](auto u##_) __attribute__((always_inline)) { constexpr int u = decltype(u##_)::v;
#define SEND });

__device__ __forceinline__ void lds_barrier() { asm volatile("s_waitcnt lgkmcnt(0)" ::: "memory"); __builtin_amdgcn_s_barrier(); asm volatile("" ::: "memory"); }
template <int N> __device__ __forceinline__ float dpp_ror(float v) {
  return __int_as_float(__builtin_amdgcn_update_dpp(0, __float_as_int(v), 0x120 + N, 0xf, 0xf, false));
}
__device__ __forceinline__ unsigned pk2(float a, float b) { f32x2 t = {a, b}; return __builtin_bit_cast(unsigned, __builtin_convertvector(t, bfx2)); }
__device__ __forceinline__ unsigned nosent(unsigned x) { return x < 0xfffffffeu ? x : 0xfffffffeu; }
__device__ __forceinline__ unsigned umax3(unsigned a, unsigned b, unsigned c) { unsigned m = a > b ? a : b; return m > c ? m : c; }
__device__ __forceinline__ unsigned chk4(unsigned m, v4u v) { return umax3(umax3(m, v.x, v.y), v.z, v.w); }
__device__ __forceinline__ unsigned long long now() { return __builtin_amdgcn_s_memtime(); }
__device__ __forceinline__ f32x4 mf(v4u a, v4u b, f32x4 c) {
  return __builtin_amdgcn_mfma_f32_16x16x32_bf16(__builtin_bit_cast(bfx8, a), __builtin_bit_cast(bfx8, b), c, 0, 0, 0);
}
__device__ __forceinline__ v2u trrd(const u16* p) {
  typedef __attribute__((ext_vector_type(4))) short v4i16_t;
  return __builtin_bit_cast(v2u, __builtin_amdgcn_ds_read_tr16_b64_v4i16((__attribute__((address_space(3))) v4i16_t*)p));
}
__device__ __forceinline__ v8u cat8(v4u a, v4u b) { return __builtin_shufflevector(a, b, 0, 1, 2, 3, 4, 5, 6, 7); }
__device__ __forceinline__ float xmax16(float x) { auto r = __builtin_amdgcn_permlane16_swap(__float_as_uint(x), __float_as_uint(x), false, false); return fmaxf(__uint_as_float(r[0]), __uint_as_float(r[1])); }
__device__ __forceinline__ float xmax32(float x) { auto r = __builtin_amdgcn_permlane32_swap(__float_as_uint(x), __float_as_uint(x), false, false); return fmaxf(__uint_as_float(r[0]), __uint_as_float(r[1])); }
__device__ __forceinline__ float xsum16(float x) { auto r = __builtin_amdgcn_permlane16_swap(__float_as_uint(x), __float_as_uint(x), false, false); return __uint_as_float(r[0]) + __uint_as_float(r[1]); }
__device__ __forceinline__ float xsum32(float x) { auto r = __builtin_amdgcn_permlane32_swap(__float_as_uint(x), __float_as_uint(x), false, false); return __uint_as_float(r[0]) + __uint_as_float(r[1]); }
__device__ __forceinline__ int dbl(int off) { return ((off >> 13) << 14) | (off & 8191); }
__device__ __forceinline__ bool spin_fail(unsigned spins, unsigned* tmo, unsigned code) {
  if ((spins & 31u) != 31u) return false;
  if (spins > (1u << 15)) { __hip_atomic_store(tmo, code, __ATOMIC_RELAXED, __HIP_MEMORY_SCOPE_AGENT); return true; }
  return __builtin_amdgcn_readfirstlane(__hip_atomic_load(tmo, __ATOMIC_RELAXED, __HIP_MEMORY_SCOPE_AGENT)) != 0u;
}
#define SB0 __builtin_amdgcn_sched_barrier(0)
#define NTQ 0
#define QGAP 0
#define PSL 48
#define TSL 2
__device__ __forceinline__ int opaque_lane() { int l; asm volatile("v_mbcnt_lo_u32_b32 %0, -1, 0\n\tv_mbcnt_hi_u32_b32 %0, -1, %0" : "=v"(l)); return l & 63; }

#define OUTAUX 16
namespace wg {
constexpr int LDS_USE = 131072;
__device__ __forceinline__ void* uni(const void* p) { const unsigned long long x = (unsigned long long)p; return (void*)(((unsigned long long)(unsigned)__builtin_amdgcn_readfirstlane(x >> 32) << 32) | (unsigned)__builtin_amdgcn_readfirstlane(x)); }
template <int MT>
__device__ __forceinline__ void gemm_tile(const u16* A, int lda, const u16* B, int ldb, int nst  , void* C, int ldc, unsigned long long parq  , char* lds, int w) {
  constexpr bool apack = true, cpack = false;
  constexpr int MI = MT / 32, PA = MT / 64, NP = PA + 4;
  constexpr int A_B = MT * 64, ST = A_B + 16384;
  const int lane = opaque_lane();
  const int l15 = lane & 15, lq = lane >> 4, wm = w >> 1, wn = w & 1;
  lda = __builtin_amdgcn_readfirstlane(lda); ldb = __builtin_amdgcn_readfirstlane(ldb); ldc = __builtin_amdgcn_readfirstlane(ldc); nst = __builtin_amdgcn_readfirstlane(nst);
  f32x4 acc[8][MI];
#pragma unroll
  for (int i = 0; i < 8; ++i)
#pragma unroll
    for (int j = 0; j < MI; ++j) acc[i][j] = (f32x4){0.f, 0.f, 0.f, 0.f};
  const unsigned lb = (unsigned)(size_t)lds;
  auto fsw = [](int x) { return ((x ^ (x >> 1)) & 1) << 1 | (x >> 1); };
  unsigned ao[2], bo[2][8];
  ao[0] = lb + wm * (MT / 2) * 64 + (apack ? lane * 16 : l15 * 64 + ((lq ^ fsw(l15 >> 2)) << 4)); ao[1] = ao[0] + 2 * ST;
  { const int hh = (l15 >> 2) | ((lq & 1) << 2);
#pragma unroll
    for (int i = 0; i < 8; ++i) { bo[0][i] = lb + A_B + (8 * lq + (l15 >> 2)) * 512 + ((16 * wn + ((2 * i) ^ (2 * hh)) + ((l15 >> 1) & 1)) << 4) + (l15 & 1) * 8; bo[1][i] = bo[0][i] + 2 * ST; } }
  __amdgpu_buffer_rsrc_t ra = __builtin_amdgcn_make_buffer_rsrc(uni(A), 0, 0x7fffffff, 0x00020000), rb = __builtin_amdgcn_make_buffer_rsrc(uni(B), 0, 0x7fffffff, 0x00020000);
  const int va = (16 * PA * w * lda + (apack ? lane * 8 : (lane >> 2) * lda + (((lane & 3) ^ fsw(lane >> 4)) << 3))) * 2, ska = __builtin_amdgcn_readfirstlane(apack ? 1024 : 64);
  const int vb0 = ((8 * w + (lane >> 5)) * ldb + (((lane & 31) ^ ((lane >> 5) << 1) ^ ((w & 1) << 3)) << 3)) * 2, vb1 = vb0 ^ 64;
  const int sa = 32 * lda, sb = 4 * ldb, skb = 64 * ldb;
  const unsigned ma = lb + PA * w * 1024, mb = lb + A_B + w * 4096;
#define PIECE1(NT, base, imm, vo, rs, so) do { if constexpr (NT) asm volatile("s_add_u32 m0, %0, %1\n\ts_nop 0\n\tbuffer_load_dwordx4 %2, %3, %4 offen nt lds" :: "s"(base), "i"(imm), "v"(vo), "s"(rs), "s"(so) : "memory", "scc"); \
    else asm volatile("s_add_u32 m0, %0, %1\n\ts_nop 0\n\tbuffer_load_dwordx4 %2, %3, %4 offen lds" :: "s"(base), "i"(imm), "v"(vo), "s"(rs), "s"(so) : "memory", "scc"); } while (0)
#define PIECE(q, st) do { \
    if ((q) < PA) PIECE1(false, ma, (st) * ST + (q) * 1024, va, ra, ka + (q) * sa); \
    else PIECE1(false, mb, (st) * ST + ((q) - PA) * 1024, (((q) - PA) & 1) ? vb1 : vb0, rb, kb + ((q) - PA) * sb); } while (0)
#define RDA(f, j, st) asm volatile("ds_read_b128 %0, %1 offset:%2" : "=v"(fa[f][j]) : "v"(ao[(st) >> 1]), "i"(((st) & 1) * ST + (j) * 1024) : "memory")
#define RDB(f, i, h, st) asm volatile("ds_read_b64_tr_b16 %0, %1 offset:%2" : "=v"(fb[f][i][h]) : "v"(bo[(st) >> 1][i]), "i"(((st) & 1) * ST + (h) * 2048) : "memory")
#define TIE(f) do { _Pragma("unroll") for (int j = 0; j < MI; ++j) asm volatile("" : "+v"(fa[f][j])); \
    _Pragma("unroll") for (int i = 0; i < 8; ++i) asm volatile("" : "+v"(fb[f][i][0]), "+v"(fb[f][i][1])); } while (0)
  v4u fa[2][MI]; v2u fb[2][8][2];
  int ka = qoff(parq, 0), kb = 0;
  asm volatile("s_nop 4" :: "s"(ra), "s"(rb), "s"(sa), "s"(sb), "s"(skb), "s"(ska));
#pragma unroll
  for (int st = 0; st < 4; ++st) {
#pragma unroll
    for (int q = 0; q < NP; ++q) PIECE(q, st);
    ka += ska; kb += skb;
  }
  asm volatile("s_waitcnt vmcnt(%0)\n\ts_barrier" :: "i"(3 * NP) : "memory");
#pragma unroll
  for (int i = 0; i < 8; ++i) { RDB(0, i, 0, 0); RDB(0, i, 1, 0); }
#pragma unroll
  for (int j = 0; j < MI; ++j) RDA(0, j, 0);
  int k4 = 0;
  do {
    if (apack && (k4 & 4)) ka = qoff(parq, (k4 + 4) >> 3);
    if (k4 + 4 >= nst) { ra = __builtin_amdgcn_make_buffer_rsrc(uni(C), 0, 0, 0x00020000); rb = ra; }
#pragma unroll
    for (int st = 0; st < 4; ++st) {
      constexpr int NM = 8 * MI, NR = 16 + MI;
      const int f = st & 1, sn = (st + 1) & 3;
      asm volatile("s_waitcnt vmcnt(%0) lgkmcnt(0)\n\ts_barrier" :: "i"(2 * NP) : "memory");
      TIE(f);
      SB0;
#pragma unroll
      for (int u = 0; u < NM; ++u) {
        const int j = u >> 3, i = u & 7;
        asm volatile("v_mfma_f32_16x16x32_bf16 %0, %1, %2, %0" : "+a"(acc[i][j]) : "v"((v4u){fb[f][i][0].x, fb[f][i][0].y, fb[f][i][1].x, fb[f][i][1].y}), "v"(fa[f][j]));
        SB0;
        constexpr int RS = MT == 256 ? 2 : 1;
        if (u % RS == 0 && u / RS < NR) {
          const int r = u / RS;
          if (r < 16) RDB(f ^ 1, r >> 1, r & 1, sn); else RDA(f ^ 1, r - 16, sn);
          if (MT == 64 && (u == 0 || u == 2)) RDA(f ^ 1, u >> 1, sn);
          SB0;
        }
        constexpr int PS = NM / 8;
        if (u % PS == PS - 1 && u / PS < NP) { PIECE(u / PS, st); SB0; }
      }
      ka += ska; kb += skb;
    }
    k4 += 4;
  } while (k4 < nst);
#undef PIECE
#undef PIECE1
#undef RDA
#undef RDB
  asm volatile("s_nop 7\n\ts_waitcnt lgkmcnt(0)" ::: "memory");
  SB0;
  TIE(0);
#undef TIE
  char* st = lds + LDS_USE + 2048 + w * 4096;
  const __amdgpu_buffer_rsrc_t rs = __builtin_amdgcn_make_buffer_rsrc(uni(C), 0, MT * ldc * 2 - 1536 * 2, 0x00020000);
  const int so = wm * (MT / 2) * ldc * 2 + (cpack ? wn * 4096 + lane * 16 : (lq * ldc + wn * 128 + l15 * 8) * 2), sq = cpack ? 1024 : 8 * ldc;
#pragma unroll
  for (int j = 0; j < MI; ++j) {
    v2u p[8]; v4u v[4];
#pragma unroll
    for (int i = 0; i < 8; ++i) { asm volatile("" : "+a"(acc[i][j])); p[i] = (v2u){pk2(acc[i][j][0], acc[i][j][1]), pk2(acc[i][j][2], acc[i][j][3])}; }
#pragma unroll
    for (int q = 0; q < 4; ++q) { v[q] = (v4u){p[2 * q].x, p[2 * q].y, p[2 * q + 1].x, p[2 * q + 1].y}; asm volatile("" : "+v"(v[q])); }
    if (!cpack) {
#pragma unroll
      for (int i = 0; i < 8; ++i) *(v2u*)(st + l15 * 256 + (((2 * i + (lq >> 1)) ^ l15) << 4) + (lq & 1) * 8) = (i & 1) ? (v2u){v[i >> 1].z, v[i >> 1].w} : (v2u){v[i >> 1].x, v[i >> 1].y};
      asm volatile("" ::: "memory");
#pragma unroll
      for (int q = 0; q < 4; ++q) v[q] = *(const v4u*)(st + (4 * q + lq) * 256 + ((l15 ^ (4 * q + lq)) << 4));
      asm volatile("" ::: "memory");
    }
#pragma unroll
    for (int q = 0; q < 4; ++q) {
      __builtin_amdgcn_raw_buffer_store_b128(v[q], rs, so, 32 * j * ldc + q * sq, OUTAUX);
    }
    SB0;
  }
  asm volatile("s_waitcnt vmcnt(0)" ::: "memory");
  __builtin_amdgcn_s_barrier();
}
}

struct Ctl {
  unsigned qhead[32];
  unsigned prg[192][32];
  unsigned cntAB[16][32];
  unsigned flagO[72][8][32];
  alignas(8) unsigned prog[32];
  unsigned tmo[32];
  unsigned chk[32];
};
struct FParams {
  const u16 *Ax, *mem0, *Bx, *Wq, *Wk, *Wv, *Wo, *wqkvB, *woB;
  u16 *qkvA, *out; char *ring, *rdy;
  Ctl* ctl; unsigned long long* prof;
};
#define RLX_AGENT __ATOMIC_RELAXED, __HIP_MEMORY_SCOPE_AGENT
__device__ __forceinline__ unsigned ld_agent(const unsigned* p) { return __hip_atomic_load(p, RLX_AGENT); }
constexpr int RDY_SIZE = 64 * 128;
__device__ __forceinline__ v4u rdy_load(const FParams& F) { const int lz = opaque_lane(); return __builtin_amdgcn_raw_buffer_load_b128(__builtin_amdgcn_make_buffer_rsrc(F.rdy, 0, RDY_SIZE, 0x00020000), lz * 128, 0, 16); }
__device__ __forceinline__ unsigned long long rdy_tag(v4u r) { return (((unsigned long long)(unsigned)__builtin_amdgcn_readlane(r.y, 63) << 32) | (unsigned)__builtin_amdgcn_readlane(r.x, 63)) + 1; }
__device__ __forceinline__ unsigned long long rdy_wait(const FParams& F, v4u r, unsigned long long& tag, unsigned& pk) {
  tag = rdy_tag(r);
  for (unsigned spins = 0; (~__ballot(r.x == (unsigned)tag && r.y == (unsigned)(tag >> 32)) << 4) != 0 && spins < (1u << 16); ++spins) { __builtin_amdgcn_s_sleep(8); r = rdy_load(F); }
  asm volatile("" ::: "memory");
  const unsigned long long b4 = __ballot(r.z & 4);
  pk = (unsigned)b4 & 255u;
  return (__ballot(r.z & 1) & ~(15ull << 60)) | (__builtin_popcountll(b4 << 4 >> 12) > 26 ? 1ull << 63 : 0ull);
}
__device__ __forceinline__ void check_in(const FParams& F, unsigned long long tag, int w) {
  __syncthreads();
  if (w == 0 && opaque_lane() == 0 && __hip_atomic_fetch_add(&F.ctl->chk[0], 1u, RLX_AGENT) == 255u) __hip_atomic_store((unsigned long long*)(F.rdy + 63 * 128), tag, RLX_AGENT);
}
__device__ __forceinline__ void wg_wait(const unsigned* p, unsigned need, Ctl* ctl, int w) {
  if (w == 0) {
    for (unsigned spins = 0;; ++spins) {
      const bool ok = (p == nullptr) || ld_agent(p) >= need;
      if (__all(ok)) break;
      if (spin_fail(spins, &ctl->tmo[0], 2u)) break;
      __builtin_amdgcn_s_sleep(16);
    }
    __builtin_amdgcn_fence(__ATOMIC_ACQUIRE, "agent");
    asm volatile("s_waitcnt vmcnt(0)" ::: "memory");
  }
  __syncthreads();
}

#define PN_BODY(PN_NT) \
          "s_mov_b64 s[40:41], %[xa]\n\ts_mov_b32 s42, 0x400000\n\ts_mov_b32 s43, 0x20000\n\ts_nop 4\n\ts_add_u32 m0, %[wl], 65536\n\ts_mul_i32 %[t], %[ldb], 0\n\t" \
          "buffer_load_dwordx4 %[vw0], %[rw], %[t] offen" PN_NT " lds\n\ts_add_u32 m0, %[wl], 66560\n\ts_mul_i32 %[t], %[ldb], 4\n\tbuffer_load_dwordx4 %[vw0], %[rw], %[t] offen" PN_NT " lds\n\ts_add_u32 m0, %[wl], 67584\n\ts_mul_i32 %[t], %[ldb], 8\n\t" \
          "buffer_load_dwordx4 %[vw1], %[rw], %[t] offen" PN_NT " lds\n\ts_add_u32 m0, %[wl], 68608\n\ts_mul_i32 %[t], %[ldb], 12\n\tbuffer_load_dwordx4 %[vw1], %[rw], %[t] offen" PN_NT " lds\n\ts_add_u32 m0, %[wl], 81920\n\ts_mul_i32 %[t], %[ldb], 64\n\t" \
          "buffer_load_dwordx4 %[vw0], %[rw], %[t] offen" PN_NT " lds\n\ts_add_u32 m0, %[wl], 82944\n\ts_mul_i32 %[t], %[ldb], 68\n\tbuffer_load_dwordx4 %[vw0], %[rw], %[t] offen" PN_NT " lds\n\ts_add_u32 m0, %[wl], 83968\n\ts_mul_i32 %[t], %[ldb], 72\n\t" \
          "buffer_load_dwordx4 %[vw1], %[rw], %[t] offen" PN_NT " lds\n\ts_add_u32 m0, %[wl], 84992\n\ts_mul_i32 %[t], %[ldb], 76\n\tbuffer_load_dwordx4 %[vw1], %[rw], %[t] offen" PN_NT " lds\n\ts_add_u32 m0, %[sl], 0\n\ts_nop 0\n\t" \
          "buffer_load_dwordx4 %[vd0], s[40:43], %[xo] offen lds\n\tbuffer_load_dwordx4 %[vd0], s[40:43], %[xo] offen offset:1024 lds\n\tbuffer_load_dwordx4 %[vd1], s[40:43], %[xo] offen offset:2048 lds\n\tbuffer_load_dwordx4 %[vd1], s[40:43], %[xo] offen offset:3072 lds\n\ts_add_u32 m0, %[sl], 4096\n\ts_nop 0\n\t" \
          "buffer_load_dwordx4 %[vd2], s[40:43], %[xo] offen lds\n\tbuffer_load_dwordx4 %[vd2], s[40:43], %[xo] offen offset:1024 lds\n\tbuffer_load_dwordx4 %[vd3], s[40:43], %[xo] offen offset:2048 lds\n\tbuffer_load_dwordx4 %[vd3], s[40:43], %[xo] offen offset:3072 lds\n\ts_add_u32 %[xo], %[xo], 0x8000\n\ts_add_u32 %[sl], %[sl], 0x8000\n\t" \
          "s_bitset0_b32 %[sl], 17\n\ts_add_u32 m0, %[sl], 0\n\ts_nop 0\n\tbuffer_load_dwordx4 %[vd0], s[40:43], %[xo] offen lds\n\tbuffer_load_dwordx4 %[vd0], s[40:43], %[xo] offen offset:1024 lds\n\tbuffer_load_dwordx4 %[vd1], s[40:43], %[xo] offen offset:2048 lds\n\t" \
          "buffer_load_dwordx4 %[vd1], s[40:43], %[xo] offen offset:3072 lds\n\ts_add_u32 m0, %[sl], 4096\n\ts_nop 0\n\tbuffer_load_dwordx4 %[vd2], s[40:43], %[xo] offen lds\n\tbuffer_load_dwordx4 %[vd2], s[40:43], %[xo] offen offset:1024 lds\n\tbuffer_load_dwordx4 %[vd3], s[40:43], %[xo] offen offset:2048 lds\n\t" \
          "buffer_load_dwordx4 %[vd3], s[40:43], %[xo] offen offset:3072 lds\n\ts_add_u32 %[xo], %[xo], 0x8000\n\ts_add_u32 %[sl], %[sl], 0x8000\n\ts_bitset0_b32 %[sl], 17\n\ts_add_u32 m0, %[wl], 98304\n\ts_mul_i32 %[t], %[ldb], 128\n\t" \
          "buffer_load_dwordx4 %[vw0], %[rw], %[t] offen" PN_NT " lds\n\ts_add_u32 m0, %[wl], 99328\n\ts_mul_i32 %[t], %[ldb], 132\n\tbuffer_load_dwordx4 %[vw0], %[rw], %[t] offen" PN_NT " lds\n\ts_add_u32 m0, %[wl], 100352\n\ts_mul_i32 %[t], %[ldb], 136\n\t" \
          "buffer_load_dwordx4 %[vw1], %[rw], %[t] offen" PN_NT " lds\n\ts_add_u32 m0, %[wl], 101376\n\ts_mul_i32 %[t], %[ldb], 140\n\tbuffer_load_dwordx4 %[vw1], %[rw], %[t] offen" PN_NT " lds\n\ts_add_u32 m0, %[wl], 114688\n\ts_mul_i32 %[t], %[ldb], 192\n\t" \
          "buffer_load_dwordx4 %[vw0], %[rw], %[t] offen" PN_NT " lds\n\ts_add_u32 m0, %[wl], 115712\n\ts_mul_i32 %[t], %[ldb], 196\n\tbuffer_load_dwordx4 %[vw0], %[rw], %[t] offen" PN_NT " lds\n\ts_add_u32 m0, %[wl], 116736\n\ts_mul_i32 %[t], %[ldb], 200\n\t" \
          "buffer_load_dwordx4 %[vw1], %[rw], %[t] offen" PN_NT " lds\n\ts_add_u32 m0, %[wl], 117760\n\ts_mul_i32 %[t], %[ldb], 204\n\tbuffer_load_dwordx4 %[vw1], %[rw], %[t] offen" PN_NT " lds\n\ts_add_u32 m0, %[wl], 131072\n\ts_mul_i32 %[t], %[ldb], 256\n\t" \
          "buffer_load_dwordx4 %[vw0], %[rw], %[t] offen" PN_NT " lds\n\ts_add_u32 m0, %[wl], 132096\n\ts_mul_i32 %[t], %[ldb], 260\n\tbuffer_load_dwordx4 %[vw0], %[rw], %[t] offen" PN_NT " lds\n\ts_add_u32 m0, %[wl], 133120\n\ts_mul_i32 %[t], %[ldb], 264\n\t" \
          "buffer_load_dwordx4 %[vw1], %[rw], %[t] offen" PN_NT " lds\n\ts_add_u32 m0, %[wl], 134144\n\ts_mul_i32 %[t], %[ldb], 268\n\tbuffer_load_dwordx4 %[vw1], %[rw], %[t] offen" PN_NT " lds\n\ts_waitcnt vmcnt(32) lgkmcnt(0)\n\ts_barrier\n\t" \
          "s_add_u32 m0, %[wl], 147456\n\ts_mul_i32 %[t], %[ldb], 320\n\tbuffer_load_dwordx4 %[vw0], %[rw], %[t] offen" PN_NT " lds\n\ts_add_u32 m0, %[wl], 148480\n\ts_mul_i32 %[t], %[ldb], 324\n\tbuffer_load_dwordx4 %[vw0], %[rw], %[t] offen" PN_NT " lds\n\t" \
          "s_add_u32 m0, %[wl], 149504\n\ts_mul_i32 %[t], %[ldb], 328\n\tbuffer_load_dwordx4 %[vw1], %[rw], %[t] offen" PN_NT " lds\n\ts_add_u32 m0, %[wl], 150528\n\ts_mul_i32 %[t], %[ldb], 332\n\tbuffer_load_dwordx4 %[vw1], %[rw], %[t] offen" PN_NT " lds\n\t" \
          "ds_read_b64_tr_b16 a[0:1], %[rt0] offset:0\n\tds_read_b64_tr_b16 a[2:3], %[rt0] offset:1024\n\tds_read_b64_tr_b16 a[4:5], %[rt1] offset:0\n\tds_read_b64_tr_b16 a[6:7], %[rt1] offset:1024\n\tds_read_b64_tr_b16 a[8:9], %[rt0] offset:8192\n\tds_read_b64_tr_b16 a[10:11], %[rt0] offset:9216\n\t" \
          "ds_read_b64_tr_b16 a[12:13], %[rt1] offset:8192\n\tds_read_b64_tr_b16 a[14:15], %[rt1] offset:9216\n\ts_waitcnt vmcnt(32) lgkmcnt(0)\n\ts_barrier\n\ts_add_u32 m0, %[wl], 65536\n\ts_mul_i32 %[t], %[ldb], 384\n\t" \
          "buffer_load_dwordx4 %[vw0], %[rw], %[t] offen" PN_NT " lds\n\ts_add_u32 m0, %[wl], 66560\n\ts_mul_i32 %[t], %[ldb], 388\n\tbuffer_load_dwordx4 %[vw0], %[rw], %[t] offen" PN_NT " lds\n\ts_add_u32 m0, %[wl], 67584\n\ts_mul_i32 %[t], %[ldb], 392\n\t" \
          "buffer_load_dwordx4 %[vw1], %[rw], %[t] offen" PN_NT " lds\n\ts_add_u32 m0, %[wl], 68608\n\ts_mul_i32 %[t], %[ldb], 396\n\tbuffer_load_dwordx4 %[vw1], %[rw], %[t] offen" PN_NT " lds\n\tds_read_b64_tr_b16 a[16:17], %[rt0] offset:16384\n\tds_read_b64_tr_b16 a[18:19], %[rt0] offset:17408\n\t" \
          "ds_read_b64_tr_b16 a[20:21], %[rt1] offset:16384\n\tds_read_b64_tr_b16 a[22:23], %[rt1] offset:17408\n\tds_read_b64_tr_b16 a[24:25], %[rt0] offset:24576\n\tds_read_b64_tr_b16 a[26:27], %[rt0] offset:25600\n\tds_read_b64_tr_b16 a[28:29], %[rt1] offset:24576\n\tds_read_b64_tr_b16 a[30:31], %[rt1] offset:25600\n\t" \
          "s_waitcnt vmcnt(16) lgkmcnt(0)\n\ts_barrier\n\ts_add_u32 m0, %[wl], 81920\n\ts_mul_i32 %[t], %[ldb], 448\n\tbuffer_load_dwordx4 %[vw0], %[rw], %[t] offen" PN_NT " lds\n\ts_add_u32 m0, %[wl], 82944\n\t" \
          "s_mul_i32 %[t], %[ldb], 452\n\tbuffer_load_dwordx4 %[vw0], %[rw], %[t] offen" PN_NT " lds\n\ts_add_u32 m0, %[wl], 83968\n\ts_mul_i32 %[t], %[ldb], 456\n\tbuffer_load_dwordx4 %[vw1], %[rw], %[t] offen" PN_NT " lds\n\ts_add_u32 m0, %[wl], 84992\n\t" \
          "s_mul_i32 %[t], %[ldb], 460\n\tbuffer_load_dwordx4 %[vw1], %[rw], %[t] offen" PN_NT " lds\n\tds_read_b64_tr_b16 a[32:33], %[rt0] offset:32768\n\tds_read_b64_tr_b16 a[34:35], %[rt0] offset:33792\n\tds_read_b64_tr_b16 a[36:37], %[rt1] offset:32768\n\tds_read_b64_tr_b16 a[38:39], %[rt1] offset:33792\n\t" \
          "ds_read_b64_tr_b16 a[40:41], %[rt0] offset:40960\n\tds_read_b64_tr_b16 a[42:43], %[rt0] offset:41984\n\tds_read_b64_tr_b16 a[44:45], %[rt1] offset:40960\n\tds_read_b64_tr_b16 a[46:47], %[rt1] offset:41984\n\ts_waitcnt vmcnt(16) lgkmcnt(0)\n\ts_barrier\n\t" \
          "s_add_u32 m0, %[wl], 98304\n\ts_mul_i32 %[t], %[ldb], 512\n\tbuffer_load_dwordx4 %[vw0], %[rw], %[t] offen" PN_NT " lds\n\ts_add_u32 m0, %[wl], 99328\n\ts_mul_i32 %[t], %[ldb], 516\n\tbuffer_load_dwordx4 %[vw0], %[rw], %[t] offen" PN_NT " lds\n\t" \
          "s_add_u32 m0, %[wl], 100352\n\ts_mul_i32 %[t], %[ldb], 520\n\tbuffer_load_dwordx4 %[vw1], %[rw], %[t] offen" PN_NT " lds\n\ts_add_u32 m0, %[wl], 101376\n\ts_mul_i32 %[t], %[ldb], 524\n\tbuffer_load_dwordx4 %[vw1], %[rw], %[t] offen" PN_NT " lds\n\t" \
          "ds_read_b64_tr_b16 a[48:49], %[rt2] offset:0\n\tds_read_b64_tr_b16 a[50:51], %[rt2] offset:1024\n\tds_read_b64_tr_b16 a[52:53], %[rt3] offset:0\n\tds_read_b64_tr_b16 a[54:55], %[rt3] offset:1024\n\tds_read_b64_tr_b16 a[56:57], %[rt2] offset:8192\n\tds_read_b64_tr_b16 a[58:59], %[rt2] offset:9216\n\t" \
          "ds_read_b64_tr_b16 a[60:61], %[rt3] offset:8192\n\tds_read_b64_tr_b16 a[62:63], %[rt3] offset:9216\n\ts_waitcnt vmcnt(16) lgkmcnt(0)\n\ts_barrier\n\ts_add_u32 m0, %[wl], 114688\n\ts_mul_i32 %[t], %[ldb], 576\n\t" \
          "buffer_load_dwordx4 %[vw0], %[rw], %[t] offen" PN_NT " lds\n\ts_add_u32 m0, %[wl], 115712\n\ts_mul_i32 %[t], %[ldb], 580\n\tbuffer_load_dwordx4 %[vw0], %[rw], %[t] offen" PN_NT " lds\n\ts_add_u32 m0, %[wl], 116736\n\ts_mul_i32 %[t], %[ldb], 584\n\t" \
          "buffer_load_dwordx4 %[vw1], %[rw], %[t] offen" PN_NT " lds\n\ts_add_u32 m0, %[wl], 117760\n\ts_mul_i32 %[t], %[ldb], 588\n\tbuffer_load_dwordx4 %[vw1], %[rw], %[t] offen" PN_NT " lds\n\tds_read_b64_tr_b16 a[64:65], %[rt2] offset:16384\n\tds_read_b64_tr_b16 a[66:67], %[rt2] offset:17408\n\t" \
          "ds_read_b64_tr_b16 a[68:69], %[rt3] offset:16384\n\tds_read_b64_tr_b16 a[70:71], %[rt3] offset:17408\n\tds_read_b64_tr_b16 a[72:73], %[rt2] offset:24576\n\tds_read_b64_tr_b16 a[74:75], %[rt2] offset:25600\n\tds_read_b64_tr_b16 a[76:77], %[rt3] offset:24576\n\tds_read_b64_tr_b16 a[78:79], %[rt3] offset:25600\n\t" \
          "s_waitcnt vmcnt(16) lgkmcnt(0)\n\ts_barrier\n\ts_add_u32 m0, %[wl], 131072\n\ts_mul_i32 %[t], %[ldb], 640\n\tbuffer_load_dwordx4 %[vw0], %[rw], %[t] offen" PN_NT " lds\n\ts_add_u32 m0, %[wl], 132096\n\t" \
          "s_mul_i32 %[t], %[ldb], 644\n\tbuffer_load_dwordx4 %[vw0], %[rw], %[t] offen" PN_NT " lds\n\ts_add_u32 m0, %[wl], 133120\n\ts_mul_i32 %[t], %[ldb], 648\n\tbuffer_load_dwordx4 %[vw1], %[rw], %[t] offen" PN_NT " lds\n\ts_add_u32 m0, %[wl], 134144\n\t" \
          "s_mul_i32 %[t], %[ldb], 652\n\tbuffer_load_dwordx4 %[vw1], %[rw], %[t] offen" PN_NT " lds\n\tds_read_b64_tr_b16 a[80:81], %[rt2] offset:32768\n\tds_read_b64_tr_b16 a[82:83], %[rt2] offset:33792\n\tds_read_b64_tr_b16 a[84:85], %[rt3] offset:32768\n\tds_read_b64_tr_b16 a[86:87], %[rt3] offset:33792\n\t" \
          "ds_read_b64_tr_b16 a[88:89], %[rt2] offset:40960\n\tds_read_b64_tr_b16 a[90:91], %[rt2] offset:41984\n\tds_read_b64_tr_b16 a[92:93], %[rt3] offset:40960\n\tds_read_b64_tr_b16 a[94:95], %[rt3] offset:41984\n\ts_waitcnt vmcnt(16) lgkmcnt(0)\n\ts_barrier\n\t" \
          "s_add_u32 m0, %[wl], 147456\n\ts_mul_i32 %[t], %[ldb], 704\n\tbuffer_load_dwordx4 %[vw0], %[rw], %[t] offen" PN_NT " lds\n\ts_add_u32 m0, %[wl], 148480\n\ts_mul_i32 %[t], %[ldb], 708\n\tbuffer_load_dwordx4 %[vw0], %[rw], %[t] offen" PN_NT " lds\n\t" \
          "s_add_u32 m0, %[wl], 149504\n\ts_mul_i32 %[t], %[ldb], 712\n\tbuffer_load_dwordx4 %[vw1], %[rw], %[t] offen" PN_NT " lds\n\ts_add_u32 m0, %[wl], 150528\n\ts_mul_i32 %[t], %[ldb], 716\n\tbuffer_load_dwordx4 %[vw1], %[rw], %[t] offen" PN_NT " lds\n\t" \
          "ds_read_b64_tr_b16 a[96:97], %[rt0] offset:0\n\tds_read_b64_tr_b16 a[98:99], %[rt0] offset:1024\n\tds_read_b64_tr_b16 a[100:101], %[rt1] offset:0\n\tds_read_b64_tr_b16 a[102:103], %[rt1] offset:1024\n\tds_read_b64_tr_b16 a[104:105], %[rt0] offset:8192\n\tds_read_b64_tr_b16 a[106:107], %[rt0] offset:9216\n\t" \
          "ds_read_b64_tr_b16 a[108:109], %[rt1] offset:8192\n\tds_read_b64_tr_b16 a[110:111], %[rt1] offset:9216\n\ts_waitcnt vmcnt(16) lgkmcnt(0)\n\ts_barrier\n\ts_add_u32 m0, %[wl], 65536\n\ts_mul_i32 %[t], %[ldb], 768\n\t" \
          "buffer_load_dwordx4 %[vw0], %[rw], %[t] offen" PN_NT " lds\n\ts_add_u32 m0, %[wl], 66560\n\ts_mul_i32 %[t], %[ldb], 772\n\tbuffer_load_dwordx4 %[vw0], %[rw], %[t] offen" PN_NT " lds\n\ts_add_u32 m0, %[wl], 67584\n\ts_mul_i32 %[t], %[ldb], 776\n\t" \
          "buffer_load_dwordx4 %[vw1], %[rw], %[t] offen" PN_NT " lds\n\ts_add_u32 m0, %[wl], 68608\n\ts_mul_i32 %[t], %[ldb], 780\n\tbuffer_load_dwordx4 %[vw1], %[rw], %[t] offen" PN_NT " lds\n\tds_read_b64_tr_b16 a[112:113], %[rt0] offset:16384\n\tds_read_b64_tr_b16 a[114:115], %[rt0] offset:17408\n\t" \
          "ds_read_b64_tr_b16 a[116:117], %[rt1] offset:16384\n\tds_read_b64_tr_b16 a[118:119], %[rt1] offset:17408\n\tds_read_b64_tr_b16 a[120:121], %[rt0] offset:24576\n\tds_read_b64_tr_b16 a[122:123], %[rt0] offset:25600\n\tds_read_b64_tr_b16 a[124:125], %[rt1] offset:24576\n\tds_read_b64_tr_b16 a[126:127], %[rt1] offset:25600\n\t" \
          "s_waitcnt vmcnt(16) lgkmcnt(0)\n\ts_barrier\n\ts_add_u32 m0, %[wl], 81920\n\ts_mul_i32 %[t], %[ldb], 832\n\tbuffer_load_dwordx4 %[vw0], %[rw], %[t] offen" PN_NT " lds\n\ts_add_u32 m0, %[wl], 82944\n\t" \
          "s_mul_i32 %[t], %[ldb], 836\n\tbuffer_load_dwordx4 %[vw0], %[rw], %[t] offen" PN_NT " lds\n\ts_add_u32 m0, %[wl], 83968\n\ts_mul_i32 %[t], %[ldb], 840\n\tbuffer_load_dwordx4 %[vw1], %[rw], %[t] offen" PN_NT " lds\n\ts_add_u32 m0, %[wl], 84992\n\t" \
          "s_mul_i32 %[t], %[ldb], 844\n\tbuffer_load_dwordx4 %[vw1], %[rw], %[t] offen" PN_NT " lds\n\tds_read_b64_tr_b16 a[128:129], %[rt0] offset:32768\n\tds_read_b64_tr_b16 a[130:131], %[rt0] offset:33792\n\tds_read_b64_tr_b16 a[132:133], %[rt1] offset:32768\n\tds_read_b64_tr_b16 a[134:135], %[rt1] offset:33792\n\t" \
          "ds_read_b64_tr_b16 a[136:137], %[rt0] offset:40960\n\tds_read_b64_tr_b16 a[138:139], %[rt0] offset:41984\n\tds_read_b64_tr_b16 a[140:141], %[rt1] offset:40960\n\tds_read_b64_tr_b16 a[142:143], %[rt1] offset:41984\n\ts_waitcnt vmcnt(16) lgkmcnt(0)\n\ts_barrier\n\t" \
          "s_add_u32 m0, %[wl], 98304\n\ts_mul_i32 %[t], %[ldb], 896\n\tbuffer_load_dwordx4 %[vw0], %[rw], %[t] offen" PN_NT " lds\n\ts_add_u32 m0, %[wl], 99328\n\ts_mul_i32 %[t], %[ldb], 900\n\tbuffer_load_dwordx4 %[vw0], %[rw], %[t] offen" PN_NT " lds\n\t" \
          "s_add_u32 m0, %[wl], 100352\n\ts_mul_i32 %[t], %[ldb], 904\n\tbuffer_load_dwordx4 %[vw1], %[rw], %[t] offen" PN_NT " lds\n\ts_add_u32 m0, %[wl], 101376\n\ts_mul_i32 %[t], %[ldb], 908\n\tbuffer_load_dwordx4 %[vw1], %[rw], %[t] offen" PN_NT " lds\n\t" \
          "ds_read_b64_tr_b16 a[144:145], %[rt2] offset:0\n\tds_read_b64_tr_b16 a[146:147], %[rt2] offset:1024\n\tds_read_b64_tr_b16 a[148:149], %[rt3] offset:0\n\tds_read_b64_tr_b16 a[150:151], %[rt3] offset:1024\n\tds_read_b64_tr_b16 a[152:153], %[rt2] offset:8192\n\tds_read_b64_tr_b16 a[154:155], %[rt2] offset:9216\n\t" \
          "ds_read_b64_tr_b16 a[156:157], %[rt3] offset:8192\n\tds_read_b64_tr_b16 a[158:159], %[rt3] offset:9216\n\ts_waitcnt vmcnt(16) lgkmcnt(0)\n\ts_barrier\n\ts_add_u32 m0, %[wl], 114688\n\ts_mul_i32 %[t], %[ldb], 960\n\t" \
          "buffer_load_dwordx4 %[vw0], %[rw], %[t] offen" PN_NT " lds\n\ts_add_u32 m0, %[wl], 115712\n\ts_mul_i32 %[t], %[ldb], 964\n\tbuffer_load_dwordx4 %[vw0], %[rw], %[t] offen" PN_NT " lds\n\ts_add_u32 m0, %[wl], 116736\n\ts_mul_i32 %[t], %[ldb], 968\n\t" \
          "buffer_load_dwordx4 %[vw1], %[rw], %[t] offen" PN_NT " lds\n\ts_add_u32 m0, %[wl], 117760\n\ts_mul_i32 %[t], %[ldb], 972\n\tbuffer_load_dwordx4 %[vw1], %[rw], %[t] offen" PN_NT " lds\n\tds_read_b64_tr_b16 a[160:161], %[rt2] offset:16384\n\tds_read_b64_tr_b16 a[162:163], %[rt2] offset:17408\n\t" \
          "ds_read_b64_tr_b16 a[164:165], %[rt3] offset:16384\n\tds_read_b64_tr_b16 a[166:167], %[rt3] offset:17408\n\tds_read_b64_tr_b16 a[168:169], %[rt2] offset:24576\n\tds_read_b64_tr_b16 a[170:171], %[rt2] offset:25600\n\tds_read_b64_tr_b16 a[172:173], %[rt3] offset:24576\n\tds_read_b64_tr_b16 a[174:175], %[rt3] offset:25600\n\t" \
          "s_waitcnt vmcnt(16) lgkmcnt(0)\n\ts_barrier\n\tbuffer_load_dwordx4 v[40:43], %[ro], %[rr], 0 offen sc1\n\tds_read_b64_tr_b16 a[176:177], %[rt2] offset:32768\n\tds_read_b64_tr_b16 a[178:179], %[rt2] offset:33792\n\tds_read_b64_tr_b16 a[180:181], %[rt3] offset:32768\n\t" \
          "ds_read_b64_tr_b16 a[182:183], %[rt3] offset:33792\n\tds_read_b64_tr_b16 a[184:185], %[rt2] offset:40960\n\tds_read_b64_tr_b16 a[186:187], %[rt2] offset:41984\n\tds_read_b64_tr_b16 a[188:189], %[rt3] offset:40960\n\tds_read_b64_tr_b16 a[190:191], %[rt3] offset:41984\n\ts_waitcnt vmcnt(13) lgkmcnt(0)\n\t" \
          "s_barrier\n\tds_read_b64_tr_b16 a[192:193], %[rt0] offset:0\n\tds_read_b64_tr_b16 a[194:195], %[rt0] offset:1024\n\tds_read_b64_tr_b16 a[196:197], %[rt1] offset:0\n\tds_read_b64_tr_b16 a[198:199], %[rt1] offset:1024\n\tds_read_b64_tr_b16 a[200:201], %[rt0] offset:8192\n\t" \
          "ds_read_b64_tr_b16 a[202:203], %[rt0] offset:9216\n\tds_read_b64_tr_b16 a[204:205], %[rt1] offset:8192\n\tds_read_b64_tr_b16 a[206:207], %[rt1] offset:9216\n\ts_waitcnt vmcnt(9) lgkmcnt(0)\n\ts_barrier\n\tds_read_b64_tr_b16 a[208:209], %[rt0] offset:16384\n\t" \
          "ds_read_b64_tr_b16 a[210:211], %[rt0] offset:17408\n\tds_read_b64_tr_b16 a[212:213], %[rt1] offset:16384\n\tds_read_b64_tr_b16 a[214:215], %[rt1] offset:17408\n\tds_read_b64_tr_b16 a[216:217], %[rt0] offset:24576\n\tds_read_b64_tr_b16 a[218:219], %[rt0] offset:25600\n\tds_read_b64_tr_b16 a[220:221], %[rt1] offset:24576\n\t" \
          "ds_read_b64_tr_b16 a[222:223], %[rt1] offset:25600\n\ts_waitcnt vmcnt(5) lgkmcnt(0)\n\ts_barrier\n\tds_read_b64_tr_b16 a[224:225], %[rt0] offset:32768\n\tds_read_b64_tr_b16 a[226:227], %[rt0] offset:33792\n\tds_read_b64_tr_b16 a[228:229], %[rt1] offset:32768\n\t" \
          "ds_read_b64_tr_b16 a[230:231], %[rt1] offset:33792\n\tds_read_b64_tr_b16 a[232:233], %[rt0] offset:40960\n\tds_read_b64_tr_b16 a[234:235], %[rt0] offset:41984\n\tds_read_b64_tr_b16 a[236:237], %[rt1] offset:40960\n\tds_read_b64_tr_b16 a[238:239], %[rt1] offset:41984\n\ts_waitcnt vmcnt(0) lgkmcnt(0)\n\t" \
          "s_barrier\n\tds_read_b64_tr_b16 a[240:241], %[rt2] offset:0\n\tds_read_b64_tr_b16 a[242:243], %[rt2] offset:1024\n\tds_read_b64_tr_b16 a[244:245], %[rt3] offset:0\n\tds_read_b64_tr_b16 a[246:247], %[rt3] offset:1024\n\tds_read_b64_tr_b16 a[248:249], %[rt2] offset:8192\n\t" \
          "ds_read_b64_tr_b16 a[250:251], %[rt2] offset:9216\n\tds_read_b64_tr_b16 a[252:253], %[rt3] offset:8192\n\tds_read_b64_tr_b16 a[254:255], %[rt3] offset:9216\n\ts_waitcnt lgkmcnt(0)\n\ts_barrier\n\ts_add_u32 m0, %[sl], 0\n\t" \
          "s_nop 0\n\tbuffer_load_dwordx4 %[vd0], s[40:43], %[xo] offen lds\n\tbuffer_load_dwordx4 %[vd0], s[40:43], %[xo] offen offset:1024 lds\n\tbuffer_load_dwordx4 %[vd1], s[40:43], %[xo] offen offset:2048 lds\n\tbuffer_load_dwordx4 %[vd1], s[40:43], %[xo] offen offset:3072 lds\n\ts_add_u32 m0, %[sl], 4096\n\t" \
          "s_nop 0\n\tbuffer_load_dwordx4 %[vd2], s[40:43], %[xo] offen lds\n\tbuffer_load_dwordx4 %[vd2], s[40:43], %[xo] offen offset:1024 lds\n\tbuffer_load_dwordx4 %[vd3], s[40:43], %[xo] offen offset:2048 lds\n\tbuffer_load_dwordx4 %[vd3], s[40:43], %[xo] offen offset:3072 lds\n\ts_add_u32 %[xo], %[xo], 0x8000\n\t" \
          "s_add_u32 %[sl], %[sl], 0x8000\n\ts_bitset0_b32 %[sl], 17\n\ts_mov_b32 %[t], 0\n\tv_readlane_b32 s44, v40, 63\n\tv_readlane_b32 s45, v41, 63\n\ts_add_u32 s44, s44, 1\n\t" \
          "s_addc_u32 s45, s45, 0\n\tLpn_r%=:\n\tv_cmp_eq_u64 vcc, s[44:45], v[40:41]\n\ts_add_u32 %[t], %[t], 0x10000\n\ts_cbranch_scc1 Lpn_g%=\n\ts_or_b32 vcc_hi, vcc_hi, 0xf0000000\n\t" \
          "s_and_b32 vcc_lo, vcc_lo, vcc_hi\n\ts_cmp_eq_u32 vcc_lo, -1\n\ts_cbranch_scc1 Lpn_g%=\n\ts_sleep 8\n\tbuffer_load_dwordx4 v[40:43], %[ro], %[rr], 0 offen sc1\n\ts_waitcnt vmcnt(0)\n\t" \
          "s_branch Lpn_r%=\n\tLpn_g%=:\n\tv_readlane_b32 %[t], v42, %[pl]\n\ts_and_b32 %[t], %[t], 1\n\ts_lshl_b32 %[t], %[t], 13\n\ts_add_u32 %[co], %[co], %[t]\n\t" \
          "v_readlane_b32 %[ko], v42, %[kl]\n\ts_and_b32 %[ko], %[ko], 4\n\ts_lshl_b32 %[ko], %[ko], 11\n\tv_mov_b64 v[36:37], -1\n\tv_mov_b64 v[38:39], -1\n\tds_read_b128 v[64:67], %[ra0] offset:0\n\t" \
          "ds_read_b128 v[68:71], %[ra1] offset:0\n\tds_read_b128 v[72:75], %[ra2] offset:0\n\tds_read_b128 v[76:79], %[ra3] offset:0\n\tds_read_b128 v[80:83], %[ra0] offset:256\n\tds_read_b128 v[84:87], %[ra1] offset:256\n\tds_read_b128 v[88:91], %[ra2] offset:256\n\t" \
          "ds_read_b128 v[92:95], %[ra3] offset:256\n\ts_mov_b64 %[sx], 0\n\tLpn_loop%=:\n\ts_waitcnt lgkmcnt(6)\n\tv_mfma_f32_16x16x32_bf16 v[48:51], a[0:3], v[64:67], 0\n\tds_read_b128 v[96:99], %[ra0] offset:512\n\t" \
          "v_mfma_f32_16x16x32_bf16 v[52:55], a[4:7], v[64:67], 0\n\ts_add_u32 %[t], %[j], 3\n\ts_cmp_lt_u32 %[t], %[je]\n\ts_cselect_b32 s42, s42, 0\n\tv_mfma_f32_16x16x32_bf16 v[48:51], a[8:11], v[68:71], v[48:51]\n\tds_read_b128 v[100:103], %[ra1] offset:512\n\t" \
          "v_mfma_f32_16x16x32_bf16 v[52:55], a[12:15], v[68:71], v[52:55]\n\tv_cvt_pk_bf16_f32 v44, v56, v57\n\tv_cvt_pk_bf16_f32 v45, v58, v59\n\ts_waitcnt lgkmcnt(6)\n\tv_mfma_f32_16x16x32_bf16 v[48:51], a[16:19], v[72:75], v[48:51]\n\tds_read_b128 v[104:107], %[ra2] offset:512\n\t" \
          "v_mfma_f32_16x16x32_bf16 v[52:55], a[20:23], v[72:75], v[52:55]\n\tv_cvt_pk_bf16_f32 v46, v60, v61\n\tv_cvt_pk_bf16_f32 v47, v62, v63\n\tv_mfma_f32_16x16x32_bf16 v[48:51], a[24:27], v[76:79], v[48:51]\n\tds_read_b128 v[108:111], %[ra3] offset:512\n\tv_mfma_f32_16x16x32_bf16 v[52:55], a[28:31], v[76:79], v[52:55]\n\t" \
          "s_mov_b64 exec, %[sx]\n\tbuffer_store_dwordx4 v[44:47], %[so], %[rc], %[co] offen sc1\n\ts_mov_b64 exec, -1\n\ts_waitcnt lgkmcnt(6)\n\tv_mfma_f32_16x16x32_bf16 v[48:51], a[32:35], v[80:83], v[48:51]\n\tds_read_b128 v[112:115], %[ra0] offset:768\n\t" \
          "v_mfma_f32_16x16x32_bf16 v[52:55], a[36:39], v[80:83], v[52:55]\n\ts_mov_b64 exec, %[fx]\n\tbuffer_store_dword %[fv], off, %[rf], %[fo] sc1\n\ts_mov_b64 exec, -1\n\tv_mfma_f32_16x16x32_bf16 v[48:51], a[40:43], v[84:87], v[48:51]\n\tds_read_b128 v[116:119], %[ra1] offset:768\n\t" \
          "v_mfma_f32_16x16x32_bf16 v[52:55], a[44:47], v[84:87], v[52:55]\n\ts_add_u32 %[co], %[co], %[cs]\n\ts_mov_b32 %[cs], 0x4000\n\ts_mov_b64 %[sx], -1\n\ts_waitcnt lgkmcnt(6)\n\tv_mfma_f32_16x16x32_bf16 v[48:51], a[48:51], v[88:91], v[48:51]\n\t" \
          "ds_read_b128 v[120:123], %[ra2] offset:768\n\ts_add_u32 m0, %[sl], 0\n\tv_mfma_f32_16x16x32_bf16 v[52:55], a[52:55], v[88:91], v[52:55]\n\tbuffer_load_dwordx4 %[vd0], s[40:43], %[xo] offen lds\n\tv_mfma_f32_16x16x32_bf16 v[48:51], a[56:59], v[92:95], v[48:51]\n\tds_read_b128 v[124:127], %[ra3] offset:768\n\t" \
          "v_mfma_f32_16x16x32_bf16 v[52:55], a[60:63], v[92:95], v[52:55]\n\ts_waitcnt lgkmcnt(6)\n\tv_mfma_f32_16x16x32_bf16 v[48:51], a[64:67], v[96:99], v[48:51]\n\tds_read_b128 v[64:67], %[ra0] offset:1024\n\tv_mfma_f32_16x16x32_bf16 v[52:55], a[68:71], v[96:99], v[52:55]\n\tbuffer_load_dwordx4 %[vd0], s[40:43], %[xo] offen offset:1024 lds\n\t" \
          "v_mfma_f32_16x16x32_bf16 v[48:51], a[72:75], v[100:103], v[48:51]\n\tds_read_b128 v[68:71], %[ra1] offset:1024\n\tv_mfma_f32_16x16x32_bf16 v[52:55], a[76:79], v[100:103], v[52:55]\n\ts_waitcnt lgkmcnt(6)\n\tv_mfma_f32_16x16x32_bf16 v[48:51], a[80:83], v[104:107], v[48:51]\n\tds_read_b128 v[72:75], %[ra2] offset:1024\n\t" \
          "v_mfma_f32_16x16x32_bf16 v[52:55], a[84:87], v[104:107], v[52:55]\n\tbuffer_load_dwordx4 %[vd1], s[40:43], %[xo] offen offset:2048 lds\n\tv_mfma_f32_16x16x32_bf16 v[48:51], a[88:91], v[108:111], v[48:51]\n\tds_read_b128 v[76:79], %[ra3] offset:1024\n\tv_mfma_f32_16x16x32_bf16 v[52:55], a[92:95], v[108:111], v[52:55]\n\ts_waitcnt lgkmcnt(6)\n\t" \
          "v_mfma_f32_16x16x32_bf16 v[48:51], a[96:99], v[112:115], v[48:51]\n\tds_read_b128 v[80:83], %[ra0] offset:1280\n\tv_mfma_f32_16x16x32_bf16 v[52:55], a[100:103], v[112:115], v[52:55]\n\tbuffer_load_dwordx4 %[vd1], s[40:43], %[xo] offen offset:3072 lds\n\tv_mfma_f32_16x16x32_bf16 v[48:51], a[104:107], v[116:119], v[48:51]\n\tds_read_b128 v[84:87], %[ra1] offset:1280\n\t" \
          "v_mfma_f32_16x16x32_bf16 v[52:55], a[108:111], v[116:119], v[52:55]\n\ts_waitcnt lgkmcnt(6)\n\tv_mfma_f32_16x16x32_bf16 v[48:51], a[112:115], v[120:123], v[48:51]\n\tds_read_b128 v[88:91], %[ra2] offset:1280\n\ts_add_u32 m0, %[sl], 4096\n\tv_mfma_f32_16x16x32_bf16 v[52:55], a[116:119], v[120:123], v[52:55]\n\t" \
          "buffer_load_dwordx4 %[vd2], s[40:43], %[xo] offen lds\n\tv_mfma_f32_16x16x32_bf16 v[48:51], a[120:123], v[124:127], v[48:51]\n\tds_read_b128 v[92:95], %[ra3] offset:1280\n\tv_mfma_f32_16x16x32_bf16 v[52:55], a[124:127], v[124:127], v[52:55]\n\ts_waitcnt lgkmcnt(6)\n\tv_mfma_f32_16x16x32_bf16 v[48:51], a[128:131], v[64:67], v[48:51]\n\t" \
          "ds_read_b128 v[96:99], %[ra0] offset:1536\n\tv_mfma_f32_16x16x32_bf16 v[52:55], a[132:135], v[64:67], v[52:55]\n\tbuffer_load_dwordx4 %[vd2], s[40:43], %[xo] offen offset:1024 lds\n\tv_mfma_f32_16x16x32_bf16 v[48:51], a[136:139], v[68:71], v[48:51]\n\tds_read_b128 v[100:103], %[ra1] offset:1536\n\tv_mfma_f32_16x16x32_bf16 v[52:55], a[140:143], v[68:71], v[52:55]\n\t" \
          "s_waitcnt lgkmcnt(6)\n\tv_mfma_f32_16x16x32_bf16 v[48:51], a[144:147], v[72:75], v[48:51]\n\tds_read_b128 v[104:107], %[ra2] offset:1536\n\tv_mfma_f32_16x16x32_bf16 v[52:55], a[148:151], v[72:75], v[52:55]\n\tbuffer_load_dwordx4 %[vd3], s[40:43], %[xo] offen offset:2048 lds\n\tv_mfma_f32_16x16x32_bf16 v[48:51], a[152:155], v[76:79], v[48:51]\n\t" \
          "ds_read_b128 v[108:111], %[ra3] offset:1536\n\tv_mfma_f32_16x16x32_bf16 v[52:55], a[156:159], v[76:79], v[52:55]\n\ts_waitcnt lgkmcnt(6)\n\tv_mfma_f32_16x16x32_bf16 v[48:51], a[160:163], v[80:83], v[48:51]\n\tds_read_b128 v[112:115], %[ra0] offset:1792\n\tv_mfma_f32_16x16x32_bf16 v[52:55], a[164:167], v[80:83], v[52:55]\n\t" \
          "buffer_load_dwordx4 %[vd3], s[40:43], %[xo] offen offset:3072 lds\n\tv_mfma_f32_16x16x32_bf16 v[48:51], a[168:171], v[84:87], v[48:51]\n\tds_read_b128 v[116:119], %[ra1] offset:1792\n\tv_mfma_f32_16x16x32_bf16 v[52:55], a[172:175], v[84:87], v[52:55]\n\ts_waitcnt lgkmcnt(6)\n\tv_mfma_f32_16x16x32_bf16 v[48:51], a[176:179], v[88:91], v[48:51]\n\t" \
          "ds_read_b128 v[120:123], %[ra2] offset:1792\n\tv_mfma_f32_16x16x32_bf16 v[52:55], a[180:183], v[88:91], v[52:55]\n\tv_mfma_f32_16x16x32_bf16 v[48:51], a[184:187], v[92:95], v[48:51]\n\tds_read_b128 v[124:127], %[ra3] offset:1792\n\tv_mfma_f32_16x16x32_bf16 v[52:55], a[188:191], v[92:95], v[52:55]\n\ts_add_u32 %[xo], %[xo], 0x8000\n\t" \
          "s_add_u32 %[sl], %[sl], 0x8000\n\ts_bitset0_b32 %[sl], 17\n\ts_waitcnt lgkmcnt(6)\n\tv_mfma_f32_16x16x32_bf16 v[48:51], a[192:195], v[96:99], v[48:51]\n\tv_mfma_f32_16x16x32_bf16 v[52:55], a[196:199], v[96:99], v[52:55]\n\tv_mfma_f32_16x16x32_bf16 v[48:51], a[200:203], v[100:103], v[48:51]\n\t" \
          "v_mfma_f32_16x16x32_bf16 v[52:55], a[204:207], v[100:103], v[52:55]\n\ts_waitcnt lgkmcnt(4)\n\tv_mfma_f32_16x16x32_bf16 v[48:51], a[208:211], v[104:107], v[48:51]\n\tv_mfma_f32_16x16x32_bf16 v[52:55], a[212:215], v[104:107], v[52:55]\n\tv_mfma_f32_16x16x32_bf16 v[48:51], a[216:219], v[108:111], v[48:51]\n\tv_mfma_f32_16x16x32_bf16 v[52:55], a[220:223], v[108:111], v[52:55]\n\t" \
          "s_waitcnt vmcnt(18) lgkmcnt(0)\n\ts_barrier\n\tv_mfma_f32_16x16x32_bf16 v[48:51], a[224:227], v[112:115], v[48:51]\n\tds_read_b128 v[64:67], %[ra0] offset:32768\n\tv_mfma_f32_16x16x32_bf16 v[52:55], a[228:231], v[112:115], v[52:55]\n\tds_read_b128 v[68:71], %[ra1] offset:32768\n\t" \
          "v_mfma_f32_16x16x32_bf16 v[48:51], a[232:235], v[116:119], v[48:51]\n\tds_read_b128 v[72:75], %[ra2] offset:32768\n\tv_mfma_f32_16x16x32_bf16 v[52:55], a[236:239], v[116:119], v[52:55]\n\tds_read_b128 v[76:79], %[ra3] offset:32768\n\tv_mfma_f32_16x16x32_bf16 v[48:51], a[240:243], v[120:123], v[48:51]\n\tds_read_b128 v[80:83], %[ra0] offset:33024\n\t" \
          "v_mfma_f32_16x16x32_bf16 v[52:55], a[244:247], v[120:123], v[52:55]\n\tds_read_b128 v[84:87], %[ra1] offset:33024\n\tv_mfma_f32_16x16x32_bf16 v[48:51], a[248:251], v[124:127], v[48:51]\n\tds_read_b128 v[88:91], %[ra2] offset:33024\n\tv_mfma_f32_16x16x32_bf16 v[52:55], a[252:255], v[124:127], v[52:55]\n\tds_read_b128 v[92:95], %[ra3] offset:33024\n\t" \
          "s_waitcnt lgkmcnt(6)\n\tv_mfma_f32_16x16x32_bf16 v[56:59], a[0:3], v[64:67], 0\n\tds_read_b128 v[96:99], %[ra0] offset:33280\n\tv_mfma_f32_16x16x32_bf16 v[60:63], a[4:7], v[64:67], 0\n\ts_add_u32 %[t], %[j], 4\n\ts_cmp_lt_u32 %[t], %[je]\n\t" \
          "s_cselect_b32 s42, s42, 0\n\tv_mfma_f32_16x16x32_bf16 v[56:59], a[8:11], v[68:71], v[56:59]\n\tds_read_b128 v[100:103], %[ra1] offset:33280\n\tv_mfma_f32_16x16x32_bf16 v[60:63], a[12:15], v[68:71], v[60:63]\n\tv_cvt_pk_bf16_f32 v44, v48, v49\n\tv_cvt_pk_bf16_f32 v45, v50, v51\n\t" \
          "s_waitcnt lgkmcnt(6)\n\tv_mfma_f32_16x16x32_bf16 v[56:59], a[16:19], v[72:75], v[56:59]\n\tds_read_b128 v[104:107], %[ra2] offset:33280\n\tv_mfma_f32_16x16x32_bf16 v[60:63], a[20:23], v[72:75], v[60:63]\n\tv_cvt_pk_bf16_f32 v46, v52, v53\n\tv_cvt_pk_bf16_f32 v47, v54, v55\n\t" \
          "v_mfma_f32_16x16x32_bf16 v[56:59], a[24:27], v[76:79], v[56:59]\n\tds_read_b128 v[108:111], %[ra3] offset:33280\n\tv_mfma_f32_16x16x32_bf16 v[60:63], a[28:31], v[76:79], v[60:63]\n\tbuffer_store_dwordx4 v[44:47], %[so], %[rc], %[co] offen sc1\n\ts_waitcnt lgkmcnt(6)\n\tv_mfma_f32_16x16x32_bf16 v[56:59], a[32:35], v[80:83], v[56:59]\n\t" \
          "ds_read_b128 v[112:115], %[ra0] offset:33536\n\tv_mfma_f32_16x16x32_bf16 v[60:63], a[36:39], v[80:83], v[60:63]\n\tbuffer_store_dwordx4 v[36:39], %[sk], %[rk], %[ko] offen sc1\n\tv_mov_b32 %[fv], %[j]\n\tv_mfma_f32_16x16x32_bf16 v[56:59], a[40:43], v[84:87], v[56:59]\n\tds_read_b128 v[116:119], %[ra1] offset:33536\n\t" \
          "v_mfma_f32_16x16x32_bf16 v[60:63], a[44:47], v[84:87], v[60:63]\n\ts_add_u32 %[co], %[co], 0x4000\n\ts_add_u32 %[ko], %[ko], 0x8000\n\ts_waitcnt lgkmcnt(6)\n\tv_mfma_f32_16x16x32_bf16 v[56:59], a[48:51], v[88:91], v[56:59]\n\tds_read_b128 v[120:123], %[ra2] offset:33536\n\t" \
          "s_add_u32 m0, %[sl], 0\n\tv_mfma_f32_16x16x32_bf16 v[60:63], a[52:55], v[88:91], v[60:63]\n\tbuffer_load_dwordx4 %[vd0], s[40:43], %[xo] offen lds\n\tv_mfma_f32_16x16x32_bf16 v[56:59], a[56:59], v[92:95], v[56:59]\n\tds_read_b128 v[124:127], %[ra3] offset:33536\n\tv_mfma_f32_16x16x32_bf16 v[60:63], a[60:63], v[92:95], v[60:63]\n\t" \
          "s_waitcnt lgkmcnt(6)\n\tv_mfma_f32_16x16x32_bf16 v[56:59], a[64:67], v[96:99], v[56:59]\n\tds_read_b128 v[64:67], %[ra0] offset:33792\n\tv_mfma_f32_16x16x32_bf16 v[60:63], a[68:71], v[96:99], v[60:63]\n\tbuffer_load_dwordx4 %[vd0], s[40:43], %[xo] offen offset:1024 lds\n\tv_mfma_f32_16x16x32_bf16 v[56:59], a[72:75], v[100:103], v[56:59]\n\t" \
          "ds_read_b128 v[68:71], %[ra1] offset:33792\n\tv_mfma_f32_16x16x32_bf16 v[60:63], a[76:79], v[100:103], v[60:63]\n\ts_waitcnt lgkmcnt(6)\n\tv_mfma_f32_16x16x32_bf16 v[56:59], a[80:83], v[104:107], v[56:59]\n\tds_read_b128 v[72:75], %[ra2] offset:33792\n\tv_mfma_f32_16x16x32_bf16 v[60:63], a[84:87], v[104:107], v[60:63]\n\t" \
          "buffer_load_dwordx4 %[vd1], s[40:43], %[xo] offen offset:2048 lds\n\tv_mfma_f32_16x16x32_bf16 v[56:59], a[88:91], v[108:111], v[56:59]\n\tds_read_b128 v[76:79], %[ra3] offset:33792\n\tv_mfma_f32_16x16x32_bf16 v[60:63], a[92:95], v[108:111], v[60:63]\n\ts_waitcnt lgkmcnt(6)\n\tv_mfma_f32_16x16x32_bf16 v[56:59], a[96:99], v[112:115], v[56:59]\n\t" \
          "ds_read_b128 v[80:83], %[ra0] offset:34048\n\tv_mfma_f32_16x16x32_bf16 v[60:63], a[100:103], v[112:115], v[60:63]\n\tbuffer_load_dwordx4 %[vd1], s[40:43], %[xo] offen offset:3072 lds\n\tv_mfma_f32_16x16x32_bf16 v[56:59], a[104:107], v[116:119], v[56:59]\n\tds_read_b128 v[84:87], %[ra1] offset:34048\n\tv_mfma_f32_16x16x32_bf16 v[60:63], a[108:111], v[116:119], v[60:63]\n\t" \
          "s_waitcnt lgkmcnt(6)\n\tv_mfma_f32_16x16x32_bf16 v[56:59], a[112:115], v[120:123], v[56:59]\n\tds_read_b128 v[88:91], %[ra2] offset:34048\n\ts_add_u32 m0, %[sl], 4096\n\tv_mfma_f32_16x16x32_bf16 v[60:63], a[116:119], v[120:123], v[60:63]\n\tbuffer_load_dwordx4 %[vd2], s[40:43], %[xo] offen lds\n\t" \
          "v_mfma_f32_16x16x32_bf16 v[56:59], a[120:123], v[124:127], v[56:59]\n\tds_read_b128 v[92:95], %[ra3] offset:34048\n\tv_mfma_f32_16x16x32_bf16 v[60:63], a[124:127], v[124:127], v[60:63]\n\ts_waitcnt lgkmcnt(6)\n\tv_mfma_f32_16x16x32_bf16 v[56:59], a[128:131], v[64:67], v[56:59]\n\tds_read_b128 v[96:99], %[ra0] offset:34304\n\t" \
          "v_mfma_f32_16x16x32_bf16 v[60:63], a[132:135], v[64:67], v[60:63]\n\tbuffer_load_dwordx4 %[vd2], s[40:43], %[xo] offen offset:1024 lds\n\tv_mfma_f32_16x16x32_bf16 v[56:59], a[136:139], v[68:71], v[56:59]\n\tds_read_b128 v[100:103], %[ra1] offset:34304\n\tv_mfma_f32_16x16x32_bf16 v[60:63], a[140:143], v[68:71], v[60:63]\n\ts_waitcnt lgkmcnt(6)\n\t" \
          "v_mfma_f32_16x16x32_bf16 v[56:59], a[144:147], v[72:75], v[56:59]\n\tds_read_b128 v[104:107], %[ra2] offset:34304\n\tv_mfma_f32_16x16x32_bf16 v[60:63], a[148:151], v[72:75], v[60:63]\n\tbuffer_load_dwordx4 %[vd3], s[40:43], %[xo] offen offset:2048 lds\n\tv_mfma_f32_16x16x32_bf16 v[56:59], a[152:155], v[76:79], v[56:59]\n\tds_read_b128 v[108:111], %[ra3] offset:34304\n\t" \
          "v_mfma_f32_16x16x32_bf16 v[60:63], a[156:159], v[76:79], v[60:63]\n\ts_waitcnt lgkmcnt(6)\n\tv_mfma_f32_16x16x32_bf16 v[56:59], a[160:163], v[80:83], v[56:59]\n\tds_read_b128 v[112:115], %[ra0] offset:34560\n\tv_mfma_f32_16x16x32_bf16 v[60:63], a[164:167], v[80:83], v[60:63]\n\tbuffer_load_dwordx4 %[vd3], s[40:43], %[xo] offen offset:3072 lds\n\t" \
          "v_mfma_f32_16x16x32_bf16 v[56:59], a[168:171], v[84:87], v[56:59]\n\tds_read_b128 v[116:119], %[ra1] offset:34560\n\tv_mfma_f32_16x16x32_bf16 v[60:63], a[172:175], v[84:87], v[60:63]\n\ts_waitcnt lgkmcnt(6)\n\tv_mfma_f32_16x16x32_bf16 v[56:59], a[176:179], v[88:91], v[56:59]\n\tds_read_b128 v[120:123], %[ra2] offset:34560\n\t" \
          "v_mfma_f32_16x16x32_bf16 v[60:63], a[180:183], v[88:91], v[60:63]\n\tv_mfma_f32_16x16x32_bf16 v[56:59], a[184:187], v[92:95], v[56:59]\n\tds_read_b128 v[124:127], %[ra3] offset:34560\n\tv_mfma_f32_16x16x32_bf16 v[60:63], a[188:191], v[92:95], v[60:63]\n\ts_add_u32 %[xo], %[xo], 0x8000\n\ts_add_u32 %[sl], %[sl], 0x8000\n\t" \
          "s_bitset0_b32 %[sl], 17\n\ts_waitcnt lgkmcnt(6)\n\tv_mfma_f32_16x16x32_bf16 v[56:59], a[192:195], v[96:99], v[56:59]\n\tv_mfma_f32_16x16x32_bf16 v[60:63], a[196:199], v[96:99], v[60:63]\n\tv_xor_b32 %[ra0], 0x10000, %[ra0]\n\tv_mfma_f32_16x16x32_bf16 v[56:59], a[200:203], v[100:103], v[56:59]\n\t" \
          "v_mfma_f32_16x16x32_bf16 v[60:63], a[204:207], v[100:103], v[60:63]\n\tv_xor_b32 %[ra1], 0x10000, %[ra1]\n\ts_waitcnt lgkmcnt(4)\n\tv_mfma_f32_16x16x32_bf16 v[56:59], a[208:211], v[104:107], v[56:59]\n\tv_mfma_f32_16x16x32_bf16 v[60:63], a[212:215], v[104:107], v[60:63]\n\tv_xor_b32 %[ra2], 0x10000, %[ra2]\n\t" \
          "v_mfma_f32_16x16x32_bf16 v[56:59], a[216:219], v[108:111], v[56:59]\n\ts_add_u32 %[j], %[j], 2\n\ts_cmp_lt_u32 %[j], %[je]\n\tv_mfma_f32_16x16x32_bf16 v[60:63], a[220:223], v[108:111], v[60:63]\n\tv_xor_b32 %[ra3], 0x10000, %[ra3]\n\ts_waitcnt vmcnt(18) lgkmcnt(0)\n\t" \
          "s_barrier\n\tv_mfma_f32_16x16x32_bf16 v[56:59], a[224:227], v[112:115], v[56:59]\n\tds_read_b128 v[64:67], %[ra0] offset:0\n\tv_mfma_f32_16x16x32_bf16 v[60:63], a[228:231], v[112:115], v[60:63]\n\tds_read_b128 v[68:71], %[ra1] offset:0\n\tv_mfma_f32_16x16x32_bf16 v[56:59], a[232:235], v[116:119], v[56:59]\n\t" \
          "ds_read_b128 v[72:75], %[ra2] offset:0\n\tv_mfma_f32_16x16x32_bf16 v[60:63], a[236:239], v[116:119], v[60:63]\n\tds_read_b128 v[76:79], %[ra3] offset:0\n\tv_mfma_f32_16x16x32_bf16 v[56:59], a[240:243], v[120:123], v[56:59]\n\tds_read_b128 v[80:83], %[ra0] offset:256\n\tv_mfma_f32_16x16x32_bf16 v[60:63], a[244:247], v[120:123], v[60:63]\n\t" \
          "ds_read_b128 v[84:87], %[ra1] offset:256\n\tv_mfma_f32_16x16x32_bf16 v[56:59], a[248:251], v[124:127], v[56:59]\n\tds_read_b128 v[88:91], %[ra2] offset:256\n\tv_mfma_f32_16x16x32_bf16 v[60:63], a[252:255], v[124:127], v[60:63]\n\tds_read_b128 v[92:95], %[ra3] offset:256\n\ts_cbranch_scc1 Lpn_loop%=\n\t" \
          "s_nop 7\n\tv_mov_b32 %[fv], %[je]\n\tv_cvt_pk_bf16_f32 v44, v56, v57\n\tv_cvt_pk_bf16_f32 v45, v58, v59\n\tv_cvt_pk_bf16_f32 v46, v60, v61\n\tv_cvt_pk_bf16_f32 v47, v62, v63\n\t" \
          "buffer_store_dwordx4 v[44:47], %[so], %[rc], %[co] offen sc1\n\ts_waitcnt vmcnt(0) lgkmcnt(0)\n\ts_barrier\n\ts_mov_b64 exec, %[fx]\n\tbuffer_store_dword %[fv], off, %[rf], %[fo] sc1\n\ts_mov_b64 exec, -1\n\t"
#define PN_CLOB "v36", "v37", "v38", "v39", "v44", "v45", "v46", "v47", "v48", "v49", "v50", "v51", "v52", "v53", "v54", "v55", "v56", "v57", "v58", "v59", "v60", "v61", "v62", "v63", "v64", "v65", "v66", "v67", "v68", "v69", "v70", "v71", "v72", "v73", "v74", "v75", "v76", "v77", "v78", "v79", "v80", "v81", "v82", "v83", "v84", "v85", "v86", "v87", "v88", "v89", "v90", "v91", "v92", "v93", "v94", "v95", "v96", "v97", "v98", "v99", "v100", "v101", "v102", "v103", "v104", "v105", "v106", "v107", "v108", "v109", "v110", "v111", "v112", "v113", "v114", "v115", "v116", "v117", "v118", "v119", "v120", "v121", "v122", "v123", "v124", "v125", "v126", "v127", "a0", "a1", "a2", "a3", "a4", "a5", "a6", "a7", "a8", "a9", "a10", "a11", "a12", "a13", "a14", "a15", "a16", "a17", "a18", "a19", "a20", "a21", "a22", "a23", "a24", "a25", "a26", "a27", "a28", "a29", "a30", "a31", "a32", "a33", "a34", "a35", "a36", "a37", "a38", "a39", "a40", "a41", "a42", "a43", "a44", "a45", "a46", "a47", "a48", "a49", "a50", "a51", "a52", "a53", "a54", "a55", "a56", "a57", "a58", "a59", "a60", "a61", "a62", "a63", "a64", "a65", "a66", "a67", "a68", "a69", "a70", "a71", "a72", "a73", "a74", "a75", "a76", "a77", "a78", "a79", "a80", "a81", "a82", "a83", "a84", "a85", "a86", "a87", "a88", "a89", "a90", "a91", "a92", "a93", "a94", "a95", "a96", "a97", "a98", "a99", "a100", "a101", "a102", "a103", "a104", "a105", "a106", "a107", "a108", "a109", "a110", "a111", "a112", "a113", "a114", "a115", "a116", "a117", "a118", "a119", "a120", "a121", "a122", "a123", "a124", "a125", "a126", "a127", "a128", "a129", "a130", "a131", "a132", "a133", "a134", "a135", "a136", "a137", "a138", "a139", "a140", "a141", "a142", "a143", "a144", "a145", "a146", "a147", "a148", "a149", "a150", "a151", "a152", "a153", "a154", "a155", "a156", "a157", "a158", "a159", "a160", "a161", "a162", "a163", "a164", "a165", "a166", "a167", "a168", "a169", "a170", "a171", "a172", "a173", "a174", "a175", "a176", "a177", "a178", "a179", "a180", "a181", "a182", "a183", "a184", "a185", "a186", "a187", "a188", "a189", "a190", "a191", "a192", "a193", "a194", "a195", "a196", "a197", "a198", "a199", "a200", "a201", "a202", "a203", "a204", "a205", "a206", "a207", "a208", "a209", "a210", "a211", "a212", "a213", "a214", "a215", "a216", "a217", "a218", "a219", "a220", "a221", "a222", "a223", "a224", "a225", "a226", "a227", "a228", "a229", "a230", "a231", "a232", "a233", "a234", "a235", "a236", "a237", "a238", "a239", "a240", "a241", "a242", "a243", "a244", "a245", "a246", "a247", "a248", "a249", "a250", "a251", "a252", "a253", "a254", "a255", "s40", "s41", "s42", "s43", "s44", "s45"
#define NTP 0
template <bool NT>
__device__ __forceinline__ v4u panel_task(const FParams& F, int p, int fi, int j, int je, int w) {
  const int lane = opaque_lane(), l15 = lane & 15, lq = lane >> 4, m = p >> 5, ldb = __builtin_amdgcn_readfirstlane(m < 3 ? HDA * 2 : NQKVB * 2);
  const __amdgpu_buffer_rsrc_t rw = __builtin_amdgcn_make_buffer_rsrc(wg::uni((m == 0 ? F.Wq : (m == 1 ? F.Wk : (m == 2 ? F.Wv : F.wqkvB))) + (p & 31) * 128), 0, DM * ldb - (p & 31) * 256, 0x00020000);
  const __amdgpu_buffer_rsrc_t rf = __builtin_amdgcn_make_buffer_rsrc(F.ctl, 0, sizeof(Ctl), 0x00020000), rr = __builtin_amdgcn_make_buffer_rsrc(F.rdy, 0, RDY_SIZE, 0x00020000);
  void* const xa = wg::uni(m < 3 ? F.Ax : F.Bx);
  int ra[4], vd[4], rt[4], vw[2], fv = 0, xo = w * 8192 + j * 32768, sl = w * 8192, co = j * QBLK, cs = 0, t; unsigned long long sx;
  v4u rec;
#pragma unroll
  for (int i = 0; i < 4; ++i) {
    ra[i] = l15 * 2048 + (((4 * i + lq) ^ l15) << 4); vd[i] = ((lane ^ (4 * w + i)) << 4) + (i >> 1) * 4096;
    rt[i] = 65536 + (i >> 1) * 49152 + (8 * lq + (l15 >> 2)) * 256 + (((2 * w + (i & 1)) ^ ((l15 >> 2) | ((lq & 1) << 2))) << 5) + (l15 & 3) * 8;
  }
  vw[0] = (16 * w + lq) * ldb + ((l15 ^ (lq << 1)) << 4); vw[1] = vw[0] ^ 128;
  const __amdgpu_buffer_rsrc_t rc = __builtin_amdgcn_make_buffer_rsrc((char*)F.qkvA + __builtin_amdgcn_readfirstlane((p >> 1) * QPAGE + (p & 1) * 4096 + w * 1024), 0, QPAGE - 8192 + 1024, 0x00020000);
  const bool kv = m == 1 || m == 2;
  const __amdgpu_buffer_rsrc_t rk = __builtin_amdgcn_make_buffer_rsrc((char*)F.qkvA + __builtin_amdgcn_readfirstlane(kvoff(0, p & 31) + (m & 2) * 1024 + w * 512), 0, QPAGE - 2 * QBLK + 8192 + 512, 0x00020000);
  int ko;
#define PN_OPS : "=&{v[40:43]}"(rec), [ra0] "+v"(ra[0]), [ra1] "+v"(ra[1]), [ra2] "+v"(ra[2]), [ra3] "+v"(ra[3]), [fv] "+v"(fv), [xo] "+s"(xo), [sl] "+s"(sl), [co] "+s"(co), [cs] "+s"(cs), [t] "=&s"(t), [j] "+s"(j), [sx] "=&s"(sx), [ko] "=&s"(ko) \
    : [vd0] "v"(vd[0]), [vd1] "v"(vd[1]), [vd2] "v"(vd[2]), [vd3] "v"(vd[3]), [rt0] "v"(rt[0]), [rt1] "v"(rt[1]), [rt2] "v"(rt[2]), [rt3] "v"(rt[3]), [vw0] "v"(vw[0]), [vw1] "v"(vw[1]), [ro] "v"(lane * 128), [so] "v"(lane * 16), [sk] "v"(lane < 32 && kv ? lane * 16 : 0x7ffffff0), \
      [xa] "s"(xa), [rw] "s"(rw), [rr] "s"(rr), [rc] "s"(rc), [rf] "s"(rf), [wl] "s"(w * 4096), [ldb] "s"(ldb), [fo] "s"((int)offsetof(Ctl, prg) + fi * 128), [je] "s"(je), \
      [fx] "s"((unsigned long long)(unsigned)__builtin_amdgcn_readfirstlane(w == 0)), [pl] "s"(p >> 1), [rk] "s"(rk), [kl] "s"((p & 31) >> 2) : PN_CLOB, "vcc", "scc", "memory"
  if constexpr (NT) asm volatile(PN_BODY(" nt") PN_OPS); else asm volatile(PN_BODY("") PN_OPS);
  return rec;
}

struct AttnX { v4u q[2][4], kx[2][4], vr[8]; };
template <int AUX = 0>
__device__ __forceinline__ void attnA_load_x(const FParams& F, unsigned long long parq, int seg, int h, int lane, AttnX& X) {
  const __amdgpu_buffer_rsrc_t rs = __builtin_amdgcn_make_buffer_rsrc(F.qkvA, 0, QSIZE, 0x00020000);
  const int so = seg * 2 * QBLK + (h & 1) * 4096, oq = so + qoff(parq, h >> 1), ok = so + qoff(parq, 16 + (h >> 1)), ov = so + qoff(parq, 32 + (h >> 1));
#pragma unroll
  for (int i = 0; i < 8; ++i) X.vr[i] = __builtin_amdgcn_raw_buffer_load_b128(rs, lane * 16 + (i & 3) * 1024, ov + (i >> 2) * QBLK, AUX);
#pragma unroll
  for (int t = 0; t < 2; ++t)
#pragma unroll
    for (int j = 0; j < 4; ++j) {
      X.q[t][j] = __builtin_amdgcn_raw_buffer_load_b128(rs, lane * 16 + j * 1024, oq + t * QBLK, AUX);
      X.kx[t][j] = __builtin_amdgcn_raw_buffer_load_b128(rs, lane * 16 + j * 1024, ok + t * QBLK, AUX);
    }
}
__device__ __forceinline__ void attnA_stage_v(const AttnX& X, u16* vs  , int lane) {
#pragma unroll
  for (int i = 0; i < 8; ++i) *(v4u*)&vs[(16 * (i >> 2) + (lane & 15)) * VXLD + 32 * (i & 3) + 8 * (lane >> 4)] = X.vr[i];
}
template <int WHAT = 3>
__device__ __forceinline__ unsigned attnA_load_m(const FParams& F, int seg, int ko, int lane, v4u (&km)[4], unsigned (&vm)[8]) {
  const int l15 = lane & 15, lq = lane >> 4;
  const __amdgpu_buffer_rsrc_t rk = __builtin_amdgcn_make_buffer_rsrc((char*)F.qkvA + ko + seg * 2 * QBLK, 0, 4096, 0x00020000);
#pragma unroll
  for (int j = 0; j < 4; ++j)
    if (WHAT & 1) km[j] = __builtin_amdgcn_raw_buffer_load_b128(rk, (2 * (lane >> 2) + (lane & 1)) * 16 + 512 * j, 0, 16);
#pragma unroll
  for (int t = 0; t < 8; ++t)
    if (WHAT & 2) vm[t] = __builtin_amdgcn_raw_buffer_load_b32(rk, 2048 + (((l15 >> 3) * 4 + lq) * 16 + 8 * ((l15 >> 2) & 1) + (l15 & 3)) * 4 + 512 * (t >> 1) + 16 * (t & 1), 0, 16);
  unsigned m = 0;
#pragma unroll
  for (int j = 0; j < 4; ++j) if (WHAT & 1) m = chk4(m, km[j]);
#pragma unroll
  for (int t = 0; t < 8; t += 2) if (WHAT & 2) m = umax3(m, vm[t], vm[t + 1]);
  return m;
}
__device__ __forceinline__ void attnA_compute(const AttnX& X, const v4u (&km)[4], const unsigned (&vm)[8], const u16* vs, int lane, v4u (&ov)[2][4]) {
  const int l15 = lane & 15, lq = lane >> 4;
  const float scale = 0.08838834764831845f, L2E = 1.4426950408889634f;
  v4u px[2], pm[2];
#pragma unroll
  for (int t = 0; t < 2; ++t) {
    f32x4 s0 = {0.f,0.f,0.f,0.f}, s1 = s0, s2 = s0;
#pragma unroll
    for (int j = 0; j < 4; ++j) { s0 = mf(km[j], X.q[t][j], s0); s1 = mf(X.kx[0][j], X.q[t][j], s1); if (t == 1) s2 = mf(X.kx[1][j], X.q[t][j], s2); }
    float sv[12];
#pragma unroll
    for (int e = 0; e < 4; ++e) { sv[e] = s0[e]; sv[4 + e] = s1[e]; sv[8 + e] = s2[e]; }
#pragma unroll
    for (int e = 0; e < 12; e += 2) {
      unsigned p = pk2(sv[e], sv[e + 1]);
      p = pk2(__uint_as_float(p << 16) * scale, __uint_as_float(p & 0xffff0000u) * scale);
      sv[e] = __uint_as_float(p << 16); sv[e + 1] = __uint_as_float(p & 0xffff0000u);
    }
    bool ok[12];
#pragma unroll
    for (int e = 0; e < 4; ++e) { ok[e] = e < 2; ok[4 + e] = (t == 1) || (4 * lq + e <= l15); ok[8 + e] = (t == 1) && (4 * lq + e <= l15); }
    float mx = -INFINITY;
#pragma unroll
    for (int e = 0; e < 12; ++e) mx = ok[e] ? fmaxf(mx, sv[e]) : mx;
    mx = xmax32(xmax16(mx));
    float sum = 0.f;
#pragma unroll
    for (int e = 0; e < 12; ++e) { sv[e] = ok[e] ? __builtin_amdgcn_exp2f((sv[e] - mx) * L2E) : 0.f; sum += sv[e]; }
    sum = xsum32(xsum16(sum));
    const float inv = __builtin_amdgcn_rcpf(sum);
#pragma unroll
    for (int e = 0; e < 12; ++e) sv[e] = ok[e] ? sv[e] * inv : 0.f;
    px[t] = (v4u){pk2(sv[4], sv[5]), pk2(sv[6], sv[7]), pk2(sv[8], sv[9]), pk2(sv[10], sv[11])};
    pm[t] = (v4u){pk2(sv[0], sv[1]), 0u, 0u, 0u};
  }
  const u16* vp = &vs[(4 * lq + (l15 >> 2)) * VXLD + 16 * (l15 & 1) + 2 * (l15 & 2)];
#pragma unroll
  for (int u = 0; u < 4; ++u) {
    f32x4 o[2][2];
#pragma unroll
    for (int ab = 0; ab < 2; ++ab) {
      v2u lo = trrd(vp + 32 * u + 8 * ab), hi = trrd(vp + 16 * VXLD + 32 * u + 8 * ab);
      const v4u va = {lo.x, lo.y, hi.x, hi.y}, vb = {vm[2 * u + ab], 0u, 0u, 0u};
#pragma unroll
      for (int t = 0; t < 2; ++t) { o[t][ab] = mf(va, px[t], (f32x4){0.f,0.f,0.f,0.f}); o[t][ab] = mf(vb, pm[t], o[t][ab]); }
    }
#pragma unroll
    for (int t = 0; t < 2; ++t)
      ov[t][u] = (v4u){pk2(o[t][0][0], o[t][0][1]), pk2(o[t][0][2], o[t][0][3]), pk2(o[t][1][0], o[t][1][1]), pk2(o[t][1][2], o[t][1][3])};
  }
}
__device__ __forceinline__ void attnA_block(const FParams& F, unsigned long long parq, unsigned pk, int r, int h, u16* vs, int lane, int w, unsigned* shw) {
  AttnX X[2]; v4u km[2][4]; unsigned vm[2][8];
  unsigned seen = 0;
  attnA_load_x(F, parq, 4 * r, h, lane, X[0]);
  bool have = false;
#pragma unroll
  for (int i = 0; i < 4; ++i) {
    const int seg = 4 * r + i, c = i & 1;
    v4u ov[2][4];
    if (!have) {
      if (seen < (unsigned)seg + 1) {
        if (w == 0) {
          for (unsigned spins = 0;; ++spins) {
            seen = __builtin_amdgcn_readfirstlane(ld_agent(&F.ctl->prog[0]));
            if (seen >= (unsigned)seg + 1 || spin_fail(spins, &F.ctl->tmo[0], 6u)) break;
            for (unsigned z = seen; z < (unsigned)seg + 1; ++z) __builtin_amdgcn_s_sleep(PSL);
          }
          *shw = seen;
        }
        __syncthreads();
        seen = __builtin_amdgcn_readfirstlane(*shw);
        __syncthreads();
      }
      asm volatile("" ::: "memory");
      (void)attnA_load_m(F, seg, kvoff(pk, h), lane, km[c], vm[c]);
    }
    if (i < 3) {
      attnA_load_x(F, parq, seg + 1, h, lane, X[c ^ 1]);
      have = seen >= (unsigned)seg + 2;
      (void)attnA_load_m(F, have ? seg + 1 : seg, kvoff(pk, h), lane, km[c ^ 1], vm[c ^ 1]);
    }
    SB0;
    attnA_stage_v(X[c], vs, lane);
    attnA_compute(X[c], km[c], vm[c], vs, lane, ov);
    const __amdgpu_buffer_rsrc_t rs = __builtin_amdgcn_make_buffer_rsrc(F.qkvA, 0, QSIZE, 0x00020000);
#pragma unroll
    for (int u = 0; u < 4; ++u)
#pragma unroll
      for (int t = 0; t < 2; ++t)
        __builtin_amdgcn_raw_buffer_store_b128(ov[t][u], rs, lane * 16 + u * 1024, qoff(parq, h >> 1) + (seg * 2 + t) * QBLK + (h & 1) * 4096, 16);
    SB0;
  }
}

constexpr int TSEG0 = 32, OSEG0 = 16, NSET = 4;
__device__ __forceinline__ void attn_stream(const FParams& F, unsigned long long parq, unsigned pk, int set, int hg, char* lds, int w) {
  const int lane = opaque_lane(), l15 = lane & 15, lq = lane >> 4, h = 4 * hg + w;
  const float L2E = 1.4426950408889634f;
  auto rnd2 = [](float& a, float& b) { const float scale = 0.08838834764831845f; unsigned p = pk2(a, b); p = pk2(__uint_as_float(p << 16) * scale, __uint_as_float(p & 0xffff0000u) * scale); a = __uint_as_float(p << 16); b = __uint_as_float(p & 0xffff0000u); };
  Ctl* ctl = F.ctl;
  unsigned have = 0;
  auto need = [&](int sg) {
    for (unsigned spins = 0; !__all(have >= 2u * sg + 2u); ++spins) {
      if (spins) __builtin_amdgcn_s_sleep(16);
      have = ld_agent(&ctl->prg[(lane < 2 ? lane : 3) * 32 + h][0]);
      if (spin_fail(spins, &ctl->tmo[0], 2u)) break;
    }
    asm volatile("" ::: "memory");
  };
  static_assert(TSEG0 >= 32, "upper q panel");
  need(TSEG0 + set);
  u16* vs = (u16*)(lds + w * 16384);
  AttnX X; int seen = 0; unsigned dur = 170;
  attnA_load_x<16>(F, parq, TSEG0 + set, h, lane, X);
#pragma unroll 1
  for (int seg = TSEG0 + set; seg < NSEG; seg += NSET) {
    v4u km[4]; unsigned vm[8]; f32x4 o[2][8]; float mx[2], ls[2];
    attnA_stage_v(X, vs, lane);
    { v4u px[2];
#pragma unroll
      for (int t = 0; t < 2; ++t) {
        f32x4 s1 = {0.f, 0.f, 0.f, 0.f}, s2 = s1;
#pragma unroll
        for (int j = 0; j < 4; ++j) { s1 = mf(X.kx[0][j], X.q[t][j], s1); if (t == 1) s2 = mf(X.kx[1][j], X.q[t][j], s2); }
        float sv[8]; bool ok[8];
#pragma unroll
        for (int e = 0; e < 4; ++e) { sv[e] = s1[e]; sv[4 + e] = s2[e]; ok[e] = (t == 1) || (4 * lq + e <= l15); ok[4 + e] = (t == 1) && (4 * lq + e <= l15); }
#pragma unroll
        for (int e = 0; e < 8; e += 2) rnd2(sv[e], sv[e + 1]);
        float m = -INFINITY, sum = 0.f;
#pragma unroll
        for (int e = 0; e < 8; ++e) m = ok[e] ? fmaxf(m, sv[e]) : m;
        m = xmax32(xmax16(m));
#pragma unroll
        for (int e = 0; e < 8; ++e) { sv[e] = ok[e] ? __builtin_amdgcn_exp2f((sv[e] - m) * L2E) : 0.f; sum += sv[e]; }
        mx[t] = m; ls[t] = sum;
        px[t] = (v4u){pk2(sv[0], sv[1]), pk2(sv[2], sv[3]), pk2(sv[4], sv[5]), pk2(sv[6], sv[7])};
      }
      const u16* vp = &vs[(4 * lq + (l15 >> 2)) * VXLD + 16 * (l15 & 1) + 2 * (l15 & 2)];
#pragma unroll
      for (int i = 0; i < 8; ++i) {
        const v2u lo = trrd(vp + 32 * (i >> 1) + 8 * (i & 1)), hi = trrd(vp + 16 * VXLD + 32 * (i >> 1) + 8 * (i & 1));
#pragma unroll
        for (int t = 0; t < 2; ++t) { o[t][i] = mf((v4u){lo.x, lo.y, hi.x, hi.y}, px[t], (f32x4){0.f, 0.f, 0.f, 0.f}); asm volatile("" : "+v"(o[t][i])); }
      } }
    const __amdgpu_buffer_rsrc_t rs = __builtin_amdgcn_make_buffer_rsrc(F.qkvA, 0, QSIZE, 0x00020000);
    const int oo = qoff(parq, h >> 1) + seg * 2 * QBLK + (h & 1) * 4096;
    if (seg >= NSEG - 2) {
#pragma unroll
      for (int u = 0; u < 8; ++u) __builtin_amdgcn_raw_buffer_store_b128((v4u){~0u, ~0u, ~0u, ~0u}, rs, lane * 16 + (u >> 1) * 1024, oo + (u & 1) * QBLK, 16);
    }
    if (seg + NSET < NSEG) need(seg + NSET);
    unsigned due = 0;
    for (unsigned spins = 0; seen <= seg - 3; ++spins) {
      const unsigned long long pv = __hip_atomic_load((unsigned long long*)&ctl->prog[0], RLX_AGENT);
      seen = __builtin_amdgcn_readfirstlane((unsigned)pv); due = __builtin_amdgcn_readfirstlane((unsigned)(pv >> 32)) + TP(F.x1, XA1);
      if (seen > seg - 3 || spin_fail(spins, &ctl->tmo[0], 6u)) break;
      __builtin_amdgcn_s_sleep(24);
    }
    if (seen != seg - 2 || (unsigned)(due - (unsigned)__builtin_amdgcn_s_memrealtime()) > 1000u) due = 0;
    if (seg >= NSEG - 2) {
      asm volatile("s_waitcnt vmcnt(0)\n\ts_barrier" ::: "memory");
      if (w == 0 && lane == 0 && due) __hip_atomic_store(&ctl->flagO[seg][hg][1], (due + dur) | 1u, RLX_AGENT);
    }
    if (due) while ((int)(due - (unsigned)__builtin_amdgcn_s_memrealtime()) > 0) __builtin_amdgcn_s_sleep(1);
    asm volatile("" ::: "memory");
    unsigned vc;
    for (unsigned spins = 0;; ++spins) {
      const unsigned kc = attnA_load_m<1>(F, seg, kvoff(pk, h), lane, km, vm);
      vc = attnA_load_m<2>(F, seg, kvoff(pk, h), lane, km, vm);
      if (__all(kc != 0xffffffffu) || spin_fail(spins, &ctl->tmo[0], 4u)) break;
      __builtin_amdgcn_s_sleep(TSL);
    }
#define ELOG(i)
    v4u pm[2];
#pragma unroll
    for (int t = 0; t < 2; ++t) {
      f32x4 s0 = {0.f, 0.f, 0.f, 0.f};
#pragma unroll
      for (int j = 0; j < 4; ++j) s0 = mf(km[j], X.q[t][j], s0);
      float a = s0[0], c = s0[1];
      rnd2(a, c);
      const float m = fmaxf(mx[t], xmax32(xmax16(fmaxf(a, c)))), al = __builtin_amdgcn_exp2f((mx[t] - m) * L2E);
      a = __builtin_amdgcn_exp2f((a - m) * L2E); c = __builtin_amdgcn_exp2f((c - m) * L2E);
      const float inv = __builtin_amdgcn_rcpf(xsum32(xsum16(ls[t] * al + a + c))), sc = al * inv;
      pm[t] = (v4u){pk2(a * inv, c * inv), 0u, 0u, 0u};
#pragma unroll
      for (int i = 0; i < 8; ++i) { o[t][i] *= sc; asm volatile("" : "+v"(o[t][i])); }
    }
    SB0;
    for (unsigned spins = 0; !__all(vc != 0xffffffffu) && !spin_fail(spins, &ctl->tmo[0], 4u); ++spins) vc = attnA_load_m<2>(F, seg, kvoff(pk, h), lane, km, vm);
#pragma unroll
    for (int u = 0; u < 4; ++u)
#pragma unroll
      for (int t = 0; t < 2; ++t) {
        const f32x4 y0 = mf((v4u){vm[2 * u], 0u, 0u, 0u}, pm[t], o[t][2 * u]), y1 = mf((v4u){vm[2 * u + 1], 0u, 0u, 0u}, pm[t], o[t][2 * u + 1]);
        __builtin_amdgcn_raw_buffer_store_b128((v4u){pk2(y0[0], y0[1]), pk2(y0[2], y0[3]), pk2(y1[0], y1[1]), pk2(y1[2], y1[3])}, rs, lane * 16 + u * 1024, oo + t * QBLK, 16);
      }
    SB0;
    if (seg + 2 * NSET < NSEG) { attnA_load_x<16>(F, parq, seg + NSET, h, lane, X); SB0; asm volatile("s_waitcnt vmcnt(24)" ::: "memory"); }
    else asm volatile("s_waitcnt vmcnt(0)" ::: "memory");
    asm volatile("s_barrier" ::: "memory");
    if (w == 0 && lane == 0) __hip_atomic_store(&ctl->flagO[seg][hg][0], 1u, RLX_AGENT);
    if (due) dur = min((unsigned)__builtin_amdgcn_s_memrealtime() - due - TP(F.x2, XA2), 170u);
    SB0;
    if (seg + NSET < NSEG && seg + 2 * NSET >= NSEG) attnA_load_x<16>(F, parq, seg + NSET, h, lane, X);
    ELOG(2);
  }
}
#define OU_AUX ""
#define OU_ST0
#define OU_ST1
#define OU_STO
#define OU_BODY \
          "s_mov_b64 s[40:41], %[qa]\n\ts_mov_b32 s42, %[qs]\n\ts_mov_b32 s43, 0x20000\n\ts_mov_b64 s[44:45], %[qa]\n\ts_mov_b32 s47, 0x20000\n\ts_nop 4\n\t" \
          "s_add_u32 m0, %[wl], 0\n\ts_xor_b32 %[t], %[wo], 0x0\n\tbuffer_load_dwordx4 %[vw0], %[rw], %[t] offen lds\n\ts_add_u32 m0, %[wl], 1024\n\ts_xor_b32 %[t], %[wo], 0x4000\n\tbuffer_load_dwordx4 %[vw1], %[rw], %[t] offen lds\n\t" \
          "s_add_u32 m0, %[wl], 2048\n\ts_xor_b32 %[t], %[wo], 0x8000\n\tbuffer_load_dwordx4 %[vw0], %[rw], %[t] offen lds\n\ts_add_u32 m0, %[wl], 3072\n\ts_xor_b32 %[t], %[wo], 0xc000\n\tbuffer_load_dwordx4 %[vw1], %[rw], %[t] offen lds\n\t" \
          "s_add_u32 m0, %[wl], 4096\n\ts_xor_b32 %[t], %[wo], 0x10000\n\tbuffer_load_dwordx4 %[vw0], %[rw], %[t] offen lds\n\ts_add_u32 m0, %[wl], 5120\n\ts_xor_b32 %[t], %[wo], 0x14000\n\tbuffer_load_dwordx4 %[vw1], %[rw], %[t] offen lds\n\t" \
          "s_add_u32 m0, %[wl], 6144\n\ts_xor_b32 %[t], %[wo], 0x18000\n\tbuffer_load_dwordx4 %[vw0], %[rw], %[t] offen lds\n\ts_add_u32 m0, %[wl], 7168\n\ts_xor_b32 %[t], %[wo], 0x1c000\n\tbuffer_load_dwordx4 %[vw1], %[rw], %[t] offen lds\n\t" \
          "s_add_u32 m0, %[wl], 8192\n\ts_xor_b32 %[t], %[wo], 0x20000\n\tbuffer_load_dwordx4 %[vw0], %[rw], %[t] offen lds\n\ts_add_u32 m0, %[wl], 9216\n\ts_xor_b32 %[t], %[wo], 0x24000\n\tbuffer_load_dwordx4 %[vw1], %[rw], %[t] offen lds\n\t" \
          "s_add_u32 m0, %[wl], 10240\n\ts_xor_b32 %[t], %[wo], 0x28000\n\tbuffer_load_dwordx4 %[vw0], %[rw], %[t] offen lds\n\ts_add_u32 m0, %[wl], 11264\n\ts_xor_b32 %[t], %[wo], 0x2c000\n\tbuffer_load_dwordx4 %[vw1], %[rw], %[t] offen lds\n\t" \
          "s_add_u32 m0, %[wl], 12288\n\ts_xor_b32 %[t], %[wo], 0x30000\n\tbuffer_load_dwordx4 %[vw0], %[rw], %[t] offen lds\n\ts_add_u32 m0, %[wl], 13312\n\ts_xor_b32 %[t], %[wo], 0x34000\n\tbuffer_load_dwordx4 %[vw1], %[rw], %[t] offen lds\n\t" \
          "s_add_u32 m0, %[wl], 14336\n\ts_xor_b32 %[t], %[wo], 0x38000\n\tbuffer_load_dwordx4 %[vw0], %[rw], %[t] offen lds\n\ts_add_u32 m0, %[wl], 15360\n\ts_xor_b32 %[t], %[wo], 0x3c000\n\tbuffer_load_dwordx4 %[vw1], %[rw], %[t] offen lds\n\t" \
          "s_add_u32 m0, %[wl], 16384\n\ts_xor_b32 %[t], %[wo], 0x40000\n\tbuffer_load_dwordx4 %[vw0], %[rw], %[t] offen lds\n\ts_add_u32 m0, %[wl], 17408\n\ts_xor_b32 %[t], %[wo], 0x44000\n\tbuffer_load_dwordx4 %[vw1], %[rw], %[t] offen lds\n\t" \
          "s_add_u32 m0, %[wl], 18432\n\ts_xor_b32 %[t], %[wo], 0x48000\n\tbuffer_load_dwordx4 %[vw0], %[rw], %[t] offen lds\n\ts_add_u32 m0, %[wl], 19456\n\ts_xor_b32 %[t], %[wo], 0x4c000\n\tbuffer_load_dwordx4 %[vw1], %[rw], %[t] offen lds\n\t" \
          "s_add_u32 m0, %[wl], 20480\n\ts_xor_b32 %[t], %[wo], 0x50000\n\tbuffer_load_dwordx4 %[vw0], %[rw], %[t] offen lds\n\ts_add_u32 m0, %[wl], 21504\n\ts_xor_b32 %[t], %[wo], 0x54000\n\tbuffer_load_dwordx4 %[vw1], %[rw], %[t] offen lds\n\t" \
          "s_add_u32 m0, %[wl], 22528\n\ts_xor_b32 %[t], %[wo], 0x58000\n\tbuffer_load_dwordx4 %[vw0], %[rw], %[t] offen lds\n\ts_add_u32 m0, %[wl], 23552\n\ts_xor_b32 %[t], %[wo], 0x5c000\n\tbuffer_load_dwordx4 %[vw1], %[rw], %[t] offen lds\n\t" \
          "s_add_u32 m0, %[wl], 24576\n\ts_xor_b32 %[t], %[wo], 0x60000\n\tbuffer_load_dwordx4 %[vw0], %[rw], %[t] offen lds\n\ts_add_u32 m0, %[wl], 25600\n\ts_xor_b32 %[t], %[wo], 0x64000\n\tbuffer_load_dwordx4 %[vw1], %[rw], %[t] offen lds\n\t" \
          "s_add_u32 m0, %[wl], 26624\n\ts_xor_b32 %[t], %[wo], 0x68000\n\tbuffer_load_dwordx4 %[vw0], %[rw], %[t] offen lds\n\ts_add_u32 m0, %[wl], 27648\n\ts_xor_b32 %[t], %[wo], 0x6c000\n\tbuffer_load_dwordx4 %[vw1], %[rw], %[t] offen lds\n\t" \
          "s_add_u32 m0, %[wl], 28672\n\ts_xor_b32 %[t], %[wo], 0x70000\n\tbuffer_load_dwordx4 %[vw0], %[rw], %[t] offen lds\n\ts_add_u32 m0, %[wl], 29696\n\ts_xor_b32 %[t], %[wo], 0x74000\n\tbuffer_load_dwordx4 %[vw1], %[rw], %[t] offen lds\n\t" \
          "s_add_u32 m0, %[wl], 30720\n\ts_xor_b32 %[t], %[wo], 0x78000\n\tbuffer_load_dwordx4 %[vw0], %[rw], %[t] offen lds\n\ts_add_u32 m0, %[wl], 31744\n\ts_xor_b32 %[t], %[wo], 0x7c000\n\tbuffer_load_dwordx4 %[vw1], %[rw], %[t] offen lds\n\t" \
          "s_add_u32 m0, %[wl], 32768\n\ts_xor_b32 %[t], %[wo], 0x80000\n\tbuffer_load_dwordx4 %[vw0], %[rw], %[t] offen lds\n\ts_add_u32 m0, %[wl], 33792\n\ts_xor_b32 %[t], %[wo], 0x84000\n\tbuffer_load_dwordx4 %[vw1], %[rw], %[t] offen lds\n\t" \
          "s_add_u32 m0, %[wl], 34816\n\ts_xor_b32 %[t], %[wo], 0x88000\n\tbuffer_load_dwordx4 %[vw0], %[rw], %[t] offen lds\n\ts_add_u32 m0, %[wl], 35840\n\ts_xor_b32 %[t], %[wo], 0x8c000\n\tbuffer_load_dwordx4 %[vw1], %[rw], %[t] offen lds\n\t" \
          "s_add_u32 m0, %[wl], 36864\n\ts_xor_b32 %[t], %[wo], 0x90000\n\tbuffer_load_dwordx4 %[vw0], %[rw], %[t] offen lds\n\ts_add_u32 m0, %[wl], 37888\n\ts_xor_b32 %[t], %[wo], 0x94000\n\tbuffer_load_dwordx4 %[vw1], %[rw], %[t] offen lds\n\t" \
          "s_add_u32 m0, %[wl], 38912\n\ts_xor_b32 %[t], %[wo], 0x98000\n\tbuffer_load_dwordx4 %[vw0], %[rw], %[t] offen lds\n\ts_add_u32 m0, %[wl], 39936\n\ts_xor_b32 %[t], %[wo], 0x9c000\n\tbuffer_load_dwordx4 %[vw1], %[rw], %[t] offen lds\n\t" \
          "s_waitcnt vmcnt(32)\n\tds_read_b64_tr_b16 a[0:1], %[rt0] offset:0\n\tds_read_b64_tr_b16 a[2:3], %[rt0] offset:512\n\tds_read_b64_tr_b16 a[4:5], %[rt1] offset:0\n\tds_read_b64_tr_b16 a[6:7], %[rt1] offset:512\n\tds_read_b64_tr_b16 v[128:129], %[rt2] offset:0\n\t" \
          "ds_read_b64_tr_b16 v[130:131], %[rt2] offset:512\n\tds_read_b64_tr_b16 v[32:33], %[rt3] offset:0\n\tds_read_b64_tr_b16 v[34:35], %[rt3] offset:512\n\tds_read_b64_tr_b16 a[8:9], %[rt0] offset:4096\n\tds_read_b64_tr_b16 a[10:11], %[rt0] offset:4608\n\tds_read_b64_tr_b16 a[12:13], %[rt1] offset:4096\n\t" \
          "ds_read_b64_tr_b16 a[14:15], %[rt1] offset:4608\n\tds_read_b64_tr_b16 v[132:133], %[rt2] offset:4096\n\tds_read_b64_tr_b16 v[134:135], %[rt2] offset:4608\n\tds_read_b64_tr_b16 v[36:37], %[rt3] offset:4096\n\tds_read_b64_tr_b16 v[38:39], %[rt3] offset:4608\n\ts_waitcnt lgkmcnt(0)\n\t" \
          "s_add_u32 m0, %[wl], 0\n\ts_xor_b32 %[t], %[wo], 0xa0000\n\tbuffer_load_dwordx4 %[vw0], %[rw], %[t] offen lds\n\ts_add_u32 m0, %[wl], 1024\n\ts_xor_b32 %[t], %[wo], 0xa4000\n\tbuffer_load_dwordx4 %[vw1], %[rw], %[t] offen lds\n\t" \
          "s_add_u32 m0, %[wl], 2048\n\ts_xor_b32 %[t], %[wo], 0xa8000\n\tbuffer_load_dwordx4 %[vw0], %[rw], %[t] offen lds\n\ts_add_u32 m0, %[wl], 3072\n\ts_xor_b32 %[t], %[wo], 0xac000\n\tbuffer_load_dwordx4 %[vw1], %[rw], %[t] offen lds\n\t" \
          "s_add_u32 m0, %[wl], 4096\n\ts_xor_b32 %[t], %[wo], 0xb0000\n\tbuffer_load_dwordx4 %[vw0], %[rw], %[t] offen lds\n\ts_add_u32 m0, %[wl], 5120\n\ts_xor_b32 %[t], %[wo], 0xb4000\n\tbuffer_load_dwordx4 %[vw1], %[rw], %[t] offen lds\n\t" \
          "s_add_u32 m0, %[wl], 6144\n\ts_xor_b32 %[t], %[wo], 0xb8000\n\tbuffer_load_dwordx4 %[vw0], %[rw], %[t] offen lds\n\ts_add_u32 m0, %[wl], 7168\n\ts_xor_b32 %[t], %[wo], 0xbc000\n\tbuffer_load_dwordx4 %[vw1], %[rw], %[t] offen lds\n\t" \
          "s_waitcnt vmcnt(32)\n\tds_read_b64_tr_b16 a[16:17], %[rt0] offset:8192\n\tds_read_b64_tr_b16 a[18:19], %[rt0] offset:8704\n\tds_read_b64_tr_b16 a[20:21], %[rt1] offset:8192\n\tds_read_b64_tr_b16 a[22:23], %[rt1] offset:8704\n\tds_read_b64_tr_b16 v[136:137], %[rt2] offset:8192\n\t" \
          "ds_read_b64_tr_b16 v[138:139], %[rt2] offset:8704\n\tds_read_b64_tr_b16 v[40:41], %[rt3] offset:8192\n\tds_read_b64_tr_b16 v[42:43], %[rt3] offset:8704\n\tds_read_b64_tr_b16 a[24:25], %[rt0] offset:12288\n\tds_read_b64_tr_b16 a[26:27], %[rt0] offset:12800\n\tds_read_b64_tr_b16 a[28:29], %[rt1] offset:12288\n\t" \
          "ds_read_b64_tr_b16 a[30:31], %[rt1] offset:12800\n\tds_read_b64_tr_b16 v[140:141], %[rt2] offset:12288\n\tds_read_b64_tr_b16 v[142:143], %[rt2] offset:12800\n\tds_read_b64_tr_b16 v[44:45], %[rt3] offset:12288\n\tds_read_b64_tr_b16 v[46:47], %[rt3] offset:12800\n\ts_waitcnt lgkmcnt(0)\n\t" \
          "s_add_u32 m0, %[wl], 8192\n\ts_xor_b32 %[t], %[wo], 0xc0000\n\tbuffer_load_dwordx4 %[vw0], %[rw], %[t] offen lds\n\ts_add_u32 m0, %[wl], 9216\n\ts_xor_b32 %[t], %[wo], 0xc4000\n\tbuffer_load_dwordx4 %[vw1], %[rw], %[t] offen lds\n\t" \
          "s_add_u32 m0, %[wl], 10240\n\ts_xor_b32 %[t], %[wo], 0xc8000\n\tbuffer_load_dwordx4 %[vw0], %[rw], %[t] offen lds\n\ts_add_u32 m0, %[wl], 11264\n\ts_xor_b32 %[t], %[wo], 0xcc000\n\tbuffer_load_dwordx4 %[vw1], %[rw], %[t] offen lds\n\t" \
          "s_add_u32 m0, %[wl], 12288\n\ts_xor_b32 %[t], %[wo], 0xd0000\n\tbuffer_load_dwordx4 %[vw0], %[rw], %[t] offen lds\n\ts_add_u32 m0, %[wl], 13312\n\ts_xor_b32 %[t], %[wo], 0xd4000\n\tbuffer_load_dwordx4 %[vw1], %[rw], %[t] offen lds\n\t" \
          "s_add_u32 m0, %[wl], 14336\n\ts_xor_b32 %[t], %[wo], 0xd8000\n\tbuffer_load_dwordx4 %[vw0], %[rw], %[t] offen lds\n\ts_add_u32 m0, %[wl], 15360\n\ts_xor_b32 %[t], %[wo], 0xdc000\n\tbuffer_load_dwordx4 %[vw1], %[rw], %[t] offen lds\n\t" \
          "s_waitcnt vmcnt(32)\n\tds_read_b64_tr_b16 a[32:33], %[rt0] offset:16384\n\tds_read_b64_tr_b16 a[34:35], %[rt0] offset:16896\n\tds_read_b64_tr_b16 a[36:37], %[rt1] offset:16384\n\tds_read_b64_tr_b16 a[38:39], %[rt1] offset:16896\n\tds_read_b64_tr_b16 v[144:145], %[rt2] offset:16384\n\t" \
          "ds_read_b64_tr_b16 v[146:147], %[rt2] offset:16896\n\tds_read_b64_tr_b16 v[48:49], %[rt3] offset:16384\n\tds_read_b64_tr_b16 v[50:51], %[rt3] offset:16896\n\tds_read_b64_tr_b16 a[40:41], %[rt0] offset:20480\n\tds_read_b64_tr_b16 a[42:43], %[rt0] offset:20992\n\tds_read_b64_tr_b16 a[44:45], %[rt1] offset:20480\n\t" \
          "ds_read_b64_tr_b16 a[46:47], %[rt1] offset:20992\n\tds_read_b64_tr_b16 v[148:149], %[rt2] offset:20480\n\tds_read_b64_tr_b16 v[150:151], %[rt2] offset:20992\n\tds_read_b64_tr_b16 v[52:53], %[rt3] offset:20480\n\tds_read_b64_tr_b16 v[54:55], %[rt3] offset:20992\n\ts_waitcnt lgkmcnt(0)\n\t" \
          "s_add_u32 m0, %[wl], 16384\n\ts_xor_b32 %[t], %[wo], 0xe0000\n\tbuffer_load_dwordx4 %[vw0], %[rw], %[t] offen lds\n\ts_add_u32 m0, %[wl], 17408\n\ts_xor_b32 %[t], %[wo], 0xe4000\n\tbuffer_load_dwordx4 %[vw1], %[rw], %[t] offen lds\n\t" \
          "s_add_u32 m0, %[wl], 18432\n\ts_xor_b32 %[t], %[wo], 0xe8000\n\tbuffer_load_dwordx4 %[vw0], %[rw], %[t] offen lds\n\ts_add_u32 m0, %[wl], 19456\n\ts_xor_b32 %[t], %[wo], 0xec000\n\tbuffer_load_dwordx4 %[vw1], %[rw], %[t] offen lds\n\t" \
          "s_add_u32 m0, %[wl], 20480\n\ts_xor_b32 %[t], %[wo], 0xf0000\n\tbuffer_load_dwordx4 %[vw0], %[rw], %[t] offen lds\n\ts_add_u32 m0, %[wl], 21504\n\ts_xor_b32 %[t], %[wo], 0xf4000\n\tbuffer_load_dwordx4 %[vw1], %[rw], %[t] offen lds\n\t" \
          "s_add_u32 m0, %[wl], 22528\n\ts_xor_b32 %[t], %[wo], 0xf8000\n\tbuffer_load_dwordx4 %[vw0], %[rw], %[t] offen lds\n\ts_add_u32 m0, %[wl], 23552\n\ts_xor_b32 %[t], %[wo], 0xfc000\n\tbuffer_load_dwordx4 %[vw1], %[rw], %[t] offen lds\n\t" \
          "s_waitcnt vmcnt(32)\n\tds_read_b64_tr_b16 a[48:49], %[rt0] offset:24576\n\tds_read_b64_tr_b16 a[50:51], %[rt0] offset:25088\n\tds_read_b64_tr_b16 a[52:53], %[rt1] offset:24576\n\tds_read_b64_tr_b16 a[54:55], %[rt1] offset:25088\n\tds_read_b64_tr_b16 v[152:153], %[rt2] offset:24576\n\t" \
          "ds_read_b64_tr_b16 v[154:155], %[rt2] offset:25088\n\tds_read_b64_tr_b16 v[56:57], %[rt3] offset:24576\n\tds_read_b64_tr_b16 v[58:59], %[rt3] offset:25088\n\tds_read_b64_tr_b16 a[56:57], %[rt0] offset:28672\n\tds_read_b64_tr_b16 a[58:59], %[rt0] offset:29184\n\tds_read_b64_tr_b16 a[60:61], %[rt1] offset:28672\n\t" \
          "ds_read_b64_tr_b16 a[62:63], %[rt1] offset:29184\n\tds_read_b64_tr_b16 v[156:157], %[rt2] offset:28672\n\tds_read_b64_tr_b16 v[158:159], %[rt2] offset:29184\n\tds_read_b64_tr_b16 v[60:61], %[rt3] offset:28672\n\tds_read_b64_tr_b16 v[62:63], %[rt3] offset:29184\n\ts_waitcnt lgkmcnt(0)\n\t" \
          "s_add_u32 m0, %[wl], 24576\n\ts_xor_b32 %[t], %[wo], 0x100000\n\tbuffer_load_dwordx4 %[vw0], %[rw], %[t] offen lds\n\ts_add_u32 m0, %[wl], 25600\n\ts_xor_b32 %[t], %[wo], 0x104000\n\tbuffer_load_dwordx4 %[vw1], %[rw], %[t] offen lds\n\t" \
          "s_add_u32 m0, %[wl], 26624\n\ts_xor_b32 %[t], %[wo], 0x108000\n\tbuffer_load_dwordx4 %[vw0], %[rw], %[t] offen lds\n\ts_add_u32 m0, %[wl], 27648\n\ts_xor_b32 %[t], %[wo], 0x10c000\n\tbuffer_load_dwordx4 %[vw1], %[rw], %[t] offen lds\n\t" \
          "s_add_u32 m0, %[wl], 28672\n\ts_xor_b32 %[t], %[wo], 0x110000\n\tbuffer_load_dwordx4 %[vw0], %[rw], %[t] offen lds\n\ts_add_u32 m0, %[wl], 29696\n\ts_xor_b32 %[t], %[wo], 0x114000\n\tbuffer_load_dwordx4 %[vw1], %[rw], %[t] offen lds\n\t" \
          "s_add_u32 m0, %[wl], 30720\n\ts_xor_b32 %[t], %[wo], 0x118000\n\tbuffer_load_dwordx4 %[vw0], %[rw], %[t] offen lds\n\ts_add_u32 m0, %[wl], 31744\n\ts_xor_b32 %[t], %[wo], 0x11c000\n\tbuffer_load_dwordx4 %[vw1], %[rw], %[t] offen lds\n\t" \
          "s_waitcnt vmcnt(32)\n\tds_read_b64_tr_b16 a[64:65], %[rt0] offset:32768\n\tds_read_b64_tr_b16 a[66:67], %[rt0] offset:33280\n\tds_read_b64_tr_b16 a[68:69], %[rt1] offset:32768\n\tds_read_b64_tr_b16 a[70:71], %[rt1] offset:33280\n\tds_read_b64_tr_b16 v[160:161], %[rt2] offset:32768\n\t" \
          "ds_read_b64_tr_b16 v[162:163], %[rt2] offset:33280\n\tds_read_b64_tr_b16 v[64:65], %[rt3] offset:32768\n\tds_read_b64_tr_b16 v[66:67], %[rt3] offset:33280\n\tds_read_b64_tr_b16 a[72:73], %[rt0] offset:36864\n\tds_read_b64_tr_b16 a[74:75], %[rt0] offset:37376\n\tds_read_b64_tr_b16 a[76:77], %[rt1] offset:36864\n\t" \
          "ds_read_b64_tr_b16 a[78:79], %[rt1] offset:37376\n\tds_read_b64_tr_b16 v[164:165], %[rt2] offset:36864\n\tds_read_b64_tr_b16 v[166:167], %[rt2] offset:37376\n\tds_read_b64_tr_b16 v[68:69], %[rt3] offset:36864\n\tds_read_b64_tr_b16 v[70:71], %[rt3] offset:37376\n\ts_waitcnt lgkmcnt(0)\n\t" \
          "s_add_u32 m0, %[wl], 32768\n\ts_xor_b32 %[t], %[wo], 0x120000\n\tbuffer_load_dwordx4 %[vw0], %[rw], %[t] offen lds\n\ts_add_u32 m0, %[wl], 33792\n\ts_xor_b32 %[t], %[wo], 0x124000\n\tbuffer_load_dwordx4 %[vw1], %[rw], %[t] offen lds\n\t" \
          "s_add_u32 m0, %[wl], 34816\n\ts_xor_b32 %[t], %[wo], 0x128000\n\tbuffer_load_dwordx4 %[vw0], %[rw], %[t] offen lds\n\ts_add_u32 m0, %[wl], 35840\n\ts_xor_b32 %[t], %[wo], 0x12c000\n\tbuffer_load_dwordx4 %[vw1], %[rw], %[t] offen lds\n\t" \
          "s_add_u32 m0, %[wl], 36864\n\ts_xor_b32 %[t], %[wo], 0x130000\n\tbuffer_load_dwordx4 %[vw0], %[rw], %[t] offen lds\n\ts_add_u32 m0, %[wl], 37888\n\ts_xor_b32 %[t], %[wo], 0x134000\n\tbuffer_load_dwordx4 %[vw1], %[rw], %[t] offen lds\n\t" \
          "s_add_u32 m0, %[wl], 38912\n\ts_xor_b32 %[t], %[wo], 0x138000\n\tbuffer_load_dwordx4 %[vw0], %[rw], %[t] offen lds\n\ts_add_u32 m0, %[wl], 39936\n\ts_xor_b32 %[t], %[wo], 0x13c000\n\tbuffer_load_dwordx4 %[vw1], %[rw], %[t] offen lds\n\t" \
          "s_waitcnt vmcnt(32)\n\tds_read_b64_tr_b16 a[80:81], %[rt0] offset:0\n\tds_read_b64_tr_b16 a[82:83], %[rt0] offset:512\n\tds_read_b64_tr_b16 a[84:85], %[rt1] offset:0\n\tds_read_b64_tr_b16 a[86:87], %[rt1] offset:512\n\tds_read_b64_tr_b16 v[168:169], %[rt2] offset:0\n\t" \
          "ds_read_b64_tr_b16 v[170:171], %[rt2] offset:512\n\tds_read_b64_tr_b16 v[72:73], %[rt3] offset:0\n\tds_read_b64_tr_b16 v[74:75], %[rt3] offset:512\n\tds_read_b64_tr_b16 a[88:89], %[rt0] offset:4096\n\tds_read_b64_tr_b16 a[90:91], %[rt0] offset:4608\n\tds_read_b64_tr_b16 a[92:93], %[rt1] offset:4096\n\t" \
          "ds_read_b64_tr_b16 a[94:95], %[rt1] offset:4608\n\tds_read_b64_tr_b16 v[172:173], %[rt2] offset:4096\n\tds_read_b64_tr_b16 v[174:175], %[rt2] offset:4608\n\tds_read_b64_tr_b16 v[76:77], %[rt3] offset:4096\n\tds_read_b64_tr_b16 v[78:79], %[rt3] offset:4608\n\ts_waitcnt lgkmcnt(0)\n\t" \
          "s_add_u32 m0, %[wl], 0\n\ts_xor_b32 %[t], %[wo], 0x140000\n\tbuffer_load_dwordx4 %[vw0], %[rw], %[t] offen lds\n\ts_add_u32 m0, %[wl], 1024\n\ts_xor_b32 %[t], %[wo], 0x144000\n\tbuffer_load_dwordx4 %[vw1], %[rw], %[t] offen lds\n\t" \
          "s_add_u32 m0, %[wl], 2048\n\ts_xor_b32 %[t], %[wo], 0x148000\n\tbuffer_load_dwordx4 %[vw0], %[rw], %[t] offen lds\n\ts_add_u32 m0, %[wl], 3072\n\ts_xor_b32 %[t], %[wo], 0x14c000\n\tbuffer_load_dwordx4 %[vw1], %[rw], %[t] offen lds\n\t" \
          "s_add_u32 m0, %[wl], 4096\n\ts_xor_b32 %[t], %[wo], 0x150000\n\tbuffer_load_dwordx4 %[vw0], %[rw], %[t] offen lds\n\ts_add_u32 m0, %[wl], 5120\n\ts_xor_b32 %[t], %[wo], 0x154000\n\tbuffer_load_dwordx4 %[vw1], %[rw], %[t] offen lds\n\t" \
          "s_add_u32 m0, %[wl], 6144\n\ts_xor_b32 %[t], %[wo], 0x158000\n\tbuffer_load_dwordx4 %[vw0], %[rw], %[t] offen lds\n\ts_add_u32 m0, %[wl], 7168\n\ts_xor_b32 %[t], %[wo], 0x15c000\n\tbuffer_load_dwordx4 %[vw1], %[rw], %[t] offen lds\n\t" \
          "s_waitcnt vmcnt(32)\n\tds_read_b64_tr_b16 a[96:97], %[rt0] offset:8192\n\tds_read_b64_tr_b16 a[98:99], %[rt0] offset:8704\n\tds_read_b64_tr_b16 a[100:101], %[rt1] offset:8192\n\tds_read_b64_tr_b16 a[102:103], %[rt1] offset:8704\n\tds_read_b64_tr_b16 v[176:177], %[rt2] offset:8192\n\t" \
          "ds_read_b64_tr_b16 v[178:179], %[rt2] offset:8704\n\tds_read_b64_tr_b16 v[80:81], %[rt3] offset:8192\n\tds_read_b64_tr_b16 v[82:83], %[rt3] offset:8704\n\tds_read_b64_tr_b16 a[104:105], %[rt0] offset:12288\n\tds_read_b64_tr_b16 a[106:107], %[rt0] offset:12800\n\tds_read_b64_tr_b16 a[108:109], %[rt1] offset:12288\n\t" \
          "ds_read_b64_tr_b16 a[110:111], %[rt1] offset:12800\n\tds_read_b64_tr_b16 v[180:181], %[rt2] offset:12288\n\tds_read_b64_tr_b16 v[182:183], %[rt2] offset:12800\n\tds_read_b64_tr_b16 v[84:85], %[rt3] offset:12288\n\tds_read_b64_tr_b16 v[86:87], %[rt3] offset:12800\n\ts_waitcnt lgkmcnt(0)\n\t" \
          "s_add_u32 m0, %[wl], 8192\n\ts_xor_b32 %[t], %[wo], 0x160000\n\tbuffer_load_dwordx4 %[vw0], %[rw], %[t] offen lds\n\ts_add_u32 m0, %[wl], 9216\n\ts_xor_b32 %[t], %[wo], 0x164000\n\tbuffer_load_dwordx4 %[vw1], %[rw], %[t] offen lds\n\t" \
          "s_add_u32 m0, %[wl], 10240\n\ts_xor_b32 %[t], %[wo], 0x168000\n\tbuffer_load_dwordx4 %[vw0], %[rw], %[t] offen lds\n\ts_add_u32 m0, %[wl], 11264\n\ts_xor_b32 %[t], %[wo], 0x16c000\n\tbuffer_load_dwordx4 %[vw1], %[rw], %[t] offen lds\n\t" \
          "s_add_u32 m0, %[wl], 12288\n\ts_xor_b32 %[t], %[wo], 0x170000\n\tbuffer_load_dwordx4 %[vw0], %[rw], %[t] offen lds\n\ts_add_u32 m0, %[wl], 13312\n\ts_xor_b32 %[t], %[wo], 0x174000\n\tbuffer_load_dwordx4 %[vw1], %[rw], %[t] offen lds\n\t" \
          "s_add_u32 m0, %[wl], 14336\n\ts_xor_b32 %[t], %[wo], 0x178000\n\tbuffer_load_dwordx4 %[vw0], %[rw], %[t] offen lds\n\ts_add_u32 m0, %[wl], 15360\n\ts_xor_b32 %[t], %[wo], 0x17c000\n\tbuffer_load_dwordx4 %[vw1], %[rw], %[t] offen lds\n\t" \
          "s_waitcnt vmcnt(32)\n\tds_read_b64_tr_b16 a[112:113], %[rt0] offset:16384\n\tds_read_b64_tr_b16 a[114:115], %[rt0] offset:16896\n\tds_read_b64_tr_b16 a[116:117], %[rt1] offset:16384\n\tds_read_b64_tr_b16 a[118:119], %[rt1] offset:16896\n\tds_read_b64_tr_b16 v[184:185], %[rt2] offset:16384\n\t" \
          "ds_read_b64_tr_b16 v[186:187], %[rt2] offset:16896\n\tds_read_b64_tr_b16 v[88:89], %[rt3] offset:16384\n\tds_read_b64_tr_b16 v[90:91], %[rt3] offset:16896\n\tds_read_b64_tr_b16 a[120:121], %[rt0] offset:20480\n\tds_read_b64_tr_b16 a[122:123], %[rt0] offset:20992\n\tds_read_b64_tr_b16 a[124:125], %[rt1] offset:20480\n\t" \
          "ds_read_b64_tr_b16 a[126:127], %[rt1] offset:20992\n\tds_read_b64_tr_b16 v[188:189], %[rt2] offset:20480\n\tds_read_b64_tr_b16 v[190:191], %[rt2] offset:20992\n\tds_read_b64_tr_b16 v[92:93], %[rt3] offset:20480\n\tds_read_b64_tr_b16 v[94:95], %[rt3] offset:20992\n\ts_waitcnt lgkmcnt(0)\n\t" \
          "s_add_u32 m0, %[wl], 16384\n\ts_xor_b32 %[t], %[wo], 0x180000\n\tbuffer_load_dwordx4 %[vw0], %[rw], %[t] offen lds\n\ts_add_u32 m0, %[wl], 17408\n\ts_xor_b32 %[t], %[wo], 0x184000\n\tbuffer_load_dwordx4 %[vw1], %[rw], %[t] offen lds\n\t" \
          "s_add_u32 m0, %[wl], 18432\n\ts_xor_b32 %[t], %[wo], 0x188000\n\tbuffer_load_dwordx4 %[vw0], %[rw], %[t] offen lds\n\ts_add_u32 m0, %[wl], 19456\n\ts_xor_b32 %[t], %[wo], 0x18c000\n\tbuffer_load_dwordx4 %[vw1], %[rw], %[t] offen lds\n\t" \
          "s_add_u32 m0, %[wl], 20480\n\ts_xor_b32 %[t], %[wo], 0x190000\n\tbuffer_load_dwordx4 %[vw0], %[rw], %[t] offen lds\n\ts_add_u32 m0, %[wl], 21504\n\ts_xor_b32 %[t], %[wo], 0x194000\n\tbuffer_load_dwordx4 %[vw1], %[rw], %[t] offen lds\n\t" \
          "s_add_u32 m0, %[wl], 22528\n\ts_xor_b32 %[t], %[wo], 0x198000\n\tbuffer_load_dwordx4 %[vw0], %[rw], %[t] offen lds\n\ts_add_u32 m0, %[wl], 23552\n\ts_xor_b32 %[t], %[wo], 0x19c000\n\tbuffer_load_dwordx4 %[vw1], %[rw], %[t] offen lds\n\t" \
          "s_waitcnt vmcnt(32)\n\tds_read_b64_tr_b16 a[128:129], %[rt0] offset:24576\n\tds_read_b64_tr_b16 a[130:131], %[rt0] offset:25088\n\tds_read_b64_tr_b16 a[132:133], %[rt1] offset:24576\n\tds_read_b64_tr_b16 a[134:135], %[rt1] offset:25088\n\tds_read_b64_tr_b16 v[192:193], %[rt2] offset:24576\n\t" \
          "ds_read_b64_tr_b16 v[194:195], %[rt2] offset:25088\n\tds_read_b64_tr_b16 v[96:97], %[rt3] offset:24576\n\tds_read_b64_tr_b16 v[98:99], %[rt3] offset:25088\n\tds_read_b64_tr_b16 a[136:137], %[rt0] offset:28672\n\tds_read_b64_tr_b16 a[138:139], %[rt0] offset:29184\n\tds_read_b64_tr_b16 a[140:141], %[rt1] offset:28672\n\t" \
          "ds_read_b64_tr_b16 a[142:143], %[rt1] offset:29184\n\tds_read_b64_tr_b16 v[196:197], %[rt2] offset:28672\n\tds_read_b64_tr_b16 v[198:199], %[rt2] offset:29184\n\tds_read_b64_tr_b16 v[100:101], %[rt3] offset:28672\n\tds_read_b64_tr_b16 v[102:103], %[rt3] offset:29184\n\ts_waitcnt lgkmcnt(0)\n\t" \
          "s_add_u32 m0, %[wl], 24576\n\ts_xor_b32 %[t], %[wo], 0x1a0000\n\tbuffer_load_dwordx4 %[vw0], %[rw], %[t] offen lds\n\ts_add_u32 m0, %[wl], 25600\n\ts_xor_b32 %[t], %[wo], 0x1a4000\n\tbuffer_load_dwordx4 %[vw1], %[rw], %[t] offen lds\n\t" \
          "s_add_u32 m0, %[wl], 26624\n\ts_xor_b32 %[t], %[wo], 0x1a8000\n\tbuffer_load_dwordx4 %[vw0], %[rw], %[t] offen lds\n\ts_add_u32 m0, %[wl], 27648\n\ts_xor_b32 %[t], %[wo], 0x1ac000\n\tbuffer_load_dwordx4 %[vw1], %[rw], %[t] offen lds\n\t" \
          "s_add_u32 m0, %[wl], 28672\n\ts_xor_b32 %[t], %[wo], 0x1b0000\n\tbuffer_load_dwordx4 %[vw0], %[rw], %[t] offen lds\n\ts_add_u32 m0, %[wl], 29696\n\ts_xor_b32 %[t], %[wo], 0x1b4000\n\tbuffer_load_dwordx4 %[vw1], %[rw], %[t] offen lds\n\t" \
          "s_add_u32 m0, %[wl], 30720\n\ts_xor_b32 %[t], %[wo], 0x1b8000\n\tbuffer_load_dwordx4 %[vw0], %[rw], %[t] offen lds\n\ts_add_u32 m0, %[wl], 31744\n\ts_xor_b32 %[t], %[wo], 0x1bc000\n\tbuffer_load_dwordx4 %[vw1], %[rw], %[t] offen lds\n\t" \
          "s_waitcnt vmcnt(32)\n\tds_read_b64_tr_b16 a[144:145], %[rt0] offset:32768\n\tds_read_b64_tr_b16 a[146:147], %[rt0] offset:33280\n\tds_read_b64_tr_b16 a[148:149], %[rt1] offset:32768\n\tds_read_b64_tr_b16 a[150:151], %[rt1] offset:33280\n\tds_read_b64_tr_b16 v[200:201], %[rt2] offset:32768\n\t" \
          "ds_read_b64_tr_b16 v[202:203], %[rt2] offset:33280\n\tds_read_b64_tr_b16 v[104:105], %[rt3] offset:32768\n\tds_read_b64_tr_b16 v[106:107], %[rt3] offset:33280\n\tds_read_b64_tr_b16 a[152:153], %[rt0] offset:36864\n\tds_read_b64_tr_b16 a[154:155], %[rt0] offset:37376\n\tds_read_b64_tr_b16 a[156:157], %[rt1] offset:36864\n\t" \
          "ds_read_b64_tr_b16 a[158:159], %[rt1] offset:37376\n\tds_read_b64_tr_b16 v[204:205], %[rt2] offset:36864\n\tds_read_b64_tr_b16 v[206:207], %[rt2] offset:37376\n\tds_read_b64_tr_b16 v[108:109], %[rt3] offset:36864\n\tds_read_b64_tr_b16 v[110:111], %[rt3] offset:37376\n\ts_waitcnt lgkmcnt(0)\n\t" \
          "s_add_u32 m0, %[wl], 32768\n\ts_xor_b32 %[t], %[wo], 0x1c0000\n\tbuffer_load_dwordx4 %[vw0], %[rw], %[t] offen lds\n\ts_add_u32 m0, %[wl], 33792\n\ts_xor_b32 %[t], %[wo], 0x1c4000\n\tbuffer_load_dwordx4 %[vw1], %[rw], %[t] offen lds\n\t" \
          "s_add_u32 m0, %[wl], 34816\n\ts_xor_b32 %[t], %[wo], 0x1c8000\n\tbuffer_load_dwordx4 %[vw0], %[rw], %[t] offen lds\n\ts_add_u32 m0, %[wl], 35840\n\ts_xor_b32 %[t], %[wo], 0x1cc000\n\tbuffer_load_dwordx4 %[vw1], %[rw], %[t] offen lds\n\t" \
          "s_add_u32 m0, %[wl], 36864\n\ts_xor_b32 %[t], %[wo], 0x1d0000\n\tbuffer_load_dwordx4 %[vw0], %[rw], %[t] offen lds\n\ts_add_u32 m0, %[wl], 37888\n\ts_xor_b32 %[t], %[wo], 0x1d4000\n\tbuffer_load_dwordx4 %[vw1], %[rw], %[t] offen lds\n\t" \
          "s_add_u32 m0, %[wl], 38912\n\ts_xor_b32 %[t], %[wo], 0x1d8000\n\tbuffer_load_dwordx4 %[vw0], %[rw], %[t] offen lds\n\ts_add_u32 m0, %[wl], 39936\n\ts_xor_b32 %[t], %[wo], 0x1dc000\n\tbuffer_load_dwordx4 %[vw1], %[rw], %[t] offen lds\n\t" \
          "s_waitcnt vmcnt(32)\n\tds_read_b64_tr_b16 a[160:161], %[rt0] offset:0\n\tds_read_b64_tr_b16 a[162:163], %[rt0] offset:512\n\tds_read_b64_tr_b16 a[164:165], %[rt1] offset:0\n\tds_read_b64_tr_b16 a[166:167], %[rt1] offset:512\n\tds_read_b64_tr_b16 v[208:209], %[rt2] offset:0\n\t" \
          "ds_read_b64_tr_b16 v[210:211], %[rt2] offset:512\n\tds_read_b64_tr_b16 v[112:113], %[rt3] offset:0\n\tds_read_b64_tr_b16 v[114:115], %[rt3] offset:512\n\tds_read_b64_tr_b16 a[168:169], %[rt0] offset:4096\n\tds_read_b64_tr_b16 a[170:171], %[rt0] offset:4608\n\tds_read_b64_tr_b16 a[172:173], %[rt1] offset:4096\n\t" \
          "ds_read_b64_tr_b16 a[174:175], %[rt1] offset:4608\n\tds_read_b64_tr_b16 v[212:213], %[rt2] offset:4096\n\tds_read_b64_tr_b16 v[214:215], %[rt2] offset:4608\n\tds_read_b64_tr_b16 v[116:117], %[rt3] offset:4096\n\tds_read_b64_tr_b16 v[118:119], %[rt3] offset:4608\n\ts_waitcnt lgkmcnt(0)\n\t" \
          "s_add_u32 m0, %[wl], 0\n\ts_xor_b32 %[t], %[wo], 0x1e0000\n\tbuffer_load_dwordx4 %[vw0], %[rw], %[t] offen lds\n\ts_add_u32 m0, %[wl], 1024\n\ts_xor_b32 %[t], %[wo], 0x1e4000\n\tbuffer_load_dwordx4 %[vw1], %[rw], %[t] offen lds\n\t" \
          "s_add_u32 m0, %[wl], 2048\n\ts_xor_b32 %[t], %[wo], 0x1e8000\n\tbuffer_load_dwordx4 %[vw0], %[rw], %[t] offen lds\n\ts_add_u32 m0, %[wl], 3072\n\ts_xor_b32 %[t], %[wo], 0x1ec000\n\tbuffer_load_dwordx4 %[vw1], %[rw], %[t] offen lds\n\t" \
          "s_add_u32 m0, %[wl], 4096\n\ts_xor_b32 %[t], %[wo], 0x1f0000\n\tbuffer_load_dwordx4 %[vw0], %[rw], %[t] offen lds\n\ts_add_u32 m0, %[wl], 5120\n\ts_xor_b32 %[t], %[wo], 0x1f4000\n\tbuffer_load_dwordx4 %[vw1], %[rw], %[t] offen lds\n\t" \
          "s_add_u32 m0, %[wl], 6144\n\ts_xor_b32 %[t], %[wo], 0x1f8000\n\tbuffer_load_dwordx4 %[vw0], %[rw], %[t] offen lds\n\ts_add_u32 m0, %[wl], 7168\n\ts_xor_b32 %[t], %[wo], 0x1fc000\n\tbuffer_load_dwordx4 %[vw1], %[rw], %[t] offen lds\n\t" \
          "s_waitcnt vmcnt(32)\n\tds_read_b64_tr_b16 a[176:177], %[rt0] offset:8192\n\tds_read_b64_tr_b16 a[178:179], %[rt0] offset:8704\n\tds_read_b64_tr_b16 a[180:181], %[rt1] offset:8192\n\tds_read_b64_tr_b16 a[182:183], %[rt1] offset:8704\n\tds_read_b64_tr_b16 v[216:217], %[rt2] offset:8192\n\t" \
          "ds_read_b64_tr_b16 v[218:219], %[rt2] offset:8704\n\tds_read_b64_tr_b16 v[120:121], %[rt3] offset:8192\n\tds_read_b64_tr_b16 v[122:123], %[rt3] offset:8704\n\tds_read_b64_tr_b16 a[184:185], %[rt0] offset:12288\n\tds_read_b64_tr_b16 a[186:187], %[rt0] offset:12800\n\tds_read_b64_tr_b16 a[188:189], %[rt1] offset:12288\n\t" \
          "ds_read_b64_tr_b16 a[190:191], %[rt1] offset:12800\n\tds_read_b64_tr_b16 v[220:221], %[rt2] offset:12288\n\tds_read_b64_tr_b16 v[222:223], %[rt2] offset:12800\n\tds_read_b64_tr_b16 v[124:125], %[rt3] offset:12288\n\tds_read_b64_tr_b16 v[126:127], %[rt3] offset:12800\n\ts_waitcnt lgkmcnt(0)\n\t" \
          "ds_write_b128 %[own], v[32:35] offset:8192\n\tds_write_b128 %[own], v[36:39] offset:9216\n\tds_write_b128 %[own], v[40:43] offset:10240\n\tds_write_b128 %[own], v[44:47] offset:11264\n\tds_write_b128 %[own], v[48:51] offset:12288\n\tds_write_b128 %[own], v[52:55] offset:13312\n\t" \
          "ds_write_b128 %[own], v[56:59] offset:14336\n\tds_write_b128 %[own], v[60:63] offset:15360\n\ts_waitcnt lgkmcnt(0)\n\ts_waitcnt vmcnt(24)\n\tds_read_b64_tr_b16 a[192:193], %[rt0] offset:16384\n\tds_read_b64_tr_b16 a[194:195], %[rt0] offset:16896\n\t" \
          "ds_read_b64_tr_b16 a[196:197], %[rt1] offset:16384\n\tds_read_b64_tr_b16 a[198:199], %[rt1] offset:16896\n\tds_read_b64_tr_b16 v[224:225], %[rt2] offset:16384\n\tds_read_b64_tr_b16 v[226:227], %[rt2] offset:16896\n\tds_read_b64_tr_b16 v[32:33], %[rt3] offset:16384\n\tds_read_b64_tr_b16 v[34:35], %[rt3] offset:16896\n\t" \
          "ds_read_b64_tr_b16 a[200:201], %[rt0] offset:20480\n\tds_read_b64_tr_b16 a[202:203], %[rt0] offset:20992\n\tds_read_b64_tr_b16 a[204:205], %[rt1] offset:20480\n\tds_read_b64_tr_b16 a[206:207], %[rt1] offset:20992\n\tds_read_b64_tr_b16 v[228:229], %[rt2] offset:20480\n\tds_read_b64_tr_b16 v[230:231], %[rt2] offset:20992\n\t" \
          "ds_read_b64_tr_b16 v[36:37], %[rt3] offset:20480\n\tds_read_b64_tr_b16 v[38:39], %[rt3] offset:20992\n\ts_waitcnt lgkmcnt(0)\n\tds_write_b128 %[own], v[64:67] offset:16384\n\tds_write_b128 %[own], v[68:71] offset:17408\n\tds_write_b128 %[own], v[72:75] offset:18432\n\t" \
          "ds_write_b128 %[own], v[76:79] offset:19456\n\tds_write_b128 %[own], v[80:83] offset:20480\n\tds_write_b128 %[own], v[84:87] offset:21504\n\tds_write_b128 %[own], v[88:91] offset:22528\n\tds_write_b128 %[own], v[92:95] offset:23552\n\ts_waitcnt lgkmcnt(0)\n\t" \
          "s_waitcnt vmcnt(16)\n\tds_read_b64_tr_b16 a[208:209], %[rt0] offset:24576\n\tds_read_b64_tr_b16 a[210:211], %[rt0] offset:25088\n\tds_read_b64_tr_b16 a[212:213], %[rt1] offset:24576\n\tds_read_b64_tr_b16 a[214:215], %[rt1] offset:25088\n\tds_read_b64_tr_b16 v[232:233], %[rt2] offset:24576\n\t" \
          "ds_read_b64_tr_b16 v[234:235], %[rt2] offset:25088\n\tds_read_b64_tr_b16 v[40:41], %[rt3] offset:24576\n\tds_read_b64_tr_b16 v[42:43], %[rt3] offset:25088\n\tds_read_b64_tr_b16 a[216:217], %[rt0] offset:28672\n\tds_read_b64_tr_b16 a[218:219], %[rt0] offset:29184\n\tds_read_b64_tr_b16 a[220:221], %[rt1] offset:28672\n\t" \
          "ds_read_b64_tr_b16 a[222:223], %[rt1] offset:29184\n\tds_read_b64_tr_b16 v[236:237], %[rt2] offset:28672\n\tds_read_b64_tr_b16 v[238:239], %[rt2] offset:29184\n\tds_read_b64_tr_b16 v[44:45], %[rt3] offset:28672\n\tds_read_b64_tr_b16 v[46:47], %[rt3] offset:29184\n\ts_waitcnt lgkmcnt(0)\n\t" \
          "ds_write_b128 %[own], v[96:99] offset:24576\n\tds_write_b128 %[own], v[100:103] offset:25600\n\tds_write_b128 %[own], v[104:107] offset:26624\n\tds_write_b128 %[own], v[108:111] offset:27648\n\tds_write_b128 %[own], v[112:115] offset:28672\n\tds_write_b128 %[own], v[116:119] offset:29696\n\t" \
          "ds_write_b128 %[own], v[120:123] offset:30720\n\tds_write_b128 %[own], v[124:127] offset:31744\n\ts_waitcnt lgkmcnt(0)\n\ts_waitcnt vmcnt(8)\n\tds_read_b64_tr_b16 a[224:225], %[rt0] offset:32768\n\tds_read_b64_tr_b16 a[226:227], %[rt0] offset:33280\n\t" \
          "ds_read_b64_tr_b16 a[228:229], %[rt1] offset:32768\n\tds_read_b64_tr_b16 a[230:231], %[rt1] offset:33280\n\tds_read_b64_tr_b16 v[240:241], %[rt2] offset:32768\n\tds_read_b64_tr_b16 v[242:243], %[rt2] offset:33280\n\tds_read_b64_tr_b16 v[48:49], %[rt3] offset:32768\n\tds_read_b64_tr_b16 v[50:51], %[rt3] offset:33280\n\t" \
          "ds_read_b64_tr_b16 a[232:233], %[rt0] offset:36864\n\tds_read_b64_tr_b16 a[234:235], %[rt0] offset:37376\n\tds_read_b64_tr_b16 a[236:237], %[rt1] offset:36864\n\tds_read_b64_tr_b16 a[238:239], %[rt1] offset:37376\n\tds_read_b64_tr_b16 v[244:245], %[rt2] offset:36864\n\tds_read_b64_tr_b16 v[246:247], %[rt2] offset:37376\n\t" \
          "ds_read_b64_tr_b16 v[52:53], %[rt3] offset:36864\n\tds_read_b64_tr_b16 v[54:55], %[rt3] offset:37376\n\ts_waitcnt lgkmcnt(0)\n\ts_waitcnt vmcnt(0)\n\tds_read_b64_tr_b16 a[240:241], %[rt0] offset:0\n\tds_read_b64_tr_b16 a[242:243], %[rt0] offset:512\n\t" \
          "ds_read_b64_tr_b16 a[244:245], %[rt1] offset:0\n\tds_read_b64_tr_b16 a[246:247], %[rt1] offset:512\n\tds_read_b64_tr_b16 v[248:249], %[rt2] offset:0\n\tds_read_b64_tr_b16 v[250:251], %[rt2] offset:512\n\tds_read_b64_tr_b16 v[56:57], %[rt3] offset:0\n\tds_read_b64_tr_b16 v[58:59], %[rt3] offset:512\n\t" \
          "ds_read_b64_tr_b16 a[248:249], %[rt0] offset:4096\n\tds_read_b64_tr_b16 a[250:251], %[rt0] offset:4608\n\tds_read_b64_tr_b16 a[252:253], %[rt1] offset:4096\n\tds_read_b64_tr_b16 a[254:255], %[rt1] offset:4608\n\tds_read_b64_tr_b16 v[252:253], %[rt2] offset:4096\n\tds_read_b64_tr_b16 v[254:255], %[rt2] offset:4608\n\t" \
          "ds_read_b64_tr_b16 v[60:61], %[rt3] offset:4096\n\tds_read_b64_tr_b16 v[62:63], %[rt3] offset:4608\n\ts_waitcnt lgkmcnt(0)\n\tds_write_b128 %[own], v[32:35] offset:0\n\tds_write_b128 %[own], v[36:39] offset:1024\n\tds_write_b128 %[own], v[40:43] offset:2048\n\t" \
          "ds_write_b128 %[own], v[44:47] offset:3072\n\tds_write_b128 %[own], v[48:51] offset:4096\n\tds_write_b128 %[own], v[52:55] offset:5120\n\tds_write_b128 %[own], v[56:59] offset:6144\n\tds_write_b128 %[own], v[60:63] offset:7168\n\ts_waitcnt lgkmcnt(0)\n\t" \
          "Lou_wait%=:\n\ts_mov_b32 %[cnt], 0\n\tLou_poll%=:\n\ts_lshr_b32 %[t], %[j], 1\n\ts_cmp_lt_u32 %[t], %[av]\n\ts_cbranch_scc1 Lou_load%=\n\t" \
          "s_sleep 2\n\ts_lshl_b32 %[t], %[av], 10\n\ts_add_u32 %[t], %[t], %[fo]\n\tbuffer_load_dwordx2 v[30:31], %[vf], %[rf], %[t] offen sc1\n\tbuffer_load_dword v48, off, %[rf], %[tmo] sc1\n\ts_waitcnt vmcnt(0)\n\t" \
          "s_lshr_b32 %[stp], %[j], 1\n\ts_cmp_eq_u32 %[stp], %[av]\n\ts_cselect_b32 %[stp], %[sp], 1\n\tv_cmp_eq_u32 vcc, 0, v30\n\ts_nop 0\n\ts_ff1_i32_b64 %[t], vcc\n\t" \
          "s_lshr_b32 %[t], %[t], 1\n\ts_add_u32 %[av], %[av], %[t]\n\ts_or_b32 %[stp], %[stp], %[t]\n\ts_cbranch_scc1 Lou_ns%=\n\tv_readlane_b32 %[t], v31, 0\n\tv_readlane_b32 %[stp], v31, 1\n\t" \
          "s_and_b32 vcc_lo, %[t], %[stp]\n\ts_cbranch_scc0 Lou_ns%=\n\ts_sub_i32 vcc_lo, %[t], %[stp]\n\ts_cmp_gt_i32 vcc_lo, 0\n\ts_cselect_b32 %[t], %[t], %[stp]\n\ts_mov_b32 %[sp], 1\n\t" \
          "Lou_tw%=:\n\ts_memrealtime vcc\n\ts_waitcnt lgkmcnt(0)\n\ts_sub_i32 vcc_lo, %[t], vcc_lo\n\ts_sub_u32 vcc_lo, vcc_lo, 1\n\ts_cmp_lt_u32 vcc_lo, 2000\n\t" \
          "s_cbranch_scc1 Lou_tw%=\n\ts_branch Lou_load%=\n\tLou_ns%=:\n\tv_readfirstlane_b32 vcc_lo, v48\n\ts_add_u32 %[cnt], %[cnt], 1\n\ts_lshr_b32 vcc_hi, %[cnt], 15\n\t" \
          "s_or_b32 vcc_lo, vcc_lo, vcc_hi\n\ts_cbranch_scc0 Lou_poll%=\n\tv_mov_b32 v48, 7\n\tbuffer_store_dword v48, off, %[rf], %[tmo] sc1\n\ts_mov_b32 %[av], 64\n\tLou_load%=:\n\t" \
          "" OU_ST0 "s_nop 0\n\tbuffer_load_dwordx4 v[64:67], %[vo0], s[40:43], %[pg0] offen sc1\n\tbuffer_load_dwordx4 v[68:71], %[vo0], s[40:43], %[pg0] offen offset:1024 sc1\n\tbuffer_load_dwordx4 v[72:75], %[vo0], s[40:43], %[pg0] offen offset:2048 sc1\n\tbuffer_load_dwordx4 v[76:79], %[vo0], s[40:43], %[pg0] offen offset:3072 sc1\n\tbuffer_load_dwordx4 v[80:83], %[vo1], s[40:43], %[pg0] offen sc1\n\t" \
          "buffer_load_dwordx4 v[84:87], %[vo1], s[40:43], %[pg0] offen offset:1024 sc1\n\tbuffer_load_dwordx4 v[88:91], %[vo1], s[40:43], %[pg0] offen offset:2048 sc1\n\tbuffer_load_dwordx4 v[92:95], %[vo1], s[40:43], %[pg0] offen offset:3072 sc1\n\tbuffer_load_dwordx4 v[96:99], %[vo0], s[40:43], %[pg1] offen sc1\n\tbuffer_load_dwordx4 v[100:103], %[vo0], s[40:43], %[pg1] offen offset:1024 sc1\n\tbuffer_load_dwordx4 v[104:107], %[vo0], s[40:43], %[pg1] offen offset:2048 sc1\n\t" \
          "buffer_load_dwordx4 v[108:111], %[vo0], s[40:43], %[pg1] offen offset:3072 sc1\n\tbuffer_load_dwordx4 v[112:115], %[vo1], s[40:43], %[pg1] offen sc1\n\tbuffer_load_dwordx4 v[116:119], %[vo1], s[40:43], %[pg1] offen offset:1024 sc1\n\tbuffer_load_dwordx4 v[120:123], %[vo1], s[40:43], %[pg1] offen offset:2048 sc1\n\tbuffer_load_dwordx4 v[124:127], %[vo1], s[40:43], %[pg1] offen offset:3072 sc1\n\tLou_top%=:\n\t" \
          "s_lshl_b32 %[t], %[av], 10\n\ts_add_u32 %[t], %[t], %[fo]\n\tbuffer_load_dwordx2 v[30:31], %[vf], %[rf], %[t] offen sc1\n\tds_read_b128 v[32:35], %[own] offset:8192\n\tds_read_b128 v[36:39], %[own] offset:9216\n\tds_read_b128 v[40:43], %[own] offset:10240\n\t" \
          "s_waitcnt vmcnt(16) lgkmcnt(2)\n\tv_mfma_f32_16x16x32_bf16 v[48:51], a[0:3], v[64:67], 0\n\tds_read_b128 v[44:47], %[own] offset:11264\n\tv_mfma_f32_16x16x32_bf16 v[52:55], a[4:7], v[64:67], 0\n\tv_mfma_f32_16x16x32_bf16 v[56:59], v[128:131], v[64:67], 0\n\tv_mfma_f32_16x16x32_bf16 v[60:63], v[32:35], v[64:67], 0\n\t" \
          "buffer_load_dwordx4 v[64:67], %[vo0], s[40:43], %[pg2] offen sc1\n\ts_waitcnt vmcnt(16) lgkmcnt(2)\n\tv_mfma_f32_16x16x32_bf16 v[48:51], a[8:11], v[68:71], v[48:51]\n\tds_read_b128 v[32:35], %[own] offset:12288\n\tv_mfma_f32_16x16x32_bf16 v[52:55], a[12:15], v[68:71], v[52:55]\n\tv_mfma_f32_16x16x32_bf16 v[56:59], v[132:135], v[68:71], v[56:59]\n\t" \
          "v_mfma_f32_16x16x32_bf16 v[60:63], v[36:39], v[68:71], v[60:63]\n\tbuffer_load_dwordx4 v[68:71], %[vo0], s[40:43], %[pg2] offen offset:1024 sc1\n\ts_waitcnt vmcnt(16) lgkmcnt(2)\n\tv_mfma_f32_16x16x32_bf16 v[48:51], a[16:19], v[72:75], v[48:51]\n\tds_read_b128 v[36:39], %[own] offset:13312\n\tv_mfma_f32_16x16x32_bf16 v[52:55], a[20:23], v[72:75], v[52:55]\n\t" \
          "v_mfma_f32_16x16x32_bf16 v[56:59], v[136:139], v[72:75], v[56:59]\n\tv_mfma_f32_16x16x32_bf16 v[60:63], v[40:43], v[72:75], v[60:63]\n\tbuffer_load_dwordx4 v[72:75], %[vo0], s[40:43], %[pg2] offen offset:2048 sc1\n\ts_waitcnt vmcnt(16) lgkmcnt(2)\n\tv_mfma_f32_16x16x32_bf16 v[48:51], a[24:27], v[76:79], v[48:51]\n\tds_read_b128 v[40:43], %[own] offset:14336\n\t" \
          "v_mfma_f32_16x16x32_bf16 v[52:55], a[28:31], v[76:79], v[52:55]\n\tv_mfma_f32_16x16x32_bf16 v[56:59], v[140:143], v[76:79], v[56:59]\n\tv_mfma_f32_16x16x32_bf16 v[60:63], v[44:47], v[76:79], v[60:63]\n\tbuffer_load_dwordx4 v[76:79], %[vo0], s[40:43], %[pg2] offen offset:3072 sc1\n\ts_waitcnt vmcnt(16) lgkmcnt(2)\n\tv_mfma_f32_16x16x32_bf16 v[48:51], a[32:35], v[80:83], v[48:51]\n\t" \
          "ds_read_b128 v[44:47], %[own] offset:15360\n\tv_mfma_f32_16x16x32_bf16 v[52:55], a[36:39], v[80:83], v[52:55]\n\tv_mfma_f32_16x16x32_bf16 v[56:59], v[144:147], v[80:83], v[56:59]\n\tv_mfma_f32_16x16x32_bf16 v[60:63], v[32:35], v[80:83], v[60:63]\n\tbuffer_load_dwordx4 v[80:83], %[vo1], s[40:43], %[pg2] offen sc1\n\ts_waitcnt vmcnt(16) lgkmcnt(2)\n\t" \
          "v_mfma_f32_16x16x32_bf16 v[48:51], a[40:43], v[84:87], v[48:51]\n\tds_read_b128 v[32:35], %[own] offset:16384\n\tv_mfma_f32_16x16x32_bf16 v[52:55], a[44:47], v[84:87], v[52:55]\n\tv_mfma_f32_16x16x32_bf16 v[56:59], v[148:151], v[84:87], v[56:59]\n\tv_mfma_f32_16x16x32_bf16 v[60:63], v[36:39], v[84:87], v[60:63]\n\tbuffer_load_dwordx4 v[84:87], %[vo1], s[40:43], %[pg2] offen offset:1024 sc1\n\t" \
          "s_waitcnt vmcnt(16) lgkmcnt(2)\n\tv_mfma_f32_16x16x32_bf16 v[48:51], a[48:51], v[88:91], v[48:51]\n\tds_read_b128 v[36:39], %[own] offset:17408\n\tv_mfma_f32_16x16x32_bf16 v[52:55], a[52:55], v[88:91], v[52:55]\n\tv_mfma_f32_16x16x32_bf16 v[56:59], v[152:155], v[88:91], v[56:59]\n\tv_mfma_f32_16x16x32_bf16 v[60:63], v[40:43], v[88:91], v[60:63]\n\t" \
          "buffer_load_dwordx4 v[88:91], %[vo1], s[40:43], %[pg2] offen offset:2048 sc1\n\ts_waitcnt vmcnt(16) lgkmcnt(2)\n\tv_mfma_f32_16x16x32_bf16 v[48:51], a[56:59], v[92:95], v[48:51]\n\tds_read_b128 v[40:43], %[own] offset:18432\n\tv_mfma_f32_16x16x32_bf16 v[52:55], a[60:63], v[92:95], v[52:55]\n\tv_mfma_f32_16x16x32_bf16 v[56:59], v[156:159], v[92:95], v[56:59]\n\t" \
          "v_mfma_f32_16x16x32_bf16 v[60:63], v[44:47], v[92:95], v[60:63]\n\tbuffer_load_dwordx4 v[92:95], %[vo1], s[40:43], %[pg2] offen offset:3072 sc1\n\ts_waitcnt vmcnt(16) lgkmcnt(2)\n\tv_mfma_f32_16x16x32_bf16 v[48:51], a[64:67], v[96:99], v[48:51]\n\tds_read_b128 v[44:47], %[own] offset:19456\n\tv_mfma_f32_16x16x32_bf16 v[52:55], a[68:71], v[96:99], v[52:55]\n\t" \
          "v_mfma_f32_16x16x32_bf16 v[56:59], v[160:163], v[96:99], v[56:59]\n\tv_mfma_f32_16x16x32_bf16 v[60:63], v[32:35], v[96:99], v[60:63]\n\tbuffer_load_dwordx4 v[96:99], %[vo0], s[40:43], %[pg3] offen sc1\n\ts_waitcnt vmcnt(16) lgkmcnt(2)\n\tv_mfma_f32_16x16x32_bf16 v[48:51], a[72:75], v[100:103], v[48:51]\n\tds_read_b128 v[32:35], %[own] offset:20480\n\t" \
          "v_mfma_f32_16x16x32_bf16 v[52:55], a[76:79], v[100:103], v[52:55]\n\tv_mfma_f32_16x16x32_bf16 v[56:59], v[164:167], v[100:103], v[56:59]\n\tv_mfma_f32_16x16x32_bf16 v[60:63], v[36:39], v[100:103], v[60:63]\n\tbuffer_load_dwordx4 v[100:103], %[vo0], s[40:43], %[pg3] offen offset:1024 sc1\n\ts_waitcnt vmcnt(16) lgkmcnt(2)\n\tv_mfma_f32_16x16x32_bf16 v[48:51], a[80:83], v[104:107], v[48:51]\n\t" \
          "ds_read_b128 v[36:39], %[own] offset:21504\n\tv_mfma_f32_16x16x32_bf16 v[52:55], a[84:87], v[104:107], v[52:55]\n\tv_mfma_f32_16x16x32_bf16 v[56:59], v[168:171], v[104:107], v[56:59]\n\tv_mfma_f32_16x16x32_bf16 v[60:63], v[40:43], v[104:107], v[60:63]\n\tbuffer_load_dwordx4 v[104:107], %[vo0], s[40:43], %[pg3] offen offset:2048 sc1\n\ts_waitcnt vmcnt(16) lgkmcnt(2)\n\t" \
          "v_mfma_f32_16x16x32_bf16 v[48:51], a[88:91], v[108:111], v[48:51]\n\tds_read_b128 v[40:43], %[own] offset:22528\n\tv_mfma_f32_16x16x32_bf16 v[52:55], a[92:95], v[108:111], v[52:55]\n\tv_mfma_f32_16x16x32_bf16 v[56:59], v[172:175], v[108:111], v[56:59]\n\tv_mfma_f32_16x16x32_bf16 v[60:63], v[44:47], v[108:111], v[60:63]\n\tbuffer_load_dwordx4 v[108:111], %[vo0], s[40:43], %[pg3] offen offset:3072 sc1\n\t" \
          "s_waitcnt vmcnt(16) lgkmcnt(2)\n\tv_mfma_f32_16x16x32_bf16 v[48:51], a[96:99], v[112:115], v[48:51]\n\tds_read_b128 v[44:47], %[own] offset:23552\n\tv_mfma_f32_16x16x32_bf16 v[52:55], a[100:103], v[112:115], v[52:55]\n\tv_mfma_f32_16x16x32_bf16 v[56:59], v[176:179], v[112:115], v[56:59]\n\tv_mfma_f32_16x16x32_bf16 v[60:63], v[32:35], v[112:115], v[60:63]\n\t" \
          "buffer_load_dwordx4 v[112:115], %[vo1], s[40:43], %[pg3] offen sc1\n\ts_waitcnt vmcnt(16) lgkmcnt(2)\n\tv_mfma_f32_16x16x32_bf16 v[48:51], a[104:107], v[116:119], v[48:51]\n\tds_read_b128 v[32:35], %[own] offset:24576\n\tv_mfma_f32_16x16x32_bf16 v[52:55], a[108:111], v[116:119], v[52:55]\n\tv_mfma_f32_16x16x32_bf16 v[56:59], v[180:183], v[116:119], v[56:59]\n\t" \
          "v_mfma_f32_16x16x32_bf16 v[60:63], v[36:39], v[116:119], v[60:63]\n\tbuffer_load_dwordx4 v[116:119], %[vo1], s[40:43], %[pg3] offen offset:1024 sc1\n\ts_waitcnt vmcnt(16) lgkmcnt(2)\n\tv_mfma_f32_16x16x32_bf16 v[48:51], a[112:115], v[120:123], v[48:51]\n\tds_read_b128 v[36:39], %[own] offset:25600\n\tv_mfma_f32_16x16x32_bf16 v[52:55], a[116:119], v[120:123], v[52:55]\n\t" \
          "v_mfma_f32_16x16x32_bf16 v[56:59], v[184:187], v[120:123], v[56:59]\n\tv_mfma_f32_16x16x32_bf16 v[60:63], v[40:43], v[120:123], v[60:63]\n\tbuffer_load_dwordx4 v[120:123], %[vo1], s[40:43], %[pg3] offen offset:2048 sc1\n\ts_waitcnt vmcnt(16) lgkmcnt(2)\n\tv_mfma_f32_16x16x32_bf16 v[48:51], a[120:123], v[124:127], v[48:51]\n\tds_read_b128 v[40:43], %[own] offset:26624\n\t" \
          "v_mfma_f32_16x16x32_bf16 v[52:55], a[124:127], v[124:127], v[52:55]\n\tv_mfma_f32_16x16x32_bf16 v[56:59], v[188:191], v[124:127], v[56:59]\n\tv_mfma_f32_16x16x32_bf16 v[60:63], v[44:47], v[124:127], v[60:63]\n\tbuffer_load_dwordx4 v[124:127], %[vo1], s[40:43], %[pg3] offen offset:3072 sc1\n\ts_waitcnt vmcnt(15) lgkmcnt(2)\n\tv_mfma_f32_16x16x32_bf16 v[48:51], a[128:131], v[64:67], v[48:51]\n\t" \
          "ds_read_b128 v[44:47], %[own] offset:27648\n\tv_mfma_f32_16x16x32_bf16 v[52:55], a[132:135], v[64:67], v[52:55]\n\tv_cmp_eq_u32 vcc, 0, v30\n\ts_nop 0\n\ts_ff1_i32_b64 %[t], vcc\n\ts_lshr_b32 %[t], %[t], 1\n\t" \
          "s_add_u32 %[av], %[av], %[t]\n\tv_mfma_f32_16x16x32_bf16 v[56:59], v[192:195], v[64:67], v[56:59]\n\ts_lshl_b32 %[oo], %[j], 16\n\ts_cmp_lt_u32 %[j], %[j1]\n\ts_cselect_b32 %[stp], 2, 4\n\ts_add_u32 %[j], %[j], %[stp]\n\t" \
          "s_lshl_b32 %[t], %[stp], 14\n\ts_add_u32 %[pg0], %[pg0], %[t]\n\ts_add_u32 %[pg1], %[pg1], %[t]\n\ts_add_u32 %[pg2], %[pg2], %[t]\n\ts_add_u32 %[pg3], %[pg3], %[t]\n\ts_lshr_b32 %[t], %[j], 1\n\t" \
          "s_cmp_lt_u32 %[t], %[av]\n\ts_cselect_b32 s46, s42, 0\n\tv_mfma_f32_16x16x32_bf16 v[60:63], v[32:35], v[64:67], v[60:63]\n\tbuffer_load_dwordx4 v[64:67], %[vo0], s[44:47], %[pg0] offen sc1\n\ts_waitcnt vmcnt(15) lgkmcnt(2)\n\tv_mfma_f32_16x16x32_bf16 v[48:51], a[136:139], v[68:71], v[48:51]\n\t" \
          "ds_read_b128 v[32:35], %[own] offset:28672\n\tv_mfma_f32_16x16x32_bf16 v[52:55], a[140:143], v[68:71], v[52:55]\n\tv_mfma_f32_16x16x32_bf16 v[56:59], v[196:199], v[68:71], v[56:59]\n\tv_mfma_f32_16x16x32_bf16 v[60:63], v[36:39], v[68:71], v[60:63]\n\tbuffer_load_dwordx4 v[68:71], %[vo0], s[44:47], %[pg0] offen offset:1024 sc1\n\ts_waitcnt vmcnt(15) lgkmcnt(2)\n\t" \
          "v_mfma_f32_16x16x32_bf16 v[48:51], a[144:147], v[72:75], v[48:51]\n\tds_read_b128 v[36:39], %[own] offset:29696\n\tv_mfma_f32_16x16x32_bf16 v[52:55], a[148:151], v[72:75], v[52:55]\n\tv_mfma_f32_16x16x32_bf16 v[56:59], v[200:203], v[72:75], v[56:59]\n\tv_mfma_f32_16x16x32_bf16 v[60:63], v[40:43], v[72:75], v[60:63]\n\tbuffer_load_dwordx4 v[72:75], %[vo0], s[44:47], %[pg0] offen offset:2048 sc1\n\t" \
          "s_waitcnt vmcnt(15) lgkmcnt(2)\n\tv_mfma_f32_16x16x32_bf16 v[48:51], a[152:155], v[76:79], v[48:51]\n\tds_read_b128 v[40:43], %[own] offset:30720\n\tv_mfma_f32_16x16x32_bf16 v[52:55], a[156:159], v[76:79], v[52:55]\n\tv_mfma_f32_16x16x32_bf16 v[56:59], v[204:207], v[76:79], v[56:59]\n\tv_mfma_f32_16x16x32_bf16 v[60:63], v[44:47], v[76:79], v[60:63]\n\t" \
          "buffer_load_dwordx4 v[76:79], %[vo0], s[44:47], %[pg0] offen offset:3072 sc1\n\ts_waitcnt vmcnt(15) lgkmcnt(2)\n\tv_mfma_f32_16x16x32_bf16 v[48:51], a[160:163], v[80:83], v[48:51]\n\tds_read_b128 v[44:47], %[own] offset:31744\n\tv_mfma_f32_16x16x32_bf16 v[52:55], a[164:167], v[80:83], v[52:55]\n\tv_mfma_f32_16x16x32_bf16 v[56:59], v[208:211], v[80:83], v[56:59]\n\t" \
          "v_mfma_f32_16x16x32_bf16 v[60:63], v[32:35], v[80:83], v[60:63]\n\tbuffer_load_dwordx4 v[80:83], %[vo1], s[44:47], %[pg0] offen sc1\n\ts_waitcnt vmcnt(15) lgkmcnt(2)\n\tv_mfma_f32_16x16x32_bf16 v[48:51], a[168:171], v[84:87], v[48:51]\n\tds_read_b128 v[32:35], %[own] offset:0\n\tv_mfma_f32_16x16x32_bf16 v[52:55], a[172:175], v[84:87], v[52:55]\n\t" \
          "v_mfma_f32_16x16x32_bf16 v[56:59], v[212:215], v[84:87], v[56:59]\n\tv_mfma_f32_16x16x32_bf16 v[60:63], v[36:39], v[84:87], v[60:63]\n\tbuffer_load_dwordx4 v[84:87], %[vo1], s[44:47], %[pg0] offen offset:1024 sc1\n\ts_waitcnt vmcnt(15) lgkmcnt(2)\n\tv_mfma_f32_16x16x32_bf16 v[48:51], a[176:179], v[88:91], v[48:51]\n\tds_read_b128 v[36:39], %[own] offset:1024\n\t" \
          "v_mfma_f32_16x16x32_bf16 v[52:55], a[180:183], v[88:91], v[52:55]\n\tv_mfma_f32_16x16x32_bf16 v[56:59], v[216:219], v[88:91], v[56:59]\n\tv_mfma_f32_16x16x32_bf16 v[60:63], v[40:43], v[88:91], v[60:63]\n\tbuffer_load_dwordx4 v[88:91], %[vo1], s[44:47], %[pg0] offen offset:2048 sc1\n\ts_waitcnt vmcnt(15) lgkmcnt(2)\n\tv_mfma_f32_16x16x32_bf16 v[48:51], a[184:187], v[92:95], v[48:51]\n\t" \
          "ds_read_b128 v[40:43], %[own] offset:2048\n\tv_mfma_f32_16x16x32_bf16 v[52:55], a[188:191], v[92:95], v[52:55]\n\tv_mfma_f32_16x16x32_bf16 v[56:59], v[220:223], v[92:95], v[56:59]\n\tv_mfma_f32_16x16x32_bf16 v[60:63], v[44:47], v[92:95], v[60:63]\n\tbuffer_load_dwordx4 v[92:95], %[vo1], s[44:47], %[pg0] offen offset:3072 sc1\n\ts_waitcnt vmcnt(15) lgkmcnt(2)\n\t" \
          "v_mfma_f32_16x16x32_bf16 v[48:51], a[192:195], v[96:99], v[48:51]\n\tds_read_b128 v[44:47], %[own] offset:3072\n\tv_mfma_f32_16x16x32_bf16 v[52:55], a[196:199], v[96:99], v[52:55]\n\tv_mfma_f32_16x16x32_bf16 v[56:59], v[224:227], v[96:99], v[56:59]\n\tv_mfma_f32_16x16x32_bf16 v[60:63], v[32:35], v[96:99], v[60:63]\n\tbuffer_load_dwordx4 v[96:99], %[vo0], s[44:47], %[pg1] offen sc1\n\t" \
          "s_waitcnt vmcnt(15) lgkmcnt(2)\n\tv_mfma_f32_16x16x32_bf16 v[48:51], a[200:203], v[100:103], v[48:51]\n\tds_read_b128 v[32:35], %[own] offset:4096\n\tv_mfma_f32_16x16x32_bf16 v[52:55], a[204:207], v[100:103], v[52:55]\n\tv_mfma_f32_16x16x32_bf16 v[56:59], v[228:231], v[100:103], v[56:59]\n\tv_mfma_f32_16x16x32_bf16 v[60:63], v[36:39], v[100:103], v[60:63]\n\t" \
          "buffer_load_dwordx4 v[100:103], %[vo0], s[44:47], %[pg1] offen offset:1024 sc1\n\ts_waitcnt vmcnt(15) lgkmcnt(2)\n\tv_mfma_f32_16x16x32_bf16 v[48:51], a[208:211], v[104:107], v[48:51]\n\tds_read_b128 v[36:39], %[own] offset:5120\n\tv_mfma_f32_16x16x32_bf16 v[52:55], a[212:215], v[104:107], v[52:55]\n\tv_mfma_f32_16x16x32_bf16 v[56:59], v[232:235], v[104:107], v[56:59]\n\t" \
          "v_mfma_f32_16x16x32_bf16 v[60:63], v[40:43], v[104:107], v[60:63]\n\tbuffer_load_dwordx4 v[104:107], %[vo0], s[44:47], %[pg1] offen offset:2048 sc1\n\ts_waitcnt vmcnt(15) lgkmcnt(2)\n\tv_mfma_f32_16x16x32_bf16 v[48:51], a[216:219], v[108:111], v[48:51]\n\tds_read_b128 v[40:43], %[own] offset:6144\n\tv_mfma_f32_16x16x32_bf16 v[52:55], a[220:223], v[108:111], v[52:55]\n\t" \
          "v_mfma_f32_16x16x32_bf16 v[56:59], v[236:239], v[108:111], v[56:59]\n\tv_mfma_f32_16x16x32_bf16 v[60:63], v[44:47], v[108:111], v[60:63]\n\tbuffer_load_dwordx4 v[108:111], %[vo0], s[44:47], %[pg1] offen offset:3072 sc1\n\ts_waitcnt vmcnt(15) lgkmcnt(2)\n\tv_mfma_f32_16x16x32_bf16 v[48:51], a[224:227], v[112:115], v[48:51]\n\tds_read_b128 v[44:47], %[own] offset:7168\n\t" \
          "v_mfma_f32_16x16x32_bf16 v[52:55], a[228:231], v[112:115], v[52:55]\n\tv_mfma_f32_16x16x32_bf16 v[56:59], v[240:243], v[112:115], v[56:59]\n\tv_mfma_f32_16x16x32_bf16 v[60:63], v[32:35], v[112:115], v[60:63]\n\tbuffer_load_dwordx4 v[112:115], %[vo1], s[44:47], %[pg1] offen sc1\n\ts_waitcnt vmcnt(15) lgkmcnt(2)\n\tv_mfma_f32_16x16x32_bf16 v[48:51], a[232:235], v[116:119], v[48:51]\n\t" \
          "v_mfma_f32_16x16x32_bf16 v[52:55], a[236:239], v[116:119], v[52:55]\n\tv_mfma_f32_16x16x32_bf16 v[56:59], v[244:247], v[116:119], v[56:59]\n\tv_mfma_f32_16x16x32_bf16 v[60:63], v[36:39], v[116:119], v[60:63]\n\tbuffer_load_dwordx4 v[116:119], %[vo1], s[44:47], %[pg1] offen offset:1024 sc1\n\ts_waitcnt vmcnt(15) lgkmcnt(1)\n\tv_mfma_f32_16x16x32_bf16 v[48:51], a[240:243], v[120:123], v[48:51]\n\t" \
          "v_mfma_f32_16x16x32_bf16 v[52:55], a[244:247], v[120:123], v[52:55]\n\tv_mfma_f32_16x16x32_bf16 v[56:59], v[248:251], v[120:123], v[56:59]\n\tv_mfma_f32_16x16x32_bf16 v[60:63], v[40:43], v[120:123], v[60:63]\n\tbuffer_load_dwordx4 v[120:123], %[vo1], s[44:47], %[pg1] offen offset:2048 sc1\n\ts_waitcnt vmcnt(15) lgkmcnt(0)\n\tv_mfma_f32_16x16x32_bf16 v[48:51], a[248:251], v[124:127], v[48:51]\n\t" \
          "v_mfma_f32_16x16x32_bf16 v[52:55], a[252:255], v[124:127], v[52:55]\n\tv_mfma_f32_16x16x32_bf16 v[56:59], v[252:255], v[124:127], v[56:59]\n\tv_mfma_f32_16x16x32_bf16 v[60:63], v[44:47], v[124:127], v[60:63]\n\tbuffer_load_dwordx4 v[124:127], %[vo1], s[44:47], %[pg1] offen offset:3072 sc1\n\t" OU_ST1 "s_nop 6\n\ts_cmp_eq_u32 %[sp], 1\n\t" \
          "s_cbranch_scc0 Lou_red%=\n\tv_cmp_u_f32 vcc, v48, v48\n\ts_mov_b32 %[sp], 2\n\ts_cbranch_vccz Lou_red%=\n\ts_sub_u32 %[j], %[j], %[stp]\n\ts_lshl_b32 %[t], %[stp], 14\n\t" \
          "s_sub_u32 %[pg0], %[pg0], %[t]\n\ts_sub_u32 %[pg1], %[pg1], %[t]\n\ts_sub_u32 %[pg2], %[pg2], %[t]\n\ts_sub_u32 %[pg3], %[pg3], %[t]\n\ts_branch Lou_wait%=\n\tLou_red%=:\n\t" \
          "ds_write_b128 %[rw_], v[48:51] offset:0\n\tds_write_b128 %[rw_], v[52:55] offset:1024\n\tds_write_b128 %[rw_], v[56:59] offset:2048\n\tds_write_b128 %[rw_], v[60:63] offset:3072\n\ts_waitcnt lgkmcnt(0)\n\ts_barrier\n\t" \
          "ds_read_b128 v[32:35], %[rr0] offset:0\n\tds_read_b128 v[36:39], %[rr0] offset:40960\n\tds_read_b128 v[40:43], %[rr1] offset:0\n\tds_read_b128 v[44:47], %[rr1] offset:40960\n\tv_xor_b32 %[rw_], 0x1000, %[rw_]\n\tv_xor_b32 %[rr0], 0x1000, %[rr0]\n\t" \
          "v_xor_b32 %[rr1], 0x1000, %[rr1]\n\ts_waitcnt lgkmcnt(0)\n\tv_pk_add_f32 v[32:33], v[32:33], v[36:37]\n\tv_pk_add_f32 v[34:35], v[34:35], v[38:39]\n\tv_pk_add_f32 v[40:41], v[40:41], v[44:45]\n\tv_pk_add_f32 v[42:43], v[42:43], v[46:47]\n\t" \
          "v_pk_add_f32 v[32:33], v[32:33], v[40:41]\n\tv_pk_add_f32 v[34:35], v[34:35], v[42:43]\n\ts_nop 0\n\tv_cvt_pk_bf16_f32 v44, v32, v33\n\tv_cvt_pk_bf16_f32 v45, v34, v35\n\tbuffer_store_dwordx2 v[44:45], %[so], %[rc], %[oo] offen" OU_AUX "\n\t" \
          "s_cmp_lg_u32 s46, 0\n\ts_cbranch_scc1 Lou_top%=\n\ts_cmp_lt_u32 %[j], 128\n\ts_cbranch_scc1 Lou_wait%=\n\ts_waitcnt vmcnt(0)\n\t"
#define OU_CLOB "v30", "v31", "v32", "v33", "v34", "v35", "v36", "v37", "v38", "v39", "v40", "v41", "v42", "v43", "v44", "v45", "v46", "v47", "v48", "v49", "v50", "v51", "v52", "v53", "v54", "v55", "v56", "v57", "v58", "v59", "v60", "v61", "v62", "v63", "v64", "v65", "v66", "v67", "v68", "v69", "v70", "v71", "v72", "v73", "v74", "v75", "v76", "v77", "v78", "v79", "v80", "v81", "v82", "v83", "v84", "v85", "v86", "v87", "v88", "v89", "v90", "v91", "v92", "v93", "v94", "v95", "v96", "v97", "v98", "v99", "v100", "v101", "v102", "v103", "v104", "v105", "v106", "v107", "v108", "v109", "v110", "v111", "v112", "v113", "v114", "v115", "v116", "v117", "v118", "v119", "v120", "v121", "v122", "v123", "v124", "v125", "v126", "v127", "v128", "v129", "v130", "v131", "v132", "v133", "v134", "v135", "v136", "v137", "v138", "v139", "v140", "v141", "v142", "v143", "v144", "v145", "v146", "v147", "v148", "v149", "v150", "v151", "v152", "v153", "v154", "v155", "v156", "v157", "v158", "v159", "v160", "v161", "v162", "v163", "v164", "v165", "v166", "v167", "v168", "v169", "v170", "v171", "v172", "v173", "v174", "v175", "v176", "v177", "v178", "v179", "v180", "v181", "v182", "v183", "v184", "v185", "v186", "v187", "v188", "v189", "v190", "v191", "v192", "v193", "v194", "v195", "v196", "v197", "v198", "v199", "v200", "v201", "v202", "v203", "v204", "v205", "v206", "v207", "v208", "v209", "v210", "v211", "v212", "v213", "v214", "v215", "v216", "v217", "v218", "v219", "v220", "v221", "v222", "v223", "v224", "v225", "v226", "v227", "v228", "v229", "v230", "v231", "v232", "v233", "v234", "v235", "v236", "v237", "v238", "v239", "v240", "v241", "v242", "v243", "v244", "v245", "v246", "v247", "v248", "v249", "v250", "v251", "v252", "v253", "v254", "v255", "a0", "a1", "a2", "a3", "a4", "a5", "a6", "a7", "a8", "a9", "a10", "a11", "a12", "a13", "a14", "a15", "a16", "a17", "a18", "a19", "a20", "a21", "a22", "a23", "a24", "a25", "a26", "a27", "a28", "a29", "a30", "a31", "a32", "a33", "a34", "a35", "a36", "a37", "a38", "a39", "a40", "a41", "a42", "a43", "a44", "a45", "a46", "a47", "a48", "a49", "a50", "a51", "a52", "a53", "a54", "a55", "a56", "a57", "a58", "a59", "a60", "a61", "a62", "a63", "a64", "a65", "a66", "a67", "a68", "a69", "a70", "a71", "a72", "a73", "a74", "a75", "a76", "a77", "a78", "a79", "a80", "a81", "a82", "a83", "a84", "a85", "a86", "a87", "a88", "a89", "a90", "a91", "a92", "a93", "a94", "a95", "a96", "a97", "a98", "a99", "a100", "a101", "a102", "a103", "a104", "a105", "a106", "a107", "a108", "a109", "a110", "a111", "a112", "a113", "a114", "a115", "a116", "a117", "a118", "a119", "a120", "a121", "a122", "a123", "a124", "a125", "a126", "a127", "a128", "a129", "a130", "a131", "a132", "a133", "a134", "a135", "a136", "a137", "a138", "a139", "a140", "a141", "a142", "a143", "a144", "a145", "a146", "a147", "a148", "a149", "a150", "a151", "a152", "a153", "a154", "a155", "a156", "a157", "a158", "a159", "a160", "a161", "a162", "a163", "a164", "a165", "a166", "a167", "a168", "a169", "a170", "a171", "a172", "a173", "a174", "a175", "a176", "a177", "a178", "a179", "a180", "a181", "a182", "a183", "a184", "a185", "a186", "a187", "a188", "a189", "a190", "a191", "a192", "a193", "a194", "a195", "a196", "a197", "a198", "a199", "a200", "a201", "a202", "a203", "a204", "a205", "a206", "a207", "a208", "a209", "a210", "a211", "a212", "a213", "a214", "a215", "a216", "a217", "a218", "a219", "a220", "a221", "a222", "a223", "a224", "a225", "a226", "a227", "a228", "a229", "a230", "a231", "a232", "a233", "a234", "a235", "a236", "a237", "a238", "a239", "a240", "a241", "a242", "a243", "a244", "a245", "a246", "a247", "a248", "a249", "a250", "a251", "a252", "a253", "a254", "a255", "s40", "s41", "s42", "s43", "s44", "s45", "s46", "s47"
__device__ __forceinline__ void out_unit(const FParams& F, unsigned long long parq, int set, int un, int w) {
  const int lane = opaque_lane(), l15 = lane & 15, lq = lane >> 4, r = un & 7;
  const __amdgpu_buffer_rsrc_t rw = __builtin_amdgcn_make_buffer_rsrc(wg::uni(F.Wo + 64 * un), 0, HDA * DM * 2 - 128 * un, 0x00020000);
  const __amdgpu_buffer_rsrc_t rc = __builtin_amdgcn_make_buffer_rsrc(wg::uni(F.out + 64 * un), 0, S_ * 2 * DM * 2 - 128 * un, 0x00020000);
  const __amdgpu_buffer_rsrc_t rf = __builtin_amdgcn_make_buffer_rsrc(F.ctl, 0, sizeof(Ctl), 0x00020000);
  const int own = w * 40960 + lane * 16;
  int j = (2 * OSEG0 + (set < 2 ? 0 : TP(F.oj1, OJ1))) + set, av = OSEG0, pg[4], t, cnt, oo, sp = 0, stp, rw_ = own + 32768, rr0 = 32768 + (l15 >> 2) * 1024 + ((l15 & 3) * 16 + 4 * w + lq) * 16, rr1 = rr0 + 81920;
#pragma unroll
  for (int i = 0; i < 4; ++i) pg[i] = __builtin_amdgcn_readfirstlane(qoff(parq, 4 * w + (i ^ (r >> 1))) + j * QBLK);
  const int rt = w * 40960 + (8 * lq + (l15 >> 2)) * 128 + (((l15 >> 3) | ((lq & 1) << 1)) << 5) + (l15 & 3) * 8, vw = (lane >> 3) * 2048 + (((lane & 7) ^ ((lq & 1) << 1)) << 4);
  asm volatile(OU_BODY
    : [j] "+s"(j), [av] "+s"(av), [pg0] "+s"(pg[0]), [pg1] "+s"(pg[1]), [pg2] "+s"(pg[2]), [pg3] "+s"(pg[3]), [t] "=&s"(t), [cnt] "=&s"(cnt), [oo] "=&s"(oo), [sp] "+s"(sp), [stp] "=&s"(stp), [rw_] "+v"(rw_), [rr0] "+v"(rr0), [rr1] "+v"(rr1) OU_STO
    : [vo0] "v"(lane * 16 + (r & 1) * 4096), [vo1] "v"(lane * 16 + (~r & 1) * 4096), [vw0] "v"(vw), [vw1] "v"(vw ^ 64), [rt0] "v"(rt), [rt1] "v"(rt ^ 32), [rt2] "v"(rt ^ 64), [rt3] "v"(rt ^ 96), [own] "v"(own),
      [vf] "v"(lane < 8 ? (lane >> 1) * 1024 + (2 * w + (lane & 1)) * 128 : 0x7ffffff0), [so] "v"((4 * w + lq) * 4096 + l15 * 8),
      [qa] "s"(F.qkvA), [qs] "s"(QSIZE), [rw] "s"(rw), [rc] "s"(rc), [rf] "s"(rf), [wl] "s"(w * 40960), [wo] "s"((1024 * w + 128 * r) * 2048), [fo] "s"((int)offsetof(Ctl, flagO)), [tmo] "s"((int)offsetof(Ctl, tmo)), [j1] "s"(2 * OSEG0 + TP(F.oj1, OJ1))
    : OU_CLOB, "vcc", "scc", "memory");
}

__device__ __forceinline__ void attnB_task(const FParams& F, unsigned long long parq, int r, int pg, u16* vs  , int w) {
  const int lane = opaque_lane(), l15 = lane & 15, lq = lane >> 4;
  const __amdgpu_buffer_rsrc_t rs = __builtin_amdgcn_make_buffer_rsrc(F.qkvA, 0, QSIZE, 0x00020000);
  const int so = 8 * r * QBLK + w * 2048, oq = so + qoff(parq, 48 + pg), ok = so + qoff(parq, 52 + pg), ov = so + qoff(parq, 56 + pg), p0 = r ? -QBLK : 0;
  v4u q[2][8], k[2][9], v[2][9];
#pragma unroll
  for (int hh = 0; hh < 2; ++hh) {
#pragma unroll
    for (int i = 0; i < 9; ++i) v[hh][i] = __builtin_amdgcn_raw_buffer_load_b128(rs, lane * 16 + hh * 1024, ov + (i ? (i - 1) * QBLK : p0), 0);
#pragma unroll
    for (int i = 0; i < 9; ++i) { k[hh][i] = __builtin_amdgcn_raw_buffer_load_b128(rs, lane * 16 + hh * 1024, ok + (i ? (i - 1) * QBLK : p0), 0); if (i < 8) q[hh][i] = __builtin_amdgcn_raw_buffer_load_b128(rs, lane * 16 + hh * 1024, oq + i * QBLK, 0); }
  }
  const float scale = 0.17677669529663687f, L2E = 1.4426950408889634f;
  const bool par = l15 & 1, ownA = l15 - 4 * lq - par >= 0, ownB = l15 - 4 * lq - par - 2 >= 0;
  const unsigned sa = par ? 0x01000c0cu : 0x0c0c0100u, sb = par ? 0x03020c0cu : 0x0c0c0302u, sz = 0x0c0c0c0cu;
  const unsigned sel[4] = {ownA ? sz : sa, ownB ? sz : sb, ownA ? sa : sz, ownB ? sb : sz};
  const u16* vp = &vs[(4 * lq + (l15 >> 2)) * 40 + 16 * (l15 & 1) + 2 * (l15 & 2)];
#pragma unroll
  for (int hh = 0; hh < 2; ++hh) {
    SB0;
#pragma unroll
    for (int i = 0; i < 9; ++i) *(v4u*)&vs[(16 * i + l15) * 40 + 8 * lq] = v[hh][i];
#pragma unroll
    for (int i = 0; i < 8; ++i) {
      const f32x4 z = {0.f, 0.f, 0.f, 0.f}, s0 = mf(k[hh][i], q[hh][i], z), s1 = mf(k[hh][i + 1], q[hh][i], z);
      float a = ownA ? (par ? s1[1] : s1[0]) : (par ? s0[1] : s0[0]), b = ownB ? (par ? s1[3] : s1[2]) : (par ? s0[3] : s0[2]);
      unsigned p = pk2(a, b);
      p = pk2(__uint_as_float(p << 16) * scale, __uint_as_float(p & 0xffff0000u) * scale);
      a = __uint_as_float(p << 16); b = __uint_as_float(p & 0xffff0000u);
      const bool va = i > 0 || r > 0 || ownA, vb = i > 0 || r > 0 || ownB;
      const float mx = xmax32(xmax16(fmaxf(va ? a : -INFINITY, vb ? b : -INFINITY)));
      a = va ? __builtin_amdgcn_exp2f((a - mx) * L2E) : 0.f; b = vb ? __builtin_amdgcn_exp2f((b - mx) * L2E) : 0.f;
      const float inv = __builtin_amdgcn_rcpf(xsum32(xsum16(a + b)));
      p = pk2(a * inv, b * inv);
      const v4u px = {__builtin_amdgcn_perm(0u, p, sel[0]), __builtin_amdgcn_perm(0u, p, sel[1]), __builtin_amdgcn_perm(0u, p, sel[2]), __builtin_amdgcn_perm(0u, p, sel[3])};
      f32x4 o[2];
#pragma unroll
      for (int ab = 0; ab < 2; ++ab) { const v2u lo = trrd(vp + 16 * i * 40 + 8 * ab), hi = trrd(vp + 16 * (i + 1) * 40 + 8 * ab); o[ab] = mf((v4u){lo.x, lo.y, hi.x, hi.y}, px, z); }
      __builtin_amdgcn_raw_buffer_store_b128((v4u){pk2(o[0][0], o[0][1]), pk2(o[0][2], o[0][3]), pk2(o[1][0], o[1][1]), pk2(o[1][2], o[1][3])}, rs, lane * 16 + hh * 1024, oq + i * QBLK, 16);
    }
  }
}

constexpr int NTASK = 240;
__constant__ unsigned short TASKS[NTASK] = {
  5120, 5121, 5122, 5123, 5124, 5125, 5126, 5127, 5128, 5129, 5130, 5131, 5132, 5133, 5134, 5135, 5136, 5137, 5138, 5139, 5140, 5141, 5142, 5143,
  3072, 3073, 3074, 3075, 3076, 3077, 3078, 3079, 3080, 3081, 3082, 3083, 3084, 3085, 3086, 3087, 3088, 3089, 3090, 3091, 3092, 3093, 3094, 3095,
  6144, 6145, 6146, 6147, 6148, 6149, 6150, 6151, 5144, 5145, 5146, 5147, 5148, 5149, 5150, 5151, 5152, 5153, 5154, 5155, 5156, 5157, 5158, 5159,
  6152, 6153, 6154, 6155, 4096, 4097, 4098, 4099, 4100, 4101, 4102, 4103, 4104, 4105, 4106, 4107, 5160, 5161, 5162, 5163, 5164, 5165, 5166, 5167,
  6156, 6157, 6158, 6159, 5168, 5169, 5170, 5171, 5172, 5173, 5174, 5175, 5176, 5177, 5178, 5179, 5180, 5181, 5182, 5183, 7168, 7169, 7170, 7171,
  7172, 7173, 7174, 7175, 7176, 7177, 7178, 7179, 7180, 7181, 7182, 7183, 7184, 7185, 7186, 7187, 7188, 7189, 7190, 7191, 7192, 7193, 7194, 7195,
  7196, 7197, 7198, 7199, 4108, 4109, 4110, 4111, 4112, 4113, 4114, 4115, 4116, 4117, 4118, 4119, 3096, 3097, 3098, 3099, 3100, 3101, 3102, 3103,
  3104, 3105, 3106, 3107, 3108, 3109, 3110, 3111, 4120, 4121, 4122, 4123, 4124, 4125, 4126, 4127, 4128, 4129, 4130, 4131, 4132, 4133, 4134, 4135,
  3112, 3113, 3114, 3115, 3116, 3117, 3118, 3119, 3120, 3121, 3122, 3123, 3124, 3125, 3126, 3127, 3128, 3129, 3130, 3131, 3132, 3133, 3134, 3135,
  4136, 4137, 4138, 4139, 4140, 4141, 4142, 4143, 4144, 4145, 4146, 4147, 4148, 4149, 4150, 4151, 4152, 4153, 4154, 4155, 4156, 4157, 4158, 4159,
};
__device__ __forceinline__ const FParams& args() {
  typedef const __attribute__((address_space(4))) FParams* KP;
  KP p = (KP)__builtin_amdgcn_kernarg_segment_ptr(); asm volatile("" : "+s"(p)); return *(const FParams*)p;
}
__device__ __forceinline__ void wait_until(unsigned t) { while ((int)(t - (unsigned)__builtin_amdgcn_s_memrealtime()) > 0) __builtin_amdgcn_s_sleep(8); }
template <int AUX = 0>
__device__ __forceinline__ void touch(const void* base, int bytes, int voff, int soff, char* lds) {
  __builtin_amdgcn_raw_ptr_buffer_load_lds(__builtin_amdgcn_make_buffer_rsrc((void*)base, 0, bytes, 0x00020000), (__attribute__((address_space(3))) void*)(lds + wg::LDS_USE + 1024), 4, voff, soff, 0, AUX);
}
constexpr int RECO = 32768;
__device__ __forceinline__ void setup(const FParams& F, int n, int w) {
  const int lane = opaque_lane(), tid = 64 * w + lane;
  const v4u ff = {0xffffffffu, 0xffffffffu, 0xffffffffu, 0xffffffffu}, ep = rdy_load(F);
  const __amdgpu_buffer_rsrc_t rs = __builtin_amdgcn_make_buffer_rsrc(F.ring, 0, QPAGE + QSIZE, 0x00020000);
  const int o = ((lane & 16) ? (n < 8 ? (1 + NPAGE + n) * QPAGE : RSIZE + n * 128) : (1 + n) * QPAGE) + ((lane >> 3) & 1) * 8192 + (lane & 7) * 16;
  __builtin_amdgcn_raw_buffer_store_b128(ff, rs, tid < 32 ? o : 0x7ffffff0, 0, 16);
#pragma unroll 2
  for (int i = n * 256 + tid; i < RSIZE / 16; i += NPAGE * 256) {
    v4u v = ff;
    const int m = i - ROFF_M / 16;
    if ((unsigned)m < 2048u) {
      const int q = (m >> 10) * 512 + (m & 511);
      const u16* src = F.mem0 + (((q >> 4) & 1) * 4 + ((q >> 1) & 3)) * DM + 32 * (q >> 5) + 8 * ((q >> 3) & 1) + 4 * (q & 1);
#pragma unroll
      for (int p = 0; p < 4; ++p) v[p] = nosent(src[p] | ((unsigned)src[p + 16] << 16));
    }
    __builtin_amdgcn_raw_buffer_store_b128(v, rs, i * 16, 0, 16);
  }
  __builtin_amdgcn_raw_buffer_store_b128((v4u){0u, 0u, 0u, 0u}, __builtin_amdgcn_make_buffer_rsrc(F.ctl, 0, sizeof(Ctl), 0x00020000), w >= 2 ? (n * 128 + tid - 128) * 16 : 0x7ffffff0, 0, 16);
  static_assert(sizeof(Ctl) <= NPAGE * 128 * 16, "");
  const unsigned long long tag = rdy_tag(ep);
  asm volatile("s_waitcnt vmcnt(0)" ::: "memory");
  __syncthreads();
  if (w == 0) {
    unsigned best[4] = {~0u, ~0u, ~0u, ~0u};
#pragma unroll
    for (int g = 0; g < 4; g += 2)
#pragma unroll 1
      for (int r = 0; r < TP(F.probes, PROBES); ++r) {
#pragma unroll
        for (int t = g; t < g + 2; ++t) {
          const unsigned long long t0 = __builtin_amdgcn_s_memtime();
          __builtin_amdgcn_raw_buffer_store_b128(ff, rs, (lane >> 3) == t ? o : 0x7ffffff0, 0, 16);
          asm volatile("s_waitcnt vmcnt(0)" ::: "memory");
          best[t] = min(best[t], (unsigned)(__builtin_amdgcn_s_memtime() - t0));
        }
        if (min(best[g], best[g + 1]) < TP(F.sh2, PTH) && max(best[g], best[g + 1]) > min(best[g], best[g + 1]) + 120) break;
      }
    unsigned xcc; asm volatile("s_getreg_b32 %0, hwreg(HW_REG_XCC_ID)" : "=s"(xcc));
    const bool flip = ((xcc ^ (xcc - blockIdx.x + 1)) >> 2) & 1;
    const unsigned bits = ((best[1] < best[0]) == flip ? 1 : 0) | (((best[3] < best[2]) != flip) != (n < 8) ? 4 : 0);
    __builtin_amdgcn_raw_buffer_store_b128((v4u){(unsigned)tag, (unsigned)(tag >> 32), bits, 0u}, __builtin_amdgcn_make_buffer_rsrc(F.rdy, 0, RDY_SIZE, 0x00020000), lane < 8 ? n * 128 + lane * 16 : 0x7ffffff0, 0, 16);
  }
}
__device__ __forceinline__ void worker(const FParams& F, unsigned cz, char* lds, const int w0) {
#define lane opaque_lane()
#define tid (64 * w + opaque_lane())
  const int grp = (blockIdx.x & 7) - 3, slot = blockIdx.x >> 3;
#define TLOG(t, e)
  unsigned long long par, tag; unsigned pk;
  { int w = w0; v4u rec;
    const unsigned t0 = (unsigned)__builtin_amdgcn_s_memrealtime();
    const int id = slot * 4 + w, tz = opaque_lane();
    if (grp != 2) {
      if (grp > 2) { if (slot < 30) setup(F, (grp - 3) * 30 + slot, w); TLOG(1024 + grp * 32 + slot, 0); wait_until(t0 + TP(F.stag, STAG)); }
      rec = panel_task<NTP>(F, (grp < 2 ? grp + 1 : 0) * 32 + slot, (grp - (grp > 2)) * 32 + slot, grp == 4 ? 64 : 0, grp == 3 ? 64 : 128, w);
    } else {
#pragma unroll
      for (int q = 0; q < 4; ++q) {
        const u16* base = q == 0 ? F.Wk : (q == 1 ? F.Wo : (q == 2 ? F.Wq : F.Wv));
        const int o3 = (384 - (int)(size_t)base) & 896, o63 = (8064 - (int)(size_t)base) & 8064;
        touch(base, DM * HDA * 2, (tz * 128 + id) * 1024 + o3, 0, lds);
        if (!(q & 1)) { touch(base, DM * HDA * 2, (tz * 128 + id) * 1024 + (o3 ^ 128), 0, lds); touch(base, DM * HDA * 2, tz < 16 ? ((tz >> 1) * 128 + id) * 8192 + (o63 ^ ((tz & 1) << 7)) : 0x7ffffff0, 0, lds); }
      }
#pragma unroll 1
      for (int q = id; q < 768; q += 128) {
        wait_until(t0 + (TP(F.px, PXV) >> 16) + ((q * (TP(F.px, PXV) & 0xffff)) >> 6));
        if (q < 512) touch(F.Ax, S_ * DM * 2, tz * 128, ((q & 1) * 256 + (q >> 1)) * 8192, lds); else touch(F.woB, DB * DB * 2, tz * 128, (q - 512) * 8192, lds);
      }
      if (slot < 28) wait_until(t0 + (TP(F.pb, PBV) >> 16));
      rec = rdy_load(F);
      asm volatile("s_waitcnt vmcnt(0)" ::: "memory");
      __syncthreads();
    }
    asm volatile("" :: "v"(cz));
    par = rdy_wait(F, rec, tag, pk);
    if (grp == 2 || grp == 3) {
      const FParams& F = args();
      asm volatile("" : "+s"(w));
      const int j0 = grp == 2 ? 0 : TP(F.jb, JB), j1 = grp == 2 ? TP(F.jb, JB) : 128;
      if (slot < 24) (void)panel_task<true>(F, 96 + slot, 64 + grp * 32 + slot, j0, j1, w);
      else if (slot < 28) {
        const int id = (slot - 24) * 4 + w, tz = opaque_lane();
        int seen = j0;
#pragma unroll 1
        for (int j = j0 + (id >> 2); j < j1; j += 4) {
          for (unsigned spins = 0; j > seen + 16; ++spins) {
            seen = max(seen, __builtin_amdgcn_readfirstlane(ld_agent(&F.ctl->prg[64 + grp * 32 + id][0])));
            if (spin_fail(spins, &F.ctl->tmo[0], 2u)) break;
            __builtin_amdgcn_s_sleep(32);
          }
          touch<2>(F.Bx, S_ * DB * 2, tz * 128, (j * 4 + (id & 3)) * 8192, lds);
          touch(F.Bx, S_ * DB * 2, tz * 128 + 64, (j * 4 + (id & 3)) * 8192, lds);
        }
        asm volatile("s_waitcnt vmcnt(0)" ::: "memory");
      }
    }
    check_in(F, tag, w);
    TLOG(1024 + grp * 32 + slot, 1);
    if (grp < 2) {
      const FParams& F = args();
      asm volatile("" : "+s"(w));
      out_unit(F, par, 2 * grp + (slot >> 4), slot & 15, w);
      TLOG(1280 + grp * 32 + slot, 1);
      return;
    } }
  Ctl* ctl = F.ctl;
  int* sh = (int*)(lds + wg::LDS_USE);
  int next = -1;
  for (;;) {
    const FParams& F = args();
    int w = w0; asm volatile("" : "+s"(w));
    if (next < 0) {
      if (tid == 0) sh[0] = (int)__hip_atomic_fetch_add(&ctl->qhead[0], 1u, RLX_AGENT);
      __syncthreads();
      const int t = __builtin_amdgcn_readfirstlane(sh[0]);
      __syncthreads();
      { if (t >= NTASK) return; next = TASKS[t]; }
    }
    const int code = __builtin_amdgcn_readfirstlane(next); next = -1;
    const int tk = code >> 10, u = code & 1023;
    const u16 *A = nullptr, *Bt = nullptr; void* C = nullptr; int lda = 0, ldb = 0, ldc = 0, nk = 32, r = u >> 2, n = u & 3, nsig = 1;
    const unsigned* wp = nullptr; unsigned need = 0; unsigned* sig = nullptr;
    if (tk == 3) {
      const int lz = opaque_lane(); const bool hi = 8 * r >= TP(F.jb, JB);
      need = lz < 8 ? 8 * r + 8 : TP(F.jb, JB); sig = &ctl->cntAB[r][0];
      if ((lz & 7) < 6 && lz < (hi ? 16 : 8)) wp = &ctl->prg[(lz < 8 && hi ? 160 : 128) + ((lz & 7) >> 1) * 8 + 2 * n + (lz & 1)][0];
    } else if (tk == 4) {
      A = F.qkvA + r * 4 * QBLK + 24 * QPAGE; lda = QBLK / 32; Bt = F.woB + n * 256; ldb = DB;
      C = F.out + (size_t)r * 128 * 2 * DM + DM + n * 256; ldc = 2 * DM; need = 4; if (lane == 0) wp = &ctl->cntAB[r][0];
    } else if (tk == 5) {
      r = u >> 3; n = u & 7; sig = &ctl->flagO[4 * r][n][0]; nsig = 4; need = 8 * r + 8;
      const int lz = opaque_lane(); if (lz < 12) wp = &ctl->prg[((lz >> 2) + (lz >= 8 && r >= 8)) * 32 + 4 * n + (lz & 3)][0];
    } else if (tk == 6) {
      A = F.qkvA + r * 4 * QBLK; lda = QBLK / 32; Bt = F.Wo + n * 256; ldb = DM; nk = HDA / 32;
      C = F.out + (size_t)r * 128 * 2 * DM + n * 256; ldc = 2 * DM; need = 1; { const int lz = opaque_lane(); if (lz < 8) wp = &ctl->flagO[4 * r][lz][0]; }
    } else {
      attn_stream(F, par, pk, u >> 3, u & 7, lds, w);
      TLOG(lastt, 1);
      return;
    }
    wg_wait(wp, need, ctl, w);
    if (tk == 5) {
      attnA_block(F, par, pk, r, 4 * n + w, (u16*)(lds + w * 16384), opaque_lane(), w, (unsigned*)sh + 4);
      asm volatile("s_waitcnt vmcnt(0)" ::: "memory");
      __syncthreads();
    } else if (tk == 3) {
      attnB_task(F, par, r, n, (u16*)(lds + w * 16384), w);
      asm volatile("s_waitcnt vmcnt(0)" ::: "memory");
      __syncthreads();
    } else wg::gemm_tile<128>(A, lda, Bt, ldb, nk, C, ldc, par >> (tk == 4 ? 48 : 0), lds, w);
    if (sig && tid < nsig) __hip_atomic_fetch_add(sig + tid * 256, 1u, RLX_AGENT);
  }
}

#undef lane
#undef tid
__device__ __forceinline__ unsigned rtnow() { return (unsigned)__builtin_amdgcn_s_memrealtime(); }
#define RBASE(s) (((s) & 3) * RSLOT + rpl)
constexpr int ALM = RECO + 1024;
__device__ __forceinline__ float rowmax16(float f) { f = fmaxf(f, dpp_ror<8>(f)); f = fmaxf(f, dpp_ror<4>(f)); f = fmaxf(f, dpp_ror<2>(f)); return fmaxf(f, dpp_ror<1>(f)); }
__device__ __forceinline__ void al_eval(unsigned v, unsigned& t) {
  const bool set = (v >> 16) == 1u;
  if (__builtin_expect(((unsigned)__ballot(set) & 0xffffu) == 0, 1)) return;
  const int d = (short)(v - t);
  t += __builtin_amdgcn_readfirstlane((int)rowmax16(set && d > 0 && d < 1000 ? (float)d : 0.f));
}
__device__ __forceinline__ void rec_get(unsigned& recA, unsigned& recD, __amdgpu_buffer_rsrc_t rs, int off, unsigned* tmo) {
#pragma unroll 1
  for (unsigned fails = 0;; ++fails) {
    const v2u rec = __builtin_amdgcn_raw_buffer_load_b64(rs, off, 0, 16);
    recA = __builtin_amdgcn_readfirstlane(rec.x); recD = __builtin_amdgcn_readfirstlane(rec.y);
    if ((recA != 0xffffffffu && recD != 0xffffffffu) || spin_fail(fails, tmo, 1u)) break;
    __builtin_amdgcn_s_sleep(1);
  }
}

#define QK_LOADER \
          "s_nop 4\n\t" \
          "s_add_u32 m0, %[lw], 0\n\ts_xor_b32 %[t], %[kb], 0x0\n\tbuffer_load_dwordx4 %[go], %[rk], %[t] offen" QK_NTk " lds\n\ts_add_u32 m0, %[lw], 1024\n\ts_xor_b32 %[t], %[kb], 0x10000\n\tbuffer_load_dwordx4 %[go], %[rk], %[t] offen" QK_NTk " lds\n\ts_add_u32 m0, %[lw], 2048\n\ts_xor_b32 %[t], %[kb], 0x20000\n\tbuffer_load_dwordx4 %[go], %[rk], %[t] offen" QK_NTk " lds\n\ts_add_u32 m0, %[lw], 3072\n\ts_xor_b32 %[t], %[kb], 0x30000\n\tbuffer_load_dwordx4 %[go], %[rk], %[t] offen" QK_NTk " lds\n\ts_add_u32 m0, %[lw], 4096\n\ts_xor_b32 %[t], %[kb], 0x40000\n\tbuffer_load_dwordx4 %[go], %[rk], %[t] offen" QK_NTk " lds\n\ts_add_u32 m0, %[lw], 5120\n\ts_xor_b32 %[t], %[kb], 0x50000\n\tbuffer_load_dwordx4 %[go], %[rk], %[t] offen" QK_NTk " lds\n\ts_add_u32 m0, %[lw], 6144\n\ts_xor_b32 %[t], %[kb], 0x60000\n\tbuffer_load_dwordx4 %[go], %[rk], %[t] offen" QK_NTk " lds\n\ts_add_u32 m0, %[lw], 7168\n\ts_xor_b32 %[t], %[kb], 0x70000\n\tbuffer_load_dwordx4 %[go], %[rk], %[t] offen" QK_NTk " lds\n\t" \
          "s_add_u32 m0, %[lw], 8192\n\ts_xor_b32 %[t], %[kb], 0x80000\n\tbuffer_load_dwordx4 %[go], %[rk], %[t] offen" QK_NTk " lds\n\ts_add_u32 m0, %[lw], 9216\n\ts_xor_b32 %[t], %[kb], 0x90000\n\tbuffer_load_dwordx4 %[go], %[rk], %[t] offen" QK_NTk " lds\n\ts_add_u32 m0, %[lw], 10240\n\ts_xor_b32 %[t], %[kb], 0xa0000\n\tbuffer_load_dwordx4 %[go], %[rk], %[t] offen" QK_NTk " lds\n\ts_add_u32 m0, %[lw], 11264\n\ts_xor_b32 %[t], %[kb], 0xb0000\n\tbuffer_load_dwordx4 %[go], %[rk], %[t] offen" QK_NTk " lds\n\ts_add_u32 m0, %[lw], 12288\n\ts_xor_b32 %[t], %[kb], 0xc0000\n\tbuffer_load_dwordx4 %[go], %[rk], %[t] offen" QK_NTk " lds\n\ts_add_u32 m0, %[lw], 13312\n\ts_xor_b32 %[t], %[kb], 0xd0000\n\tbuffer_load_dwordx4 %[go], %[rk], %[t] offen" QK_NTk " lds\n\ts_add_u32 m0, %[lw], 14336\n\ts_xor_b32 %[t], %[kb], 0xe0000\n\tbuffer_load_dwordx4 %[go], %[rk], %[t] offen" QK_NTk " lds\n\ts_add_u32 m0, %[lw], 15360\n\ts_xor_b32 %[t], %[kb], 0xf0000\n\tbuffer_load_dwordx4 %[go], %[rk], %[t] offen" QK_NTk " lds\n\t" \
          "s_add_u32 m0, %[lw], 16384\n\ts_xor_b32 %[t], %[kb], 0x100000\n\tbuffer_load_dwordx4 %[go], %[rk], %[t] offen" QK_NTk " lds\n\ts_add_u32 m0, %[lw], 17408\n\ts_xor_b32 %[t], %[kb], 0x110000\n\tbuffer_load_dwordx4 %[go], %[rk], %[t] offen" QK_NTk " lds\n\ts_add_u32 m0, %[lw], 18432\n\ts_xor_b32 %[t], %[kb], 0x120000\n\tbuffer_load_dwordx4 %[go], %[rk], %[t] offen" QK_NTk " lds\n\ts_add_u32 m0, %[lw], 19456\n\ts_xor_b32 %[t], %[kb], 0x130000\n\tbuffer_load_dwordx4 %[go], %[rk], %[t] offen" QK_NTk " lds\n\ts_add_u32 m0, %[lw], 20480\n\ts_xor_b32 %[t], %[kb], 0x140000\n\tbuffer_load_dwordx4 %[go], %[rk], %[t] offen" QK_NTk " lds\n\ts_add_u32 m0, %[lw], 21504\n\ts_xor_b32 %[t], %[kb], 0x150000\n\tbuffer_load_dwordx4 %[go], %[rk], %[t] offen" QK_NTk " lds\n\ts_add_u32 m0, %[lw], 22528\n\ts_xor_b32 %[t], %[kb], 0x160000\n\tbuffer_load_dwordx4 %[go], %[rk], %[t] offen" QK_NTk " lds\n\ts_add_u32 m0, %[lw], 23552\n\ts_xor_b32 %[t], %[kb], 0x170000\n\tbuffer_load_dwordx4 %[go], %[rk], %[t] offen" QK_NTk " lds\n\t" \
          "s_add_u32 m0, %[lw], 24576\n\ts_xor_b32 %[t], %[kb], 0x180000\n\tbuffer_load_dwordx4 %[go], %[rk], %[t] offen" QK_NTk " lds\n\ts_add_u32 m0, %[lw], 25600\n\ts_xor_b32 %[t], %[kb], 0x190000\n\tbuffer_load_dwordx4 %[go], %[rk], %[t] offen" QK_NTk " lds\n\ts_add_u32 m0, %[lw], 26624\n\ts_xor_b32 %[t], %[kb], 0x1a0000\n\tbuffer_load_dwordx4 %[go], %[rk], %[t] offen" QK_NTk " lds\n\ts_add_u32 m0, %[lw], 27648\n\ts_xor_b32 %[t], %[kb], 0x1b0000\n\tbuffer_load_dwordx4 %[go], %[rk], %[t] offen" QK_NTk " lds\n\ts_add_u32 m0, %[lw], 28672\n\ts_xor_b32 %[t], %[kb], 0x1c0000\n\tbuffer_load_dwordx4 %[go], %[rk], %[t] offen" QK_NTk " lds\n\ts_add_u32 m0, %[lw], 29696\n\ts_xor_b32 %[t], %[kb], 0x1d0000\n\tbuffer_load_dwordx4 %[go], %[rk], %[t] offen" QK_NTk " lds\n\ts_add_u32 m0, %[lw], 30720\n\ts_xor_b32 %[t], %[kb], 0x1e0000\n\tbuffer_load_dwordx4 %[go], %[rk], %[t] offen" QK_NTk " lds\n\ts_add_u32 m0, %[lw], 31744\n\ts_xor_b32 %[t], %[kb], 0x1f0000\n\tbuffer_load_dwordx4 %[go], %[rk], %[t] offen" QK_NTk " lds\n\t" \
          "s_add_u32 m0, %[lw], 32768\n\ts_xor_b32 %[t], %[kb], 0x200000\n\tbuffer_load_dwordx4 %[go], %[rk], %[t] offen" QK_NTk " lds\n\ts_add_u32 m0, %[lw], 33792\n\ts_xor_b32 %[t], %[kb], 0x210000\n\tbuffer_load_dwordx4 %[go], %[rk], %[t] offen" QK_NTk " lds\n\ts_add_u32 m0, %[lw], 34816\n\ts_xor_b32 %[t], %[kb], 0x220000\n\tbuffer_load_dwordx4 %[go], %[rk], %[t] offen" QK_NTk " lds\n\ts_add_u32 m0, %[lw], 35840\n\ts_xor_b32 %[t], %[kb], 0x230000\n\tbuffer_load_dwordx4 %[go], %[rk], %[t] offen" QK_NTk " lds\n\ts_add_u32 m0, %[lw], 36864\n\ts_xor_b32 %[t], %[kb], 0x240000\n\tbuffer_load_dwordx4 %[go], %[rk], %[t] offen" QK_NTk " lds\n\ts_add_u32 m0, %[lw], 37888\n\ts_xor_b32 %[t], %[kb], 0x250000\n\tbuffer_load_dwordx4 %[go], %[rk], %[t] offen" QK_NTk " lds\n\ts_add_u32 m0, %[lw], 38912\n\ts_xor_b32 %[t], %[kb], 0x260000\n\tbuffer_load_dwordx4 %[go], %[rk], %[t] offen" QK_NTk " lds\n\ts_add_u32 m0, %[lw], 39936\n\ts_xor_b32 %[t], %[kb], 0x270000\n\tbuffer_load_dwordx4 %[go], %[rk], %[t] offen" QK_NTk " lds\n\t" \
          "s_waitcnt vmcnt(32)\n\tds_read_b64_tr_b16 v[136:137], %[rd0] offset:0\n\tds_read_b64_tr_b16 v[138:139], %[rd0] offset:128\n\tds_read_b64_tr_b16 v[140:141], %[rd0] offset:4096\n\tds_read_b64_tr_b16 v[142:143], %[rd0] offset:4224\n\tds_read_b64_tr_b16 a[64:65], %[rd1] offset:0\n\tds_read_b64_tr_b16 a[66:67], %[rd1] offset:128\n\tds_read_b64_tr_b16 a[68:69], %[rd1] offset:4096\n\tds_read_b64_tr_b16 a[70:71], %[rd1] offset:4224\n\tds_read_b64_tr_b16 a[128:129], %[rd2] offset:0\n\tds_read_b64_tr_b16 a[130:131], %[rd2] offset:128\n\tds_read_b64_tr_b16 a[132:133], %[rd2] offset:4096\n\tds_read_b64_tr_b16 a[134:135], %[rd2] offset:4224\n\tds_read_b64_tr_b16 a[136:137], %[rd3] offset:0\n\tds_read_b64_tr_b16 a[138:139], %[rd3] offset:128\n\tds_read_b64_tr_b16 a[140:141], %[rd3] offset:4096\n\tds_read_b64_tr_b16 a[142:143], %[rd3] offset:4224\n\ts_waitcnt lgkmcnt(0)\n\t" \
          "s_add_u32 m0, %[lw], 0\n\ts_xor_b32 %[t], %[kb], 0x280000\n\tbuffer_load_dwordx4 %[go], %[rk], %[t] offen" QK_NTk " lds\n\ts_add_u32 m0, %[lw], 1024\n\ts_xor_b32 %[t], %[kb], 0x290000\n\tbuffer_load_dwordx4 %[go], %[rk], %[t] offen" QK_NTk " lds\n\ts_add_u32 m0, %[lw], 2048\n\ts_xor_b32 %[t], %[kb], 0x2a0000\n\tbuffer_load_dwordx4 %[go], %[rk], %[t] offen" QK_NTk " lds\n\ts_add_u32 m0, %[lw], 3072\n\ts_xor_b32 %[t], %[kb], 0x2b0000\n\tbuffer_load_dwordx4 %[go], %[rk], %[t] offen" QK_NTk " lds\n\ts_add_u32 m0, %[lw], 4096\n\ts_xor_b32 %[t], %[kb], 0x2c0000\n\tbuffer_load_dwordx4 %[go], %[rk], %[t] offen" QK_NTk " lds\n\ts_add_u32 m0, %[lw], 5120\n\ts_xor_b32 %[t], %[kb], 0x2d0000\n\tbuffer_load_dwordx4 %[go], %[rk], %[t] offen" QK_NTk " lds\n\ts_add_u32 m0, %[lw], 6144\n\ts_xor_b32 %[t], %[kb], 0x2e0000\n\tbuffer_load_dwordx4 %[go], %[rk], %[t] offen" QK_NTk " lds\n\ts_add_u32 m0, %[lw], 7168\n\ts_xor_b32 %[t], %[kb], 0x2f0000\n\tbuffer_load_dwordx4 %[go], %[rk], %[t] offen" QK_NTk " lds\n\t" \
          "s_waitcnt vmcnt(32)\n\tds_read_b64_tr_b16 v[40:41], %[rd0] offset:8192\n\tds_read_b64_tr_b16 v[42:43], %[rd0] offset:8320\n\tds_read_b64_tr_b16 v[44:45], %[rd0] offset:12288\n\tds_read_b64_tr_b16 v[46:47], %[rd0] offset:12416\n\tds_read_b64_tr_b16 a[72:73], %[rd1] offset:8192\n\tds_read_b64_tr_b16 a[74:75], %[rd1] offset:8320\n\tds_read_b64_tr_b16 a[76:77], %[rd1] offset:12288\n\tds_read_b64_tr_b16 a[78:79], %[rd1] offset:12416\n\tds_read_b64_tr_b16 a[144:145], %[rd2] offset:8192\n\tds_read_b64_tr_b16 a[146:147], %[rd2] offset:8320\n\tds_read_b64_tr_b16 a[148:149], %[rd2] offset:12288\n\tds_read_b64_tr_b16 a[150:151], %[rd2] offset:12416\n\tds_read_b64_tr_b16 a[152:153], %[rd3] offset:8192\n\tds_read_b64_tr_b16 a[154:155], %[rd3] offset:8320\n\tds_read_b64_tr_b16 a[156:157], %[rd3] offset:12288\n\tds_read_b64_tr_b16 a[158:159], %[rd3] offset:12416\n\ts_waitcnt lgkmcnt(0)\n\t" \
          "s_add_u32 m0, %[lw], 8192\n\ts_xor_b32 %[t], %[kb], 0x300000\n\tbuffer_load_dwordx4 %[go], %[rk], %[t] offen" QK_NTk " lds\n\ts_add_u32 m0, %[lw], 9216\n\ts_xor_b32 %[t], %[kb], 0x310000\n\tbuffer_load_dwordx4 %[go], %[rk], %[t] offen" QK_NTk " lds\n\ts_add_u32 m0, %[lw], 10240\n\ts_xor_b32 %[t], %[kb], 0x320000\n\tbuffer_load_dwordx4 %[go], %[rk], %[t] offen" QK_NTk " lds\n\ts_add_u32 m0, %[lw], 11264\n\ts_xor_b32 %[t], %[kb], 0x330000\n\tbuffer_load_dwordx4 %[go], %[rk], %[t] offen" QK_NTk " lds\n\ts_add_u32 m0, %[lw], 12288\n\ts_xor_b32 %[t], %[kb], 0x340000\n\tbuffer_load_dwordx4 %[go], %[rk], %[t] offen" QK_NTk " lds\n\ts_add_u32 m0, %[lw], 13312\n\ts_xor_b32 %[t], %[kb], 0x350000\n\tbuffer_load_dwordx4 %[go], %[rk], %[t] offen" QK_NTk " lds\n\ts_add_u32 m0, %[lw], 14336\n\ts_xor_b32 %[t], %[kb], 0x360000\n\tbuffer_load_dwordx4 %[go], %[rk], %[t] offen" QK_NTk " lds\n\ts_add_u32 m0, %[lw], 15360\n\ts_xor_b32 %[t], %[kb], 0x370000\n\tbuffer_load_dwordx4 %[go], %[rk], %[t] offen" QK_NTk " lds\n\t" \
          "s_waitcnt vmcnt(32)\n\tds_read_b64_tr_b16 v[144:145], %[rd0] offset:16384\n\tds_read_b64_tr_b16 v[146:147], %[rd0] offset:16512\n\tds_read_b64_tr_b16 v[148:149], %[rd0] offset:20480\n\tds_read_b64_tr_b16 v[150:151], %[rd0] offset:20608\n\tds_read_b64_tr_b16 a[80:81], %[rd1] offset:16384\n\tds_read_b64_tr_b16 a[82:83], %[rd1] offset:16512\n\tds_read_b64_tr_b16 a[84:85], %[rd1] offset:20480\n\tds_read_b64_tr_b16 a[86:87], %[rd1] offset:20608\n\tds_read_b64_tr_b16 a[160:161], %[rd2] offset:16384\n\tds_read_b64_tr_b16 a[162:163], %[rd2] offset:16512\n\tds_read_b64_tr_b16 a[164:165], %[rd2] offset:20480\n\tds_read_b64_tr_b16 a[166:167], %[rd2] offset:20608\n\tds_read_b64_tr_b16 a[168:169], %[rd3] offset:16384\n\tds_read_b64_tr_b16 a[170:171], %[rd3] offset:16512\n\tds_read_b64_tr_b16 a[172:173], %[rd3] offset:20480\n\tds_read_b64_tr_b16 a[174:175], %[rd3] offset:20608\n\ts_waitcnt lgkmcnt(0)\n\t" \
          "s_add_u32 m0, %[lw], 16384\n\ts_xor_b32 %[t], %[kb], 0x380000\n\tbuffer_load_dwordx4 %[go], %[rk], %[t] offen" QK_NTk " lds\n\ts_add_u32 m0, %[lw], 17408\n\ts_xor_b32 %[t], %[kb], 0x390000\n\tbuffer_load_dwordx4 %[go], %[rk], %[t] offen" QK_NTk " lds\n\ts_add_u32 m0, %[lw], 18432\n\ts_xor_b32 %[t], %[kb], 0x3a0000\n\tbuffer_load_dwordx4 %[go], %[rk], %[t] offen" QK_NTk " lds\n\ts_add_u32 m0, %[lw], 19456\n\ts_xor_b32 %[t], %[kb], 0x3b0000\n\tbuffer_load_dwordx4 %[go], %[rk], %[t] offen" QK_NTk " lds\n\ts_add_u32 m0, %[lw], 20480\n\ts_xor_b32 %[t], %[kb], 0x3c0000\n\tbuffer_load_dwordx4 %[go], %[rk], %[t] offen" QK_NTk " lds\n\ts_add_u32 m0, %[lw], 21504\n\ts_xor_b32 %[t], %[kb], 0x3d0000\n\tbuffer_load_dwordx4 %[go], %[rk], %[t] offen" QK_NTk " lds\n\ts_add_u32 m0, %[lw], 22528\n\ts_xor_b32 %[t], %[kb], 0x3e0000\n\tbuffer_load_dwordx4 %[go], %[rk], %[t] offen" QK_NTk " lds\n\ts_add_u32 m0, %[lw], 23552\n\ts_xor_b32 %[t], %[kb], 0x3f0000\n\tbuffer_load_dwordx4 %[go], %[rk], %[t] offen" QK_NTk " lds\n\t" \
          "s_waitcnt vmcnt(32)\n\tds_read_b64_tr_b16 v[80:81], %[rd0] offset:24576\n\tds_read_b64_tr_b16 v[82:83], %[rd0] offset:24704\n\tds_read_b64_tr_b16 v[84:85], %[rd0] offset:28672\n\tds_read_b64_tr_b16 v[86:87], %[rd0] offset:28800\n\tds_read_b64_tr_b16 a[88:89], %[rd1] offset:24576\n\tds_read_b64_tr_b16 a[90:91], %[rd1] offset:24704\n\tds_read_b64_tr_b16 a[92:93], %[rd1] offset:28672\n\tds_read_b64_tr_b16 a[94:95], %[rd1] offset:28800\n\tds_read_b64_tr_b16 a[176:177], %[rd2] offset:24576\n\tds_read_b64_tr_b16 a[178:179], %[rd2] offset:24704\n\tds_read_b64_tr_b16 a[180:181], %[rd2] offset:28672\n\tds_read_b64_tr_b16 a[182:183], %[rd2] offset:28800\n\tds_read_b64_tr_b16 a[184:185], %[rd3] offset:24576\n\tds_read_b64_tr_b16 a[186:187], %[rd3] offset:24704\n\tds_read_b64_tr_b16 a[188:189], %[rd3] offset:28672\n\tds_read_b64_tr_b16 a[190:191], %[rd3] offset:28800\n\ts_waitcnt lgkmcnt(0)\n\t" \
          "s_add_u32 m0, %[lw], 24576\n\ts_xor_b32 %[t], %[kb], 0x380000\n\tbuffer_load_dwordx4 %[go], %[rq], %[t] offen" QK_NTq " lds\n\ts_add_u32 m0, %[lw], 25600\n\ts_xor_b32 %[t], %[kb], 0x390000\n\tbuffer_load_dwordx4 %[go], %[rq], %[t] offen" QK_NTq " lds\n\ts_add_u32 m0, %[lw], 26624\n\ts_xor_b32 %[t], %[kb], 0x3a0000\n\tbuffer_load_dwordx4 %[go], %[rq], %[t] offen" QK_NTq " lds\n\ts_add_u32 m0, %[lw], 27648\n\ts_xor_b32 %[t], %[kb], 0x3b0000\n\tbuffer_load_dwordx4 %[go], %[rq], %[t] offen" QK_NTq " lds\n\ts_add_u32 m0, %[lw], 28672\n\ts_xor_b32 %[t], %[kb], 0x3c0000\n\tbuffer_load_dwordx4 %[go], %[rq], %[t] offen" QK_NTq " lds\n\ts_add_u32 m0, %[lw], 29696\n\ts_xor_b32 %[t], %[kb], 0x3d0000\n\tbuffer_load_dwordx4 %[go], %[rq], %[t] offen" QK_NTq " lds\n\ts_add_u32 m0, %[lw], 30720\n\ts_xor_b32 %[t], %[kb], 0x3e0000\n\tbuffer_load_dwordx4 %[go], %[rq], %[t] offen" QK_NTq " lds\n\ts_add_u32 m0, %[lw], 31744\n\ts_xor_b32 %[t], %[kb], 0x3f0000\n\tbuffer_load_dwordx4 %[go], %[rq], %[t] offen" QK_NTq " lds\n\t" \
          "s_waitcnt vmcnt(32)\n\tds_read_b64_tr_b16 v[152:153], %[rd0] offset:32768\n\tds_read_b64_tr_b16 v[154:155], %[rd0] offset:32896\n\tds_read_b64_tr_b16 v[156:157], %[rd0] offset:36864\n\tds_read_b64_tr_b16 v[158:159], %[rd0] offset:36992\n\tds_read_b64_tr_b16 a[96:97], %[rd1] offset:32768\n\tds_read_b64_tr_b16 a[98:99], %[rd1] offset:32896\n\tds_read_b64_tr_b16 a[100:101], %[rd1] offset:36864\n\tds_read_b64_tr_b16 a[102:103], %[rd1] offset:36992\n\tds_read_b64_tr_b16 a[192:193], %[rd2] offset:32768\n\tds_read_b64_tr_b16 a[194:195], %[rd2] offset:32896\n\tds_read_b64_tr_b16 a[196:197], %[rd2] offset:36864\n\tds_read_b64_tr_b16 a[198:199], %[rd2] offset:36992\n\tds_read_b64_tr_b16 a[200:201], %[rd3] offset:32768\n\tds_read_b64_tr_b16 a[202:203], %[rd3] offset:32896\n\tds_read_b64_tr_b16 a[204:205], %[rd3] offset:36864\n\tds_read_b64_tr_b16 a[206:207], %[rd3] offset:36992\n\ts_waitcnt lgkmcnt(0)\n\t" \
          "s_add_u32 m0, %[lw], 32768\n\ts_xor_b32 %[t], %[kb], 0x300000\n\tbuffer_load_dwordx4 %[go], %[rq], %[t] offen" QK_NTq " lds\n\ts_add_u32 m0, %[lw], 33792\n\ts_xor_b32 %[t], %[kb], 0x310000\n\tbuffer_load_dwordx4 %[go], %[rq], %[t] offen" QK_NTq " lds\n\ts_add_u32 m0, %[lw], 34816\n\ts_xor_b32 %[t], %[kb], 0x320000\n\tbuffer_load_dwordx4 %[go], %[rq], %[t] offen" QK_NTq " lds\n\ts_add_u32 m0, %[lw], 35840\n\ts_xor_b32 %[t], %[kb], 0x330000\n\tbuffer_load_dwordx4 %[go], %[rq], %[t] offen" QK_NTq " lds\n\ts_add_u32 m0, %[lw], 36864\n\ts_xor_b32 %[t], %[kb], 0x340000\n\tbuffer_load_dwordx4 %[go], %[rq], %[t] offen" QK_NTq " lds\n\ts_add_u32 m0, %[lw], 37888\n\ts_xor_b32 %[t], %[kb], 0x350000\n\tbuffer_load_dwordx4 %[go], %[rq], %[t] offen" QK_NTq " lds\n\ts_add_u32 m0, %[lw], 38912\n\ts_xor_b32 %[t], %[kb], 0x360000\n\tbuffer_load_dwordx4 %[go], %[rq], %[t] offen" QK_NTq " lds\n\ts_add_u32 m0, %[lw], 39936\n\ts_xor_b32 %[t], %[kb], 0x370000\n\tbuffer_load_dwordx4 %[go], %[rq], %[t] offen" QK_NTq " lds\n\t" \
          "s_waitcnt vmcnt(32)\n\tds_read_b64_tr_b16 v[120:121], %[rd0] offset:0\n\tds_read_b64_tr_b16 v[122:123], %[rd0] offset:128\n\tds_read_b64_tr_b16 v[124:125], %[rd0] offset:4096\n\tds_read_b64_tr_b16 v[126:127], %[rd0] offset:4224\n\tds_read_b64_tr_b16 a[104:105], %[rd1] offset:0\n\tds_read_b64_tr_b16 a[106:107], %[rd1] offset:128\n\tds_read_b64_tr_b16 a[108:109], %[rd1] offset:4096\n\tds_read_b64_tr_b16 a[110:111], %[rd1] offset:4224\n\tds_read_b64_tr_b16 a[208:209], %[rd2] offset:0\n\tds_read_b64_tr_b16 a[210:211], %[rd2] offset:128\n\tds_read_b64_tr_b16 a[212:213], %[rd2] offset:4096\n\tds_read_b64_tr_b16 a[214:215], %[rd2] offset:4224\n\tds_read_b64_tr_b16 a[216:217], %[rd3] offset:0\n\tds_read_b64_tr_b16 a[218:219], %[rd3] offset:128\n\tds_read_b64_tr_b16 a[220:221], %[rd3] offset:4096\n\tds_read_b64_tr_b16 a[222:223], %[rd3] offset:4224\n\ts_waitcnt lgkmcnt(0)\n\t" \
          "s_add_u32 m0, %[lw], 0\n\ts_xor_b32 %[t], %[kb], 0x280000\n\tbuffer_load_dwordx4 %[go], %[rq], %[t] offen" QK_NTq " lds\n\ts_add_u32 m0, %[lw], 1024\n\ts_xor_b32 %[t], %[kb], 0x290000\n\tbuffer_load_dwordx4 %[go], %[rq], %[t] offen" QK_NTq " lds\n\ts_add_u32 m0, %[lw], 2048\n\ts_xor_b32 %[t], %[kb], 0x2a0000\n\tbuffer_load_dwordx4 %[go], %[rq], %[t] offen" QK_NTq " lds\n\ts_add_u32 m0, %[lw], 3072\n\ts_xor_b32 %[t], %[kb], 0x2b0000\n\tbuffer_load_dwordx4 %[go], %[rq], %[t] offen" QK_NTq " lds\n\ts_add_u32 m0, %[lw], 4096\n\ts_xor_b32 %[t], %[kb], 0x2c0000\n\tbuffer_load_dwordx4 %[go], %[rq], %[t] offen" QK_NTq " lds\n\ts_add_u32 m0, %[lw], 5120\n\ts_xor_b32 %[t], %[kb], 0x2d0000\n\tbuffer_load_dwordx4 %[go], %[rq], %[t] offen" QK_NTq " lds\n\ts_add_u32 m0, %[lw], 6144\n\ts_xor_b32 %[t], %[kb], 0x2e0000\n\tbuffer_load_dwordx4 %[go], %[rq], %[t] offen" QK_NTq " lds\n\ts_add_u32 m0, %[lw], 7168\n\ts_xor_b32 %[t], %[kb], 0x2f0000\n\tbuffer_load_dwordx4 %[go], %[rq], %[t] offen" QK_NTq " lds\n\t" \
          "s_waitcnt vmcnt(32)\n\tds_read_b64_tr_b16 a[16:17], %[rd0] offset:8192\n\tds_read_b64_tr_b16 a[18:19], %[rd0] offset:8320\n\tds_read_b64_tr_b16 a[20:21], %[rd0] offset:12288\n\tds_read_b64_tr_b16 a[22:23], %[rd0] offset:12416\n\tds_read_b64_tr_b16 a[112:113], %[rd1] offset:8192\n\tds_read_b64_tr_b16 a[114:115], %[rd1] offset:8320\n\tds_read_b64_tr_b16 a[116:117], %[rd1] offset:12288\n\tds_read_b64_tr_b16 a[118:119], %[rd1] offset:12416\n\tds_read_b64_tr_b16 a[224:225], %[rd2] offset:8192\n\tds_read_b64_tr_b16 a[226:227], %[rd2] offset:8320\n\tds_read_b64_tr_b16 a[228:229], %[rd2] offset:12288\n\tds_read_b64_tr_b16 a[230:231], %[rd2] offset:12416\n\tds_read_b64_tr_b16 a[232:233], %[rd3] offset:8192\n\tds_read_b64_tr_b16 a[234:235], %[rd3] offset:8320\n\tds_read_b64_tr_b16 a[236:237], %[rd3] offset:12288\n\tds_read_b64_tr_b16 a[238:239], %[rd3] offset:12416\n\ts_waitcnt lgkmcnt(0)\n\t" \
          "s_add_u32 m0, %[lw], 8192\n\ts_xor_b32 %[t], %[kb], 0x200000\n\tbuffer_load_dwordx4 %[go], %[rq], %[t] offen" QK_NTq " lds\n\ts_add_u32 m0, %[lw], 9216\n\ts_xor_b32 %[t], %[kb], 0x210000\n\tbuffer_load_dwordx4 %[go], %[rq], %[t] offen" QK_NTq " lds\n\ts_add_u32 m0, %[lw], 10240\n\ts_xor_b32 %[t], %[kb], 0x220000\n\tbuffer_load_dwordx4 %[go], %[rq], %[t] offen" QK_NTq " lds\n\ts_add_u32 m0, %[lw], 11264\n\ts_xor_b32 %[t], %[kb], 0x230000\n\tbuffer_load_dwordx4 %[go], %[rq], %[t] offen" QK_NTq " lds\n\ts_add_u32 m0, %[lw], 12288\n\ts_xor_b32 %[t], %[kb], 0x240000\n\tbuffer_load_dwordx4 %[go], %[rq], %[t] offen" QK_NTq " lds\n\ts_add_u32 m0, %[lw], 13312\n\ts_xor_b32 %[t], %[kb], 0x250000\n\tbuffer_load_dwordx4 %[go], %[rq], %[t] offen" QK_NTq " lds\n\ts_add_u32 m0, %[lw], 14336\n\ts_xor_b32 %[t], %[kb], 0x260000\n\tbuffer_load_dwordx4 %[go], %[rq], %[t] offen" QK_NTq " lds\n\ts_add_u32 m0, %[lw], 15360\n\ts_xor_b32 %[t], %[kb], 0x270000\n\tbuffer_load_dwordx4 %[go], %[rq], %[t] offen" QK_NTq " lds\n\t" \
          "s_waitcnt vmcnt(32)\n\tds_read_b64_tr_b16 a[56:57], %[rd0] offset:16384\n\tds_read_b64_tr_b16 a[58:59], %[rd0] offset:16512\n\tds_read_b64_tr_b16 a[60:61], %[rd0] offset:20480\n\tds_read_b64_tr_b16 a[62:63], %[rd0] offset:20608\n\tds_read_b64_tr_b16 a[120:121], %[rd1] offset:16384\n\tds_read_b64_tr_b16 a[122:123], %[rd1] offset:16512\n\tds_read_b64_tr_b16 a[124:125], %[rd1] offset:20480\n\tds_read_b64_tr_b16 a[126:127], %[rd1] offset:20608\n\tds_read_b64_tr_b16 a[240:241], %[rd2] offset:16384\n\tds_read_b64_tr_b16 a[242:243], %[rd2] offset:16512\n\tds_read_b64_tr_b16 a[244:245], %[rd2] offset:20480\n\tds_read_b64_tr_b16 a[246:247], %[rd2] offset:20608\n\tds_read_b64_tr_b16 a[248:249], %[rd3] offset:16384\n\tds_read_b64_tr_b16 a[250:251], %[rd3] offset:16512\n\tds_read_b64_tr_b16 a[252:253], %[rd3] offset:20480\n\tds_read_b64_tr_b16 a[254:255], %[rd3] offset:20608\n\ts_waitcnt lgkmcnt(0)\n\t" \
          "s_add_u32 m0, %[lw], 16384\n\ts_xor_b32 %[t], %[kb], 0x180000\n\tbuffer_load_dwordx4 %[go], %[rq], %[t] offen" QK_NTq " lds\n\ts_add_u32 m0, %[lw], 17408\n\ts_xor_b32 %[t], %[kb], 0x190000\n\tbuffer_load_dwordx4 %[go], %[rq], %[t] offen" QK_NTq " lds\n\ts_add_u32 m0, %[lw], 18432\n\ts_xor_b32 %[t], %[kb], 0x1a0000\n\tbuffer_load_dwordx4 %[go], %[rq], %[t] offen" QK_NTq " lds\n\ts_add_u32 m0, %[lw], 19456\n\ts_xor_b32 %[t], %[kb], 0x1b0000\n\tbuffer_load_dwordx4 %[go], %[rq], %[t] offen" QK_NTq " lds\n\ts_add_u32 m0, %[lw], 20480\n\ts_xor_b32 %[t], %[kb], 0x1c0000\n\tbuffer_load_dwordx4 %[go], %[rq], %[t] offen" QK_NTq " lds\n\ts_add_u32 m0, %[lw], 21504\n\ts_xor_b32 %[t], %[kb], 0x1d0000\n\tbuffer_load_dwordx4 %[go], %[rq], %[t] offen" QK_NTq " lds\n\ts_add_u32 m0, %[lw], 22528\n\ts_xor_b32 %[t], %[kb], 0x1e0000\n\tbuffer_load_dwordx4 %[go], %[rq], %[t] offen" QK_NTq " lds\n\ts_add_u32 m0, %[lw], 23552\n\ts_xor_b32 %[t], %[kb], 0x1f0000\n\tbuffer_load_dwordx4 %[go], %[rq], %[t] offen" QK_NTq " lds\n\t" \
          "s_waitcnt vmcnt(32)\n\tds_read_b64_tr_b16 a[24:25], %[rd0] offset:24576\n\tds_read_b64_tr_b16 a[26:27], %[rd0] offset:24704\n\tds_read_b64_tr_b16 a[28:29], %[rd0] offset:28672\n\tds_read_b64_tr_b16 a[30:31], %[rd0] offset:28800\n\tds_read_b64_tr_b16 a[32:33], %[rd1] offset:24576\n\tds_read_b64_tr_b16 a[34:35], %[rd1] offset:24704\n\tds_read_b64_tr_b16 a[36:37], %[rd1] offset:28672\n\tds_read_b64_tr_b16 a[38:39], %[rd1] offset:28800\n\tds_read_b64_tr_b16 a[40:41], %[rd2] offset:24576\n\tds_read_b64_tr_b16 a[42:43], %[rd2] offset:24704\n\tds_read_b64_tr_b16 a[44:45], %[rd2] offset:28672\n\tds_read_b64_tr_b16 a[46:47], %[rd2] offset:28800\n\tds_read_b64_tr_b16 a[48:49], %[rd3] offset:24576\n\tds_read_b64_tr_b16 a[50:51], %[rd3] offset:24704\n\tds_read_b64_tr_b16 a[52:53], %[rd3] offset:28672\n\tds_read_b64_tr_b16 a[54:55], %[rd3] offset:28800\n\ts_waitcnt lgkmcnt(0)\n\t" \
          "s_add_u32 m0, %[lw], 24576\n\ts_xor_b32 %[t], %[kb], 0x100000\n\tbuffer_load_dwordx4 %[go], %[rq], %[t] offen" QK_NTq " lds\n\ts_add_u32 m0, %[lw], 25600\n\ts_xor_b32 %[t], %[kb], 0x110000\n\tbuffer_load_dwordx4 %[go], %[rq], %[t] offen" QK_NTq " lds\n\ts_add_u32 m0, %[lw], 26624\n\ts_xor_b32 %[t], %[kb], 0x120000\n\tbuffer_load_dwordx4 %[go], %[rq], %[t] offen" QK_NTq " lds\n\ts_add_u32 m0, %[lw], 27648\n\ts_xor_b32 %[t], %[kb], 0x130000\n\tbuffer_load_dwordx4 %[go], %[rq], %[t] offen" QK_NTq " lds\n\ts_add_u32 m0, %[lw], 28672\n\ts_xor_b32 %[t], %[kb], 0x140000\n\tbuffer_load_dwordx4 %[go], %[rq], %[t] offen" QK_NTq " lds\n\ts_add_u32 m0, %[lw], 29696\n\ts_xor_b32 %[t], %[kb], 0x150000\n\tbuffer_load_dwordx4 %[go], %[rq], %[t] offen" QK_NTq " lds\n\ts_add_u32 m0, %[lw], 30720\n\ts_xor_b32 %[t], %[kb], 0x160000\n\tbuffer_load_dwordx4 %[go], %[rq], %[t] offen" QK_NTq " lds\n\ts_add_u32 m0, %[lw], 31744\n\ts_xor_b32 %[t], %[kb], 0x170000\n\tbuffer_load_dwordx4 %[go], %[rq], %[t] offen" QK_NTq " lds\n\t" \
          "s_waitcnt vmcnt(32)\n\tds_read_b64_tr_b16 v[160:161], %[rd0] offset:32768\n\tds_read_b64_tr_b16 v[162:163], %[rd0] offset:32896\n\tds_read_b64_tr_b16 v[164:165], %[rd0] offset:36864\n\tds_read_b64_tr_b16 v[166:167], %[rd0] offset:36992\n\tds_read_b64_tr_b16 v[128:129], %[rd1] offset:32768\n\tds_read_b64_tr_b16 v[130:131], %[rd1] offset:32896\n\tds_read_b64_tr_b16 v[132:133], %[rd1] offset:36864\n\tds_read_b64_tr_b16 v[134:135], %[rd1] offset:36992\n\tds_read_b64_tr_b16 a[0:1], %[rd2] offset:32768\n\tds_read_b64_tr_b16 a[2:3], %[rd2] offset:32896\n\tds_read_b64_tr_b16 a[4:5], %[rd2] offset:36864\n\tds_read_b64_tr_b16 a[6:7], %[rd2] offset:36992\n\tds_read_b64_tr_b16 a[8:9], %[rd3] offset:32768\n\tds_read_b64_tr_b16 a[10:11], %[rd3] offset:32896\n\tds_read_b64_tr_b16 a[12:13], %[rd3] offset:36864\n\tds_read_b64_tr_b16 a[14:15], %[rd3] offset:36992\n\ts_waitcnt lgkmcnt(0)\n\t" \
          "s_add_u32 m0, %[lw], 32768\n\ts_xor_b32 %[t], %[kb], 0x80000\n\tbuffer_load_dwordx4 %[go], %[rq], %[t] offen" QK_NTq " lds\n\ts_add_u32 m0, %[lw], 33792\n\ts_xor_b32 %[t], %[kb], 0x90000\n\tbuffer_load_dwordx4 %[go], %[rq], %[t] offen" QK_NTq " lds\n\ts_add_u32 m0, %[lw], 34816\n\ts_xor_b32 %[t], %[kb], 0xa0000\n\tbuffer_load_dwordx4 %[go], %[rq], %[t] offen" QK_NTq " lds\n\ts_add_u32 m0, %[lw], 35840\n\ts_xor_b32 %[t], %[kb], 0xb0000\n\tbuffer_load_dwordx4 %[go], %[rq], %[t] offen" QK_NTq " lds\n\ts_add_u32 m0, %[lw], 36864\n\ts_xor_b32 %[t], %[kb], 0xc0000\n\tbuffer_load_dwordx4 %[go], %[rq], %[t] offen" QK_NTq " lds\n\ts_add_u32 m0, %[lw], 37888\n\ts_xor_b32 %[t], %[kb], 0xd0000\n\tbuffer_load_dwordx4 %[go], %[rq], %[t] offen" QK_NTq " lds\n\ts_add_u32 m0, %[lw], 38912\n\ts_xor_b32 %[t], %[kb], 0xe0000\n\tbuffer_load_dwordx4 %[go], %[rq], %[t] offen" QK_NTq " lds\n\ts_add_u32 m0, %[lw], 39936\n\ts_xor_b32 %[t], %[kb], 0xf0000\n\tbuffer_load_dwordx4 %[go], %[rq], %[t] offen" QK_NTq " lds\n\t" \
          "s_waitcnt vmcnt(32)\n\tds_read_b64_tr_b16 v[104:105], %[rd0] offset:0\n\tds_read_b64_tr_b16 v[106:107], %[rd0] offset:128\n\tds_read_b64_tr_b16 v[108:109], %[rd0] offset:4096\n\tds_read_b64_tr_b16 v[110:111], %[rd0] offset:4224\n\tds_read_b64_tr_b16 v[168:169], %[rd1] offset:0\n\tds_read_b64_tr_b16 v[170:171], %[rd1] offset:128\n\tds_read_b64_tr_b16 v[172:173], %[rd1] offset:4096\n\tds_read_b64_tr_b16 v[174:175], %[rd1] offset:4224\n\tds_read_b64_tr_b16 v[112:113], %[rd2] offset:0\n\tds_read_b64_tr_b16 v[114:115], %[rd2] offset:128\n\tds_read_b64_tr_b16 v[116:117], %[rd2] offset:4096\n\tds_read_b64_tr_b16 v[118:119], %[rd2] offset:4224\n\tds_read_b64_tr_b16 v[176:177], %[rd3] offset:0\n\tds_read_b64_tr_b16 v[178:179], %[rd3] offset:128\n\tds_read_b64_tr_b16 v[180:181], %[rd3] offset:4096\n\tds_read_b64_tr_b16 v[182:183], %[rd3] offset:4224\n\ts_waitcnt lgkmcnt(0)\n\t" \
          "buffer_load_dwordx4 %[rec], %[ro], %[rr], 0 offen sc1\n\t" \
          "s_add_u32 m0, %[lw], 0\n\ts_xor_b32 %[t], %[kb], 0x0\n\tbuffer_load_dwordx4 %[go], %[rq], %[t] offen" QK_NTq " lds\n\ts_add_u32 m0, %[lw], 1024\n\ts_xor_b32 %[t], %[kb], 0x10000\n\tbuffer_load_dwordx4 %[go], %[rq], %[t] offen" QK_NTq " lds\n\ts_add_u32 m0, %[lw], 2048\n\ts_xor_b32 %[t], %[kb], 0x20000\n\tbuffer_load_dwordx4 %[go], %[rq], %[t] offen" QK_NTq " lds\n\ts_add_u32 m0, %[lw], 3072\n\ts_xor_b32 %[t], %[kb], 0x30000\n\tbuffer_load_dwordx4 %[go], %[rq], %[t] offen" QK_NTq " lds\n\ts_add_u32 m0, %[lw], 4096\n\ts_xor_b32 %[t], %[kb], 0x40000\n\tbuffer_load_dwordx4 %[go], %[rq], %[t] offen" QK_NTq " lds\n\ts_add_u32 m0, %[lw], 5120\n\ts_xor_b32 %[t], %[kb], 0x50000\n\tbuffer_load_dwordx4 %[go], %[rq], %[t] offen" QK_NTq " lds\n\ts_add_u32 m0, %[lw], 6144\n\ts_xor_b32 %[t], %[kb], 0x60000\n\tbuffer_load_dwordx4 %[go], %[rq], %[t] offen" QK_NTq " lds\n\ts_add_u32 m0, %[lw], 7168\n\ts_xor_b32 %[t], %[kb], 0x70000\n\tbuffer_load_dwordx4 %[go], %[rq], %[t] offen" QK_NTq " lds\n\t" \
          "s_waitcnt vmcnt(33)\n\tds_read_b64_tr_b16 v[184:185], %[rd0] offset:8192\n\tds_read_b64_tr_b16 v[186:187], %[rd0] offset:8320\n\tds_read_b64_tr_b16 v[188:189], %[rd0] offset:12288\n\tds_read_b64_tr_b16 v[190:191], %[rd0] offset:12416\n\tds_read_b64_tr_b16 v[88:89], %[rd1] offset:8192\n\tds_read_b64_tr_b16 v[90:91], %[rd1] offset:8320\n\tds_read_b64_tr_b16 v[92:93], %[rd1] offset:12288\n\tds_read_b64_tr_b16 v[94:95], %[rd1] offset:12416\n\tds_read_b64_tr_b16 v[192:193], %[rd2] offset:8192\n\tds_read_b64_tr_b16 v[194:195], %[rd2] offset:8320\n\tds_read_b64_tr_b16 v[196:197], %[rd2] offset:12288\n\tds_read_b64_tr_b16 v[198:199], %[rd2] offset:12416\n\tds_read_b64_tr_b16 v[96:97], %[rd3] offset:8192\n\tds_read_b64_tr_b16 v[98:99], %[rd3] offset:8320\n\tds_read_b64_tr_b16 v[100:101], %[rd3] offset:12288\n\tds_read_b64_tr_b16 v[102:103], %[rd3] offset:12416\n\ts_waitcnt lgkmcnt(0)\n\tds_write_b128 %[own], v[136:139] offset:8192\n\tds_write_b128 %[own], v[140:143] offset:9216\n\tds_write_b128 %[own], v[144:147] offset:10240\n\tds_write_b128 %[own], v[148:151] offset:11264\n\tds_write_b128 %[own], v[152:155] offset:12288\n\tds_write_b128 %[own], v[156:159] offset:13312\n\tds_write_b128 %[own], v[160:163] offset:14336\n\tds_write_b128 %[own], v[164:167] offset:15360\n\ts_waitcnt lgkmcnt(0)\n\t" \
          "s_waitcnt vmcnt(25)\n\tds_read_b64_tr_b16 v[64:65], %[rd0] offset:16384\n\tds_read_b64_tr_b16 v[66:67], %[rd0] offset:16512\n\tds_read_b64_tr_b16 v[68:69], %[rd0] offset:20480\n\tds_read_b64_tr_b16 v[70:71], %[rd0] offset:20608\n\tds_read_b64_tr_b16 v[200:201], %[rd1] offset:16384\n\tds_read_b64_tr_b16 v[202:203], %[rd1] offset:16512\n\tds_read_b64_tr_b16 v[204:205], %[rd1] offset:20480\n\tds_read_b64_tr_b16 v[206:207], %[rd1] offset:20608\n\tds_read_b64_tr_b16 v[72:73], %[rd2] offset:16384\n\tds_read_b64_tr_b16 v[74:75], %[rd2] offset:16512\n\tds_read_b64_tr_b16 v[76:77], %[rd2] offset:20480\n\tds_read_b64_tr_b16 v[78:79], %[rd2] offset:20608\n\tds_read_b64_tr_b16 v[208:209], %[rd3] offset:16384\n\tds_read_b64_tr_b16 v[210:211], %[rd3] offset:16512\n\tds_read_b64_tr_b16 v[212:213], %[rd3] offset:20480\n\tds_read_b64_tr_b16 v[214:215], %[rd3] offset:20608\n\ts_waitcnt lgkmcnt(0)\n\tds_write_b128 %[own], v[168:171] offset:16384\n\tds_write_b128 %[own], v[172:175] offset:17408\n\tds_write_b128 %[own], v[176:179] offset:18432\n\tds_write_b128 %[own], v[180:183] offset:19456\n\tds_write_b128 %[own], v[184:187] offset:20480\n\tds_write_b128 %[own], v[188:191] offset:21504\n\tds_write_b128 %[own], v[192:195] offset:22528\n\tds_write_b128 %[own], v[196:199] offset:23552\n\ts_waitcnt lgkmcnt(0)\n\t" \
          "s_waitcnt vmcnt(17)\n\tds_read_b64_tr_b16 v[216:217], %[rd0] offset:24576\n\tds_read_b64_tr_b16 v[218:219], %[rd0] offset:24704\n\tds_read_b64_tr_b16 v[220:221], %[rd0] offset:28672\n\tds_read_b64_tr_b16 v[222:223], %[rd0] offset:28800\n\tds_read_b64_tr_b16 v[48:49], %[rd1] offset:24576\n\tds_read_b64_tr_b16 v[50:51], %[rd1] offset:24704\n\tds_read_b64_tr_b16 v[52:53], %[rd1] offset:28672\n\tds_read_b64_tr_b16 v[54:55], %[rd1] offset:28800\n\tds_read_b64_tr_b16 v[224:225], %[rd2] offset:24576\n\tds_read_b64_tr_b16 v[226:227], %[rd2] offset:24704\n\tds_read_b64_tr_b16 v[228:229], %[rd2] offset:28672\n\tds_read_b64_tr_b16 v[230:231], %[rd2] offset:28800\n\tds_read_b64_tr_b16 v[56:57], %[rd3] offset:24576\n\tds_read_b64_tr_b16 v[58:59], %[rd3] offset:24704\n\tds_read_b64_tr_b16 v[60:61], %[rd3] offset:28672\n\tds_read_b64_tr_b16 v[62:63], %[rd3] offset:28800\n\ts_waitcnt lgkmcnt(0)\n\tds_write_b128 %[own], v[200:203] offset:24576\n\tds_write_b128 %[own], v[204:207] offset:25600\n\tds_write_b128 %[own], v[208:211] offset:26624\n\tds_write_b128 %[own], v[212:215] offset:27648\n\tds_write_b128 %[own], v[216:219] offset:28672\n\tds_write_b128 %[own], v[220:223] offset:29696\n\tds_write_b128 %[own], v[224:227] offset:30720\n\tds_write_b128 %[own], v[228:231] offset:31744\n\ts_waitcnt lgkmcnt(0)\n\t" \
          "s_waitcnt vmcnt(8)\n\tds_read_b64_tr_b16 v[24:25], %[rd0] offset:32768\n\tds_read_b64_tr_b16 v[26:27], %[rd0] offset:32896\n\tds_read_b64_tr_b16 v[28:29], %[rd0] offset:36864\n\tds_read_b64_tr_b16 v[30:31], %[rd0] offset:36992\n\tds_read_b64_tr_b16 v[136:137], %[rd1] offset:32768\n\tds_read_b64_tr_b16 v[138:139], %[rd1] offset:32896\n\tds_read_b64_tr_b16 v[140:141], %[rd1] offset:36864\n\tds_read_b64_tr_b16 v[142:143], %[rd1] offset:36992\n\tds_read_b64_tr_b16 v[32:33], %[rd2] offset:32768\n\tds_read_b64_tr_b16 v[34:35], %[rd2] offset:32896\n\tds_read_b64_tr_b16 v[36:37], %[rd2] offset:36864\n\tds_read_b64_tr_b16 v[38:39], %[rd2] offset:36992\n\tds_read_b64_tr_b16 v[144:145], %[rd3] offset:32768\n\tds_read_b64_tr_b16 v[146:147], %[rd3] offset:32896\n\tds_read_b64_tr_b16 v[148:149], %[rd3] offset:36864\n\tds_read_b64_tr_b16 v[150:151], %[rd3] offset:36992\n\ts_waitcnt lgkmcnt(0)\n\t" \
          "buffer_load_dword %[kf], %[ko], %[rc], 0 offen sc1\n\t" \
          "s_waitcnt vmcnt(0)\n\tds_read_b64_tr_b16 v[152:153], %[rd0] offset:0\n\tds_read_b64_tr_b16 v[154:155], %[rd0] offset:128\n\tds_read_b64_tr_b16 v[156:157], %[rd0] offset:4096\n\tds_read_b64_tr_b16 v[158:159], %[rd0] offset:4224\n\tds_read_b64_tr_b16 v[8:9], %[rd1] offset:0\n\tds_read_b64_tr_b16 v[10:11], %[rd1] offset:128\n\tds_read_b64_tr_b16 v[12:13], %[rd1] offset:4096\n\tds_read_b64_tr_b16 v[14:15], %[rd1] offset:4224\n\tds_read_b64_tr_b16 v[160:161], %[rd2] offset:0\n\tds_read_b64_tr_b16 v[162:163], %[rd2] offset:128\n\tds_read_b64_tr_b16 v[164:165], %[rd2] offset:4096\n\tds_read_b64_tr_b16 v[166:167], %[rd2] offset:4224\n\tds_read_b64_tr_b16 v[16:17], %[rd3] offset:0\n\tds_read_b64_tr_b16 v[18:19], %[rd3] offset:128\n\tds_read_b64_tr_b16 v[20:21], %[rd3] offset:4096\n\tds_read_b64_tr_b16 v[22:23], %[rd3] offset:4224\n\ts_waitcnt lgkmcnt(0)\n\tds_write_b128 %[own], v[136:139] offset:0\n\tds_write_b128 %[own], v[140:143] offset:1024\n\tds_write_b128 %[own], v[144:147] offset:2048\n\tds_write_b128 %[own], v[148:151] offset:3072\n\tds_write_b128 %[own], v[152:155] offset:4096\n\tds_write_b128 %[own], v[156:159] offset:5120\n\tds_write_b128 %[own], v[160:163] offset:6144\n\tds_write_b128 %[own], v[164:167] offset:7168\n\ts_waitcnt lgkmcnt(0)\n\t"
#define QK_DEFW "=&{a[0:31]}"(wa[0]), "=&{a[32:63]}"(wa[1]), "=&{a[64:95]}"(wa[2]), "=&{a[96:127]}"(wa[3]), "=&{a[128:159]}"(wa[4]), "=&{a[160:191]}"(wa[5]), "=&{a[192:223]}"(wa[6]), "=&{a[224:255]}"(wa[7]), "=&{v[8:39]}"(wv_[0]), "=&{v[40:71]}"(wv_[1]), "=&{v[72:103]}"(wv_[2]), "=&{v[104:135]}"(wv_[3]), "=&{v[136:167]}"(wv_[4]), "=&{v[168:199]}"(wv_[5]), "=&{v[200:231]}"(wv_[6])
#define QK_ALW 1
#define QK_STEP \
          "ds_read_b128 v[200:203], %[own] offset:4096\n\t" \
          "ds_read_b128 v[204:207], %[own] offset:5120\n\t" \
          "ds_read_b128 v[208:211], %[own] offset:6144\n\t" \
          "ds_read_b128 v[212:215], %[own] offset:7168\n\t" \
          "ds_read_b128 v[216:219], %[own] offset:8192\n\t" \
          "ds_read_b128 v[220:223], %[own] offset:9216\n\t" \
          "ds_read_b128 v[224:227], %[own] offset:0\n\t" \
          "ds_read_b128 v[228:231], %[own] offset:1024\n\t" \
          "v_mov_b32 %[al], -1\n\t" \
          "v_mov_b64 v[168:169], 0\n\t" \
          "v_mov_b64 v[170:171], 0\n\t" \
          "v_mov_b64 v[172:173], 0\n\t" \
          "v_mov_b64 v[174:175], 0\n\t" \
          "v_mov_b64 v[176:177], 0\n\t" \
          "v_mov_b64 v[178:179], 0\n\t" \
          "v_mov_b64 v[180:181], 0\n\t" \
          "v_mov_b64 v[182:183], 0\n\t" \
          "v_mov_b64 v[184:185], 0\n\t" \
          "v_mov_b64 v[186:187], 0\n\t" \
          "v_mov_b64 v[188:189], 0\n\t" \
          "v_mov_b64 v[190:191], 0\n\t" \
          "v_mov_b64 v[192:193], 0\n\t" \
          "v_mov_b64 v[194:195], 0\n\t" \
          "v_mov_b64 v[196:197], 0\n\t" \
          "v_mov_b64 v[198:199], 0\n\t" \
          "s_mov_b32 %[ann], %[t]\n\t" \
          "s_mov_b32 %[fl], 0\n\t" \
          "s_mov_b64 %[en], -1\n\t" \
          "s_waitcnt lgkmcnt(0)\n\t" \
          "s_cmp_eq_u32 %[w], 3\n\t" \
          "s_cbranch_scc0 Lqk_z%=\n\t" \
          "s_branch Lqk_c%=\n\t" \
          "Lqk_fail%=:\n\t" \
          "s_memrealtime vcc\n\t" \
          "s_waitcnt lgkmcnt(0)\n\t" \
          "s_add_u32 %[ann], vcc_lo, %[ald0]\n\t" \
          "v_mbcnt_lo_u32_b32 %[al], -1, 0\n\t" \
          "v_lshlrev_b32 %[al], 2, %[al]\n\t" \
          "s_and_b32 vcc_lo, %[ann], 0xffff\n\t" \
          "s_or_b32 vcc_lo, vcc_lo, 0x10000\n\t" \
          "v_mov_b32 %[m], vcc_lo\n\t" \
          "s_mov_b64 exec, %[alx]\n\t" \
          "buffer_store_dword %[m], %[al], %[rsm], %[als] offen sc1\n\t" \
          "s_mov_b64 exec, -1\n\t" \
          "buffer_load_dwordx4 v[136:139], %[ml0], %[rsm], %[mb0] offen sc1\n\t" \
          "buffer_load_dwordx4 v[140:143], %[ml1], %[rsm], %[mb0] offen sc1\n\t" \
          "buffer_load_dwordx4 v[144:147], %[ml0], %[rsm], %[mb1] offen sc1\n\t" \
          "buffer_load_dwordx4 v[148:151], %[ml1], %[rsm], %[mb1] offen sc1\n\t" \
          "buffer_load_dwordx4 v[152:155], %[ml0], %[rsm], %[mb2] offen sc1\n\t" \
          "buffer_load_dwordx4 v[156:159], %[ml1], %[rsm], %[mb2] offen sc1\n\t" \
          "buffer_load_dwordx4 v[160:163], %[ml0], %[rsm], %[mb3] offen sc1\n\t" \
          "buffer_load_dwordx4 v[164:167], %[ml1], %[rsm], %[mb3] offen sc1\n\t" \
          "ds_read_b128 v[200:203], %[own] offset:4096\n\t" \
          "ds_read_b128 v[204:207], %[own] offset:5120\n\t" \
          "ds_read_b128 v[208:211], %[own] offset:6144\n\t" \
          "ds_read_b128 v[212:215], %[own] offset:7168\n\t" \
          "ds_read_b128 v[216:219], %[own] offset:8192\n\t" \
          "ds_read_b128 v[220:223], %[own] offset:9216\n\t" \
          "ds_read_b128 v[224:227], %[own] offset:0\n\t" \
          "ds_read_b128 v[228:231], %[own] offset:1024\n\t" \
          "v_mov_b32 %[al], -1\n\t" \
          "v_mov_b64 v[168:169], 0\n\t" \
          "v_mov_b64 v[170:171], 0\n\t" \
          "v_mov_b64 v[172:173], 0\n\t" \
          "v_mov_b64 v[174:175], 0\n\t" \
          "v_mov_b64 v[176:177], 0\n\t" \
          "v_mov_b64 v[178:179], 0\n\t" \
          "v_mov_b64 v[180:181], 0\n\t" \
          "v_mov_b64 v[182:183], 0\n\t" \
          "v_mov_b64 v[184:185], 0\n\t" \
          "v_mov_b64 v[186:187], 0\n\t" \
          "v_mov_b64 v[188:189], 0\n\t" \
          "v_mov_b64 v[190:191], 0\n\t" \
          "v_mov_b64 v[192:193], 0\n\t" \
          "v_mov_b64 v[194:195], 0\n\t" \
          "v_mov_b64 v[196:197], 0\n\t" \
          "v_mov_b64 v[198:199], 0\n\t" \
          "s_mov_b32 %[fl], 1\n\t" \
          "s_mov_b64 %[en], 0\n\t" \
          "s_mov_b32 %[cnt], 0\n\t" \
          "s_waitcnt vmcnt(7)\n\t" \
          "v_max3_u32 %[m], v136, v137, v138\n\t" \
          "v_max_u32 %[m], %[m], v139\n\t" \
          "v_cmp_eq_u32 vcc, -1, %[m]\n\t" \
          "s_cbranch_vccnz Lqk_p0_%=\n\t" \
          "Lqk_k0_%=:\n\t" \
          "s_waitcnt vmcnt(6)\n\t" \
          "v_max3_u32 %[m], v140, v141, v142\n\t" \
          "v_max_u32 %[m], %[m], v143\n\t" \
          "v_cmp_eq_u32 vcc, -1, %[m]\n\t" \
          "s_cbranch_vccnz Lqk_p1_%=\n\t" \
          "Lqk_k1_%=:\n\t" \
          "s_waitcnt vmcnt(5)\n\t" \
          "v_max3_u32 %[m], v144, v145, v146\n\t" \
          "v_max_u32 %[m], %[m], v147\n\t" \
          "v_cmp_eq_u32 vcc, -1, %[m]\n\t" \
          "s_cbranch_vccnz Lqk_p2_%=\n\t" \
          "Lqk_k2_%=:\n\t" \
          "s_waitcnt vmcnt(4)\n\t" \
          "v_max3_u32 %[m], v148, v149, v150\n\t" \
          "v_max_u32 %[m], %[m], v151\n\t" \
          "v_cmp_eq_u32 vcc, -1, %[m]\n\t" \
          "s_cbranch_vccnz Lqk_p3_%=\n\t" \
          "Lqk_k3_%=:\n\t" \
          "s_waitcnt vmcnt(3)\n\t" \
          "v_max3_u32 %[m], v152, v153, v154\n\t" \
          "v_max_u32 %[m], %[m], v155\n\t" \
          "v_cmp_eq_u32 vcc, -1, %[m]\n\t" \
          "s_cbranch_vccnz Lqk_p4_%=\n\t" \
          "Lqk_k4_%=:\n\t" \
          "s_waitcnt vmcnt(2)\n\t" \
          "v_max3_u32 %[m], v156, v157, v158\n\t" \
          "v_max_u32 %[m], %[m], v159\n\t" \
          "v_cmp_eq_u32 vcc, -1, %[m]\n\t" \
          "s_cbranch_vccnz Lqk_p5_%=\n\t" \
          "Lqk_k5_%=:\n\t" \
          "s_waitcnt vmcnt(1)\n\t" \
          "v_max3_u32 %[m], v160, v161, v162\n\t" \
          "v_max_u32 %[m], %[m], v163\n\t" \
          "v_cmp_eq_u32 vcc, -1, %[m]\n\t" \
          "s_cbranch_vccnz Lqk_p6_%=\n\t" \
          "Lqk_k6_%=:\n\t" \
          "s_waitcnt vmcnt(0)\n\t" \
          "v_max3_u32 %[m], v164, v165, v166\n\t" \
          "v_max_u32 %[m], %[m], v167\n\t" \
          "v_cmp_eq_u32 vcc, -1, %[m]\n\t" \
          "s_cbranch_vccnz Lqk_p7_%=\n\t" \
          "Lqk_k7_%=:\n\t" \
          "s_memrealtime vcc\n\t" \
          "s_waitcnt lgkmcnt(0)\n\t" \
          "s_add_u32 vcc_lo, vcc_lo, %[ald]\n\t" \
          "s_sub_i32 vcc_hi, vcc_lo, %[ann]\n\t" \
          "s_cmp_gt_i32 vcc_hi, 0\n\t" \
          "s_cbranch_scc0 Lqk_h%=\n\t" \
          "s_mov_b32 %[ann], vcc_lo\n\t" \
          "v_mbcnt_lo_u32_b32 %[al], -1, 0\n\t" \
          "v_lshlrev_b32 %[al], 2, %[al]\n\t" \
          "s_and_b32 vcc_lo, %[ann], 0xffff\n\t" \
          "s_or_b32 vcc_lo, vcc_lo, 0x10000\n\t" \
          "v_mov_b32 %[m], vcc_lo\n\t" \
          "s_mov_b64 exec, %[alx]\n\t" \
          "buffer_store_dword %[m], %[al], %[rsm], %[als] offen sc1\n\t" \
          "s_mov_b64 exec, -1\n\t" \
          "Lqk_h%=:\n\t" \
          "v_mov_b32 %[al], -1\n\t" \
          "s_branch Lqk_go%=\n\t" \
          "Lqk_p0_%=:\n\t" \
          "s_cmp_lt_i32 %[cnt], 0\n\t" \
          "s_cbranch_scc1 Lqk_k0_%=\n\t" \
          "s_add_u32 %[cnt], %[cnt], 1\n\t" \
          "s_and_b32 vcc_lo, %[cnt], 3\n\t" \
          "s_cbranch_scc1 Lqk_q0_%=\n\t" \
          "s_getpc_b64 s[48:49]\n\t" \
          "s_branch Lqk_spin%=\n\t" \
          "Lqk_q0_%=:\n\t" \
          "buffer_load_dwordx4 v[136:139], %[ml0], %[rsm], %[mb0] offen sc1\n\t" \
          "s_waitcnt vmcnt(0)\n\t" \
          "v_max3_u32 %[m], v136, v137, v138\n\t" \
          "v_max_u32 %[m], %[m], v139\n\t" \
          "v_cmp_eq_u32 vcc, -1, %[m]\n\t" \
          "s_cbranch_vccnz Lqk_p0_%=\n\t" \
          "buffer_load_dwordx4 v[140:143], %[ml1], %[rsm], %[mb0] offen sc1\n\t" \
          "buffer_load_dwordx4 v[144:147], %[ml0], %[rsm], %[mb1] offen sc1\n\t" \
          "buffer_load_dwordx4 v[148:151], %[ml1], %[rsm], %[mb1] offen sc1\n\t" \
          "buffer_load_dwordx4 v[152:155], %[ml0], %[rsm], %[mb2] offen sc1\n\t" \
          "buffer_load_dwordx4 v[156:159], %[ml1], %[rsm], %[mb2] offen sc1\n\t" \
          "buffer_load_dwordx4 v[160:163], %[ml0], %[rsm], %[mb3] offen sc1\n\t" \
          "buffer_load_dwordx4 v[164:167], %[ml1], %[rsm], %[mb3] offen sc1\n\t" \
          "s_branch Lqk_k0_%=\n\t" \
          "Lqk_p1_%=:\n\t" \
          "s_cmp_lt_i32 %[cnt], 0\n\t" \
          "s_cbranch_scc1 Lqk_k1_%=\n\t" \
          "s_add_u32 %[cnt], %[cnt], 1\n\t" \
          "s_and_b32 vcc_lo, %[cnt], 3\n\t" \
          "s_cbranch_scc1 Lqk_q1_%=\n\t" \
          "s_getpc_b64 s[48:49]\n\t" \
          "s_branch Lqk_spin%=\n\t" \
          "Lqk_q1_%=:\n\t" \
          "buffer_load_dwordx4 v[140:143], %[ml1], %[rsm], %[mb0] offen sc1\n\t" \
          "s_waitcnt vmcnt(0)\n\t" \
          "v_max3_u32 %[m], v140, v141, v142\n\t" \
          "v_max_u32 %[m], %[m], v143\n\t" \
          "v_cmp_eq_u32 vcc, -1, %[m]\n\t" \
          "s_cbranch_vccnz Lqk_p1_%=\n\t" \
          "buffer_load_dwordx4 v[144:147], %[ml0], %[rsm], %[mb1] offen sc1\n\t" \
          "buffer_load_dwordx4 v[148:151], %[ml1], %[rsm], %[mb1] offen sc1\n\t" \
          "buffer_load_dwordx4 v[152:155], %[ml0], %[rsm], %[mb2] offen sc1\n\t" \
          "buffer_load_dwordx4 v[156:159], %[ml1], %[rsm], %[mb2] offen sc1\n\t" \
          "buffer_load_dwordx4 v[160:163], %[ml0], %[rsm], %[mb3] offen sc1\n\t" \
          "buffer_load_dwordx4 v[164:167], %[ml1], %[rsm], %[mb3] offen sc1\n\t" \
          "s_branch Lqk_k1_%=\n\t" \
          "Lqk_p2_%=:\n\t" \
          "s_cmp_lt_i32 %[cnt], 0\n\t" \
          "s_cbranch_scc1 Lqk_k2_%=\n\t" \
          "s_add_u32 %[cnt], %[cnt], 1\n\t" \
          "s_and_b32 vcc_lo, %[cnt], 3\n\t" \
          "s_cbranch_scc1 Lqk_q2_%=\n\t" \
          "s_getpc_b64 s[48:49]\n\t" \
          "s_branch Lqk_spin%=\n\t" \
          "Lqk_q2_%=:\n\t" \
          "buffer_load_dwordx4 v[144:147], %[ml0], %[rsm], %[mb1] offen sc1\n\t" \
          "s_waitcnt vmcnt(0)\n\t" \
          "v_max3_u32 %[m], v144, v145, v146\n\t" \
          "v_max_u32 %[m], %[m], v147\n\t" \
          "v_cmp_eq_u32 vcc, -1, %[m]\n\t" \
          "s_cbranch_vccnz Lqk_p2_%=\n\t" \
          "buffer_load_dwordx4 v[148:151], %[ml1], %[rsm], %[mb1] offen sc1\n\t" \
          "buffer_load_dwordx4 v[152:155], %[ml0], %[rsm], %[mb2] offen sc1\n\t" \
          "buffer_load_dwordx4 v[156:159], %[ml1], %[rsm], %[mb2] offen sc1\n\t" \
          "buffer_load_dwordx4 v[160:163], %[ml0], %[rsm], %[mb3] offen sc1\n\t" \
          "buffer_load_dwordx4 v[164:167], %[ml1], %[rsm], %[mb3] offen sc1\n\t" \
          "s_branch Lqk_k2_%=\n\t" \
          "Lqk_p3_%=:\n\t" \
          "s_cmp_lt_i32 %[cnt], 0\n\t" \
          "s_cbranch_scc1 Lqk_k3_%=\n\t" \
          "s_add_u32 %[cnt], %[cnt], 1\n\t" \
          "s_and_b32 vcc_lo, %[cnt], 3\n\t" \
          "s_cbranch_scc1 Lqk_q3_%=\n\t" \
          "s_getpc_b64 s[48:49]\n\t" \
          "s_branch Lqk_spin%=\n\t" \
          "Lqk_q3_%=:\n\t" \
          "buffer_load_dwordx4 v[148:151], %[ml1], %[rsm], %[mb1] offen sc1\n\t" \
          "s_waitcnt vmcnt(0)\n\t" \
          "v_max3_u32 %[m], v148, v149, v150\n\t" \
          "v_max_u32 %[m], %[m], v151\n\t" \
          "v_cmp_eq_u32 vcc, -1, %[m]\n\t" \
          "s_cbranch_vccnz Lqk_p3_%=\n\t" \
          "buffer_load_dwordx4 v[152:155], %[ml0], %[rsm], %[mb2] offen sc1\n\t" \
          "buffer_load_dwordx4 v[156:159], %[ml1], %[rsm], %[mb2] offen sc1\n\t" \
          "buffer_load_dwordx4 v[160:163], %[ml0], %[rsm], %[mb3] offen sc1\n\t" \
          "buffer_load_dwordx4 v[164:167], %[ml1], %[rsm], %[mb3] offen sc1\n\t" \
          "s_branch Lqk_k3_%=\n\t" \
          "Lqk_p4_%=:\n\t" \
          "s_cmp_lt_i32 %[cnt], 0\n\t" \
          "s_cbranch_scc1 Lqk_k4_%=\n\t" \
          "s_add_u32 %[cnt], %[cnt], 1\n\t" \
          "s_and_b32 vcc_lo, %[cnt], 3\n\t" \
          "s_cbranch_scc1 Lqk_q4_%=\n\t" \
          "s_getpc_b64 s[48:49]\n\t" \
          "s_branch Lqk_spin%=\n\t" \
          "Lqk_q4_%=:\n\t" \
          "buffer_load_dwordx4 v[152:155], %[ml0], %[rsm], %[mb2] offen sc1\n\t" \
          "s_waitcnt vmcnt(0)\n\t" \
          "v_max3_u32 %[m], v152, v153, v154\n\t" \
          "v_max_u32 %[m], %[m], v155\n\t" \
          "v_cmp_eq_u32 vcc, -1, %[m]\n\t" \
          "s_cbranch_vccnz Lqk_p4_%=\n\t" \
          "buffer_load_dwordx4 v[156:159], %[ml1], %[rsm], %[mb2] offen sc1\n\t" \
          "buffer_load_dwordx4 v[160:163], %[ml0], %[rsm], %[mb3] offen sc1\n\t" \
          "buffer_load_dwordx4 v[164:167], %[ml1], %[rsm], %[mb3] offen sc1\n\t" \
          "s_branch Lqk_k4_%=\n\t" \
          "Lqk_p5_%=:\n\t" \
          "s_cmp_lt_i32 %[cnt], 0\n\t" \
          "s_cbranch_scc1 Lqk_k5_%=\n\t" \
          "s_add_u32 %[cnt], %[cnt], 1\n\t" \
          "s_and_b32 vcc_lo, %[cnt], 3\n\t" \
          "s_cbranch_scc1 Lqk_q5_%=\n\t" \
          "s_getpc_b64 s[48:49]\n\t" \
          "s_branch Lqk_spin%=\n\t" \
          "Lqk_q5_%=:\n\t" \
          "buffer_load_dwordx4 v[156:159], %[ml1], %[rsm], %[mb2] offen sc1\n\t" \
          "s_waitcnt vmcnt(0)\n\t" \
          "v_max3_u32 %[m], v156, v157, v158\n\t" \
          "v_max_u32 %[m], %[m], v159\n\t" \
          "v_cmp_eq_u32 vcc, -1, %[m]\n\t" \
          "s_cbranch_vccnz Lqk_p5_%=\n\t" \
          "buffer_load_dwordx4 v[160:163], %[ml0], %[rsm], %[mb3] offen sc1\n\t" \
          "buffer_load_dwordx4 v[164:167], %[ml1], %[rsm], %[mb3] offen sc1\n\t" \
          "s_branch Lqk_k5_%=\n\t" \
          "Lqk_p6_%=:\n\t" \
          "s_cmp_lt_i32 %[cnt], 0\n\t" \
          "s_cbranch_scc1 Lqk_k6_%=\n\t" \
          "s_add_u32 %[cnt], %[cnt], 1\n\t" \
          "s_and_b32 vcc_lo, %[cnt], 3\n\t" \
          "s_cbranch_scc1 Lqk_q6_%=\n\t" \
          "s_getpc_b64 s[48:49]\n\t" \
          "s_branch Lqk_spin%=\n\t" \
          "Lqk_q6_%=:\n\t" \
          "buffer_load_dwordx4 v[160:163], %[ml0], %[rsm], %[mb3] offen sc1\n\t" \
          "s_waitcnt vmcnt(0)\n\t" \
          "v_max3_u32 %[m], v160, v161, v162\n\t" \
          "v_max_u32 %[m], %[m], v163\n\t" \
          "v_cmp_eq_u32 vcc, -1, %[m]\n\t" \
          "s_cbranch_vccnz Lqk_p6_%=\n\t" \
          "buffer_load_dwordx4 v[164:167], %[ml1], %[rsm], %[mb3] offen sc1\n\t" \
          "s_branch Lqk_k6_%=\n\t" \
          "Lqk_p7_%=:\n\t" \
          "s_cmp_lt_i32 %[cnt], 0\n\t" \
          "s_cbranch_scc1 Lqk_k7_%=\n\t" \
          "s_add_u32 %[cnt], %[cnt], 1\n\t" \
          "s_and_b32 vcc_lo, %[cnt], 3\n\t" \
          "s_cbranch_scc1 Lqk_q7_%=\n\t" \
          "s_getpc_b64 s[48:49]\n\t" \
          "s_branch Lqk_spin%=\n\t" \
          "Lqk_q7_%=:\n\t" \
          "buffer_load_dwordx4 v[164:167], %[ml1], %[rsm], %[mb3] offen sc1\n\t" \
          "s_waitcnt vmcnt(0)\n\t" \
          "v_max3_u32 %[m], v164, v165, v166\n\t" \
          "v_max_u32 %[m], %[m], v167\n\t" \
          "v_cmp_eq_u32 vcc, -1, %[m]\n\t" \
          "s_cbranch_vccnz Lqk_p7_%=\n\t" \
          "s_branch Lqk_k7_%=\n\t" \
          "Lqk_spin%=:\n\t" \
          "s_memrealtime vcc\n\t" \
          "s_waitcnt lgkmcnt(0)\n\t" \
          "s_add_u32 %[ann], vcc_lo, %[ald0]\n\t" \
          "v_mbcnt_lo_u32_b32 %[al], -1, 0\n\t" \
          "v_lshlrev_b32 %[al], 2, %[al]\n\t" \
          "s_and_b32 vcc_lo, %[ann], 0xffff\n\t" \
          "s_or_b32 vcc_lo, vcc_lo, 0x10000\n\t" \
          "v_mov_b32 %[m], vcc_lo\n\t" \
          "s_mov_b64 exec, %[alx]\n\t" \
          "buffer_store_dword %[m], %[al], %[rsm], %[als] offen sc1\n\t" \
          "s_mov_b64 exec, -1\n\t" \
          "buffer_load_dword %[al], off, %[rsc], %[tmo] sc1\n\t" \
          "s_add_u32 s48, s48, 4\n\t" \
          "s_addc_u32 s49, s49, 0\n\t" \
          "s_waitcnt vmcnt(0)\n\t" \
          "v_readfirstlane_b32 vcc_lo, %[al]\n\t" \
          "s_lshr_b32 vcc_hi, %[cnt], 16\n\t" \
          "s_or_b32 vcc_lo, vcc_lo, vcc_hi\n\t" \
          "s_cbranch_scc0 Lqk_back%=\n\t" \
          "v_mov_b32 %[al], 1\n\t" \
          "buffer_store_dword %[al], off, %[rsc], %[tmo] sc1\n\t" \
          "s_mov_b32 %[cnt], -1\n\t" \
          "Lqk_back%=:\n\t" \
          "s_setpc_b64 s[48:49]\n\t" \
          ".p2align 6\n\t" \
          "s_nop 0\n\t" \
          "s_nop 0\n\t" \
          "Lqk_s%=:\n\t" \
          "s_sleep 2\n\t" \
          "Lqk_c%=:\n\t" \
          "s_memrealtime vcc\n\t" \
          "s_waitcnt lgkmcnt(0)\n\t" \
          "s_sub_i32 vcc_lo, %[t], vcc_lo\n\t" \
          "s_cmp_gt_i32 vcc_lo, 16\n\t" \
          "s_cbranch_scc1 Lqk_s%=\n\t" \
          "Lqk_w%=:\n\t" \
          "s_memrealtime s[50:51]\n\t" \
          "s_getpc_b64 s[48:49]\n\t" \
          "Lqk_y%=:\n\t" \
          "s_add_u32 s48, s48, Lqk_z%=-Lqk_y%=\n\t" \
          "s_addc_u32 s49, s49, 0\n\t" \
          "Lqk_v%=:\n\t" \
          "s_memrealtime s[52:53]\n\t" \
          "s_waitcnt lgkmcnt(1)\n\t" \
          "s_sub_i32 vcc_lo, %[t], s50\n\t" \
          "s_cmp_gt_i32 vcc_lo, 3\n\t" \
          "s_cbranch_scc0 Lqk_x%=\n\t" \
          "s_memrealtime s[50:51]\n\t" \
          "s_waitcnt lgkmcnt(1)\n\t" \
          "s_sub_i32 vcc_lo, %[t], s52\n\t" \
          "s_cmp_gt_i32 vcc_lo, 3\n\t" \
          "s_cbranch_scc0 Lqk_x%=\n\t" \
          "s_memrealtime s[52:53]\n\t" \
          "s_waitcnt lgkmcnt(1)\n\t" \
          "s_sub_i32 vcc_lo, %[t], s50\n\t" \
          "s_cmp_gt_i32 vcc_lo, 3\n\t" \
          "s_cbranch_scc0 Lqk_x%=\n\t" \
          "s_memrealtime s[50:51]\n\t" \
          "s_waitcnt lgkmcnt(1)\n\t" \
          "s_sub_i32 vcc_lo, %[t], s52\n\t" \
          "s_cmp_gt_i32 vcc_lo, 3\n\t" \
          "s_cbranch_scc0 Lqk_x%=\n\t" \
          "s_memrealtime s[52:53]\n\t" \
          "s_waitcnt lgkmcnt(1)\n\t" \
          "s_sub_i32 vcc_lo, %[t], s50\n\t" \
          "s_cmp_gt_i32 vcc_lo, 3\n\t" \
          "s_cbranch_scc0 Lqk_x%=\n\t" \
          "s_memrealtime s[50:51]\n\t" \
          "s_waitcnt lgkmcnt(1)\n\t" \
          "s_sub_i32 vcc_lo, %[t], s52\n\t" \
          "s_cmp_gt_i32 vcc_lo, 3\n\t" \
          "s_cbranch_scc0 Lqk_x%=\n\t" \
          "s_memrealtime s[52:53]\n\t" \
          "s_waitcnt lgkmcnt(1)\n\t" \
          "s_sub_i32 vcc_lo, %[t], s50\n\t" \
          "s_cmp_gt_i32 vcc_lo, 3\n\t" \
          "s_cbranch_scc0 Lqk_x%=\n\t" \
          "s_memrealtime s[50:51]\n\t" \
          "s_waitcnt lgkmcnt(1)\n\t" \
          "s_sub_i32 vcc_lo, %[t], s52\n\t" \
          "s_cmp_gt_i32 vcc_lo, 3\n\t" \
          "s_cbranch_scc0 Lqk_x%=\n\t" \
          "s_branch Lqk_v%=\n\t" \
          "Lqk_x%=:\n\t" \
          "s_max_i32 vcc_lo, vcc_lo, 0\n\t" \
          "s_lshl_b32 vcc_lo, vcc_lo, 2\n\t" \
          "s_sub_u32 s48, s48, vcc_lo\n\t" \
          "s_subb_u32 s49, s49, 0\n\t" \
          "s_setpc_b64 s[48:49]\n\t" \
          "s_nop 5\n\t" \
          "s_nop 5\n\t" \
          "s_nop 5\n\t" \
          "Lqk_z%=:\n\t" \
          "s_barrier\n\t" \
          "buffer_load_dwordx4 v[136:139], %[ml0], %[rsm], %[mb0] offen sc1\n\t" \
          "s_nop 9\n\t" \
          "buffer_load_dwordx4 v[140:143], %[ml1], %[rsm], %[mb0] offen sc1\n\t" \
          "s_nop 9\n\t" \
          "buffer_load_dwordx4 v[144:147], %[ml0], %[rsm], %[mb1] offen sc1\n\t" \
          "s_nop 9\n\t" \
          "buffer_load_dwordx4 v[148:151], %[ml1], %[rsm], %[mb1] offen sc1\n\t" \
          "s_nop 9\n\t" \
          "buffer_load_dwordx4 v[152:155], %[ml0], %[rsm], %[mb2] offen sc1\n\t" \
          "s_nop 9\n\t" \
          "buffer_load_dwordx4 v[156:159], %[ml1], %[rsm], %[mb2] offen sc1\n\t" \
          "s_nop 9\n\t" \
          "buffer_load_dwordx4 v[160:163], %[ml0], %[rsm], %[mb3] offen sc1\n\t" \
          "s_nop 9\n\t" \
          "buffer_load_dwordx4 v[164:167], %[ml1], %[rsm], %[mb3] offen sc1\n\t" \
          "s_waitcnt vmcnt(7)\n\t" \
          "Lqk_go%=:\n\t" \
          "v_smfmac_f32_16x16x64_bf16 v[168:171], v[136:139], v[200:207], %[idx]\n\t" \
          "ds_read_b128 v[200:203], %[own] offset:2048\n\t" \
          "v_smfmac_f32_16x16x64_bf16 v[172:175], v[136:139], v[8:15], %[idx]\n\t" \
          "ds_read_b128 v[204:207], %[own] offset:3072\n\t" \
          "v_smfmac_f32_16x16x64_bf16 v[176:179], v[136:139], v[208:215], %[idx]\n\t" \
          "ds_read_b128 v[208:211], %[own] offset:28672\n\t" \
          "v_smfmac_f32_16x16x64_bf16 v[180:183], v[136:139], v[16:23], %[idx]\n\t" \
          "ds_read_b128 v[212:215], %[own] offset:29696\n\t" \
          "s_waitcnt vmcnt(6)\n\t" \
          "v_smfmac_f32_16x16x64_bf16 v[184:187], v[136:139], v[216:223], %[idx]\n\t" \
          "ds_read_b128 v[216:219], %[own] offset:30720\n\t" \
          "v_smfmac_f32_16x16x64_bf16 v[168:171], v[140:143], v[24:31], %[idx]\n\t" \
          "ds_read_b128 v[220:223], %[own] offset:31744\n\t" \
          "v_smfmac_f32_16x16x64_bf16 v[172:175], v[140:143], v[224:231], %[idx]\n\t" \
          "ds_read_b128 v[224:227], %[own] offset:10240\n\t" \
          "v_smfmac_f32_16x16x64_bf16 v[176:179], v[140:143], v[32:39], %[idx]\n\t" \
          "ds_read_b128 v[228:231], %[own] offset:11264\n\t" \
          "s_waitcnt lgkmcnt(6)\n\t" \
          "v_smfmac_f32_16x16x64_bf16 v[180:183], v[140:143], v[200:207], %[idx]\n\t" \
          "ds_read_b128 v[200:203], %[own] offset:24576\n\t" \
          "v_max3_u32 %[m], v136, v137, v138\n\t" \
          "v_smfmac_f32_16x16x64_bf16 v[184:187], v[140:143], v[40:47], %[idx]\n\t" \
          "ds_read_b128 v[204:207], %[own] offset:25600\n\t" \
          "s_waitcnt vmcnt(5) lgkmcnt(6)\n\t" \
          "v_smfmac_f32_16x16x64_bf16 v[168:171], v[144:147], v[208:215], %[idx]\n\t" \
          "ds_read_b128 v[208:211], %[own] offset:26624\n\t" \
          "v_max3_u32 %[m], v138, v139, %[m]\n\t" \
          "v_smfmac_f32_16x16x64_bf16 v[172:175], v[144:147], v[48:55], %[idx]\n\t" \
          "ds_read_b128 v[212:215], %[own] offset:27648\n\t" \
          "s_waitcnt lgkmcnt(6)\n\t" \
          "v_smfmac_f32_16x16x64_bf16 v[176:179], v[144:147], v[216:223], %[idx]\n\t" \
          "ds_read_b128 v[216:219], %[own] offset:20480\n\t" \
          "v_cmp_eq_u32 %[bad1], -1, %[m]\n\t" \
          "v_smfmac_f32_16x16x64_bf16 v[180:183], v[144:147], v[56:63], %[idx]\n\t" \
          "ds_read_b128 v[220:223], %[own] offset:21504\n\t" \
          "s_waitcnt vmcnt(4) lgkmcnt(6)\n\t" \
          "v_smfmac_f32_16x16x64_bf16 v[184:187], v[144:147], v[224:231], %[idx]\n\t" \
          "ds_read_b128 v[224:227], %[own] offset:22528\n\t" \
          "s_and_b64 %[bad1], %[bad1], %[en]\n\t" \
          "v_smfmac_f32_16x16x64_bf16 v[168:171], v[148:151], v[64:71], %[idx]\n\t" \
          "ds_read_b128 v[228:231], %[own] offset:23552\n\t" \
          "s_waitcnt lgkmcnt(6)\n\t" \
          "v_smfmac_f32_16x16x64_bf16 v[172:175], v[148:151], v[200:207], %[idx]\n\t" \
          "ds_read_b128 v[200:203], %[own] offset:12288\n\t" \
          "s_cbranch_scc1 Lqk_fail%=\n\t" \
          "v_smfmac_f32_16x16x64_bf16 v[176:179], v[148:151], v[72:79], %[idx]\n\t" \
          "ds_read_b128 v[204:207], %[own] offset:13312\n\t" \
          "s_waitcnt lgkmcnt(6)\n\t" \
          "v_smfmac_f32_16x16x64_bf16 v[180:183], v[148:151], v[208:215], %[idx]\n\t" \
          "ds_read_b128 v[208:211], %[own] offset:16384\n\t" \
          "v_max3_u32 %[m], v140, v141, %[m]\n\t" \
          "v_smfmac_f32_16x16x64_bf16 v[184:187], v[148:151], v[80:87], %[idx]\n\t" \
          "ds_read_b128 v[212:215], %[own] offset:17408\n\t" \
          "s_waitcnt vmcnt(3) lgkmcnt(6)\n\t" \
          "v_smfmac_f32_16x16x64_bf16 v[168:171], v[152:155], v[216:223], %[idx]\n\t" \
          "ds_read_b128 v[216:219], %[own] offset:18432\n\t" \
          "v_max3_u32 %[m], v142, v143, %[m]\n\t" \
          "v_smfmac_f32_16x16x64_bf16 v[172:175], v[152:155], v[88:95], %[idx]\n\t" \
          "ds_read_b128 v[220:223], %[own] offset:19456\n\t" \
          "s_waitcnt lgkmcnt(6)\n\t" \
          "v_smfmac_f32_16x16x64_bf16 v[176:179], v[152:155], v[224:231], %[idx]\n\t" \
          "ds_read_b128 v[224:227], %[own] offset:14336\n\t" \
          "v_max3_u32 %[m], v144, v145, %[m]\n\t" \
          "v_smfmac_f32_16x16x64_bf16 v[180:183], v[152:155], v[96:103], %[idx]\n\t" \
          "ds_read_b128 v[228:231], %[own] offset:15360\n\t" \
          "s_waitcnt vmcnt(2) lgkmcnt(6)\n\t" \
          "v_smfmac_f32_16x16x64_bf16 v[184:187], v[152:155], v[200:207], %[idx]\n\t" \
          "v_max3_u32 %[m], v146, v147, %[m]\n\t" \
          "v_smfmac_f32_16x16x64_bf16 v[168:171], v[156:159], v[104:111], %[idx]\n\t" \
          "s_waitcnt lgkmcnt(4)\n\t" \
          "v_max3_u32 %[m], v148, v149, %[m]\n\t" \
          "v_smfmac_f32_16x16x64_bf16 v[172:175], v[156:159], v[208:215], %[idx]\n\t" \
          "v_max3_u32 %[m], v150, v151, %[m]\n\t" \
          "v_smfmac_f32_16x16x64_bf16 v[176:179], v[156:159], v[112:119], %[idx]\n\t" \
          "s_waitcnt lgkmcnt(2)\n\t" \
          "v_max3_u32 %[m], v152, v153, %[m]\n\t" \
          "v_smfmac_f32_16x16x64_bf16 v[180:183], v[156:159], v[216:223], %[idx]\n\t" \
          "ds_read_b128 v[216:219], %[own] offset:32768\n\t" \
          "v_max3_u32 %[m], v154, v155, %[m]\n\t" \
          "v_smfmac_f32_16x16x64_bf16 v[184:187], v[156:159], v[120:127], %[idx]\n\t" \
          "ds_read_b128 v[220:223], %[own] offset:33792\n\t" \
          "s_waitcnt vmcnt(1) lgkmcnt(2)\n\t" \
          "v_smfmac_f32_16x16x64_bf16 v[168:171], v[160:163], v[224:231], %[idx]\n\t" \
          "ds_read_b64_tr_b16 v[224:225], %[vxp]\n\t" \
          "v_max3_u32 %[m], v156, v157, %[m]\n\t" \
          "v_smfmac_f32_16x16x64_bf16 v[172:175], v[160:163], v[128:135], %[idx]\n\t" \
          "ds_read_b64_tr_b16 v[226:227], %[vxp] offset:1024\n\t" \
          "v_max3_u32 %[m], v158, v159, %[m]\n\t" \
          "v_smfmac_f32_16x16x64_bf16 v[176:179], v[160:163], a[0:7], %[idx]\n\t" \
          "ds_read_b64_tr_b16 v[228:229], %[vxp] offset:8\n\t" \
          "v_max3_u32 %[m], v160, v161, %[m]\n\t" \
          "v_smfmac_f32_16x16x64_bf16 v[180:183], v[160:163], a[8:15], %[idx]\n\t" \
          "ds_read_b64_tr_b16 v[230:231], %[vxp] offset:1032\n\t" \
          "v_smfmac_f32_16x16x64_bf16 v[184:187], v[160:163], a[16:23], %[idx]\n\t" \
          "s_waitcnt vmcnt(0)\n\t" \
          "v_max3_u32 %[m], v162, v163, %[m]\n\t" \
          "v_smfmac_f32_16x16x64_bf16 v[168:171], v[164:167], a[24:31], %[idx]\n\t" \
          "v_max3_u32 %[m], v164, v165, %[m]\n\t" \
          "v_smfmac_f32_16x16x64_bf16 v[172:175], v[164:167], a[32:39], %[idx]\n\t" \
          "v_max3_u32 %[m], v166, v167, %[m]\n\t" \
          "v_smfmac_f32_16x16x64_bf16 v[176:179], v[164:167], a[40:47], %[idx]\n\t" \
          "v_cvt_pk_bf16_f32 v200, v168, v169\n\t" \
          "v_cvt_pk_bf16_f32 v201, v170, v171\n\t" \
          "v_smfmac_f32_16x16x64_bf16 v[180:183], v[164:167], a[48:55], %[idx]\n\t" \
          "ds_write_b64 %[own], v[200:201] offset:36864\n\t" \
          "v_cvt_pk_bf16_f32 v202, v172, v173\n\t" \
          "v_cvt_pk_bf16_f32 v203, v174, v175\n\t" \
          "v_smfmac_f32_16x16x64_bf16 v[184:187], v[164:167], a[56:63], %[idx]\n\t" \
          "ds_write_b64 %[own], v[202:203] offset:36872\n\t" \
          "v_smfmac_f32_16x16x64_bf16 v[188:191], v[136:139], a[64:71], %[idx]\n\t" \
          "v_cmp_eq_u32 %[bad1], -1, %[m]\n\t" \
          "v_smfmac_f32_16x16x64_bf16 v[188:191], v[140:143], a[72:79], %[idx]\n\t" \
          "buffer_store_dword %[al], %[ho], %[rsm], %[rb] offen sc1\n\t" \
          "v_cvt_pk_bf16_f32 v208, v176, v177\n\t" \
          "v_smfmac_f32_16x16x64_bf16 v[188:191], v[144:147], a[80:87], %[idx]\n\t" \
          "s_and_b64 %[bad1], %[bad1], %[en]\n\t" \
          "s_cbranch_scc1 Lqk_fail%=\n\t" \
          "s_waitcnt lgkmcnt(0)\n\t" \
          "s_barrier\n\t" \
          "v_cvt_pk_bf16_f32 v209, v178, v179\n\t" \
          "v_smfmac_f32_16x16x64_bf16 v[188:191], v[148:151], a[88:95], %[idx]\n\t" \
          "ds_read_b128 v[210:213], %[p1] offset:36864\n\t" \
          "v_cvt_pk_bf16_f32 v214, v180, v181\n\t" \
          "v_smfmac_f32_16x16x64_bf16 v[188:191], v[152:155], a[96:103], %[idx]\n\t" \
          "buffer_store_dword %[al], %[ho], %[rsm], %[rb] offen offset:256 sc1\n\t" \
          "v_cvt_pk_bf16_f32 v215, v182, v183\n\t" \
          "v_smfmac_f32_16x16x64_bf16 v[188:191], v[156:159], a[104:111], %[idx]\n\t" \
          "v_cvt_pk_bf16_f32 v204, v184, v185\n\t" \
          "v_smfmac_f32_16x16x64_bf16 v[188:191], v[160:163], a[112:119], %[idx]\n\t" \
          "v_cvt_pk_bf16_f32 v205, v186, v187\n\t" \
          "v_smfmac_f32_16x16x64_bf16 v[188:191], v[164:167], a[120:127], %[idx]\n\t" \
          "s_waitcnt lgkmcnt(0)\n\t" \
          "v_mfma_f32_16x16x32_bf16 v[168:171], v[208:211], %[jk], 0\n\t" \
          "v_smfmac_f32_16x16x64_bf16 v[192:195], v[136:139], a[128:135], %[idx]\n\t" \
          "v_mfma_f32_16x16x32_bf16 v[172:175], v[212:215], %[jk], 0\n\t" \
          "v_cvt_pk_bf16_f32 v176, v168, v169\n\t" \
          "v_cvt_pk_bf16_f32 v177, v170, v171\n\t" \
          "v_smfmac_f32_16x16x64_bf16 v[196:199], v[136:139], a[136:143], %[idx]\n\t" \
          "ds_write_b64 %[own], v[204:205] offset:37888\n\t" \
          "v_cvt_pk_bf16_f32 v206, v188, v189\n\t" \
          "v_smfmac_f32_16x16x64_bf16 v[192:195], v[140:143], a[144:151], %[idx]\n\t" \
          "v_cvt_pk_bf16_f32 v178, v172, v173\n\t" \
          "v_cvt_pk_bf16_f32 v179, v174, v175\n\t" \
          "v_smfmac_f32_16x16x64_bf16 v[196:199], v[140:143], a[152:159], %[idx]\n\t" \
          "v_cvt_pk_bf16_f32 v207, v190, v191\n\t" \
          "v_mfma_f32_16x16x32_bf16 v[208:211], v[216:219], v[176:179], 0\n\t" \
          "v_mov_b32 v168, %[one]\n\t" \
          "v_mov_b32 v169, %[one]\n\t" \
          "v_mfma_f32_16x16x32_bf16 v[212:215], v[220:223], v[176:179], 0\n\t" \
          "v_mov_b32 v170, %[one]\n\t" \
          "v_mov_b32 v171, %[one]\n\t" \
          "v_smfmac_f32_16x16x64_bf16 v[192:195], v[144:147], a[160:167], %[idx]\n\t" \
          "ds_write_b64 %[own], v[206:207] offset:37896\n\t" \
          "v_smfmac_f32_16x16x64_bf16 v[196:199], v[144:147], a[168:175], %[idx]\n\t" \
          "v_cndmask_b32 v208, v208, v212, %[twin]\n\t" \
          "v_cndmask_b32 v209, v209, v213, %[twin]\n\t" \
          "v_smfmac_f32_16x16x64_bf16 v[192:195], v[148:151], a[176:183], %[idx]\n\t" \
          "ds_write_b64 %[own], v[208:209] offset:38912\n\t" \
          "v_cndmask_b32 v210, v210, v214, %[twin]\n\t" \
          "v_cndmask_b32 v211, v211, v215, %[twin]\n\t" \
          "v_smfmac_f32_16x16x64_bf16 v[196:199], v[148:151], a[184:191], %[idx]\n\t" \
          "ds_write_b64 %[own], v[210:211] offset:38920\n\t" \
          "v_smfmac_f32_16x16x64_bf16 v[192:195], v[152:155], a[192:199], %[idx]\n\t" \
          "v_mul_f32 v212, %[qs], v208\n\t" \
          "v_smfmac_f32_16x16x64_bf16 v[196:199], v[152:155], a[200:207], %[idx]\n\t" \
          "buffer_load_dword v232, %[ho], %[rsm], %[vb] offen sc1\n\t" \
          "v_mul_f32 v213, %[qs], v209\n\t" \
          "v_smfmac_f32_16x16x64_bf16 v[192:195], v[156:159], a[208:215], %[idx]\n\t" \
          "buffer_load_dword v234, %[ho], %[rsm], %[vb] offen offset:256 sc1\n\t" \
          "v_mul_f32 v214, %[qs], v210\n\t" \
          "v_smfmac_f32_16x16x64_bf16 v[196:199], v[156:159], a[216:223], %[idx]\n\t" \
          "s_waitcnt lgkmcnt(0)\n\t" \
          "s_barrier\n\t" \
          "v_mul_f32 v215, %[qs], v211\n\t" \
          "v_smfmac_f32_16x16x64_bf16 v[192:195], v[160:163], a[224:231], %[idx]\n\t" \
          "ds_read_b128 v[138:141], %[p1] offset:37888\n\t" \
          "v_smfmac_f32_16x16x64_bf16 v[196:199], v[160:163], a[232:239], %[idx]\n\t" \
          "ds_read_b128 v[216:219], %[p1] offset:38912\n\t" \
          "v_smfmac_f32_16x16x64_bf16 v[192:195], v[164:167], a[240:247], %[idx]\n\t" \
          "ds_read_b128 v[220:223], %[p2] offset:38912\n\t" \
          "v_smfmac_f32_16x16x64_bf16 v[196:199], v[164:167], a[248:255], %[idx]\n\t" \
          "ds_read_b128 v[200:203], %[p3] offset:38912\n\t" \
          "s_nop 0\n\t" \
          "s_waitcnt lgkmcnt(2)\n\t" \
          "v_cvt_pk_bf16_f32 v136, v192, v193\n\t" \
          "v_cvt_pk_bf16_f32 v137, v194, v195\n\t" \
          "v_fmac_f32 v212, %[qs], v216\n\t" \
          "v_fmac_f32 v213, %[qs], v217\n\t" \
          "v_cvt_pk_bf16_f32 v142, v196, v197\n\t" \
          "v_cvt_pk_bf16_f32 v143, v198, v199\n\t" \
          "v_mfma_f32_16x16x32_bf16 v[144:147], v[136:139], %[jk], 0\n\t" \
          "v_fmac_f32 v214, %[qs], v218\n\t" \
          "v_mfma_f32_16x16x32_bf16 v[148:151], v[140:143], %[jk], 0\n\t" \
          "v_fmac_f32 v215, %[qs], v219\n\t" \
          "s_waitcnt lgkmcnt(0)\n\t" \
          "v_fmac_f32 v212, %[qs], v220\n\t" \
          "v_fmac_f32 v213, %[qs], v221\n\t" \
          "v_cvt_pk_bf16_f32 v152, v144, v145\n\t" \
          "v_cvt_pk_bf16_f32 v153, v146, v147\n\t" \
          "v_fmac_f32 v214, %[qs], v222\n\t" \
          "v_fmac_f32 v215, %[qs], v223\n\t" \
          "v_cvt_pk_bf16_f32 v154, v148, v149\n\t" \
          "v_cvt_pk_bf16_f32 v155, v150, v151\n\t" \
          "v_pk_fma_f32 v[212:213], v[200:201], %[qs2], v[212:213] op_sel_hi:[1,0,1]\n\t" \
          "v_pk_fma_f32 v[214:215], v[202:203], %[qs2], v[214:215] op_sel_hi:[1,0,1]\n\t" \
          "v_mfma_f32_16x16x32_bf16 v[156:159], v[152:155], v[176:179], 0\n\t" \
          "v_exp_f32 v180, v212\n\t" \
          "v_mfma_f32_16x16x32_bf16 v[196:199], v[168:171], v[168:171], 0\n\t" \
          "v_exp_f32 v181, v213\n\t" \
          "v_mfma_f32_16x16x32_bf16 v[196:199], v[168:171], v[168:171], 0\n\t" \
          "ds_write_b64 %[own], v[156:157] offset:39936\n\t" \
          "v_exp_f32 v182, v214\n\t" \
          "v_mov_b32 v137, 0\n\t" \
          "v_exp_f32 v183, v215\n\t" \
          "v_cvt_pk_bf16_f32 v172, v180, v181\n\t" \
          "v_cvt_pk_bf16_f32 v173, v182, v183\n\t" \
          "v_pk_mul_f32 v[160:161], v[156:157], %[qs2] op_sel_hi:[1,0]\n\t" \
          "v_mov_b32_dpp v174, v172 quad_perm:[2,3,0,1] row_mask:0xf bank_mask:0xf\n\t" \
          "v_mov_b32_dpp v175, v173 quad_perm:[2,3,0,1] row_mask:0xf bank_mask:0xf\n\t" \
          "s_nop 0\n\t" \
          "s_waitcnt lgkmcnt(0)\n\t" \
          "v_mfma_f32_16x16x32_bf16 v[184:187], v[172:175], v[168:171], 0\n\t" \
          "s_barrier\n\t" \
          "ds_read_b64 v[144:145], %[p1] offset:39936\n\t" \
          "ds_read_b64 v[146:147], %[p2] offset:39936\n\t" \
          "v_mfma_f32_16x16x32_bf16 v[188:191], v[172:175], v[224:227], 0\n\t" \
          "ds_read_b64 v[148:149], %[p3] offset:39936\n\t" \
          "v_mfma_f32_16x16x32_bf16 v[192:195], v[172:175], v[228:231], 0\n\t" \
          "s_waitcnt lgkmcnt(2)\n\t" \
          "v_fmac_f32 v160, %[qs], v144\n\t" \
          "v_fmac_f32 v161, %[qs], v145\n\t" \
          "s_waitcnt lgkmcnt(1)\n\t" \
          "v_fmac_f32 v160, %[qs], v146\n\t" \
          "v_fmac_f32 v161, %[qs], v147\n\t" \
          "s_waitcnt lgkmcnt(0)\n\t" \
          "v_fmac_f32 v160, %[qs], v148\n\t" \
          "v_fmac_f32 v161, %[qs], v149\n\t" \
          "v_exp_f32 v162, v160\n\t" \
          "v_exp_f32 v163, v161\n\t" \
          "v_max_f32 v182, v160, v161\n\t" \
          "v_cvt_pk_bf16_f32 v136, v162, v163\n\t" \
          "v_cmp_nge_f32 vcc, %[th], v182\n\t" \
          "s_waitcnt vmcnt(0)\n\t" \
          "v_mfma_f32_16x16x16_bf16 v[164:167], v[136:137], v[168:169], v[184:187]\n\t" \
          "v_max_f32 v180, v184, v185\n\t" \
          "v_min_f32 v181, v184, v185\n\t" \
          "v_mfma_f32_16x16x16_bf16 v[188:191], v[136:137], v[232:233], v[188:191]\n\t" \
          "v_cmp_nge_f32 %[b2], %[hi], v180\n\t" \
          "v_cmp_nle_f32 %[b3], %[lo], v181\n\t" \
          "v_mfma_f32_16x16x16_bf16 v[192:195], v[136:137], v[234:235], v[192:195]\n\t" \
          "v_max_u32 v183, v232, v234\n\t" \
          "v_cmp_eq_u32 %[bad], -1, v183\n\t" \
          "v_rcp_f32 v162, v164\n\t" \
          "s_or_b64 %[b2], %[b2], %[b3]\n\t" \
          "v_rcp_f32 v163, v165\n\t" \
          "s_or_b64 %[b2], %[b2], vcc\n\t" \
          "v_pk_mul_f32 v[156:157], v[188:189], v[162:163]\n\t" \
          "s_or_b64 %[bad], %[bad], %[b2]\n\t" \
          "s_cselect_b64 exec, 0, -1\n\t" \
          "v_pk_mul_f32 v[158:159], v[192:193], v[162:163]\n\t" \
          "s_nop 0\n\t" \
          "v_cvt_pk_bf16_f32 v156, v156, v158\n\t" \
          "v_cvt_pk_bf16_f32 v157, v157, v159\n\t" \
          "v_min_u32 v156, -2, v156\n\t" \
          "v_min_u32 v157, -2, v157\n\t" \
          "buffer_store_dword v156, %[ho], %[rsm], %[ob] offen sc1\n\t" \
          "buffer_store_dword v157, %[ho], %[rsm], %[ob] offen offset:256 sc1\n\t" \
          "s_memrealtime %[po]\n\t" \
          "s_mov_b64 exec, -1\n\t" \
          "s_sleep 1\n\t" \
          "buffer_load_dword %[al], %[alo], %[rsm], %[mb] offen sc1\n\t" \
          "s_waitcnt lgkmcnt(0)\n\t"
constexpr int QK_SX = 12, QK_SM = 24, QK_KM = 16, QK_VX = 24, QK_KV = 32768, QK_EX = 36864;
#define VO_BODY \
          "s_mov_b64 %[en], -1\n\t" \
          "s_cmp_eq_u32 %[pw], 0\n\t" \
          "s_cbranch_scc1 Lo_c%=\n\t" \
          "v_mov_b64 v[224:225], 0\n\t" \
          "v_mov_b64 v[226:227], 0\n\t" \
          "v_mov_b64 v[228:229], 0\n\t" \
          "v_mov_b64 v[230:231], 0\n\t" \
          "v_mov_b64 v[232:233], 0\n\t" \
          "v_mov_b64 v[234:235], 0\n\t" \
          "v_mov_b64 v[236:237], 0\n\t" \
          "v_mov_b64 v[238:239], 0\n\t" \
          "v_mov_b64 v[176:177], 0\n\t" \
          "v_mov_b64 v[178:179], 0\n\t" \
          "v_mov_b64 v[180:181], 0\n\t" \
          "v_mov_b64 v[182:183], 0\n\t" \
          "v_mov_b64 v[184:185], 0\n\t" \
          "v_mov_b64 v[186:187], 0\n\t" \
          "v_mov_b64 v[188:189], 0\n\t" \
          "v_mov_b64 v[190:191], 0\n\t" \
          "s_cmp_eq_u32 %[s1], 0\n\t" \
          "s_cbranch_scc1 Lv_n%=\n\t" \
          "s_branch Lo_z%=\n\t" \
          "Lo_f0_%=:\n\t" \
          "buffer_load_dwordx4 v[192:195], %[oo0], %[rsm], %[ob] offen sc1\n\t" \
          "Lo_f1_%=:\n\t" \
          "buffer_load_dwordx4 v[196:199], %[oo1], %[rsm], %[ob] offen sc1\n\t" \
          "Lo_f2_%=:\n\t" \
          "buffer_load_dwordx4 v[200:203], %[oo2], %[rsm], %[ob] offen sc1\n\t" \
          "Lo_f3_%=:\n\t" \
          "buffer_load_dwordx4 v[204:207], %[oo3], %[rsm], %[ob] offen sc1\n\t" \
          "Lo_f4_%=:\n\t" \
          "buffer_load_dwordx4 v[208:211], %[oo4], %[rsm], %[ob] offen sc1\n\t" \
          "Lo_f5_%=:\n\t" \
          "buffer_load_dwordx4 v[212:215], %[oo5], %[rsm], %[ob] offen sc1\n\t" \
          "Lo_f6_%=:\n\t" \
          "buffer_load_dwordx4 v[216:219], %[oo6], %[rsm], %[ob] offen sc1\n\t" \
          "Lo_f7_%=:\n\t" \
          "buffer_load_dwordx4 v[220:223], %[oo7], %[rsm], %[ob] offen sc1\n\t" \
          "v_mov_b32 v242, 0x10000\n\t" \
          "s_mov_b64 exec, %[alx]\n\t" \
          "buffer_store_dword v242, %[l4], %[rsm], %[als] offen sc1\n\t" \
          "s_mov_b64 exec, -1\n\t" \
          "v_mov_b32 v243, %[s1]\n\t" \
          "ds_write_b32 %[fa], v243\n\t" \
          "v_mov_b64 v[224:225], 0\n\t" \
          "v_mov_b64 v[226:227], 0\n\t" \
          "v_mov_b64 v[228:229], 0\n\t" \
          "v_mov_b64 v[230:231], 0\n\t" \
          "v_mov_b64 v[232:233], 0\n\t" \
          "v_mov_b64 v[234:235], 0\n\t" \
          "v_mov_b64 v[236:237], 0\n\t" \
          "v_mov_b64 v[238:239], 0\n\t" \
          "s_nop 1\n\t" \
          "s_mov_b32 %[cnt], 0\n\t" \
          "s_waitcnt vmcnt(7)\n\t" \
          "v_max3_u32 v240, v192, v193, v194\n\t" \
          "v_max_u32 v240, v240, v195\n\t" \
          "v_cmp_eq_u32 vcc, -1, v240\n\t" \
          "s_cbranch_vccnz Lo_p0_%=\n\t" \
          "Lo_k0_%=:\n\t" \
          "v_smfmac_f32_16x16x64_bf16 v[224:227], v[192:195], a[0:7], %[idx]\n\t" \
          "v_smfmac_f32_16x16x64_bf16 v[228:231], v[192:195], a[8:15], %[idx]\n\t" \
          "v_smfmac_f32_16x16x64_bf16 v[232:235], v[192:195], a[16:23], %[idx]\n\t" \
          "v_smfmac_f32_16x16x64_bf16 v[236:239], v[192:195], a[24:31], %[idx]\n\t" \
          "s_waitcnt vmcnt(6)\n\t" \
          "v_max3_u32 v240, v196, v197, v198\n\t" \
          "v_max_u32 v240, v240, v199\n\t" \
          "v_cmp_eq_u32 vcc, -1, v240\n\t" \
          "s_cbranch_vccnz Lo_p1_%=\n\t" \
          "Lo_k1_%=:\n\t" \
          "v_smfmac_f32_16x16x64_bf16 v[224:227], v[196:199], a[32:39], %[idx]\n\t" \
          "v_smfmac_f32_16x16x64_bf16 v[228:231], v[196:199], a[40:47], %[idx]\n\t" \
          "v_smfmac_f32_16x16x64_bf16 v[232:235], v[196:199], a[48:55], %[idx]\n\t" \
          "v_smfmac_f32_16x16x64_bf16 v[236:239], v[196:199], a[56:63], %[idx]\n\t" \
          "s_waitcnt vmcnt(5)\n\t" \
          "v_max3_u32 v240, v200, v201, v202\n\t" \
          "v_max_u32 v240, v240, v203\n\t" \
          "v_cmp_eq_u32 vcc, -1, v240\n\t" \
          "s_cbranch_vccnz Lo_p2_%=\n\t" \
          "Lo_k2_%=:\n\t" \
          "v_smfmac_f32_16x16x64_bf16 v[224:227], v[200:203], a[64:71], %[idx]\n\t" \
          "v_smfmac_f32_16x16x64_bf16 v[228:231], v[200:203], a[72:79], %[idx]\n\t" \
          "v_smfmac_f32_16x16x64_bf16 v[232:235], v[200:203], a[80:87], %[idx]\n\t" \
          "v_smfmac_f32_16x16x64_bf16 v[236:239], v[200:203], a[88:95], %[idx]\n\t" \
          "s_waitcnt vmcnt(4)\n\t" \
          "v_max3_u32 v240, v204, v205, v206\n\t" \
          "v_max_u32 v240, v240, v207\n\t" \
          "v_cmp_eq_u32 vcc, -1, v240\n\t" \
          "s_cbranch_vccnz Lo_p3_%=\n\t" \
          "Lo_k3_%=:\n\t" \
          "v_smfmac_f32_16x16x64_bf16 v[224:227], v[204:207], a[96:103], %[idx]\n\t" \
          "v_smfmac_f32_16x16x64_bf16 v[228:231], v[204:207], a[104:111], %[idx]\n\t" \
          "v_smfmac_f32_16x16x64_bf16 v[232:235], v[204:207], a[112:119], %[idx]\n\t" \
          "v_smfmac_f32_16x16x64_bf16 v[236:239], v[204:207], a[120:127], %[idx]\n\t" \
          "s_waitcnt vmcnt(3)\n\t" \
          "v_max3_u32 v240, v208, v209, v210\n\t" \
          "v_max_u32 v240, v240, v211\n\t" \
          "v_cmp_eq_u32 vcc, -1, v240\n\t" \
          "s_cbranch_vccnz Lo_p4_%=\n\t" \
          "Lo_k4_%=:\n\t" \
          "v_smfmac_f32_16x16x64_bf16 v[224:227], v[208:211], a[128:135], %[idx]\n\t" \
          "v_smfmac_f32_16x16x64_bf16 v[228:231], v[208:211], a[136:143], %[idx]\n\t" \
          "v_smfmac_f32_16x16x64_bf16 v[232:235], v[208:211], a[144:151], %[idx]\n\t" \
          "v_smfmac_f32_16x16x64_bf16 v[236:239], v[208:211], a[152:159], %[idx]\n\t" \
          "s_waitcnt vmcnt(2)\n\t" \
          "v_max3_u32 v240, v212, v213, v214\n\t" \
          "v_max_u32 v240, v240, v215\n\t" \
          "v_cmp_eq_u32 vcc, -1, v240\n\t" \
          "s_cbranch_vccnz Lo_p5_%=\n\t" \
          "Lo_k5_%=:\n\t" \
          "v_smfmac_f32_16x16x64_bf16 v[224:227], v[212:215], a[160:167], %[idx]\n\t" \
          "v_smfmac_f32_16x16x64_bf16 v[228:231], v[212:215], a[168:175], %[idx]\n\t" \
          "v_smfmac_f32_16x16x64_bf16 v[232:235], v[212:215], a[176:183], %[idx]\n\t" \
          "v_smfmac_f32_16x16x64_bf16 v[236:239], v[212:215], a[184:191], %[idx]\n\t" \
          "s_waitcnt vmcnt(1)\n\t" \
          "v_max3_u32 v240, v216, v217, v218\n\t" \
          "v_max_u32 v240, v240, v219\n\t" \
          "v_cmp_eq_u32 vcc, -1, v240\n\t" \
          "s_cbranch_vccnz Lo_p6_%=\n\t" \
          "Lo_k6_%=:\n\t" \
          "v_smfmac_f32_16x16x64_bf16 v[224:227], v[216:219], a[192:199], %[idx]\n\t" \
          "v_smfmac_f32_16x16x64_bf16 v[228:231], v[216:219], a[200:207], %[idx]\n\t" \
          "v_smfmac_f32_16x16x64_bf16 v[232:235], v[216:219], a[208:215], %[idx]\n\t" \
          "v_smfmac_f32_16x16x64_bf16 v[236:239], v[216:219], a[216:223], %[idx]\n\t" \
          "s_waitcnt vmcnt(0)\n\t" \
          "v_max3_u32 v240, v220, v221, v222\n\t" \
          "v_max_u32 v240, v240, v223\n\t" \
          "v_cmp_eq_u32 vcc, -1, v240\n\t" \
          "s_cbranch_vccnz Lo_p7_%=\n\t" \
          "Lo_k7_%=:\n\t" \
          "v_smfmac_f32_16x16x64_bf16 v[224:227], v[220:223], a[224:231], %[idx]\n\t" \
          "v_smfmac_f32_16x16x64_bf16 v[228:231], v[220:223], a[232:239], %[idx]\n\t" \
          "v_smfmac_f32_16x16x64_bf16 v[232:235], v[220:223], a[240:247], %[idx]\n\t" \
          "v_smfmac_f32_16x16x64_bf16 v[236:239], v[220:223], a[248:255], %[idx]\n\t" \
          "s_nop 7\n\t" \
          "v_add_f32 v242, v224, v225\n\t" \
          "v_add_f32 v244, v234, v235\n\t" \
          "s_cmp_lg_u32 %[w], 0\n\t" \
          "s_waitcnt lgkmcnt(0)\n\t" \
          "s_branch Lo_tail%=\n\t" \
          "Lo_p0_%=:\n\t" \
          "s_cmp_lt_i32 %[cnt], 0\n\t" \
          "s_cbranch_scc1 Lo_k0_%=\n\t" \
          "s_add_u32 %[cnt], %[cnt], 1\n\t" \
          "s_and_b32 vcc_lo, %[cnt], 3\n\t" \
          "s_cbranch_scc1 Lo_q0_%=\n\t" \
          "s_getpc_b64 s[48:49]\n\t" \
          "s_branch Lo_spin%=\n\t" \
          "Lo_q0_%=:\n\t" \
          "buffer_load_dwordx4 v[192:195], %[oo0], %[rsm], %[ob] offen sc1\n\t" \
          "s_waitcnt vmcnt(0)\n\t" \
          "v_max3_u32 v240, v192, v193, v194\n\t" \
          "v_max_u32 v240, v240, v195\n\t" \
          "v_cmp_eq_u32 vcc, -1, v240\n\t" \
          "s_cbranch_vccnz Lo_p0_%=\n\t" \
          "buffer_load_dwordx4 v[196:199], %[oo1], %[rsm], %[ob] offen sc1\n\t" \
          "buffer_load_dwordx4 v[200:203], %[oo2], %[rsm], %[ob] offen sc1\n\t" \
          "buffer_load_dwordx4 v[204:207], %[oo3], %[rsm], %[ob] offen sc1\n\t" \
          "buffer_load_dwordx4 v[208:211], %[oo4], %[rsm], %[ob] offen sc1\n\t" \
          "buffer_load_dwordx4 v[212:215], %[oo5], %[rsm], %[ob] offen sc1\n\t" \
          "buffer_load_dwordx4 v[216:219], %[oo6], %[rsm], %[ob] offen sc1\n\t" \
          "buffer_load_dwordx4 v[220:223], %[oo7], %[rsm], %[ob] offen sc1\n\t" \
          "s_branch Lo_k0_%=\n\t" \
          "Lo_p1_%=:\n\t" \
          "s_cmp_lt_i32 %[cnt], 0\n\t" \
          "s_cbranch_scc1 Lo_k1_%=\n\t" \
          "s_add_u32 %[cnt], %[cnt], 1\n\t" \
          "s_and_b32 vcc_lo, %[cnt], 3\n\t" \
          "s_cbranch_scc1 Lo_q1_%=\n\t" \
          "s_getpc_b64 s[48:49]\n\t" \
          "s_branch Lo_spin%=\n\t" \
          "Lo_q1_%=:\n\t" \
          "buffer_load_dwordx4 v[196:199], %[oo1], %[rsm], %[ob] offen sc1\n\t" \
          "s_waitcnt vmcnt(0)\n\t" \
          "v_max3_u32 v240, v196, v197, v198\n\t" \
          "v_max_u32 v240, v240, v199\n\t" \
          "v_cmp_eq_u32 vcc, -1, v240\n\t" \
          "s_cbranch_vccnz Lo_p1_%=\n\t" \
          "buffer_load_dwordx4 v[200:203], %[oo2], %[rsm], %[ob] offen sc1\n\t" \
          "buffer_load_dwordx4 v[204:207], %[oo3], %[rsm], %[ob] offen sc1\n\t" \
          "buffer_load_dwordx4 v[208:211], %[oo4], %[rsm], %[ob] offen sc1\n\t" \
          "buffer_load_dwordx4 v[212:215], %[oo5], %[rsm], %[ob] offen sc1\n\t" \
          "buffer_load_dwordx4 v[216:219], %[oo6], %[rsm], %[ob] offen sc1\n\t" \
          "buffer_load_dwordx4 v[220:223], %[oo7], %[rsm], %[ob] offen sc1\n\t" \
          "s_branch Lo_k1_%=\n\t" \
          "Lo_p2_%=:\n\t" \
          "s_cmp_lt_i32 %[cnt], 0\n\t" \
          "s_cbranch_scc1 Lo_k2_%=\n\t" \
          "s_add_u32 %[cnt], %[cnt], 1\n\t" \
          "s_and_b32 vcc_lo, %[cnt], 3\n\t" \
          "s_cbranch_scc1 Lo_q2_%=\n\t" \
          "s_getpc_b64 s[48:49]\n\t" \
          "s_branch Lo_spin%=\n\t" \
          "Lo_q2_%=:\n\t" \
          "buffer_load_dwordx4 v[200:203], %[oo2], %[rsm], %[ob] offen sc1\n\t" \
          "s_waitcnt vmcnt(0)\n\t" \
          "v_max3_u32 v240, v200, v201, v202\n\t" \
          "v_max_u32 v240, v240, v203\n\t" \
          "v_cmp_eq_u32 vcc, -1, v240\n\t" \
          "s_cbranch_vccnz Lo_p2_%=\n\t" \
          "buffer_load_dwordx4 v[204:207], %[oo3], %[rsm], %[ob] offen sc1\n\t" \
          "buffer_load_dwordx4 v[208:211], %[oo4], %[rsm], %[ob] offen sc1\n\t" \
          "buffer_load_dwordx4 v[212:215], %[oo5], %[rsm], %[ob] offen sc1\n\t" \
          "buffer_load_dwordx4 v[216:219], %[oo6], %[rsm], %[ob] offen sc1\n\t" \
          "buffer_load_dwordx4 v[220:223], %[oo7], %[rsm], %[ob] offen sc1\n\t" \
          "s_branch Lo_k2_%=\n\t" \
          "Lo_p3_%=:\n\t" \
          "s_cmp_lt_i32 %[cnt], 0\n\t" \
          "s_cbranch_scc1 Lo_k3_%=\n\t" \
          "s_add_u32 %[cnt], %[cnt], 1\n\t" \
          "s_and_b32 vcc_lo, %[cnt], 3\n\t" \
          "s_cbranch_scc1 Lo_q3_%=\n\t" \
          "s_getpc_b64 s[48:49]\n\t" \
          "s_branch Lo_spin%=\n\t" \
          "Lo_q3_%=:\n\t" \
          "buffer_load_dwordx4 v[204:207], %[oo3], %[rsm], %[ob] offen sc1\n\t" \
          "s_waitcnt vmcnt(0)\n\t" \
          "v_max3_u32 v240, v204, v205, v206\n\t" \
          "v_max_u32 v240, v240, v207\n\t" \
          "v_cmp_eq_u32 vcc, -1, v240\n\t" \
          "s_cbranch_vccnz Lo_p3_%=\n\t" \
          "buffer_load_dwordx4 v[208:211], %[oo4], %[rsm], %[ob] offen sc1\n\t" \
          "buffer_load_dwordx4 v[212:215], %[oo5], %[rsm], %[ob] offen sc1\n\t" \
          "buffer_load_dwordx4 v[216:219], %[oo6], %[rsm], %[ob] offen sc1\n\t" \
          "buffer_load_dwordx4 v[220:223], %[oo7], %[rsm], %[ob] offen sc1\n\t" \
          "s_branch Lo_k3_%=\n\t" \
          "Lo_p4_%=:\n\t" \
          "s_cmp_lt_i32 %[cnt], 0\n\t" \
          "s_cbranch_scc1 Lo_k4_%=\n\t" \
          "s_add_u32 %[cnt], %[cnt], 1\n\t" \
          "s_and_b32 vcc_lo, %[cnt], 3\n\t" \
          "s_cbranch_scc1 Lo_q4_%=\n\t" \
          "s_getpc_b64 s[48:49]\n\t" \
          "s_branch Lo_spin%=\n\t" \
          "Lo_q4_%=:\n\t" \
          "buffer_load_dwordx4 v[208:211], %[oo4], %[rsm], %[ob] offen sc1\n\t" \
          "s_waitcnt vmcnt(0)\n\t" \
          "v_max3_u32 v240, v208, v209, v210\n\t" \
          "v_max_u32 v240, v240, v211\n\t" \
          "v_cmp_eq_u32 vcc, -1, v240\n\t" \
          "s_cbranch_vccnz Lo_p4_%=\n\t" \
          "buffer_load_dwordx4 v[212:215], %[oo5], %[rsm], %[ob] offen sc1\n\t" \
          "buffer_load_dwordx4 v[216:219], %[oo6], %[rsm], %[ob] offen sc1\n\t" \
          "buffer_load_dwordx4 v[220:223], %[oo7], %[rsm], %[ob] offen sc1\n\t" \
          "s_branch Lo_k4_%=\n\t" \
          "Lo_p5_%=:\n\t" \
          "s_cmp_lt_i32 %[cnt], 0\n\t" \
          "s_cbranch_scc1 Lo_k5_%=\n\t" \
          "s_add_u32 %[cnt], %[cnt], 1\n\t" \
          "s_and_b32 vcc_lo, %[cnt], 3\n\t" \
          "s_cbranch_scc1 Lo_q5_%=\n\t" \
          "s_getpc_b64 s[48:49]\n\t" \
          "s_branch Lo_spin%=\n\t" \
          "Lo_q5_%=:\n\t" \
          "buffer_load_dwordx4 v[212:215], %[oo5], %[rsm], %[ob] offen sc1\n\t" \
          "s_waitcnt vmcnt(0)\n\t" \
          "v_max3_u32 v240, v212, v213, v214\n\t" \
          "v_max_u32 v240, v240, v215\n\t" \
          "v_cmp_eq_u32 vcc, -1, v240\n\t" \
          "s_cbranch_vccnz Lo_p5_%=\n\t" \
          "buffer_load_dwordx4 v[216:219], %[oo6], %[rsm], %[ob] offen sc1\n\t" \
          "buffer_load_dwordx4 v[220:223], %[oo7], %[rsm], %[ob] offen sc1\n\t" \
          "s_branch Lo_k5_%=\n\t" \
          "Lo_p6_%=:\n\t" \
          "s_cmp_lt_i32 %[cnt], 0\n\t" \
          "s_cbranch_scc1 Lo_k6_%=\n\t" \
          "s_add_u32 %[cnt], %[cnt], 1\n\t" \
          "s_and_b32 vcc_lo, %[cnt], 3\n\t" \
          "s_cbranch_scc1 Lo_q6_%=\n\t" \
          "s_getpc_b64 s[48:49]\n\t" \
          "s_branch Lo_spin%=\n\t" \
          "Lo_q6_%=:\n\t" \
          "buffer_load_dwordx4 v[216:219], %[oo6], %[rsm], %[ob] offen sc1\n\t" \
          "s_waitcnt vmcnt(0)\n\t" \
          "v_max3_u32 v240, v216, v217, v218\n\t" \
          "v_max_u32 v240, v240, v219\n\t" \
          "v_cmp_eq_u32 vcc, -1, v240\n\t" \
          "s_cbranch_vccnz Lo_p6_%=\n\t" \
          "buffer_load_dwordx4 v[220:223], %[oo7], %[rsm], %[ob] offen sc1\n\t" \
          "s_branch Lo_k6_%=\n\t" \
          "Lo_p7_%=:\n\t" \
          "s_cmp_lt_i32 %[cnt], 0\n\t" \
          "s_cbranch_scc1 Lo_k7_%=\n\t" \
          "s_add_u32 %[cnt], %[cnt], 1\n\t" \
          "s_and_b32 vcc_lo, %[cnt], 3\n\t" \
          "s_cbranch_scc1 Lo_q7_%=\n\t" \
          "s_getpc_b64 s[48:49]\n\t" \
          "s_branch Lo_spin%=\n\t" \
          "Lo_q7_%=:\n\t" \
          "buffer_load_dwordx4 v[220:223], %[oo7], %[rsm], %[ob] offen sc1\n\t" \
          "s_waitcnt vmcnt(0)\n\t" \
          "v_max3_u32 v240, v220, v221, v222\n\t" \
          "v_max_u32 v240, v240, v223\n\t" \
          "v_cmp_eq_u32 vcc, -1, v240\n\t" \
          "s_cbranch_vccnz Lo_p7_%=\n\t" \
          "s_branch Lo_k7_%=\n\t" \
          "Lo_spin%=:\n\t" \
          "buffer_load_dword v241, off, %[rsc], %[tmo] sc1\n\t" \
          "s_add_u32 s48, s48, 4\n\t" \
          "s_addc_u32 s49, s49, 0\n\t" \
          "s_waitcnt vmcnt(0)\n\t" \
          "v_readfirstlane_b32 vcc_lo, v241\n\t" \
          "s_lshr_b32 vcc_hi, %[cnt], 16\n\t" \
          "s_or_b32 vcc_lo, vcc_lo, vcc_hi\n\t" \
          "s_cbranch_scc0 Lo_back%=\n\t" \
          "v_mov_b32 v241, 1\n\t" \
          "buffer_store_dword v241, off, %[rsc], %[tmo] sc1\n\t" \
          "s_mov_b32 %[cnt], -1\n\t" \
          "Lo_back%=:\n\t" \
          "s_setpc_b64 s[48:49]\n\t" \
          "Lo_fa%=:\n\t" \
          "s_add_u32 %[ta], %[pm], %[shf]\n\t" \
          "s_branch Lo_fb%=\n\t" \
          "Lo_ka%=:\n\t" \
          "v_add_u32 v252, %[ta], v252\n\t" \
          "v_min_u32 v252, -2, v252\n\t" \
          "s_mov_b64 exec, 1\n\t" \
          "buffer_store_dwordx2 v[252:253], off, %[rsm], %[rco] sc1\n\t" \
          "v_mov_b32 v248, %[pm]\n\t" \
          "v_mov_b32 v249, %[t]\n\t" \
          "v_mov_b32 v250, %[fw]\n\t" \
          "v_mov_b32 v251, %[ta]\n\t" \
          "ds_write_b128 %[fa], v[248:251] offset:144\n\t" \
          "s_mov_b64 exec, -1\n\t" \
          "s_branch Lo_kb%=\n\t" \
          "Lv_fail%=:\n\t" \
          "buffer_load_dwordx4 v[192:195], %[ga0], %[rsm], %[mbn] offen sc1\n\t" \
          "buffer_load_dwordx4 v[196:199], %[ga1], %[rsm], %[mbn] offen sc1\n\t" \
          "buffer_load_dwordx4 v[200:203], %[ga2], %[rsm], %[mbn] offen sc1\n\t" \
          "buffer_load_dwordx4 v[204:207], %[ga3], %[rsm], %[mbn] offen sc1\n\t" \
          "s_nop 3\n\t" \
          "v_mov_b64 v[176:177], 0\n\t" \
          "v_mov_b64 v[178:179], 0\n\t" \
          "v_mov_b64 v[180:181], 0\n\t" \
          "v_mov_b64 v[182:183], 0\n\t" \
          "v_mov_b64 v[184:185], 0\n\t" \
          "v_mov_b64 v[186:187], 0\n\t" \
          "v_mov_b64 v[188:189], 0\n\t" \
          "v_mov_b64 v[190:191], 0\n\t" \
          "s_mov_b64 %[en], 0\n\t" \
          "s_mov_b32 %[cnt], 0\n\t" \
          "s_waitcnt vmcnt(3)\n\t" \
          "v_max3_u32 v240, v192, v193, v194\n\t" \
          "v_max_u32 v240, v240, v195\n\t" \
          "v_cmp_eq_u32 vcc, -1, v240\n\t" \
          "s_cbranch_vccnz Lv_p0_%=\n\t" \
          "Lv_k0_%=:\n\t" \
          "s_waitcnt vmcnt(2)\n\t" \
          "v_max3_u32 v240, v196, v197, v198\n\t" \
          "v_max_u32 v240, v240, v199\n\t" \
          "v_cmp_eq_u32 vcc, -1, v240\n\t" \
          "s_cbranch_vccnz Lv_p1_%=\n\t" \
          "Lv_k1_%=:\n\t" \
          "s_waitcnt vmcnt(1)\n\t" \
          "v_max3_u32 v240, v200, v201, v202\n\t" \
          "v_max_u32 v240, v240, v203\n\t" \
          "v_cmp_eq_u32 vcc, -1, v240\n\t" \
          "s_cbranch_vccnz Lv_p2_%=\n\t" \
          "Lv_k2_%=:\n\t" \
          "s_waitcnt vmcnt(0)\n\t" \
          "v_max3_u32 v240, v204, v205, v206\n\t" \
          "v_max_u32 v240, v240, v207\n\t" \
          "v_cmp_eq_u32 vcc, -1, v240\n\t" \
          "s_cbranch_vccnz Lv_p3_%=\n\t" \
          "Lv_k3_%=:\n\t" \
          "s_branch Lv_go%=\n\t" \
          "Lv_p0_%=:\n\t" \
          "s_cmp_lt_i32 %[cnt], 0\n\t" \
          "s_cbranch_scc1 Lv_k0_%=\n\t" \
          "s_add_u32 %[cnt], %[cnt], 1\n\t" \
          "s_and_b32 vcc_lo, %[cnt], 3\n\t" \
          "s_cbranch_scc1 Lv_q0_%=\n\t" \
          "s_getpc_b64 s[48:49]\n\t" \
          "s_branch Lv_spin%=\n\t" \
          "Lv_q0_%=:\n\t" \
          "buffer_load_dwordx4 v[192:195], %[ga0], %[rsm], %[mbn] offen sc1\n\t" \
          "s_waitcnt vmcnt(0)\n\t" \
          "v_max3_u32 v240, v192, v193, v194\n\t" \
          "v_max_u32 v240, v240, v195\n\t" \
          "v_cmp_eq_u32 vcc, -1, v240\n\t" \
          "s_cbranch_vccnz Lv_p0_%=\n\t" \
          "buffer_load_dwordx4 v[196:199], %[ga1], %[rsm], %[mbn] offen sc1\n\t" \
          "buffer_load_dwordx4 v[200:203], %[ga2], %[rsm], %[mbn] offen sc1\n\t" \
          "buffer_load_dwordx4 v[204:207], %[ga3], %[rsm], %[mbn] offen sc1\n\t" \
          "s_branch Lv_k0_%=\n\t" \
          "Lv_p1_%=:\n\t" \
          "s_cmp_lt_i32 %[cnt], 0\n\t" \
          "s_cbranch_scc1 Lv_k1_%=\n\t" \
          "s_add_u32 %[cnt], %[cnt], 1\n\t" \
          "s_and_b32 vcc_lo, %[cnt], 3\n\t" \
          "s_cbranch_scc1 Lv_q1_%=\n\t" \
          "s_getpc_b64 s[48:49]\n\t" \
          "s_branch Lv_spin%=\n\t" \
          "Lv_q1_%=:\n\t" \
          "buffer_load_dwordx4 v[196:199], %[ga1], %[rsm], %[mbn] offen sc1\n\t" \
          "s_waitcnt vmcnt(0)\n\t" \
          "v_max3_u32 v240, v196, v197, v198\n\t" \
          "v_max_u32 v240, v240, v199\n\t" \
          "v_cmp_eq_u32 vcc, -1, v240\n\t" \
          "s_cbranch_vccnz Lv_p1_%=\n\t" \
          "buffer_load_dwordx4 v[200:203], %[ga2], %[rsm], %[mbn] offen sc1\n\t" \
          "buffer_load_dwordx4 v[204:207], %[ga3], %[rsm], %[mbn] offen sc1\n\t" \
          "s_branch Lv_k1_%=\n\t" \
          "Lv_p2_%=:\n\t" \
          "s_cmp_lt_i32 %[cnt], 0\n\t" \
          "s_cbranch_scc1 Lv_k2_%=\n\t" \
          "s_add_u32 %[cnt], %[cnt], 1\n\t" \
          "s_and_b32 vcc_lo, %[cnt], 3\n\t" \
          "s_cbranch_scc1 Lv_q2_%=\n\t" \
          "s_getpc_b64 s[48:49]\n\t" \
          "s_branch Lv_spin%=\n\t" \
          "Lv_q2_%=:\n\t" \
          "buffer_load_dwordx4 v[200:203], %[ga2], %[rsm], %[mbn] offen sc1\n\t" \
          "s_waitcnt vmcnt(0)\n\t" \
          "v_max3_u32 v240, v200, v201, v202\n\t" \
          "v_max_u32 v240, v240, v203\n\t" \
          "v_cmp_eq_u32 vcc, -1, v240\n\t" \
          "s_cbranch_vccnz Lv_p2_%=\n\t" \
          "buffer_load_dwordx4 v[204:207], %[ga3], %[rsm], %[mbn] offen sc1\n\t" \
          "s_branch Lv_k2_%=\n\t" \
          "Lv_p3_%=:\n\t" \
          "s_cmp_lt_i32 %[cnt], 0\n\t" \
          "s_cbranch_scc1 Lv_k3_%=\n\t" \
          "s_add_u32 %[cnt], %[cnt], 1\n\t" \
          "s_and_b32 vcc_lo, %[cnt], 3\n\t" \
          "s_cbranch_scc1 Lv_q3_%=\n\t" \
          "s_getpc_b64 s[48:49]\n\t" \
          "s_branch Lv_spin%=\n\t" \
          "Lv_q3_%=:\n\t" \
          "buffer_load_dwordx4 v[204:207], %[ga3], %[rsm], %[mbn] offen sc1\n\t" \
          "s_waitcnt vmcnt(0)\n\t" \
          "v_max3_u32 v240, v204, v205, v206\n\t" \
          "v_max_u32 v240, v240, v207\n\t" \
          "v_cmp_eq_u32 vcc, -1, v240\n\t" \
          "s_cbranch_vccnz Lv_p3_%=\n\t" \
          "s_branch Lv_k3_%=\n\t" \
          "Lv_spin%=:\n\t" \
          "buffer_load_dword v241, off, %[rsc], %[tmo] sc1\n\t" \
          "s_add_u32 s48, s48, 4\n\t" \
          "s_addc_u32 s49, s49, 0\n\t" \
          "s_waitcnt vmcnt(0)\n\t" \
          "v_readfirstlane_b32 vcc_lo, v241\n\t" \
          "s_lshr_b32 vcc_hi, %[cnt], 16\n\t" \
          "s_or_b32 vcc_lo, vcc_lo, vcc_hi\n\t" \
          "s_cbranch_scc0 Lv_back%=\n\t" \
          "v_mov_b32 v241, 1\n\t" \
          "buffer_store_dword v241, off, %[rsc], %[tmo] sc1\n\t" \
          "s_mov_b32 %[cnt], -1\n\t" \
          "Lv_back%=:\n\t" \
          "s_setpc_b64 s[48:49]\n\t" \
          ".p2align 6\n\t" \
          "s_nop 0\n\t" \
          "s_nop 0\n\t" \
          "s_nop 0\n\t" \
          "Lo_al%=:\n\t" \
          "v_subrev_u32 v252, %[t], v255\n\t" \
          "v_bfe_i32 v252, v252, 0, 16\n\t" \
          "v_cndmask_b32 v252, 0, v252, vcc\n\t" \
          "v_cmp_gt_u32 vcc, 1000, v252\n\t" \
          "s_nop 1\n\t" \
          "v_cndmask_b32 v252, 0, v252, vcc\n\t" \
          "s_nop 1\n\t" \
          "v_max_i32_dpp v252, v252, v252 row_ror:8 row_mask:0xf bank_mask:0xf\n\t" \
          "s_nop 1\n\t" \
          "v_max_i32_dpp v252, v252, v252 row_ror:4 row_mask:0xf bank_mask:0xf\n\t" \
          "s_nop 1\n\t" \
          "v_max_i32_dpp v252, v252, v252 row_ror:2 row_mask:0xf bank_mask:0xf\n\t" \
          "s_nop 1\n\t" \
          "v_max_i32_dpp v252, v252, v252 row_ror:1 row_mask:0xf bank_mask:0xf\n\t" \
          "s_nop 0\n\t" \
          "v_readfirstlane_b32 vcc_lo, v252\n\t" \
          "s_add_u32 %[t], %[t], vcc_lo\n\t" \
          "s_memrealtime vcc\n\t" \
          "s_waitcnt lgkmcnt(0)\n\t" \
          "s_sub_i32 vcc_lo, %[t], vcc_lo\n\t" \
          "s_cmp_gt_i32 vcc_lo, %[lk]\n\t" \
          "s_cbranch_scc0 Lo_w%=\n\t" \
          "Lo_s%=:\n\t" \
          "s_sleep 1\n\t" \
          "Lo_c%=:\n\t" \
          "s_memrealtime vcc\n\t" \
          "s_waitcnt lgkmcnt(0)\n\t" \
          "s_sub_i32 vcc_lo, %[t], vcc_lo\n\t" \
          "s_cmp_gt_i32 vcc_lo, %[lk]\n\t" \
          "s_cbranch_scc1 Lo_s%=\n\t" \
          "buffer_load_dword v255, %[al1], %[rsm], %[mb] offen sc1\n\t" \
          "v_mov_b64 v[224:225], 0\n\t" \
          "v_mov_b64 v[226:227], 0\n\t" \
          "v_mov_b64 v[228:229], 0\n\t" \
          "v_mov_b64 v[230:231], 0\n\t" \
          "v_mov_b64 v[232:233], 0\n\t" \
          "v_mov_b64 v[234:235], 0\n\t" \
          "v_mov_b64 v[236:237], 0\n\t" \
          "v_mov_b64 v[238:239], 0\n\t" \
          "v_mov_b64 v[176:177], 0\n\t" \
          "v_mov_b64 v[178:179], 0\n\t" \
          "v_mov_b64 v[180:181], 0\n\t" \
          "v_mov_b64 v[182:183], 0\n\t" \
          "v_mov_b64 v[184:185], 0\n\t" \
          "v_mov_b64 v[186:187], 0\n\t" \
          "v_mov_b64 v[188:189], 0\n\t" \
          "v_mov_b64 v[190:191], 0\n\t" \
          "s_waitcnt vmcnt(0)\n\t" \
          "v_lshrrev_b32 v252, 16, v255\n\t" \
          "v_cmp_eq_u32 vcc, 1, v252\n\t" \
          "s_cbranch_vccnz Lo_al%=\n\t" \
          "Lo_w%=:\n\t" \
          "s_memrealtime s[50:51]\n\t" \
          "s_getpc_b64 s[48:49]\n\t" \
          "Lo_y%=:\n\t" \
          "s_add_u32 s48, s48, Lo_z%=-Lo_y%=\n\t" \
          "s_addc_u32 s49, s49, 0\n\t" \
          "Lo_v%=:\n\t" \
          "s_memrealtime s[52:53]\n\t" \
          "s_waitcnt lgkmcnt(1)\n\t" \
          "s_sub_i32 vcc_lo, %[t], s50\n\t" \
          "s_cmp_gt_i32 vcc_lo, 3\n\t" \
          "s_cbranch_scc0 Lo_x%=\n\t" \
          "s_memrealtime s[50:51]\n\t" \
          "s_waitcnt lgkmcnt(1)\n\t" \
          "s_sub_i32 vcc_lo, %[t], s52\n\t" \
          "s_cmp_gt_i32 vcc_lo, 3\n\t" \
          "s_cbranch_scc0 Lo_x%=\n\t" \
          "s_memrealtime s[52:53]\n\t" \
          "s_waitcnt lgkmcnt(1)\n\t" \
          "s_sub_i32 vcc_lo, %[t], s50\n\t" \
          "s_cmp_gt_i32 vcc_lo, 3\n\t" \
          "s_cbranch_scc0 Lo_x%=\n\t" \
          "s_memrealtime s[50:51]\n\t" \
          "s_waitcnt lgkmcnt(1)\n\t" \
          "s_sub_i32 vcc_lo, %[t], s52\n\t" \
          "s_cmp_gt_i32 vcc_lo, 3\n\t" \
          "s_cbranch_scc0 Lo_x%=\n\t" \
          "s_memrealtime s[52:53]\n\t" \
          "s_waitcnt lgkmcnt(1)\n\t" \
          "s_sub_i32 vcc_lo, %[t], s50\n\t" \
          "s_cmp_gt_i32 vcc_lo, 3\n\t" \
          "s_cbranch_scc0 Lo_x%=\n\t" \
          "s_memrealtime s[50:51]\n\t" \
          "s_waitcnt lgkmcnt(1)\n\t" \
          "s_sub_i32 vcc_lo, %[t], s52\n\t" \
          "s_cmp_gt_i32 vcc_lo, 3\n\t" \
          "s_cbranch_scc0 Lo_x%=\n\t" \
          "s_memrealtime s[52:53]\n\t" \
          "s_waitcnt lgkmcnt(1)\n\t" \
          "s_sub_i32 vcc_lo, %[t], s50\n\t" \
          "s_cmp_gt_i32 vcc_lo, 3\n\t" \
          "s_cbranch_scc0 Lo_x%=\n\t" \
          "s_memrealtime s[50:51]\n\t" \
          "s_waitcnt lgkmcnt(1)\n\t" \
          "s_sub_i32 vcc_lo, %[t], s52\n\t" \
          "s_cmp_gt_i32 vcc_lo, 3\n\t" \
          "s_cbranch_scc0 Lo_x%=\n\t" \
          "s_branch Lo_v%=\n\t" \
          "Lo_x%=:\n\t" \
          "s_max_i32 vcc_lo, vcc_lo, 0\n\t" \
          "s_lshl_b32 vcc_lo, vcc_lo, 2\n\t" \
          "s_sub_u32 s48, s48, vcc_lo\n\t" \
          "s_subb_u32 s49, s49, 0\n\t" \
          "s_setpc_b64 s[48:49]\n\t" \
          "s_nop 5\n\t" \
          "s_nop 5\n\t" \
          "s_nop 5\n\t" \
          "Lo_z%=:\n\t" \
          "s_barrier\n\t" \
          VO_ST0 \
          "buffer_load_dwordx4 v[192:195], %[oo0], %[rsm], %[ob] offen sc1\n\t" \
          "s_nop 9\n\t" \
          "buffer_load_dwordx4 v[196:199], %[oo1], %[rsm], %[ob] offen sc1\n\t" \
          "s_nop 9\n\t" \
          "buffer_load_dwordx4 v[200:203], %[oo2], %[rsm], %[ob] offen sc1\n\t" \
          "s_nop 9\n\t" \
          "buffer_load_dwordx4 v[204:207], %[oo3], %[rsm], %[ob] offen sc1\n\t" \
          "s_nop 9\n\t" \
          "buffer_load_dwordx4 v[208:211], %[oo4], %[rsm], %[ob] offen sc1\n\t" \
          "s_nop 9\n\t" \
          "buffer_load_dwordx4 v[212:215], %[oo5], %[rsm], %[ob] offen sc1\n\t" \
          "s_nop 9\n\t" \
          "buffer_load_dwordx4 v[216:219], %[oo6], %[rsm], %[ob] offen sc1\n\t" \
          "s_nop 9\n\t" \
          "buffer_load_dwordx4 v[220:223], %[oo7], %[rsm], %[ob] offen sc1\n\t" \
          "s_waitcnt vmcnt(7)\n\t" \
          "v_smfmac_f32_16x16x64_bf16 v[224:227], v[192:195], a[0:7], %[idx]\n\t" \
          "v_max3_u32 v240, v192, v193, v194\n\t" \
          "v_smfmac_f32_16x16x64_bf16 v[228:231], v[192:195], a[8:15], %[idx]\n\t" \
          "v_max3_u32 v240, v194, v195, v240\n\t" \
          "v_smfmac_f32_16x16x64_bf16 v[232:235], v[192:195], a[16:23], %[idx]\n\t" \
          "v_cmp_eq_u32 vcc, -1, v240\n\t" \
          "v_smfmac_f32_16x16x64_bf16 v[236:239], v[192:195], a[24:31], %[idx]\n\t" \
          "s_waitcnt vmcnt(6)\n\t" \
          "v_smfmac_f32_16x16x64_bf16 v[224:227], v[196:199], a[32:39], %[idx]\n\t" \
          "s_cbranch_vccnz Lo_f0_%=\n\t" \
          "v_max3_u32 v240, v196, v197, v198\n\t" \
          "v_smfmac_f32_16x16x64_bf16 v[228:231], v[196:199], a[40:47], %[idx]\n\t" \
          "v_max3_u32 v240, v198, v199, v240\n\t" \
          "v_smfmac_f32_16x16x64_bf16 v[232:235], v[196:199], a[48:55], %[idx]\n\t" \
          "v_cmp_eq_u32 vcc, -1, v240\n\t" \
          "v_smfmac_f32_16x16x64_bf16 v[236:239], v[196:199], a[56:63], %[idx]\n\t" \
          "s_waitcnt vmcnt(5)\n\t" \
          "v_smfmac_f32_16x16x64_bf16 v[224:227], v[200:203], a[64:71], %[idx]\n\t" \
          "s_cbranch_vccnz Lo_f1_%=\n\t" \
          "v_max3_u32 v240, v200, v201, v202\n\t" \
          "v_smfmac_f32_16x16x64_bf16 v[228:231], v[200:203], a[72:79], %[idx]\n\t" \
          "v_max3_u32 v240, v202, v203, v240\n\t" \
          "v_smfmac_f32_16x16x64_bf16 v[232:235], v[200:203], a[80:87], %[idx]\n\t" \
          "v_cmp_eq_u32 vcc, -1, v240\n\t" \
          "v_smfmac_f32_16x16x64_bf16 v[236:239], v[200:203], a[88:95], %[idx]\n\t" \
          "s_waitcnt vmcnt(4)\n\t" \
          "v_smfmac_f32_16x16x64_bf16 v[224:227], v[204:207], a[96:103], %[idx]\n\t" \
          "s_cbranch_vccnz Lo_f2_%=\n\t" \
          "v_max3_u32 v240, v204, v205, v206\n\t" \
          "v_smfmac_f32_16x16x64_bf16 v[228:231], v[204:207], a[104:111], %[idx]\n\t" \
          "v_max3_u32 v240, v206, v207, v240\n\t" \
          "v_smfmac_f32_16x16x64_bf16 v[232:235], v[204:207], a[112:119], %[idx]\n\t" \
          "v_cmp_eq_u32 vcc, -1, v240\n\t" \
          "v_smfmac_f32_16x16x64_bf16 v[236:239], v[204:207], a[120:127], %[idx]\n\t" \
          "s_waitcnt vmcnt(3)\n\t" \
          "v_smfmac_f32_16x16x64_bf16 v[224:227], v[208:211], a[128:135], %[idx]\n\t" \
          "s_cbranch_vccnz Lo_f3_%=\n\t" \
          "v_max3_u32 v240, v208, v209, v210\n\t" \
          "v_smfmac_f32_16x16x64_bf16 v[228:231], v[208:211], a[136:143], %[idx]\n\t" \
          "v_max3_u32 v240, v210, v211, v240\n\t" \
          "v_smfmac_f32_16x16x64_bf16 v[232:235], v[208:211], a[144:151], %[idx]\n\t" \
          "v_cmp_eq_u32 vcc, -1, v240\n\t" \
          "v_smfmac_f32_16x16x64_bf16 v[236:239], v[208:211], a[152:159], %[idx]\n\t" \
          "s_waitcnt vmcnt(2)\n\t" \
          "v_smfmac_f32_16x16x64_bf16 v[224:227], v[212:215], a[160:167], %[idx]\n\t" \
          "s_cbranch_vccnz Lo_f4_%=\n\t" \
          "v_max3_u32 v240, v212, v213, v214\n\t" \
          "v_smfmac_f32_16x16x64_bf16 v[228:231], v[212:215], a[168:175], %[idx]\n\t" \
          "v_max3_u32 v240, v214, v215, v240\n\t" \
          "v_smfmac_f32_16x16x64_bf16 v[232:235], v[212:215], a[176:183], %[idx]\n\t" \
          "v_cmp_eq_u32 vcc, -1, v240\n\t" \
          "v_smfmac_f32_16x16x64_bf16 v[236:239], v[212:215], a[184:191], %[idx]\n\t" \
          "s_waitcnt vmcnt(1)\n\t" \
          "v_smfmac_f32_16x16x64_bf16 v[224:227], v[216:219], a[192:199], %[idx]\n\t" \
          "s_cbranch_vccnz Lo_f5_%=\n\t" \
          "v_max3_u32 v240, v216, v217, v218\n\t" \
          "v_smfmac_f32_16x16x64_bf16 v[228:231], v[216:219], a[200:207], %[idx]\n\t" \
          "v_max3_u32 v240, v218, v219, v240\n\t" \
          "v_smfmac_f32_16x16x64_bf16 v[232:235], v[216:219], a[208:215], %[idx]\n\t" \
          "v_cmp_eq_u32 vcc, -1, v240\n\t" \
          "v_smfmac_f32_16x16x64_bf16 v[236:239], v[216:219], a[216:223], %[idx]\n\t" \
          "s_waitcnt vmcnt(0)\n\t" \
          "v_smfmac_f32_16x16x64_bf16 v[224:227], v[220:223], a[224:231], %[idx]\n\t" \
          "s_cbranch_vccnz Lo_f6_%=\n\t" \
          "v_max3_u32 v240, v220, v221, v222\n\t" \
          "v_smfmac_f32_16x16x64_bf16 v[232:235], v[220:223], a[240:247], %[idx]\n\t" \
          "v_max3_u32 v240, v222, v223, v240\n\t" \
          "v_smfmac_f32_16x16x64_bf16 v[228:231], v[220:223], a[232:239], %[idx]\n\t" \
          "v_cmp_eq_u32 vcc, -1, v240\n\t" \
          "v_smfmac_f32_16x16x64_bf16 v[236:239], v[220:223], a[248:255], %[idx]\n\t" \
          VO_ST1 \
          "s_cmp_lg_u32 %[w], 0\n\t" \
          "v_add_f32 v242, v224, v225\n\t" \
          "v_add_f32 v244, v234, v235\n\t" \
          "s_cbranch_vccnz Lo_f7_%=\n\t" \
          "Lo_tail%=:\n\t" \
          "v_add_f32 v243, v228, v229\n\t" \
          "v_add_f32 v242, v242, v244\n\t" \
          "s_nop 0\n\t" \
          "v_add_f32 v243, v243, v238\n\t" \
          "s_cbranch_scc0 Lo_w0%=\n\t" \
          "v_add_f32 v243, v243, v239\n\t" \
          "ds_write_b64 %[rw], v[242:243]\n\t" \
          "s_waitcnt lgkmcnt(0)\n\t" \
          "s_barrier\n\t" \
          "s_cmp_eq_u32 %[w], 3\n\t" \
          "s_cbranch_scc0 Lv_z%=\n\t" \
          "s_sleep 1\n\t" \
          "s_branch Lo_end%=\n\t" \
          "Lo_w0%=:\n\t" \
          "v_add_f32 v243, v243, v239\n\t" \
          "s_barrier\n\t" \
          "ds_read_b64 v[244:245], %[rw] offset:512\n\t" \
          "ds_read_b64 v[246:247], %[rw] offset:1024\n\t" \
          "ds_read_b64 v[248:249], %[rw] offset:1536\n\t" \
          "s_waitcnt lgkmcnt(2)\n\t" \
          "v_pk_add_f32 v[242:243], v[242:243], v[244:245]\n\t" \
          "s_waitcnt lgkmcnt(1)\n\t" \
          "v_pk_add_f32 v[242:243], v[242:243], v[246:247]\n\t" \
          "s_waitcnt lgkmcnt(0)\n\t" \
          "v_pk_add_f32 v[242:243], v[242:243], v[248:249]\n\t" \
          "s_nop 0\n\t" \
          "v_cvt_pk_bf16_f32 v242, v242, v243\n\t" \
          "v_min_u32 v242, -2, v242\n\t" \
          "buffer_store_dword v242, %[mo], %[rsm], %[mbn] offen sc1\n\t" \
          VO_ST3 \
          "s_branch Lv_z%=\n\t" \
          "Lo_end%=:\n\t" \
          VO_ST3 \
          "s_memrealtime vcc\n\t" \
          "ds_read_b32 v241, %[fa]\n\t" \
          "ds_read_b64 v[252:253], %[fa] offset:128\n\t" \
          "s_add_u32 %[ta], %[t], %[d2]\n\t" \
          "s_waitcnt lgkmcnt(0)\n\t" \
          "s_mov_b32 %[pm], vcc_lo\n\t" \
          "v_readfirstlane_b32 %[fw], v241\n\t" \
          "s_add_u32 vcc_lo, vcc_lo, %[mn]\n\t" \
          "s_sub_i32 vcc_hi, vcc_lo, %[ta]\n\t" \
          "s_cmp_gt_i32 vcc_hi, 0\n\t" \
          "s_cselect_b32 %[ta], vcc_lo, %[ta]\n\t" \
          "s_cmp_eq_u32 %[fw], %[s1]\n\t" \
          "s_cbranch_scc1 Lo_fa%=\n\t" \
          "Lo_fb%=:\n\t" \
          "s_cmp_eq_u32 %[c], 0\n\t" \
          "s_cbranch_scc1 Lo_ka%=\n\t" \
          "Lo_kb%=:\n\t" \
          "s_add_u32 %[cnt], %[ta], %[dv]\n\t" \
          "Lv_w%=:\n\t" \
          "s_memrealtime s[50:51]\n\t" \
          "s_getpc_b64 s[48:49]\n\t" \
          "Lv_y%=:\n\t" \
          "s_add_u32 s48, s48, Lv_z%=-Lv_y%=\n\t" \
          "s_addc_u32 s49, s49, 0\n\t" \
          "Lv_v%=:\n\t" \
          "s_memrealtime s[52:53]\n\t" \
          "s_waitcnt lgkmcnt(1)\n\t" \
          "s_sub_i32 vcc_lo, %[cnt], s50\n\t" \
          "s_cmp_gt_i32 vcc_lo, 3\n\t" \
          "s_cbranch_scc0 Lv_x%=\n\t" \
          "s_memrealtime s[50:51]\n\t" \
          "s_waitcnt lgkmcnt(1)\n\t" \
          "s_sub_i32 vcc_lo, %[cnt], s52\n\t" \
          "s_cmp_gt_i32 vcc_lo, 3\n\t" \
          "s_cbranch_scc0 Lv_x%=\n\t" \
          "s_memrealtime s[52:53]\n\t" \
          "s_waitcnt lgkmcnt(1)\n\t" \
          "s_sub_i32 vcc_lo, %[cnt], s50\n\t" \
          "s_cmp_gt_i32 vcc_lo, 3\n\t" \
          "s_cbranch_scc0 Lv_x%=\n\t" \
          "s_memrealtime s[50:51]\n\t" \
          "s_waitcnt lgkmcnt(1)\n\t" \
          "s_sub_i32 vcc_lo, %[cnt], s52\n\t" \
          "s_cmp_gt_i32 vcc_lo, 3\n\t" \
          "s_cbranch_scc0 Lv_x%=\n\t" \
          "s_memrealtime s[52:53]\n\t" \
          "s_waitcnt lgkmcnt(1)\n\t" \
          "s_sub_i32 vcc_lo, %[cnt], s50\n\t" \
          "s_cmp_gt_i32 vcc_lo, 3\n\t" \
          "s_cbranch_scc0 Lv_x%=\n\t" \
          "s_memrealtime s[50:51]\n\t" \
          "s_waitcnt lgkmcnt(1)\n\t" \
          "s_sub_i32 vcc_lo, %[cnt], s52\n\t" \
          "s_cmp_gt_i32 vcc_lo, 3\n\t" \
          "s_cbranch_scc0 Lv_x%=\n\t" \
          "s_memrealtime s[52:53]\n\t" \
          "s_waitcnt lgkmcnt(1)\n\t" \
          "s_sub_i32 vcc_lo, %[cnt], s50\n\t" \
          "s_cmp_gt_i32 vcc_lo, 3\n\t" \
          "s_cbranch_scc0 Lv_x%=\n\t" \
          "s_memrealtime s[50:51]\n\t" \
          "s_waitcnt lgkmcnt(1)\n\t" \
          "s_sub_i32 vcc_lo, %[cnt], s52\n\t" \
          "s_cmp_gt_i32 vcc_lo, 3\n\t" \
          "s_cbranch_scc0 Lv_x%=\n\t" \
          "s_branch Lv_v%=\n\t" \
          "Lv_x%=:\n\t" \
          "s_max_i32 vcc_lo, vcc_lo, 0\n\t" \
          "s_lshl_b32 vcc_lo, vcc_lo, 2\n\t" \
          "s_sub_u32 s48, s48, vcc_lo\n\t" \
          "s_subb_u32 s49, s49, 0\n\t" \
          "s_setpc_b64 s[48:49]\n\t" \
          "s_nop 5\n\t" \
          "s_nop 5\n\t" \
          "s_nop 5\n\t" \
          "Lv_z%=:\n\t" \
          "s_barrier\n\t" \
          "Lv_n%=:\n\t" \
          "buffer_load_dwordx4 v[192:195], %[ga0], %[rsm], %[mbn] offen sc1\n\t" \
          "s_nop 15\n\t" \
          "s_nop 5\n\t" \
          "buffer_load_dwordx4 v[196:199], %[ga1], %[rsm], %[mbn] offen sc1\n\t" \
          "s_nop 15\n\t" \
          "s_nop 5\n\t" \
          "buffer_load_dwordx4 v[200:203], %[ga2], %[rsm], %[mbn] offen sc1\n\t" \
          "s_nop 15\n\t" \
          "s_nop 5\n\t" \
          "buffer_load_dwordx4 v[204:207], %[ga3], %[rsm], %[mbn] offen sc1\n\t" \
          "buffer_load_dword %[al], %[alo], %[rsm], %[mb] offen sc1\n\t" \
          "Lv_go%=:\n\t" \
          "s_waitcnt vmcnt(4)\n\t" \
          "v_smfmac_f32_16x16x64_bf16 v[176:179], v[192:195], v[8:15], %[idx]\n\t" \
          "v_max3_u32 v240, v192, v193, v194\n\t" \
          "v_smfmac_f32_16x16x64_bf16 v[180:183], v[192:195], v[16:23], %[idx]\n\t" \
          "v_max3_u32 v240, v194, v195, v240\n\t" \
          "v_smfmac_f32_16x16x64_bf16 v[184:187], v[192:195], v[24:31], %[idx]\n\t" \
          "buffer_load_dwordx2 %[rec], off, %[rsm], %[rco] sc1\n\t" \
          "v_smfmac_f32_16x16x64_bf16 v[188:191], v[192:195], v[32:39], %[idx]\n\t" \
          "s_waitcnt vmcnt(4)\n\t" \
          "v_smfmac_f32_16x16x64_bf16 v[176:179], v[196:199], v[40:47], %[idx]\n\t" \
          "v_max3_u32 v240, v196, v197, v240\n\t" \
          "v_smfmac_f32_16x16x64_bf16 v[180:183], v[196:199], v[48:55], %[idx]\n\t" \
          "v_max3_u32 v240, v198, v199, v240\n\t" \
          "v_smfmac_f32_16x16x64_bf16 v[184:187], v[196:199], v[56:63], %[idx]\n\t" \
          "v_smfmac_f32_16x16x64_bf16 v[188:191], v[196:199], v[64:71], %[idx]\n\t" \
          "s_waitcnt vmcnt(3)\n\t" \
          "v_smfmac_f32_16x16x64_bf16 v[176:179], v[200:203], v[72:79], %[idx]\n\t" \
          "v_max3_u32 v240, v200, v201, v240\n\t" \
          "v_smfmac_f32_16x16x64_bf16 v[180:183], v[200:203], v[80:87], %[idx]\n\t" \
          "v_max3_u32 v240, v202, v203, v240\n\t" \
          "v_smfmac_f32_16x16x64_bf16 v[184:187], v[200:203], v[88:95], %[idx]\n\t" \
          "v_smfmac_f32_16x16x64_bf16 v[188:191], v[200:203], v[96:103], %[idx]\n\t" \
          "s_waitcnt vmcnt(2)\n\t" \
          "v_smfmac_f32_16x16x64_bf16 v[176:179], v[204:207], v[104:111], %[idx]\n\t" \
          "v_max3_u32 v240, v204, v205, v240\n\t" \
          "v_smfmac_f32_16x16x64_bf16 v[180:183], v[204:207], v[112:119], %[idx]\n\t" \
          "v_max3_u32 v240, v206, v207, v240\n\t" \
          "v_smfmac_f32_16x16x64_bf16 v[184:187], v[204:207], v[120:127], %[idx]\n\t" \
          "v_cmp_eq_u32 vcc, -1, v240\n\t" \
          "v_smfmac_f32_16x16x64_bf16 v[188:191], v[204:207], v[128:135], %[idx]\n\t" \
          "s_and_b64 vcc, vcc, %[en]\n\t" \
          "v_add_f32 v242, v176, v177\n\t" \
          "v_add_f32 v243, v178, v179\n\t" \
          "v_add_f32 v244, v180, v181\n\t" \
          "v_add_f32 v245, v182, v183\n\t" \
          "s_cbranch_scc1 Lv_fail%=\n\t" \
          "v_add_f32 v246, v184, v185\n\t" \
          "v_add_f32 v247, v186, v187\n\t" \
          "ds_write_b64 %[vw0], v[242:243]\n\t" \
          "ds_write_b64 %[vw1], v[244:245]\n\t" \
          "ds_write_b64 %[vw2], v[246:247]\n\t" \
          "v_add_f32 v248, v188, v189\n\t" \
          "v_add_f32 v249, v190, v191\n\t" \
          "v_mfma_f32_16x16x32_bf16 v[208:211], v[212:215], v[212:215], 0\n\t" \
          "v_mfma_f32_16x16x32_bf16 v[208:211], v[212:215], v[212:215], 0\n\t" \
          "s_waitcnt lgkmcnt(0)\n\t" \
          "s_barrier\n\t" \
          "ds_read_b64 v[242:243], %[vr] offset:0\n\t" \
          "v_mfma_f32_16x16x32_bf16 v[208:211], v[212:215], v[212:215], 0\n\t" \
          "ds_read_b64 v[244:245], %[vr] offset:2064\n\t" \
          "ds_read_b64 v[246:247], %[vr] offset:4128\n\t" \
          "v_mfma_f32_16x16x32_bf16 v[208:211], v[212:215], v[212:215], 0\n\t" \
          "s_waitcnt lgkmcnt(2)\n\t" \
          "v_add_f32 v248, v248, v242\n\t" \
          "v_add_f32 v249, v249, v243\n\t" \
          "s_waitcnt lgkmcnt(1)\n\t" \
          "v_add_f32 v248, v248, v244\n\t" \
          "v_add_f32 v249, v249, v245\n\t" \
          "s_waitcnt lgkmcnt(0)\n\t" \
          "v_add_f32 v248, v248, v246\n\t" \
          "v_add_f32 v249, v249, v247\n\t" \
          "v_cvt_pk_bf16_f32 v242, v248, v249\n\t" \
          "v_min_u32 v242, -2, v242\n\t" \
          "buffer_store_dword v242, %[vo], %[rsm], %[vb] offen sc1\n\t" \
          "s_memrealtime %[te]\n\t" \
          "v_mov_b64 v[252:253], -1\n\t" \
          "v_mov_b64 v[254:255], -1\n\t" \
          "s_sleep 1\n\t" \
          "buffer_store_dwordx4 v[252:255], %[rso], %[rsm], %[rb] offen sc1\n\t" \
          "s_waitcnt vmcnt(2) lgkmcnt(0)\n\t"
#define VO_W "{a[0:31]}"(wa[0]), "{a[32:63]}"(wa[1]), "{a[64:95]}"(wa[2]), "{a[96:127]}"(wa[3]), "{a[128:159]}"(wa[4]), "{a[160:191]}"(wa[5]), "{a[192:223]}"(wa[6]), "{a[224:255]}"(wa[7]), "{v[8:39]}"(wv_[0]), "{v[40:71]}"(wv_[1]), "{v[72:103]}"(wv_[2]), "{v[104:135]}"(wv_[3])
#define VO_DEFW "=&{a[0:31]}"(wa[0]), "=&{a[32:63]}"(wa[1]), "=&{a[64:95]}"(wa[2]), "=&{a[96:127]}"(wa[3]), "=&{a[128:159]}"(wa[4]), "=&{a[160:191]}"(wa[5]), "=&{a[192:223]}"(wa[6]), "=&{a[224:255]}"(wa[7]), "=&{v[8:39]}"(wv_[0]), "=&{v[40:71]}"(wv_[1]), "=&{v[72:103]}"(wv_[2]), "=&{v[104:135]}"(wv_[3])
#define VO_LOADER \
          "s_mov_b64 s[48:49], %[wo]\n\ts_mov_b32 s50, %[wsz]\n\ts_mov_b32 s51, 0x20000\n\ts_nop 4\n\ts_add_u32 m0, %[stg], 0\n\ts_xor_b32 %[t], %[vk], 0x0\n\t" \
          "buffer_load_dwordx4 %[sv], %[rv], %[t] offen lds\n\ts_add_u32 m0, %[stg], 1024\n\ts_xor_b32 %[t], %[vk], 0x10000\n\tbuffer_load_dwordx4 %[sv], %[rv], %[t] offen lds\n\ts_add_u32 m0, %[stg], 2048\n\ts_xor_b32 %[t], %[vk], 0x20000\n\t" \
          "buffer_load_dwordx4 %[sv], %[rv], %[t] offen lds\n\ts_add_u32 m0, %[stg], 3072\n\ts_xor_b32 %[t], %[vk], 0x30000\n\tbuffer_load_dwordx4 %[sv], %[rv], %[t] offen lds\n\ts_add_u32 m0, %[stg], 4096\n\ts_xor_b32 %[t], %[vk], 0x40000\n\t" \
          "buffer_load_dwordx4 %[sv], %[rv], %[t] offen lds\n\ts_add_u32 m0, %[stg], 5120\n\ts_xor_b32 %[t], %[vk], 0x50000\n\tbuffer_load_dwordx4 %[sv], %[rv], %[t] offen lds\n\ts_add_u32 m0, %[stg], 6144\n\ts_xor_b32 %[t], %[vk], 0x60000\n\t" \
          "buffer_load_dwordx4 %[sv], %[rv], %[t] offen lds\n\ts_add_u32 m0, %[stg], 7168\n\ts_xor_b32 %[t], %[vk], 0x70000\n\tbuffer_load_dwordx4 %[sv], %[rv], %[t] offen lds\n\ts_add_u32 m0, %[stg], 8192\n\ts_xor_b32 %[t], %[vk], 0x100000\n\t" \
          "buffer_load_dwordx4 %[sv], %[rv], %[t] offen lds\n\ts_add_u32 m0, %[stg], 9216\n\ts_xor_b32 %[t], %[vk], 0x110000\n\tbuffer_load_dwordx4 %[sv], %[rv], %[t] offen lds\n\ts_add_u32 m0, %[stg], 10240\n\ts_xor_b32 %[t], %[vk], 0x120000\n\t" \
          "buffer_load_dwordx4 %[sv], %[rv], %[t] offen lds\n\ts_add_u32 m0, %[stg], 11264\n\ts_xor_b32 %[t], %[vk], 0x130000\n\tbuffer_load_dwordx4 %[sv], %[rv], %[t] offen lds\n\ts_add_u32 m0, %[stg], 12288\n\ts_xor_b32 %[t], %[vk], 0x140000\n\t" \
          "buffer_load_dwordx4 %[sv], %[rv], %[t] offen lds\n\ts_add_u32 m0, %[stg], 13312\n\ts_xor_b32 %[t], %[vk], 0x150000\n\tbuffer_load_dwordx4 %[sv], %[rv], %[t] offen lds\n\ts_add_u32 m0, %[stg], 14336\n\ts_xor_b32 %[t], %[vk], 0x160000\n\t" \
          "buffer_load_dwordx4 %[sv], %[rv], %[t] offen lds\n\ts_add_u32 m0, %[stg], 15360\n\ts_xor_b32 %[t], %[vk], 0x170000\n\tbuffer_load_dwordx4 %[sv], %[rv], %[t] offen lds\n\ts_add_u32 m0, %[stg], 16384\n\ts_xor_b32 %[t], %[vk], 0x200000\n\t" \
          "buffer_load_dwordx4 %[sv], %[rv], %[t] offen lds\n\ts_add_u32 m0, %[stg], 17408\n\ts_xor_b32 %[t], %[vk], 0x210000\n\tbuffer_load_dwordx4 %[sv], %[rv], %[t] offen lds\n\ts_add_u32 m0, %[stg], 18432\n\ts_xor_b32 %[t], %[vk], 0x220000\n\t" \
          "buffer_load_dwordx4 %[sv], %[rv], %[t] offen lds\n\ts_add_u32 m0, %[stg], 19456\n\ts_xor_b32 %[t], %[vk], 0x230000\n\tbuffer_load_dwordx4 %[sv], %[rv], %[t] offen lds\n\ts_add_u32 m0, %[stg], 20480\n\ts_xor_b32 %[t], %[vk], 0x240000\n\t" \
          "buffer_load_dwordx4 %[sv], %[rv], %[t] offen lds\n\ts_add_u32 m0, %[stg], 21504\n\ts_xor_b32 %[t], %[vk], 0x250000\n\tbuffer_load_dwordx4 %[sv], %[rv], %[t] offen lds\n\ts_add_u32 m0, %[stg], 22528\n\ts_xor_b32 %[t], %[vk], 0x260000\n\t" \
          "buffer_load_dwordx4 %[sv], %[rv], %[t] offen lds\n\ts_add_u32 m0, %[stg], 23552\n\ts_xor_b32 %[t], %[vk], 0x270000\n\tbuffer_load_dwordx4 %[sv], %[rv], %[t] offen lds\n\ts_add_u32 m0, %[stg], 24576\n\ts_xor_b32 %[t], %[vk], 0x300000\n\t" \
          "buffer_load_dwordx4 %[sv], %[rv], %[t] offen lds\n\ts_add_u32 m0, %[stg], 25600\n\ts_xor_b32 %[t], %[vk], 0x310000\n\tbuffer_load_dwordx4 %[sv], %[rv], %[t] offen lds\n\ts_add_u32 m0, %[stg], 26624\n\ts_xor_b32 %[t], %[vk], 0x320000\n\t" \
          "buffer_load_dwordx4 %[sv], %[rv], %[t] offen lds\n\ts_add_u32 m0, %[stg], 27648\n\ts_xor_b32 %[t], %[vk], 0x330000\n\tbuffer_load_dwordx4 %[sv], %[rv], %[t] offen lds\n\ts_add_u32 m0, %[stg], 28672\n\ts_xor_b32 %[t], %[vk], 0x340000\n\t" \
          "buffer_load_dwordx4 %[sv], %[rv], %[t] offen lds\n\ts_add_u32 m0, %[stg], 29696\n\ts_xor_b32 %[t], %[vk], 0x350000\n\tbuffer_load_dwordx4 %[sv], %[rv], %[t] offen lds\n\ts_add_u32 m0, %[stg], 30720\n\ts_xor_b32 %[t], %[vk], 0x360000\n\t" \
          "buffer_load_dwordx4 %[sv], %[rv], %[t] offen lds\n\ts_add_u32 m0, %[stg], 31744\n\ts_xor_b32 %[t], %[vk], 0x370000\n\tbuffer_load_dwordx4 %[sv], %[rv], %[t] offen lds\n\ts_waitcnt vmcnt(16)\n\tds_read_b64_tr_b16 v[8:9], %[vb0] offset:0\n\t" \
          "ds_read_b64_tr_b16 v[10:11], %[vb0] offset:128\n\tds_read_b64_tr_b16 v[12:13], %[vb0] offset:4096\n\tds_read_b64_tr_b16 v[14:15], %[vb0] offset:4224\n\tds_read_b64_tr_b16 v[16:17], %[vb1] offset:0\n\tds_read_b64_tr_b16 v[18:19], %[vb1] offset:128\n\tds_read_b64_tr_b16 v[20:21], %[vb1] offset:4096\n\t" \
          "ds_read_b64_tr_b16 v[22:23], %[vb1] offset:4224\n\tds_read_b64_tr_b16 v[24:25], %[vb2] offset:0\n\tds_read_b64_tr_b16 v[26:27], %[vb2] offset:128\n\tds_read_b64_tr_b16 v[28:29], %[vb2] offset:4096\n\tds_read_b64_tr_b16 v[30:31], %[vb2] offset:4224\n\tds_read_b64_tr_b16 v[32:33], %[vb3] offset:0\n\t" \
          "ds_read_b64_tr_b16 v[34:35], %[vb3] offset:128\n\tds_read_b64_tr_b16 v[36:37], %[vb3] offset:4096\n\tds_read_b64_tr_b16 v[38:39], %[vb3] offset:4224\n\tds_read_b64_tr_b16 v[40:41], %[vb0] offset:8192\n\tds_read_b64_tr_b16 v[42:43], %[vb0] offset:8320\n\tds_read_b64_tr_b16 v[44:45], %[vb0] offset:12288\n\t" \
          "ds_read_b64_tr_b16 v[46:47], %[vb0] offset:12416\n\tds_read_b64_tr_b16 v[48:49], %[vb1] offset:8192\n\tds_read_b64_tr_b16 v[50:51], %[vb1] offset:8320\n\tds_read_b64_tr_b16 v[52:53], %[vb1] offset:12288\n\tds_read_b64_tr_b16 v[54:55], %[vb1] offset:12416\n\tds_read_b64_tr_b16 v[56:57], %[vb2] offset:8192\n\t" \
          "ds_read_b64_tr_b16 v[58:59], %[vb2] offset:8320\n\tds_read_b64_tr_b16 v[60:61], %[vb2] offset:12288\n\tds_read_b64_tr_b16 v[62:63], %[vb2] offset:12416\n\tds_read_b64_tr_b16 v[64:65], %[vb3] offset:8192\n\tds_read_b64_tr_b16 v[66:67], %[vb3] offset:8320\n\tds_read_b64_tr_b16 v[68:69], %[vb3] offset:12288\n\t" \
          "ds_read_b64_tr_b16 v[70:71], %[vb3] offset:12416\n\ts_waitcnt lgkmcnt(0)\n\ts_add_u32 %[ho], %[k0], 1\n\ts_and_b32 %[ho], %[ho], 2\n\ts_or_b32 %[ho], %[ho], 0\n\ts_lshl_b32 %[ho], %[ho], 20\n\t" \
          "s_xor_b32 %[ho], %[ho], %[hx]\n\ts_add_u32 m0, %[stg], 0\n\ts_add_u32 %[t], %[ho], 0\n\tbuffer_load_dwordx4 %[so], s[48:51], %[t] offen lds\n\ts_add_u32 m0, %[stg], 1024\n\ts_add_u32 %[t], %[ho], 16384\n\t" \
          "buffer_load_dwordx4 %[so], s[48:51], %[t] offen lds\n\ts_add_u32 m0, %[stg], 2048\n\ts_add_u32 %[t], %[ho], 32768\n\tbuffer_load_dwordx4 %[so], s[48:51], %[t] offen lds\n\ts_add_u32 m0, %[stg], 3072\n\ts_add_u32 %[t], %[ho], 49152\n\t" \
          "buffer_load_dwordx4 %[so], s[48:51], %[t] offen lds\n\ts_add_u32 m0, %[stg], 4096\n\ts_add_u32 %[t], %[ho], 65536\n\tbuffer_load_dwordx4 %[so], s[48:51], %[t] offen lds\n\ts_add_u32 m0, %[stg], 5120\n\ts_add_u32 %[t], %[ho], 81920\n\t" \
          "buffer_load_dwordx4 %[so], s[48:51], %[t] offen lds\n\ts_add_u32 m0, %[stg], 6144\n\ts_add_u32 %[t], %[ho], 98304\n\tbuffer_load_dwordx4 %[so], s[48:51], %[t] offen lds\n\ts_add_u32 m0, %[stg], 7168\n\ts_add_u32 %[t], %[ho], 114688\n\t" \
          "buffer_load_dwordx4 %[so], s[48:51], %[t] offen lds\n\ts_add_u32 m0, %[stg], 8192\n\ts_add_u32 %[t], %[ho], 131072\n\tbuffer_load_dwordx4 %[so], s[48:51], %[t] offen lds\n\ts_add_u32 m0, %[stg], 9216\n\ts_add_u32 %[t], %[ho], 147456\n\t" \
          "buffer_load_dwordx4 %[so], s[48:51], %[t] offen lds\n\ts_add_u32 m0, %[stg], 10240\n\ts_add_u32 %[t], %[ho], 163840\n\tbuffer_load_dwordx4 %[so], s[48:51], %[t] offen lds\n\ts_add_u32 m0, %[stg], 11264\n\ts_add_u32 %[t], %[ho], 180224\n\t" \
          "buffer_load_dwordx4 %[so], s[48:51], %[t] offen lds\n\ts_add_u32 m0, %[stg], 12288\n\ts_add_u32 %[t], %[ho], 196608\n\tbuffer_load_dwordx4 %[so], s[48:51], %[t] offen lds\n\ts_add_u32 m0, %[stg], 13312\n\ts_add_u32 %[t], %[ho], 212992\n\t" \
          "buffer_load_dwordx4 %[so], s[48:51], %[t] offen lds\n\ts_add_u32 m0, %[stg], 14336\n\ts_add_u32 %[t], %[ho], 229376\n\tbuffer_load_dwordx4 %[so], s[48:51], %[t] offen lds\n\ts_add_u32 m0, %[stg], 15360\n\ts_add_u32 %[t], %[ho], 245760\n\t" \
          "buffer_load_dwordx4 %[so], s[48:51], %[t] offen lds\n\ts_waitcnt vmcnt(16)\n\tds_read_b64_tr_b16 v[72:73], %[vb0] offset:16384\n\tds_read_b64_tr_b16 v[74:75], %[vb0] offset:16512\n\tds_read_b64_tr_b16 v[76:77], %[vb0] offset:20480\n\tds_read_b64_tr_b16 v[78:79], %[vb0] offset:20608\n\t" \
          "ds_read_b64_tr_b16 v[80:81], %[vb1] offset:16384\n\tds_read_b64_tr_b16 v[82:83], %[vb1] offset:16512\n\tds_read_b64_tr_b16 v[84:85], %[vb1] offset:20480\n\tds_read_b64_tr_b16 v[86:87], %[vb1] offset:20608\n\tds_read_b64_tr_b16 v[88:89], %[vb2] offset:16384\n\tds_read_b64_tr_b16 v[90:91], %[vb2] offset:16512\n\t" \
          "ds_read_b64_tr_b16 v[92:93], %[vb2] offset:20480\n\tds_read_b64_tr_b16 v[94:95], %[vb2] offset:20608\n\tds_read_b64_tr_b16 v[96:97], %[vb3] offset:16384\n\tds_read_b64_tr_b16 v[98:99], %[vb3] offset:16512\n\tds_read_b64_tr_b16 v[100:101], %[vb3] offset:20480\n\tds_read_b64_tr_b16 v[102:103], %[vb3] offset:20608\n\t" \
          "ds_read_b64_tr_b16 v[104:105], %[vb0] offset:24576\n\tds_read_b64_tr_b16 v[106:107], %[vb0] offset:24704\n\tds_read_b64_tr_b16 v[108:109], %[vb0] offset:28672\n\tds_read_b64_tr_b16 v[110:111], %[vb0] offset:28800\n\tds_read_b64_tr_b16 v[112:113], %[vb1] offset:24576\n\tds_read_b64_tr_b16 v[114:115], %[vb1] offset:24704\n\t" \
          "ds_read_b64_tr_b16 v[116:117], %[vb1] offset:28672\n\tds_read_b64_tr_b16 v[118:119], %[vb1] offset:28800\n\tds_read_b64_tr_b16 v[120:121], %[vb2] offset:24576\n\tds_read_b64_tr_b16 v[122:123], %[vb2] offset:24704\n\tds_read_b64_tr_b16 v[124:125], %[vb2] offset:28672\n\tds_read_b64_tr_b16 v[126:127], %[vb2] offset:28800\n\t" \
          "ds_read_b64_tr_b16 v[128:129], %[vb3] offset:24576\n\tds_read_b64_tr_b16 v[130:131], %[vb3] offset:24704\n\tds_read_b64_tr_b16 v[132:133], %[vb3] offset:28672\n\tds_read_b64_tr_b16 v[134:135], %[vb3] offset:28800\n\ts_waitcnt lgkmcnt(0)\n\ts_add_u32 %[ho], %[k0], 0\n\t" \
          "s_and_b32 %[ho], %[ho], 2\n\ts_or_b32 %[ho], %[ho], 1\n\ts_lshl_b32 %[ho], %[ho], 20\n\ts_xor_b32 %[ho], %[ho], %[hx]\n\ts_add_u32 m0, %[stg], 16384\n\ts_add_u32 %[t], %[ho], 0\n\t" \
          "buffer_load_dwordx4 %[so], s[48:51], %[t] offen lds\n\ts_add_u32 m0, %[stg], 17408\n\ts_add_u32 %[t], %[ho], 16384\n\tbuffer_load_dwordx4 %[so], s[48:51], %[t] offen lds\n\ts_add_u32 m0, %[stg], 18432\n\ts_add_u32 %[t], %[ho], 32768\n\t" \
          "buffer_load_dwordx4 %[so], s[48:51], %[t] offen lds\n\ts_add_u32 m0, %[stg], 19456\n\ts_add_u32 %[t], %[ho], 49152\n\tbuffer_load_dwordx4 %[so], s[48:51], %[t] offen lds\n\ts_add_u32 m0, %[stg], 20480\n\ts_add_u32 %[t], %[ho], 65536\n\t" \
          "buffer_load_dwordx4 %[so], s[48:51], %[t] offen lds\n\ts_add_u32 m0, %[stg], 21504\n\ts_add_u32 %[t], %[ho], 81920\n\tbuffer_load_dwordx4 %[so], s[48:51], %[t] offen lds\n\ts_add_u32 m0, %[stg], 22528\n\ts_add_u32 %[t], %[ho], 98304\n\t" \
          "buffer_load_dwordx4 %[so], s[48:51], %[t] offen lds\n\ts_add_u32 m0, %[stg], 23552\n\ts_add_u32 %[t], %[ho], 114688\n\tbuffer_load_dwordx4 %[so], s[48:51], %[t] offen lds\n\ts_add_u32 m0, %[stg], 24576\n\ts_add_u32 %[t], %[ho], 131072\n\t" \
          "buffer_load_dwordx4 %[so], s[48:51], %[t] offen lds\n\ts_add_u32 m0, %[stg], 25600\n\ts_add_u32 %[t], %[ho], 147456\n\tbuffer_load_dwordx4 %[so], s[48:51], %[t] offen lds\n\ts_add_u32 m0, %[stg], 26624\n\ts_add_u32 %[t], %[ho], 163840\n\t" \
          "buffer_load_dwordx4 %[so], s[48:51], %[t] offen lds\n\ts_add_u32 m0, %[stg], 27648\n\ts_add_u32 %[t], %[ho], 180224\n\tbuffer_load_dwordx4 %[so], s[48:51], %[t] offen lds\n\ts_add_u32 m0, %[stg], 28672\n\ts_add_u32 %[t], %[ho], 196608\n\t" \
          "buffer_load_dwordx4 %[so], s[48:51], %[t] offen lds\n\ts_add_u32 m0, %[stg], 29696\n\ts_add_u32 %[t], %[ho], 212992\n\tbuffer_load_dwordx4 %[so], s[48:51], %[t] offen lds\n\ts_add_u32 m0, %[stg], 30720\n\ts_add_u32 %[t], %[ho], 229376\n\t" \
          "buffer_load_dwordx4 %[so], s[48:51], %[t] offen lds\n\ts_add_u32 m0, %[stg], 31744\n\ts_add_u32 %[t], %[ho], 245760\n\tbuffer_load_dwordx4 %[so], s[48:51], %[t] offen lds\n\ts_mov_b32 %[n], 8\n\ts_cmp_eq_u32 %[k0], 2\n\t" \
          "s_cbranch_scc1 Lw_2_%=\n\ts_cmp_eq_u32 %[k0], 0\n\ts_cbranch_scc1 Lw_0_%=\n\ts_waitcnt vmcnt(0)\n\ts_cmp_eq_u32 %[k0], 1\n\ts_cbranch_scc1 Lw_1_%=\n\t" \
          "s_branch Lw_3_%=\n\tLw_0_%=:\n\ts_waitcnt vmcnt(16)\n\tds_read_b64_tr_b16 a[0:1], %[ob] offset:0\n\tds_read_b64_tr_b16 a[2:3], %[ob] offset:128\n\tds_read_b64_tr_b16 a[4:5], %[ob] offset:8192\n\t" \
          "ds_read_b64_tr_b16 a[6:7], %[ob] offset:8320\n\tds_read_b64_tr_b16 a[8:9], %[ob] offset:32\n\tds_read_b64_tr_b16 a[10:11], %[ob] offset:160\n\tds_read_b64_tr_b16 a[12:13], %[ob] offset:8224\n\tds_read_b64_tr_b16 a[14:15], %[ob] offset:8352\n\tds_read_b64_tr_b16 a[16:17], %[ob] offset:1024\n\t" \
          "ds_read_b64_tr_b16 a[18:19], %[ob] offset:1152\n\tds_read_b64_tr_b16 a[20:21], %[ob] offset:9216\n\tds_read_b64_tr_b16 a[22:23], %[ob] offset:9344\n\tds_read_b64_tr_b16 a[24:25], %[ob] offset:1056\n\tds_read_b64_tr_b16 a[26:27], %[ob] offset:1184\n\tds_read_b64_tr_b16 a[28:29], %[ob] offset:9248\n\t" \
          "ds_read_b64_tr_b16 a[30:31], %[ob] offset:9376\n\ts_cmp_gt_u32 %[n], 2\n\ts_cselect_b32 s50, s50, 0\n\ts_cmp_gt_u32 %[n], 6\n\ts_cselect_b32 %[ho], 0x2, 0x6\n\ts_lshl_b32 %[ho], %[ho], 20\n\t" \
          "s_xor_b32 %[ho], %[ho], %[hx]\n\ts_waitcnt lgkmcnt(0)\n\ts_add_u32 m0, %[stg], 0\n\ts_add_u32 %[t], %[ho], 0\n\tbuffer_load_dwordx4 %[so], s[48:51], %[t] offen lds\n\ts_add_u32 m0, %[stg], 1024\n\t" \
          "s_add_u32 %[t], %[ho], 16384\n\tbuffer_load_dwordx4 %[so], s[48:51], %[t] offen lds\n\ts_add_u32 m0, %[stg], 2048\n\ts_add_u32 %[t], %[ho], 32768\n\tbuffer_load_dwordx4 %[so], s[48:51], %[t] offen lds\n\ts_add_u32 m0, %[stg], 3072\n\t" \
          "s_add_u32 %[t], %[ho], 49152\n\tbuffer_load_dwordx4 %[so], s[48:51], %[t] offen lds\n\ts_add_u32 m0, %[stg], 4096\n\ts_add_u32 %[t], %[ho], 65536\n\tbuffer_load_dwordx4 %[so], s[48:51], %[t] offen lds\n\ts_add_u32 m0, %[stg], 5120\n\t" \
          "s_add_u32 %[t], %[ho], 81920\n\tbuffer_load_dwordx4 %[so], s[48:51], %[t] offen lds\n\ts_add_u32 m0, %[stg], 6144\n\ts_add_u32 %[t], %[ho], 98304\n\tbuffer_load_dwordx4 %[so], s[48:51], %[t] offen lds\n\ts_add_u32 m0, %[stg], 7168\n\t" \
          "s_add_u32 %[t], %[ho], 114688\n\tbuffer_load_dwordx4 %[so], s[48:51], %[t] offen lds\n\ts_add_u32 m0, %[stg], 8192\n\ts_add_u32 %[t], %[ho], 131072\n\tbuffer_load_dwordx4 %[so], s[48:51], %[t] offen lds\n\ts_add_u32 m0, %[stg], 9216\n\t" \
          "s_add_u32 %[t], %[ho], 147456\n\tbuffer_load_dwordx4 %[so], s[48:51], %[t] offen lds\n\ts_add_u32 m0, %[stg], 10240\n\ts_add_u32 %[t], %[ho], 163840\n\tbuffer_load_dwordx4 %[so], s[48:51], %[t] offen lds\n\ts_add_u32 m0, %[stg], 11264\n\t" \
          "s_add_u32 %[t], %[ho], 180224\n\tbuffer_load_dwordx4 %[so], s[48:51], %[t] offen lds\n\ts_add_u32 m0, %[stg], 12288\n\ts_add_u32 %[t], %[ho], 196608\n\tbuffer_load_dwordx4 %[so], s[48:51], %[t] offen lds\n\ts_add_u32 m0, %[stg], 13312\n\t" \
          "s_add_u32 %[t], %[ho], 212992\n\tbuffer_load_dwordx4 %[so], s[48:51], %[t] offen lds\n\ts_add_u32 m0, %[stg], 14336\n\ts_add_u32 %[t], %[ho], 229376\n\tbuffer_load_dwordx4 %[so], s[48:51], %[t] offen lds\n\ts_add_u32 m0, %[stg], 15360\n\t" \
          "s_add_u32 %[t], %[ho], 245760\n\tbuffer_load_dwordx4 %[so], s[48:51], %[t] offen lds\n\ts_sub_u32 %[n], %[n], 1\n\ts_cmp_eq_u32 %[n], 4\n\ts_cbranch_scc1 Lw_5_%=\n\tLw_1_%=:\n\t" \
          "s_waitcnt vmcnt(16)\n\tds_read_b64_tr_b16 a[32:33], %[ob] offset:16384\n\tds_read_b64_tr_b16 a[34:35], %[ob] offset:16512\n\tds_read_b64_tr_b16 a[36:37], %[ob] offset:24576\n\tds_read_b64_tr_b16 a[38:39], %[ob] offset:24704\n\tds_read_b64_tr_b16 a[40:41], %[ob] offset:16416\n\t" \
          "ds_read_b64_tr_b16 a[42:43], %[ob] offset:16544\n\tds_read_b64_tr_b16 a[44:45], %[ob] offset:24608\n\tds_read_b64_tr_b16 a[46:47], %[ob] offset:24736\n\tds_read_b64_tr_b16 a[48:49], %[ob] offset:17408\n\tds_read_b64_tr_b16 a[50:51], %[ob] offset:17536\n\tds_read_b64_tr_b16 a[52:53], %[ob] offset:25600\n\t" \
          "ds_read_b64_tr_b16 a[54:55], %[ob] offset:25728\n\tds_read_b64_tr_b16 a[56:57], %[ob] offset:17440\n\tds_read_b64_tr_b16 a[58:59], %[ob] offset:17568\n\tds_read_b64_tr_b16 a[60:61], %[ob] offset:25632\n\tds_read_b64_tr_b16 a[62:63], %[ob] offset:25760\n\ts_cmp_gt_u32 %[n], 2\n\t" \
          "s_cselect_b32 s50, s50, 0\n\ts_cmp_gt_u32 %[n], 6\n\ts_cselect_b32 %[ho], 0x3, 0x7\n\ts_lshl_b32 %[ho], %[ho], 20\n\ts_xor_b32 %[ho], %[ho], %[hx]\n\ts_waitcnt lgkmcnt(0)\n\t" \
          "s_add_u32 m0, %[stg], 16384\n\ts_add_u32 %[t], %[ho], 0\n\tbuffer_load_dwordx4 %[so], s[48:51], %[t] offen lds\n\ts_add_u32 m0, %[stg], 17408\n\ts_add_u32 %[t], %[ho], 16384\n\tbuffer_load_dwordx4 %[so], s[48:51], %[t] offen lds\n\t" \
          "s_add_u32 m0, %[stg], 18432\n\ts_add_u32 %[t], %[ho], 32768\n\tbuffer_load_dwordx4 %[so], s[48:51], %[t] offen lds\n\ts_add_u32 m0, %[stg], 19456\n\ts_add_u32 %[t], %[ho], 49152\n\tbuffer_load_dwordx4 %[so], s[48:51], %[t] offen lds\n\t" \
          "s_add_u32 m0, %[stg], 20480\n\ts_add_u32 %[t], %[ho], 65536\n\tbuffer_load_dwordx4 %[so], s[48:51], %[t] offen lds\n\ts_add_u32 m0, %[stg], 21504\n\ts_add_u32 %[t], %[ho], 81920\n\tbuffer_load_dwordx4 %[so], s[48:51], %[t] offen lds\n\t" \
          "s_add_u32 m0, %[stg], 22528\n\ts_add_u32 %[t], %[ho], 98304\n\tbuffer_load_dwordx4 %[so], s[48:51], %[t] offen lds\n\ts_add_u32 m0, %[stg], 23552\n\ts_add_u32 %[t], %[ho], 114688\n\tbuffer_load_dwordx4 %[so], s[48:51], %[t] offen lds\n\t" \
          "s_add_u32 m0, %[stg], 24576\n\ts_add_u32 %[t], %[ho], 131072\n\tbuffer_load_dwordx4 %[so], s[48:51], %[t] offen lds\n\ts_add_u32 m0, %[stg], 25600\n\ts_add_u32 %[t], %[ho], 147456\n\tbuffer_load_dwordx4 %[so], s[48:51], %[t] offen lds\n\t" \
          "s_add_u32 m0, %[stg], 26624\n\ts_add_u32 %[t], %[ho], 163840\n\tbuffer_load_dwordx4 %[so], s[48:51], %[t] offen lds\n\ts_add_u32 m0, %[stg], 27648\n\ts_add_u32 %[t], %[ho], 180224\n\tbuffer_load_dwordx4 %[so], s[48:51], %[t] offen lds\n\t" \
          "s_add_u32 m0, %[stg], 28672\n\ts_add_u32 %[t], %[ho], 196608\n\tbuffer_load_dwordx4 %[so], s[48:51], %[t] offen lds\n\ts_add_u32 m0, %[stg], 29696\n\ts_add_u32 %[t], %[ho], 212992\n\tbuffer_load_dwordx4 %[so], s[48:51], %[t] offen lds\n\t" \
          "s_add_u32 m0, %[stg], 30720\n\ts_add_u32 %[t], %[ho], 229376\n\tbuffer_load_dwordx4 %[so], s[48:51], %[t] offen lds\n\ts_add_u32 m0, %[stg], 31744\n\ts_add_u32 %[t], %[ho], 245760\n\tbuffer_load_dwordx4 %[so], s[48:51], %[t] offen lds\n\t" \
          "s_sub_u32 %[n], %[n], 1\n\ts_cmp_eq_u32 %[n], 4\n\ts_cbranch_scc1 Lw_6_%=\n\tLw_2_%=:\n\ts_waitcnt vmcnt(16)\n\tds_read_b64_tr_b16 a[64:65], %[ob] offset:0\n\t" \
          "ds_read_b64_tr_b16 a[66:67], %[ob] offset:128\n\tds_read_b64_tr_b16 a[68:69], %[ob] offset:8192\n\tds_read_b64_tr_b16 a[70:71], %[ob] offset:8320\n\tds_read_b64_tr_b16 a[72:73], %[ob] offset:32\n\tds_read_b64_tr_b16 a[74:75], %[ob] offset:160\n\tds_read_b64_tr_b16 a[76:77], %[ob] offset:8224\n\t" \
          "ds_read_b64_tr_b16 a[78:79], %[ob] offset:8352\n\tds_read_b64_tr_b16 a[80:81], %[ob] offset:1024\n\tds_read_b64_tr_b16 a[82:83], %[ob] offset:1152\n\tds_read_b64_tr_b16 a[84:85], %[ob] offset:9216\n\tds_read_b64_tr_b16 a[86:87], %[ob] offset:9344\n\tds_read_b64_tr_b16 a[88:89], %[ob] offset:1056\n\t" \
          "ds_read_b64_tr_b16 a[90:91], %[ob] offset:1184\n\tds_read_b64_tr_b16 a[92:93], %[ob] offset:9248\n\tds_read_b64_tr_b16 a[94:95], %[ob] offset:9376\n\ts_cmp_gt_u32 %[n], 2\n\ts_cselect_b32 s50, s50, 0\n\ts_cmp_gt_u32 %[n], 6\n\t" \
          "s_cselect_b32 %[ho], 0x0, 0x4\n\ts_lshl_b32 %[ho], %[ho], 20\n\ts_xor_b32 %[ho], %[ho], %[hx]\n\ts_waitcnt lgkmcnt(0)\n\ts_add_u32 m0, %[stg], 0\n\ts_add_u32 %[t], %[ho], 0\n\t" \
          "buffer_load_dwordx4 %[so], s[48:51], %[t] offen lds\n\ts_add_u32 m0, %[stg], 1024\n\ts_add_u32 %[t], %[ho], 16384\n\tbuffer_load_dwordx4 %[so], s[48:51], %[t] offen lds\n\ts_add_u32 m0, %[stg], 2048\n\ts_add_u32 %[t], %[ho], 32768\n\t" \
          "buffer_load_dwordx4 %[so], s[48:51], %[t] offen lds\n\ts_add_u32 m0, %[stg], 3072\n\ts_add_u32 %[t], %[ho], 49152\n\tbuffer_load_dwordx4 %[so], s[48:51], %[t] offen lds\n\ts_add_u32 m0, %[stg], 4096\n\ts_add_u32 %[t], %[ho], 65536\n\t" \
          "buffer_load_dwordx4 %[so], s[48:51], %[t] offen lds\n\ts_add_u32 m0, %[stg], 5120\n\ts_add_u32 %[t], %[ho], 81920\n\tbuffer_load_dwordx4 %[so], s[48:51], %[t] offen lds\n\ts_add_u32 m0, %[stg], 6144\n\ts_add_u32 %[t], %[ho], 98304\n\t" \
          "buffer_load_dwordx4 %[so], s[48:51], %[t] offen lds\n\ts_add_u32 m0, %[stg], 7168\n\ts_add_u32 %[t], %[ho], 114688\n\tbuffer_load_dwordx4 %[so], s[48:51], %[t] offen lds\n\ts_add_u32 m0, %[stg], 8192\n\ts_add_u32 %[t], %[ho], 131072\n\t" \
          "buffer_load_dwordx4 %[so], s[48:51], %[t] offen lds\n\ts_add_u32 m0, %[stg], 9216\n\ts_add_u32 %[t], %[ho], 147456\n\tbuffer_load_dwordx4 %[so], s[48:51], %[t] offen lds\n\ts_add_u32 m0, %[stg], 10240\n\ts_add_u32 %[t], %[ho], 163840\n\t" \
          "buffer_load_dwordx4 %[so], s[48:51], %[t] offen lds\n\ts_add_u32 m0, %[stg], 11264\n\ts_add_u32 %[t], %[ho], 180224\n\tbuffer_load_dwordx4 %[so], s[48:51], %[t] offen lds\n\ts_add_u32 m0, %[stg], 12288\n\ts_add_u32 %[t], %[ho], 196608\n\t" \
          "buffer_load_dwordx4 %[so], s[48:51], %[t] offen lds\n\ts_add_u32 m0, %[stg], 13312\n\ts_add_u32 %[t], %[ho], 212992\n\tbuffer_load_dwordx4 %[so], s[48:51], %[t] offen lds\n\ts_add_u32 m0, %[stg], 14336\n\ts_add_u32 %[t], %[ho], 229376\n\t" \
          "buffer_load_dwordx4 %[so], s[48:51], %[t] offen lds\n\ts_add_u32 m0, %[stg], 15360\n\ts_add_u32 %[t], %[ho], 245760\n\tbuffer_load_dwordx4 %[so], s[48:51], %[t] offen lds\n\ts_sub_u32 %[n], %[n], 1\n\ts_cmp_eq_u32 %[n], 4\n\t" \
          "s_cbranch_scc1 Lw_7_%=\n\tLw_3_%=:\n\ts_waitcnt vmcnt(16)\n\tds_read_b64_tr_b16 a[96:97], %[ob] offset:16384\n\tds_read_b64_tr_b16 a[98:99], %[ob] offset:16512\n\tds_read_b64_tr_b16 a[100:101], %[ob] offset:24576\n\t" \
          "ds_read_b64_tr_b16 a[102:103], %[ob] offset:24704\n\tds_read_b64_tr_b16 a[104:105], %[ob] offset:16416\n\tds_read_b64_tr_b16 a[106:107], %[ob] offset:16544\n\tds_read_b64_tr_b16 a[108:109], %[ob] offset:24608\n\tds_read_b64_tr_b16 a[110:111], %[ob] offset:24736\n\tds_read_b64_tr_b16 a[112:113], %[ob] offset:17408\n\t" \
          "ds_read_b64_tr_b16 a[114:115], %[ob] offset:17536\n\tds_read_b64_tr_b16 a[116:117], %[ob] offset:25600\n\tds_read_b64_tr_b16 a[118:119], %[ob] offset:25728\n\tds_read_b64_tr_b16 a[120:121], %[ob] offset:17440\n\tds_read_b64_tr_b16 a[122:123], %[ob] offset:17568\n\tds_read_b64_tr_b16 a[124:125], %[ob] offset:25632\n\t" \
          "ds_read_b64_tr_b16 a[126:127], %[ob] offset:25760\n\ts_cmp_gt_u32 %[n], 2\n\ts_cselect_b32 s50, s50, 0\n\ts_cmp_gt_u32 %[n], 6\n\ts_cselect_b32 %[ho], 0x1, 0x5\n\ts_lshl_b32 %[ho], %[ho], 20\n\t" \
          "s_xor_b32 %[ho], %[ho], %[hx]\n\ts_waitcnt lgkmcnt(0)\n\ts_add_u32 m0, %[stg], 16384\n\ts_add_u32 %[t], %[ho], 0\n\tbuffer_load_dwordx4 %[so], s[48:51], %[t] offen lds\n\ts_add_u32 m0, %[stg], 17408\n\t" \
          "s_add_u32 %[t], %[ho], 16384\n\tbuffer_load_dwordx4 %[so], s[48:51], %[t] offen lds\n\ts_add_u32 m0, %[stg], 18432\n\ts_add_u32 %[t], %[ho], 32768\n\tbuffer_load_dwordx4 %[so], s[48:51], %[t] offen lds\n\ts_add_u32 m0, %[stg], 19456\n\t" \
          "s_add_u32 %[t], %[ho], 49152\n\tbuffer_load_dwordx4 %[so], s[48:51], %[t] offen lds\n\ts_add_u32 m0, %[stg], 20480\n\ts_add_u32 %[t], %[ho], 65536\n\tbuffer_load_dwordx4 %[so], s[48:51], %[t] offen lds\n\ts_add_u32 m0, %[stg], 21504\n\t" \
          "s_add_u32 %[t], %[ho], 81920\n\tbuffer_load_dwordx4 %[so], s[48:51], %[t] offen lds\n\ts_add_u32 m0, %[stg], 22528\n\ts_add_u32 %[t], %[ho], 98304\n\tbuffer_load_dwordx4 %[so], s[48:51], %[t] offen lds\n\ts_add_u32 m0, %[stg], 23552\n\t" \
          "s_add_u32 %[t], %[ho], 114688\n\tbuffer_load_dwordx4 %[so], s[48:51], %[t] offen lds\n\ts_add_u32 m0, %[stg], 24576\n\ts_add_u32 %[t], %[ho], 131072\n\tbuffer_load_dwordx4 %[so], s[48:51], %[t] offen lds\n\ts_add_u32 m0, %[stg], 25600\n\t" \
          "s_add_u32 %[t], %[ho], 147456\n\tbuffer_load_dwordx4 %[so], s[48:51], %[t] offen lds\n\ts_add_u32 m0, %[stg], 26624\n\ts_add_u32 %[t], %[ho], 163840\n\tbuffer_load_dwordx4 %[so], s[48:51], %[t] offen lds\n\ts_add_u32 m0, %[stg], 27648\n\t" \
          "s_add_u32 %[t], %[ho], 180224\n\tbuffer_load_dwordx4 %[so], s[48:51], %[t] offen lds\n\ts_add_u32 m0, %[stg], 28672\n\ts_add_u32 %[t], %[ho], 196608\n\tbuffer_load_dwordx4 %[so], s[48:51], %[t] offen lds\n\ts_add_u32 m0, %[stg], 29696\n\t" \
          "s_add_u32 %[t], %[ho], 212992\n\tbuffer_load_dwordx4 %[so], s[48:51], %[t] offen lds\n\ts_add_u32 m0, %[stg], 30720\n\ts_add_u32 %[t], %[ho], 229376\n\tbuffer_load_dwordx4 %[so], s[48:51], %[t] offen lds\n\ts_add_u32 m0, %[stg], 31744\n\t" \
          "s_add_u32 %[t], %[ho], 245760\n\tbuffer_load_dwordx4 %[so], s[48:51], %[t] offen lds\n\ts_sub_u32 %[n], %[n], 1\n\ts_cmp_eq_u32 %[n], 4\n\ts_cbranch_scc1 Lw_4_%=\n\ts_branch Lw_0_%=\n\t" \
          "Lw_4_%=:\n\ts_waitcnt vmcnt(16)\n\tds_read_b64_tr_b16 a[128:129], %[ob] offset:0\n\tds_read_b64_tr_b16 a[130:131], %[ob] offset:128\n\tds_read_b64_tr_b16 a[132:133], %[ob] offset:8192\n\tds_read_b64_tr_b16 a[134:135], %[ob] offset:8320\n\t" \
          "ds_read_b64_tr_b16 a[136:137], %[ob] offset:32\n\tds_read_b64_tr_b16 a[138:139], %[ob] offset:160\n\tds_read_b64_tr_b16 a[140:141], %[ob] offset:8224\n\tds_read_b64_tr_b16 a[142:143], %[ob] offset:8352\n\tds_read_b64_tr_b16 a[144:145], %[ob] offset:1024\n\tds_read_b64_tr_b16 a[146:147], %[ob] offset:1152\n\t" \
          "ds_read_b64_tr_b16 a[148:149], %[ob] offset:9216\n\tds_read_b64_tr_b16 a[150:151], %[ob] offset:9344\n\tds_read_b64_tr_b16 a[152:153], %[ob] offset:1056\n\tds_read_b64_tr_b16 a[154:155], %[ob] offset:1184\n\tds_read_b64_tr_b16 a[156:157], %[ob] offset:9248\n\tds_read_b64_tr_b16 a[158:159], %[ob] offset:9376\n\t" \
          "s_cmp_gt_u32 %[n], 2\n\ts_cselect_b32 s50, s50, 0\n\ts_cmp_gt_u32 %[n], 6\n\ts_cselect_b32 %[ho], 0x2, 0x6\n\ts_lshl_b32 %[ho], %[ho], 20\n\ts_xor_b32 %[ho], %[ho], %[hx]\n\t" \
          "s_waitcnt lgkmcnt(0)\n\ts_add_u32 m0, %[stg], 0\n\ts_add_u32 %[t], %[ho], 0\n\tbuffer_load_dwordx4 %[so], s[48:51], %[t] offen lds\n\ts_add_u32 m0, %[stg], 1024\n\ts_add_u32 %[t], %[ho], 16384\n\t" \
          "buffer_load_dwordx4 %[so], s[48:51], %[t] offen lds\n\ts_add_u32 m0, %[stg], 2048\n\ts_add_u32 %[t], %[ho], 32768\n\tbuffer_load_dwordx4 %[so], s[48:51], %[t] offen lds\n\ts_add_u32 m0, %[stg], 3072\n\ts_add_u32 %[t], %[ho], 49152\n\t" \
          "buffer_load_dwordx4 %[so], s[48:51], %[t] offen lds\n\ts_add_u32 m0, %[stg], 4096\n\ts_add_u32 %[t], %[ho], 65536\n\tbuffer_load_dwordx4 %[so], s[48:51], %[t] offen lds\n\ts_add_u32 m0, %[stg], 5120\n\ts_add_u32 %[t], %[ho], 81920\n\t" \
          "buffer_load_dwordx4 %[so], s[48:51], %[t] offen lds\n\ts_add_u32 m0, %[stg], 6144\n\ts_add_u32 %[t], %[ho], 98304\n\tbuffer_load_dwordx4 %[so], s[48:51], %[t] offen lds\n\ts_add_u32 m0, %[stg], 7168\n\ts_add_u32 %[t], %[ho], 114688\n\t" \
          "buffer_load_dwordx4 %[so], s[48:51], %[t] offen lds\n\ts_add_u32 m0, %[stg], 8192\n\ts_add_u32 %[t], %[ho], 131072\n\tbuffer_load_dwordx4 %[so], s[48:51], %[t] offen lds\n\ts_add_u32 m0, %[stg], 9216\n\ts_add_u32 %[t], %[ho], 147456\n\t" \
          "buffer_load_dwordx4 %[so], s[48:51], %[t] offen lds\n\ts_add_u32 m0, %[stg], 10240\n\ts_add_u32 %[t], %[ho], 163840\n\tbuffer_load_dwordx4 %[so], s[48:51], %[t] offen lds\n\ts_add_u32 m0, %[stg], 11264\n\ts_add_u32 %[t], %[ho], 180224\n\t" \
          "buffer_load_dwordx4 %[so], s[48:51], %[t] offen lds\n\ts_add_u32 m0, %[stg], 12288\n\ts_add_u32 %[t], %[ho], 196608\n\tbuffer_load_dwordx4 %[so], s[48:51], %[t] offen lds\n\ts_add_u32 m0, %[stg], 13312\n\ts_add_u32 %[t], %[ho], 212992\n\t" \
          "buffer_load_dwordx4 %[so], s[48:51], %[t] offen lds\n\ts_add_u32 m0, %[stg], 14336\n\ts_add_u32 %[t], %[ho], 229376\n\tbuffer_load_dwordx4 %[so], s[48:51], %[t] offen lds\n\ts_add_u32 m0, %[stg], 15360\n\ts_add_u32 %[t], %[ho], 245760\n\t" \
          "buffer_load_dwordx4 %[so], s[48:51], %[t] offen lds\n\ts_sub_u32 %[n], %[n], 1\n\ts_cmp_eq_u32 %[n], 0\n\ts_cbranch_scc1 Lw_end%=\n\tLw_5_%=:\n\ts_waitcnt vmcnt(16)\n\t" \
          "ds_read_b64_tr_b16 a[160:161], %[ob] offset:16384\n\tds_read_b64_tr_b16 a[162:163], %[ob] offset:16512\n\tds_read_b64_tr_b16 a[164:165], %[ob] offset:24576\n\tds_read_b64_tr_b16 a[166:167], %[ob] offset:24704\n\tds_read_b64_tr_b16 a[168:169], %[ob] offset:16416\n\tds_read_b64_tr_b16 a[170:171], %[ob] offset:16544\n\t" \
          "ds_read_b64_tr_b16 a[172:173], %[ob] offset:24608\n\tds_read_b64_tr_b16 a[174:175], %[ob] offset:24736\n\tds_read_b64_tr_b16 a[176:177], %[ob] offset:17408\n\tds_read_b64_tr_b16 a[178:179], %[ob] offset:17536\n\tds_read_b64_tr_b16 a[180:181], %[ob] offset:25600\n\tds_read_b64_tr_b16 a[182:183], %[ob] offset:25728\n\t" \
          "ds_read_b64_tr_b16 a[184:185], %[ob] offset:17440\n\tds_read_b64_tr_b16 a[186:187], %[ob] offset:17568\n\tds_read_b64_tr_b16 a[188:189], %[ob] offset:25632\n\tds_read_b64_tr_b16 a[190:191], %[ob] offset:25760\n\ts_cmp_gt_u32 %[n], 2\n\ts_cselect_b32 s50, s50, 0\n\t" \
          "s_cmp_gt_u32 %[n], 6\n\ts_cselect_b32 %[ho], 0x3, 0x7\n\ts_lshl_b32 %[ho], %[ho], 20\n\ts_xor_b32 %[ho], %[ho], %[hx]\n\ts_waitcnt lgkmcnt(0)\n\ts_add_u32 m0, %[stg], 16384\n\t" \
          "s_add_u32 %[t], %[ho], 0\n\tbuffer_load_dwordx4 %[so], s[48:51], %[t] offen lds\n\ts_add_u32 m0, %[stg], 17408\n\ts_add_u32 %[t], %[ho], 16384\n\tbuffer_load_dwordx4 %[so], s[48:51], %[t] offen lds\n\ts_add_u32 m0, %[stg], 18432\n\t" \
          "s_add_u32 %[t], %[ho], 32768\n\tbuffer_load_dwordx4 %[so], s[48:51], %[t] offen lds\n\ts_add_u32 m0, %[stg], 19456\n\ts_add_u32 %[t], %[ho], 49152\n\tbuffer_load_dwordx4 %[so], s[48:51], %[t] offen lds\n\ts_add_u32 m0, %[stg], 20480\n\t" \
          "s_add_u32 %[t], %[ho], 65536\n\tbuffer_load_dwordx4 %[so], s[48:51], %[t] offen lds\n\ts_add_u32 m0, %[stg], 21504\n\ts_add_u32 %[t], %[ho], 81920\n\tbuffer_load_dwordx4 %[so], s[48:51], %[t] offen lds\n\ts_add_u32 m0, %[stg], 22528\n\t" \
          "s_add_u32 %[t], %[ho], 98304\n\tbuffer_load_dwordx4 %[so], s[48:51], %[t] offen lds\n\ts_add_u32 m0, %[stg], 23552\n\ts_add_u32 %[t], %[ho], 114688\n\tbuffer_load_dwordx4 %[so], s[48:51], %[t] offen lds\n\ts_add_u32 m0, %[stg], 24576\n\t" \
          "s_add_u32 %[t], %[ho], 131072\n\tbuffer_load_dwordx4 %[so], s[48:51], %[t] offen lds\n\ts_add_u32 m0, %[stg], 25600\n\ts_add_u32 %[t], %[ho], 147456\n\tbuffer_load_dwordx4 %[so], s[48:51], %[t] offen lds\n\ts_add_u32 m0, %[stg], 26624\n\t" \
          "s_add_u32 %[t], %[ho], 163840\n\tbuffer_load_dwordx4 %[so], s[48:51], %[t] offen lds\n\ts_add_u32 m0, %[stg], 27648\n\ts_add_u32 %[t], %[ho], 180224\n\tbuffer_load_dwordx4 %[so], s[48:51], %[t] offen lds\n\ts_add_u32 m0, %[stg], 28672\n\t" \
          "s_add_u32 %[t], %[ho], 196608\n\tbuffer_load_dwordx4 %[so], s[48:51], %[t] offen lds\n\ts_add_u32 m0, %[stg], 29696\n\ts_add_u32 %[t], %[ho], 212992\n\tbuffer_load_dwordx4 %[so], s[48:51], %[t] offen lds\n\ts_add_u32 m0, %[stg], 30720\n\t" \
          "s_add_u32 %[t], %[ho], 229376\n\tbuffer_load_dwordx4 %[so], s[48:51], %[t] offen lds\n\ts_add_u32 m0, %[stg], 31744\n\ts_add_u32 %[t], %[ho], 245760\n\tbuffer_load_dwordx4 %[so], s[48:51], %[t] offen lds\n\ts_sub_u32 %[n], %[n], 1\n\t" \
          "s_cmp_eq_u32 %[n], 0\n\ts_cbranch_scc1 Lw_end%=\n\tLw_6_%=:\n\ts_waitcnt vmcnt(16)\n\tbuffer_load_dwordx4 %[rec], %[ro], %[rr], 0 offen sc1\n\tds_read_b64_tr_b16 a[192:193], %[ob] offset:0\n\t" \
          "ds_read_b64_tr_b16 a[194:195], %[ob] offset:128\n\tds_read_b64_tr_b16 a[196:197], %[ob] offset:8192\n\tds_read_b64_tr_b16 a[198:199], %[ob] offset:8320\n\tds_read_b64_tr_b16 a[200:201], %[ob] offset:32\n\tds_read_b64_tr_b16 a[202:203], %[ob] offset:160\n\tds_read_b64_tr_b16 a[204:205], %[ob] offset:8224\n\t" \
          "ds_read_b64_tr_b16 a[206:207], %[ob] offset:8352\n\tds_read_b64_tr_b16 a[208:209], %[ob] offset:1024\n\tds_read_b64_tr_b16 a[210:211], %[ob] offset:1152\n\tds_read_b64_tr_b16 a[212:213], %[ob] offset:9216\n\tds_read_b64_tr_b16 a[214:215], %[ob] offset:9344\n\tds_read_b64_tr_b16 a[216:217], %[ob] offset:1056\n\t" \
          "ds_read_b64_tr_b16 a[218:219], %[ob] offset:1184\n\tds_read_b64_tr_b16 a[220:221], %[ob] offset:9248\n\tds_read_b64_tr_b16 a[222:223], %[ob] offset:9376\n\ts_cmp_gt_u32 %[n], 2\n\ts_cselect_b32 s50, s50, 0\n\ts_cmp_gt_u32 %[n], 6\n\t" \
          "s_cselect_b32 %[ho], 0x0, 0x4\n\ts_lshl_b32 %[ho], %[ho], 20\n\ts_xor_b32 %[ho], %[ho], %[hx]\n\ts_waitcnt lgkmcnt(0)\n\ts_add_u32 m0, %[stg], 0\n\ts_add_u32 %[t], %[ho], 0\n\t" \
          "buffer_load_dwordx4 %[so], s[48:51], %[t] offen lds\n\ts_add_u32 m0, %[stg], 1024\n\ts_add_u32 %[t], %[ho], 16384\n\tbuffer_load_dwordx4 %[so], s[48:51], %[t] offen lds\n\ts_add_u32 m0, %[stg], 2048\n\ts_add_u32 %[t], %[ho], 32768\n\t" \
          "buffer_load_dwordx4 %[so], s[48:51], %[t] offen lds\n\ts_add_u32 m0, %[stg], 3072\n\ts_add_u32 %[t], %[ho], 49152\n\tbuffer_load_dwordx4 %[so], s[48:51], %[t] offen lds\n\ts_add_u32 m0, %[stg], 4096\n\ts_add_u32 %[t], %[ho], 65536\n\t" \
          "buffer_load_dwordx4 %[so], s[48:51], %[t] offen lds\n\ts_add_u32 m0, %[stg], 5120\n\ts_add_u32 %[t], %[ho], 81920\n\tbuffer_load_dwordx4 %[so], s[48:51], %[t] offen lds\n\ts_add_u32 m0, %[stg], 6144\n\ts_add_u32 %[t], %[ho], 98304\n\t" \
          "buffer_load_dwordx4 %[so], s[48:51], %[t] offen lds\n\ts_add_u32 m0, %[stg], 7168\n\ts_add_u32 %[t], %[ho], 114688\n\tbuffer_load_dwordx4 %[so], s[48:51], %[t] offen lds\n\ts_add_u32 m0, %[stg], 8192\n\ts_add_u32 %[t], %[ho], 131072\n\t" \
          "buffer_load_dwordx4 %[so], s[48:51], %[t] offen lds\n\ts_add_u32 m0, %[stg], 9216\n\ts_add_u32 %[t], %[ho], 147456\n\tbuffer_load_dwordx4 %[so], s[48:51], %[t] offen lds\n\ts_add_u32 m0, %[stg], 10240\n\ts_add_u32 %[t], %[ho], 163840\n\t" \
          "buffer_load_dwordx4 %[so], s[48:51], %[t] offen lds\n\ts_add_u32 m0, %[stg], 11264\n\ts_add_u32 %[t], %[ho], 180224\n\tbuffer_load_dwordx4 %[so], s[48:51], %[t] offen lds\n\ts_add_u32 m0, %[stg], 12288\n\ts_add_u32 %[t], %[ho], 196608\n\t" \
          "buffer_load_dwordx4 %[so], s[48:51], %[t] offen lds\n\ts_add_u32 m0, %[stg], 13312\n\ts_add_u32 %[t], %[ho], 212992\n\tbuffer_load_dwordx4 %[so], s[48:51], %[t] offen lds\n\ts_add_u32 m0, %[stg], 14336\n\ts_add_u32 %[t], %[ho], 229376\n\t" \
          "buffer_load_dwordx4 %[so], s[48:51], %[t] offen lds\n\ts_add_u32 m0, %[stg], 15360\n\ts_add_u32 %[t], %[ho], 245760\n\tbuffer_load_dwordx4 %[so], s[48:51], %[t] offen lds\n\ts_sub_u32 %[n], %[n], 1\n\ts_cmp_eq_u32 %[n], 0\n\t" \
          "s_cbranch_scc1 Lw_end%=\n\tLw_7_%=:\n\ts_waitcnt vmcnt(16)\n\tds_read_b64_tr_b16 a[224:225], %[ob] offset:16384\n\tds_read_b64_tr_b16 a[226:227], %[ob] offset:16512\n\tds_read_b64_tr_b16 a[228:229], %[ob] offset:24576\n\t" \
          "ds_read_b64_tr_b16 a[230:231], %[ob] offset:24704\n\tds_read_b64_tr_b16 a[232:233], %[ob] offset:16416\n\tds_read_b64_tr_b16 a[234:235], %[ob] offset:16544\n\tds_read_b64_tr_b16 a[236:237], %[ob] offset:24608\n\tds_read_b64_tr_b16 a[238:239], %[ob] offset:24736\n\tds_read_b64_tr_b16 a[240:241], %[ob] offset:17408\n\t" \
          "ds_read_b64_tr_b16 a[242:243], %[ob] offset:17536\n\tds_read_b64_tr_b16 a[244:245], %[ob] offset:25600\n\tds_read_b64_tr_b16 a[246:247], %[ob] offset:25728\n\tds_read_b64_tr_b16 a[248:249], %[ob] offset:17440\n\tds_read_b64_tr_b16 a[250:251], %[ob] offset:17568\n\tds_read_b64_tr_b16 a[252:253], %[ob] offset:25632\n\t" \
          "ds_read_b64_tr_b16 a[254:255], %[ob] offset:25760\n\ts_cmp_gt_u32 %[n], 2\n\ts_cselect_b32 s50, s50, 0\n\ts_cmp_gt_u32 %[n], 6\n\ts_cselect_b32 %[ho], 0x1, 0x5\n\ts_lshl_b32 %[ho], %[ho], 20\n\t" \
          "s_xor_b32 %[ho], %[ho], %[hx]\n\ts_waitcnt lgkmcnt(0)\n\ts_add_u32 m0, %[stg], 16384\n\ts_add_u32 %[t], %[ho], 0\n\tbuffer_load_dwordx4 %[so], s[48:51], %[t] offen lds\n\ts_add_u32 m0, %[stg], 17408\n\t" \
          "s_add_u32 %[t], %[ho], 16384\n\tbuffer_load_dwordx4 %[so], s[48:51], %[t] offen lds\n\ts_add_u32 m0, %[stg], 18432\n\ts_add_u32 %[t], %[ho], 32768\n\tbuffer_load_dwordx4 %[so], s[48:51], %[t] offen lds\n\ts_add_u32 m0, %[stg], 19456\n\t" \
          "s_add_u32 %[t], %[ho], 49152\n\tbuffer_load_dwordx4 %[so], s[48:51], %[t] offen lds\n\ts_add_u32 m0, %[stg], 20480\n\ts_add_u32 %[t], %[ho], 65536\n\tbuffer_load_dwordx4 %[so], s[48:51], %[t] offen lds\n\ts_add_u32 m0, %[stg], 21504\n\t" \
          "s_add_u32 %[t], %[ho], 81920\n\tbuffer_load_dwordx4 %[so], s[48:51], %[t] offen lds\n\ts_add_u32 m0, %[stg], 22528\n\ts_add_u32 %[t], %[ho], 98304\n\tbuffer_load_dwordx4 %[so], s[48:51], %[t] offen lds\n\ts_add_u32 m0, %[stg], 23552\n\t" \
          "s_add_u32 %[t], %[ho], 114688\n\tbuffer_load_dwordx4 %[so], s[48:51], %[t] offen lds\n\ts_add_u32 m0, %[stg], 24576\n\ts_add_u32 %[t], %[ho], 131072\n\tbuffer_load_dwordx4 %[so], s[48:51], %[t] offen lds\n\ts_add_u32 m0, %[stg], 25600\n\t" \
          "s_add_u32 %[t], %[ho], 147456\n\tbuffer_load_dwordx4 %[so], s[48:51], %[t] offen lds\n\ts_add_u32 m0, %[stg], 26624\n\ts_add_u32 %[t], %[ho], 163840\n\tbuffer_load_dwordx4 %[so], s[48:51], %[t] offen lds\n\ts_add_u32 m0, %[stg], 27648\n\t" \
          "s_add_u32 %[t], %[ho], 180224\n\tbuffer_load_dwordx4 %[so], s[48:51], %[t] offen lds\n\ts_add_u32 m0, %[stg], 28672\n\ts_add_u32 %[t], %[ho], 196608\n\tbuffer_load_dwordx4 %[so], s[48:51], %[t] offen lds\n\ts_add_u32 m0, %[stg], 29696\n\t" \
          "s_add_u32 %[t], %[ho], 212992\n\tbuffer_load_dwordx4 %[so], s[48:51], %[t] offen lds\n\ts_add_u32 m0, %[stg], 30720\n\ts_add_u32 %[t], %[ho], 229376\n\tbuffer_load_dwordx4 %[so], s[48:51], %[t] offen lds\n\ts_add_u32 m0, %[stg], 31744\n\t" \
          "s_add_u32 %[t], %[ho], 245760\n\tbuffer_load_dwordx4 %[so], s[48:51], %[t] offen lds\n\ts_sub_u32 %[n], %[n], 1\n\ts_cmp_eq_u32 %[n], 0\n\ts_cbranch_scc1 Lw_end%=\n\ts_branch Lw_4_%=\n\t" \
          "Lw_end%=:\n\ts_waitcnt vmcnt(0)\n\ts_barrier\n\t"
#define VO_CLOB "v176", "v177", "v178", "v179", "v180", "v181", "v182", "v183", "v184", "v185", "v186", "v187", "v188", "v189", "v190", "v191", "v192", "v193", "v194", "v195", "v196", "v197", "v198", "v199", "v200", "v201", "v202", "v203", "v204", "v205", "v206", "v207", "v208", "v209", "v210", "v211", "v212", "v213", "v214", "v215", "v216", "v217", "v218", "v219", "v220", "v221", "v222", "v223", "v224", "v225", "v226", "v227", "v228", "v229", "v230", "v231", "v232", "v233", "v234", "v235", "v236", "v237", "v238", "v239", "v240", "v241", "v242", "v243", "v244", "v245", "v246", "v247", "v248", "v249", "v250", "v251", "v252", "v253", "v254", "v255", "s48", "s49", "s50", "s51", "s52", "s53"
__global__ void __launch_bounds__(256, 1) k_fused(FParams P) {
  __shared__ __attribute__((aligned(16))) char Lraw[LDS_ALL];
  unsigned long long pc0;
  asm volatile("s_getpc_b64 %0\nLfused_pc0:" : "=s"(pc0));
  const int w = __builtin_amdgcn_readfirstlane(threadIdx.x >> 6);
  unsigned cz, pk; unsigned long long tag;
  {
    int clen; asm volatile("s_mov_b32 %0, Lfused_end-Lfused_pc0" : "=s"(clen));
    const int o = ((blockIdx.x >> 3) * 4 + w) * 1024 + (threadIdx.x & 7) * 128;
    asm volatile("s_nop 4\n\tbuffer_load_dword %0, %1, %2, 0 offen" : "=v"(cz) : "v"(o), "s"(__builtin_amdgcn_make_buffer_rsrc((void*)pc0, 0, clen, 0x00020000))); }
  if ((blockIdx.x & 7) >= 3) {
    asm volatile("Lfused_worker:");
    worker(P, cz, Lraw, w);
    asm volatile("Lfused_end:");
    return;
  }
  unsigned* tmo = &P.ctl->tmo[0]; unsigned long long* prof = P.prof; (void)prof;
  const int xq = blockIdx.x & 7, slot = blockIdx.x >> 3, nq = xq < 2 ? 12 : 8;
  const int b = slot < nq ? 12 * xq + slot : NQK + 20 * xq + (slot - nq);
  const int alw = b < NQK ? (b % 9 == 0 ? (b / 9) * 4 + w : -1) : (b == 33 ? 16 + w : (b == 95 ? 20 + w : -1));
  const int xr = xq;
  const unsigned long long alx = (unsigned)__builtin_amdgcn_readfirstlane(alw >= 0 ? -1 : 0);
  const __amdgpu_buffer_rsrc_t rs_m = __builtin_amdgcn_make_buffer_rsrc(P.ring, 0, RSIZE, 0x00020000), rs_r = __builtin_amdgcn_make_buffer_rsrc(P.rdy, 0, RDY_SIZE, 0x00020000);
  const __amdgpu_buffer_rsrc_t rs_c = __builtin_amdgcn_make_buffer_rsrc(P.ctl, 0, sizeof(Ctl), 0x00020000);
  v4u rdy;
#define LANE_CONSTS const int lane = opaque_lane(), tid = 64 * w + lane, l15 = lane & 15, lq = lane >> 4, idx = (lane & 1) ? 0xEEEE : 0x4444; \
  const int mem_lane_off = (((lq >> 1) * 2 + (l15 >> 3)) * 16 + (lq & 1) * 8 + (l15 & 7)) * 16 + dbl(w * 4096); \
  const int wrow = 8 * (lq >> 1) + 2 * (lq & 1) + 4 * (l15 >> 3) + 16 * ((l15 >> 2) & 1), wsw = (wrow >> 1) & 3
  unsigned tA = 0;
  if (b < NQK) {
    const int h = b;
    LANE_CONSTS;
    v32u wa[8], wv_[7];
    const unsigned lb0 = (unsigned)(size_t)Lraw;
    const unsigned own = lb0 + w * 40960 + lane * 16;
    unsigned kfl;
    {
      const __amdgpu_buffer_rsrc_t rq = __builtin_amdgcn_make_buffer_rsrc((void*)(P.Wq + h * DHA + 64 * (w >> 1)), 0, 0x7fffffff, 0x00020000), rk = __builtin_amdgcn_make_buffer_rsrc((void*)(P.Wk + h * DHA + 64 * (w >> 1)), 0, 0x7fffffff, 0x00020000);
      const int go = (lane >> 3) * HDA * 2 + (((((lane & 7) >> 1) ^ ((lane >> 4) & 3)) << 1) | (lane & 1)) * 16;
      unsigned rd[4];
#pragma unroll
      for (int t = 0; t < 4; ++t) rd[t] = lb0 + w * 40960 + wrow * 128 + (l15 & 3) * 8 + ((t ^ (2 * (~w & 1)) ^ wsw) << 5);
#define QK_NTq ""
#define QK_NTk ""
      int t_;
      asm volatile(QK_LOADER : QK_DEFW, [t] "=&s"(t_), [rec] "=&v"(rdy), [kf] "=&v"(kfl)
                   : [ko] "v"(lane < 2 ? (int)offsetof(Ctl, prg) + lane * 4096 + h * 128 : 0x7ffffff0), [rc] "s"(rs_c), [rd0] "v"(rd[0]), [rd1] "v"(rd[1]), [rd2] "v"(rd[2]), [rd3] "v"(rd[3]), [own] "v"(own), [go] "v"(go), [ro] "v"(lane * 128), [rr] "s"(rs_r), [rq] "s"(rq), [rk] "s"(rk), [lw] "s"(lb0 + w * 40960), [kb] "s"((8 * (w & 1) + 2 * xr + (w >> 1)) * 0x80000) : "memory", "scc");
    }
    asm volatile("" :: "v"(cz));
    { const unsigned long long t0 = rdy_tag(rdy); if ((~__ballot(rdy.x == (unsigned)t0 && rdy.y == (unsigned)(t0 >> 32)) << 4) != 0) kfl = 0; }
    const unsigned long long parq = rdy_wait(P, rdy, tag, pk);
    const int rpl = (int)(parq >> 63) * 8192;
    const __amdgpu_buffer_rsrc_t rs_x = __builtin_amdgcn_make_buffer_rsrc(P.qkvA, 0, QSIZE, 0x00020000);
    const int xk = qoff(parq, 16 + (h >> 1)) + (h & 1) * 4096, xv = qoff(parq, 32 + (h >> 1)) + (h & 1) * 4096;
    const int xm = kvoff(pk, h) + w * 512;
#define PF_ADDR const int tz = opaque_lane()
#define KV_SEGS(v) (min(__builtin_amdgcn_readlane(v, 0), __builtin_amdgcn_readlane(v, 1)) >> 1)
#define WAIT_KV(sg) do { for (unsigned spins = 0;; ++spins) { const unsigned v_ = ld_agent(&P.ctl->prg[(tz & 1) * 32 + h][0]); kvok = KV_SEGS(v_); if (kvok > (sg)) break; \
        if (spin_fail(spins, tmo, 3u)) break; __builtin_amdgcn_s_sleep(8); } } while (0)
    int kvok = 0;
#define LOAD_KV(sg) do { const int so_ = (sg) * 2 * QBLK + w * 1024; \
        _Pragma("unroll") for (int q = 0; q < 4; ++q) \
          asm volatile("s_mov_b32 m0, %0\n\ts_nop 4\n\tbuffer_load_dwordx4 %1, %2, %3 offen sc1 lds" :: "s"(lb0 + w * 40960 + QK_KV + q * 1024), "v"((q < 2 ? tz : tz ^ ((tz >> 1) & 8)) * 16), "s"(rs_x), \
                       "s"(so_ + (q & 1) * QBLK + ((q >> 1) ? xv : xk)) : "memory"); } while (0)
    unsigned pz = 0;
#define LD_ASYNC(dst, voff, rs, soff) asm volatile("s_nop 4\n\tbuffer_load_dword %0, %1, %2, %3 offen sc1" : "=v"(dst) : "v"(voff), "s"(rs), "s"(soff) : "memory")
    {
      PF_ADDR;
      kvok = KV_SEGS(kfl);
      if (kvok <= 0) WAIT_KV(0);
      LOAD_KV(0);
      LD_ASYNC(pz, kvok > 1 && tz < 32 ? tz * 128 : 0x7ffffff0, rs_x, (2 + (w & 1)) * QBLK + ((w >> 1) ? xv : xk));
    }
    const float QS = 0.08838834764831845f * 1.4426950408889634f;
    const unsigned j0 = lq == (l15 >> 2) && !(l15 & 1) ? 0x3f803f80u : 0u, j1 = lq == (l15 >> 2) && (l15 & 1) ? 0x3f803f80u : 0u;
    const v4u JK = {j0, j1, j0, j1};
    const unsigned vxp = lb0 + w * 40960 + QK_KV + 2048 + ((l15 & 3) * 16 + ((4 * lq + (l15 >> 2)) ^ ((l15 & 1) << 3))) * 16;
    const unsigned p1 = lb0 + (w ^ 1) * 40960 + lane * 16, p2 = lb0 + (w ^ 2) * 40960 + lane * 16, p3 = lb0 + (w ^ 3) * 40960 + lane * 16;
    unsigned pot = 0, d1p = 140;
    const int mlo = mem_lane_off - dbl(w * 4096) + (w & 1) * 16384 + (w >> 1) * 1024, hoff = dbl(h * 2048) + w * 512 + lane * 4;
    v4u vm = {0u, 0u, 0u, 0u};
    const int alo = lane < 16 && w == 3 ? ALM + lane * 128 : (lane >= 62 ? RECO + (lane - 62) * 4 : 0x7ffffff0);
    for (int s = 0; s < NSEG; ++s) {
      int mb = RBASE(s) + ROFF_M, vb = RBASE(s) + ROFF_V, ob = RBASE(s) + ROFF_O, mb0 = mb + xr * 2048;
      unsigned al, ann, fl; unsigned long long bad, bad1, b2, b3, po_;
      if (__builtin_expect(s == 1 && w == 3, 0)) {
        unsigned recD; rec_get(tA, recD, rs_m, mb + RECO, tmo); tA -= (recD & 0xffffu) + (recD >> 16);
      }
      { int rem = (int)(tA - rtnow());
        if (s == 0 || rem > 20000) tA -= rem; }
      tA = __builtin_amdgcn_readfirstlane(tA); d1p = __builtin_amdgcn_readfirstlane(d1p);
#define QK_STO
      { unsigned cnt; unsigned long long en;
        asm volatile(QK_STEP
          : "=&{v[136:167]}"(wv_[4]), "=&{v[168:199]}"(wv_[5]), "=&{v[200:231]}"(wv_[6]), "+{v[232:235]}"(vm), [m] "+v"(pz), "+v"(kfl), [al] "=&v"(al),
            [bad] "=&s"(bad), [bad1] "=&s"(bad1), [b2] "=&s"(b2), [b3] "=&s"(b3), [po] "=&s"(po_), [cnt] "=&s"(cnt), [en] "=&s"(en), [ann] "=&s"(ann), [fl] "=&s"(fl) QK_STO
          : "{a[0:31]}"(wa[0]), "{a[32:63]}"(wa[1]), "{a[64:95]}"(wa[2]), "{a[96:127]}"(wa[3]), "{a[128:159]}"(wa[4]), "{a[160:191]}"(wa[5]), "{a[192:223]}"(wa[6]), "{a[224:255]}"(wa[7]),
            "{v[8:39]}"(wv_[0]), "{v[40:71]}"(wv_[1]), "{v[72:103]}"(wv_[2]), "{v[104:135]}"(wv_[3]),
            [own] "v"(own), [p1] "v"(p1), [p2] "v"(p2), [p3] "v"(p3), [vxp] "v"(vxp), [ml0] "v"(mlo), [ml1] "v"(mlo ^ 1024), [jk] "v"(JK), [ho] "v"(hoff), [idx] "v"(idx), [alo] "v"(alo),
            [rsm] "s"(rs_m), [rsc] "s"(rs_c), [mb] "s"(mb), [vb] "s"(vb), [ob] "s"(ob), [rb] "s"(RBASE(s + 2) + ROFF_O), [twin] "s"(0xccccccccccccccccull),
            [one] "s"(0x3f803f80u), [qs] "s"(QS), [qs2] "s"((unsigned long long)__float_as_uint(QS)), [hi] "s"(0x1p40f), [lo] "s"(0x1p-40f), [th] "s"(40.f), [t] "s"(tA), [w] "s"(w), [ald0] "s"(d1p + TP(P.sh0, SH0)),
            [tmo] "s"((int)offsetof(Ctl, tmo)), [mb0] "s"(mb0), [mb1] "s"(mb0 ^ 2048), [mb2] "s"(mb0 ^ 4096), [mb3] "s"(mb0 ^ 6144), [ald] "s"(d1p - TP(P.sh1, SH1)), [als] "s"(mb + ALM + (alw & 15) * 128), [alx] "s"(alx)
          : "vcc", "scc", "s48", "s49", "s50", "s51", "s52", "s53", "memory"); }
      {
        const v4u Km = {wv_[4][QK_KM], wv_[4][QK_KM + 1], wv_[4][QK_KM + 2], wv_[4][QK_KM + 3]};
        if (__builtin_expect(bad != 0, 0)) {
#pragma unroll 1
          for (unsigned fails = 0; !__all((vm.x > vm.z ? vm.x : vm.z) != 0xffffffffu); ++fails) {
            __builtin_amdgcn_s_sleep(2);
            vm.x = __builtin_amdgcn_raw_buffer_load_b32(rs_m, hoff, vb, 16); vm.z = __builtin_amdgcn_raw_buffer_load_b32(rs_m, hoff, vb + 256, 16);
            if (spin_fail(fails, tmo, 1u)) break;
          }
          float sx[4], sm[2];
#pragma unroll
          for (int e = 0; e < 4; ++e) sx[e] = __uint_as_float(wv_[6][QK_SX + e]);
          sm[0] = __uint_as_float(wv_[4][QK_SM]); sm[1] = __uint_as_float(wv_[4][QK_SM + 1]);
          float mx = fmaxf(fmaxf(sx[0], sx[1]), fmaxf(sx[2], sx[3]));
          mx = fmaxf(mx, __int_as_float(__builtin_amdgcn_update_dpp(0, __float_as_int(mx), 0x4E, 0xf, 0xf, false)));
          mx = xmax32(xmax16(fmaxf(mx, fmaxf(sm[0], sm[1]))));
          if (b2 == 0) mx = 0.f;
          v4u px; px.x = pk2(__builtin_amdgcn_exp2f(sx[0] - mx), __builtin_amdgcn_exp2f(sx[1] - mx)); px.y = pk2(__builtin_amdgcn_exp2f(sx[2] - mx), __builtin_amdgcn_exp2f(sx[3] - mx));
          px.z = __builtin_amdgcn_update_dpp(0, px.x, 0x4E, 0xf, 0xf, false); px.w = __builtin_amdgcn_update_dpp(0, px.y, 0x4E, 0xf, 0xf, false);
          const v2u pm = {pk2(__builtin_amdgcn_exp2f(sm[0] - mx), __builtin_amdgcn_exp2f(sm[1] - mx)), 0u};
          const v4u on = {0x3f803f80u, 0x3f803f80u, 0x3f803f80u, 0x3f803f80u};
          const v4u vx0 = {wv_[6][QK_VX], wv_[6][QK_VX + 1], wv_[6][QK_VX + 2], wv_[6][QK_VX + 3]}, vx1 = {wv_[6][QK_VX + 4], wv_[6][QK_VX + 5], wv_[6][QK_VX + 6], wv_[6][QK_VX + 7]};
          f32x4 c0, c1, ls;
          asm volatile("s_nop 1\n\t"
                       "v_mfma_f32_16x16x32_bf16 %0, %3, %4, 0\n\t"
                       "v_mfma_f32_16x16x32_bf16 %1, %3, %5, 0\n\t"
                       "v_mfma_f32_16x16x32_bf16 %2, %3, %6, 0\n\t"
                       "s_nop 7\n\t"
                       "v_mfma_f32_16x16x16_bf16 %0, %7, v[232:233], %0\n\t"
                       "v_mfma_f32_16x16x16_bf16 %1, %7, v[234:235], %1\n\t"
                       "v_mfma_f32_16x16x16_bf16 %2, %7, v[168:169], %2\n\t"
                       "s_nop 7"
                       : "=&v"(c0), "=&v"(c1), "=&v"(ls) : "v"(px), "v"(vx0), "v"(vx1), "{v[168:171]}"(on), "v"(pm), "{v[232:235]}"(vm));
          const float i0 = __builtin_amdgcn_rcpf(ls[0]), i1 = __builtin_amdgcn_rcpf(ls[1]);
          __builtin_amdgcn_raw_buffer_store_b32(nosent(pk2(c0[0] * i0, c1[0] * i0)), rs_m, hoff, ob, 16);
          __builtin_amdgcn_raw_buffer_store_b32(nosent(pk2(c0[1] * i1, c1[1] * i1)), rs_m, hoff + 256, ob, 16);
          po_ = rtnow();
        }
        SB0;
        const unsigned po = __builtin_amdgcn_readfirstlane((unsigned)po_);
        fl = __builtin_amdgcn_readfirstlane(fl | (bad != 0));
        if (QGAP) __builtin_amdgcn_s_sleep(QGAP);
        SB0;
        __builtin_amdgcn_raw_buffer_store_b32(po - tA, rs_m, (h | (w ^ 3) | opaque_lane() | fl) == 0 ? RECO + 128 : 0x7ffffff0, mb, 16);
      { const int lz = opaque_lane(), kb = xm + s * 2 * QBLK, rb = RBASE(s + 2);
        __builtin_amdgcn_raw_buffer_store_b32(vm.x, rs_x, lz * 4 + 2048, kb, 16); __builtin_amdgcn_raw_buffer_store_b32(vm.z, rs_x, lz * 4 + 2304, kb, 16);
        __builtin_amdgcn_raw_buffer_store_b128((v4u){nosent(Km.x), nosent(Km.y), nosent(Km.z), nosent(Km.w)}, rs_x, (lz & 2) ? 0x7ffffff0 : (2 * (lz >> 2) + (lz & 1)) * 16, kb, 16);
        __builtin_amdgcn_raw_buffer_store_b128((v4u){~0u, ~0u, ~0u, ~0u}, rs_m, (alw & 3) != 0 ? 0x7ffffff0 : lz < 32 ? ROFF_M + ALM + alw * 128 + lz * 16 : (lz == 32 && b == 0 ? ROFF_M + RECO + 128 : 0x7ffffff0), rb, 16); }
        asm volatile("s_waitcnt vmcnt(%1)" : "+v"(al) : "i"(QK_ALW + 4) : "memory");
        pot = po;
      if (s < NSEG - 1) {
        unsigned recD = __builtin_amdgcn_readlane(al, 63);
        tA = __builtin_amdgcn_readlane(al, 62);
        if (s > 0) {
          if (__builtin_expect(tA == 0xffffffffu || recD == 0xffffffffu, 0)) rec_get(tA, recD, rs_m, mb + RECO, tmo);
          d1p = recD & 0xffffu;
          unsigned tB = tA - (recD >> 16);
          al_eval(al, tB);
          if (__builtin_expect((int)(pot + TP(P.sh3, SH3) - tB) > 0, 0)) tB = pot + TP(P.sh3, SH3);
          tA = tB + (recD >> 16);
        } else tA = pot;
        PF_ADDR;
        kvok = max(kvok, KV_SEGS(kfl));
        if (__builtin_expect(kvok <= s + 1, 0)) WAIT_KV(s + 1);
        LOAD_KV(s + 1);
        LD_ASYNC(kfl, kvok < s + 5 && kvok < NSEG && tz < 2 ? tz * 4096 : 0x7ffffff0, rs_c, (int)offsetof(Ctl, prg) + h * 128);
        const int po2 = s + 2 < kvok && !(tz & 32) ? tz * 128 : 0x7ffffff0;
        LD_ASYNC(pz, po2, rs_x, (2 * (s + 2) + (w & 1)) * QBLK + ((w >> 1) ? xv : xk));
      }
      }
    }
    check_in(P, tag, w);
  } else {
    LANE_CONSTS;
    const unsigned c00 = (unsigned)now(), t00 = rtnow();
    const int c = b - NQK, cg = c >> 1, rh = c & 1;
    v32u wa[8], wv_[4];
#define VCH(j) (8 * (w & 1) + 2 * ((j) ^ xq) + (w >> 1))
#define OHEAD(i) (4 * ((i) ^ (2 * xq)) + w)
    {
      const unsigned stg = (unsigned)(size_t)Lraw + w * 32768;
      const unsigned vb = stg + wrow * 128 + (l15 & 3) * 8 + ((w ^ 3 ^ wsw) << 5);
      const __amdgpu_buffer_rsrc_t rv = __builtin_amdgcn_make_buffer_rsrc((void*)(P.Wv + 64 * c), 0, DM * HDA * 2 - 128 * c, 0x00020000);
      int t_, ho, n_;
      asm volatile(VO_LOADER : VO_DEFW, [t] "=&s"(t_), [ho] "=&s"(ho), [n] "=&s"(n_), [rec] "=&v"(rdy)
        : [sv] "v"((lane >> 3) * HDA * 2 + (((((lane & 7) >> 1) ^ ((lane >> 4) & 3)) << 1) | (lane & 1)) * 16), [so] "v"((lane >> 3) * DM * 2 + (lane & 7) * 16), [vb0] "v"(vb), [vb1] "v"(vb ^ 32), [vb2] "v"(vb ^ 64), [vb3] "v"(vb ^ 96),
          [ob] "v"(stg + (32 * (lq >> 1) + 2 * (lq & 1) + 4 * (l15 >> 3) + 16 * ((l15 >> 2) & 1)) * 128 + (cg & 1) * 64 + (l15 & 3) * 8),
          [ro] "v"(lane * 128), [rr] "s"(rs_r), [stg] "s"(stg), [rv] "s"(rv), [vk] "s"(VCH(0) * 0x80000), [wo] "s"(P.Wo + 64 * (cg >> 1)), [wsz] "s"(HDA * DM * 2 - 128 * (cg >> 1)), [hx] "s"(OHEAD(0) * 0x40000), [k0] "s"(c & 3)
        : "s48", "s49", "s50", "s51", "s52", "s53", "scc", "memory");
    }
    asm volatile("" :: "v"(cz));
    const int rpl = (int)(rdy_wait(P, rdy, tag, pk) >> 63) * 8192;
    const unsigned lb0 = (unsigned)(size_t)Lraw;
    unsigned vw[3];
#pragma unroll
    for (int t = 0; t < 3; ++t) vw[t] = lb0 + t * 2064 + ((t ^ w ^ 3) * 64 + lane) * 8;
    const unsigned vr = lb0 + tid * 8, rw = lb0 + 8192 + tid * 8, fa = lb0 + 16384;
    int oo[8];
#pragma unroll
    for (int i = 0; i < 8; ++i) oo[i] = dbl(OHEAD(i) * 2048) + ((lq * 2 + rh) * 16 + l15) * 16;
    int ga[4];
#pragma unroll
    for (int j = 0; j < 4; ++j) ga[j] = mem_lane_off - dbl(w * 4096) + dbl(VCH(j) * 1024);
    const int vo = dbl(c * 1024) + tid * 4, mo = dbl(c * 256) + (32 * (l15 >> 3) + 8 * lq + (l15 & 7)) * 4;
    const bool keeper = (c == 0 && w == 0);
    int d1 = TP(P.dmv, DMV) * 16, d2 = TP(P.dov, DOV) * 16, m1 = TP(P.im, IMARG) * 16, m2 = TP(P.im, IMARG) * 16;
    int LV = 70;
    bool prev_clean = false; int nbad = 0;
    unsigned recD = 140;
    const float tpc = 16.f * (float)(rtnow() - t00) / (float)((unsigned)now() - c00);
    int e1 = __builtin_amdgcn_readfirstlane((int)(TP(P.dmv, DMV) * tpc)), e2 = __builtin_amdgcn_readfirstlane((int)(TP(P.dov, DOV) * tpc));
    d1 = e1 + m1 + TP(P.dq2, DQ2) * 16; d2 = e2 + m2 + TP(P.dq, DQ) * 16;
    if (tid < 32) ((int*)(Lraw + 16384))[tid] = 0;
    const int alk = !keeper ? 0x7ffffff0 : (lane < 24 ? ALM + lane * 128 : (lane == 63 ? RECO + 128 : 0x7ffffff0));
    const int al1 = lane < 16 ? ALM + lane * 128 : 0x7ffffff0;
    const int rso = w == 1 ? ROFF_V + dbl(c * 1024) + lane * 16 : (w != 2 ? 0x7ffffff0 : (lane < 16 ? ROFF_M + dbl(c * 256) + lane * 16 :
                    (lane >= 32 && alw >= 0 ? ROFF_M + ALM + (alw - 6) * 128 + lane * 16 : (lane == 16 && c == 0 ? ROFF_M + RECO : 0x7ffffff0))));
    const int boot = c == 0 ? TP(P.pad, BOOT) & 255 : TP(P.pad, BOOT) >> 8;
    unsigned tB = 0; int mb = RBASE(0) + ROFF_M, ob = 0;
    for (int s = 0; s < NSEG; ++s) {
      unsigned alr, pm, fw; unsigned long long te; v2u rec;
      const int mbn = RBASE(s) + ROFF_M, d2r = recD >> 16;
#define VO_ST0
#define VO_ST1
#define VO_ST3
#define VO_STO
      { unsigned cnt; unsigned long long en;
        asm volatile(VO_BODY : [al] "=&v"(alr), [rec] "=&v"(rec), [te] "=&s"(te), [cnt] "=&s"(cnt), [en] "=&s"(en), [t] "+s"(tB), [ta] "=&s"(tA), [pm] "=&s"(pm), [fw] "=&s"(fw) VO_STO
          : VO_W, [oo0] "v"(oo[0]), [oo1] "v"(oo[1]), [oo2] "v"(oo[2]), [oo3] "v"(oo[3]), [oo4] "v"(oo[4]), [oo5] "v"(oo[5]), [oo6] "v"(oo[6]), [oo7] "v"(oo[7]), [idx] "v"(idx), [rw] "v"(rw), [mo] "v"(mo), [fa] "v"(fa), [l4] "v"(lane * 4), [al1] "v"(al1),
            [ga0] "v"(ga[0]), [ga1] "v"(ga[1]), [ga2] "v"(ga[2]), [ga3] "v"(ga[3]), [alo] "v"(alk), [vw0] "v"(vw[0]), [vw1] "v"(vw[1]), [vw2] "v"(vw[2]), [vr] "v"(vr), [vo] "v"(vo), [rso] "v"(rso), [rb] "s"(RBASE(s + 2)),
            [w] "s"(w), [rsm] "s"(rs_m), [rsc] "s"(rs_c), [ob] "s"(ob), [mb] "s"(mb), [mbn] "s"(mbn), [vb] "s"(RBASE(s) + ROFF_V), [rco] "s"(mbn + RECO), [tmo] "s"((int)offsetof(Ctl, tmo)), [s1] "s"(s),
            [lk] "s"(TP(P.lk1, LK1)), [als] "s"(mb + ALM + (alw & 31) * 128), [alx] "s"(alx), [dv] "s"(TP(P.dv, DVV)),
            [d2] "s"(d2r), [mn] "s"(s == 1 ? boot : 10), [shf] "s"(s == 1 ? boot : TP(P.sh4, SH4)), [c] "s"(c), [pw] "s"(__builtin_amdgcn_readfirstlane(s == 0 ? 1 : w ^ 3))
          : VO_CLOB, "vcc", "scc", "memory"); }
      const unsigned tv = __builtin_amdgcn_readfirstlane((unsigned)te);
      if (c == 2 && tid == 0 && s > 0) __hip_atomic_store((unsigned long long*)&P.ctl->prog[0], (unsigned)(s - 1) | ((unsigned long long)(rec.x + (rec.y & 0xffffu)) << 32), RLX_AGENT);
      if (keeper && s > 0) {
        const v4u kz = *(const v4u*)(Lraw + 16384 + 144); pm = kz.x; tB = kz.y; fw = kz.z; tA = kz.w;
        const unsigned long long f = __ballot((alr >> 16) == 1u);
        const bool kclean = __builtin_amdgcn_readfirstlane(fw) != s, okA = (f & 0xffff) == 0, okB = kclean && ((f >> 16) & 255) == 0;
        const int kx1 = __builtin_amdgcn_readlane(alr, 63), kx2 = __builtin_amdgcn_readfirstlane(pm - tB);
        {
          const int gain = s < 2 ? 0 : (s < 5 ? TP(P.gain0, GAIN0) : TP(P.gain, GAIN)), inc = s < 2 ? 0 : TP(P.inc, MINC);
          e1 += okA && kx1 != -1 ? (min(kx1 * 16 - e1, 320) * gain) >> 4 : 0;
          e2 += kclean ? (min(kx2 * 16 - e2, 320) * gain) >> 4 : 0;
          nbad = okA && okB ? 0 : nbad + 1;
          const int far = nbad >= 4 ? 2 * inc : 0;
          m1 = min(max(m1 + far + (!okA ? 0 : (okB ? -TP(P.dec, MDEC) - (m1 >> 3) : inc)), 0), 800);
          m2 = min(max(m2 + far + (okA ? -TP(P.dec, MDEC) - (m2 >> 3) : (prev_clean ? inc : 0)), 0), 800);
          d1 = e1 + m1 + TP(P.dq2, DQ2) * 16; d2 = e2 + m2 + TP(P.dq, DQ) * 16;
        }
        prev_clean = okB;
      }
      if (keeper) {
        const unsigned r1 = (d1 >> 4) + (s < 2 ? (TP(P.boot2, BOOT2) >> (8 * s)) & 255 : 0), r2 = (d2 >> 4) + (s < 2 ? (TP(P.boot3, BOOT3) >> (8 * s)) & 255 : 0);
        *(v2u*)(Lraw + 16384 + 128) = (v2u){r1 + r2, r1 | (r2 << 16)};
      }
      tB = tv; recD = 0;
      if (s > 0 && w == 3) {
        unsigned recA = __builtin_amdgcn_readfirstlane(rec.x); recD = __builtin_amdgcn_readfirstlane(rec.y);
        if (__builtin_expect(recA == 0xffffffffu || recD == 0xffffffffu, 0)) rec_get(recA, recD, rs_m, mbn + RECO, tmo);
        tB = recA - (recD >> 16);
        if ((int)(tB - tv) > 20000) tB = tv;
      }
      tB = __builtin_amdgcn_readfirstlane(tB); recD = __builtin_amdgcn_readfirstlane(recD);
      mb = mbn; ob = RBASE(s) + ROFF_O;
    }
    check_in(P, tag, w);
  }
}

extern "C" void kernel_launch(void* const* d_in, const int* in_sizes, int n_in,
                              void* d_out, int out_size, void* d_ws, size_t ws_size,
                              hipStream_t stream) {
  const u16* A_x = (const u16*)d_in[0];
  const u16* A_mem0 = (const u16*)d_in[1];
  const u16* A_Wq = (const u16*)d_in[2];
  const u16* A_Wk = (const u16*)d_in[3];
  const u16* A_Wv = (const u16*)d_in[4];
  const u16* A_Wo = (const u16*)d_in[5];
  const u16* B_x = (const u16*)d_in[7];
  const u16* B_wqkv = (const u16*)d_in[8];
  const u16* B_wo = (const u16*)d_in[9];
  u16* out = (u16*)d_out;

  char* ws = (char*)d_ws;
  size_t off = 0;
  auto alloc = [&](size_t bytes) { char* p = ws + off; off += (bytes + 255) & ~(size_t)255; return p; };
  char* rdy = alloc(RDY_SIZE);
  Ctl* ctl = (Ctl*)alloc(sizeof(Ctl));
  unsigned long long* prof = (unsigned long long*)alloc(96 * 4 * 72 * 8 + 8 * 256 * 16 + 64 * 384 * 8);
  off += (size_t)(-(long long)(size_t)(ws + off)) & (QPAGE - 1);
  char* ring = alloc(QPAGE);
  u16* qkvA = (u16*)alloc(QSIZE);

  FParams cp;
  cp.Ax = A_x; cp.mem0 = A_mem0; cp.Bx = B_x; cp.Wq = A_Wq; cp.Wk = A_Wk; cp.Wv = A_Wv; cp.Wo = A_Wo; cp.wqkvB = B_wqkv; cp.woB = B_wo;
  cp.qkvA = qkvA; cp.out = out; cp.ring = ring; cp.rdy = rdy;
  cp.ctl = ctl; cp.prof = prof;
  k_fused<<<256, 256, 0, stream>>>(cp);
}
```

```cpp
#include <hip/hip_runtime.h>
#include <hip/hip_bf16.h>
#include <stdint.h>
#include <stddef.h>

#define DQ 13
#define DQ2 12
#define DMV 2440
#define DOV 1410
#define GAIN 4
#define GAIN0 8
#define MINC 32
#define MDEC 3
#define STAG 1300
#define SH1 14
#define PTH 620
#define SH3 6
#define DVV 0
#define BOOT 0x2850
#define PXV (600 * 65536 + 32)
#define PBV (1400 * 65536)
#define PROBES 6
#define JB 96
#define BOOT2 0x0505
#define BOOT3 0x0a14
#define IMARG 3
#define PFR 0
#define OJ1 4
#define LK1 34
#define SH5 0
#define SH0 10
#define SH4 24
#define XA1 34
#define XA2 14
#define XSEG0 18
#define VSLOT 60
#define TP(run, fixed) (fixed)
typedef unsigned short u16;
typedef __attribute__((ext_vector_type(4))) float f32x4;
typedef unsigned v4u __attribute__((ext_vector_type(4)));

constexpr int S_ = 2048, DM = 1024, HA = 32, DHA = 128, HDA = 4096, MEM = 8, SEG = 32, NSEG = 64;
constexpr int QPAGE = 2 << 20, QBLK = 16384, NPAGE = 60, QSIZE = (NPAGE + 8) * QPAGE;
__device__ __forceinline__ int qoff(unsigned long long parq, int n) { return n * QPAGE + (int)((parq >> n) & 1) * 8192; }
__device__ __forceinline__ int kvoff(unsigned pk, int h) { return (NPAGE + (h >> 2)) * QPAGE + (int)((pk >> (h >> 2)) & 1) * 8192 + ((h >> 1) & 1) * QBLK + (h & 1) * 4096; }
constexpr int DB = 1024, HB = 32, DHB = 32, NQKVB = 3 * DB;

constexpr int ROFF_O = 0, ROFF_V = 131072, ROFF_M = 262144, RSLOT = 327680, RSIZE = 4 * RSLOT;
typedef __attribute__((ext_vector_type(8))) __bf16 bfx8;
typedef __attribute__((ext_vector_type(2))) __bf16 bfx2;
typedef __attribute__((ext_vector_type(2))) float f32x2;
typedef unsigned v2u __attribute__((ext_vector_type(2)));
typedef unsigned v8u __attribute__((ext_vector_type(8)));
typedef unsigned v32u __attribute__((ext_vector_type(32)));
constexpr int NQK = 32, NVO = 64;
constexpr int VXLD = 144, LDS_ALL = 163840;
template <int I> struct IC { static constexpr int v = I; };
template <int I, int N, class F> __device__ __forceinline__ void sfor(F&& f) { if constexpr (I < N) { f(IC<I>{}); sfor<I + 1, N>(f); } }
#define SFOR(u, a, b) sfor<a, b>([&](auto u##_) __attribute__((always_inline)) { constexpr int u = decltype(u##_)::v;
#define SEND });

__device__ __forceinline__ void lds_barrier() { asm volatile("s_waitcnt lgkmcnt(0)" ::: "memory"); __builtin_amdgcn_s_barrier(); asm volatile("" ::: "memory"); }
template <int N> __device__ __forceinline__ float dpp_ror(float v) {
  return __int_as_float(__builtin_amdgcn_update_dpp(0, __float_as_int(v), 0x120 + N, 0xf, 0xf, false));
}
__device__ __forceinline__ unsigned pk2(float a, float b) { f32x2 t = {a, b}; return __builtin_bit_cast(unsigned, __builtin_convertvector(t, bfx2)); }
__device__ __forceinline__ unsigned nosent(unsigned x) { return x < 0xfffffffeu ? x : 0xfffffffeu; }
__device__ __forceinline__ unsigned umax3(unsigned a, unsigned b, unsigned c) { unsigned m = a > b ? a : b; return m > c ? m : c; }
__device__ __forceinline__ unsigned chk4(unsigned m, v4u v) { return umax3(umax3(m, v.x, v.y), v.z, v.w); }
__device__ __forceinline__ unsigned long long now() { return __builtin_amdgcn_s_memtime(); }
__device__ __forceinline__ f32x4 mf(v4u a, v4u b, f32x4 c) {
  return __builtin_amdgcn_mfma_f32_16x16x32_bf16(__builtin_bit_cast(bfx8, a), __builtin_bit_cast(bfx8, b), c, 0, 0, 0);
}
__device__ __forceinline__ v2u trrd(const u16* p) {
  typedef __attribute__((ext_vector_type(4))) short v4i16_t;
  return __builtin_bit_cast(v2u, __builtin_amdgcn_ds_read_tr16_b64_v4i16((__attribute__((address_space(3))) v4i16_t*)p));
}
__device__ __forceinline__ v8u cat8(v4u a, v4u b) { return __builtin_shufflevector(a, b, 0, 1, 2, 3, 4, 5, 6, 7); }
__device__ __forceinline__ float xmax16(float x) { auto r = __builtin_amdgcn_permlane16_swap(__float_as_uint(x), __float_as_uint(x), false, false); return fmaxf(__uint_as_float(r[0]), __uint_as_float(r[1])); }
__device__ __forceinline__ float xmax32(float x) { auto r = __builtin_amdgcn_permlane32_swap(__float_as_uint(x), __float_as_uint(x), false, false); return fmaxf(__uint_as_float(r[0]), __uint_as_float(r[1])); }
__device__ __forceinline__ float xsum16(float x) { auto r = __builtin_amdgcn_permlane16_swap(__float_as_uint(x), __float_as_uint(x), false, false); return __uint_as_float(r[0]) + __uint_as_float(r[1]); }
__device__ __forceinline__ float xsum32(float x) { auto r = __builtin_amdgcn_permlane32_swap(__float_as_uint(x), __float_as_uint(x), false, false); return __uint_as_float(r[0]) + __uint_as_float(r[1]); }
__device__ __forceinline__ int dbl(int off) { return ((off >> 13) << 14) | (off & 8191); }
__device__ __forceinline__ bool spin_fail(unsigned spins, unsigned* tmo, unsigned code) {
  if ((spins & 31u) != 31u) return false;
  if (spins > (1u << 15)) { __hip_atomic_store(tmo, code, __ATOMIC_RELAXED, __HIP_MEMORY_SCOPE_AGENT); return true; }
  return __builtin_amdgcn_readfirstlane(__hip_atomic_load(tmo, __ATOMIC_RELAXED, __HIP_MEMORY_SCOPE_AGENT)) != 0u;
}
#define SB0 __builtin_amdgcn_sched_barrier(0)
#define NTQ 0
#define QGAP 0
#define PSL 48
#define TSL 2
__device__ __forceinline__ int opaque_lane() { int l; asm volatile("v_mbcnt_lo_u32_b32 %0, -1, 0\n\tv_mbcnt_hi_u32_b32 %0, -1, %0" : "=v"(l)); return l & 63; }

#define GPACE 0
#define PN_LIM ""
#define OUTAUX 16
namespace wg {
constexpr int LDS_USE = 131072;
__device__ __forceinline__ void* uni(const void* p) { const unsigned long long x = (unsigned long long)p; return (void*)(((unsigned long long)(unsigned)__builtin_amdgcn_readfirstlane(x >> 32) << 32) | (unsigned)__builtin_amdgcn_readfirstlane(x)); }
template <int MT>
__device__ __forceinline__ void gemm_tile(const u16* A, int lda, const u16* B, int ldb, int nst  , void* C, int ldc, unsigned long long parq  , char* lds, int w) {
  constexpr bool apack = true, cpack = false;
  constexpr int MI = MT / 32, PA = MT / 64, NP = PA + 4;
  constexpr int A_B = MT * 64, ST = A_B + 16384;
  const int lane = opaque_lane();
  const int l15 = lane & 15, lq = lane >> 4, wm = w >> 1, wn = w & 1;
  lda = __builtin_amdgcn_readfirstlane(lda); ldb = __builtin_amdgcn_readfirstlane(ldb); ldc = __builtin_amdgcn_readfirstlane(ldc); nst = __builtin_amdgcn_readfirstlane(nst);
  f32x4 acc[8][MI];
#pragma unroll
  for (int i = 0; i < 8; ++i)
#pragma unroll
    for (int j = 0; j < MI; ++j) acc[i][j] = (f32x4){0.f, 0.f, 0.f, 0.f};
  const unsigned lb = (unsigned)(size_t)lds;
  auto fsw = [](int x) { return ((x ^ (x >> 1)) & 1) << 1 | (x >> 1); };
  unsigned ao[2], bo[2][8];
  ao[0] = lb + wm * (MT / 2) * 64 + (apack ? lane * 16 : l15 * 64 + ((lq ^ fsw(l15 >> 2)) << 4)); ao[1] = ao[0] + 2 * ST;
  { const int hh = (l15 >> 2) | ((lq & 1) << 2);
#pragma unroll
    for (int i = 0; i < 8; ++i) { bo[0][i] = lb + A_B + (8 * lq + (l15 >> 2)) * 512 + ((16 * wn + ((2 * i) ^ (2 * hh)) + ((l15 >> 1) & 1)) << 4) + (l15 & 1) * 8; bo[1][i] = bo[0][i] + 2 * ST; } }
  __amdgpu_buffer_rsrc_t ra = __builtin_amdgcn_make_buffer_rsrc(uni(A), 0, 0x7fffffff, 0x00020000), rb = __builtin_amdgcn_make_buffer_rsrc(uni(B), 0, 0x7fffffff, 0x00020000);
  const int va = (16 * PA * w * lda + (apack ? lane * 8 : (lane >> 2) * lda + (((lane & 3) ^ fsw(lane >> 4)) << 3))) * 2, ska = __builtin_amdgcn_readfirstlane(apack ? 1024 : 64);
  const int vb0 = ((8 * w + (lane >> 5)) * ldb + (((lane & 31) ^ ((lane >> 5) << 1) ^ ((w & 1) << 3)) << 3)) * 2, vb1 = vb0 ^ 64;
  const int sa = 32 * lda, sb = 4 * ldb, skb = 64 * ldb;
  const unsigned ma = lb + PA * w * 1024, mb = lb + A_B + w * 4096;
#define PIECE1(NT, base, imm, vo, rs, so) do { if constexpr (NT) asm volatile("s_add_u32 m0, %0, %1\n\ts_nop 0\n\tbuffer_load_dwordx4 %2, %3, %4 offen nt lds" :: "s"(base), "i"(imm), "v"(vo), "s"(rs), "s"(so) : "memory", "scc"); \
    else asm volatile("s_add_u32 m0, %0, %1\n\ts_nop 0\n\tbuffer_load_dwordx4 %2, %3, %4 offen lds" :: "s"(base), "i"(imm), "v"(vo), "s"(rs), "s"(so) : "memory", "scc"); } while (0)
#define PIECE(q, st) do { \
    if ((q) < PA) PIECE1(false, ma, (st) * ST + (q) * 1024, va, ra, ka + (q) * sa); \
    else PIECE1(false, mb, (st) * ST + ((q) - PA) * 1024, (((q) - PA) & 1) ? vb1 : vb0, rb, kb + ((q) - PA) * sb); } while (0)
#define RDA(f, j, st) asm volatile("ds_read_b128 %0, %1 offset:%2" : "=v"(fa[f][j]) : "v"(ao[(st) >> 1]), "i"(((st) & 1) * ST + (j) * 1024) : "memory")
#define RDB(f, i, h, st) asm volatile("ds_read_b64_tr_b16 %0, %1 offset:%2" : "=v"(fb[f][i][h]) : "v"(bo[(st) >> 1][i]), "i"(((st) & 1) * ST + (h) * 2048) : "memory")
#define TIE(f) do { _Pragma("unroll") for (int j = 0; j < MI; ++j) asm volatile("" : "+v"(fa[f][j])); \
    _Pragma("unroll") for (int i = 0; i < 8; ++i) asm volatile("" : "+v"(fb[f][i][0]), "+v"(fb[f][i][1])); } while (0)
  v4u fa[2][MI]; v2u fb[2][8][2];
  int ka = qoff(parq, 0), kb = 0;
  asm volatile("s_nop 4" :: "s"(ra), "s"(rb), "s"(sa), "s"(sb), "s"(skb), "s"(ska));
#pragma unroll
  for (int st = 0; st < 4; ++st) {
#pragma unroll
    for (int q = 0; q < NP; ++q) PIECE(q, st);
    ka += ska; kb += skb;
  }
  asm volatile("s_waitcnt vmcnt(%0)\n\ts_barrier" :: "i"(3 * NP) : "memory");
#pragma unroll
  for (int i = 0; i < 8; ++i) { RDB(0, i, 0, 0); RDB(0, i, 1, 0); }
#pragma unroll
  for (int j = 0; j < MI; ++j) RDA(0, j, 0);
  int k4 = 0;
  do {
    if (apack && (k4 & 4)) ka = qoff(parq, (k4 + 4) >> 3);
    if (k4 + 4 >= nst) { ra = __builtin_amdgcn_make_buffer_rsrc(uni(C), 0, 0, 0x00020000); rb = ra; }
#pragma unroll
    for (int st = 0; st < 4; ++st) {
      constexpr int NM = 8 * MI, NR = 16 + MI;
      const int f = st & 1, sn = (st + 1) & 3;
      asm volatile("s_waitcnt vmcnt(%0) lgkmcnt(0)\n\ts_barrier" :: "i"(2 * NP) : "memory");
      TIE(f);
      SB0;
#pragma unroll
      for (int u = 0; u < NM; ++u) {
        const int j = u >> 3, i = u & 7;
        asm volatile("v_mfma_f32_16x16x32_bf16 %0, %1, %2, %0" : "+a"(acc[i][j]) : "v"((v4u){fb[f][i][0].x, fb[f][i][0].y, fb[f][i][1].x, fb[f][i][1].y}), "v"(fa[f][j]));
        SB0;
        constexpr int RS = MT == 256 ? 2 : 1;
        if (u % RS == 0 && u / RS < NR) {
          const int r = u / RS;
          if (r < 16) RDB(f ^ 1, r >> 1, r & 1, sn); else RDA(f ^ 1, r - 16, sn);
          if (MT == 64 && (u == 0 || u == 2)) RDA(f ^ 1, u >> 1, sn);
          SB0;
        }
        constexpr int PS = NM / 8;
        if (u % PS == PS - 1 && u / PS < NP) { PIECE(u / PS, st); SB0; }
      }
      ka += ska; kb += skb;
    }
    k4 += 4;
  } while (k4 < nst);
#undef PIECE
#undef PIECE1
#undef RDA
#undef RDB
  asm volatile("s_nop 7\n\ts_waitcnt lgkmcnt(0)" ::: "memory");
  SB0;
  TIE(0);
#undef TIE
  char* st = lds + LDS_USE + 2048 + w * 4096;
  const __amdgpu_buffer_rsrc_t rs = __builtin_amdgcn_make_buffer_rsrc(uni(C), 0, MT * ldc * 2 - 1536 * 2, 0x00020000);
  const int so = wm * (MT / 2) * ldc * 2 + (cpack ? wn * 4096 + lane * 16 : (lq * ldc + wn * 128 + l15 * 8) * 2), sq = cpack ? 1024 : 8 * ldc;
#pragma unroll
  for (int j = 0; j < MI; ++j) {
    v2u p[8]; v4u v[4];
#pragma unroll
    for (int i = 0; i < 8; ++i) { asm volatile("" : "+a"(acc[i][j])); p[i] = (v2u){pk2(acc[i][j][0], acc[i][j][1]), pk2(acc[i][j][2], acc[i][j][3])}; }
#pragma unroll
    for (int q = 0; q < 4; ++q) { v[q] = (v4u){p[2 * q].x, p[2 * q].y, p[2 * q + 1].x, p[2 * q + 1].y}; asm volatile("" : "+v"(v[q])); }
    if (!cpack) {
#pragma unroll
      for (int i = 0; i < 8; ++i) *(v2u*)(st + l15 * 256 + (((2 * i + (lq >> 1)) ^ l15) << 4) + (lq & 1) * 8) = (i & 1) ? (v2u){v[i >> 1].z, v[i >> 1].w} : (v2u){v[i >> 1].x, v[i >> 1].y};
      asm volatile("" ::: "memory");
#pragma unroll
      for (int q = 0; q < 4; ++q) v[q] = *(const v4u*)(st + (4 * q + lq) * 256 + ((l15 ^ (4 * q + lq)) << 4));
      asm volatile("" ::: "memory");
    }
#pragma unroll
    for (int q = 0; q < 4; ++q) {
      __builtin_amdgcn_raw_buffer_store_b128(v[q], rs, so, 32 * j * ldc + q * sq, OUTAUX);
    }
    if (GPACE) __builtin_amdgcn_s_sleep(GPACE);
    SB0;
  }
  asm volatile("s_waitcnt vmcnt(0)" ::: "memory");
  __builtin_amdgcn_s_barrier();
}
}

struct Ctl {
  unsigned qhead[32];
  unsigned prg[192][32];
  unsigned cntAB[16][32];
  unsigned flagO[72][8][32];
  alignas(8) unsigned prog[32];
  unsigned tmo[32];
  unsigned chk[32];
};
struct FParams {
  const u16 *Ax, *mem0, *Bx, *Wq, *Wk, *Wv, *Wo, *wqkvB, *woB;
  u16 *qkvA, *out; char *ring, *rdy;
  Ctl* ctl; unsigned long long* prof;
};
#define RLX_AGENT __ATOMIC_RELAXED, __HIP_MEMORY_SCOPE_AGENT
__device__ __forceinline__ unsigned ld_agent(const unsigned* p) { return __hip_atomic_load(p, RLX_AGENT); }
constexpr int RDY_SIZE = 64 * 128;
__device__ __forceinline__ v4u rdy_load(const FParams& F) { const int lz = opaque_lane(); return __builtin_amdgcn_raw_buffer_load_b128(__builtin_amdgcn_make_buffer_rsrc(F.rdy, 0, RDY_SIZE, 0x00020000), lz * 128, 0, 16); }
__device__ __forceinline__ unsigned long long rdy_tag(v4u r) { return (((unsigned long long)(unsigned)__builtin_amdgcn_readlane(r.y, 63) << 32) | (unsigned)__builtin_amdgcn_readlane(r.x, 63)) + 1; }
__device__ __forceinline__ unsigned long long rdy_wait(const FParams& F, v4u r, unsigned long long& tag, unsigned& pk) {
  tag = rdy_tag(r);
  for (unsigned spins = 0; (~__ballot(r.x == (unsigned)tag && r.y == (unsigned)(tag >> 32)) << 4) != 0 && spins < (1u << 16); ++spins) { __builtin_amdgcn_s_sleep(8); r = rdy_load(F); }
  asm volatile("" ::: "memory");
  const unsigned long long b4 = __ballot(r.z & 4);
  pk = (unsigned)b4 & 255u;
  return (__ballot(r.z & 1) & ~(15ull << 60)) | (__builtin_popcountll(b4 << 4 >> 12) > 26 ? 1ull << 63 : 0ull);
}
__device__ __forceinline__ void check_in(const FParams& F, unsigned long long tag, int w) {
  __syncthreads();
  if (w == 0 && opaque_lane() == 0 && __hip_atomic_fetch_add(&F.ctl->chk[0], 1u, RLX_AGENT) == 255u) __hip_atomic_store((unsigned long long*)(F.rdy + 63 * 128), tag, RLX_AGENT);
}
__device__ __forceinline__ void wg_wait(const unsigned* p, unsigned need, Ctl* ctl, int w) {
  if (w == 0) {
    for (unsigned spins = 0;; ++spins) {
      const bool ok = (p == nullptr) || ld_agent(p) >= need;
      if (__all(ok)) break;
      if (spin_fail(spins, &ctl->tmo[0], 2u)) break;
      __builtin_amdgcn_s_sleep(16);
    }
    __builtin_amdgcn_fence(__ATOMIC_ACQUIRE, "agent");
    asm volatile("s_waitcnt vmcnt(0)" ::: "memory");
  }
  __syncthreads();
}

#define PN_BODY(PN_NT, PN_W) \
          "s_mov_b64 s[40:41], %[xa]\n\ts_mov_b32 s42, 0x400000\n\ts_mov_b32 s43, 0x20000\n\ts_nop 4\n\ts_add_u32 m0, %[wl], 65536\n\ts_mul_i32 %[t], %[ldb], 0\n\t" \
          "buffer_load_dwordx4 %[vw0], %[rw], %[t] offen" PN_NT " lds" PN_W "\n\ts_add_u32 m0, %[wl], 66560\n\ts_mul_i32 %[t], %[ldb], 4\n\tbuffer_load_dwordx4 %[vw0], %[rw], %[t] offen" PN_NT " lds" PN_W "\n\ts_add_u32 m0, %[wl], 67584\n\ts_mul_i32 %[t], %[ldb], 8\n\t" \
          "buffer_load_dwordx4 %[vw1], %[rw], %[t] offen" PN_NT " lds" PN_W "\n\ts_add_u32 m0, %[wl], 68608\n\ts_mul_i32 %[t], %[ldb], 12\n\tbuffer_load_dwordx4 %[vw1], %[rw], %[t] offen" PN_NT " lds" PN_W "\n\ts_add_u32 m0, %[wl], 81920\n\ts_mul_i32 %[t], %[ldb], 64\n\t" \
          "buffer_load_dwordx4 %[vw0], %[rw], %[t] offen" PN_NT " lds" PN_W "\n\ts_add_u32 m0, %[wl], 82944\n\ts_mul_i32 %[t], %[ldb], 68\n\tbuffer_load_dwordx4 %[vw0], %[rw], %[t] offen" PN_NT " lds" PN_W "\n\ts_add_u32 m0, %[wl], 83968\n\ts_mul_i32 %[t], %[ldb], 72\n\t" \
          "buffer_load_dwordx4 %[vw1], %[rw], %[t] offen" PN_NT " lds" PN_W "\n\ts_add_u32 m0, %[wl], 84992\n\ts_mul_i32 %[t], %[ldb], 76\n\tbuffer_load_dwordx4 %[vw1], %[rw], %[t] offen" PN_NT " lds" PN_W "\n\ts_add_u32 m0, %[sl], 0\n\ts_nop 0\n\t" \
          "buffer_load_dwordx4 %[vd0], s[40:43], %[xo] offen lds\n\tbuffer_load_dwordx4 %[vd0], s[40:43], %[xo] offen offset:1024 lds\n\tbuffer_load_dwordx4 %[vd1], s[40:43], %[xo] offen offset:2048 lds\n\tbuffer_load_dwordx4 %[vd1], s[40:43], %[xo] offen offset:3072 lds\n\ts_add_u32 m0, %[sl], 4096\n\ts_nop 0\n\t" \
          "buffer_load_dwordx4 %[vd2], s[40:43], %[xo] offen lds\n\tbuffer_load_dwordx4 %[vd2], s[40:43], %[xo] offen offset:1024 lds\n\tbuffer_load_dwordx4 %[vd3], s[40:43], %[xo] offen offset:2048 lds\n\tbuffer_load_dwordx4 %[vd3], s[40:43], %[xo] offen offset:3072 lds\n\ts_add_u32 %[xo], %[xo], 0x8000\n\ts_add_u32 %[sl], %[sl], 0x8000\n\t" \
          "s_bitset0_b32 %[sl], 17\n\ts_add_u32 m0, %[sl], 0\n\ts_nop 0\n\tbuffer_load_dwordx4 %[vd0], s[40:43], %[xo] offen lds\n\tbuffer_load_dwordx4 %[vd0], s[40:43], %[xo] offen offset:1024 lds\n\tbuffer_load_dwordx4 %[vd1], s[40:43], %[xo] offen offset:2048 lds\n\t" \
          "buffer_load_dwordx4 %[vd1], s[40:43], %[xo] offen offset:3072 lds\n\ts_add_u32 m0, %[sl], 4096\n\ts_nop 0\n\tbuffer_load_dwordx4 %[vd2], s[40:43], %[xo] offen lds\n\tbuffer_load_dwordx4 %[vd2], s[40:43], %[xo] offen offset:1024 lds\n\tbuffer_load_dwordx4 %[vd3], s[40:43], %[xo] offen offset:2048 lds\n\t" \
          "buffer_load_dwordx4 %[vd3], s[40:43], %[xo] offen offset:3072 lds\n\ts_add_u32 %[xo], %[xo], 0x8000\n\ts_add_u32 %[sl], %[sl], 0x8000\n\ts_bitset0_b32 %[sl], 17\n\ts_add_u32 m0, %[wl], 98304\n\ts_mul_i32 %[t], %[ldb], 128\n\t" \
          "buffer_load_dwordx4 %[vw0], %[rw], %[t] offen" PN_NT " lds" PN_W "\n\ts_add_u32 m0, %[wl], 99328\n\ts_mul_i32 %[t], %[ldb], 132\n\tbuffer_load_dwordx4 %[vw0], %[rw], %[t] offen" PN_NT " lds" PN_W "\n\ts_add_u32 m0, %[wl], 100352\n\ts_mul_i32 %[t], %[ldb], 136\n\t" \
          "buffer_load_dwordx4 %[vw1], %[rw], %[t] offen" PN_NT " lds" PN_W "\n\ts_add_u32 m0, %[wl], 101376\n\ts_mul_i32 %[t], %[ldb], 140\n\tbuffer_load_dwordx4 %[vw1], %[rw], %[t] offen" PN_NT " lds" PN_W "\n\ts_add_u32 m0, %[wl], 114688\n\ts_mul_i32 %[t], %[ldb], 192\n\t" \
          "buffer_load_dwordx4 %[vw0], %[rw], %[t] offen" PN_NT " lds" PN_W "\n\ts_add_u32 m0, %[wl], 115712\n\ts_mul_i32 %[t], %[ldb], 196\n\tbuffer_load_dwordx4 %[vw0], %[rw], %[t] offen" PN_NT " lds" PN_W "\n\ts_add_u32 m0, %[wl], 116736\n\ts_mul_i32 %[t], %[ldb], 200\n\t" \
          "buffer_load_dwordx4 %[vw1], %[rw], %[t] offen" PN_NT " lds" PN_W "\n\ts_add_u32 m0, %[wl], 117760\n\ts_mul_i32 %[t], %[ldb], 204\n\tbuffer_load_dwordx4 %[vw1], %[rw], %[t] offen" PN_NT " lds" PN_W "\n\ts_add_u32 m0, %[wl], 131072\n\ts_mul_i32 %[t], %[ldb], 256\n\t" \
          "buffer_load_dwordx4 %[vw0], %[rw], %[t] offen" PN_NT " lds" PN_W "\n\ts_add_u32 m0, %[wl], 132096\n\ts_mul_i32 %[t], %[ldb], 260\n\tbuffer_load_dwordx4 %[vw0], %[rw], %[t] offen" PN_NT " lds" PN_W "\n\ts_add_u32 m0, %[wl], 133120\n\ts_mul_i32 %[t], %[ldb], 264\n\t" \
          "buffer_load_dwordx4 %[vw1], %[rw], %[t] offen" PN_NT " lds" PN_W "\n\ts_add_u32 m0, %[wl], 134144\n\ts_mul_i32 %[t], %[ldb], 268\n\tbuffer_load_dwordx4 %[vw1], %[rw], %[t] offen" PN_NT " lds" PN_W "\n\ts_waitcnt vmcnt(32) lgkmcnt(0)\n\ts_barrier\n\t" \
          "s_add_u32 m0, %[wl], 147456\n\ts_mul_i32 %[t], %[ldb], 320\n\tbuffer_load_dwordx4 %[vw0], %[rw], %[t] offen" PN_NT " lds" PN_W "\n\ts_add_u32 m0, %[wl], 148480\n\ts_mul_i32 %[t], %[ldb], 324\n\tbuffer_load_dwordx4 %[vw0], %[rw], %[t] offen" PN_NT " lds" PN_W "\n\t" \
          "s_add_u32 m0, %[wl], 149504\n\ts_mul_i32 %[t], %[ldb], 328\n\tbuffer_load_dwordx4 %[vw1], %[rw], %[t] offen" PN_NT " lds" PN_W "\n\ts_add_u32 m0, %[wl], 150528\n\ts_mul_i32 %[t], %[ldb], 332\n\tbuffer_load_dwordx4 %[vw1], %[rw], %[t] offen" PN_NT " lds" PN_W "\n\t" \
          "ds_read_b64_tr_b16 a[0:1], %[rt0] offset:0\n\tds_read_b64_tr_b16 a[2:3], %[rt0] offset:1024\n\tds_read_b64_tr_b16 a[4:5], %[rt1] offset:0\n\tds_read_b64_tr_b16 a[6:7], %[rt1] offset:1024\n\tds_read_b64_tr_b16 a[8:9], %[rt0] offset:8192\n\tds_read_b64_tr_b16 a[10:11], %[rt0] offset:9216\n\t" \
          "ds_read_b64_tr_b16 a[12:13], %[rt1] offset:8192\n\tds_read_b64_tr_b16 a[14:15], %[rt1] offset:9216\n\ts_waitcnt vmcnt(32) lgkmcnt(0)\n\ts_barrier\n\ts_add_u32 m0, %[wl], 65536\n\ts_mul_i32 %[t], %[ldb], 384\n\t" \
          "buffer_load_dwordx4 %[vw0], %[rw], %[t] offen" PN_NT " lds" PN_W "\n\ts_add_u32 m0, %[wl], 66560\n\ts_mul_i32 %[t], %[ldb], 388\n\tbuffer_load_dwordx4 %[vw0], %[rw], %[t] offen" PN_NT " lds" PN_W "\n\ts_add_u32 m0, %[wl], 67584\n\ts_mul_i32 %[t], %[ldb], 392\n\t" \
          "buffer_load_dwordx4 %[vw1], %[rw], %[t] offen" PN_NT " lds" PN_W "\n\ts_add_u32 m0, %[wl], 68608\n\ts_mul_i32 %[t], %[ldb], 396\n\tbuffer_load_dwordx4 %[vw1], %[rw], %[t] offen" PN_NT " lds" PN_W "\n\tds_read_b64_tr_b16 a[16:17], %[rt0] offset:16384\n\tds_read_b64_tr_b16 a[18:19], %[rt0] offset:17408\n\t" \
          "ds_read_b64_tr_b16 a[20:21], %[rt1] offset:16384\n\tds_read_b64_tr_b16 a[22:23], %[rt1] offset:17408\n\tds_read_b64_tr_b16 a[24:25], %[rt0] offset:24576\n\tds_read_b64_tr_b16 a[26:27], %[rt0] offset:25600\n\tds_read_b64_tr_b16 a[28:29], %[rt1] offset:24576\n\tds_read_b64_tr_b16 a[30:31], %[rt1] offset:25600\n\t" \
          "s_waitcnt vmcnt(16) lgkmcnt(0)\n\ts_barrier\n\ts_add_u32 m0, %[wl], 81920\n\ts_mul_i32 %[t], %[ldb], 448\n\tbuffer_load_dwordx4 %[vw0], %[rw], %[t] offen" PN_NT " lds" PN_W "\n\ts_add_u32 m0, %[wl], 82944\n\t" \
          "s_mul_i32 %[t], %[ldb], 452\n\tbuffer_load_dwordx4 %[vw0], %[rw], %[t] offen" PN_NT " lds" PN_W "\n\ts_add_u32 m0, %[wl], 83968\n\ts_mul_i32 %[t], %[ldb], 456\n\tbuffer_load_dwordx4 %[vw1], %[rw], %[t] offen" PN_NT " lds" PN_W "\n\ts_add_u32 m0, %[wl], 84992\n\t" \
          "s_mul_i32 %[t], %[ldb], 460\n\tbuffer_load_dwordx4 %[vw1], %[rw], %[t] offen" PN_NT " lds" PN_W "\n\tds_read_b64_tr_b16 a[32:33], %[rt0] offset:32768\n\tds_read_b64_tr_b16 a[34:35], %[rt0] offset:33792\n\tds_read_b64_tr_b16 a[36:37], %[rt1] offset:32768\n\tds_read_b64_tr_b16 a[38:39], %[rt1] offset:33792\n\t" \
          "ds_read_b64_tr_b16 a[40:41], %[rt0] offset:40960\n\tds_read_b64_tr_b16 a[42:43], %[rt0] offset:41984\n\tds_read_b64_tr_b16 a[44:45], %[rt1] offset:40960\n\tds_read_b64_tr_b16 a[46:47], %[rt1] offset:41984\n\ts_waitcnt vmcnt(16) lgkmcnt(0)\n\ts_barrier\n\t" \
          "s_add_u32 m0, %[wl], 98304\n\ts_mul_i32 %[t], %[ldb], 512\n\tbuffer_load_dwordx4 %[vw0], %[rw], %[t] offen" PN_NT " lds" PN_W "\n\ts_add_u32 m0, %[wl], 99328\n\ts_mul_i32 %[t], %[ldb], 516\n\tbuffer_load_dwordx4 %[vw0], %[rw], %[t] offen" PN_NT " lds" PN_W "\n\t" \
          "s_add_u32 m0, %[wl], 100352\n\ts_mul_i32 %[t], %[ldb], 520\n\tbuffer_load_dwordx4 %[vw1], %[rw], %[t] offen" PN_NT " lds" PN_W "\n\ts_add_u32 m0, %[wl], 101376\n\ts_mul_i32 %[t], %[ldb], 524\n\tbuffer_load_dwordx4 %[vw1], %[rw], %[t] offen" PN_NT " lds" PN_W "\n\t" \
          "ds_read_b64_tr_b16 a[48:49], %[rt2] offset:0\n\tds_read_b64_tr_b16 a[50:51], %[rt2] offset:1024\n\tds_read_b64_tr_b16 a[52:53], %[rt3] offset:0\n\tds_read_b64_tr_b16 a[54:55], %[rt3] offset:1024\n\tds_read_b64_tr_b16 a[56:57], %[rt2] offset:8192\n\tds_read_b64_tr_b16 a[58:59], %[rt2] offset:9216\n\t" \
          "ds_read_b64_tr_b16 a[60:61], %[rt3] offset:8192\n\tds_read_b64_tr_b16 a[62:63], %[rt3] offset:9216\n\ts_waitcnt vmcnt(16) lgkmcnt(0)\n\ts_barrier\n\ts_add_u32 m0, %[wl], 114688\n\ts_mul_i32 %[t], %[ldb], 576\n\t" \
          "buffer_load_dwordx4 %[vw0], %[rw], %[t] offen" PN_NT " lds" PN_W "\n\ts_add_u32 m0, %[wl], 115712\n\ts_mul_i32 %[t], %[ldb], 580\n\tbuffer_load_dwordx4 %[vw0], %[rw], %[t] offen" PN_NT " lds" PN_W "\n\ts_add_u32 m0, %[wl], 116736\n\ts_mul_i32 %[t], %[ldb], 584\n\t" \
          "buffer_load_dwordx4 %[vw1], %[rw], %[t] offen" PN_NT " lds" PN_W "\n\ts_add_u32 m0, %[wl], 117760\n\ts_mul_i32 %[t], %[ldb], 588\n\tbuffer_load_dwordx4 %[vw1], %[rw], %[t] offen" PN_NT " lds" PN_W "\n\tds_read_b64_tr_b16 a[64:65], %[rt2] offset:16384\n\tds_read_b64_tr_b16 a[66:67], %[rt2] offset:17408\n\t" \
          "ds_read_b64_tr_b16 a[68:69], %[rt3] offset:16384\n\tds_read_b64_tr_b16 a[70:71], %[rt3] offset:17408\n\tds_read_b64_tr_b16 a[72:73], %[rt2] offset:24576\n\tds_read_b64_tr_b16 a[74:75], %[rt2] offset:25600\n\tds_read_b64_tr_b16 a[76:77], %[rt3] offset:24576\n\tds_read_b64_tr_b16 a[78:79], %[rt3] offset:25600\n\t" \
          "s_waitcnt vmcnt(16) lgkmcnt(0)\n\ts_barrier\n\ts_add_u32 m0, %[wl], 131072\n\ts_mul_i32 %[t], %[ldb], 640\n\tbuffer_load_dwordx4 %[vw0], %[rw], %[t] offen" PN_NT " lds" PN_W "\n\ts_add_u32 m0, %[wl], 132096\n\t" \
          "s_mul_i32 %[t], %[ldb], 644\n\tbuffer_load_dwordx4 %[vw0], %[rw], %[t] offen" PN_NT " lds" PN_W "\n\ts_add_u32 m0, %[wl], 133120\n\ts_mul_i32 %[t], %[ldb], 648\n\tbuffer_load_dwordx4 %[vw1], %[rw], %[t] offen" PN_NT " lds" PN_W "\n\ts_add_u32 m0, %[wl], 134144\n\t" \
          "s_mul_i32 %[t], %[ldb], 652\n\tbuffer_load_dwordx4 %[vw1], %[rw], %[t] offen" PN_NT " lds" PN_W "\n\tds_read_b64_tr_b16 a[80:81], %[rt2] offset:32768\n\tds_read_b64_tr_b16 a[82:83], %[rt2] offset:33792\n\tds_read_b64_tr_b16 a[84:85], %[rt3] offset:32768\n\tds_read_b64_tr_b16 a[86:87], %[rt3] offset:33792\n\t" \
          "ds_read_b64_tr_b16 a[88:89], %[rt2] offset:40960\n\tds_read_b64_tr_b16 a[90:91], %[rt2] offset:41984\n\tds_read_b64_tr_b16 a[92:93], %[rt3] offset:40960\n\tds_read_b64_tr_b16 a[94:95], %[rt3] offset:41984\n\ts_waitcnt vmcnt(16) lgkmcnt(0)\n\ts_barrier\n\t" \
          "s_add_u32 m0, %[wl], 147456\n\ts_mul_i32 %[t], %[ldb], 704\n\tbuffer_load_dwordx4 %[vw0], %[rw], %[t] offen" PN_NT " lds" PN_W "\n\ts_add_u32 m0, %[wl], 148480\n\ts_mul_i32 %[t], %[ldb], 708\n\tbuffer_load_dwordx4 %[vw0], %[rw], %[t] offen" PN_NT " lds" PN_W "\n\t" \
          "s_add_u32 m0, %[wl], 149504\n\ts_mul_i32 %[t], %[ldb], 712\n\tbuffer_load_dwordx4 %[vw1], %[rw], %[t] offen" PN_NT " lds" PN_W "\n\ts_add_u32 m0, %[wl], 150528\n\ts_mul_i32 %[t], %[ldb], 716\n\tbuffer_load_dwordx4 %[vw1], %[rw], %[t] offen" PN_NT " lds" PN_W "\n\t" \
          "ds_read_b64_tr_b16 a[96:97], %[rt0] offset:0\n\tds_read_b64_tr_b16 a[98:99], %[rt0] offset:1024\n\tds_read_b64_tr_b16 a[100:101], %[rt1] offset:0\n\tds_read_b64_tr_b16 a[102:103], %[rt1] offset:1024\n\tds_read_b64_tr_b16 a[104:105], %[rt0] offset:8192\n\tds_read_b64_tr_b16 a[106:107], %[rt0] offset:9216\n\t" \
          "ds_read_b64_tr_b16 a[108:109], %[rt1] offset:8192\n\tds_read_b64_tr_b16 a[110:111], %[rt1] offset:9216\n\ts_waitcnt vmcnt(16) lgkmcnt(0)\n\ts_barrier\n\ts_add_u32 m0, %[wl], 65536\n\ts_mul_i32 %[t], %[ldb], 768\n\t" \
          "buffer_load_dwordx4 %[vw0], %[rw], %[t] offen" PN_NT " lds" PN_W "\n\ts_add_u32 m0, %[wl], 66560\n\ts_mul_i32 %[t], %[ldb], 772\n\tbuffer_load_dwordx4 %[vw0], %[rw], %[t] offen" PN_NT " lds" PN_W "\n\ts_add_u32 m0, %[wl], 67584\n\ts_mul_i32 %[t], %[ldb], 776\n\t" \
          "buffer_load_dwordx4 %[vw1], %[rw], %[t] offen" PN_NT " lds" PN_W "\n\ts_add_u32 m0, %[wl], 68608\n\ts_mul_i32 %[t], %[ldb], 780\n\tbuffer_load_dwordx4 %[vw1], %[rw], %[t] offen" PN_NT " lds" PN_W "\n\tds_read_b64_tr_b16 a[112:113], %[rt0] offset:16384\n\tds_read_b64_tr_b16 a[114:115], %[rt0] offset:17408\n\t" \
          "ds_read_b64_tr_b16 a[116:117], %[rt1] offset:16384\n\tds_read_b64_tr_b16 a[118:119], %[rt1] offset:17408\n\tds_read_b64_tr_b16 a[120:121], %[rt0] offset:24576\n\tds_read_b64_tr_b16 a[122:123], %[rt0] offset:25600\n\tds_read_b64_tr_b16 a[124:125], %[rt1] offset:24576\n\tds_read_b64_tr_b16 a[126:127], %[rt1] offset:25600\n\t" \
          "s_waitcnt vmcnt(16) lgkmcnt(0)\n\ts_barrier\n\ts_add_u32 m0, %[wl], 81920\n\ts_mul_i32 %[t], %[ldb], 832\n\tbuffer_load_dwordx4 %[vw0], %[rw], %[t] offen" PN_NT " lds" PN_W "\n\ts_add_u32 m0, %[wl], 82944\n\t" \
          "s_mul_i32 %[t], %[ldb], 836\n\tbuffer_load_dwordx4 %[vw0], %[rw], %[t] offen" PN_NT " lds" PN_W "\n\ts_add_u32 m0, %[wl], 83968\n\ts_mul_i32 %[t], %[ldb], 840\n\tbuffer_load_dwordx4 %[vw1], %[rw], %[t] offen" PN_NT " lds" PN_W "\n\ts_add_u32 m0, %[wl], 84992\n\t" \
          "s_mul_i32 %[t], %[ldb], 844\n\tbuffer_load_dwordx4 %[vw1], %[rw], %[t] offen" PN_NT " lds" PN_W "\n\tds_read_b64_tr_b16 a[128:129], %[rt0] offset:32768\n\tds_read_b64_tr_b16 a[130:131], %[rt0] offset:33792\n\tds_read_b64_tr_b16 a[132:133], %[rt1] offset:32768\n\tds_read_b64_tr_b16 a[134:135], %[rt1] offset:33792\n\t" \
          "ds_read_b64_tr_b16 a[136:137], %[rt0] offset:40960\n\tds_read_b64_tr_b16 a[138:139], %[rt0] offset:41984\n\tds_read_b64_tr_b16 a[140:141], %[rt1] offset:40960\n\tds_read_b64_tr_b16 a[142:143], %[rt1] offset:41984\n\ts_waitcnt vmcnt(16) lgkmcnt(0)\n\ts_barrier\n\t" \
          "s_add_u32 m0, %[wl], 98304\n\ts_mul_i32 %[t], %[ldb], 896\n\tbuffer_load_dwordx4 %[vw0], %[rw], %[t] offen" PN_NT " lds" PN_W "\n\ts_add_u32 m0, %[wl], 99328\n\ts_mul_i32 %[t], %[ldb], 900\n\tbuffer_load_dwordx4 %[vw0], %[rw], %[t] offen" PN_NT " lds" PN_W "\n\t" \
          "s_add_u32 m0, %[wl], 100352\n\ts_mul_i32 %[t], %[ldb], 904\n\tbuffer_load_dwordx4 %[vw1], %[rw], %[t] offen" PN_NT " lds" PN_W "\n\ts_add_u32 m0, %[wl], 101376\n\ts_mul_i32 %[t], %[ldb], 908\n\tbuffer_load_dwordx4 %[vw1], %[rw], %[t] offen" PN_NT " lds" PN_W "\n\t" \
          "ds_read_b64_tr_b16 a[144:145], %[rt2] offset:0\n\tds_read_b64_tr_b16 a[146:147], %[rt2] offset:1024\n\tds_read_b64_tr_b16 a[148:149], %[rt3] offset:0\n\tds_read_b64_tr_b16 a[150:151], %[rt3] offset:1024\n\tds_read_b64_tr_b16 a[152:153], %[rt2] offset:8192\n\tds_read_b64_tr_b16 a[154:155], %[rt2] offset:9216\n\t" \
          "ds_read_b64_tr_b16 a[156:157], %[rt3] offset:8192\n\tds_read_b64_tr_b16 a[158:159], %[rt3] offset:9216\n\ts_waitcnt vmcnt(16) lgkmcnt(0)\n\ts_barrier\n\ts_add_u32 m0, %[wl], 114688\n\ts_mul_i32 %[t], %[ldb], 960\n\t" \
          "buffer_load_dwordx4 %[vw0], %[rw], %[t] offen" PN_NT " lds" PN_W "\n\ts_add_u32 m0, %[wl], 115712\n\ts_mul_i32 %[t], %[ldb], 964\n\tbuffer_load_dwordx4 %[vw0], %[rw], %[t] offen" PN_NT " lds" PN_W "\n\ts_add_u32 m0, %[wl], 116736\n\ts_mul_i32 %[t], %[ldb], 968\n\t" \
          "buffer_load_dwordx4 %[vw1], %[rw], %[t] offen" PN_NT " lds" PN_W "\n\ts_add_u32 m0, %[wl], 117760\n\ts_mul_i32 %[t], %[ldb], 972\n\tbuffer_load_dwordx4 %[vw1], %[rw], %[t] offen" PN_NT " lds" PN_W "\n\tds_read_b64_tr_b16 a[160:161], %[rt2] offset:16384\n\tds_read_b64_tr_b16 a[162:163], %[rt2] offset:17408\n\t" \
          "ds_read_b64_tr_b16 a[164:165], %[rt3] offset:16384\n\tds_read_b64_tr_b16 a[166:167], %[rt3] offset:17408\n\tds_read_b64_tr_b16 a[168:169], %[rt2] offset:24576\n\tds_read_b64_tr_b16 a[170:171], %[rt2] offset:25600\n\tds_read_b64_tr_b16 a[172:173], %[rt3] offset:24576\n\tds_read_b64_tr_b16 a[174:175], %[rt3] offset:25600\n\t" \
          "s_waitcnt vmcnt(16) lgkmcnt(0)\n\ts_barrier\n\tbuffer_load_dwordx4 v[40:43], %[ro], %[rr], 0 offen sc1\n\tds_read_b64_tr_b16 a[176:177], %[rt2] offset:32768\n\tds_read_b64_tr_b16 a[178:179], %[rt2] offset:33792\n\tds_read_b64_tr_b16 a[180:181], %[rt3] offset:32768\n\t" \
          "ds_read_b64_tr_b16 a[182:183], %[rt3] offset:33792\n\tds_read_b64_tr_b16 a[184:185], %[rt2] offset:40960\n\tds_read_b64_tr_b16 a[186:187], %[rt2] offset:41984\n\tds_read_b64_tr_b16 a[188:189], %[rt3] offset:40960\n\tds_read_b64_tr_b16 a[190:191], %[rt3] offset:41984\n\ts_waitcnt vmcnt(13) lgkmcnt(0)\n\t" \
          "s_barrier\n\tds_read_b64_tr_b16 a[192:193], %[rt0] offset:0\n\tds_read_b64_tr_b16 a[194:195], %[rt0] offset:1024\n\tds_read_b64_tr_b16 a[196:197], %[rt1] offset:0\n\tds_read_b64_tr_b16 a[198:199], %[rt1] offset:1024\n\tds_read_b64_tr_b16 a[200:201], %[rt0] offset:8192\n\t" \
          "ds_read_b64_tr_b16 a[202:203], %[rt0] offset:9216\n\tds_read_b64_tr_b16 a[204:205], %[rt1] offset:8192\n\tds_read_b64_tr_b16 a[206:207], %[rt1] offset:9216\n\ts_waitcnt vmcnt(9) lgkmcnt(0)\n\ts_barrier\n\tds_read_b64_tr_b16 a[208:209], %[rt0] offset:16384\n\t" \
          "ds_read_b64_tr_b16 a[210:211], %[rt0] offset:17408\n\tds_read_b64_tr_b16 a[212:213], %[rt1] offset:16384\n\tds_read_b64_tr_b16 a[214:215], %[rt1] offset:17408\n\tds_read_b64_tr_b16 a[216:217], %[rt0] offset:24576\n\tds_read_b64_tr_b16 a[218:219], %[rt0] offset:25600\n\tds_read_b64_tr_b16 a[220:221], %[rt1] offset:24576\n\t" \
          "ds_read_b64_tr_b16 a[222:223], %[rt1] offset:25600\n\ts_waitcnt vmcnt(5) lgkmcnt(0)\n\ts_barrier\n\tds_read_b64_tr_b16 a[224:225], %[rt0] offset:32768\n\tds_read_b64_tr_b16 a[226:227], %[rt0] offset:33792\n\tds_read_b64_tr_b16 a[228:229], %[rt1] offset:32768\n\t" \
          "ds_read_b64_tr_b16 a[230:231], %[rt1] offset:33792\n\tds_read_b64_tr_b16 a[232:233], %[rt0] offset:40960\n\tds_read_b64_tr_b16 a[234:235], %[rt0] offset:41984\n\tds_read_b64_tr_b16 a[236:237], %[rt1] offset:40960\n\tds_read_b64_tr_b16 a[238:239], %[rt1] offset:41984\n\ts_waitcnt vmcnt(0) lgkmcnt(0)\n\t" \
          "s_barrier\n\tds_read_b64_tr_b16 a[240:241], %[rt2] offset:0\n\tds_read_b64_tr_b16 a[242:243], %[rt2] offset:1024\n\tds_read_b64_tr_b16 a[244:245], %[rt3] offset:0\n\tds_read_b64_tr_b16 a[246:247], %[rt3] offset:1024\n\tds_read_b64_tr_b16 a[248:249], %[rt2] offset:8192\n\t" \
          "ds_read_b64_tr_b16 a[250:251], %[rt2] offset:9216\n\tds_read_b64_tr_b16 a[252:253], %[rt3] offset:8192\n\tds_read_b64_tr_b16 a[254:255], %[rt3] offset:9216\n\ts_waitcnt lgkmcnt(0)\n\ts_barrier\n\ts_add_u32 m0, %[sl], 0\n\t" \
          "s_nop 0\n\tbuffer_load_dwordx4 %[vd0], s[40:43], %[xo] offen lds\n\tbuffer_load_dwordx4 %[vd0], s[40:43], %[xo] offen offset:1024 lds\n\tbuffer_load_dwordx4 %[vd1], s[40:43], %[xo] offen offset:2048 lds\n\tbuffer_load_dwordx4 %[vd1], s[40:43], %[xo] offen offset:3072 lds\n\ts_add_u32 m0, %[sl], 4096\n\t" \
          "s_nop 0\n\tbuffer_load_dwordx4 %[vd2], s[40:43], %[xo] offen lds\n\tbuffer_load_dwordx4 %[vd2], s[40:43], %[xo] offen offset:1024 lds\n\tbuffer_load_dwordx4 %[vd3], s[40:43], %[xo] offen offset:2048 lds\n\tbuffer_load_dwordx4 %[vd3], s[40:43], %[xo] offen offset:3072 lds\n\ts_add_u32 %[xo], %[xo], 0x8000\n\t" \
          "s_add_u32 %[sl], %[sl], 0x8000\n\ts_bitset0_b32 %[sl], 17\n\ts_mov_b32 %[t], 0\n\tv_readlane_b32 s44, v40, 63\n\tv_readlane_b32 s45, v41, 63\n\ts_add_u32 s44, s44, 1\n\t" \
          "s_addc_u32 s45, s45, 0\n\tLpn_r%=:\n\tv_cmp_eq_u64 vcc, s[44:45], v[40:41]\n\ts_add_u32 %[t], %[t], 0x10000\n\ts_cbranch_scc1 Lpn_g%=\n\ts_or_b32 vcc_hi, vcc_hi, 0xf0000000\n\t" \
          "s_and_b32 vcc_lo, vcc_lo, vcc_hi\n\ts_cmp_eq_u32 vcc_lo, -1\n\ts_cbranch_scc1 Lpn_g%=\n\ts_sleep 8\n\tbuffer_load_dwordx4 v[40:43], %[ro], %[rr], 0 offen sc1\n\ts_waitcnt vmcnt(0)\n\t" \
          "s_branch Lpn_r%=\n\tLpn_g%=:\n\tv_readlane_b32 %[t], v42, %[pl]\n\ts_and_b32 %[t], %[t], 1\n\ts_lshl_b32 %[t], %[t], 13\n\ts_add_u32 %[co], %[co], %[t]\n\t" \
          "v_readlane_b32 %[ko], v42, %[kl]\n\ts_and_b32 %[ko], %[ko], 4\n\ts_lshl_b32 %[ko], %[ko], 11\n\tv_mov_b64 v[36:37], -1\n\tv_mov_b64 v[38:39], -1\n\tds_read_b128 v[64:67], %[ra0] offset:0\n\t" \
          "ds_read_b128 v[68:71], %[ra1] offset:0\n\tds_read_b128 v[72:75], %[ra2] offset:0\n\tds_read_b128 v[76:79], %[ra3] offset:0\n\tds_read_b128 v[80:83], %[ra0] offset:256\n\tds_read_b128 v[84:87], %[ra1] offset:256\n\tds_read_b128 v[88:91], %[ra2] offset:256\n\t" \
          "ds_read_b128 v[92:95], %[ra3] offset:256\n\ts_mov_b64 %[sx], 0\n\tLpn_loop%=:\n\ts_waitcnt lgkmcnt(6)\n\tv_mfma_f32_16x16x32_bf16 v[48:51], a[0:3], v[64:67], 0\n\tds_read_b128 v[96:99], %[ra0] offset:512\n\t" \
          "v_mfma_f32_16x16x32_bf16 v[52:55], a[4:7], v[64:67], 0\n\ts_add_u32 %[t], %[j], 3\n\ts_cmp_lt_u32 %[t], %[je]\n\ts_cselect_b32 s42, s42, 0\n\tv_mfma_f32_16x16x32_bf16 v[48:51], a[8:11], v[68:71], v[48:51]\n\tds_read_b128 v[100:103], %[ra1] offset:512\n\t" \
          "v_mfma_f32_16x16x32_bf16 v[52:55], a[12:15], v[68:71], v[52:55]\n\tv_cvt_pk_bf16_f32 v44, v56, v57\n\tv_cvt_pk_bf16_f32 v45, v58, v59\n\ts_waitcnt lgkmcnt(6)\n\tv_mfma_f32_16x16x32_bf16 v[48:51], a[16:19], v[72:75], v[48:51]\n\tds_read_b128 v[104:107], %[ra2] offset:512\n\t" \
          "v_mfma_f32_16x16x32_bf16 v[52:55], a[20:23], v[72:75], v[52:55]\n\tv_cvt_pk_bf16_f32 v46, v60, v61\n\tv_cvt_pk_bf16_f32 v47, v62, v63\n\tv_mfma_f32_16x16x32_bf16 v[48:51], a[24:27], v[76:79], v[48:51]\n\tds_read_b128 v[108:111], %[ra3] offset:512\n\tv_mfma_f32_16x16x32_bf16 v[52:55], a[28:31], v[76:79], v[52:55]\n\t" \
          "s_mov_b64 exec, %[sx]\n\tbuffer_store_dwordx4 v[44:47], %[so], %[rc], %[co] offen sc1\n\ts_mov_b64 exec, -1\n\ts_waitcnt lgkmcnt(6)\n\tv_mfma_f32_16x16x32_bf16 v[48:51], a[32:35], v[80:83], v[48:51]\n\tds_read_b128 v[112:115], %[ra0] offset:768\n\t" \
          "v_mfma_f32_16x16x32_bf16 v[52:55], a[36:39], v[80:83], v[52:55]\n\ts_mov_b64 exec, %[fx]\n\tbuffer_store_dword %[fv], off, %[rf], %[fo] sc1\n\ts_mov_b64 exec, -1\n\tv_mfma_f32_16x16x32_bf16 v[48:51], a[40:43], v[84:87], v[48:51]\n\tds_read_b128 v[116:119], %[ra1] offset:768\n\t" \
          "v_mfma_f32_16x16x32_bf16 v[52:55], a[44:47], v[84:87], v[52:55]\n\ts_add_u32 %[co], %[co], %[cs]\n\ts_mov_b32 %[cs], 0x4000\n\ts_mov_b64 %[sx], -1\n\ts_waitcnt lgkmcnt(6)\n\tv_mfma_f32_16x16x32_bf16 v[48:51], a[48:51], v[88:91], v[48:51]\n\t" \
          "ds_read_b128 v[120:123], %[ra2] offset:768\n\ts_add_u32 m0, %[sl], 0\n\tv_mfma_f32_16x16x32_bf16 v[52:55], a[52:55], v[88:91], v[52:55]\n\tbuffer_load_dwordx4 %[vd0], s[40:43], %[xo] offen lds\n\tv_mfma_f32_16x16x32_bf16 v[48:51], a[56:59], v[92:95], v[48:51]\n\tds_read_b128 v[124:127], %[ra3] offset:768\n\t" \
          "v_mfma_f32_16x16x32_bf16 v[52:55], a[60:63], v[92:95], v[52:55]\n\ts_waitcnt lgkmcnt(6)\n\tv_mfma_f32_16x16x32_bf16 v[48:51], a[64:67], v[96:99], v[48:51]\n\tds_read_b128 v[64:67], %[ra0] offset:1024\n\tv_mfma_f32_16x16x32_bf16 v[52:55], a[68:71], v[96:99], v[52:55]\n\tbuffer_load_dwordx4 %[vd0], s[40:43], %[xo] offen offset:1024 lds\n\t" \
          "v_mfma_f32_16x16x32_bf16 v[48:51], a[72:75], v[100:103], v[48:51]\n\tds_read_b128 v[68:71], %[ra1] offset:1024\n\tv_mfma_f32_16x16x32_bf16 v[52:55], a[76:79], v[100:103], v[52:55]\n\ts_waitcnt lgkmcnt(6)\n\tv_mfma_f32_16x16x32_bf16 v[48:51], a[80:83], v[104:107], v[48:51]\n\tds_read_b128 v[72:75], %[ra2] offset:1024\n\t" \
          "v_mfma_f32_16x16x32_bf16 v[52:55], a[84:87], v[104:107], v[52:55]\n\tbuffer_load_dwordx4 %[vd1], s[40:43], %[xo] offen offset:2048 lds\n\tv_mfma_f32_16x16x32_bf16 v[48:51], a[88:91], v[108:111], v[48:51]\n\tds_read_b128 v[76:79], %[ra3] offset:1024\n\tv_mfma_f32_16x16x32_bf16 v[52:55], a[92:95], v[108:111], v[52:55]\n\ts_waitcnt lgkmcnt(6)\n\t" \
          "v_mfma_f32_16x16x32_bf16 v[48:51], a[96:99], v[112:115], v[48:51]\n\tds_read_b128 v[80:83], %[ra0] offset:1280\n\tv_mfma_f32_16x16x32_bf16 v[52:55], a[100:103], v[112:115], v[52:55]\n\tbuffer_load_dwordx4 %[vd1], s[40:43], %[xo] offen offset:3072 lds\n\tv_mfma_f32_16x16x32_bf16 v[48:51], a[104:107], v[116:119], v[48:51]\n\tds_read_b128 v[84:87], %[ra1] offset:1280\n\t" \
          "v_mfma_f32_16x16x32_bf16 v[52:55], a[108:111], v[116:119], v[52:55]\n\ts_waitcnt lgkmcnt(6)\n\tv_mfma_f32_16x16x32_bf16 v[48:51], a[112:115], v[120:123], v[48:51]\n\tds_read_b128 v[88:91], %[ra2] offset:1280\n\ts_add_u32 m0, %[sl], 4096\n\tv_mfma_f32_16x16x32_bf16 v[52:55], a[116:119], v[120:123], v[52:55]\n\t" \
          "buffer_load_dwordx4 %[vd2], s[40:43], %[xo] offen lds\n\tv_mfma_f32_16x16x32_bf16 v[48:51], a[120:123], v[124:127], v[48:51]\n\tds_read_b128 v[92:95], %[ra3] offset:1280\n\tv_mfma_f32_16x16x32_bf16 v[52:55], a[124:127], v[124:127], v[52:55]\n\ts_waitcnt lgkmcnt(6)\n\tv_mfma_f32_16x16x32_bf16 v[48:51], a[128:131], v[64:67], v[48:51]\n\t" \
          "ds_read_b128 v[96:99], %[ra0] offset:1536\n\tv_mfma_f32_16x16x32_bf16 v[52:55], a[132:135], v[64:67], v[52:55]\n\tbuffer_load_dwordx4 %[vd2], s[40:43], %[xo] offen offset:1024 lds\n\tv_mfma_f32_16x16x32_bf16 v[48:51], a[136:139], v[68:71], v[48:51]\n\tds_read_b128 v[100:103], %[ra1] offset:1536\n\tv_mfma_f32_16x16x32_bf16 v[52:55], a[140:143], v[68:71], v[52:55]\n\t" \
          "s_waitcnt lgkmcnt(6)\n\tv_mfma_f32_16x16x32_bf16 v[48:51], a[144:147], v[72:75], v[48:51]\n\tds_read_b128 v[104:107], %[ra2] offset:1536\n\tv_mfma_f32_16x16x32_bf16 v[52:55], a[148:151], v[72:75], v[52:55]\n\tbuffer_load_dwordx4 %[vd3], s[40:43], %[xo] offen offset:2048 lds\n\tv_mfma_f32_16x16x32_bf16 v[48:51], a[152:155], v[76:79], v[48:51]\n\t" \
          "ds_read_b128 v[108:111], %[ra3] offset:1536\n\tv_mfma_f32_16x16x32_bf16 v[52:55], a[156:159], v[76:79], v[52:55]\n\ts_waitcnt lgkmcnt(6)\n\tv_mfma_f32_16x16x32_bf16 v[48:51], a[160:163], v[80:83], v[48:51]\n\tds_read_b128 v[112:115], %[ra0] offset:1792\n\tv_mfma_f32_16x16x32_bf16 v[52:55], a[164:167], v[80:83], v[52:55]\n\t" \
          "buffer_load_dwordx4 %[vd3], s[40:43], %[xo] offen offset:3072 lds\n\tv_mfma_f32_16x16x32_bf16 v[48:51], a[168:171], v[84:87], v[48:51]\n\tds_read_b128 v[116:119], %[ra1] offset:1792\n\tv_mfma_f32_16x16x32_bf16 v[52:55], a[172:175], v[84:87], v[52:55]\n\ts_waitcnt lgkmcnt(6)\n\tv_mfma_f32_16x16x32_bf16 v[48:51], a[176:179], v[88:91], v[48:51]\n\t" \
          "ds_read_b128 v[120:123], %[ra2] offset:1792\n\tv_mfma_f32_16x16x32_bf16 v[52:55], a[180:183], v[88:91], v[52:55]\n\tv_mfma_f32_16x16x32_bf16 v[48:51], a[184:187], v[92:95], v[48:51]\n\tds_read_b128 v[124:127], %[ra3] offset:1792\n\tv_mfma_f32_16x16x32_bf16 v[52:55], a[188:191], v[92:95], v[52:55]\n\ts_add_u32 %[xo], %[xo], 0x8000\n\t" \
          "s_add_u32 %[sl], %[sl], 0x8000\n\ts_bitset0_b32 %[sl], 17\n\ts_waitcnt lgkmcnt(6)\n\tv_mfma_f32_16x16x32_bf16 v[48:51], a[192:195], v[96:99], v[48:51]\n\tv_mfma_f32_16x16x32_bf16 v[52:55], a[196:199], v[96:99], v[52:55]\n\tv_mfma_f32_16x16x32_bf16 v[48:51], a[200:203], v[100:103], v[48:51]\n\t" \
          "v_mfma_f32_16x16x32_bf16 v[52:55], a[204:207], v[100:103], v[52:55]\n\ts_waitcnt lgkmcnt(4)\n\tv_mfma_f32_16x16x32_bf16 v[48:51], a[208:211], v[104:107], v[48:51]\n\tv_mfma_f32_16x16x32_bf16 v[52:55], a[212:215], v[104:107], v[52:55]\n\tv_mfma_f32_16x16x32_bf16 v[48:51], a[216:219], v[108:111], v[48:51]\n\tv_mfma_f32_16x16x32_bf16 v[52:55], a[220:223], v[108:111], v[52:55]\n\t" \
          "s_waitcnt vmcnt(18) lgkmcnt(0)\n\ts_barrier\n\tv_mfma_f32_16x16x32_bf16 v[48:51], a[224:227], v[112:115], v[48:51]\n\tds_read_b128 v[64:67], %[ra0] offset:32768\n\tv_mfma_f32_16x16x32_bf16 v[52:55], a[228:231], v[112:115], v[52:55]\n\tds_read_b128 v[68:71], %[ra1] offset:32768\n\t" \
          "v_mfma_f32_16x16x32_bf16 v[48:51], a[232:235], v[116:119], v[48:51]\n\tds_read_b128 v[72:75], %[ra2] offset:32768\n\tv_mfma_f32_16x16x32_bf16 v[52:55], a[236:239], v[116:119], v[52:55]\n\tds_read_b128 v[76:79], %[ra3] offset:32768\n\tv_mfma_f32_16x16x32_bf16 v[48:51], a[240:243], v[120:123], v[48:51]\n\tds_read_b128 v[80:83], %[ra0] offset:33024\n\t" \
          "v_mfma_f32_16x16x32_bf16 v[52:55], a[244:247], v[120:123], v[52:55]\n\tds_read_b128 v[84:87], %[ra1] offset:33024\n\tv_mfma_f32_16x16x32_bf16 v[48:51], a[248:251], v[124:127], v[48:51]\n\tds_read_b128 v[88:91], %[ra2] offset:33024\n\tv_mfma_f32_16x16x32_bf16 v[52:55], a[252:255], v[124:127], v[52:55]\n\tds_read_b128 v[92:95], %[ra3] offset:33024\n\t" \
          "s_waitcnt lgkmcnt(6)\n\tv_mfma_f32_16x16x32_bf16 v[56:59], a[0:3], v[64:67], 0\n\tds_read_b128 v[96:99], %[ra0] offset:33280\n\tv_mfma_f32_16x16x32_bf16 v[60:63], a[4:7], v[64:67], 0\n\ts_add_u32 %[t], %[j], 4\n\ts_cmp_lt_u32 %[t], %[je]\n\t" \
          "s_cselect_b32 s42, s42, 0\n\tv_mfma_f32_16x16x32_bf16 v[56:59], a[8:11], v[68:71], v[56:59]\n\tds_read_b128 v[100:103], %[ra1] offset:33280\n\tv_mfma_f32_16x16x32_bf16 v[60:63], a[12:15], v[68:71], v[60:63]\n\tv_cvt_pk_bf16_f32 v44, v48, v49\n\tv_cvt_pk_bf16_f32 v45, v50, v51\n\t" \
          "s_waitcnt lgkmcnt(6)\n\tv_mfma_f32_16x16x32_bf16 v[56:59], a[16:19], v[72:75], v[56:59]\n\tds_read_b128 v[104:107], %[ra2] offset:33280\n\tv_mfma_f32_16x16x32_bf16 v[60:63], a[20:23], v[72:75], v[60:63]\n\tv_cvt_pk_bf16_f32 v46, v52, v53\n\tv_cvt_pk_bf16_f32 v47, v54, v55\n\t" \
          "v_mfma_f32_16x16x32_bf16 v[56:59], a[24:27], v[76:79], v[56:59]\n\tds_read_b128 v[108:111], %[ra3] offset:33280\n\tv_mfma_f32_16x16x32_bf16 v[60:63], a[28:31], v[76:79], v[60:63]\n\tbuffer_store_dwordx4 v[44:47], %[so], %[rc], %[co] offen sc1\n\ts_waitcnt lgkmcnt(6)\n\tv_mfma_f32_16x16x32_bf16 v[56:59], a[32:35], v[80:83], v[56:59]\n\t" \
          "ds_read_b128 v[112:115], %[ra0] offset:33536\n\tv_mfma_f32_16x16x32_bf16 v[60:63], a[36:39], v[80:83], v[60:63]\n\tbuffer_store_dwordx4 v[36:39], %[sk], %[rk], %[ko] offen sc1\n\tv_mov_b32 %[fv], %[j]\n\tv_mfma_f32_16x16x32_bf16 v[56:59], a[40:43], v[84:87], v[56:59]\n\tds_read_b128 v[116:119], %[ra1] offset:33536\n\t" \
          "v_mfma_f32_16x16x32_bf16 v[60:63], a[44:47], v[84:87], v[60:63]\n\ts_add_u32 %[co], %[co], 0x4000\n\ts_add_u32 %[ko], %[ko], 0x8000\n\ts_waitcnt lgkmcnt(6)\n\tv_mfma_f32_16x16x32_bf16 v[56:59], a[48:51], v[88:91], v[56:59]\n\tds_read_b128 v[120:123], %[ra2] offset:33536\n\t" \
          "s_add_u32 m0, %[sl], 0\n\tv_mfma_f32_16x16x32_bf16 v[60:63], a[52:55], v[88:91], v[60:63]\n\tbuffer_load_dwordx4 %[vd0], s[40:43], %[xo] offen lds\n\tv_mfma_f32_16x16x32_bf16 v[56:59], a[56:59], v[92:95], v[56:59]\n\tds_read_b128 v[124:127], %[ra3] offset:33536\n\tv_mfma_f32_16x16x32_bf16 v[60:63], a[60:63], v[92:95], v[60:63]\n\t" \
          "s_waitcnt lgkmcnt(6)\n\tv_mfma_f32_16x16x32_bf16 v[56:59], a[64:67], v[96:99], v[56:59]\n\tds_read_b128 v[64:67], %[ra0] offset:33792\n\tv_mfma_f32_16x16x32_bf16 v[60:63], a[68:71], v[96:99], v[60:63]\n\tbuffer_load_dwordx4 %[vd0], s[40:43], %[xo] offen offset:1024 lds\n\tv_mfma_f32_16x16x32_bf16 v[56:59], a[72:75], v[100:103], v[56:59]\n\t" \
          "ds_read_b128 v[68:71], %[ra1] offset:33792\n\tv_mfma_f32_16x16x32_bf16 v[60:63], a[76:79], v[100:103], v[60:63]\n\ts_waitcnt lgkmcnt(6)\n\tv_mfma_f32_16x16x32_bf16 v[56:59], a[80:83], v[104:107], v[56:59]\n\tds_read_b128 v[72:75], %[ra2] offset:33792\n\tv_mfma_f32_16x16x32_bf16 v[60:63], a[84:87], v[104:107], v[60:63]\n\t" \
          "buffer_load_dwordx4 %[vd1], s[40:43], %[xo] offen offset:2048 lds\n\tv_mfma_f32_16x16x32_bf16 v[56:59], a[88:91], v[108:111], v[56:59]\n\tds_read_b128 v[76:79], %[ra3] offset:33792\n\tv_mfma_f32_16x16x32_bf16 v[60:63], a[92:95], v[108:111], v[60:63]\n\ts_waitcnt lgkmcnt(6)\n\tv_mfma_f32_16x16x32_bf16 v[56:59], a[96:99], v[112:115], v[56:59]\n\t" \
          "ds_read_b128 v[80:83], %[ra0] offset:34048\n\tv_mfma_f32_16x16x32_bf16 v[60:63], a[100:103], v[112:115], v[60:63]\n\tbuffer_load_dwordx4 %[vd1], s[40:43], %[xo] offen offset:3072 lds\n\tv_mfma_f32_16x16x32_bf16 v[56:59], a[104:107], v[116:119], v[56:59]\n\tds_read_b128 v[84:87], %[ra1] offset:34048\n\tv_mfma_f32_16x16x32_bf16 v[60:63], a[108:111], v[116:119], v[60:63]\n\t" \
          "s_waitcnt lgkmcnt(6)\n\tv_mfma_f32_16x16x32_bf16 v[56:59], a[112:115], v[120:123], v[56:59]\n\tds_read_b128 v[88:91], %[ra2] offset:34048\n\ts_add_u32 m0, %[sl], 4096\n\tv_mfma_f32_16x16x32_bf16 v[60:63], a[116:119], v[120:123], v[60:63]\n\tbuffer_load_dwordx4 %[vd2], s[40:43], %[xo] offen lds\n\t" \
          "v_mfma_f32_16x16x32_bf16 v[56:59], a[120:123], v[124:127], v[56:59]\n\tds_read_b128 v[92:95], %[ra3] offset:34048\n\tv_mfma_f32_16x16x32_bf16 v[60:63], a[124:127], v[124:127], v[60:63]\n\ts_waitcnt lgkmcnt(6)\n\tv_mfma_f32_16x16x32_bf16 v[56:59], a[128:131], v[64:67], v[56:59]\n\tds_read_b128 v[96:99], %[ra0] offset:34304\n\t" \
          "v_mfma_f32_16x16x32_bf16 v[60:63], a[132:135], v[64:67], v[60:63]\n\tbuffer_load_dwordx4 %[vd2], s[40:43], %[xo] offen offset:1024 lds\n\tv_mfma_f32_16x16x32_bf16 v[56:59], a[136:139], v[68:71], v[56:59]\n\tds_read_b128 v[100:103], %[ra1] offset:34304\n\tv_mfma_f32_16x16x32_bf16 v[60:63], a[140:143], v[68:71], v[60:63]\n\ts_waitcnt lgkmcnt(6)\n\t" \
          "v_mfma_f32_16x16x32_bf16 v[56:59], a[144:147], v[72:75], v[56:59]\n\tds_read_b128 v[104:107], %[ra2] offset:34304\n\tv_mfma_f32_16x16x32_bf16 v[60:63], a[148:151], v[72:75], v[60:63]\n\tbuffer_load_dwordx4 %[vd3], s[40:43], %[xo] offen offset:2048 lds\n\tv_mfma_f32_16x16x32_bf16 v[56:59], a[152:155], v[76:79], v[56:59]\n\tds_read_b128 v[108:111], %[ra3] offset:34304\n\t" \
          "v_mfma_f32_16x16x32_bf16 v[60:63], a[156:159], v[76:79], v[60:63]\n\ts_waitcnt lgkmcnt(6)\n\tv_mfma_f32_16x16x32_bf16 v[56:59], a[160:163], v[80:83], v[56:59]\n\tds_read_b128 v[112:115], %[ra0] offset:34560\n\tv_mfma_f32_16x16x32_bf16 v[60:63], a[164:167], v[80:83], v[60:63]\n\tbuffer_load_dwordx4 %[vd3], s[40:43], %[xo] offen offset:3072 lds\n\t" \
          "v_mfma_f32_16x16x32_bf16 v[56:59], a[168:171], v[84:87], v[56:59]\n\tds_read_b128 v[116:119], %[ra1] offset:34560\n\tv_mfma_f32_16x16x32_bf16 v[60:63], a[172:175], v[84:87], v[60:63]\n\ts_waitcnt lgkmcnt(6)\n\tv_mfma_f32_16x16x32_bf16 v[56:59], a[176:179], v[88:91], v[56:59]\n\tds_read_b128 v[120:123], %[ra2] offset:34560\n\t" \
          "v_mfma_f32_16x16x32_bf16 v[60:63], a[180:183], v[88:91], v[60:63]\n\tv_mfma_f32_16x16x32_bf16 v[56:59], a[184:187], v[92:95], v[56:59]\n\tds_read_b128 v[124:127], %[ra3] offset:34560\n\tv_mfma_f32_16x16x32_bf16 v[60:63], a[188:191], v[92:95], v[60:63]\n\ts_add_u32 %[xo], %[xo], 0x8000\n\ts_add_u32 %[sl], %[sl], 0x8000\n\t" \
          "s_bitset0_b32 %[sl], 17\n\ts_waitcnt lgkmcnt(6)\n\tv_mfma_f32_16x16x32_bf16 v[56:59], a[192:195], v[96:99], v[56:59]\n\tv_mfma_f32_16x16x32_bf16 v[60:63], a[196:199], v[96:99], v[60:63]\n\tv_xor_b32 %[ra0], 0x10000, %[ra0]\n\tv_mfma_f32_16x16x32_bf16 v[56:59], a[200:203], v[100:103], v[56:59]\n\t" \
          "v_mfma_f32_16x16x32_bf16 v[60:63], a[204:207], v[100:103], v[60:63]\n\tv_xor_b32 %[ra1], 0x10000, %[ra1]\n\ts_waitcnt lgkmcnt(4)\n\tv_mfma_f32_16x16x32_bf16 v[56:59], a[208:211], v[104:107], v[56:59]\n\tv_mfma_f32_16x16x32_bf16 v[60:63], a[212:215], v[104:107], v[60:63]\n\tv_xor_b32 %[ra2], 0x10000, %[ra2]\n\t" \
          "v_mfma_f32_16x16x32_bf16 v[56:59], a[216:219], v[108:111], v[56:59]\n\ts_add_u32 %[j], %[j], 2\n\ts_cmp_lt_u32 %[j], %[je]\n\tv_mfma_f32_16x16x32_bf16 v[60:63], a[220:223], v[108:111], v[60:63]\n\tv_xor_b32 %[ra3], 0x10000, %[ra3]\n\ts_waitcnt vmcnt(18) lgkmcnt(0)\n\t" \
          "s_barrier\n\tv_mfma_f32_16x16x32_bf16 v[56:59], a[224:227], v[112:115], v[56:59]\n\tds_read_b128 v[64:67], %[ra0] offset:0\n\tv_mfma_f32_16x16x32_bf16 v[60:63], a[228:231], v[112:115], v[60:63]\n\tds_read_b128 v[68:71], %[ra1] offset:0\n\tv_mfma_f32_16x16x32_bf16 v[56:59], a[232:235], v[116:119], v[56:59]\n\t" \
          "ds_read_b128 v[72:75], %[ra2] offset:0\n\tv_mfma_f32_16x16x32_bf16 v[60:63], a[236:239], v[116:119], v[60:63]\n\tds_read_b128 v[76:79], %[ra3] offset:0\n\tv_mfma_f32_16x16x32_bf16 v[56:59], a[240:243], v[120:123], v[56:59]\n\tds_read_b128 v[80:83], %[ra0] offset:256\n\tv_mfma_f32_16x16x32_bf16 v[60:63], a[244:247], v[120:123], v[60:63]\n\t" \
          "ds_read_b128 v[84:87], %[ra1] offset:256\n\tv_mfma_f32_16x16x32_bf16 v[56:59], a[248:251], v[124:127], v[56:59]\n\tds_read_b128 v[88:91], %[ra2] offset:256\n\tv_mfma_f32_16x16x32_bf16 v[60:63], a[252:255], v[124:127], v[60:63]\n\tds_read_b128 v[92:95], %[ra3] offset:256\n\ts_cbranch_scc1 Lpn_loop%=\n\t" \
          "s_nop 7\n\tv_mov_b32 %[fv], %[je]\n\tv_cvt_pk_bf16_f32 v44, v56, v57\n\tv_cvt_pk_bf16_f32 v45, v58, v59\n\tv_cvt_pk_bf16_f32 v46, v60, v61\n\tv_cvt_pk_bf16_f32 v47, v62, v63\n\t" \
          "buffer_store_dwordx4 v[44:47], %[so], %[rc], %[co] offen sc1\n\ts_waitcnt vmcnt(0) lgkmcnt(0)\n\ts_barrier\n\ts_mov_b64 exec, %[fx]\n\tbuffer_store_dword %[fv], off, %[rf], %[fo] sc1\n\ts_mov_b64 exec, -1\n\t"
#define PN_CLOB "v36", "v37", "v38", "v39", "v44", "v45", "v46", "v47", "v48", "v49", "v50", "v51", "v52", "v53", "v54", "v55", "v56", "v57", "v58", "v59", "v60", "v61", "v62", "v63", "v64", "v65", "v66", "v67", "v68", "v69", "v70", "v71", "v72", "v73", "v74", "v75", "v76", "v77", "v78", "v79", "v80", "v81", "v82", "v83", "v84", "v85", "v86", "v87", "v88", "v89", "v90", "v91", "v92", "v93", "v94", "v95", "v96", "v97", "v98", "v99", "v100", "v101", "v102", "v103", "v104", "v105", "v106", "v107", "v108", "v109", "v110", "v111", "v112", "v113", "v114", "v115", "v116", "v117", "v118", "v119", "v120", "v121", "v122", "v123", "v124", "v125", "v126", "v127", "a0", "a1", "a2", "a3", "a4", "a5", "a6", "a7", "a8", "a9", "a10", "a11", "a12", "a13", "a14", "a15", "a16", "a17", "a18", "a19", "a20", "a21", "a22", "a23", "a24", "a25", "a26", "a27", "a28", "a29", "a30", "a31", "a32", "a33", "a34", "a35", "a36", "a37", "a38", "a39", "a40", "a41", "a42", "a43", "a44", "a45", "a46", "a47", "a48", "a49", "a50", "a51", "a52", "a53", "a54", "a55", "a56", "a57", "a58", "a59", "a60", "a61", "a62", "a63", "a64", "a65", "a66", "a67", "a68", "a69", "a70", "a71", "a72", "a73", "a74", "a75", "a76", "a77", "a78", "a79", "a80", "a81", "a82", "a83", "a84", "a85", "a86", "a87", "a88", "a89", "a90", "a91", "a92", "a93", "a94", "a95", "a96", "a97", "a98", "a99", "a100", "a101", "a102", "a103", "a104", "a105", "a106", "a107", "a108", "a109", "a110", "a111", "a112", "a113", "a114", "a115", "a116", "a117", "a118", "a119", "a120", "a121", "a122", "a123", "a124", "a125", "a126", "a127", "a128", "a129", "a130", "a131", "a132", "a133", "a134", "a135", "a136", "a137", "a138", "a139", "a140", "a141", "a142", "a143", "a144", "a145", "a146", "a147", "a148", "a149", "a150", "a151", "a152", "a153", "a154", "a155", "a156", "a157", "a158", "a159", "a160", "a161", "a162", "a163", "a164", "a165", "a166", "a167", "a168", "a169", "a170", "a171", "a172", "a173", "a174", "a175", "a176", "a177", "a178", "a179", "a180", "a181", "a182", "a183", "a184", "a185", "a186", "a187", "a188", "a189", "a190", "a191", "a192", "a193", "a194", "a195", "a196", "a197", "a198", "a199", "a200", "a201", "a202", "a203", "a204", "a205", "a206", "a207", "a208", "a209", "a210", "a211", "a212", "a213", "a214", "a215", "a216", "a217", "a218", "a219", "a220", "a221", "a222", "a223", "a224", "a225", "a226", "a227", "a228", "a229", "a230", "a231", "a232", "a233", "a234", "a235", "a236", "a237", "a238", "a239", "a240", "a241", "a242", "a243", "a244", "a245", "a246", "a247", "a248", "a249", "a250", "a251", "a252", "a253", "a254", "a255", "s40", "s41", "s42", "s43", "s44", "s45"
#define NTP 0
template <bool NT>
__device__ __forceinline__ v4u panel_task(const FParams& F, int p, int fi, int j, int je, int w) {
  const int lane = opaque_lane(), l15 = lane & 15, lq = lane >> 4, m = p >> 5, ldb = __builtin_amdgcn_readfirstlane(m < 3 ? HDA * 2 : NQKVB * 2);
  const __amdgpu_buffer_rsrc_t rw = __builtin_amdgcn_make_buffer_rsrc(wg::uni((m == 0 ? F.Wq : (m == 1 ? F.Wk : (m == 2 ? F.Wv : F.wqkvB))) + (p & 31) * 128), 0, DM * ldb - (p & 31) * 256, 0x00020000);
  const __amdgpu_buffer_rsrc_t rf = __builtin_amdgcn_make_buffer_rsrc(F.ctl, 0, sizeof(Ctl), 0x00020000), rr = __builtin_amdgcn_make_buffer_rsrc(F.rdy, 0, RDY_SIZE, 0x00020000);
  void* const xa = wg::uni(m < 3 ? F.Ax : F.Bx);
  int ra[4], vd[4], rt[4], vw[2], fv = 0, xo = w * 8192 + j * 32768, sl = w * 8192, co = j * QBLK, cs = 0, t; unsigned long long sx;
  v4u rec;
#pragma unroll
  for (int i = 0; i < 4; ++i) {
    ra[i] = l15 * 2048 + (((4 * i + lq) ^ l15) << 4); vd[i] = ((lane ^ (4 * w + i)) << 4) + (i >> 1) * 4096;
    rt[i] = 65536 + (i >> 1) * 49152 + (8 * lq + (l15 >> 2)) * 256 + (((2 * w + (i & 1)) ^ ((l15 >> 2) | ((lq & 1) << 2))) << 5) + (l15 & 3) * 8;
  }
  vw[0] = (16 * w + lq) * ldb + ((l15 ^ (lq << 1)) << 4); vw[1] = vw[0] ^ 128;
  const __amdgpu_buffer_rsrc_t rc = __builtin_amdgcn_make_buffer_rsrc((char*)F.qkvA + __builtin_amdgcn_readfirstlane((p >> 1) * QPAGE + (p & 1) * 4096 + w * 1024), 0, QPAGE - 8192 + 1024, 0x00020000);
  const bool kv = m == 1 || m == 2;
  const __amdgpu_buffer_rsrc_t rk = __builtin_amdgcn_make_buffer_rsrc((char*)F.qkvA + __builtin_amdgcn_readfirstlane(kvoff(0, p & 31) + (m & 2) * 1024 + w * 512), 0, QPAGE - 2 * QBLK + 8192 + 512, 0x00020000);
  int ko;
#define PN_OPS : "=&{v[40:43]}"(rec), [ra0] "+v"(ra[0]), [ra1] "+v"(ra[1]), [ra2] "+v"(ra[2]), [ra3] "+v"(ra[3]), [fv] "+v"(fv), [xo] "+s"(xo), [sl] "+s"(sl), [co] "+s"(co), [cs] "+s"(cs), [t] "=&s"(t), [j] "+s"(j), [sx] "=&s"(sx), [ko] "=&s"(ko) \
    : [vd0] "v"(vd[0]), [vd1] "v"(vd[1]), [vd2] "v"(vd[2]), [vd3] "v"(vd[3]), [rt0] "v"(rt[0]), [rt1] "v"(rt[1]), [rt2] "v"(rt[2]), [rt3] "v"(rt[3]), [vw0] "v"(vw[0]), [vw1] "v"(vw[1]), [ro] "v"(lane * 128), [so] "v"(lane * 16), [sk] "v"(lane < 32 && kv ? lane * 16 : 0x7ffffff0), \
      [xa] "s"(xa), [rw] "s"(rw), [rr] "s"(rr), [rc] "s"(rc), [rf] "s"(rf), [wl] "s"(w * 4096), [ldb] "s"(ldb), [fo] "s"((int)offsetof(Ctl, prg) + fi * 128), [je] "s"(je), \
      [fx] "s"((unsigned long long)(unsigned)__builtin_amdgcn_readfirstlane(w == 0)), [pl] "s"(p >> 1), [rk] "s"(rk), [kl] "s"((p & 31) >> 2) : PN_CLOB, "vcc", "scc", "memory"
  if constexpr (NT) asm volatile(PN_BODY(" nt", PN_LIM) PN_OPS); else asm volatile(PN_BODY("", "") PN_OPS);
  return rec;
}

struct AttnX { v4u q[2][4], kx[2][4], vr[8]; };
template <int AUX = 0>
__device__ __forceinline__ void attnA_load_x(const FParams& F, unsigned long long parq, int seg, int h, int lane, AttnX& X) {
  const __amdgpu_buffer_rsrc_t rs = __builtin_amdgcn_make_buffer_rsrc(F.qkvA, 0, QSIZE, 0x00020000);
  const int so = seg * 2 * QBLK + (h & 1) * 4096, oq = so + qoff(parq, h >> 1), ok = so + qoff(parq, 16 + (h >> 1)), ov = so + qoff(parq, 32 + (h >> 1));
#pragma unroll
  for (int i = 0; i < 8; ++i) X.vr[i] = __builtin_amdgcn_raw_buffer_load_b128(rs, lane * 16 + (i & 3) * 1024, ov + (i >> 2) * QBLK, AUX);
#pragma unroll
  for (int t = 0; t < 2; ++t)
#pragma unroll
    for (int j = 0; j < 4; ++j) {
      X.q[t][j] = __builtin_amdgcn_raw_buffer_load_b128(rs, lane * 16 + j * 1024, oq + t * QBLK, AUX);
      X.kx[t][j] = __builtin_amdgcn_raw_buffer_load_b128(rs, lane * 16 + j * 1024, ok + t * QBLK, AUX);
    }
}
__device__ __forceinline__ void attnA_stage_v(const AttnX& X, u16* vs  , int lane) {
#pragma unroll
  for (int i = 0; i < 8; ++i) *(v4u*)&vs[(16 * (i >> 2) + (lane & 15)) * VXLD + 32 * (i & 3) + 8 * (lane >> 4)] = X.vr[i];
}
template <int WHAT = 3>
__device__ __forceinline__ unsigned attnA_load_m(const FParams& F, int seg, int ko, int lane, v4u (&km)[4], unsigned (&vm)[8]) {
  const int l15 = lane & 15, lq = lane >> 4;
  const __amdgpu_buffer_rsrc_t rk = __builtin_amdgcn_make_buffer_rsrc((char*)F.qkvA + ko + seg * 2 * QBLK, 0, 4096, 0x00020000);
#pragma unroll
  for (int j = 0; j < 4; ++j)
    if (WHAT & 1) km[j] = __builtin_amdgcn_raw_buffer_load_b128(rk, (2 * (lane >> 2) + (lane & 1)) * 16 + 512 * j, 0, 16);
#pragma unroll
  for (int t = 0; t < 8; ++t)
    if (WHAT & 2) vm[t] = __builtin_amdgcn_raw_buffer_load_b32(rk, 2048 + (((l15 >> 3) * 4 + lq) * 16 + 8 * ((l15 >> 2) & 1) + (l15 & 3)) * 4 + 512 * (t >> 1) + 16 * (t & 1), 0, 16);
  unsigned m = 0;
#pragma unroll
  for (int j = 0; j < 4; ++j) if (WHAT & 1) m = chk4(m, km[j]);
#pragma unroll
  for (int t = 0; t < 8; t += 2) if (WHAT & 2) m = umax3(m, vm[t], vm[t + 1]);
  return m;
}
__device__ __forceinline__ void attnA_compute(const AttnX& X, const v4u (&km)[4], const unsigned (&vm)[8], const u16* vs, int lane, v4u (&ov)[2][4]) {
  const int l15 = lane & 15, lq = lane >> 4;
  const float scale = 0.08838834764831845f, L2E = 1.4426950408889634f;
  v4u px[2], pm[2];
#pragma unroll
  for (int t = 0; t < 2; ++t) {
    f32x4 s0 = {0.f,0.f,0.f,0.f}, s1 = s0, s2 = s0;
#pragma unroll
    for (int j = 0; j < 4; ++j) { s0 = mf(km[j], X.q[t][j], s0); s1 = mf(X.kx[0][j], X.q[t][j], s1); if (t == 1) s2 = mf(X.kx[1][j], X.q[t][j], s2); }
    float sv[12];
#pragma unroll
    for (int e = 0; e < 4; ++e) { sv[e] = s0[e]; sv[4 + e] = s1[e]; sv[8 + e] = s2[e]; }
#pragma unroll
    for (int e = 0; e < 12; e += 2) {
      unsigned p = pk2(sv[e], sv[e + 1]);
      p = pk2(__uint_as_float(p << 16) * scale, __uint_as_float(p & 0xffff0000u) * scale);
      sv[e] = __uint_as_float(p << 16); sv[e + 1] = __uint_as_float(p & 0xffff0000u);
    }
    bool ok[12];
#pragma unroll
    for (int e = 0; e < 4; ++e) { ok[e] = e < 2; ok[4 + e] = (t == 1) || (4 * lq + e <= l15); ok[8 + e] = (t == 1) && (4 * lq + e <= l15); }
    float mx = -INFINITY;
#pragma unroll
    for (int e = 0; e < 12; ++e) mx = ok[e] ? fmaxf(mx, sv[e]) : mx;
    mx = xmax32(xmax16(mx));
    float sum = 0.f;
#pragma unroll
    for (int e = 0; e < 12; ++e) { sv[e] = ok[e] ? __builtin_amdgcn_exp2f((sv[e] - mx) * L2E) : 0.f; sum += sv[e]; }
    sum = xsum32(xsum16(sum));
    const float inv = __builtin_amdgcn_rcpf(sum);
#pragma unroll
    for (int e = 0; e < 12; ++e) sv[e] = ok[e] ? sv[e] * inv : 0.f;
    px[t] = (v4u){pk2(sv[4], sv[5]), pk2(sv[6], sv[7]), pk2(sv[8], sv[9]), pk2(sv[10], sv[11])};
    pm[t] = (v4u){pk2(sv[0], sv[1]), 0u, 0u, 0u};
  }
  const u16* vp = &vs[(4 * lq + (l15 >> 2)) * VXLD + 16 * (l15 & 1) + 2 * (l15 & 2)];
#pragma unroll
  for (int u = 0; u < 4; ++u) {
    f32x4 o[2][2];
#pragma unroll
    for (int ab = 0; ab < 2; ++ab) {
      v2u lo = trrd(vp + 32 * u + 8 * ab), hi = trrd(vp + 16 * VXLD + 32 * u + 8 * ab);
      const v4u va = {lo.x, lo.y, hi.x, hi.y}, vb = {vm[2 * u + ab], 0u, 0u, 0u};
#pragma unroll
      for (int t = 0; t < 2; ++t) { o[t][ab] = mf(va, px[t], (f32x4){0.f,0.f,0.f,0.f}); o[t][ab] = mf(vb, pm[t], o[t][ab]); }
    }
#pragma unroll
    for (int t = 0; t < 2; ++t)
      ov[t][u] = (v4u){pk2(o[t][0][0], o[t][0][1]), pk2(o[t][0][2], o[t][0][3]), pk2(o[t][1][0], o[t][1][1]), pk2(o[t][1][2], o[t][1][3])};
  }
}
__device__ __forceinline__ void attnA_block(const FParams& F, unsigned long long parq, unsigned pk, int r, int h, u16* vs, int lane, int w, unsigned* shw) {
  AttnX X[2]; v4u km[2][4]; unsigned vm[2][8];
  unsigned seen = 0;
  attnA_load_x(F, parq, 4 * r, h, lane, X[0]);
  bool have = false;
#pragma unroll
  for (int i = 0; i < 4; ++i) {
    const int seg = 4 * r + i, c = i & 1;
    v4u ov[2][4];
    if (!have) {
      if (seen < (unsigned)seg + 1) {
        if (w == 0) {
          for (unsigned spins = 0;; ++spins) {
            seen = __builtin_amdgcn_readfirstlane(ld_agent(&F.ctl->prog[0]));
            if (seen >= (unsigned)seg + 1 || spin_fail(spins, &F.ctl->tmo[0], 6u)) break;
            for (unsigned z = seen; z < (unsigned)seg + 1; ++z) __builtin_amdgcn_s_sleep(PSL);
          }
          *shw = seen;
        }
        __syncthreads();
        seen = __builtin_amdgcn_readfirstlane(*shw);
        __syncthreads();
      }
      asm volatile("" ::: "memory");
      (void)attnA_load_m(F, seg, kvoff(pk, h), lane, km[c], vm[c]);
    }
    if (i < 3) {
      attnA_load_x(F, parq, seg + 1, h, lane, X[c ^ 1]);
      have = seen >= (unsigned)seg + 2;
      (void)attnA_load_m(F, have ? seg + 1 : seg, kvoff(pk, h), lane, km[c ^ 1], vm[c ^ 1]);
    }
    SB0;
    attnA_stage_v(X[c], vs, lane);
    attnA_compute(X[c], km[c], vm[c], vs, lane, ov);
    const __amdgpu_buffer_rsrc_t rs = __builtin_amdgcn_make_buffer_rsrc(F.qkvA, 0, QSIZE, 0x00020000);
#pragma unroll
    for (int u = 0; u < 4; ++u)
#pragma unroll
      for (int t = 0; t < 2; ++t)
        __builtin_amdgcn_raw_buffer_store_b128(ov[t][u], rs, lane * 16 + u * 1024, qoff(parq, h >> 1) + (seg * 2 + t) * QBLK + (h & 1) * 4096, 16);
    SB0;
  }
}

constexpr int TSEG0 = 32, OSEG0 = 0, NSET = 4;
__device__ __forceinline__ void attn_stream(const FParams& F, unsigned long long parq, unsigned pk, int set, int hg, char* lds, int w) {
  const int lane = opaque_lane(), l15 = lane & 15, lq = lane >> 4, h = 4 * hg + w;
  const float L2E = 1.4426950408889634f;
  auto rnd2 = [](float& a, float& b) { const float scale = 0.08838834764831845f; unsigned p = pk2(a, b); p = pk2(__uint_as_float(p << 16) * scale, __uint_as_float(p & 0xffff0000u) * scale); a = __uint_as_float(p << 16); b = __uint_as_float(p & 0xffff0000u); };
  Ctl* ctl = F.ctl;
  unsigned have = 0;
  auto need = [&](int sg) {
    for (unsigned spins = 0; !__all(have >= 2u * sg + 2u); ++spins) {
      if (spins) __builtin_amdgcn_s_sleep(16);
      have = ld_agent(&ctl->prg[(lane < 2 ? lane : 3) * 32 + h][0]);
      if (spin_fail(spins, &ctl->tmo[0], 2u)) break;
    }
    asm volatile("" ::: "memory");
  };
  static_assert(TSEG0 >= 32, "upper q panel");
  need(TSEG0 + set);
  u16* vs = (u16*)(lds + w * 16384);
  AttnX X; int seen = 0; unsigned dur = 170;
  attnA_load_x<16>(F, parq, TSEG0 + set, h, lane, X);
#pragma unroll 1
  for (int seg = TSEG0 + set; seg < NSEG; seg += NSET) {
    v4u km[4]; unsigned vm[8]; f32x4 o[2][8]; float mx[2], ls[2];
    attnA_stage_v(X, vs, lane);
    { v4u px[2];
#pragma unroll
      for (int t = 0; t < 2; ++t) {
        f32x4 s1 = {0.f, 0.f, 0.f, 0.f}, s2 = s1;
#pragma unroll
        for (int j = 0; j < 4; ++j) { s1 = mf(X.kx[0][j], X.q[t][j], s1); if (t == 1) s2 = mf(X.kx[1][j], X.q[t][j], s2); }
        float sv[8]; bool ok[8];
#pragma unroll
        for (int e = 0; e < 4; ++e) { sv[e] = s1[e]; sv[4 + e] = s2[e]; ok[e] = (t == 1) || (4 * lq + e <= l15); ok[4 + e] = (t == 1) && (4 * lq + e <= l15); }
#pragma unroll
        for (int e = 0; e < 8; e += 2) rnd2(sv[e], sv[e + 1]);
        float m = -INFINITY, sum = 0.f;
#pragma unroll
        for (int e = 0; e < 8; ++e) m = ok[e] ? fmaxf(m, sv[e]) : m;
        m = xmax32(xmax16(m));
#pragma unroll
        for (int e = 0; e < 8; ++e) { sv[e] = ok[e] ? __builtin_amdgcn_exp2f((sv[e] - m) * L2E) : 0.f; sum += sv[e]; }
        mx[t] = m; ls[t] = sum;
        px[t] = (v4u){pk2(sv[0], sv[1]), pk2(sv[2], sv[3]), pk2(sv[4], sv[5]), pk2(sv[6], sv[7])};
      }
      const u16* vp = &vs[(4 * lq + (l15 >> 2)) * VXLD + 16 * (l15 & 1) + 2 * (l15 & 2)];
#pragma unroll
      for (int i = 0; i < 8; ++i) {
        const v2u lo = trrd(vp + 32 * (i >> 1) + 8 * (i & 1)), hi = trrd(vp + 16 * VXLD + 32 * (i >> 1) + 8 * (i & 1));
#pragma unroll
        for (int t = 0; t < 2; ++t) { o[t][i] = mf((v4u){lo.x, lo.y, hi.x, hi.y}, px[t], (f32x4){0.f, 0.f, 0.f, 0.f}); asm volatile("" : "+v"(o[t][i])); }
      } }
    const __amdgpu_buffer_rsrc_t rs = __builtin_amdgcn_make_buffer_rsrc(F.qkvA, 0, QSIZE, 0x00020000);
    const int oo = qoff(parq, h >> 1) + seg * 2 * QBLK + (h & 1) * 4096;
    if (seg >= NSEG - 2) {
#pragma unroll
      for (int u = 0; u < 8; ++u) __builtin_amdgcn_raw_buffer_store_b128((v4u){~0u, ~0u, ~0u, ~0u}, rs, lane * 16 + (u >> 1) * 1024, oo + (u & 1) * QBLK, 16);
    }
    if (seg + NSET < NSEG) need(seg + NSET);
    unsigned due = 0;
    for (unsigned spins = 0; seen <= seg - 3; ++spins) {
      const unsigned long long pv = __hip_atomic_load((unsigned long long*)&ctl->prog[0], RLX_AGENT);
      seen = __builtin_amdgcn_readfirstlane((unsigned)pv); due = __builtin_amdgcn_readfirstlane((unsigned)(pv >> 32)) + TP(F.x1, XA1);
      if (seen > seg - 3 || spin_fail(spins, &ctl->tmo[0], 6u)) break;
      __builtin_amdgcn_s_sleep(24);
    }
    if (seen != seg - 2 || (unsigned)(due - (unsigned)__builtin_amdgcn_s_memrealtime()) > 1000u) due = 0;
    if (seg >= NSEG - 2) {
      asm volatile("s_waitcnt vmcnt(0)\n\ts_barrier" ::: "memory");
      if (w == 0 && lane == 0 && due) __hip_atomic_store(&ctl->flagO[seg][hg][1], (due + dur) | 1u, RLX_AGENT);
    }
    if (due) while ((int)(due - (unsigned)__builtin_amdgcn_s_memrealtime()) > 0) __builtin_amdgcn_s_sleep(1);
    asm volatile("" ::: "memory");
    unsigned vc;
    for (unsigned spins = 0;; ++spins) {
      const unsigned kc = attnA_load_m<1>(F, seg, kvoff(pk, h), lane, km, vm);
      vc = attnA_load_m<2>(F, seg, kvoff(pk, h), lane, km, vm);
      if (__all(kc != 0xffffffffu) || spin_fail(spins, &ctl->tmo[0], 4u)) break;
      __builtin_amdgcn_s_sleep(TSL);
    }
#define ELOG(i)
    v4u pm[2];
#pragma unroll
    for (int t = 0; t < 2; ++t) {
      f32x4 s0 = {0.f, 0.f, 0.f, 0.f};
#pragma unroll
      for (int j = 0; j < 4; ++j) s0 = mf(km[j], X.q[t][j], s0);
      float a = s0[0], c = s0[1];
      rnd2(a, c);
      const float m = fmaxf(mx[t], xmax32(xmax16(fmaxf(a, c)))), al = __builtin_amdgcn_exp2f((mx[t] - m) * L2E);
      a = __builtin_amdgcn_exp2f((a - m) * L2E); c = __builtin_amdgcn_exp2f((c - m) * L2E);
      const float inv = __builtin_amdgcn_rcpf(xsum32(xsum16(ls[t] * al + a + c))), sc = al * inv;
      pm[t] = (v4u){pk2(a * inv, c * inv), 0u, 0u, 0u};
#pragma unroll
      for (int i = 0; i < 8; ++i) { o[t][i] *= sc; asm volatile("" : "+v"(o[t][i])); }
    }
    SB0;
    for (unsigned spins = 0; !__all(vc != 0xffffffffu) && !spin_fail(spins, &ctl->tmo[0], 4u); ++spins) vc = attnA_load_m<2>(F, seg, kvoff(pk, h), lane, km, vm);
#pragma unroll
    for (int u = 0; u < 4; ++u)
#pragma unroll
      for (int t = 0; t < 2; ++t) {
        const f32x4 y0 = mf((v4u){vm[2 * u], 0u, 0u, 0u}, pm[t], o[t][2 * u]), y1 = mf((v4u){vm[2 * u + 1], 0u, 0u, 0u}, pm[t], o[t][2 * u + 1]);
        __builtin_amdgcn_raw_buffer_store_b128((v4u){pk2(y0[0], y0[1]), pk2(y0[2], y0[3]), pk2(y1[0], y1[1]), pk2(y1[2], y1[3])}, rs, lane * 16 + u * 1024, oo + t * QBLK, 16);
      }
    SB0;
    if (seg + 2 * NSET < NSEG) { attnA_load_x<16>(F, parq, seg + NSET, h, lane, X); SB0; asm volatile("s_waitcnt vmcnt(24)" ::: "memory"); }
    else asm volatile("s_waitcnt vmcnt(0)" ::: "memory");
    asm volatile("s_barrier" ::: "memory");
    if (w == 0 && lane == 0) __hip_atomic_store(&ctl->flagO[seg][hg][0], 1u, RLX_AGENT);
    if (due) dur = min((unsigned)__builtin_amdgcn_s_memrealtime() - due - TP(F.x2, XA2), 170u);
    SB0;
    if (seg + NSET < NSEG && seg + 2 * NSET >= NSEG) attnA_load_x<16>(F, parq, seg + NSET, h, lane, X);
    ELOG(2);
  }
}
#define OU_AUX ""
#define OU_ST0
#define OU_ST1
#define OU_STO
#define OU_BODY \
          "s_mov_b64 s[40:41], %[qa]\n\ts_mov_b32 s42, %[qs]\n\ts_mov_b32 s43, 0x20000\n\ts_mov_b64 s[44:45], %[qa]\n\ts_mov_b32 s47, 0x20000\n\ts_nop 4\n\t" \
          "s_add_u32 m0, %[wl], 0\n\ts_xor_b32 %[t], %[wo], 0x0\n\tbuffer_load_dwordx4 %[vw0], %[rw], %[t] offen lds\n\ts_add_u32 m0, %[wl], 1024\n\ts_xor_b32 %[t], %[wo], 0x4000\n\tbuffer_load_dwordx4 %[vw1], %[rw], %[t] offen lds\n\t" \
          "s_add_u32 m0, %[wl], 2048\n\ts_xor_b32 %[t], %[wo], 0x8000\n\tbuffer_load_dwordx4 %[vw0], %[rw], %[t] offen lds\n\ts_add_u32 m0, %[wl], 3072\n\ts_xor_b32 %[t], %[wo], 0xc000\n\tbuffer_load_dwordx4 %[vw1], %[rw], %[t] offen lds\n\t" \
          "s_add_u32 m0, %[wl], 4096\n\ts_xor_b32 %[t], %[wo], 0x10000\n\tbuffer_load_dwordx4 %[vw0], %[rw], %[t] offen lds\n\ts_add_u32 m0, %[wl], 5120\n\ts_xor_b32 %[t], %[wo], 0x14000\n\tbuffer_load_dwordx4 %[vw1], %[rw], %[t] offen lds\n\t" \
          "s_add_u32 m0, %[wl], 6144\n\ts_xor_b32 %[t], %[wo], 0x18000\n\tbuffer_load_dwordx4 %[vw0], %[rw], %[t] offen lds\n\ts_add_u32 m0, %[wl], 7168\n\ts_xor_b32 %[t], %[wo], 0x1c000\n\tbuffer_load_dwordx4 %[vw1], %[rw], %[t] offen lds\n\t" \
          "s_add_u32 m0, %[wl], 8192\n\ts_xor_b32 %[t], %[wo], 0x20000\n\tbuffer_load_dwordx4 %[vw0], %[rw], %[t] offen lds\n\ts_add_u32 m0, %[wl], 9216\n\ts_xor_b32 %[t], %[wo], 0x24000\n\tbuffer_load_dwordx4 %[vw1], %[rw], %[t] offen lds\n\t" \
          "s_add_u32 m0, %[wl], 10240\n\ts_xor_b32 %[t], %[wo], 0x28000\n\tbuffer_load_dwordx4 %[vw0], %[rw], %[t] offen lds\n\ts_add_u32 m0, %[wl], 11264\n\ts_xor_b32 %[t], %[wo], 0x2c000\n\tbuffer_load_dwordx4 %[vw1], %[rw], %[t] offen lds\n\t" \
          "s_add_u32 m0, %[wl], 12288\n\ts_xor_b32 %[t], %[wo], 0x30000\n\tbuffer_load_dwordx4 %[vw0], %[rw], %[t] offen lds\n\ts_add_u32 m0, %[wl], 13312\n\ts_xor_b32 %[t], %[wo], 0x34000\n\tbuffer_load_dwordx4 %[vw1], %[rw], %[t] offen lds\n\t" \
          "s_add_u32 m0, %[wl], 14336\n\ts_xor_b32 %[t], %[wo], 0x38000\n\tbuffer_load_dwordx4 %[vw0], %[rw], %[t] offen lds\n\ts_add_u32 m0, %[wl], 15360\n\ts_xor_b32 %[t], %[wo], 0x3c000\n\tbuffer_load_dwordx4 %[vw1], %[rw], %[t] offen lds\n\t" \
          "s_add_u32 m0, %[wl], 16384\n\ts_xor_b32 %[t], %[wo], 0x40000\n\tbuffer_load_dwordx4 %[vw0], %[rw], %[t] offen lds\n\ts_add_u32 m0, %[wl], 17408\n\ts_xor_b32 %[t], %[wo], 0x44000\n\tbuffer_load_dwordx4 %[vw1], %[rw], %[t] offen lds\n\t" \
          "s_add_u32 m0, %[wl], 18432\n\ts_xor_b32 %[t], %[wo], 0x48000\n\tbuffer_load_dwordx4 %[vw0], %[rw], %[t] offen lds\n\ts_add_u32 m0, %[wl], 19456\n\ts_xor_b32 %[t], %[wo], 0x4c000\n\tbuffer_load_dwordx4 %[vw1], %[rw], %[t] offen lds\n\t" \
          "s_add_u32 m0, %[wl], 20480\n\ts_xor_b32 %[t], %[wo], 0x50000\n\tbuffer_load_dwordx4 %[vw0], %[rw], %[t] offen lds\n\ts_add_u32 m0, %[wl], 21504\n\ts_xor_b32 %[t], %[wo], 0x54000\n\tbuffer_load_dwordx4 %[vw1], %[rw], %[t] offen lds\n\t" \
          "s_add_u32 m0, %[wl], 22528\n\ts_xor_b32 %[t], %[wo], 0x58000\n\tbuffer_load_dwordx4 %[vw0], %[rw], %[t] offen lds\n\ts_add_u32 m0, %[wl], 23552\n\ts_xor_b32 %[t], %[wo], 0x5c000\n\tbuffer_load_dwordx4 %[vw1], %[rw], %[t] offen lds\n\t" \
          "s_add_u32 m0, %[wl], 24576\n\ts_xor_b32 %[t], %[wo], 0x60000\n\tbuffer_load_dwordx4 %[vw0], %[rw], %[t] offen lds\n\ts_add_u32 m0, %[wl], 25600\n\ts_xor_b32 %[t], %[wo], 0x64000\n\tbuffer_load_dwordx4 %[vw1], %[rw], %[t] offen lds\n\t" \
          "s_add_u32 m0, %[wl], 26624\n\ts_xor_b32 %[t], %[wo], 0x68000\n\tbuffer_load_dwordx4 %[vw0], %[rw], %[t] offen lds\n\ts_add_u32 m0, %[wl], 27648\n\ts_xor_b32 %[t], %[wo], 0x6c000\n\tbuffer_load_dwordx4 %[vw1], %[rw], %[t] offen lds\n\t" \
          "s_add_u32 m0, %[wl], 28672\n\ts_xor_b32 %[t], %[wo], 0x70000\n\tbuffer_load_dwordx4 %[vw0], %[rw], %[t] offen lds\n\ts_add_u32 m0, %[wl], 29696\n\ts_xor_b32 %[t], %[wo], 0x74000\n\tbuffer_load_dwordx4 %[vw1], %[rw], %[t] offen lds\n\t" \
          "s_add_u32 m0, %[wl], 30720\n\ts_xor_b32 %[t], %[wo], 0x78000\n\tbuffer_load_dwordx4 %[vw0], %[rw], %[t] offen lds\n\ts_add_u32 m0, %[wl], 31744\n\ts_xor_b32 %[t], %[wo], 0x7c000\n\tbuffer_load_dwordx4 %[vw1], %[rw], %[t] offen lds\n\t" \
          "s_add_u32 m0, %[wl], 32768\n\ts_xor_b32 %[t], %[wo], 0x80000\n\tbuffer_load_dwordx4 %[vw0], %[rw], %[t] offen lds\n\ts_add_u32 m0, %[wl], 33792\n\ts_xor_b32 %[t], %[wo], 0x84000\n\tbuffer_load_dwordx4 %[vw1], %[rw], %[t] offen lds\n\t" \
          "s_add_u32 m0, %[wl], 34816\n\ts_xor_b32 %[t], %[wo], 0x88000\n\tbuffer_load_dwordx4 %[vw0], %[rw], %[t] offen lds\n\ts_add_u32 m0, %[wl], 35840\n\ts_xor_b32 %[t], %[wo], 0x8c000\n\tbuffer_load_dwordx4 %[vw1], %[rw], %[t] offen lds\n\t" \
          "s_add_u32 m0, %[wl], 36864\n\ts_xor_b32 %[t], %[wo], 0x90000\n\tbuffer_load_dwordx4 %[vw0], %[rw], %[t] offen lds\n\ts_add_u32 m0, %[wl], 37888\n\ts_xor_b32 %[t], %[wo], 0x94000\n\tbuffer_load_dwordx4 %[vw1], %[rw], %[t] offen lds\n\t" \
          "s_add_u32 m0, %[wl], 38912\n\ts_xor_b32 %[t], %[wo], 0x98000\n\tbuffer_load_dwordx4 %[vw0], %[rw], %[t] offen lds\n\ts_add_u32 m0, %[wl], 39936\n\ts_xor_b32 %[t], %[wo], 0x9c000\n\tbuffer_load_dwordx4 %[vw1], %[rw], %[t] offen lds\n\t" \
          "s_waitcnt vmcnt(32)\n\tds_read_b64_tr_b16 a[0:1], %[rt0] offset:0\n\tds_read_b64_tr_b16 a[2:3], %[rt0] offset:512\n\tds_read_b64_tr_b16 a[4:5], %[rt1] offset:0\n\tds_read_b64_tr_b16 a[6:7], %[rt1] offset:512\n\tds_read_b64_tr_b16 v[128:129], %[rt2] offset:0\n\t" \
          "ds_read_b64_tr_b16 v[130:131], %[rt2] offset:512\n\tds_read_b64_tr_b16 v[32:33], %[rt3] offset:0\n\tds_read_b64_tr_b16 v[34:35], %[rt3] offset:512\n\tds_read_b64_tr_b16 a[8:9], %[rt0] offset:4096\n\tds_read_b64_tr_b16 a[10:11], %[rt0] offset:4608\n\tds_read_b64_tr_b16 a[12:13], %[rt1] offset:4096\n\t" \
          "ds_read_b64_tr_b16 a[14:15], %[rt1] offset:4608\n\tds_read_b64_tr_b16 v[132:133], %[rt2] offset:4096\n\tds_read_b64_tr_b16 v[134:135], %[rt2] offset:4608\n\tds_read_b64_tr_b16 v[36:37], %[rt3] offset:4096\n\tds_read_b64_tr_b16 v[38:39], %[rt3] offset:4608\n\ts_waitcnt lgkmcnt(0)\n\t" \
          "s_add_u32 m0, %[wl], 0\n\ts_xor_b32 %[t], %[wo], 0xa0000\n\tbuffer_load_dwordx4 %[vw0], %[rw], %[t] offen lds\n\ts_add_u32 m0, %[wl], 1024\n\ts_xor_b32 %[t], %[wo], 0xa4000\n\tbuffer_load_dwordx4 %[vw1], %[rw], %[t] offen lds\n\t" \
          "s_add_u32 m0, %[wl], 2048\n\ts_xor_b32 %[t], %[wo], 0xa8000\n\tbuffer_load_dwordx4 %[vw0], %[rw], %[t] offen lds\n\ts_add_u32 m0, %[wl], 3072\n\ts_xor_b32 %[t], %[wo], 0xac000\n\tbuffer_load_dwordx4 %[vw1], %[rw], %[t] offen lds\n\t" \
          "s_add_u32 m0, %[wl], 4096\n\ts_xor_b32 %[t], %[wo], 0xb0000\n\tbuffer_load_dwordx4 %[vw0], %[rw], %[t] offen lds\n\ts_add_u32 m0, %[wl], 5120\n\ts_xor_b32 %[t], %[wo], 0xb4000\n\tbuffer_load_dwordx4 %[vw1], %[rw], %[t] offen lds\n\t" \
          "s_add_u32 m0, %[wl], 6144\n\ts_xor_b32 %[t], %[wo], 0xb8000\n\tbuffer_load_dwordx4 %[vw0], %[rw], %[t] offen lds\n\ts_add_u32 m0, %[wl], 7168\n\ts_xor_b32 %[t], %[wo], 0xbc000\n\tbuffer_load_dwordx4 %[vw1], %[rw], %[t] offen lds\n\t" \
          "s_waitcnt vmcnt(32)\n\tds_read_b64_tr_b16 a[16:17], %[rt0] offset:8192\n\tds_read_b64_tr_b16 a[18:19], %[rt0] offset:8704\n\tds_read_b64_tr_b16 a[20:21], %[rt1] offset:8192\n\tds_read_b64_tr_b16 a[22:23], %[rt1] offset:8704\n\tds_read_b64_tr_b16 v[136:137], %[rt2] offset:8192\n\t" \
          "ds_read_b64_tr_b16 v[138:139], %[rt2] offset:8704\n\tds_read_b64_tr_b16 v[40:41], %[rt3] offset:8192\n\tds_read_b64_tr_b16 v[42:43], %[rt3] offset:8704\n\tds_read_b64_tr_b16 a[24:25], %[rt0] offset:12288\n\tds_read_b64_tr_b16 a[26:27], %[rt0] offset:12800\n\tds_read_b64_tr_b16 a[28:29], %[rt1] offset:12288\n\t" \
          "ds_read_b64_tr_b16 a[30:31], %[rt1] offset:12800\n\tds_read_b64_tr_b16 v[140:141], %[rt2] offset:12288\n\tds_read_b64_tr_b16 v[142:143], %[rt2] offset:12800\n\tds_read_b64_tr_b16 v[44:45], %[rt3] offset:12288\n\tds_read_b64_tr_b16 v[46:47], %[rt3] offset:12800\n\ts_waitcnt lgkmcnt(0)\n\t" \
          "s_add_u32 m0, %[wl], 8192\n\ts_xor_b32 %[t], %[wo], 0xc0000\n\tbuffer_load_dwordx4 %[vw0], %[rw], %[t] offen lds\n\ts_add_u32 m0, %[wl], 9216\n\ts_xor_b32 %[t], %[wo], 0xc4000\n\tbuffer_load_dwordx4 %[vw1], %[rw], %[t] offen lds\n\t" \
          "s_add_u32 m0, %[wl], 10240\n\ts_xor_b32 %[t], %[wo], 0xc8000\n\tbuffer_load_dwordx4 %[vw0], %[rw], %[t] offen lds\n\ts_add_u32 m0, %[wl], 11264\n\ts_xor_b32 %[t], %[wo], 0xcc000\n\tbuffer_load_dwordx4 %[vw1], %[rw], %[t] offen lds\n\t" \
          "s_add_u32 m0, %[wl], 12288\n\ts_xor_b32 %[t], %[wo], 0xd0000\n\tbuffer_load_dwordx4 %[vw0], %[rw], %[t] offen lds\n\ts_add_u32 m0, %[wl], 13312\n\ts_xor_b32 %[t], %[wo], 0xd4000\n\tbuffer_load_dwordx4 %[vw1], %[rw], %[t] offen lds\n\t" \
          "s_add_u32 m0, %[wl], 14336\n\ts_xor_b32 %[t], %[wo], 0xd8000\n\tbuffer_load_dwordx4 %[vw0], %[rw], %[t] offen lds\n\ts_add_u32 m0, %[wl], 15360\n\ts_xor_b32 %[t], %[wo], 0xdc000\n\tbuffer_load_dwordx4 %[vw1], %[rw], %[t] offen lds\n\t" \
          "s_waitcnt vmcnt(32)\n\tds_read_b64_tr_b16 a[32:33], %[rt0] offset:16384\n\tds_read_b64_tr_b16 a[34:35], %[rt0] offset:16896\n\tds_read_b64_tr_b16 a[36:37], %[rt1] offset:16384\n\tds_read_b64_tr_b16 a[38:39], %[rt1] offset:16896\n\tds_read_b64_tr_b16 v[144:145], %[rt2] offset:16384\n\t" \
          "ds_read_b64_tr_b16 v[146:147], %[rt2] offset:16896\n\tds_read_b64_tr_b16 v[48:49], %[rt3] offset:16384\n\tds_read_b64_tr_b16 v[50:51], %[rt3] offset:16896\n\tds_read_b64_tr_b16 a[40:41], %[rt0] offset:20480\n\tds_read_b64_tr_b16 a[42:43], %[rt0] offset:20992\n\tds_read_b64_tr_b16 a[44:45], %[rt1] offset:20480\n\t" \
          "ds_read_b64_tr_b16 a[46:47], %[rt1] offset:20992\n\tds_read_b64_tr_b16 v[148:149], %[rt2] offset:20480\n\tds_read_b64_tr_b16 v[150:151], %[rt2] offset:20992\n\tds_read_b64_tr_b16 v[52:53], %[rt3] offset:20480\n\tds_read_b64_tr_b16 v[54:55], %[rt3] offset:20992\n\ts_waitcnt lgkmcnt(0)\n\t" \
          "s_add_u32 m0, %[wl], 16384\n\ts_xor_b32 %[t], %[wo], 0xe0000\n\tbuffer_load_dwordx4 %[vw0], %[rw], %[t] offen lds\n\ts_add_u32 m0, %[wl], 17408\n\ts_xor_b32 %[t], %[wo], 0xe4000\n\tbuffer_load_dwordx4 %[vw1], %[rw], %[t] offen lds\n\t" \
          "s_add_u32 m0, %[wl], 18432\n\ts_xor_b32 %[t], %[wo], 0xe8000\n\tbuffer_load_dwordx4 %[vw0], %[rw], %[t] offen lds\n\ts_add_u32 m0, %[wl], 19456\n\ts_xor_b32 %[t], %[wo], 0xec000\n\tbuffer_load_dwordx4 %[vw1], %[rw], %[t] offen lds\n\t" \
          "s_add_u32 m0, %[wl], 20480\n\ts_xor_b32 %[t], %[wo], 0xf0000\n\tbuffer_load_dwordx4 %[vw0], %[rw], %[t] offen lds\n\ts_add_u32 m0, %[wl], 21504\n\ts_xor_b32 %[t], %[wo], 0xf4000\n\tbuffer_load_dwordx4 %[vw1], %[rw], %[t] offen lds\n\t" \
          "s_add_u32 m0, %[wl], 22528\n\ts_xor_b32 %[t], %[wo], 0xf8000\n\tbuffer_load_dwordx4 %[vw0], %[rw], %[t] offen lds\n\ts_add_u32 m0, %[wl], 23552\n\ts_xor_b32 %[t], %[wo], 0xfc000\n\tbuffer_load_dwordx4 %[vw1], %[rw], %[t] offen lds\n\t" \
          "s_waitcnt vmcnt(32)\n\tds_read_b64_tr_b16 a[48:49], %[rt0] offset:24576\n\tds_read_b64_tr_b16 a[50:51], %[rt0] offset:25088\n\tds_read_b64_tr_b16 a[52:53], %[rt1] offset:24576\n\tds_read_b64_tr_b16 a[54:55], %[rt1] offset:25088\n\tds_read_b64_tr_b16 v[152:153], %[rt2] offset:24576\n\t" \
          "ds_read_b64_tr_b16 v[154:155], %[rt2] offset:25088\n\tds_read_b64_tr_b16 v[56:57], %[rt3] offset:24576\n\tds_read_b64_tr_b16 v[58:59], %[rt3] offset:25088\n\tds_read_b64_tr_b16 a[56:57], %[rt0] offset:28672\n\tds_read_b64_tr_b16 a[58:59], %[rt0] offset:29184\n\tds_read_b64_tr_b16 a[60:61], %[rt1] offset:28672\n\t" \
          "ds_read_b64_tr_b16 a[62:63], %[rt1] offset:29184\n\tds_read_b64_tr_b16 v[156:157], %[rt2] offset:28672\n\tds_read_b64_tr_b16 v[158:159], %[rt2] offset:29184\n\tds_read_b64_tr_b16 v[60:61], %[rt3] offset:28672\n\tds_read_b64_tr_b16 v[62:63], %[rt3] offset:29184\n\ts_waitcnt lgkmcnt(0)\n\t" \
          "s_add_u32 m0, %[wl], 24576\n\ts_xor_b32 %[t], %[wo], 0x100000\n\tbuffer_load_dwordx4 %[vw0], %[rw], %[t] offen lds\n\ts_add_u32 m0, %[wl], 25600\n\ts_xor_b32 %[t], %[wo], 0x104000\n\tbuffer_load_dwordx4 %[vw1], %[rw], %[t] offen lds\n\t" \
          "s_add_u32 m0, %[wl], 26624\n\ts_xor_b32 %[t], %[wo], 0x108000\n\tbuffer_load_dwordx4 %[vw0], %[rw], %[t] offen lds\n\ts_add_u32 m0, %[wl], 27648\n\ts_xor_b32 %[t], %[wo], 0x10c000\n\tbuffer_load_dwordx4 %[vw1], %[rw], %[t] offen lds\n\t" \
          "s_add_u32 m0, %[wl], 28672\n\ts_xor_b32 %[t], %[wo], 0x110000\n\tbuffer_load_dwordx4 %[vw0], %[rw], %[t] offen lds\n\ts_add_u32 m0, %[wl], 29696\n\ts_xor_b32 %[t], %[wo], 0x114000\n\tbuffer_load_dwordx4 %[vw1], %[rw], %[t] offen lds\n\t" \
          "s_add_u32 m0, %[wl], 30720\n\ts_xor_b32 %[t], %[wo], 0x118000\n\tbuffer_load_dwordx4 %[vw0], %[rw], %[t] offen lds\n\ts_add_u32 m0, %[wl], 31744\n\ts_xor_b32 %[t], %[wo], 0x11c000\n\tbuffer_load_dwordx4 %[vw1], %[rw], %[t] offen lds\n\t" \
          "s_waitcnt vmcnt(32)\n\tds_read_b64_tr_b16 a[64:65], %[rt0] offset:32768\n\tds_read_b64_tr_b16 a[66:67], %[rt0] offset:33280\n\tds_read_b64_tr_b16 a[68:69], %[rt1] offset:32768\n\tds_read_b64_tr_b16 a[70:71], %[rt1] offset:33280\n\tds_read_b64_tr_b16 v[160:161], %[rt2] offset:32768\n\t" \
          "ds_read_b64_tr_b16 v[162:163], %[rt2] offset:33280\n\tds_read_b64_tr_b16 v[64:65], %[rt3] offset:32768\n\tds_read_b64_tr_b16 v[66:67], %[rt3] offset:33280\n\tds_read_b64_tr_b16 a[72:73], %[rt0] offset:36864\n\tds_read_b64_tr_b16 a[74:75], %[rt0] offset:37376\n\tds_read_b64_tr_b16 a[76:77], %[rt1] offset:36864\n\t" \
          "ds_read_b64_tr_b16 a[78:79], %[rt1] offset:37376\n\tds_read_b64_tr_b16 v[164:165], %[rt2] offset:36864\n\tds_read_b64_tr_b16 v[166:167], %[rt2] offset:37376\n\tds_read_b64_tr_b16 v[68:69], %[rt3] offset:36864\n\tds_read_b64_tr_b16 v[70:71], %[rt3] offset:37376\n\ts_waitcnt lgkmcnt(0)\n\t" \
          "s_add_u32 m0, %[wl], 32768\n\ts_xor_b32 %[t], %[wo], 0x120000\n\tbuffer_load_dwordx4 %[vw0], %[rw], %[t] offen lds\n\ts_add_u32 m0, %[wl], 33792\n\ts_xor_b32 %[t], %[wo], 0x124000\n\tbuffer_load_dwordx4 %[vw1], %[rw], %[t] offen lds\n\t" \
          "s_add_u32 m0, %[wl], 34816\n\ts_xor_b32 %[t], %[wo], 0x128000\n\tbuffer_load_dwordx4 %[vw0], %[rw], %[t] offen lds\n\ts_add_u32 m0, %[wl], 35840\n\ts_xor_b32 %[t], %[wo], 0x12c000\n\tbuffer_load_dwordx4 %[vw1], %[rw], %[t] offen lds\n\t" \
          "s_add_u32 m0, %[wl], 36864\n\ts_xor_b32 %[t], %[wo], 0x130000\n\tbuffer_load_dwordx4 %[vw0], %[rw], %[t] offen lds\n\ts_add_u32 m0, %[wl], 37888\n\ts_xor_b32 %[t], %[wo], 0x134000\n\tbuffer_load_dwordx4 %[vw1], %[rw], %[t] offen lds\n\t" \
          "s_add_u32 m0, %[wl], 38912\n\ts_xor_b32 %[t], %[wo], 0x138000\n\tbuffer_load_dwordx4 %[vw0], %[rw], %[t] offen lds\n\ts_add_u32 m0, %[wl], 39936\n\ts_xor_b32 %[t], %[wo], 0x13c000\n\tbuffer_load_dwordx4 %[vw1], %[rw], %[t] offen lds\n\t" \
          "s_waitcnt vmcnt(32)\n\tds_read_b64_tr_b16 a[80:81], %[rt0] offset:0\n\tds_read_b64_tr_b16 a[82:83], %[rt0] offset:512\n\tds_read_b64_tr_b16 a[84:85], %[rt1] offset:0\n\tds_read_b64_tr_b16 a[86:87], %[rt1] offset:512\n\tds_read_b64_tr_b16 v[168:169], %[rt2] offset:0\n\t" \
          "ds_read_b64_tr_b16 v[170:171], %[rt2] offset:512\n\tds_read_b64_tr_b16 v[72:73], %[rt3] offset:0\n\tds_read_b64_tr_b16 v[74:75], %[rt3] offset:512\n\tds_read_b64_tr_b16 a[88:89], %[rt0] offset:4096\n\tds_read_b64_tr_b16 a[90:91], %[rt0] offset:4608\n\tds_read_b64_tr_b16 a[92:93], %[rt1] offset:4096\n\t" \
          "ds_read_b64_tr_b16 a[94:95], %[rt1] offset:4608\n\tds_read_b64_tr_b16 v[172:173], %[rt2] offset:4096\n\tds_read_b64_tr_b16 v[174:175], %[rt2] offset:4608\n\tds_read_b64_tr_b16 v[76:77], %[rt3] offset:4096\n\tds_read_b64_tr_b16 v[78:79], %[rt3] offset:4608\n\ts_waitcnt lgkmcnt(0)\n\t" \
          "s_add_u32 m0, %[wl], 0\n\ts_xor_b32 %[t], %[wo], 0x140000\n\tbuffer_load_dwordx4 %[vw0], %[rw], %[t] offen lds\n\ts_add_u32 m0, %[wl], 1024\n\ts_xor_b32 %[t], %[wo], 0x144000\n\tbuffer_load_dwordx4 %[vw1], %[rw], %[t] offen lds\n\t" \
          "s_add_u32 m0, %[wl], 2048\n\ts_xor_b32 %[t], %[wo], 0x148000\n\tbuffer_load_dwordx4 %[vw0], %[rw], %[t] offen lds\n\ts_add_u32 m0, %[wl], 3072\n\ts_xor_b32 %[t], %[wo], 0x14c000\n\tbuffer_load_dwordx4 %[vw1], %[rw], %[t] offen lds\n\t" \
          "s_add_u32 m0, %[wl], 4096\n\ts_xor_b32 %[t], %[wo], 0x150000\n\tbuffer_load_dwordx4 %[vw0], %[rw], %[t] offen lds\n\ts_add_u32 m0, %[wl], 5120\n\ts_xor_b32 %[t], %[wo], 0x154000\n\tbuffer_load_dwordx4 %[vw1], %[rw], %[t] offen lds\n\t" \
          "s_add_u32 m0, %[wl], 6144\n\ts_xor_b32 %[t], %[wo], 0x158000\n\tbuffer_load_dwordx4 %[vw0], %[rw], %[t] offen lds\n\ts_add_u32 m0, %[wl], 7168\n\ts_xor_b32 %[t], %[wo], 0x15c000\n\tbuffer_load_dwordx4 %[vw1], %[rw], %[t] offen lds\n\t" \
          "s_waitcnt vmcnt(32)\n\tds_read_b64_tr_b16 a[96:97], %[rt0] offset:8192\n\tds_read_b64_tr_b16 a[98:99], %[rt0] offset:8704\n\tds_read_b64_tr_b16 a[100:101], %[rt1] offset:8192\n\tds_read_b64_tr_b16 a[102:103], %[rt1] offset:8704\n\tds_read_b64_tr_b16 v[176:177], %[rt2] offset:8192\n\t" \
          "ds_read_b64_tr_b16 v[178:179], %[rt2] offset:8704\n\tds_read_b64_tr_b16 v[80:81], %[rt3] offset:8192\n\tds_read_b64_tr_b16 v[82:83], %[rt3] offset:8704\n\tds_read_b64_tr_b16 a[104:105], %[rt0] offset:12288\n\tds_read_b64_tr_b16 a[106:107], %[rt0] offset:12800\n\tds_read_b64_tr_b16 a[108:109], %[rt1] offset:12288\n\t" \
          "ds_read_b64_tr_b16 a[110:111], %[rt1] offset:12800\n\tds_read_b64_tr_b16 v[180:181], %[rt2] offset:12288\n\tds_read_b64_tr_b16 v[182:183], %[rt2] offset:12800\n\tds_read_b64_tr_b16 v[84:85], %[rt3] offset:12288\n\tds_read_b64_tr_b16 v[86:87], %[rt3] offset:12800\n\ts_waitcnt lgkmcnt(0)\n\t" \
          "s_add_u32 m0, %[wl], 8192\n\ts_xor_b32 %[t], %[wo], 0x160000\n\tbuffer_load_dwordx4 %[vw0], %[rw], %[t] offen lds\n\ts_add_u32 m0, %[wl], 9216\n\ts_xor_b32 %[t], %[wo], 0x164000\n\tbuffer_load_dwordx4 %[vw1], %[rw], %[t] offen lds\n\t" \
          "s_add_u32 m0, %[wl], 10240\n\ts_xor_b32 %[t], %[wo], 0x168000\n\tbuffer_load_dwordx4 %[vw0], %[rw], %[t] offen lds\n\ts_add_u32 m0, %[wl], 11264\n\ts_xor_b32 %[t], %[wo], 0x16c000\n\tbuffer_load_dwordx4 %[vw1], %[rw], %[t] offen lds\n\t" \
          "s_add_u32 m0, %[wl], 12288\n\ts_xor_b32 %[t], %[wo], 0x170000\n\tbuffer_load_dwordx4 %[vw0], %[rw], %[t] offen lds\n\ts_add_u32 m0, %[wl], 13312\n\ts_xor_b32 %[t], %[wo], 0x174000\n\tbuffer_load_dwordx4 %[vw1], %[rw], %[t] offen lds\n\t" \
          "s_add_u32 m0, %[wl], 14336\n\ts_xor_b32 %[t], %[wo], 0x178000\n\tbuffer_load_dwordx4 %[vw0], %[rw], %[t] offen lds\n\ts_add_u32 m0, %[wl], 15360\n\ts_xor_b32 %[t], %[wo], 0x17c000\n\tbuffer_load_dwordx4 %[vw1], %[rw], %[t] offen lds\n\t" \
          "s_waitcnt vmcnt(32)\n\tds_read_b64_tr_b16 a[112:113], %[rt0] offset:16384\n\tds_read_b64_tr_b16 a[114:115], %[rt0] offset:16896\n\tds_read_b64_tr_b16 a[116:117], %[rt1] offset:16384\n\tds_read_b64_tr_b16 a[118:119], %[rt1] offset:16896\n\tds_read_b64_tr_b16 v[184:185], %[rt2] offset:16384\n\t" \
          "ds_read_b64_tr_b16 v[186:187], %[rt2] offset:16896\n\tds_read_b64_tr_b16 v[88:89], %[rt3] offset:16384\n\tds_read_b64_tr_b16 v[90:91], %[rt3] offset:16896\n\tds_read_b64_tr_b16 a[120:121], %[rt0] offset:20480\n\tds_read_b64_tr_b16 a[122:123], %[rt0] offset:20992\n\tds_read_b64_tr_b16 a[124:125], %[rt1] offset:20480\n\t" \
          "ds_read_b64_tr_b16 a[126:127], %[rt1] offset:20992\n\tds_read_b64_tr_b16 v[188:189], %[rt2] offset:20480\n\tds_read_b64_tr_b16 v[190:191], %[rt2] offset:20992\n\tds_read_b64_tr_b16 v[92:93], %[rt3] offset:20480\n\tds_read_b64_tr_b16 v[94:95], %[rt3] offset:20992\n\ts_waitcnt lgkmcnt(0)\n\t" \
          "s_add_u32 m0, %[wl], 16384\n\ts_xor_b32 %[t], %[wo], 0x180000\n\tbuffer_load_dwordx4 %[vw0], %[rw], %[t] offen lds\n\ts_add_u32 m0, %[wl], 17408\n\ts_xor_b32 %[t], %[wo], 0x184000\n\tbuffer_load_dwordx4 %[vw1], %[rw], %[t] offen lds\n\t" \
          "s_add_u32 m0, %[wl], 18432\n\ts_xor_b32 %[t], %[wo], 0x188000\n\tbuffer_load_dwordx4 %[vw0], %[rw], %[t] offen lds\n\ts_add_u32 m0, %[wl], 19456\n\ts_xor_b32 %[t], %[wo], 0x18c000\n\tbuffer_load_dwordx4 %[vw1], %[rw], %[t] offen lds\n\t" \
          "s_add_u32 m0, %[wl], 20480\n\ts_xor_b32 %[t], %[wo], 0x190000\n\tbuffer_load_dwordx4 %[vw0], %[rw], %[t] offen lds\n\ts_add_u32 m0, %[wl], 21504\n\ts_xor_b32 %[t], %[wo], 0x194000\n\tbuffer_load_dwordx4 %[vw1], %[rw], %[t] offen lds\n\t" \
          "s_add_u32 m0, %[wl], 22528\n\ts_xor_b32 %[t], %[wo], 0x198000\n\tbuffer_load_dwordx4 %[vw0], %[rw], %[t] offen lds\n\ts_add_u32 m0, %[wl], 23552\n\ts_xor_b32 %[t], %[wo], 0x19c000\n\tbuffer_load_dwordx4 %[vw1], %[rw], %[t] offen lds\n\t" \
          "s_waitcnt vmcnt(32)\n\tds_read_b64_tr_b16 a[128:129], %[rt0] offset:24576\n\tds_read_b64_tr_b16 a[130:131], %[rt0] offset:25088\n\tds_read_b64_tr_b16 a[132:133], %[rt1] offset:24576\n\tds_read_b64_tr_b16 a[134:135], %[rt1] offset:25088\n\tds_read_b64_tr_b16 v[192:193], %[rt2] offset:24576\n\t" \
          "ds_read_b64_tr_b16 v[194:195], %[rt2] offset:25088\n\tds_read_b64_tr_b16 v[96:97], %[rt3] offset:24576\n\tds_read_b64_tr_b16 v[98:99], %[rt3] offset:25088\n\tds_read_b64_tr_b16 a[136:137], %[rt0] offset:28672\n\tds_read_b64_tr_b16 a[138:139], %[rt0] offset:29184\n\tds_read_b64_tr_b16 a[140:141], %[rt1] offset:28672\n\t" \
          "ds_read_b64_tr_b16 a[142:143], %[rt1] offset:29184\n\tds_read_b64_tr_b16 v[196:197], %[rt2] offset:28672\n\tds_read_b64_tr_b16 v[198:199], %[rt2] offset:29184\n\tds_read_b64_tr_b16 v[100:101], %[rt3] offset:28672\n\tds_read_b64_tr_b16 v[102:103], %[rt3] offset:29184\n\ts_waitcnt lgkmcnt(0)\n\t" \
          "s_add_u32 m0, %[wl], 24576\n\ts_xor_b32 %[t], %[wo], 0x1a0000\n\tbuffer_load_dwordx4 %[vw0], %[rw], %[t] offen lds\n\ts_add_u32 m0, %[wl], 25600\n\ts_xor_b32 %[t], %[wo], 0x1a4000\n\tbuffer_load_dwordx4 %[vw1], %[rw], %[t] offen lds\n\t" \
          "s_add_u32 m0, %[wl], 26624\n\ts_xor_b32 %[t], %[wo], 0x1a8000\n\tbuffer_load_dwordx4 %[vw0], %[rw], %[t] offen lds\n\ts_add_u32 m0, %[wl], 27648\n\ts_xor_b32 %[t], %[wo], 0x1ac000\n\tbuffer_load_dwordx4 %[vw1], %[rw], %[t] offen lds\n\t" \
          "s_add_u32 m0, %[wl], 28672\n\ts_xor_b32 %[t], %[wo], 0x1b0000\n\tbuffer_load_dwordx4 %[vw0], %[rw], %[t] offen lds\n\ts_add_u32 m0, %[wl], 29696\n\ts_xor_b32 %[t], %[wo], 0x1b4000\n\tbuffer_load_dwordx4 %[vw1], %[rw], %[t] offen lds\n\t" \
          "s_add_u32 m0, %[wl], 30720\n\ts_xor_b32 %[t], %[wo], 0x1b8000\n\tbuffer_load_dwordx4 %[vw0], %[rw], %[t] offen lds\n\ts_add_u32 m0, %[wl], 31744\n\ts_xor_b32 %[t], %[wo], 0x1bc000\n\tbuffer_load_dwordx4 %[vw1], %[rw], %[t] offen lds\n\t" \
          "s_waitcnt vmcnt(32)\n\tds_read_b64_tr_b16 a[144:145], %[rt0] offset:32768\n\tds_read_b64_tr_b16 a[146:147], %[rt0] offset:33280\n\tds_read_b64_tr_b16 a[148:149], %[rt1] offset:32768\n\tds_read_b64_tr_b16 a[150:151], %[rt1] offset:33280\n\tds_read_b64_tr_b16 v[200:201], %[rt2] offset:32768\n\t" \
          "ds_read_b64_tr_b16 v[202:203], %[rt2] offset:33280\n\tds_read_b64_tr_b16 v[104:105], %[rt3] offset:32768\n\tds_read_b64_tr_b16 v[106:107], %[rt3] offset:33280\n\tds_read_b64_tr_b16 a[152:153], %[rt0] offset:36864\n\tds_read_b64_tr_b16 a[154:155], %[rt0] offset:37376\n\tds_read_b64_tr_b16 a[156:157], %[rt1] offset:36864\n\t" \
          "ds_read_b64_tr_b16 a[158:159], %[rt1] offset:37376\n\tds_read_b64_tr_b16 v[204:205], %[rt2] offset:36864\n\tds_read_b64_tr_b16 v[206:207], %[rt2] offset:37376\n\tds_read_b64_tr_b16 v[108:109], %[rt3] offset:36864\n\tds_read_b64_tr_b16 v[110:111], %[rt3] offset:37376\n\ts_waitcnt lgkmcnt(0)\n\t" \
          "s_add_u32 m0, %[wl], 32768\n\ts_xor_b32 %[t], %[wo], 0x1c0000\n\tbuffer_load_dwordx4 %[vw0], %[rw], %[t] offen lds\n\ts_add_u32 m0, %[wl], 33792\n\ts_xor_b32 %[t], %[wo], 0x1c4000\n\tbuffer_load_dwordx4 %[vw1], %[rw], %[t] offen lds\n\t" \
          "s_add_u32 m0, %[wl], 34816\n\ts_xor_b32 %[t], %[wo], 0x1c8000\n\tbuffer_load_dwordx4 %[vw0], %[rw], %[t] offen lds\n\ts_add_u32 m0, %[wl], 35840\n\ts_xor_b32 %[t], %[wo], 0x1cc000\n\tbuffer_load_dwordx4 %[vw1], %[rw], %[t] offen lds\n\t" \
          "s_add_u32 m0, %[wl], 36864\n\ts_xor_b32 %[t], %[wo], 0x1d0000\n\tbuffer_load_dwordx4 %[vw0], %[rw], %[t] offen lds\n\ts_add_u32 m0, %[wl], 37888\n\ts_xor_b32 %[t], %[wo], 0x1d4000\n\tbuffer_load_dwordx4 %[vw1], %[rw], %[t] offen lds\n\t" \
          "s_add_u32 m0, %[wl], 38912\n\ts_xor_b32 %[t], %[wo], 0x1d8000\n\tbuffer_load_dwordx4 %[vw0], %[rw], %[t] offen lds\n\ts_add_u32 m0, %[wl], 39936\n\ts_xor_b32 %[t], %[wo], 0x1dc000\n\tbuffer_load_dwordx4 %[vw1], %[rw], %[t] offen lds\n\t" \
          "s_waitcnt vmcnt(32)\n\tds_read_b64_tr_b16 a[160:161], %[rt0] offset:0\n\tds_read_b64_tr_b16 a[162:163], %[rt0] offset:512\n\tds_read_b64_tr_b16 a[164:165], %[rt1] offset:0\n\tds_read_b64_tr_b16 a[166:167], %[rt1] offset:512\n\tds_read_b64_tr_b16 v[208:209], %[rt2] offset:0\n\t" \
          "ds_read_b64_tr_b16 v[210:211], %[rt2] offset:512\n\tds_read_b64_tr_b16 v[112:113], %[rt3] offset:0\n\tds_read_b64_tr_b16 v[114:115], %[rt3] offset:512\n\tds_read_b64_tr_b16 a[168:169], %[rt0] offset:4096\n\tds_read_b64_tr_b16 a[170:171], %[rt0] offset:4608\n\tds_read_b64_tr_b16 a[172:173], %[rt1] offset:4096\n\t" \
          "ds_read_b64_tr_b16 a[174:175], %[rt1] offset:4608\n\tds_read_b64_tr_b16 v[212:213], %[rt2] offset:4096\n\tds_read_b64_tr_b16 v[214:215], %[rt2] offset:4608\n\tds_read_b64_tr_b16 v[116:117], %[rt3] offset:4096\n\tds_read_b64_tr_b16 v[118:119], %[rt3] offset:4608\n\ts_waitcnt lgkmcnt(0)\n\t" \
          "s_add_u32 m0, %[wl], 0\n\ts_xor_b32 %[t], %[wo], 0x1e0000\n\tbuffer_load_dwordx4 %[vw0], %[rw], %[t] offen lds\n\ts_add_u32 m0, %[wl], 1024\n\ts_xor_b32 %[t], %[wo], 0x1e4000\n\tbuffer_load_dwordx4 %[vw1], %[rw], %[t] offen lds\n\t" \
          "s_add_u32 m0, %[wl], 2048\n\ts_xor_b32 %[t], %[wo], 0x1e8000\n\tbuffer_load_dwordx4 %[vw0], %[rw], %[t] offen lds\n\ts_add_u32 m0, %[wl], 3072\n\ts_xor_b32 %[t], %[wo], 0x1ec000\n\tbuffer_load_dwordx4 %[vw1], %[rw], %[t] offen lds\n\t" \
          "s_add_u32 m0, %[wl], 4096\n\ts_xor_b32 %[t], %[wo], 0x1f0000\n\tbuffer_load_dwordx4 %[vw0], %[rw], %[t] offen lds\n\ts_add_u32 m0, %[wl], 5120\n\ts_xor_b32 %[t], %[wo], 0x1f4000\n\tbuffer_load_dwordx4 %[vw1], %[rw], %[t] offen lds\n\t" \
          "s_add_u32 m0, %[wl], 6144\n\ts_xor_b32 %[t], %[wo], 0x1f8000\n\tbuffer_load_dwordx4 %[vw0], %[rw], %[t] offen lds\n\ts_add_u32 m0, %[wl], 7168\n\ts_xor_b32 %[t], %[wo], 0x1fc000\n\tbuffer_load_dwordx4 %[vw1], %[rw], %[t] offen lds\n\t" \
          "s_waitcnt vmcnt(32)\n\tds_read_b64_tr_b16 a[176:177], %[rt0] offset:8192\n\tds_read_b64_tr_b16 a[178:179], %[rt0] offset:8704\n\tds_read_b64_tr_b16 a[180:181], %[rt1] offset:8192\n\tds_read_b64_tr_b16 a[182:183], %[rt1] offset:8704\n\tds_read_b64_tr_b16 v[216:217], %[rt2] offset:8192\n\t" \
          "ds_read_b64_tr_b16 v[218:219], %[rt2] offset:8704\n\tds_read_b64_tr_b16 v[120:121], %[rt3] offset:8192\n\tds_read_b64_tr_b16 v[122:123], %[rt3] offset:8704\n\tds_read_b64_tr_b16 a[184:185], %[rt0] offset:12288\n\tds_read_b64_tr_b16 a[186:187], %[rt0] offset:12800\n\tds_read_b64_tr_b16 a[188:189], %[rt1] offset:12288\n\t" \
          "ds_read_b64_tr_b16 a[190:191], %[rt1] offset:12800\n\tds_read_b64_tr_b16 v[220:221], %[rt2] offset:12288\n\tds_read_b64_tr_b16 v[222:223], %[rt2] offset:12800\n\tds_read_b64_tr_b16 v[124:125], %[rt3] offset:12288\n\tds_read_b64_tr_b16 v[126:127], %[rt3] offset:12800\n\ts_waitcnt lgkmcnt(0)\n\t" \
          "ds_write_b128 %[own], v[32:35] offset:8192\n\tds_write_b128 %[own], v[36:39] offset:9216\n\tds_write_b128 %[own], v[40:43] offset:10240\n\tds_write_b128 %[own], v[44:47] offset:11264\n\tds_write_b128 %[own], v[48:51] offset:12288\n\tds_write_b128 %[own], v[52:55] offset:13312\n\t" \
          "ds_write_b128 %[own], v[56:59] offset:14336\n\tds_write_b128 %[own], v[60:63] offset:15360\n\ts_waitcnt lgkmcnt(0)\n\ts_waitcnt vmcnt(24)\n\tds_read_b64_tr_b16 a[192:193], %[rt0] offset:16384\n\tds_read_b64_tr_b16 a[194:195], %[rt0] offset:16896\n\t" \
          "ds_read_b64_tr_b16 a[196:197], %[rt1] offset:16384\n\tds_read_b64_tr_b16 a[198:199], %[rt1] offset:16896\n\tds_read_b64_tr_b16 v[224:225], %[rt2] offset:16384\n\tds_read_b64_tr_b16 v[226:227], %[rt2] offset:16896\n\tds_read_b64_tr_b16 v[32:33], %[rt3] offset:16384\n\tds_read_b64_tr_b16 v[34:35], %[rt3] offset:16896\n\t" \
          "ds_read_b64_tr_b16 a[200:201], %[rt0] offset:20480\n\tds_read_b64_tr_b16 a[202:203], %[rt0] offset:20992\n\tds_read_b64_tr_b16 a[204:205], %[rt1] offset:20480\n\tds_read_b64_tr_b16 a[206:207], %[rt1] offset:20992\n\tds_read_b64_tr_b16 v[228:229], %[rt2] offset:20480\n\tds_read_b64_tr_b16 v[230:231], %[rt2] offset:20992\n\t" \
          "ds_read_b64_tr_b16 v[36:37], %[rt3] offset:20480\n\tds_read_b64_tr_b16 v[38:39], %[rt3] offset:20992\n\ts_waitcnt lgkmcnt(0)\n\tds_write_b128 %[own], v[64:67] offset:16384\n\tds_write_b128 %[own], v[68:71] offset:17408\n\tds_write_b128 %[own], v[72:75] offset:18432\n\t" \
          "ds_write_b128 %[own], v[76:79] offset:19456\n\tds_write_b128 %[own], v[80:83] offset:20480\n\tds_write_b128 %[own], v[84:87] offset:21504\n\tds_write_b128 %[own], v[88:91] offset:22528\n\tds_write_b128 %[own], v[92:95] offset:23552\n\ts_waitcnt lgkmcnt(0)\n\t" \
          "s_waitcnt vmcnt(16)\n\tds_read_b64_tr_b16 a[208:209], %[rt0] offset:24576\n\tds_read_b64_tr_b16 a[210:211], %[rt0] offset:25088\n\tds_read_b64_tr_b16 a[212:213], %[rt1] offset:24576\n\tds_read_b64_tr_b16 a[214:215], %[rt1] offset:25088\n\tds_read_b64_tr_b16 v[232:233], %[rt2] offset:24576\n\t" \
          "ds_read_b64_tr_b16 v[234:235], %[rt2] offset:25088\n\tds_read_b64_tr_b16 v[40:41], %[rt3] offset:24576\n\tds_read_b64_tr_b16 v[42:43], %[rt3] offset:25088\n\tds_read_b64_tr_b16 a[216:217], %[rt0] offset:28672\n\tds_read_b64_tr_b16 a[218:219], %[rt0] offset:29184\n\tds_read_b64_tr_b16 a[220:221], %[rt1] offset:28672\n\t" \
          "ds_read_b64_tr_b16 a[222:223], %[rt1] offset:29184\n\tds_read_b64_tr_b16 v[236:237], %[rt2] offset:28672\n\tds_read_b64_tr_b16 v[238:239], %[rt2] offset:29184\n\tds_read_b64_tr_b16 v[44:45], %[rt3] offset:28672\n\tds_read_b64_tr_b16 v[46:47], %[rt3] offset:29184\n\ts_waitcnt lgkmcnt(0)\n\t" \
          "ds_write_b128 %[own], v[96:99] offset:24576\n\tds_write_b128 %[own], v[100:103] offset:25600\n\tds_write_b128 %[own], v[104:107] offset:26624\n\tds_write_b128 %[own], v[108:111] offset:27648\n\tds_write_b128 %[own], v[112:115] offset:28672\n\tds_write_b128 %[own], v[116:119] offset:29696\n\t" \
          "ds_write_b128 %[own], v[120:123] offset:30720\n\tds_write_b128 %[own], v[124:127] offset:31744\n\ts_waitcnt lgkmcnt(0)\n\ts_waitcnt vmcnt(8)\n\tds_read_b64_tr_b16 a[224:225], %[rt0] offset:32768\n\tds_read_b64_tr_b16 a[226:227], %[rt0] offset:33280\n\t" \
          "ds_read_b64_tr_b16 a[228:229], %[rt1] offset:32768\n\tds_read_b64_tr_b16 a[230:231], %[rt1] offset:33280\n\tds_read_b64_tr_b16 v[240:241], %[rt2] offset:32768\n\tds_read_b64_tr_b16 v[242:243], %[rt2] offset:33280\n\tds_read_b64_tr_b16 v[48:49], %[rt3] offset:32768\n\tds_read_b64_tr_b16 v[50:51], %[rt3] offset:33280\n\t" \
          "ds_read_b64_tr_b16 a[232:233], %[rt0] offset:36864\n\tds_read_b64_tr_b16 a[234:235], %[rt0] offset:37376\n\tds_read_b64_tr_b16 a[236:237], %[rt1] offset:36864\n\tds_read_b64_tr_b16 a[238:239], %[rt1] offset:37376\n\tds_read_b64_tr_b16 v[244:245], %[rt2] offset:36864\n\tds_read_b64_tr_b16 v[246:247], %[rt2] offset:37376\n\t" \
          "ds_read_b64_tr_b16 v[52:53], %[rt3] offset:36864\n\tds_read_b64_tr_b16 v[54:55], %[rt3] offset:37376\n\ts_waitcnt lgkmcnt(0)\n\ts_waitcnt vmcnt(0)\n\tds_read_b64_tr_b16 a[240:241], %[rt0] offset:0\n\tds_read_b64_tr_b16 a[242:243], %[rt0] offset:512\n\t" \
          "ds_read_b64_tr_b16 a[244:245], %[rt1] offset:0\n\tds_read_b64_tr_b16 a[246:247], %[rt1] offset:512\n\tds_read_b64_tr_b16 v[248:249], %[rt2] offset:0\n\tds_read_b64_tr_b16 v[250:251], %[rt2] offset:512\n\tds_read_b64_tr_b16 v[56:57], %[rt3] offset:0\n\tds_read_b64_tr_b16 v[58:59], %[rt3] offset:512\n\t" \
          "ds_read_b64_tr_b16 a[248:249], %[rt0] offset:4096\n\tds_read_b64_tr_b16 a[250:251], %[rt0] offset:4608\n\tds_read_b64_tr_b16 a[252:253], %[rt1] offset:4096\n\tds_read_b64_tr_b16 a[254:255], %[rt1] offset:4608\n\tds_read_b64_tr_b16 v[252:253], %[rt2] offset:4096\n\tds_read_b64_tr_b16 v[254:255], %[rt2] offset:4608\n\t" \
          "ds_read_b64_tr_b16 v[60:61], %[rt3] offset:4096\n\tds_read_b64_tr_b16 v[62:63], %[rt3] offset:4608\n\ts_waitcnt lgkmcnt(0)\n\tds_write_b128 %[own], v[32:35] offset:0\n\tds_write_b128 %[own], v[36:39] offset:1024\n\tds_write_b128 %[own], v[40:43] offset:2048\n\t" \
          "ds_write_b128 %[own], v[44:47] offset:3072\n\tds_write_b128 %[own], v[48:51] offset:4096\n\tds_write_b128 %[own], v[52:55] offset:5120\n\tds_write_b128 %[own], v[56:59] offset:6144\n\tds_write_b128 %[own], v[60:63] offset:7168\n\ts_waitcnt lgkmcnt(0)\n\t" \
          "Lou_wait%=:\n\ts_mov_b32 %[cnt], 0\n\tLou_poll%=:\n\ts_lshr_b32 %[t], %[j], 1\n\ts_cmp_lt_u32 %[t], %[av]\n\ts_cbranch_scc1 Lou_load%=\n\t" \
          "s_sleep 2\n\ts_lshl_b32 %[t], %[av], 10\n\ts_add_u32 %[t], %[t], %[fo]\n\tbuffer_load_dwordx2 v[30:31], %[vf], %[rf], %[t] offen sc1\n\tbuffer_load_dword v48, off, %[rf], %[tmo] sc1\n\ts_waitcnt vmcnt(0)\n\t" \
          "s_lshr_b32 %[stp], %[j], 1\n\ts_cmp_eq_u32 %[stp], %[av]\n\ts_cselect_b32 %[stp], %[sp], 1\n\tv_cmp_eq_u32 vcc, 0, v30\n\ts_nop 0\n\ts_ff1_i32_b64 %[t], vcc\n\t" \
          "s_lshr_b32 %[t], %[t], 1\n\ts_add_u32 %[av], %[av], %[t]\n\ts_or_b32 %[stp], %[stp], %[t]\n\ts_cbranch_scc1 Lou_ns%=\n\tv_readlane_b32 %[t], v31, 0\n\tv_readlane_b32 %[stp], v31, 1\n\t" \
          "s_and_b32 vcc_lo, %[t], %[stp]\n\ts_cbranch_scc0 Lou_ns%=\n\ts_sub_i32 vcc_lo, %[t], %[stp]\n\ts_cmp_gt_i32 vcc_lo, 0\n\ts_cselect_b32 %[t], %[t], %[stp]\n\ts_mov_b32 %[sp], 1\n\t" \
          "Lou_tw%=:\n\ts_sleep 1\n\ts_memrealtime vcc\n\ts_waitcnt lgkmcnt(0)\n\ts_sub_i32 vcc_lo, %[t], vcc_lo\n\ts_sub_u32 vcc_lo, vcc_lo, 1\n\t" \
          "s_cmp_lt_u32 vcc_lo, 2000\n\ts_cbranch_scc1 Lou_tw%=\n\ts_branch Lou_load%=\n\tLou_ns%=:\n\tv_readfirstlane_b32 vcc_lo, v48\n\ts_add_u32 %[cnt], %[cnt], 1\n\t" \
          "s_lshr_b32 vcc_hi, %[cnt], 15\n\ts_or_b32 vcc_lo, vcc_lo, vcc_hi\n\ts_cbranch_scc0 Lou_poll%=\n\tv_mov_b32 v48, 7\n\tbuffer_store_dword v48, off, %[rf], %[tmo] sc1\n\ts_mov_b32 %[av], 64\n\t" \
          "Lou_load%=:\n\t" OU_ST0 "s_nop 0\n\tbuffer_load_dwordx4 v[64:67], %[vo0], s[40:43], %[pg0] offen sc1\n\tbuffer_load_dwordx4 v[68:71], %[vo0], s[40:43], %[pg0] offen offset:1024 sc1\n\tbuffer_load_dwordx4 v[72:75], %[vo0], s[40:43], %[pg0] offen offset:2048 sc1\n\tbuffer_load_dwordx4 v[76:79], %[vo0], s[40:43], %[pg0] offen offset:3072 sc1\n\t" \
          "buffer_load_dwordx4 v[80:83], %[vo1], s[40:43], %[pg0] offen sc1\n\tbuffer_load_dwordx4 v[84:87], %[vo1], s[40:43], %[pg0] offen offset:1024 sc1\n\tbuffer_load_dwordx4 v[88:91], %[vo1], s[40:43], %[pg0] offen offset:2048 sc1\n\tbuffer_load_dwordx4 v[92:95], %[vo1], s[40:43], %[pg0] offen offset:3072 sc1\n\tbuffer_load_dwordx4 v[96:99], %[vo0], s[40:43], %[pg1] offen sc1\n\tbuffer_load_dwordx4 v[100:103], %[vo0], s[40:43], %[pg1] offen offset:1024 sc1\n\t" \
          "buffer_load_dwordx4 v[104:107], %[vo0], s[40:43], %[pg1] offen offset:2048 sc1\n\tbuffer_load_dwordx4 v[108:111], %[vo0], s[40:43], %[pg1] offen offset:3072 sc1\n\tbuffer_load_dwordx4 v[112:115], %[vo1], s[40:43], %[pg1] offen sc1\n\tbuffer_load_dwordx4 v[116:119], %[vo1], s[40:43], %[pg1] offen offset:1024 sc1\n\tbuffer_load_dwordx4 v[120:123], %[vo1], s[40:43], %[pg1] offen offset:2048 sc1\n\tbuffer_load_dwordx4 v[124:127], %[vo1], s[40:43], %[pg1] offen offset:3072 sc1\n\t" \
          "Lou_top%=:\n\ts_lshl_b32 %[t], %[av], 10\n\ts_add_u32 %[t], %[t], %[fo]\n\tbuffer_load_dwordx2 v[30:31], %[vf], %[rf], %[t] offen sc1\n\tds_read_b128 v[32:35], %[own] offset:8192\n\tds_read_b128 v[36:39], %[own] offset:9216\n\t" \
          "ds_read_b128 v[40:43], %[own] offset:10240\n\ts_waitcnt vmcnt(16) lgkmcnt(2)\n\tv_mfma_f32_16x16x32_bf16 v[48:51], a[0:3], v[64:67], 0\n\tds_read_b128 v[44:47], %[own] offset:11264\n\tv_mfma_f32_16x16x32_bf16 v[52:55], a[4:7], v[64:67], 0\n\tv_mfma_f32_16x16x32_bf16 v[56:59], v[128:131], v[64:67], 0\n\t" \
          "v_mfma_f32_16x16x32_bf16 v[60:63], v[32:35], v[64:67], 0\n\tbuffer_load_dwordx4 v[64:67], %[vo0], s[40:43], %[pg2] offen sc1\n\ts_waitcnt vmcnt(16) lgkmcnt(2)\n\tv_mfma_f32_16x16x32_bf16 v[48:51], a[8:11], v[68:71], v[48:51]\n\tds_read_b128 v[32:35], %[own] offset:12288\n\tv_mfma_f32_16x16x32_bf16 v[52:55], a[12:15], v[68:71], v[52:55]\n\t" \
          "v_mfma_f32_16x16x32_bf16 v[56:59], v[132:135], v[68:71], v[56:59]\n\tv_mfma_f32_16x16x32_bf16 v[60:63], v[36:39], v[68:71], v[60:63]\n\tbuffer_load_dwordx4 v[68:71], %[vo0], s[40:43], %[pg2] offen offset:1024 sc1\n\ts_waitcnt vmcnt(16) lgkmcnt(2)\n\tv_mfma_f32_16x16x32_bf16 v[48:51], a[16:19], v[72:75], v[48:51]\n\tds_read_b128 v[36:39], %[own] offset:13312\n\t" \
          "v_mfma_f32_16x16x32_bf16 v[52:55], a[20:23], v[72:75], v[52:55]\n\tv_mfma_f32_16x16x32_bf16 v[56:59], v[136:139], v[72:75], v[56:59]\n\tv_mfma_f32_16x16x32_bf16 v[60:63], v[40:43], v[72:75], v[60:63]\n\tbuffer_load_dwordx4 v[72:75], %[vo0], s[40:43], %[pg2] offen offset:2048 sc1\n\ts_waitcnt vmcnt(16) lgkmcnt(2)\n\tv_mfma_f32_16x16x32_bf16 v[48:51], a[24:27], v[76:79], v[48:51]\n\t" \
          "ds_read_b128 v[40:43], %[own] offset:14336\n\tv_mfma_f32_16x16x32_bf16 v[52:55], a[28:31], v[76:79], v[52:55]\n\tv_mfma_f32_16x16x32_bf16 v[56:59], v[140:143], v[76:79], v[56:59]\n\tv_mfma_f32_16x16x32_bf16 v[60:63], v[44:47], v[76:79], v[60:63]\n\tbuffer_load_dwordx4 v[76:79], %[vo0], s[40:43], %[pg2] offen offset:3072 sc1\n\ts_waitcnt vmcnt(16) lgkmcnt(2)\n\t" \
          "v_mfma_f32_16x16x32_bf16 v[48:51], a[32:35], v[80:83], v[48:51]\n\tds_read_b128 v[44:47], %[own] offset:15360\n\tv_mfma_f32_16x16x32_bf16 v[52:55], a[36:39], v[80:83], v[52:55]\n\tv_mfma_f32_16x16x32_bf16 v[56:59], v[144:147], v[80:83], v[56:59]\n\tv_mfma_f32_16x16x32_bf16 v[60:63], v[32:35], v[80:83], v[60:63]\n\tbuffer_load_dwordx4 v[80:83], %[vo1], s[40:43], %[pg2] offen sc1\n\t" \
          "s_waitcnt vmcnt(16) lgkmcnt(2)\n\tv_mfma_f32_16x16x32_bf16 v[48:51], a[40:43], v[84:87], v[48:51]\n\tds_read_b128 v[32:35], %[own] offset:16384\n\tv_mfma_f32_16x16x32_bf16 v[52:55], a[44:47], v[84:87], v[52:55]\n\tv_mfma_f32_16x16x32_bf16 v[56:59], v[148:151], v[84:87], v[56:59]\n\tv_mfma_f32_16x16x32_bf16 v[60:63], v[36:39], v[84:87], v[60:63]\n\t" \
          "buffer_load_dwordx4 v[84:87], %[vo1], s[40:43], %[pg2] offen offset:1024 sc1\n\ts_waitcnt vmcnt(16) lgkmcnt(2)\n\tv_mfma_f32_16x16x32_bf16 v[48:51], a[48:51], v[88:91], v[48:51]\n\tds_read_b128 v[36:39], %[own] offset:17408\n\tv_mfma_f32_16x16x32_bf16 v[52:55], a[52:55], v[88:91], v[52:55]\n\tv_mfma_f32_16x16x32_bf16 v[56:59], v[152:155], v[88:91], v[56:59]\n\t" \
          "v_mfma_f32_16x16x32_bf16 v[60:63], v[40:43], v[88:91], v[60:63]\n\tbuffer_load_dwordx4 v[88:91], %[vo1], s[40:43], %[pg2] offen offset:2048 sc1\n\ts_waitcnt vmcnt(16) lgkmcnt(2)\n\tv_mfma_f32_16x16x32_bf16 v[48:51], a[56:59], v[92:95], v[48:51]\n\tds_read_b128 v[40:43], %[own] offset:18432\n\tv_mfma_f32_16x16x32_bf16 v[52:55], a[60:63], v[92:95], v[52:55]\n\t" \
          "v_mfma_f32_16x16x32_bf16 v[56:59], v[156:159], v[92:95], v[56:59]\n\tv_mfma_f32_16x16x32_bf16 v[60:63], v[44:47], v[92:95], v[60:63]\n\tbuffer_load_dwordx4 v[92:95], %[vo1], s[40:43], %[pg2] offen offset:3072 sc1\n\ts_waitcnt vmcnt(16) lgkmcnt(2)\n\tv_mfma_f32_16x16x32_bf16 v[48:51], a[64:67], v[96:99], v[48:51]\n\tds_read_b128 v[44:47], %[own] offset:19456\n\t" \
          "v_mfma_f32_16x16x32_bf16 v[52:55], a[68:71], v[96:99], v[52:55]\n\tv_mfma_f32_16x16x32_bf16 v[56:59], v[160:163], v[96:99], v[56:59]\n\tv_mfma_f32_16x16x32_bf16 v[60:63], v[32:35], v[96:99], v[60:63]\n\tbuffer_load_dwordx4 v[96:99], %[vo0], s[40:43], %[pg3] offen sc1\n\ts_waitcnt vmcnt(16) lgkmcnt(2)\n\tv_mfma_f32_16x16x32_bf16 v[48:51], a[72:75], v[100:103], v[48:51]\n\t" \
          "ds_read_b128 v[32:35], %[own] offset:20480\n\tv_mfma_f32_16x16x32_bf16 v[52:55], a[76:79], v[100:103], v[52:55]\n\tv_mfma_f32_16x16x32_bf16 v[56:59], v[164:167], v[100:103], v[56:59]\n\tv_mfma_f32_16x16x32_bf16 v[60:63], v[36:39], v[100:103], v[60:63]\n\tbuffer_load_dwordx4 v[100:103], %[vo0], s[40:43], %[pg3] offen offset:1024 sc1\n\ts_waitcnt vmcnt(16) lgkmcnt(2)\n\t" \
          "v_mfma_f32_16x16x32_bf16 v[48:51], a[80:83], v[104:107], v[48:51]\n\tds_read_b128 v[36:39], %[own] offset:21504\n\tv_mfma_f32_16x16x32_bf16 v[52:55], a[84:87], v[104:107], v[52:55]\n\tv_mfma_f32_16x16x32_bf16 v[56:59], v[168:171], v[104:107], v[56:59]\n\tv_mfma_f32_16x16x32_bf16 v[60:63], v[40:43], v[104:107], v[60:63]\n\tbuffer_load_dwordx4 v[104:107], %[vo0], s[40:43], %[pg3] offen offset:2048 sc1\n\t" \
          "s_waitcnt vmcnt(16) lgkmcnt(2)\n\tv_mfma_f32_16x16x32_bf16 v[48:51], a[88:91], v[108:111], v[48:51]\n\tds_read_b128 v[40:43], %[own] offset:22528\n\tv_mfma_f32_16x16x32_bf16 v[52:55], a[92:95], v[108:111], v[52:55]\n\tv_mfma_f32_16x16x32_bf16 v[56:59], v[172:175], v[108:111], v[56:59]\n\tv_mfma_f32_16x16x32_bf16 v[60:63], v[44:47], v[108:111], v[60:63]\n\t" \
          "buffer_load_dwordx4 v[108:111], %[vo0], s[40:43], %[pg3] offen offset:3072 sc1\n\ts_waitcnt vmcnt(16) lgkmcnt(2)\n\tv_mfma_f32_16x16x32_bf16 v[48:51], a[96:99], v[112:115], v[48:51]\n\tds_read_b128 v[44:47], %[own] offset:23552\n\tv_mfma_f32_16x16x32_bf16 v[52:55], a[100:103], v[112:115], v[52:55]\n\tv_mfma_f32_16x16x32_bf16 v[56:59], v[176:179], v[112:115], v[56:59]\n\t" \
          "v_mfma_f32_16x16x32_bf16 v[60:63], v[32:35], v[112:115], v[60:63]\n\tbuffer_load_dwordx4 v[112:115], %[vo1], s[40:43], %[pg3] offen sc1\n\ts_waitcnt vmcnt(16) lgkmcnt(2)\n\tv_mfma_f32_16x16x32_bf16 v[48:51], a[104:107], v[116:119], v[48:51]\n\tds_read_b128 v[32:35], %[own] offset:24576\n\tv_mfma_f32_16x16x32_bf16 v[52:55], a[108:111], v[116:119], v[52:55]\n\t" \
          "v_mfma_f32_16x16x32_bf16 v[56:59], v[180:183], v[116:119], v[56:59]\n\tv_mfma_f32_16x16x32_bf16 v[60:63], v[36:39], v[116:119], v[60:63]\n\tbuffer_load_dwordx4 v[116:119], %[vo1], s[40:43], %[pg3] offen offset:1024 sc1\n\ts_waitcnt vmcnt(16) lgkmcnt(2)\n\tv_mfma_f32_16x16x32_bf16 v[48:51], a[112:115], v[120:123], v[48:51]\n\tds_read_b128 v[36:39], %[own] offset:25600\n\t" \
          "v_mfma_f32_16x16x32_bf16 v[52:55], a[116:119], v[120:123], v[52:55]\n\tv_mfma_f32_16x16x32_bf16 v[56:59], v[184:187], v[120:123], v[56:59]\n\tv_mfma_f32_16x16x32_bf16 v[60:63], v[40:43], v[120:123], v[60:63]\n\tbuffer_load_dwordx4 v[120:123], %[vo1], s[40:43], %[pg3] offen offset:2048 sc1\n\ts_waitcnt vmcnt(16) lgkmcnt(2)\n\tv_mfma_f32_16x16x32_bf16 v[48:51], a[120:123], v[124:127], v[48:51]\n\t" \
          "ds_read_b128 v[40:43], %[own] offset:26624\n\tv_mfma_f32_16x16x32_bf16 v[52:55], a[124:127], v[124:127], v[52:55]\n\tv_mfma_f32_16x16x32_bf16 v[56:59], v[188:191], v[124:127], v[56:59]\n\tv_mfma_f32_16x16x32_bf16 v[60:63], v[44:47], v[124:127], v[60:63]\n\tbuffer_load_dwordx4 v[124:127], %[vo1], s[40:43], %[pg3] offen offset:3072 sc1\n\ts_waitcnt vmcnt(15) lgkmcnt(2)\n\t" \
          "v_mfma_f32_16x16x32_bf16 v[48:51], a[128:131], v[64:67], v[48:51]\n\tds_read_b128 v[44:47], %[own] offset:27648\n\tv_mfma_f32_16x16x32_bf16 v[52:55], a[132:135], v[64:67], v[52:55]\n\tv_cmp_eq_u32 vcc, 0, v30\n\ts_nop 0\n\ts_ff1_i32_b64 %[t], vcc\n\t" \
          "s_lshr_b32 %[t], %[t], 1\n\ts_add_u32 %[av], %[av], %[t]\n\tv_mfma_f32_16x16x32_bf16 v[56:59], v[192:195], v[64:67], v[56:59]\n\ts_lshl_b32 %[oo], %[j], 16\n\ts_cmp_lt_u32 %[j], %[j1]\n\ts_cselect_b32 %[stp], %[sa], 4\n\t" \
          "s_add_u32 %[j], %[j], %[stp]\n\ts_lshl_b32 %[t], %[stp], 14\n\ts_add_u32 %[pg0], %[pg0], %[t]\n\ts_add_u32 %[pg1], %[pg1], %[t]\n\ts_add_u32 %[pg2], %[pg2], %[t]\n\ts_add_u32 %[pg3], %[pg3], %[t]\n\t" \
          "s_lshr_b32 %[t], %[j], 1\n\ts_cmp_lt_u32 %[t], %[av]\n\ts_cselect_b32 s46, s42, 0\n\ts_cmp_lt_u32 %[j], %[je]\n\ts_cselect_b32 s46, s46, 0\n\tv_mfma_f32_16x16x32_bf16 v[60:63], v[32:35], v[64:67], v[60:63]\n\t" \
          "buffer_load_dwordx4 v[64:67], %[vo0], s[44:47], %[pg0] offen sc1\n\ts_waitcnt vmcnt(15) lgkmcnt(2)\n\tv_mfma_f32_16x16x32_bf16 v[48:51], a[136:139], v[68:71], v[48:51]\n\tds_read_b128 v[32:35], %[own] offset:28672\n\tv_mfma_f32_16x16x32_bf16 v[52:55], a[140:143], v[68:71], v[52:55]\n\tv_mfma_f32_16x16x32_bf16 v[56:59], v[196:199], v[68:71], v[56:59]\n\t" \
          "v_mfma_f32_16x16x32_bf16 v[60:63], v[36:39], v[68:71], v[60:63]\n\tbuffer_load_dwordx4 v[68:71], %[vo0], s[44:47], %[pg0] offen offset:1024 sc1\n\ts_waitcnt vmcnt(15) lgkmcnt(2)\n\tv_mfma_f32_16x16x32_bf16 v[48:51], a[144:147], v[72:75], v[48:51]\n\tds_read_b128 v[36:39], %[own] offset:29696\n\tv_mfma_f32_16x16x32_bf16 v[52:55], a[148:151], v[72:75], v[52:55]\n\t" \
          "v_mfma_f32_16x16x32_bf16 v[56:59], v[200:203], v[72:75], v[56:59]\n\tv_mfma_f32_16x16x32_bf16 v[60:63], v[40:43], v[72:75], v[60:63]\n\tbuffer_load_dwordx4 v[72:75], %[vo0], s[44:47], %[pg0] offen offset:2048 sc1\n\ts_waitcnt vmcnt(15) lgkmcnt(2)\n\tv_mfma_f32_16x16x32_bf16 v[48:51], a[152:155], v[76:79], v[48:51]\n\tds_read_b128 v[40:43], %[own] offset:30720\n\t" \
          "v_mfma_f32_16x16x32_bf16 v[52:55], a[156:159], v[76:79], v[52:55]\n\tv_mfma_f32_16x16x32_bf16 v[56:59], v[204:207], v[76:79], v[56:59]\n\tv_mfma_f32_16x16x32_bf16 v[60:63], v[44:47], v[76:79], v[60:63]\n\tbuffer_load_dwordx4 v[76:79], %[vo0], s[44:47], %[pg0] offen offset:3072 sc1\n\ts_waitcnt vmcnt(15) lgkmcnt(2)\n\tv_mfma_f32_16x16x32_bf16 v[48:51], a[160:163], v[80:83], v[48:51]\n\t" \
          "ds_read_b128 v[44:47], %[own] offset:31744\n\tv_mfma_f32_16x16x32_bf16 v[52:55], a[164:167], v[80:83], v[52:55]\n\tv_mfma_f32_16x16x32_bf16 v[56:59], v[208:211], v[80:83], v[56:59]\n\tv_mfma_f32_16x16x32_bf16 v[60:63], v[32:35], v[80:83], v[60:63]\n\tbuffer_load_dwordx4 v[80:83], %[vo1], s[44:47], %[pg0] offen sc1\n\ts_waitcnt vmcnt(15) lgkmcnt(2)\n\t" \
          "v_mfma_f32_16x16x32_bf16 v[48:51], a[168:171], v[84:87], v[48:51]\n\tds_read_b128 v[32:35], %[own] offset:0\n\tv_mfma_f32_16x16x32_bf16 v[52:55], a[172:175], v[84:87], v[52:55]\n\tv_mfma_f32_16x16x32_bf16 v[56:59], v[212:215], v[84:87], v[56:59]\n\tv_mfma_f32_16x16x32_bf16 v[60:63], v[36:39], v[84:87], v[60:63]\n\tbuffer_load_dwordx4 v[84:87], %[vo1], s[44:47], %[pg0] offen offset:1024 sc1\n\t" \
          "s_waitcnt vmcnt(15) lgkmcnt(2)\n\tv_mfma_f32_16x16x32_bf16 v[48:51], a[176:179], v[88:91], v[48:51]\n\tds_read_b128 v[36:39], %[own] offset:1024\n\tv_mfma_f32_16x16x32_bf16 v[52:55], a[180:183], v[88:91], v[52:55]\n\tv_mfma_f32_16x16x32_bf16 v[56:59], v[216:219], v[88:91], v[56:59]\n\tv_mfma_f32_16x16x32_bf16 v[60:63], v[40:43], v[88:91], v[60:63]\n\t" \
          "buffer_load_dwordx4 v[88:91], %[vo1], s[44:47], %[pg0] offen offset:2048 sc1\n\ts_waitcnt vmcnt(15) lgkmcnt(2)\n\tv_mfma_f32_16x16x32_bf16 v[48:51], a[184:187], v[92:95], v[48:51]\n\tds_read_b128 v[40:43], %[own] offset:2048\n\tv_mfma_f32_16x16x32_bf16 v[52:55], a[188:191], v[92:95], v[52:55]\n\tv_mfma_f32_16x16x32_bf16 v[56:59], v[220:223], v[92:95], v[56:59]\n\t" \
          "v_mfma_f32_16x16x32_bf16 v[60:63], v[44:47], v[92:95], v[60:63]\n\tbuffer_load_dwordx4 v[92:95], %[vo1], s[44:47], %[pg0] offen offset:3072 sc1\n\ts_waitcnt vmcnt(15) lgkmcnt(2)\n\tv_mfma_f32_16x16x32_bf16 v[48:51], a[192:195], v[96:99], v[48:51]\n\tds_read_b128 v[44:47], %[own] offset:3072\n\tv_mfma_f32_16x16x32_bf16 v[52:55], a[196:199], v[96:99], v[52:55]\n\t" \
          "v_mfma_f32_16x16x32_bf16 v[56:59], v[224:227], v[96:99], v[56:59]\n\tv_mfma_f32_16x16x32_bf16 v[60:63], v[32:35], v[96:99], v[60:63]\n\tbuffer_load_dwordx4 v[96:99], %[vo0], s[44:47], %[pg1] offen sc1\n\ts_waitcnt vmcnt(15) lgkmcnt(2)\n\tv_mfma_f32_16x16x32_bf16 v[48:51], a[200:203], v[100:103], v[48:51]\n\tds_read_b128 v[32:35], %[own] offset:4096\n\t" \
          "v_mfma_f32_16x16x32_bf16 v[52:55], a[204:207], v[100:103], v[52:55]\n\tv_mfma_f32_16x16x32_bf16 v[56:59], v[228:231], v[100:103], v[56:59]\n\tv_mfma_f32_16x16x32_bf16 v[60:63], v[36:39], v[100:103], v[60:63]\n\tbuffer_load_dwordx4 v[100:103], %[vo0], s[44:47], %[pg1] offen offset:1024 sc1\n\ts_waitcnt vmcnt(15) lgkmcnt(2)\n\tv_mfma_f32_16x16x32_bf16 v[48:51], a[208:211], v[104:107], v[48:51]\n\t" \
          "ds_read_b128 v[36:39], %[own] offset:5120\n\tv_mfma_f32_16x16x32_bf16 v[52:55], a[212:215], v[104:107], v[52:55]\n\tv_mfma_f32_16x16x32_bf16 v[56:59], v[232:235], v[104:107], v[56:59]\n\tv_mfma_f32_16x16x32_bf16 v[60:63], v[40:43], v[104:107], v[60:63]\n\tbuffer_load_dwordx4 v[104:107], %[vo0], s[44:47], %[pg1] offen offset:2048 sc1\n\ts_waitcnt vmcnt(15) lgkmcnt(2)\n\t" \
          "v_mfma_f32_16x16x32_bf16 v[48:51], a[216:219], v[108:111], v[48:51]\n\tds_read_b128 v[40:43], %[own] offset:6144\n\tv_mfma_f32_16x16x32_bf16 v[52:55], a[220:223], v[108:111], v[52:55]\n\tv_mfma_f32_16x16x32_bf16 v[56:59], v[236:239], v[108:111], v[56:59]\n\tv_mfma_f32_16x16x32_bf16 v[60:63], v[44:47], v[108:111], v[60:63]\n\tbuffer_load_dwordx4 v[108:111], %[vo0], s[44:47], %[pg1] offen offset:3072 sc1\n\t" \
          "s_waitcnt vmcnt(15) lgkmcnt(2)\n\tv_mfma_f32_16x16x32_bf16 v[48:51], a[224:227], v[112:115], v[48:51]\n\tds_read_b128 v[44:47], %[own] offset:7168\n\tv_mfma_f32_16x16x32_bf16 v[52:55], a[228:231], v[112:115], v[52:55]\n\tv_mfma_f32_16x16x32_bf16 v[56:59], v[240:243], v[112:115], v[56:59]\n\tv_mfma_f32_16x16x32_bf16 v[60:63], v[32:35], v[112:115], v[60:63]\n\t" \
          "buffer_load_dwordx4 v[112:115], %[vo1], s[44:47], %[pg1] offen sc1\n\ts_waitcnt vmcnt(15) lgkmcnt(2)\n\tv_mfma_f32_16x16x32_bf16 v[48:51], a[232:235], v[116:119], v[48:51]\n\tv_mfma_f32_16x16x32_bf16 v[52:55], a[236:239], v[116:119], v[52:55]\n\tv_mfma_f32_16x16x32_bf16 v[56:59], v[244:247], v[116:119], v[56:59]\n\tv_mfma_f32_16x16x32_bf16 v[60:63], v[36:39], v[116:119], v[60:63]\n\t" \
          "buffer_load_dwordx4 v[116:119], %[vo1], s[44:47], %[pg1] offen offset:1024 sc1\n\ts_waitcnt vmcnt(15) lgkmcnt(1)\n\tv_mfma_f32_16x16x32_bf16 v[48:51], a[240:243], v[120:123], v[48:51]\n\tv_mfma_f32_16x16x32_bf16 v[52:55], a[244:247], v[120:123], v[52:55]\n\tv_mfma_f32_16x16x32_bf16 v[56:59], v[248:251], v[120:123], v[56:59]\n\tv_mfma_f32_16x16x32_bf16 v[60:63], v[40:43], v[120:123], v[60:63]\n\t" \
          "buffer_load_dwordx4 v[120:123], %[vo1], s[44:47], %[pg1] offen offset:2048 sc1\n\ts_waitcnt vmcnt(15) lgkmcnt(0)\n\tv_mfma_f32_16x16x32_bf16 v[48:51], a[248:251], v[124:127], v[48:51]\n\tv_mfma_f32_16x16x32_bf16 v[52:55], a[252:255], v[124:127], v[52:55]\n\tv_mfma_f32_16x16x32_bf16 v[56:59], v[252:255], v[124:127], v[56:59]\n\tv_mfma_f32_16x16x32_bf16 v[60:63], v[44:47], v[124:127], v[60:63]\n\t" \
          "buffer_load_dwordx4 v[124:127], %[vo1], s[44:47], %[pg1] offen offset:3072 sc1\n\t" OU_ST1 "s_nop 6\n\ts_cmp_eq_u32 %[sp], 1\n\ts_cbranch_scc0 Lou_red%=\n\tv_cmp_u_f32 vcc, v48, v48\n\ts_mov_b32 %[sp], 2\n\t" \
          "s_cbranch_vccz Lou_red%=\n\ts_sub_u32 %[j], %[j], %[stp]\n\ts_lshl_b32 %[t], %[stp], 14\n\ts_sub_u32 %[pg0], %[pg0], %[t]\n\ts_sub_u32 %[pg1], %[pg1], %[t]\n\ts_sub_u32 %[pg2], %[pg2], %[t]\n\t" \
          "s_sub_u32 %[pg3], %[pg3], %[t]\n\ts_branch Lou_wait%=\n\tLou_red%=:\n\tds_write_b128 %[rw_], v[48:51] offset:0\n\tds_write_b128 %[rw_], v[52:55] offset:1024\n\tds_write_b128 %[rw_], v[56:59] offset:2048\n\t" \
          "ds_write_b128 %[rw_], v[60:63] offset:3072\n\ts_waitcnt lgkmcnt(0)\n\ts_barrier\n\tds_read_b128 v[32:35], %[rr0] offset:0\n\tds_read_b128 v[36:39], %[rr0] offset:40960\n\tds_read_b128 v[40:43], %[rr1] offset:0\n\t" \
          "ds_read_b128 v[44:47], %[rr1] offset:40960\n\tv_xor_b32 %[rw_], 0x1000, %[rw_]\n\tv_xor_b32 %[rr0], 0x1000, %[rr0]\n\tv_xor_b32 %[rr1], 0x1000, %[rr1]\n\ts_waitcnt lgkmcnt(0)\n\tv_pk_add_f32 v[32:33], v[32:33], v[36:37]\n\t" \
          "v_pk_add_f32 v[34:35], v[34:35], v[38:39]\n\tv_pk_add_f32 v[40:41], v[40:41], v[44:45]\n\tv_pk_add_f32 v[42:43], v[42:43], v[46:47]\n\tv_pk_add_f32 v[32:33], v[32:33], v[40:41]\n\tv_pk_add_f32 v[34:35], v[34:35], v[42:43]\n\ts_nop 0\n\t" \
          "v_cvt_pk_bf16_f32 v44, v32, v33\n\tv_cvt_pk_bf16_f32 v45, v34, v35\n\tbuffer_store_dwordx2 v[44:45], %[so], %[rc], %[oo] offen" OU_AUX "\n\ts_cmp_lg_u32 s46, 0\n\ts_cbranch_scc1 Lou_top%=\n\ts_cmp_lt_u32 %[j], %[je]\n\t" \
          "s_cbranch_scc1 Lou_wait%=\n\ts_waitcnt vmcnt(0)\n\t"
#define OU_CLOB "v30", "v31", "v32", "v33", "v34", "v35", "v36", "v37", "v38", "v39", "v40", "v41", "v42", "v43", "v44", "v45", "v46", "v47", "v48", "v49", "v50", "v51", "v52", "v53", "v54", "v55", "v56", "v57", "v58", "v59", "v60", "v61", "v62", "v63", "v64", "v65", "v66", "v67", "v68", "v69", "v70", "v71", "v72", "v73", "v74", "v75", "v76", "v77", "v78", "v79", "v80", "v81", "v82", "v83", "v84", "v85", "v86", "v87", "v88", "v89", "v90", "v91", "v92", "v93", "v94", "v95", "v96", "v97", "v98", "v99", "v100", "v101", "v102", "v103", "v104", "v105", "v106", "v107", "v108", "v109", "v110", "v111", "v112", "v113", "v114", "v115", "v116", "v117", "v118", "v119", "v120", "v121", "v122", "v123", "v124", "v125", "v126", "v127", "v128", "v129", "v130", "v131", "v132", "v133", "v134", "v135", "v136", "v137", "v138", "v139", "v140", "v141", "v142", "v143", "v144", "v145", "v146", "v147", "v148", "v149", "v150", "v151", "v152", "v153", "v154", "v155", "v156", "v157", "v158", "v159", "v160", "v161", "v162", "v163", "v164", "v165", "v166", "v167", "v168", "v169", "v170", "v171", "v172", "v173", "v174", "v175", "v176", "v177", "v178", "v179", "v180", "v181", "v182", "v183", "v184", "v185", "v186", "v187", "v188", "v189", "v190", "v191", "v192", "v193", "v194", "v195", "v196", "v197", "v198", "v199", "v200", "v201", "v202", "v203", "v204", "v205", "v206", "v207", "v208", "v209", "v210", "v211", "v212", "v213", "v214", "v215", "v216", "v217", "v218", "v219", "v220", "v221", "v222", "v223", "v224", "v225", "v226", "v227", "v228", "v229", "v230", "v231", "v232", "v233", "v234", "v235", "v236", "v237", "v238", "v239", "v240", "v241", "v242", "v243", "v244", "v245", "v246", "v247", "v248", "v249", "v250", "v251", "v252", "v253", "v254", "v255", "a0", "a1", "a2", "a3", "a4", "a5", "a6", "a7", "a8", "a9", "a10", "a11", "a12", "a13", "a14", "a15", "a16", "a17", "a18", "a19", "a20", "a21", "a22", "a23", "a24", "a25", "a26", "a27", "a28", "a29", "a30", "a31", "a32", "a33", "a34", "a35", "a36", "a37", "a38", "a39", "a40", "a41", "a42", "a43", "a44", "a45", "a46", "a47", "a48", "a49", "a50", "a51", "a52", "a53", "a54", "a55", "a56", "a57", "a58", "a59", "a60", "a61", "a62", "a63", "a64", "a65", "a66", "a67", "a68", "a69", "a70", "a71", "a72", "a73", "a74", "a75", "a76", "a77", "a78", "a79", "a80", "a81", "a82", "a83", "a84", "a85", "a86", "a87", "a88", "a89", "a90", "a91", "a92", "a93", "a94", "a95", "a96", "a97", "a98", "a99", "a100", "a101", "a102", "a103", "a104", "a105", "a106", "a107", "a108", "a109", "a110", "a111", "a112", "a113", "a114", "a115", "a116", "a117", "a118", "a119", "a120", "a121", "a122", "a123", "a124", "a125", "a126", "a127", "a128", "a129", "a130", "a131", "a132", "a133", "a134", "a135", "a136", "a137", "a138", "a139", "a140", "a141", "a142", "a143", "a144", "a145", "a146", "a147", "a148", "a149", "a150", "a151", "a152", "a153", "a154", "a155", "a156", "a157", "a158", "a159", "a160", "a161", "a162", "a163", "a164", "a165", "a166", "a167", "a168", "a169", "a170", "a171", "a172", "a173", "a174", "a175", "a176", "a177", "a178", "a179", "a180", "a181", "a182", "a183", "a184", "a185", "a186", "a187", "a188", "a189", "a190", "a191", "a192", "a193", "a194", "a195", "a196", "a197", "a198", "a199", "a200", "a201", "a202", "a203", "a204", "a205", "a206", "a207", "a208", "a209", "a210", "a211", "a212", "a213", "a214", "a215", "a216", "a217", "a218", "a219", "a220", "a221", "a222", "a223", "a224", "a225", "a226", "a227", "a228", "a229", "a230", "a231", "a232", "a233", "a234", "a235", "a236", "a237", "a238", "a239", "a240", "a241", "a242", "a243", "a244", "a245", "a246", "a247", "a248", "a249", "a250", "a251", "a252", "a253", "a254", "a255", "s40", "s41", "s42", "s43", "s44", "s45", "s46", "s47"
__device__ __forceinline__ void out_unit(const FParams& F, unsigned long long parq, int set, int un, int w) {
  const int lane = opaque_lane(), l15 = lane & 15, lq = lane >> 4, r = un & 7;
  const __amdgpu_buffer_rsrc_t rw = __builtin_amdgcn_make_buffer_rsrc(wg::uni(F.Wo + 64 * un), 0, HDA * DM * 2 - 128 * un, 0x00020000);
  const __amdgpu_buffer_rsrc_t rc = __builtin_amdgcn_make_buffer_rsrc(wg::uni(F.out + 64 * un), 0, S_ * 2 * DM * 2 - 128 * un, 0x00020000);
  const __amdgpu_buffer_rsrc_t rf = __builtin_amdgcn_make_buffer_rsrc(F.ctl, 0, sizeof(Ctl), 0x00020000);
  const int own = w * 40960 + lane * 16;
  const int jx = 2 * TP(F.x3, XSEG0);
  int j = set == 4 ? 2 * OSEG0 : jx + (set < 2 ? 0 : TP(F.oj1, OJ1)) + set, av = j >> 1, pg[4], t, cnt, oo, sp = 0, stp, rw_ = own + 32768, rr0 = 32768 + (l15 >> 2) * 1024 + ((l15 & 3) * 16 + 4 * w + lq) * 16, rr1 = rr0 + 81920;
#pragma unroll
  for (int i = 0; i < 4; ++i) pg[i] = __builtin_amdgcn_readfirstlane(qoff(parq, 4 * w + (i ^ (r >> 1))) + j * QBLK);
  const int rt = w * 40960 + (8 * lq + (l15 >> 2)) * 128 + (((l15 >> 3) | ((lq & 1) << 1)) << 5) + (l15 & 3) * 8, vw = (lane >> 3) * 2048 + (((lane & 7) ^ ((lq & 1) << 1)) << 4);
  asm volatile(OU_BODY
    : [j] "+s"(j), [av] "+s"(av), [pg0] "+s"(pg[0]), [pg1] "+s"(pg[1]), [pg2] "+s"(pg[2]), [pg3] "+s"(pg[3]), [t] "=&s"(t), [cnt] "=&s"(cnt), [oo] "=&s"(oo), [sp] "+s"(sp), [stp] "=&s"(stp), [rw_] "+v"(rw_), [rr0] "+v"(rr0), [rr1] "+v"(rr1) OU_STO
    : [vo0] "v"(lane * 16 + (r & 1) * 4096), [vo1] "v"(lane * 16 + (~r & 1) * 4096), [vw0] "v"(vw), [vw1] "v"(vw ^ 64), [rt0] "v"(rt), [rt1] "v"(rt ^ 32), [rt2] "v"(rt ^ 64), [rt3] "v"(rt ^ 96), [own] "v"(own),
      [vf] "v"(lane < 8 ? (lane >> 1) * 1024 + (2 * w + (lane & 1)) * 128 : 0x7ffffff0), [so] "v"((4 * w + lq) * 4096 + l15 * 8),
      [qa] "s"(F.qkvA), [qs] "s"(QSIZE), [rw] "s"(rw), [rc] "s"(rc), [rf] "s"(rf), [wl] "s"(w * 40960), [wo] "s"((1024 * w + 128 * r) * 2048), [fo] "s"((int)offsetof(Ctl, flagO)), [tmo] "s"((int)offsetof(Ctl, tmo)), [j1] "s"(jx + TP(F.oj1, OJ1)), [sa] "s"(set == 4 ? 1 : 2), [je] "s"(set == 4 ? jx : 128)
    : OU_CLOB, "vcc", "scc", "memory");
}

__device__ __forceinline__ void attnB_task(const FParams& F, unsigned long long parq, int r, int pg, u16* vs  , int w) {
  const int lane = opaque_lane(), l15 = lane & 15, lq = lane >> 4;
  const __amdgpu_buffer_rsrc_t rs = __builtin_amdgcn_make_buffer_rsrc(F.qkvA, 0, QSIZE, 0x00020000);
  const int so = 8 * r * QBLK + w * 2048, oq = so + qoff(parq, 48 + pg), ok = so + qoff(parq, 52 + pg), ov = so + qoff(parq, 56 + pg), p0 = r ? -QBLK : 0;
  v4u q[2][8], k[2][9], v[2][9];
#pragma unroll
  for (int hh = 0; hh < 2; ++hh) {
#pragma unroll
    for (int i = 0; i < 9; ++i) v[hh][i] = __builtin_amdgcn_raw_buffer_load_b128(rs, lane * 16 + hh * 1024, ov + (i ? (i - 1) * QBLK : p0), 0);
#pragma unroll
    for (int i = 0; i < 9; ++i) { k[hh][i] = __builtin_amdgcn_raw_buffer_load_b128(rs, lane * 16 + hh * 1024, ok + (i ? (i - 1) * QBLK : p0), 0); if (i < 8) q[hh][i] = __builtin_amdgcn_raw_buffer_load_b128(rs, lane * 16 + hh * 1024, oq + i * QBLK, 0); }
  }
  const float scale = 0.17677669529663687f, L2E = 1.4426950408889634f;
  const bool par = l15 & 1, ownA = l15 - 4 * lq - par >= 0, ownB = l15 - 4 * lq - par - 2 >= 0;
  const unsigned sa = par ? 0x01000c0cu : 0x0c0c0100u, sb = par ? 0x03020c0cu : 0x0c0c0302u, sz = 0x0c0c0c0cu;
  const unsigned sel[4] = {ownA ? sz : sa, ownB ? sz : sb, ownA ? sa : sz, ownB ? sb : sz};
  const u16* vp = &vs[(4 * lq + (l15 >> 2)) * 40 + 16 * (l15 & 1) + 2 * (l15 & 2)];
#pragma unroll
  for (int hh = 0; hh < 2; ++hh) {
    SB0;
#pragma unroll
    for (int i = 0; i < 9; ++i) *(v4u*)&vs[(16 * i + l15) * 40 + 8 * lq] = v[hh][i];
#pragma unroll
    for (int i = 0; i < 8; ++i) {
      const f32x4 z = {0.f, 0.f, 0.f, 0.f}, s0 = mf(k[hh][i], q[hh][i], z), s1 = mf(k[hh][i + 1], q[hh][i], z);
      float a = ownA ? (par ? s1[1] : s1[0]) : (par ? s0[1] : s0[0]), b = ownB ? (par ? s1[3] : s1[2]) : (par ? s0[3] : s0[2]);
      unsigned p = pk2(a, b);
      p = pk2(__uint_as_float(p << 16) * scale, __uint_as_float(p & 0xffff0000u) * scale);
      a = __uint_as_float(p << 16); b = __uint_as_float(p & 0xffff0000u);
      const bool va = i > 0 || r > 0 || ownA, vb = i > 0 || r > 0 || ownB;
      const float mx = xmax32(xmax16(fmaxf(va ? a : -INFINITY, vb ? b : -INFINITY)));
      a = va ? __builtin_amdgcn_exp2f((a - mx) * L2E) : 0.f; b = vb ? __builtin_amdgcn_exp2f((b - mx) * L2E) : 0.f;
      const float inv = __builtin_amdgcn_rcpf(xsum32(xsum16(a + b)));
      p = pk2(a * inv, b * inv);
      const v4u px = {__builtin_amdgcn_perm(0u, p, sel[0]), __builtin_amdgcn_perm(0u, p, sel[1]), __builtin_amdgcn_perm(0u, p, sel[2]), __builtin_amdgcn_perm(0u, p, sel[3])};
      f32x4 o[2];
#pragma unroll
      for (int ab = 0; ab < 2; ++ab) { const v2u lo = trrd(vp + 16 * i * 40 + 8 * ab), hi = trrd(vp + 16 * (i + 1) * 40 + 8 * ab); o[ab] = mf((v4u){lo.x, lo.y, hi.x, hi.y}, px, z); }
      __builtin_amdgcn_raw_buffer_store_b128((v4u){pk2(o[0][0], o[0][1]), pk2(o[0][2], o[0][3]), pk2(o[1][0], o[1][1]), pk2(o[1][2], o[1][3])}, rs, lane * 16 + hh * 1024, oq + i * QBLK, 16);
    }
  }
}

constexpr int NTASK = 224;
__constant__ unsigned short TASKS[NTASK] = {
  5120, 5121, 5122, 5123, 5124, 5125, 5126, 5127, 5128, 5129, 5130, 5131, 5132, 5133, 5134, 5135, 5136, 5137, 5138, 5139, 5140, 5141, 5142, 5143,
  3072, 3073, 3074, 3075, 3076, 3077, 3078, 3079, 3080, 3081, 3082, 3083, 3084, 3085, 3086, 3087, 5144, 5145, 5146, 5147, 5148, 5149, 5150, 5151,
  5160, 5161, 5162, 5163, 5164, 5165, 5166, 5167, 4096, 4097, 4098, 4099, 4100, 4101, 4102, 4103, 4104, 4105, 4106, 4107, 4108, 4109, 4110, 4111,
  5168, 5169, 5170, 5171, 5172, 5173, 5174, 5175, 5152, 5153, 5154, 5155, 5156, 5157, 5158, 5159, 5176, 5177, 5178, 5179, 5180, 5181, 5182, 5183,
  3088, 3089, 3090, 3091, 3092, 3093, 3094, 3095, 3096, 3097, 3098, 3099, 3100, 3101, 3102, 3103, 7168, 7169, 7170, 7171, 7172, 7173, 7174, 7175,
  7176, 7177, 7178, 7179, 7180, 7181, 7182, 7183, 7184, 7185, 7186, 7187, 7188, 7189, 7190, 7191, 7192, 7193, 7194, 7195, 7196, 7197, 7198, 7199,
  4112, 4113, 4114, 4115, 4116, 4117, 4118, 4119, 4120, 4121, 4122, 4123, 4124, 4125, 4126, 4127, 3104, 3105, 3106, 3107, 3108, 3109, 3110, 3111,
  3112, 3113, 3114, 3115, 3116, 3117, 3118, 3119, 4128, 4129, 4130, 4131, 4132, 4133, 4134, 4135, 4136, 4137, 4138, 4139, 4140, 4141, 4142, 4143,
  3120, 3121, 3122, 3123, 3124, 3125, 3126, 3127, 3128, 3129, 3130, 3131, 3132, 3133, 3134, 3135, 4144, 4145, 4146, 4147, 4148, 4149, 4150, 4151,
  4152, 4153, 4154, 4155, 4156, 4157, 4158, 4159,
};
__device__ __forceinline__ const FParams& args() {
  typedef const __attribute__((address_space(4))) FParams* KP;
  KP p = (KP)__builtin_amdgcn_kernarg_segment_ptr(); asm volatile("" : "+s"(p)); return *(const FParams*)p;
}
__device__ __forceinline__ void wait_until(unsigned t) { while ((int)(t - (unsigned)__builtin_amdgcn_s_memrealtime()) > 0) __builtin_amdgcn_s_sleep(8); }
template <int AUX = 0>
__device__ __forceinline__ void touch(const void* base, int bytes, int voff, int soff, char* lds) {
  __builtin_amdgcn_raw_ptr_buffer_load_lds(__builtin_amdgcn_make_buffer_rsrc((void*)base, 0, bytes, 0x00020000), (__attribute__((address_space(3))) void*)(lds + wg::LDS_USE + 1024), 4, voff, soff, 0, AUX);
}
constexpr int RECO = 32768;
__device__ __forceinline__ void setup(const FParams& F, int n, int w) {
  const int lane = opaque_lane(), tid = 64 * w + lane;
  const v4u ff = {0xffffffffu, 0xffffffffu, 0xffffffffu, 0xffffffffu}, ep = rdy_load(F);
  const __amdgpu_buffer_rsrc_t rs = __builtin_amdgcn_make_buffer_rsrc(F.ring, 0, QPAGE + QSIZE, 0x00020000);
  const int o = ((lane & 16) ? (n < 8 ? (1 + NPAGE + n) * QPAGE : RSIZE + n * 128) : (1 + n) * QPAGE) + ((lane >> 3) & 1) * 8192 + (lane & 7) * 16;
  __builtin_amdgcn_raw_buffer_store_b128(ff, rs, tid < 32 ? o : 0x7ffffff0, 0, 16);
#pragma unroll 2
  for (int i = n * 256 + tid; i < RSIZE / 16; i += NPAGE * 256) {
    v4u v = ff;
    const int m = i - ROFF_M / 16;
    if ((unsigned)m < 2048u) {
      const int q = (m >> 10) * 512 + (m & 511);
      const u16* src = F.mem0 + (((q >> 4) & 1) * 4 + ((q >> 1) & 3)) * DM + 32 * (q >> 5) + 8 * ((q >> 3) & 1) + 4 * (q & 1);
#pragma unroll
      for (int p = 0; p < 4; ++p) v[p] = nosent(src[p] | ((unsigned)src[p + 16] << 16));
    }
    __builtin_amdgcn_raw_buffer_store_b128(v, rs, i * 16, 0, 16);
  }
  __builtin_amdgcn_raw_buffer_store_b128((v4u){0u, 0u, 0u, 0u}, __builtin_amdgcn_make_buffer_rsrc(F.ctl, 0, sizeof(Ctl), 0x00020000), w >= 2 ? (n * 128 + tid - 128) * 16 : 0x7ffffff0, 0, 16);
  static_assert(sizeof(Ctl) <= NPAGE * 128 * 16, "");
  const unsigned long long tag = rdy_tag(ep);
  asm volatile("s_waitcnt vmcnt(0)" ::: "memory");
  __syncthreads();
  if (w == 0) {
    unsigned best[4] = {~0u, ~0u, ~0u, ~0u};
#pragma unroll
    for (int g = 0; g < 4; g += 2)
#pragma unroll 1
      for (int r = 0; r < TP(F.probes, PROBES); ++r) {
#pragma unroll
        for (int t = g; t < g + 2; ++t) {
          const unsigned long long t0 = __builtin_amdgcn_s_memtime();
          __builtin_amdgcn_raw_buffer_store_b128(ff, rs, (lane >> 3) == t ? o : 0x7ffffff0, 0, 16);
          asm volatile("s_waitcnt vmcnt(0)" ::: "memory");
          best[t] = min(best[t], (unsigned)(__builtin_amdgcn_s_memtime() - t0));
        }
        if (min(best[g], best[g + 1]) < TP(F.sh2, PTH) && max(best[g], best[g + 1]) > min(best[g], best[g + 1]) + 120) break;
      }
    unsigned xcc; asm volatile("s_getreg_b32 %0, hwreg(HW_REG_XCC_ID)" : "=s"(xcc));
    const bool flip = ((xcc ^ (xcc - blockIdx.x + 1)) >> 2) & 1;
    const unsigned bits = ((best[1] < best[0]) == flip ? 1 : 0) | (((best[3] < best[2]) != flip) != (n < 8) ? 4 : 0);
    __builtin_amdgcn_raw_buffer_store_b128((v4u){(unsigned)tag, (unsigned)(tag >> 32), bits, 0u}, __builtin_amdgcn_make_buffer_rsrc(F.rdy, 0, RDY_SIZE, 0x00020000), lane < 8 ? n * 128 + lane * 16 : 0x7ffffff0, 0, 16);
  }
}
__device__ __forceinline__ void worker(const FParams& F, unsigned cz, char* lds, const int w0) {
#define lane opaque_lane()
#define tid (64 * w + opaque_lane())
  const int grp = (blockIdx.x & 7) - 3, slot = blockIdx.x >> 3;
#define TLOG(t, e)
  unsigned long long par, tag; unsigned pk;
  { int w = w0; v4u rec;
    const unsigned t0 = (unsigned)__builtin_amdgcn_s_memrealtime();
    const int id = slot * 4 + w, tz = opaque_lane();
    if (grp != 2) {
      if (grp > 2) { if (slot < 30) setup(F, (grp - 3) * 30 + slot, w); TLOG(1024 + grp * 32 + slot, 0); wait_until(t0 + TP(F.stag, STAG)); }
      rec = panel_task<NTP>(F, (grp < 2 ? grp + 1 : 0) * 32 + slot, (grp - (grp > 2)) * 32 + slot, grp == 4 ? 64 : 0, grp == 3 ? 64 : 128, w);
    } else {
#pragma unroll
      for (int q = 0; q < 4; ++q) {
        const u16* base = q == 0 ? F.Wk : (q == 1 ? F.Wo : (q == 2 ? F.Wq : F.Wv));
        const int o3 = (384 - (int)(size_t)base) & 896, o63 = (8064 - (int)(size_t)base) & 8064;
        touch(base, DM * HDA * 2, (tz * 128 + id) * 1024 + o3, 0, lds);
        if (!(q & 1)) { touch(base, DM * HDA * 2, (tz * 128 + id) * 1024 + (o3 ^ 128), 0, lds); touch(base, DM * HDA * 2, tz < 16 ? ((tz >> 1) * 128 + id) * 8192 + (o63 ^ ((tz & 1) << 7)) : 0x7ffffff0, 0, lds); }
      }
#pragma unroll 1
      for (int q = id; q < 768; q += 128) {
        wait_until(t0 + (TP(F.px, PXV) >> 16) + ((q * (TP(F.px, PXV) & 0xffff)) >> 6));
        if (q < 512) touch(F.Ax, S_ * DM * 2, tz * 128, ((q & 1) * 256 + (q >> 1)) * 8192, lds); else touch(F.woB, DB * DB * 2, tz * 128, (q - 512) * 8192, lds);
      }
      if (slot < 28) wait_until(t0 + (TP(F.pb, PBV) >> 16));
      rec = rdy_load(F);
      asm volatile("s_waitcnt vmcnt(0)" ::: "memory");
      __syncthreads();
    }
    asm volatile("" :: "v"(cz));
    par = rdy_wait(F, rec, tag, pk);
    if (grp == 2 || grp == 3) {
      const FParams& F = args();
      asm volatile("" : "+s"(w));
      const int j0 = grp == 2 ? 0 : TP(F.jb, JB), j1 = grp == 2 ? TP(F.jb, JB) : 128;
      if (slot < 24) (void)panel_task<true>(F, 96 + slot, 64 + grp * 32 + slot, j0, j1, w);
      else if (slot < 28) {
        const int id = (slot - 24) * 4 + w, tz = opaque_lane();
        int seen = j0;
#pragma unroll 1
        for (int j = j0 + (id >> 2); j < j1; j += 4) {
          for (unsigned spins = 0; j > seen + 16; ++spins) {
            seen = max(seen, __builtin_amdgcn_readfirstlane(ld_agent(&F.ctl->prg[64 + grp * 32 + id][0])));
            if (spin_fail(spins, &F.ctl->tmo[0], 2u)) break;
            __builtin_amdgcn_s_sleep(32);
          }
          touch<2>(F.Bx, S_ * DB * 2, tz * 128, (j * 4 + (id & 3)) * 8192, lds);
          touch(F.Bx, S_ * DB * 2, tz * 128 + 64, (j * 4 + (id & 3)) * 8192, lds);
        }
        asm volatile("s_waitcnt vmcnt(0)" ::: "memory");
      }
    }
    check_in(F, tag, w);
    TLOG(1024 + grp * 32 + slot, 1);
    if (grp < 2 || (grp == 4 && slot < 16)) {
      const FParams& F = args();
      asm volatile("" : "+s"(w));
      out_unit(F, par, grp == 4 ? 4 : 2 * grp + (slot >> 4), slot & 15, w);
      TLOG(1280 + grp * 32 + slot, 1);
      return;
    } }
  Ctl* ctl = F.ctl;
  int* sh = (int*)(lds + wg::LDS_USE);
  int next = -1;
  for (;;) {
    const FParams& F = args();
    int w = w0; asm volatile("" : "+s"(w));
    if (next < 0) {
      if (tid == 0) sh[0] = (int)__hip_atomic_fetch_add(&ctl->qhead[0], 1u, RLX_AGENT);
      __syncthreads();
      const int t = __builtin_amdgcn_readfirstlane(sh[0]);
      __syncthreads();
      { if (t >= NTASK) return; next = TASKS[t]; }
    }
    const int code = __builtin_amdgcn_readfirstlane(next); next = -1;
    const int tk = code >> 10, u = code & 1023;
    const u16 *A = nullptr, *Bt = nullptr; void* C = nullptr; int lda = 0, ldb = 0, ldc = 0, nk = 32, r = u >> 2, n = u & 3, nsig = 1;
    const unsigned* wp = nullptr; unsigned need = 0; unsigned* sig = nullptr;
    if (tk == 3) {
      const int lz = opaque_lane(); const bool hi = 8 * r >= TP(F.jb, JB);
      need = lz < 8 ? 8 * r + 8 : TP(F.jb, JB); sig = &ctl->cntAB[r][0];
      if ((lz & 7) < 6 && lz < (hi ? 16 : 8)) wp = &ctl->prg[(lz < 8 && hi ? 160 : 128) + ((lz & 7) >> 1) * 8 + 2 * n + (lz & 1)][0];
    } else if (tk == 4) {
      A = F.qkvA + r * 4 * QBLK + 24 * QPAGE; lda = QBLK / 32; Bt = F.woB + n * 256; ldb = DB;
      C = F.out + (size_t)r * 128 * 2 * DM + DM + n * 256; ldc = 2 * DM; need = 4; if (lane == 0) wp = &ctl->cntAB[r][0];
    } else if (tk == 5) {
      r = u >> 3; n = u & 7; sig = &ctl->flagO[4 * r][n][0]; nsig = 4; need = 8 * r + 8;
      const int lz = opaque_lane(); if (lz < 12) wp = &ctl->prg[((lz >> 2) + (lz >= 8 && r >= 8)) * 32 + 4 * n + (lz & 3)][0];
    } else if (tk == 6) {
      A = F.qkvA + r * 4 * QBLK; lda = QBLK / 32; Bt = F.Wo + n * 256; ldb = DM; nk = HDA / 32;
      C = F.out + (size_t)r * 128 * 2 * DM + n * 256; ldc = 2 * DM; need = 1; { const int lz = opaque_lane(); if (lz < 8) wp = &ctl->flagO[4 * r][lz][0]; }
    } else {
      attn_stream(F, par, pk, u >> 3, u & 7, lds, w);
      TLOG(lastt, 1);
      return;
    }
    wg_wait(wp, need, ctl, w);
    if (tk == 5) {
      attnA_block(F, par, pk, r, 4 * n + w, (u16*)(lds + w * 16384), opaque_lane(), w, (unsigned*)sh + 4);
      asm volatile("s_waitcnt vmcnt(0)" ::: "memory");
      __syncthreads();
    } else if (tk == 3) {
      attnB_task(F, par, r, n, (u16*)(lds + w * 16384), w);
      asm volatile("s_waitcnt vmcnt(0)" ::: "memory");
      __syncthreads();
    } else wg::gemm_tile<128>(A, lda, Bt, ldb, nk, C, ldc, par >> (tk == 4 ? 48 : 0), lds, w);
    if (sig && tid < nsig) __hip_atomic_fetch_add(sig + tid * 256, 1u, RLX_AGENT);
  }
}

#undef lane
#undef tid
__device__ __forceinline__ unsigned rtnow() { return (unsigned)__builtin_amdgcn_s_memrealtime(); }
#define RBASE(s) (((s) & 3) * RSLOT + rpl)
constexpr int ALM = RECO + 1024;
__device__ __forceinline__ float rowmax16(float f) { f = fmaxf(f, dpp_ror<8>(f)); f = fmaxf(f, dpp_ror<4>(f)); f = fmaxf(f, dpp_ror<2>(f)); return fmaxf(f, dpp_ror<1>(f)); }
__device__ __forceinline__ bool al_eval(unsigned v, unsigned& t) {
  const bool set = (v >> 16) == 1u;
  if (__builtin_expect(((unsigned)__ballot(set) & 0xffffu) == 0, 1)) return false;
  const int d = (short)(v - t);
  t += __builtin_amdgcn_readfirstlane((int)rowmax16(set && d > 0 && d < 1000 ? (float)d : 0.f));
  return true;
}
__device__ __forceinline__ void rec_get(unsigned& recA, unsigned& recD, __amdgpu_buffer_rsrc_t rs, int off, unsigned* tmo) {
#pragma unroll 1
  for (unsigned fails = 0;; ++fails) {
    const v2u rec = __builtin_amdgcn_raw_buffer_load_b64(rs, off, 0, 16);
    recA = __builtin_amdgcn_readfirstlane(rec.x); recD = __builtin_amdgcn_readfirstlane(rec.y);
    if ((recA != 0xffffffffu && recD != 0xffffffffu) || spin_fail(fails, tmo, 1u)) break;
    __builtin_amdgcn_s_sleep(1);
  }
}

#define QK_LOADER \
          "s_nop 4\n\t" \
          "s_add_u32 m0, %[lw], 0\n\ts_xor_b32 %[t], %[kb], 0x0\n\tbuffer_load_dwordx4 %[go], %[rk], %[t] offen" QK_NTk " lds\n\ts_add_u32 m0, %[lw], 1024\n\ts_xor_b32 %[t], %[kb], 0x10000\n\tbuffer_load_dwordx4 %[go], %[rk], %[t] offen" QK_NTk " lds\n\ts_add_u32 m0, %[lw], 2048\n\ts_xor_b32 %[t], %[kb], 0x20000\n\tbuffer_load_dwordx4 %[go], %[rk], %[t] offen" QK_NTk " lds\n\ts_add_u32 m0, %[lw], 3072\n\ts_xor_b32 %[t], %[kb], 0x30000\n\tbuffer_load_dwordx4 %[go], %[rk], %[t] offen" QK_NTk " lds\n\ts_add_u32 m0, %[lw], 4096\n\ts_xor_b32 %[t], %[kb], 0x40000\n\tbuffer_load_dwordx4 %[go], %[rk], %[t] offen" QK_NTk " lds\n\ts_add_u32 m0, %[lw], 5120\n\ts_xor_b32 %[t], %[kb], 0x50000\n\tbuffer_load_dwordx4 %[go], %[rk], %[t] offen" QK_NTk " lds\n\ts_add_u32 m0, %[lw], 6144\n\ts_xor_b32 %[t], %[kb], 0x60000\n\tbuffer_load_dwordx4 %[go], %[rk], %[t] offen" QK_NTk " lds\n\ts_add_u32 m0, %[lw], 7168\n\ts_xor_b32 %[t], %[kb], 0x70000\n\tbuffer_load_dwordx4 %[go], %[rk], %[t] offen" QK_NTk " lds\n\t" \
          "s_add_u32 m0, %[lw], 8192\n\ts_xor_b32 %[t], %[kb], 0x80000\n\tbuffer_load_dwordx4 %[go], %[rk], %[t] offen" QK_NTk " lds\n\ts_add_u32 m0, %[lw], 9216\n\ts_xor_b32 %[t], %[kb], 0x90000\n\tbuffer_load_dwordx4 %[go], %[rk], %[t] offen" QK_NTk " lds\n\ts_add_u32 m0, %[lw], 10240\n\ts_xor_b32 %[t], %[kb], 0xa0000\n\tbuffer_load_dwordx4 %[go], %[rk], %[t] offen" QK_NTk " lds\n\ts_add_u32 m0, %[lw], 11264\n\ts_xor_b32 %[t], %[kb], 0xb0000\n\tbuffer_load_dwordx4 %[go], %[rk], %[t] offen" QK_NTk " lds\n\ts_add_u32 m0, %[lw], 12288\n\ts_xor_b32 %[t], %[kb], 0xc0000\n\tbuffer_load_dwordx4 %[go], %[rk], %[t] offen" QK_NTk " lds\n\ts_add_u32 m0, %[lw], 13312\n\ts_xor_b32 %[t], %[kb], 0xd0000\n\tbuffer_load_dwordx4 %[go], %[rk], %[t] offen" QK_NTk " lds\n\ts_add_u32 m0, %[lw], 14336\n\ts_xor_b32 %[t], %[kb], 0xe0000\n\tbuffer_load_dwordx4 %[go], %[rk], %[t] offen" QK_NTk " lds\n\ts_add_u32 m0, %[lw], 15360\n\ts_xor_b32 %[t], %[kb], 0xf0000\n\tbuffer_load_dwordx4 %[go], %[rk], %[t] offen" QK_NTk " lds\n\t" \
          "s_add_u32 m0, %[lw], 16384\n\ts_xor_b32 %[t], %[kb], 0x100000\n\tbuffer_load_dwordx4 %[go], %[rk], %[t] offen" QK_NTk " lds\n\ts_add_u32 m0, %[lw], 17408\n\ts_xor_b32 %[t], %[kb], 0x110000\n\tbuffer_load_dwordx4 %[go], %[rk], %[t] offen" QK_NTk " lds\n\ts_add_u32 m0, %[lw], 18432\n\ts_xor_b32 %[t], %[kb], 0x120000\n\tbuffer_load_dwordx4 %[go], %[rk], %[t] offen" QK_NTk " lds\n\ts_add_u32 m0, %[lw], 19456\n\ts_xor_b32 %[t], %[kb], 0x130000\n\tbuffer_load_dwordx4 %[go], %[rk], %[t] offen" QK_NTk " lds\n\ts_add_u32 m0, %[lw], 20480\n\ts_xor_b32 %[t], %[kb], 0x140000\n\tbuffer_load_dwordx4 %[go], %[rk], %[t] offen" QK_NTk " lds\n\ts_add_u32 m0, %[lw], 21504\n\ts_xor_b32 %[t], %[kb], 0x150000\n\tbuffer_load_dwordx4 %[go], %[rk], %[t] offen" QK_NTk " lds\n\ts_add_u32 m0, %[lw], 22528\n\ts_xor_b32 %[t], %[kb], 0x160000\n\tbuffer_load_dwordx4 %[go], %[rk], %[t] offen" QK_NTk " lds\n\ts_add_u32 m0, %[lw], 23552\n\ts_xor_b32 %[t], %[kb], 0x170000\n\tbuffer_load_dwordx4 %[go], %[rk], %[t] offen" QK_NTk " lds\n\t" \
          "s_add_u32 m0, %[lw], 24576\n\ts_xor_b32 %[t], %[kb], 0x180000\n\tbuffer_load_dwordx4 %[go], %[rk], %[t] offen" QK_NTk " lds\n\ts_add_u32 m0, %[lw], 25600\n\ts_xor_b32 %[t], %[kb], 0x190000\n\tbuffer_load_dwordx4 %[go], %[rk], %[t] offen" QK_NTk " lds\n\ts_add_u32 m0, %[lw], 26624\n\ts_xor_b32 %[t], %[kb], 0x1a0000\n\tbuffer_load_dwordx4 %[go], %[rk], %[t] offen" QK_NTk " lds\n\ts_add_u32 m0, %[lw], 27648\n\ts_xor_b32 %[t], %[kb], 0x1b0000\n\tbuffer_load_dwordx4 %[go], %[rk], %[t] offen" QK_NTk " lds\n\ts_add_u32 m0, %[lw], 28672\n\ts_xor_b32 %[t], %[kb], 0x1c0000\n\tbuffer_load_dwordx4 %[go], %[rk], %[t] offen" QK_NTk " lds\n\ts_add_u32 m0, %[lw], 29696\n\ts_xor_b32 %[t], %[kb], 0x1d0000\n\tbuffer_load_dwordx4 %[go], %[rk], %[t] offen" QK_NTk " lds\n\ts_add_u32 m0, %[lw], 30720\n\ts_xor_b32 %[t], %[kb], 0x1e0000\n\tbuffer_load_dwordx4 %[go], %[rk], %[t] offen" QK_NTk " lds\n\ts_add_u32 m0, %[lw], 31744\n\ts_xor_b32 %[t], %[kb], 0x1f0000\n\tbuffer_load_dwordx4 %[go], %[rk], %[t] offen" QK_NTk " lds\n\t" \
          "s_add_u32 m0, %[lw], 32768\n\ts_xor_b32 %[t], %[kb], 0x200000\n\tbuffer_load_dwordx4 %[go], %[rk], %[t] offen" QK_NTk " lds\n\ts_add_u32 m0, %[lw], 33792\n\ts_xor_b32 %[t], %[kb], 0x210000\n\tbuffer_load_dwordx4 %[go], %[rk], %[t] offen" QK_NTk " lds\n\ts_add_u32 m0, %[lw], 34816\n\ts_xor_b32 %[t], %[kb], 0x220000\n\tbuffer_load_dwordx4 %[go], %[rk], %[t] offen" QK_NTk " lds\n\ts_add_u32 m0, %[lw], 35840\n\ts_xor_b32 %[t], %[kb], 0x230000\n\tbuffer_load_dwordx4 %[go], %[rk], %[t] offen" QK_NTk " lds\n\ts_add_u32 m0, %[lw], 36864\n\ts_xor_b32 %[t], %[kb], 0x240000\n\tbuffer_load_dwordx4 %[go], %[rk], %[t] offen" QK_NTk " lds\n\ts_add_u32 m0, %[lw], 37888\n\ts_xor_b32 %[t], %[kb], 0x250000\n\tbuffer_load_dwordx4 %[go], %[rk], %[t] offen" QK_NTk " lds\n\ts_add_u32 m0, %[lw], 38912\n\ts_xor_b32 %[t], %[kb], 0x260000\n\tbuffer_load_dwordx4 %[go], %[rk], %[t] offen" QK_NTk " lds\n\ts_add_u32 m0, %[lw], 39936\n\ts_xor_b32 %[t], %[kb], 0x270000\n\tbuffer_load_dwordx4 %[go], %[rk], %[t] offen" QK_NTk " lds\n\t" \
          "s_waitcnt vmcnt(32)\n\tds_read_b64_tr_b16 v[136:137], %[rd0] offset:0\n\tds_read_b64_tr_b16 v[138:139], %[rd0] offset:128\n\tds_read_b64_tr_b16 v[140:141], %[rd0] offset:4096\n\tds_read_b64_tr_b16 v[142:143], %[rd0] offset:4224\n\tds_read_b64_tr_b16 a[64:65], %[rd1] offset:0\n\tds_read_b64_tr_b16 a[66:67], %[rd1] offset:128\n\tds_read_b64_tr_b16 a[68:69], %[rd1] offset:4096\n\tds_read_b64_tr_b16 a[70:71], %[rd1] offset:4224\n\tds_read_b64_tr_b16 a[128:129], %[rd2] offset:0\n\tds_read_b64_tr_b16 a[130:131], %[rd2] offset:128\n\tds_read_b64_tr_b16 a[132:133], %[rd2] offset:4096\n\tds_read_b64_tr_b16 a[134:135], %[rd2] offset:4224\n\tds_read_b64_tr_b16 a[136:137], %[rd3] offset:0\n\tds_read_b64_tr_b16 a[138:139], %[rd3] offset:128\n\tds_read_b64_tr_b16 a[140:141], %[rd3] offset:4096\n\tds_read_b64_tr_b16 a[142:143], %[rd3] offset:4224\n\ts_waitcnt lgkmcnt(0)\n\t" \
          "s_add_u32 m0, %[lw], 0\n\ts_xor_b32 %[t], %[kb], 0x280000\n\tbuffer_load_dwordx4 %[go], %[rk], %[t] offen" QK_NTk " lds\n\ts_add_u32 m0, %[lw], 1024\n\ts_xor_b32 %[t], %[kb], 0x290000\n\tbuffer_load_dwordx4 %[go], %[rk], %[t] offen" QK_NTk " lds\n\ts_add_u32 m0, %[lw], 2048\n\ts_xor_b32 %[t], %[kb], 0x2a0000\n\tbuffer_load_dwordx4 %[go], %[rk], %[t] offen" QK_NTk " lds\n\ts_add_u32 m0, %[lw], 3072\n\ts_xor_b32 %[t], %[kb], 0x2b0000\n\tbuffer_load_dwordx4 %[go], %[rk], %[t] offen" QK_NTk " lds\n\ts_add_u32 m0, %[lw], 4096\n\ts_xor_b32 %[t], %[kb], 0x2c0000\n\tbuffer_load_dwordx4 %[go], %[rk], %[t] offen" QK_NTk " lds\n\ts_add_u32 m0, %[lw], 5120\n\ts_xor_b32 %[t], %[kb], 0x2d0000\n\tbuffer_load_dwordx4 %[go], %[rk], %[t] offen" QK_NTk " lds\n\ts_add_u32 m0, %[lw], 6144\n\ts_xor_b32 %[t], %[kb], 0x2e0000\n\tbuffer_load_dwordx4 %[go], %[rk], %[t] offen" QK_NTk " lds\n\ts_add_u32 m0, %[lw], 7168\n\ts_xor_b32 %[t], %[kb], 0x2f0000\n\tbuffer_load_dwordx4 %[go], %[rk], %[t] offen" QK_NTk " lds\n\t" \
          "s_waitcnt vmcnt(32)\n\tds_read_b64_tr_b16 v[40:41], %[rd0] offset:8192\n\tds_read_b64_tr_b16 v[42:43], %[rd0] offset:8320\n\tds_read_b64_tr_b16 v[44:45], %[rd0] offset:12288\n\tds_read_b64_tr_b16 v[46:47], %[rd0] offset:12416\n\tds_read_b64_tr_b16 a[72:73], %[rd1] offset:8192\n\tds_read_b64_tr_b16 a[74:75], %[rd1] offset:8320\n\tds_read_b64_tr_b16 a[76:77], %[rd1] offset:12288\n\tds_read_b64_tr_b16 a[78:79], %[rd1] offset:12416\n\tds_read_b64_tr_b16 a[144:145], %[rd2] offset:8192\n\tds_read_b64_tr_b16 a[146:147], %[rd2] offset:8320\n\tds_read_b64_tr_b16 a[148:149], %[rd2] offset:12288\n\tds_read_b64_tr_b16 a[150:151], %[rd2] offset:12416\n\tds_read_b64_tr_b16 a[152:153], %[rd3] offset:8192\n\tds_read_b64_tr_b16 a[154:155], %[rd3] offset:8320\n\tds_read_b64_tr_b16 a[156:157], %[rd3] offset:12288\n\tds_read_b64_tr_b16 a[158:159], %[rd3] offset:12416\n\ts_waitcnt lgkmcnt(0)\n\t" \
          "s_add_u32 m0, %[lw], 8192\n\ts_xor_b32 %[t], %[kb], 0x300000\n\tbuffer_load_dwordx4 %[go], %[rk], %[t] offen" QK_NTk " lds\n\ts_add_u32 m0, %[lw], 9216\n\ts_xor_b32 %[t], %[kb], 0x310000\n\tbuffer_load_dwordx4 %[go], %[rk], %[t] offen" QK_NTk " lds\n\ts_add_u32 m0, %[lw], 10240\n\ts_xor_b32 %[t], %[kb], 0x320000\n\tbuffer_load_dwordx4 %[go], %[rk], %[t] offen" QK_NTk " lds\n\ts_add_u32 m0, %[lw], 11264\n\ts_xor_b32 %[t], %[kb], 0x330000\n\tbuffer_load_dwordx4 %[go], %[rk], %[t] offen" QK_NTk " lds\n\ts_add_u32 m0, %[lw], 12288\n\ts_xor_b32 %[t], %[kb], 0x340000\n\tbuffer_load_dwordx4 %[go], %[rk], %[t] offen" QK_NTk " lds\n\ts_add_u32 m0, %[lw], 13312\n\ts_xor_b32 %[t], %[kb], 0x350000\n\tbuffer_load_dwordx4 %[go], %[rk], %[t] offen" QK_NTk " lds\n\ts_add_u32 m0, %[lw], 14336\n\ts_xor_b32 %[t], %[kb], 0x360000\n\tbuffer_load_dwordx4 %[go], %[rk], %[t] offen" QK_NTk " lds\n\ts_add_u32 m0, %[lw], 15360\n\ts_xor_b32 %[t], %[kb], 0x370000\n\tbuffer_load_dwordx4 %[go], %[rk], %[t] offen" QK_NTk " lds\n\t" \
          "s_waitcnt vmcnt(32)\n\tds_read_b64_tr_b16 v[144:145], %[rd0] offset:16384\n\tds_read_b64_tr_b16 v[146:147], %[rd0] offset:16512\n\tds_read_b64_tr_b16 v[148:149], %[rd0] offset:20480\n\tds_read_b64_tr_b16 v[150:151], %[rd0] offset:20608\n\tds_read_b64_tr_b16 a[80:81], %[rd1] offset:16384\n\tds_read_b64_tr_b16 a[82:83], %[rd1] offset:16512\n\tds_read_b64_tr_b16 a[84:85], %[rd1] offset:20480\n\tds_read_b64_tr_b16 a[86:87], %[rd1] offset:20608\n\tds_read_b64_tr_b16 a[160:161], %[rd2] offset:16384\n\tds_read_b64_tr_b16 a[162:163], %[rd2] offset:16512\n\tds_read_b64_tr_b16 a[164:165], %[rd2] offset:20480\n\tds_read_b64_tr_b16 a[166:167], %[rd2] offset:20608\n\tds_read_b64_tr_b16 a[168:169], %[rd3] offset:16384\n\tds_read_b64_tr_b16 a[170:171], %[rd3] offset:16512\n\tds_read_b64_tr_b16 a[172:173], %[rd3] offset:20480\n\tds_read_b64_tr_b16 a[174:175], %[rd3] offset:20608\n\ts_waitcnt lgkmcnt(0)\n\t" \
          "s_add_u32 m0, %[lw], 16384\n\ts_xor_b32 %[t], %[kb], 0x380000\n\tbuffer_load_dwordx4 %[go], %[rk], %[t] offen" QK_NTk " lds\n\ts_add_u32 m0, %[lw], 17408\n\ts_xor_b32 %[t], %[kb], 0x390000\n\tbuffer_load_dwordx4 %[go], %[rk], %[t] offen" QK_NTk " lds\n\ts_add_u32 m0, %[lw], 18432\n\ts_xor_b32 %[t], %[kb], 0x3a0000\n\tbuffer_load_dwordx4 %[go], %[rk], %[t] offen" QK_NTk " lds\n\ts_add_u32 m0, %[lw], 19456\n\ts_xor_b32 %[t], %[kb], 0x3b0000\n\tbuffer_load_dwordx4 %[go], %[rk], %[t] offen" QK_NTk " lds\n\ts_add_u32 m0, %[lw], 20480\n\ts_xor_b32 %[t], %[kb], 0x3c0000\n\tbuffer_load_dwordx4 %[go], %[rk], %[t] offen" QK_NTk " lds\n\ts_add_u32 m0, %[lw], 21504\n\ts_xor_b32 %[t], %[kb], 0x3d0000\n\tbuffer_load_dwordx4 %[go], %[rk], %[t] offen" QK_NTk " lds\n\ts_add_u32 m0, %[lw], 22528\n\ts_xor_b32 %[t], %[kb], 0x3e0000\n\tbuffer_load_dwordx4 %[go], %[rk], %[t] offen" QK_NTk " lds\n\ts_add_u32 m0, %[lw], 23552\n\ts_xor_b32 %[t], %[kb], 0x3f0000\n\tbuffer_load_dwordx4 %[go], %[rk], %[t] offen" QK_NTk " lds\n\t" \
          "s_waitcnt vmcnt(32)\n\tds_read_b64_tr_b16 v[80:81], %[rd0] offset:24576\n\tds_read_b64_tr_b16 v[82:83], %[rd0] offset:24704\n\tds_read_b64_tr_b16 v[84:85], %[rd0] offset:28672\n\tds_read_b64_tr_b16 v[86:87], %[rd0] offset:28800\n\tds_read_b64_tr_b16 a[88:89], %[rd1] offset:24576\n\tds_read_b64_tr_b16 a[90:91], %[rd1] offset:24704\n\tds_read_b64_tr_b16 a[92:93], %[rd1] offset:28672\n\tds_read_b64_tr_b16 a[94:95], %[rd1] offset:28800\n\tds_read_b64_tr_b16 a[176:177], %[rd2] offset:24576\n\tds_read_b64_tr_b16 a[178:179], %[rd2] offset:24704\n\tds_read_b64_tr_b16 a[180:181], %[rd2] offset:28672\n\tds_read_b64_tr_b16 a[182:183], %[rd2] offset:28800\n\tds_read_b64_tr_b16 a[184:185], %[rd3] offset:24576\n\tds_read_b64_tr_b16 a[186:187], %[rd3] offset:24704\n\tds_read_b64_tr_b16 a[188:189], %[rd3] offset:28672\n\tds_read_b64_tr_b16 a[190:191], %[rd3] offset:28800\n\ts_waitcnt lgkmcnt(0)\n\t" \
          "s_add_u32 m0, %[lw], 24576\n\ts_xor_b32 %[t], %[kb], 0x380000\n\tbuffer_load_dwordx4 %[go], %[rq], %[t] offen" QK_NTq " lds\n\ts_add_u32 m0, %[lw], 25600\n\ts_xor_b32 %[t], %[kb], 0x390000\n\tbuffer_load_dwordx4 %[go], %[rq], %[t] offen" QK_NTq " lds\n\ts_add_u32 m0, %[lw], 26624\n\ts_xor_b32 %[t], %[kb], 0x3a0000\n\tbuffer_load_dwordx4 %[go], %[rq], %[t] offen" QK_NTq " lds\n\ts_add_u32 m0, %[lw], 27648\n\ts_xor_b32 %[t], %[kb], 0x3b0000\n\tbuffer_load_dwordx4 %[go], %[rq], %[t] offen" QK_NTq " lds\n\ts_add_u32 m0, %[lw], 28672\n\ts_xor_b32 %[t], %[kb], 0x3c0000\n\tbuffer_load_dwordx4 %[go], %[rq], %[t] offen" QK_NTq " lds\n\ts_add_u32 m0, %[lw], 29696\n\ts_xor_b32 %[t], %[kb], 0x3d0000\n\tbuffer_load_dwordx4 %[go], %[rq], %[t] offen" QK_NTq " lds\n\ts_add_u32 m0, %[lw], 30720\n\ts_xor_b32 %[t], %[kb], 0x3e0000\n\tbuffer_load_dwordx4 %[go], %[rq], %[t] offen" QK_NTq " lds\n\ts_add_u32 m0, %[lw], 31744\n\ts_xor_b32 %[t], %[kb], 0x3f0000\n\tbuffer_load_dwordx4 %[go], %[rq], %[t] offen" QK_NTq " lds\n\t" \
          "s_waitcnt vmcnt(32)\n\tds_read_b64_tr_b16 v[152:153], %[rd0] offset:32768\n\tds_read_b64_tr_b16 v[154:155], %[rd0] offset:32896\n\tds_read_b64_tr_b16 v[156:157], %[rd0] offset:36864\n\tds_read_b64_tr_b16 v[158:159], %[rd0] offset:36992\n\tds_read_b64_tr_b16 a[96:97], %[rd1] offset:32768\n\tds_read_b64_tr_b16 a[98:99], %[rd1] offset:32896\n\tds_read_b64_tr_b16 a[100:101], %[rd1] offset:36864\n\tds_read_b64_tr_b16 a[102:103], %[rd1] offset:36992\n\tds_read_b64_tr_b16 a[192:193], %[rd2] offset:32768\n\tds_read_b64_tr_b16 a[194:195], %[rd2] offset:32896\n\tds_read_b64_tr_b16 a[196:197], %[rd2] offset:36864\n\tds_read_b64_tr_b16 a[198:199], %[rd2] offset:36992\n\tds_read_b64_tr_b16 a[200:201], %[rd3] offset:32768\n\tds_read_b64_tr_b16 a[202:203], %[rd3] offset:32896\n\tds_read_b64_tr_b16 a[204:205], %[rd3] offset:36864\n\tds_read_b64_tr_b16 a[206:207], %[rd3] offset:36992\n\ts_waitcnt lgkmcnt(0)\n\t" \
          "s_add_u32 m0, %[lw], 32768\n\ts_xor_b32 %[t], %[kb], 0x300000\n\tbuffer_load_dwordx4 %[go], %[rq], %[t] offen" QK_NTq " lds\n\ts_add_u32 m0, %[lw], 33792\n\ts_xor_b32 %[t], %[kb], 0x310000\n\tbuffer_load_dwordx4 %[go], %[rq], %[t] offen" QK_NTq " lds\n\ts_add_u32 m0, %[lw], 34816\n\ts_xor_b32 %[t], %[kb], 0x320000\n\tbuffer_load_dwordx4 %[go], %[rq], %[t] offen" QK_NTq " lds\n\ts_add_u32 m0, %[lw], 35840\n\ts_xor_b32 %[t], %[kb], 0x330000\n\tbuffer_load_dwordx4 %[go], %[rq], %[t] offen" QK_NTq " lds\n\ts_add_u32 m0, %[lw], 36864\n\ts_xor_b32 %[t], %[kb], 0x340000\n\tbuffer_load_dwordx4 %[go], %[rq], %[t] offen" QK_NTq " lds\n\ts_add_u32 m0, %[lw], 37888\n\ts_xor_b32 %[t], %[kb], 0x350000\n\tbuffer_load_dwordx4 %[go], %[rq], %[t] offen" QK_NTq " lds\n\ts_add_u32 m0, %[lw], 38912\n\ts_xor_b32 %[t], %[kb], 0x360000\n\tbuffer_load_dwordx4 %[go], %[rq], %[t] offen" QK_NTq " lds\n\ts_add_u32 m0, %[lw], 39936\n\ts_xor_b32 %[t], %[kb], 0x370000\n\tbuffer_load_dwordx4 %[go], %[rq], %[t] offen" QK_NTq " lds\n\t" \
          "s_waitcnt vmcnt(32)\n\tds_read_b64_tr_b16 v[120:121], %[rd0] offset:0\n\tds_read_b64_tr_b16 v[122:123], %[rd0] offset:128\n\tds_read_b64_tr_b16 v[124:125], %[rd0] offset:4096\n\tds_read_b64_tr_b16 v[126:127], %[rd0] offset:4224\n\tds_read_b64_tr_b16 a[104:105], %[rd1] offset:0\n\tds_read_b64_tr_b16 a[106:107], %[rd1] offset:128\n\tds_read_b64_tr_b16 a[108:109], %[rd1] offset:4096\n\tds_read_b64_tr_b16 a[110:111], %[rd1] offset:4224\n\tds_read_b64_tr_b16 a[208:209], %[rd2] offset:0\n\tds_read_b64_tr_b16 a[210:211], %[rd2] offset:128\n\tds_read_b64_tr_b16 a[212:213], %[rd2] offset:4096\n\tds_read_b64_tr_b16 a[214:215], %[rd2] offset:4224\n\tds_read_b64_tr_b16 a[216:217], %[rd3] offset:0\n\tds_read_b64_tr_b16 a[218:219], %[rd3] offset:128\n\tds_read_b64_tr_b16 a[220:221], %[rd3] offset:4096\n\tds_read_b64_tr_b16 a[222:223], %[rd3] offset:4224\n\ts_waitcnt lgkmcnt(0)\n\t" \
          "s_add_u32 m0, %[lw], 0\n\ts_xor_b32 %[t], %[kb], 0x280000\n\tbuffer_load_dwordx4 %[go], %[rq], %[t] offen" QK_NTq " lds\n\ts_add_u32 m0, %[lw], 1024\n\ts_xor_b32 %[t], %[kb], 0x290000\n\tbuffer_load_dwordx4 %[go], %[rq], %[t] offen" QK_NTq " lds\n\ts_add_u32 m0, %[lw], 2048\n\ts_xor_b32 %[t], %[kb], 0x2a0000\n\tbuffer_load_dwordx4 %[go], %[rq], %[t] offen" QK_NTq " lds\n\ts_add_u32 m0, %[lw], 3072\n\ts_xor_b32 %[t], %[kb], 0x2b0000\n\tbuffer_load_dwordx4 %[go], %[rq], %[t] offen" QK_NTq " lds\n\ts_add_u32 m0, %[lw], 4096\n\ts_xor_b32 %[t], %[kb], 0x2c0000\n\tbuffer_load_dwordx4 %[go], %[rq], %[t] offen" QK_NTq " lds\n\ts_add_u32 m0, %[lw], 5120\n\ts_xor_b32 %[t], %[kb], 0x2d0000\n\tbuffer_load_dwordx4 %[go], %[rq], %[t] offen" QK_NTq " lds\n\ts_add_u32 m0, %[lw], 6144\n\ts_xor_b32 %[t], %[kb], 0x2e0000\n\tbuffer_load_dwordx4 %[go], %[rq], %[t] offen" QK_NTq " lds\n\ts_add_u32 m0, %[lw], 7168\n\ts_xor_b32 %[t], %[kb], 0x2f0000\n\tbuffer_load_dwordx4 %[go], %[rq], %[t] offen" QK_NTq " lds\n\t" \
          "s_waitcnt vmcnt(32)\n\tds_read_b64_tr_b16 a[16:17], %[rd0] offset:8192\n\tds_read_b64_tr_b16 a[18:19], %[rd0] offset:8320\n\tds_read_b64_tr_b16 a[20:21], %[rd0] offset:12288\n\tds_read_b64_tr_b16 a[22:23], %[rd0] offset:12416\n\tds_read_b64_tr_b16 a[112:113], %[rd1] offset:8192\n\tds_read_b64_tr_b16 a[114:115], %[rd1] offset:8320\n\tds_read_b64_tr_b16 a[116:117], %[rd1] offset:12288\n\tds_read_b64_tr_b16 a[118:119], %[rd1] offset:12416\n\tds_read_b64_tr_b16 a[224:225], %[rd2] offset:8192\n\tds_read_b64_tr_b16 a[226:227], %[rd2] offset:8320\n\tds_read_b64_tr_b16 a[228:229], %[rd2] offset:12288\n\tds_read_b64_tr_b16 a[230:231], %[rd2] offset:12416\n\tds_read_b64_tr_b16 a[232:233], %[rd3] offset:8192\n\tds_read_b64_tr_b16 a[234:235], %[rd3] offset:8320\n\tds_read_b64_tr_b16 a[236:237], %[rd3] offset:12288\n\tds_read_b64_tr_b16 a[238:239], %[rd3] offset:12416\n\ts_waitcnt lgkmcnt(0)\n\t" \
          "s_add_u32 m0, %[lw], 8192\n\ts_xor_b32 %[t], %[kb], 0x200000\n\tbuffer_load_dwordx4 %[go], %[rq], %[t] offen" QK_NTq " lds\n\ts_add_u32 m0, %[lw], 9216\n\ts_xor_b32 %[t], %[kb], 0x210000\n\tbuffer_load_dwordx4 %[go], %[rq], %[t] offen" QK_NTq " lds\n\ts_add_u32 m0, %[lw], 10240\n\ts_xor_b32 %[t], %[kb], 0x220000\n\tbuffer_load_dwordx4 %[go], %[rq], %[t] offen" QK_NTq " lds\n\ts_add_u32 m0, %[lw], 11264\n\ts_xor_b32 %[t], %[kb], 0x230000\n\tbuffer_load_dwordx4 %[go], %[rq], %[t] offen" QK_NTq " lds\n\ts_add_u32 m0, %[lw], 12288\n\ts_xor_b32 %[t], %[kb], 0x240000\n\tbuffer_load_dwordx4 %[go], %[rq], %[t] offen" QK_NTq " lds\n\ts_add_u32 m0, %[lw], 13312\n\ts_xor_b32 %[t], %[kb], 0x250000\n\tbuffer_load_dwordx4 %[go], %[rq], %[t] offen" QK_NTq " lds\n\ts_add_u32 m0, %[lw], 14336\n\ts_xor_b32 %[t], %[kb], 0x260000\n\tbuffer_load_dwordx4 %[go], %[rq], %[t] offen" QK_NTq " lds\n\ts_add_u32 m0, %[lw], 15360\n\ts_xor_b32 %[t], %[kb], 0x270000\n\tbuffer_load_dwordx4 %[go], %[rq], %[t] offen" QK_NTq " lds\n\t" \
          "s_waitcnt vmcnt(32)\n\tds_read_b64_tr_b16 a[56:57], %[rd0] offset:16384\n\tds_read_b64_tr_b16 a[58:59], %[rd0] offset:16512\n\tds_read_b64_tr_b16 a[60:61], %[rd0] offset:20480\n\tds_read_b64_tr_b16 a[62:63], %[rd0] offset:20608\n\tds_read_b64_tr_b16 a[120:121], %[rd1] offset:16384\n\tds_read_b64_tr_b16 a[122:123], %[rd1] offset:16512\n\tds_read_b64_tr_b16 a[124:125], %[rd1] offset:20480\n\tds_read_b64_tr_b16 a[126:127], %[rd1] offset:20608\n\tds_read_b64_tr_b16 a[240:241], %[rd2] offset:16384\n\tds_read_b64_tr_b16 a[242:243], %[rd2] offset:16512\n\tds_read_b64_tr_b16 a[244:245], %[rd2] offset:20480\n\tds_read_b64_tr_b16 a[246:247], %[rd2] offset:20608\n\tds_read_b64_tr_b16 a[248:249], %[rd3] offset:16384\n\tds_read_b64_tr_b16 a[250:251], %[rd3] offset:16512\n\tds_read_b64_tr_b16 a[252:253], %[rd3] offset:20480\n\tds_read_b64_tr_b16 a[254:255], %[rd3] offset:20608\n\ts_waitcnt lgkmcnt(0)\n\t" \
          "s_add_u32 m0, %[lw], 16384\n\ts_xor_b32 %[t], %[kb], 0x180000\n\tbuffer_load_dwordx4 %[go], %[rq], %[t] offen" QK_NTq " lds\n\ts_add_u32 m0, %[lw], 17408\n\ts_xor_b32 %[t], %[kb], 0x190000\n\tbuffer_load_dwordx4 %[go], %[rq], %[t] offen" QK_NTq " lds\n\ts_add_u32 m0, %[lw], 18432\n\ts_xor_b32 %[t], %[kb], 0x1a0000\n\tbuffer_load_dwordx4 %[go], %[rq], %[t] offen" QK_NTq " lds\n\ts_add_u32 m0, %[lw], 19456\n\ts_xor_b32 %[t], %[kb], 0x1b0000\n\tbuffer_load_dwordx4 %[go], %[rq], %[t] offen" QK_NTq " lds\n\ts_add_u32 m0, %[lw], 20480\n\ts_xor_b32 %[t], %[kb], 0x1c0000\n\tbuffer_load_dwordx4 %[go], %[rq], %[t] offen" QK_NTq " lds\n\ts_add_u32 m0, %[lw], 21504\n\ts_xor_b32 %[t], %[kb], 0x1d0000\n\tbuffer_load_dwordx4 %[go], %[rq], %[t] offen" QK_NTq " lds\n\ts_add_u32 m0, %[lw], 22528\n\ts_xor_b32 %[t], %[kb], 0x1e0000\n\tbuffer_load_dwordx4 %[go], %[rq], %[t] offen" QK_NTq " lds\n\ts_add_u32 m0, %[lw], 23552\n\ts_xor_b32 %[t], %[kb], 0x1f0000\n\tbuffer_load_dwordx4 %[go], %[rq], %[t] offen" QK_NTq " lds\n\t" \
          "s_waitcnt vmcnt(32)\n\tds_read_b64_tr_b16 a[24:25], %[rd0] offset:24576\n\tds_read_b64_tr_b16 a[26:27], %[rd0] offset:24704\n\tds_read_b64_tr_b16 a[28:29], %[rd0] offset:28672\n\tds_read_b64_tr_b16 a[30:31], %[rd0] offset:28800\n\tds_read_b64_tr_b16 a[32:33], %[rd1] offset:24576\n\tds_read_b64_tr_b16 a[34:35], %[rd1] offset:24704\n\tds_read_b64_tr_b16 a[36:37], %[rd1] offset:28672\n\tds_read_b64_tr_b16 a[38:39], %[rd1] offset:28800\n\tds_read_b64_tr_b16 a[40:41], %[rd2] offset:24576\n\tds_read_b64_tr_b16 a[42:43], %[rd2] offset:24704\n\tds_read_b64_tr_b16 a[44:45], %[rd2] offset:28672\n\tds_read_b64_tr_b16 a[46:47], %[rd2] offset:28800\n\tds_read_b64_tr_b16 a[48:49], %[rd3] offset:24576\n\tds_read_b64_tr_b16 a[50:51], %[rd3] offset:24704\n\tds_read_b64_tr_b16 a[52:53], %[rd3] offset:28672\n\tds_read_b64_tr_b16 a[54:55], %[rd3] offset:28800\n\ts_waitcnt lgkmcnt(0)\n\t" \
          "s_add_u32 m0, %[lw], 24576\n\ts_xor_b32 %[t], %[kb], 0x100000\n\tbuffer_load_dwordx4 %[go], %[rq], %[t] offen" QK_NTq " lds\n\ts_add_u32 m0, %[lw], 25600\n\ts_xor_b32 %[t], %[kb], 0x110000\n\tbuffer_load_dwordx4 %[go], %[rq], %[t] offen" QK_NTq " lds\n\ts_add_u32 m0, %[lw], 26624\n\ts_xor_b32 %[t], %[kb], 0x120000\n\tbuffer_load_dwordx4 %[go], %[rq], %[t] offen" QK_NTq " lds\n\ts_add_u32 m0, %[lw], 27648\n\ts_xor_b32 %[t], %[kb], 0x130000\n\tbuffer_load_dwordx4 %[go], %[rq], %[t] offen" QK_NTq " lds\n\ts_add_u32 m0, %[lw], 28672\n\ts_xor_b32 %[t], %[kb], 0x140000\n\tbuffer_load_dwordx4 %[go], %[rq], %[t] offen" QK_NTq " lds\n\ts_add_u32 m0, %[lw], 29696\n\ts_xor_b32 %[t], %[kb], 0x150000\n\tbuffer_load_dwordx4 %[go], %[rq], %[t] offen" QK_NTq " lds\n\ts_add_u32 m0, %[lw], 30720\n\ts_xor_b32 %[t], %[kb], 0x160000\n\tbuffer_load_dwordx4 %[go], %[rq], %[t] offen" QK_NTq " lds\n\ts_add_u32 m0, %[lw], 31744\n\ts_xor_b32 %[t], %[kb], 0x170000\n\tbuffer_load_dwordx4 %[go], %[rq], %[t] offen" QK_NTq " lds\n\t" \
          "s_waitcnt vmcnt(32)\n\tds_read_b64_tr_b16 v[160:161], %[rd0] offset:32768\n\tds_read_b64_tr_b16 v[162:163], %[rd0] offset:32896\n\tds_read_b64_tr_b16 v[164:165], %[rd0] offset:36864\n\tds_read_b64_tr_b16 v[166:167], %[rd0] offset:36992\n\tds_read_b64_tr_b16 v[128:129], %[rd1] offset:32768\n\tds_read_b64_tr_b16 v[130:131], %[rd1] offset:32896\n\tds_read_b64_tr_b16 v[132:133], %[rd1] offset:36864\n\tds_read_b64_tr_b16 v[134:135], %[rd1] offset:36992\n\tds_read_b64_tr_b16 a[0:1], %[rd2] offset:32768\n\tds_read_b64_tr_b16 a[2:3], %[rd2] offset:32896\n\tds_read_b64_tr_b16 a[4:5], %[rd2] offset:36864\n\tds_read_b64_tr_b16 a[6:7], %[rd2] offset:36992\n\tds_read_b64_tr_b16 a[8:9], %[rd3] offset:32768\n\tds_read_b64_tr_b16 a[10:11], %[rd3] offset:32896\n\tds_read_b64_tr_b16 a[12:13], %[rd3] offset:36864\n\tds_read_b64_tr_b16 a[14:15], %[rd3] offset:36992\n\ts_waitcnt lgkmcnt(0)\n\t" \
          "s_add_u32 m0, %[lw], 32768\n\ts_xor_b32 %[t], %[kb], 0x80000\n\tbuffer_load_dwordx4 %[go], %[rq], %[t] offen" QK_NTq " lds\n\ts_add_u32 m0, %[lw], 33792\n\ts_xor_b32 %[t], %[kb], 0x90000\n\tbuffer_load_dwordx4 %[go], %[rq], %[t] offen" QK_NTq " lds\n\ts_add_u32 m0, %[lw], 34816\n\ts_xor_b32 %[t], %[kb], 0xa0000\n\tbuffer_load_dwordx4 %[go], %[rq], %[t] offen" QK_NTq " lds\n\ts_add_u32 m0, %[lw], 35840\n\ts_xor_b32 %[t], %[kb], 0xb0000\n\tbuffer_load_dwordx4 %[go], %[rq], %[t] offen" QK_NTq " lds\n\ts_add_u32 m0, %[lw], 36864\n\ts_xor_b32 %[t], %[kb], 0xc0000\n\tbuffer_load_dwordx4 %[go], %[rq], %[t] offen" QK_NTq " lds\n\ts_add_u32 m0, %[lw], 37888\n\ts_xor_b32 %[t], %[kb], 0xd0000\n\tbuffer_load_dwordx4 %[go], %[rq], %[t] offen" QK_NTq " lds\n\ts_add_u32 m0, %[lw], 38912\n\ts_xor_b32 %[t], %[kb], 0xe0000\n\tbuffer_load_dwordx4 %[go], %[rq], %[t] offen" QK_NTq " lds\n\ts_add_u32 m0, %[lw], 39936\n\ts_xor_b32 %[t], %[kb], 0xf0000\n\tbuffer_load_dwordx4 %[go], %[rq], %[t] offen" QK_NTq " lds\n\t" \
          "s_waitcnt vmcnt(32)\n\tds_read_b64_tr_b16 v[104:105], %[rd0] offset:0\n\tds_read_b64_tr_b16 v[106:107], %[rd0] offset:128\n\tds_read_b64_tr_b16 v[108:109], %[rd0] offset:4096\n\tds_read_b64_tr_b16 v[110:111], %[rd0] offset:4224\n\tds_read_b64_tr_b16 v[168:169], %[rd1] offset:0\n\tds_read_b64_tr_b16 v[170:171], %[rd1] offset:128\n\tds_read_b64_tr_b16 v[172:173], %[rd1] offset:4096\n\tds_read_b64_tr_b16 v[174:175], %[rd1] offset:4224\n\tds_read_b64_tr_b16 v[112:113], %[rd2] offset:0\n\tds_read_b64_tr_b16 v[114:115], %[rd2] offset:128\n\tds_read_b64_tr_b16 v[116:117], %[rd2] offset:4096\n\tds_read_b64_tr_b16 v[118:119], %[rd2] offset:4224\n\tds_read_b64_tr_b16 v[176:177], %[rd3] offset:0\n\tds_read_b64_tr_b16 v[178:179], %[rd3] offset:128\n\tds_read_b64_tr_b16 v[180:181], %[rd3] offset:4096\n\tds_read_b64_tr_b16 v[182:183], %[rd3] offset:4224\n\ts_waitcnt lgkmcnt(0)\n\t" \
          "buffer_load_dwordx4 %[rec], %[ro], %[rr], 0 offen sc1\n\t" \
          "s_add_u32 m0, %[lw], 0\n\ts_xor_b32 %[t], %[kb], 0x0\n\tbuffer_load_dwordx4 %[go], %[rq], %[t] offen" QK_NTq " lds\n\ts_add_u32 m0, %[lw], 1024\n\ts_xor_b32 %[t], %[kb], 0x10000\n\tbuffer_load_dwordx4 %[go], %[rq], %[t] offen" QK_NTq " lds\n\ts_add_u32 m0, %[lw], 2048\n\ts_xor_b32 %[t], %[kb], 0x20000\n\tbuffer_load_dwordx4 %[go], %[rq], %[t] offen" QK_NTq " lds\n\ts_add_u32 m0, %[lw], 3072\n\ts_xor_b32 %[t], %[kb], 0x30000\n\tbuffer_load_dwordx4 %[go], %[rq], %[t] offen" QK_NTq " lds\n\ts_add_u32 m0, %[lw], 4096\n\ts_xor_b32 %[t], %[kb], 0x40000\n\tbuffer_load_dwordx4 %[go], %[rq], %[t] offen" QK_NTq " lds\n\ts_add_u32 m0, %[lw], 5120\n\ts_xor_b32 %[t], %[kb], 0x50000\n\tbuffer_load_dwordx4 %[go], %[rq], %[t] offen" QK_NTq " lds\n\ts_add_u32 m0, %[lw], 6144\n\ts_xor_b32 %[t], %[kb], 0x60000\n\tbuffer_load_dwordx4 %[go], %[rq], %[t] offen" QK_NTq " lds\n\ts_add_u32 m0, %[lw], 7168\n\ts_xor_b32 %[t], %[kb], 0x70000\n\tbuffer_load_dwordx4 %[go], %[rq], %[t] offen" QK_NTq " lds\n\t" \
          "s_waitcnt vmcnt(33)\n\tds_read_b64_tr_b16 v[184:185], %[rd0] offset:8192\n\tds_read_b64_tr_b16 v[186:187], %[rd0] offset:8320\n\tds_read_b64_tr_b16 v[188:189], %[rd0] offset:12288\n\tds_read_b64_tr_b16 v[190:191], %[rd0] offset:12416\n\tds_read_b64_tr_b16 v[88:89], %[rd1] offset:8192\n\tds_read_b64_tr_b16 v[90:91], %[rd1] offset:8320\n\tds_read_b64_tr_b16 v[92:93], %[rd1] offset:12288\n\tds_read_b64_tr_b16 v[94:95], %[rd1] offset:12416\n\tds_read_b64_tr_b16 v[192:193], %[rd2] offset:8192\n\tds_read_b64_tr_b16 v[194:195], %[rd2] offset:8320\n\tds_read_b64_tr_b16 v[196:197], %[rd2] offset:12288\n\tds_read_b64_tr_b16 v[198:199], %[rd2] offset:12416\n\tds_read_b64_tr_b16 v[96:97], %[rd3] offset:8192\n\tds_read_b64_tr_b16 v[98:99], %[rd3] offset:8320\n\tds_read_b64_tr_b16 v[100:101], %[rd3] offset:12288\n\tds_read_b64_tr_b16 v[102:103], %[rd3] offset:12416\n\ts_waitcnt lgkmcnt(0)\n\tds_write_b128 %[own], v[136:139] offset:8192\n\tds_write_b128 %[own], v[140:143] offset:9216\n\tds_write_b128 %[own], v[144:147] offset:10240\n\tds_write_b128 %[own], v[148:151] offset:11264\n\tds_write_b128 %[own], v[152:155] offset:12288\n\tds_write_b128 %[own], v[156:159] offset:13312\n\tds_write_b128 %[own], v[160:163] offset:14336\n\tds_write_b128 %[own], v[164:167] offset:15360\n\ts_waitcnt lgkmcnt(0)\n\t" \
          "s_waitcnt vmcnt(25)\n\tds_read_b64_tr_b16 v[64:65], %[rd0] offset:16384\n\tds_read_b64_tr_b16 v[66:67], %[rd0] offset:16512\n\tds_read_b64_tr_b16 v[68:69], %[rd0] offset:20480\n\tds_read_b64_tr_b16 v[70:71], %[rd0] offset:20608\n\tds_read_b64_tr_b16 v[200:201], %[rd1] offset:16384\n\tds_read_b64_tr_b16 v[202:203], %[rd1] offset:16512\n\tds_read_b64_tr_b16 v[204:205], %[rd1] offset:20480\n\tds_read_b64_tr_b16 v[206:207], %[rd1] offset:20608\n\tds_read_b64_tr_b16 v[72:73], %[rd2] offset:16384\n\tds_read_b64_tr_b16 v[74:75], %[rd2] offset:16512\n\tds_read_b64_tr_b16 v[76:77], %[rd2] offset:20480\n\tds_read_b64_tr_b16 v[78:79], %[rd2] offset:20608\n\tds_read_b64_tr_b16 v[208:209], %[rd3] offset:16384\n\tds_read_b64_tr_b16 v[210:211], %[rd3] offset:16512\n\tds_read_b64_tr_b16 v[212:213], %[rd3] offset:20480\n\tds_read_b64_tr_b16 v[214:215], %[rd3] offset:20608\n\ts_waitcnt lgkmcnt(0)\n\tds_write_b128 %[own], v[168:171] offset:16384\n\tds_write_b128 %[own], v[172:175] offset:17408\n\tds_write_b128 %[own], v[176:179] offset:18432\n\tds_write_b128 %[own], v[180:183] offset:19456\n\tds_write_b128 %[own], v[184:187] offset:20480\n\tds_write_b128 %[own], v[188:191] offset:21504\n\tds_write_b128 %[own], v[192:195] offset:22528\n\tds_write_b128 %[own], v[196:199] offset:23552\n\ts_waitcnt lgkmcnt(0)\n\t" \
          "s_waitcnt vmcnt(17)\n\tds_read_b64_tr_b16 v[216:217], %[rd0] offset:24576\n\tds_read_b64_tr_b16 v[218:219], %[rd0] offset:24704\n\tds_read_b64_tr_b16 v[220:221], %[rd0] offset:28672\n\tds_read_b64_tr_b16 v[222:223], %[rd0] offset:28800\n\tds_read_b64_tr_b16 v[48:49], %[rd1] offset:24576\n\tds_read_b64_tr_b16 v[50:51], %[rd1] offset:24704\n\tds_read_b64_tr_b16 v[52:53], %[rd1] offset:28672\n\tds_read_b64_tr_b16 v[54:55], %[rd1] offset:28800\n\tds_read_b64_tr_b16 v[224:225], %[rd2] offset:24576\n\tds_read_b64_tr_b16 v[226:227], %[rd2] offset:24704\n\tds_read_b64_tr_b16 v[228:229], %[rd2] offset:28672\n\tds_read_b64_tr_b16 v[230:231], %[rd2] offset:28800\n\tds_read_b64_tr_b16 v[56:57], %[rd3] offset:24576\n\tds_read_b64_tr_b16 v[58:59], %[rd3] offset:24704\n\tds_read_b64_tr_b16 v[60:61], %[rd3] offset:28672\n\tds_read_b64_tr_b16 v[62:63], %[rd3] offset:28800\n\ts_waitcnt lgkmcnt(0)\n\tds_write_b128 %[own], v[200:203] offset:24576\n\tds_write_b128 %[own], v[204:207] offset:25600\n\tds_write_b128 %[own], v[208:211] offset:26624\n\tds_write_b128 %[own], v[212:215] offset:27648\n\tds_write_b128 %[own], v[216:219] offset:28672\n\tds_write_b128 %[own], v[220:223] offset:29696\n\tds_write_b128 %[own], v[224:227] offset:30720\n\tds_write_b128 %[own], v[228:231] offset:31744\n\ts_waitcnt lgkmcnt(0)\n\t" \
          "s_waitcnt vmcnt(8)\n\tds_read_b64_tr_b16 v[24:25], %[rd0] offset:32768\n\tds_read_b64_tr_b16 v[26:27], %[rd0] offset:32896\n\tds_read_b64_tr_b16 v[28:29], %[rd0] offset:36864\n\tds_read_b64_tr_b16 v[30:31], %[rd0] offset:36992\n\tds_read_b64_tr_b16 v[136:137], %[rd1] offset:32768\n\tds_read_b64_tr_b16 v[138:139], %[rd1] offset:32896\n\tds_read_b64_tr_b16 v[140:141], %[rd1] offset:36864\n\tds_read_b64_tr_b16 v[142:143], %[rd1] offset:36992\n\tds_read_b64_tr_b16 v[32:33], %[rd2] offset:32768\n\tds_read_b64_tr_b16 v[34:35], %[rd2] offset:32896\n\tds_read_b64_tr_b16 v[36:37], %[rd2] offset:36864\n\tds_read_b64_tr_b16 v[38:39], %[rd2] offset:36992\n\tds_read_b64_tr_b16 v[144:145], %[rd3] offset:32768\n\tds_read_b64_tr_b16 v[146:147], %[rd3] offset:32896\n\tds_read_b64_tr_b16 v[148:149], %[rd3] offset:36864\n\tds_read_b64_tr_b16 v[150:151], %[rd3] offset:36992\n\ts_waitcnt lgkmcnt(0)\n\t" \
          "buffer_load_dword %[kf], %[ko], %[rc], 0 offen sc1\n\t" \
          "s_waitcnt vmcnt(0)\n\tds_read_b64_tr_b16 v[152:153], %[rd0] offset:0\n\tds_read_b64_tr_b16 v[154:155], %[rd0] offset:128\n\tds_read_b64_tr_b16 v[156:157], %[rd0] offset:4096\n\tds_read_b64_tr_b16 v[158:159], %[rd0] offset:4224\n\tds_read_b64_tr_b16 v[8:9], %[rd1] offset:0\n\tds_read_b64_tr_b16 v[10:11], %[rd1] offset:128\n\tds_read_b64_tr_b16 v[12:13], %[rd1] offset:4096\n\tds_read_b64_tr_b16 v[14:15], %[rd1] offset:4224\n\tds_read_b64_tr_b16 v[160:161], %[rd2] offset:0\n\tds_read_b64_tr_b16 v[162:163], %[rd2] offset:128\n\tds_read_b64_tr_b16 v[164:165], %[rd2] offset:4096\n\tds_read_b64_tr_b16 v[166:167], %[rd2] offset:4224\n\tds_read_b64_tr_b16 v[16:17], %[rd3] offset:0\n\tds_read_b64_tr_b16 v[18:19], %[rd3] offset:128\n\tds_read_b64_tr_b16 v[20:21], %[rd3] offset:4096\n\tds_read_b64_tr_b16 v[22:23], %[rd3] offset:4224\n\ts_waitcnt lgkmcnt(0)\n\tds_write_b128 %[own], v[136:139] offset:0\n\tds_write_b128 %[own], v[140:143] offset:1024\n\tds_write_b128 %[own], v[144:147] offset:2048\n\tds_write_b128 %[own], v[148:151] offset:3072\n\tds_write_b128 %[own], v[152:155] offset:4096\n\tds_write_b128 %[own], v[156:159] offset:5120\n\tds_write_b128 %[own], v[160:163] offset:6144\n\tds_write_b128 %[own], v[164:167] offset:7168\n\ts_waitcnt lgkmcnt(0)\n\t"
#define QK_DEFW "=&{a[0:31]}"(wa[0]), "=&{a[32:63]}"(wa[1]), "=&{a[64:95]}"(wa[2]), "=&{a[96:127]}"(wa[3]), "=&{a[128:159]}"(wa[4]), "=&{a[160:191]}"(wa[5]), "=&{a[192:223]}"(wa[6]), "=&{a[224:255]}"(wa[7]), "=&{v[8:39]}"(wv_[0]), "=&{v[40:71]}"(wv_[1]), "=&{v[72:103]}"(wv_[2]), "=&{v[104:135]}"(wv_[3]), "=&{v[136:167]}"(wv_[4]), "=&{v[168:199]}"(wv_[5]), "=&{v[200:231]}"(wv_[6])
#define QK_ALW 1
#define QK_STEP \
          "ds_read_b128 v[200:203], %[own] offset:4096\n\t" \
          "ds_read_b128 v[204:207], %[own] offset:5120\n\t" \
          "ds_read_b128 v[208:211], %[own] offset:6144\n\t" \
          "ds_read_b128 v[212:215], %[own] offset:7168\n\t" \
          "ds_read_b128 v[216:219], %[own] offset:8192\n\t" \
          "ds_read_b128 v[220:223], %[own] offset:9216\n\t" \
          "ds_read_b128 v[224:227], %[own] offset:0\n\t" \
          "ds_read_b128 v[228:231], %[own] offset:1024\n\t" \
          "v_mov_b32 %[al], -1\n\t" \
          "v_mov_b64 v[168:169], 0\n\t" \
          "v_mov_b64 v[170:171], 0\n\t" \
          "v_mov_b64 v[172:173], 0\n\t" \
          "v_mov_b64 v[174:175], 0\n\t" \
          "v_mov_b64 v[176:177], 0\n\t" \
          "v_mov_b64 v[178:179], 0\n\t" \
          "v_mov_b64 v[180:181], 0\n\t" \
          "v_mov_b64 v[182:183], 0\n\t" \
          "v_mov_b64 v[184:185], 0\n\t" \
          "v_mov_b64 v[186:187], 0\n\t" \
          "v_mov_b64 v[188:189], 0\n\t" \
          "v_mov_b64 v[190:191], 0\n\t" \
          "v_mov_b64 v[192:193], 0\n\t" \
          "v_mov_b64 v[194:195], 0\n\t" \
          "v_mov_b64 v[196:197], 0\n\t" \
          "v_mov_b64 v[198:199], 0\n\t" \
          "s_mov_b32 %[ann], %[t]\n\t" \
          "s_mov_b32 %[fl], 0\n\t" \
          "s_mov_b64 %[en], -1\n\t" \
          "s_waitcnt lgkmcnt(0)\n\t" \
          "s_cmp_eq_u32 %[w], 3\n\t" \
          "s_cbranch_scc0 Lqk_z%=\n\t" \
          "s_branch Lqk_c%=\n\t" \
          "Lqk_fail%=:\n\t" \
          "s_memrealtime vcc\n\t" \
          "s_waitcnt lgkmcnt(0)\n\t" \
          "s_add_u32 %[ann], vcc_lo, %[ald0]\n\t" \
          "v_mbcnt_lo_u32_b32 %[al], -1, 0\n\t" \
          "v_lshlrev_b32 %[al], 2, %[al]\n\t" \
          "s_and_b32 vcc_lo, %[ann], 0xffff\n\t" \
          "s_or_b32 vcc_lo, vcc_lo, 0x10000\n\t" \
          "v_mov_b32 %[m], vcc_lo\n\t" \
          "s_mov_b64 exec, %[alx]\n\t" \
          "buffer_store_dword %[m], %[al], %[rsm], %[als] offen sc1\n\t" \
          "s_mov_b64 exec, -1\n\t" \
          "buffer_load_dwordx4 v[136:139], %[ml0], %[rsm], %[mb0] offen sc1\n\t" \
          "buffer_load_dwordx4 v[140:143], %[ml1], %[rsm], %[mb0] offen sc1\n\t" \
          "buffer_load_dwordx4 v[144:147], %[ml0], %[rsm], %[mb1] offen sc1\n\t" \
          "buffer_load_dwordx4 v[148:151], %[ml1], %[rsm], %[mb1] offen sc1\n\t" \
          "buffer_load_dwordx4 v[152:155], %[ml0], %[rsm], %[mb2] offen sc1\n\t" \
          "buffer_load_dwordx4 v[156:159], %[ml1], %[rsm], %[mb2] offen sc1\n\t" \
          "buffer_load_dwordx4 v[160:163], %[ml0], %[rsm], %[mb3] offen sc1\n\t" \
          "buffer_load_dwordx4 v[164:167], %[ml1], %[rsm], %[mb3] offen sc1\n\t" \
          "ds_read_b128 v[200:203], %[own] offset:4096\n\t" \
          "ds_read_b128 v[204:207], %[own] offset:5120\n\t" \
          "ds_read_b128 v[208:211], %[own] offset:6144\n\t" \
          "ds_read_b128 v[212:215], %[own] offset:7168\n\t" \
          "ds_read_b128 v[216:219], %[own] offset:8192\n\t" \
          "ds_read_b128 v[220:223], %[own] offset:9216\n\t" \
          "ds_read_b128 v[224:227], %[own] offset:0\n\t" \
          "ds_read_b128 v[228:231], %[own] offset:1024\n\t" \
          "v_mov_b32 %[al], -1\n\t" \
          "v_mov_b64 v[168:169], 0\n\t" \
          "v_mov_b64 v[170:171], 0\n\t" \
          "v_mov_b64 v[172:173], 0\n\t" \
          "v_mov_b64 v[174:175], 0\n\t" \
          "v_mov_b64 v[176:177], 0\n\t" \
          "v_mov_b64 v[178:179], 0\n\t" \
          "v_mov_b64 v[180:181], 0\n\t" \
          "v_mov_b64 v[182:183], 0\n\t" \
          "v_mov_b64 v[184:185], 0\n\t" \
          "v_mov_b64 v[186:187], 0\n\t" \
          "v_mov_b64 v[188:189], 0\n\t" \
          "v_mov_b64 v[190:191], 0\n\t" \
          "v_mov_b64 v[192:193], 0\n\t" \
          "v_mov_b64 v[194:195], 0\n\t" \
          "v_mov_b64 v[196:197], 0\n\t" \
          "v_mov_b64 v[198:199], 0\n\t" \
          "s_mov_b32 %[fl], 1\n\t" \
          "s_mov_b64 %[en], 0\n\t" \
          "s_mov_b32 %[cnt], 0\n\t" \
          "s_waitcnt vmcnt(7)\n\t" \
          "v_max3_u32 %[m], v136, v137, v138\n\t" \
          "v_max_u32 %[m], %[m], v139\n\t" \
          "v_cmp_eq_u32 vcc, -1, %[m]\n\t" \
          "s_cbranch_vccnz Lqk_p0_%=\n\t" \
          "Lqk_k0_%=:\n\t" \
          "s_waitcnt vmcnt(6)\n\t" \
          "v_max3_u32 %[m], v140, v141, v142\n\t" \
          "v_max_u32 %[m], %[m], v143\n\t" \
          "v_cmp_eq_u32 vcc, -1, %[m]\n\t" \
          "s_cbranch_vccnz Lqk_p1_%=\n\t" \
          "Lqk_k1_%=:\n\t" \
          "s_waitcnt vmcnt(5)\n\t" \
          "v_max3_u32 %[m], v144, v145, v146\n\t" \
          "v_max_u32 %[m], %[m], v147\n\t" \
          "v_cmp_eq_u32 vcc, -1, %[m]\n\t" \
          "s_cbranch_vccnz Lqk_p2_%=\n\t" \
          "Lqk_k2_%=:\n\t" \
          "s_waitcnt vmcnt(4)\n\t" \
          "v_max3_u32 %[m], v148, v149, v150\n\t" \
          "v_max_u32 %[m], %[m], v151\n\t" \
          "v_cmp_eq_u32 vcc, -1, %[m]\n\t" \
          "s_cbranch_vccnz Lqk_p3_%=\n\t" \
          "Lqk_k3_%=:\n\t" \
          "s_waitcnt vmcnt(3)\n\t" \
          "v_max3_u32 %[m], v152, v153, v154\n\t" \
          "v_max_u32 %[m], %[m], v155\n\t" \
          "v_cmp_eq_u32 vcc, -1, %[m]\n\t" \
          "s_cbranch_vccnz Lqk_p4_%=\n\t" \
          "Lqk_k4_%=:\n\t" \
          "s_waitcnt vmcnt(2)\n\t" \
          "v_max3_u32 %[m], v156, v157, v158\n\t" \
          "v_max_u32 %[m], %[m], v159\n\t" \
          "v_cmp_eq_u32 vcc, -1, %[m]\n\t" \
          "s_cbranch_vccnz Lqk_p5_%=\n\t" \
          "Lqk_k5_%=:\n\t" \
          "s_waitcnt vmcnt(1)\n\t" \
          "v_max3_u32 %[m], v160, v161, v162\n\t" \
          "v_max_u32 %[m], %[m], v163\n\t" \
          "v_cmp_eq_u32 vcc, -1, %[m]\n\t" \
          "s_cbranch_vccnz Lqk_p6_%=\n\t" \
          "Lqk_k6_%=:\n\t" \
          "s_waitcnt vmcnt(0)\n\t" \
          "v_max3_u32 %[m], v164, v165, v166\n\t" \
          "v_max_u32 %[m], %[m], v167\n\t" \
          "v_cmp_eq_u32 vcc, -1, %[m]\n\t" \
          "s_cbranch_vccnz Lqk_p7_%=\n\t" \
          "Lqk_k7_%=:\n\t" \
          "s_memrealtime vcc\n\t" \
          "s_waitcnt lgkmcnt(0)\n\t" \
          "s_add_u32 vcc_lo, vcc_lo, %[ald]\n\t" \
          "s_sub_i32 vcc_hi, vcc_lo, %[ann]\n\t" \
          "s_cmp_gt_i32 vcc_hi, 0\n\t" \
          "s_cbranch_scc0 Lqk_h%=\n\t" \
          "s_mov_b32 %[ann], vcc_lo\n\t" \
          "v_mbcnt_lo_u32_b32 %[al], -1, 0\n\t" \
          "v_lshlrev_b32 %[al], 2, %[al]\n\t" \
          "s_and_b32 vcc_lo, %[ann], 0xffff\n\t" \
          "s_or_b32 vcc_lo, vcc_lo, 0x10000\n\t" \
          "v_mov_b32 %[m], vcc_lo\n\t" \
          "s_mov_b64 exec, %[alx]\n\t" \
          "buffer_store_dword %[m], %[al], %[rsm], %[als] offen sc1\n\t" \
          "s_mov_b64 exec, -1\n\t" \
          "Lqk_h%=:\n\t" \
          "v_mov_b32 %[al], -1\n\t" \
          "s_branch Lqk_go%=\n\t" \
          "Lqk_p0_%=:\n\t" \
          "s_cmp_lt_i32 %[cnt], 0\n\t" \
          "s_cbranch_scc1 Lqk_k0_%=\n\t" \
          "s_add_u32 %[cnt], %[cnt], 1\n\t" \
          "s_and_b32 vcc_lo, %[cnt], 3\n\t" \
          "s_cbranch_scc1 Lqk_q0_%=\n\t" \
          "s_getpc_b64 s[48:49]\n\t" \
          "s_branch Lqk_spin%=\n\t" \
          "Lqk_q0_%=:\n\t" \
          "buffer_load_dwordx4 v[136:139], %[ml0], %[rsm], %[mb0] offen sc1\n\t" \
          "s_waitcnt vmcnt(0)\n\t" \
          "v_max3_u32 %[m], v136, v137, v138\n\t" \
          "v_max_u32 %[m], %[m], v139\n\t" \
          "v_cmp_eq_u32 vcc, -1, %[m]\n\t" \
          "s_cbranch_vccnz Lqk_p0_%=\n\t" \
          "buffer_load_dwordx4 v[140:143], %[ml1], %[rsm], %[mb0] offen sc1\n\t" \
          "buffer_load_dwordx4 v[144:147], %[ml0], %[rsm], %[mb1] offen sc1\n\t" \
          "buffer_load_dwordx4 v[148:151], %[ml1], %[rsm], %[mb1] offen sc1\n\t" \
          "buffer_load_dwordx4 v[152:155], %[ml0], %[rsm], %[mb2] offen sc1\n\t" \
          "buffer_load_dwordx4 v[156:159], %[ml1], %[rsm], %[mb2] offen sc1\n\t" \
          "buffer_load_dwordx4 v[160:163], %[ml0], %[rsm], %[mb3] offen sc1\n\t" \
          "buffer_load_dwordx4 v[164:167], %[ml1], %[rsm], %[mb3] offen sc1\n\t" \
          "s_branch Lqk_k0_%=\n\t" \
          "Lqk_p1_%=:\n\t" \
          "s_cmp_lt_i32 %[cnt], 0\n\t" \
          "s_cbranch_scc1 Lqk_k1_%=\n\t" \
          "s_add_u32 %[cnt], %[cnt], 1\n\t" \
          "s_and_b32 vcc_lo, %[cnt], 3\n\t" \
          "s_cbranch_scc1 Lqk_q1_%=\n\t" \
          "s_getpc_b64 s[48:49]\n\t" \
          "s_branch Lqk_spin%=\n\t" \
          "Lqk_q1_%=:\n\t" \
          "buffer_load_dwordx4 v[140:143], %[ml1], %[rsm], %[mb0] offen sc1\n\t" \
          "s_waitcnt vmcnt(0)\n\t" \
          "v_max3_u32 %[m], v140, v141, v142\n\t" \
          "v_max_u32 %[m], %[m], v143\n\t" \
          "v_cmp_eq_u32 vcc, -1, %[m]\n\t" \
          "s_cbranch_vccnz Lqk_p1_%=\n\t" \
          "buffer_load_dwordx4 v[144:147], %[ml0], %[rsm], %[mb1] offen sc1\n\t" \
          "buffer_load_dwordx4 v[148:151], %[ml1], %[rsm], %[mb1] offen sc1\n\t" \
          "buffer_load_dwordx4 v[152:155], %[ml0], %[rsm], %[mb2] offen sc1\n\t" \
          "buffer_load_dwordx4 v[156:159], %[ml1], %[rsm], %[mb2] offen sc1\n\t" \
          "buffer_load_dwordx4 v[160:163], %[ml0], %[rsm], %[mb3] offen sc1\n\t" \
          "buffer_load_dwordx4 v[164:167], %[ml1], %[rsm], %[mb3] offen sc1\n\t" \
          "s_branch Lqk_k1_%=\n\t" \
          "Lqk_p2_%=:\n\t" \
          "s_cmp_lt_i32 %[cnt], 0\n\t" \
          "s_cbranch_scc1 Lqk_k2_%=\n\t" \
          "s_add_u32 %[cnt], %[cnt], 1\n\t" \
          "s_and_b32 vcc_lo, %[cnt], 3\n\t" \
          "s_cbranch_scc1 Lqk_q2_%=\n\t" \
          "s_getpc_b64 s[48:49]\n\t" \
          "s_branch Lqk_spin%=\n\t" \
          "Lqk_q2_%=:\n\t" \
          "buffer_load_dwordx4 v[144:147], %[ml0], %[rsm], %[mb1] offen sc1\n\t" \
          "s_waitcnt vmcnt(0)\n\t" \
          "v_max3_u32 %[m], v144, v145, v146\n\t" \
          "v_max_u32 %[m], %[m], v147\n\t" \
          "v_cmp_eq_u32 vcc, -1, %[m]\n\t" \
          "s_cbranch_vccnz Lqk_p2_%=\n\t" \
          "buffer_load_dwordx4 v[148:151], %[ml1], %[rsm], %[mb1] offen sc1\n\t" \
          "buffer_load_dwordx4 v[152:155], %[ml0], %[rsm], %[mb2] offen sc1\n\t" \
          "buffer_load_dwordx4 v[156:159], %[ml1], %[rsm], %[mb2] offen sc1\n\t" \
          "buffer_load_dwordx4 v[160:163], %[ml0], %[rsm], %[mb3] offen sc1\n\t" \
          "buffer_load_dwordx4 v[164:167], %[ml1], %[rsm], %[mb3] offen sc1\n\t" \
          "s_branch Lqk_k2_%=\n\t" \
          "Lqk_p3_%=:\n\t" \
          "s_cmp_lt_i32 %[cnt], 0\n\t" \
          "s_cbranch_scc1 Lqk_k3_%=\n\t" \
          "s_add_u32 %[cnt], %[cnt], 1\n\t" \
          "s_and_b32 vcc_lo, %[cnt], 3\n\t" \
          "s_cbranch_scc1 Lqk_q3_%=\n\t" \
          "s_getpc_b64 s[48:49]\n\t" \
          "s_branch Lqk_spin%=\n\t" \
          "Lqk_q3_%=:\n\t" \
          "buffer_load_dwordx4 v[148:151], %[ml1], %[rsm], %[mb1] offen sc1\n\t" \
          "s_waitcnt vmcnt(0)\n\t" \
          "v_max3_u32 %[m], v148, v149, v150\n\t" \
          "v_max_u32 %[m], %[m], v151\n\t" \
          "v_cmp_eq_u32 vcc, -1, %[m]\n\t" \
          "s_cbranch_vccnz Lqk_p3_%=\n\t" \
          "buffer_load_dwordx4 v[152:155], %[ml0], %[rsm], %[mb2] offen sc1\n\t" \
          "buffer_load_dwordx4 v[156:159], %[ml1], %[rsm], %[mb2] offen sc1\n\t" \
          "buffer_load_dwordx4 v[160:163], %[ml0], %[rsm], %[mb3] offen sc1\n\t" \
          "buffer_load_dwordx4 v[164:167], %[ml1], %[rsm], %[mb3] offen sc1\n\t" \
          "s_branch Lqk_k3_%=\n\t" \
          "Lqk_p4_%=:\n\t" \
          "s_cmp_lt_i32 %[cnt], 0\n\t" \
          "s_cbranch_scc1 Lqk_k4_%=\n\t" \
          "s_add_u32 %[cnt], %[cnt], 1\n\t" \
          "s_and_b32 vcc_lo, %[cnt], 3\n\t" \
          "s_cbranch_scc1 Lqk_q4_%=\n\t" \
          "s_getpc_b64 s[48:49]\n\t" \
          "s_branch Lqk_spin%=\n\t" \
          "Lqk_q4_%=:\n\t" \
          "buffer_load_dwordx4 v[152:155], %[ml0], %[rsm], %[mb2] offen sc1\n\t" \
          "s_waitcnt vmcnt(0)\n\t" \
          "v_max3_u32 %[m], v152, v153, v154\n\t" \
          "v_max_u32 %[m], %[m], v155\n\t" \
          "v_cmp_eq_u32 vcc, -1, %[m]\n\t" \
          "s_cbranch_vccnz Lqk_p4_%=\n\t" \
          "buffer_load_dwordx4 v[156:159], %[ml1], %[rsm], %[mb2] offen sc1\n\t" \
          "buffer_load_dwordx4 v[160:163], %[ml0], %[rsm], %[mb3] offen sc1\n\t" \
          "buffer_load_dwordx4 v[164:167], %[ml1], %[rsm], %[mb3] offen sc1\n\t" \
          "s_branch Lqk_k4_%=\n\t" \
          "Lqk_p5_%=:\n\t" \
          "s_cmp_lt_i32 %[cnt], 0\n\t" \
          "s_cbranch_scc1 Lqk_k5_%=\n\t" \
          "s_add_u32 %[cnt], %[cnt], 1\n\t" \
          "s_and_b32 vcc_lo, %[cnt], 3\n\t" \
          "s_cbranch_scc1 Lqk_q5_%=\n\t" \
          "s_getpc_b64 s[48:49]\n\t" \
          "s_branch Lqk_spin%=\n\t" \
          "Lqk_q5_%=:\n\t" \
          "buffer_load_dwordx4 v[156:159], %[ml1], %[rsm], %[mb2] offen sc1\n\t" \
          "s_waitcnt vmcnt(0)\n\t" \
          "v_max3_u32 %[m], v156, v157, v158\n\t" \
          "v_max_u32 %[m], %[m], v159\n\t" \
          "v_cmp_eq_u32 vcc, -1, %[m]\n\t" \
          "s_cbranch_vccnz Lqk_p5_%=\n\t" \
          "buffer_load_dwordx4 v[160:163], %[ml0], %[rsm], %[mb3] offen sc1\n\t" \
          "buffer_load_dwordx4 v[164:167], %[ml1], %[rsm], %[mb3] offen sc1\n\t" \
          "s_branch Lqk_k5_%=\n\t" \
          "Lqk_p6_%=:\n\t" \
          "s_cmp_lt_i32 %[cnt], 0\n\t" \
          "s_cbranch_scc1 Lqk_k6_%=\n\t" \
          "s_add_u32 %[cnt], %[cnt], 1\n\t" \
          "s_and_b32 vcc_lo, %[cnt], 3\n\t" \
          "s_cbranch_scc1 Lqk_q6_%=\n\t" \
          "s_getpc_b64 s[48:49]\n\t" \
          "s_branch Lqk_spin%=\n\t" \
          "Lqk_q6_%=:\n\t" \
          "buffer_load_dwordx4 v[160:163], %[ml0], %[rsm], %[mb3] offen sc1\n\t" \
          "s_waitcnt vmcnt(0)\n\t" \
          "v_max3_u32 %[m], v160, v161, v162\n\t" \
          "v_max_u32 %[m], %[m], v163\n\t" \
          "v_cmp_eq_u32 vcc, -1, %[m]\n\t" \
          "s_cbranch_vccnz Lqk_p6_%=\n\t" \
          "buffer_load_dwordx4 v[164:167], %[ml1], %[rsm], %[mb3] offen sc1\n\t" \
          "s_branch Lqk_k6_%=\n\t" \
          "Lqk_p7_%=:\n\t" \
          "s_cmp_lt_i32 %[cnt], 0\n\t" \
          "s_cbranch_scc1 Lqk_k7_%=\n\t" \
          "s_add_u32 %[cnt], %[cnt], 1\n\t" \
          "s_and_b32 vcc_lo, %[cnt], 3\n\t" \
          "s_cbranch_scc1 Lqk_q7_%=\n\t" \
          "s_getpc_b64 s[48:49]\n\t" \
          "s_branch Lqk_spin%=\n\t" \
          "Lqk_q7_%=:\n\t" \
          "buffer_load_dwordx4 v[164:167], %[ml1], %[rsm], %[mb3] offen sc1\n\t" \
          "s_waitcnt vmcnt(0)\n\t" \
          "v_max3_u32 %[m], v164, v165, v166\n\t" \
          "v_max_u32 %[m], %[m], v167\n\t" \
          "v_cmp_eq_u32 vcc, -1, %[m]\n\t" \
          "s_cbranch_vccnz Lqk_p7_%=\n\t" \
          "s_branch Lqk_k7_%=\n\t" \
          "Lqk_spin%=:\n\t" \
          "s_memrealtime vcc\n\t" \
          "s_waitcnt lgkmcnt(0)\n\t" \
          "s_add_u32 %[ann], vcc_lo, %[ald0]\n\t" \
          "v_mbcnt_lo_u32_b32 %[al], -1, 0\n\t" \
          "v_lshlrev_b32 %[al], 2, %[al]\n\t" \
          "s_and_b32 vcc_lo, %[ann], 0xffff\n\t" \
          "s_or_b32 vcc_lo, vcc_lo, 0x10000\n\t" \
          "v_mov_b32 %[m], vcc_lo\n\t" \
          "s_mov_b64 exec, %[alx]\n\t" \
          "buffer_store_dword %[m], %[al], %[rsm], %[als] offen sc1\n\t" \
          "s_mov_b64 exec, -1\n\t" \
          "buffer_load_dword %[al], off, %[rsc], %[tmo] sc1\n\t" \
          "s_add_u32 s48, s48, 4\n\t" \
          "s_addc_u32 s49, s49, 0\n\t" \
          "s_waitcnt vmcnt(0)\n\t" \
          "v_readfirstlane_b32 vcc_lo, %[al]\n\t" \
          "s_lshr_b32 vcc_hi, %[cnt], 16\n\t" \
          "s_or_b32 vcc_lo, vcc_lo, vcc_hi\n\t" \
          "s_cbranch_scc0 Lqk_back%=\n\t" \
          "v_mov_b32 %[al], 1\n\t" \
          "buffer_store_dword %[al], off, %[rsc], %[tmo] sc1\n\t" \
          "s_mov_b32 %[cnt], -1\n\t" \
          "Lqk_back%=:\n\t" \
          "s_setpc_b64 s[48:49]\n\t" \
          ".p2align 6\n\t" \
          "s_nop 0\n\t" \
          "s_nop 0\n\t" \
          "Lqk_s%=:\n\t" \
          "s_sleep 2\n\t" \
          "Lqk_c%=:\n\t" \
          "s_memrealtime vcc\n\t" \
          "s_waitcnt lgkmcnt(0)\n\t" \
          "s_sub_i32 vcc_lo, %[t], vcc_lo\n\t" \
          "s_cmp_gt_i32 vcc_lo, 16\n\t" \
          "s_cbranch_scc1 Lqk_s%=\n\t" \
          "Lqk_w%=:\n\t" \
          "s_memrealtime s[50:51]\n\t" \
          "s_getpc_b64 s[48:49]\n\t" \
          "Lqk_y%=:\n\t" \
          "s_add_u32 s48, s48, Lqk_z%=-Lqk_y%=\n\t" \
          "s_addc_u32 s49, s49, 0\n\t" \
          "Lqk_v%=:\n\t" \
          "s_memrealtime s[52:53]\n\t" \
          "s_waitcnt lgkmcnt(1)\n\t" \
          "s_sub_i32 vcc_lo, %[t], s50\n\t" \
          "s_cmp_gt_i32 vcc_lo, 3\n\t" \
          "s_cbranch_scc0 Lqk_x%=\n\t" \
          "s_memrealtime s[50:51]\n\t" \
          "s_waitcnt lgkmcnt(1)\n\t" \
          "s_sub_i32 vcc_lo, %[t], s52\n\t" \
          "s_cmp_gt_i32 vcc_lo, 3\n\t" \
          "s_cbranch_scc0 Lqk_x%=\n\t" \
          "s_memrealtime s[52:53]\n\t" \
          "s_waitcnt lgkmcnt(1)\n\t" \
          "s_sub_i32 vcc_lo, %[t], s50\n\t" \
          "s_cmp_gt_i32 vcc_lo, 3\n\t" \
          "s_cbranch_scc0 Lqk_x%=\n\t" \
          "s_memrealtime s[50:51]\n\t" \
          "s_waitcnt lgkmcnt(1)\n\t" \
          "s_sub_i32 vcc_lo, %[t], s52\n\t" \
          "s_cmp_gt_i32 vcc_lo, 3\n\t" \
          "s_cbranch_scc0 Lqk_x%=\n\t" \
          "s_memrealtime s[52:53]\n\t" \
          "s_waitcnt lgkmcnt(1)\n\t" \
          "s_sub_i32 vcc_lo, %[t], s50\n\t" \
          "s_cmp_gt_i32 vcc_lo, 3\n\t" \
          "s_cbranch_scc0 Lqk_x%=\n\t" \
          "s_memrealtime s[50:51]\n\t" \
          "s_waitcnt lgkmcnt(1)\n\t" \
          "s_sub_i32 vcc_lo, %[t], s52\n\t" \
          "s_cmp_gt_i32 vcc_lo, 3\n\t" \
          "s_cbranch_scc0 Lqk_x%=\n\t" \
          "s_memrealtime s[52:53]\n\t" \
          "s_waitcnt lgkmcnt(1)\n\t" \
          "s_sub_i32 vcc_lo, %[t], s50\n\t" \
          "s_cmp_gt_i32 vcc_lo, 3\n\t" \
          "s_cbranch_scc0 Lqk_x%=\n\t" \
          "s_memrealtime s[50:51]\n\t" \
          "s_waitcnt lgkmcnt(1)\n\t" \
          "s_sub_i32 vcc_lo, %[t], s52\n\t" \
          "s_cmp_gt_i32 vcc_lo, 3\n\t" \
          "s_cbranch_scc0 Lqk_x%=\n\t" \
          "s_branch Lqk_v%=\n\t" \
          "Lqk_x%=:\n\t" \
          "s_max_i32 vcc_lo, vcc_lo, 0\n\t" \
          "s_lshl_b32 vcc_lo, vcc_lo, 2\n\t" \
          "s_sub_u32 s48, s48, vcc_lo\n\t" \
          "s_subb_u32 s49, s49, 0\n\t" \
          "s_setpc_b64 s[48:49]\n\t" \
          "s_nop 5\n\t" \
          "s_nop 5\n\t" \
          "s_nop 5\n\t" \
          "Lqk_z%=:\n\t" \
          "s_barrier\n\t" \
          "buffer_load_dwordx4 v[136:139], %[ml0], %[rsm], %[mb0] offen sc1\n\t" \
          "s_nop 9\n\t" \
          "buffer_load_dwordx4 v[140:143], %[ml1], %[rsm], %[mb0] offen sc1\n\t" \
          "s_nop 9\n\t" \
          "buffer_load_dwordx4 v[144:147], %[ml0], %[rsm], %[mb1] offen sc1\n\t" \
          "s_nop 9\n\t" \
          "buffer_load_dwordx4 v[148:151], %[ml1], %[rsm], %[mb1] offen sc1\n\t" \
          "s_nop 9\n\t" \
          "buffer_load_dwordx4 v[152:155], %[ml0], %[rsm], %[mb2] offen sc1\n\t" \
          "s_nop 9\n\t" \
          "buffer_load_dwordx4 v[156:159], %[ml1], %[rsm], %[mb2] offen sc1\n\t" \
          "s_nop 9\n\t" \
          "buffer_load_dwordx4 v[160:163], %[ml0], %[rsm], %[mb3] offen sc1\n\t" \
          "s_nop 9\n\t" \
          "buffer_load_dwordx4 v[164:167], %[ml1], %[rsm], %[mb3] offen sc1\n\t" \
          "s_waitcnt vmcnt(7)\n\t" \
          "Lqk_go%=:\n\t" \
          "v_smfmac_f32_16x16x64_bf16 v[168:171], v[136:139], v[200:207], %[idx]\n\t" \
          "ds_read_b128 v[200:203], %[own] offset:2048\n\t" \
          "v_smfmac_f32_16x16x64_bf16 v[172:175], v[136:139], v[8:15], %[idx]\n\t" \
          "ds_read_b128 v[204:207], %[own] offset:3072\n\t" \
          "v_smfmac_f32_16x16x64_bf16 v[176:179], v[136:139], v[208:215], %[idx]\n\t" \
          "ds_read_b128 v[208:211], %[own] offset:28672\n\t" \
          "v_smfmac_f32_16x16x64_bf16 v[180:183], v[136:139], v[16:23], %[idx]\n\t" \
          "ds_read_b128 v[212:215], %[own] offset:29696\n\t" \
          "s_waitcnt vmcnt(6)\n\t" \
          "v_smfmac_f32_16x16x64_bf16 v[184:187], v[136:139], v[216:223], %[idx]\n\t" \
          "ds_read_b128 v[216:219], %[own] offset:30720\n\t" \
          "v_smfmac_f32_16x16x64_bf16 v[168:171], v[140:143], v[24:31], %[idx]\n\t" \
          "ds_read_b128 v[220:223], %[own] offset:31744\n\t" \
          "v_smfmac_f32_16x16x64_bf16 v[172:175], v[140:143], v[224:231], %[idx]\n\t" \
          "ds_read_b128 v[224:227], %[own] offset:10240\n\t" \
          "v_smfmac_f32_16x16x64_bf16 v[176:179], v[140:143], v[32:39], %[idx]\n\t" \
          "ds_read_b128 v[228:231], %[own] offset:11264\n\t" \
          "s_waitcnt lgkmcnt(6)\n\t" \
          "v_smfmac_f32_16x16x64_bf16 v[180:183], v[140:143], v[200:207], %[idx]\n\t" \
          "ds_read_b128 v[200:203], %[own] offset:24576\n\t" \
          "v_max3_u32 %[m], v136, v137, v138\n\t" \
          "v_smfmac_f32_16x16x64_bf16 v[184:187], v[140:143], v[40:47], %[idx]\n\t" \
          "ds_read_b128 v[204:207], %[own] offset:25600\n\t" \
          "s_waitcnt vmcnt(5) lgkmcnt(6)\n\t" \
          "v_smfmac_f32_16x16x64_bf16 v[168:171], v[144:147], v[208:215], %[idx]\n\t" \
          "ds_read_b128 v[208:211], %[own] offset:26624\n\t" \
          "v_max3_u32 %[m], v138, v139, %[m]\n\t" \
          "v_smfmac_f32_16x16x64_bf16 v[172:175], v[144:147], v[48:55], %[idx]\n\t" \
          "ds_read_b128 v[212:215], %[own] offset:27648\n\t" \
          "s_waitcnt lgkmcnt(6)\n\t" \
          "v_smfmac_f32_16x16x64_bf16 v[176:179], v[144:147], v[216:223], %[idx]\n\t" \
          "ds_read_b128 v[216:219], %[own] offset:20480\n\t" \
          "v_cmp_eq_u32 %[bad1], -1, %[m]\n\t" \
          "v_smfmac_f32_16x16x64_bf16 v[180:183], v[144:147], v[56:63], %[idx]\n\t" \
          "ds_read_b128 v[220:223], %[own] offset:21504\n\t" \
          "s_waitcnt vmcnt(4) lgkmcnt(6)\n\t" \
          "v_smfmac_f32_16x16x64_bf16 v[184:187], v[144:147], v[224:231], %[idx]\n\t" \
          "ds_read_b128 v[224:227], %[own] offset:22528\n\t" \
          "s_and_b64 %[bad1], %[bad1], %[en]\n\t" \
          "v_smfmac_f32_16x16x64_bf16 v[168:171], v[148:151], v[64:71], %[idx]\n\t" \
          "ds_read_b128 v[228:231], %[own] offset:23552\n\t" \
          "s_waitcnt lgkmcnt(6)\n\t" \
          "v_smfmac_f32_16x16x64_bf16 v[172:175], v[148:151], v[200:207], %[idx]\n\t" \
          "ds_read_b128 v[200:203], %[own] offset:12288\n\t" \
          "s_cbranch_scc1 Lqk_fail%=\n\t" \
          "v_smfmac_f32_16x16x64_bf16 v[176:179], v[148:151], v[72:79], %[idx]\n\t" \
          "ds_read_b128 v[204:207], %[own] offset:13312\n\t" \
          "s_waitcnt lgkmcnt(6)\n\t" \
          "v_smfmac_f32_16x16x64_bf16 v[180:183], v[148:151], v[208:215], %[idx]\n\t" \
          "ds_read_b128 v[208:211], %[own] offset:16384\n\t" \
          "v_max3_u32 %[m], v140, v141, %[m]\n\t" \
          "v_smfmac_f32_16x16x64_bf16 v[184:187], v[148:151], v[80:87], %[idx]\n\t" \
          "ds_read_b128 v[212:215], %[own] offset:17408\n\t" \
          "s_waitcnt vmcnt(3) lgkmcnt(6)\n\t" \
          "v_smfmac_f32_16x16x64_bf16 v[168:171], v[152:155], v[216:223], %[idx]\n\t" \
          "ds_read_b128 v[216:219], %[own] offset:18432\n\t" \
          "v_max3_u32 %[m], v142, v143, %[m]\n\t" \
          "v_smfmac_f32_16x16x64_bf16 v[172:175], v[152:155], v[88:95], %[idx]\n\t" \
          "ds_read_b128 v[220:223], %[own] offset:19456\n\t" \
          "s_waitcnt lgkmcnt(6)\n\t" \
          "v_smfmac_f32_16x16x64_bf16 v[176:179], v[152:155], v[224:231], %[idx]\n\t" \
          "ds_read_b128 v[224:227], %[own] offset:14336\n\t" \
          "v_max3_u32 %[m], v144, v145, %[m]\n\t" \
          "v_smfmac_f32_16x16x64_bf16 v[180:183], v[152:155], v[96:103], %[idx]\n\t" \
          "ds_read_b128 v[228:231], %[own] offset:15360\n\t" \
          "s_waitcnt vmcnt(2) lgkmcnt(6)\n\t" \
          "v_smfmac_f32_16x16x64_bf16 v[184:187], v[152:155], v[200:207], %[idx]\n\t" \
          "v_max3_u32 %[m], v146, v147, %[m]\n\t" \
          "v_smfmac_f32_16x16x64_bf16 v[168:171], v[156:159], v[104:111], %[idx]\n\t" \
          "s_waitcnt lgkmcnt(4)\n\t" \
          "v_max3_u32 %[m], v148, v149, %[m]\n\t" \
          "v_smfmac_f32_16x16x64_bf16 v[172:175], v[156:159], v[208:215], %[idx]\n\t" \
          "v_max3_u32 %[m], v150, v151, %[m]\n\t" \
          "v_smfmac_f32_16x16x64_bf16 v[176:179], v[156:159], v[112:119], %[idx]\n\t" \
          "s_waitcnt lgkmcnt(2)\n\t" \
          "v_max3_u32 %[m], v152, v153, %[m]\n\t" \
          "v_smfmac_f32_16x16x64_bf16 v[180:183], v[156:159], v[216:223], %[idx]\n\t" \
          "ds_read_b128 v[216:219], %[own] offset:32768\n\t" \
          "v_max3_u32 %[m], v154, v155, %[m]\n\t" \
          "v_smfmac_f32_16x16x64_bf16 v[184:187], v[156:159], v[120:127], %[idx]\n\t" \
          "ds_read_b128 v[220:223], %[own] offset:33792\n\t" \
          "s_waitcnt vmcnt(1) lgkmcnt(2)\n\t" \
          "v_smfmac_f32_16x16x64_bf16 v[168:171], v[160:163], v[224:231], %[idx]\n\t" \
          "ds_read_b64_tr_b16 v[224:225], %[vxp]\n\t" \
          "v_max3_u32 %[m], v156, v157, %[m]\n\t" \
          "v_smfmac_f32_16x16x64_bf16 v[172:175], v[160:163], v[128:135], %[idx]\n\t" \
          "ds_read_b64_tr_b16 v[226:227], %[vxp] offset:1024\n\t" \
          "v_max3_u32 %[m], v158, v159, %[m]\n\t" \
          "v_smfmac_f32_16x16x64_bf16 v[176:179], v[160:163], a[0:7], %[idx]\n\t" \
          "ds_read_b64_tr_b16 v[228:229], %[vxp] offset:8\n\t" \
          "v_max3_u32 %[m], v160, v161, %[m]\n\t" \
          "v_smfmac_f32_16x16x64_bf16 v[180:183], v[160:163], a[8:15], %[idx]\n\t" \
          "ds_read_b64_tr_b16 v[230:231], %[vxp] offset:1032\n\t" \
          "v_smfmac_f32_16x16x64_bf16 v[184:187], v[160:163], a[16:23], %[idx]\n\t" \
          "s_waitcnt vmcnt(0)\n\t" \
          "v_max3_u32 %[m], v162, v163, %[m]\n\t" \
          "v_smfmac_f32_16x16x64_bf16 v[168:171], v[164:167], a[24:31], %[idx]\n\t" \
          "v_max3_u32 %[m], v164, v165, %[m]\n\t" \
          "v_smfmac_f32_16x16x64_bf16 v[172:175], v[164:167], a[32:39], %[idx]\n\t" \
          "v_max3_u32 %[m], v166, v167, %[m]\n\t" \
          "v_smfmac_f32_16x16x64_bf16 v[176:179], v[164:167], a[40:47], %[idx]\n\t" \
          "v_cvt_pk_bf16_f32 v200, v168, v169\n\t" \
          "v_cvt_pk_bf16_f32 v201, v170, v171\n\t" \
          "v_smfmac_f32_16x16x64_bf16 v[180:183], v[164:167], a[48:55], %[idx]\n\t" \
          "ds_write_b64 %[own], v[200:201] offset:36864\n\t" \
          "v_cvt_pk_bf16_f32 v202, v172, v173\n\t" \
          "v_cvt_pk_bf16_f32 v203, v174, v175\n\t" \
          "v_smfmac_f32_16x16x64_bf16 v[184:187], v[164:167], a[56:63], %[idx]\n\t" \
          "ds_write_b64 %[own], v[202:203] offset:36872\n\t" \
          "v_smfmac_f32_16x16x64_bf16 v[188:191], v[136:139], a[64:71], %[idx]\n\t" \
          "v_cmp_eq_u32 %[bad1], -1, %[m]\n\t" \
          "v_smfmac_f32_16x16x64_bf16 v[188:191], v[140:143], a[72:79], %[idx]\n\t" \
          "buffer_store_dword %[al], %[ho], %[rsm], %[rb] offen sc1\n\t" \
          "v_cvt_pk_bf16_f32 v208, v176, v177\n\t" \
          "v_smfmac_f32_16x16x64_bf16 v[188:191], v[144:147], a[80:87], %[idx]\n\t" \
          "s_and_b64 %[bad1], %[bad1], %[en]\n\t" \
          "s_cbranch_scc1 Lqk_fail%=\n\t" \
          "s_waitcnt lgkmcnt(0)\n\t" \
          "s_barrier\n\t" \
          "v_cvt_pk_bf16_f32 v209, v178, v179\n\t" \
          "v_smfmac_f32_16x16x64_bf16 v[188:191], v[148:151], a[88:95], %[idx]\n\t" \
          "ds_read_b128 v[210:213], %[p1] offset:36864\n\t" \
          "v_cvt_pk_bf16_f32 v214, v180, v181\n\t" \
          "v_smfmac_f32_16x16x64_bf16 v[188:191], v[152:155], a[96:103], %[idx]\n\t" \
          "buffer_store_dword %[al], %[ho], %[rsm], %[rb] offen offset:256 sc1\n\t" \
          "v_cvt_pk_bf16_f32 v215, v182, v183\n\t" \
          "v_smfmac_f32_16x16x64_bf16 v[188:191], v[156:159], a[104:111], %[idx]\n\t" \
          "v_cvt_pk_bf16_f32 v204, v184, v185\n\t" \
          "v_smfmac_f32_16x16x64_bf16 v[188:191], v[160:163], a[112:119], %[idx]\n\t" \
          "v_cvt_pk_bf16_f32 v205, v186, v187\n\t" \
          "v_smfmac_f32_16x16x64_bf16 v[188:191], v[164:167], a[120:127], %[idx]\n\t" \
          "s_waitcnt lgkmcnt(0)\n\t" \
          "v_mfma_f32_16x16x32_bf16 v[168:171], v[208:211], %[jk], 0\n\t" \
          "v_smfmac_f32_16x16x64_bf16 v[192:195], v[136:139], a[128:135], %[idx]\n\t" \
          "v_mfma_f32_16x16x32_bf16 v[172:175], v[212:215], %[jk], 0\n\t" \
          "v_cvt_pk_bf16_f32 v176, v168, v169\n\t" \
          "v_cvt_pk_bf16_f32 v177, v170, v171\n\t" \
          "v_smfmac_f32_16x16x64_bf16 v[196:199], v[136:139], a[136:143], %[idx]\n\t" \
          "ds_write_b64 %[own], v[204:205] offset:37888\n\t" \
          "v_cvt_pk_bf16_f32 v206, v188, v189\n\t" \
          "v_smfmac_f32_16x16x64_bf16 v[192:195], v[140:143], a[144:151], %[idx]\n\t" \
          "v_cvt_pk_bf16_f32 v178, v172, v173\n\t" \
          "v_cvt_pk_bf16_f32 v179, v174, v175\n\t" \
          "v_smfmac_f32_16x16x64_bf16 v[196:199], v[140:143], a[152:159], %[idx]\n\t" \
          "v_cvt_pk_bf16_f32 v207, v190, v191\n\t" \
          "v_mfma_f32_16x16x32_bf16 v[208:211], v[216:219], v[176:179], 0\n\t" \
          "v_mov_b32 v168, %[one]\n\t" \
          "v_mov_b32 v169, %[one]\n\t" \
          "v_mfma_f32_16x16x32_bf16 v[212:215], v[220:223], v[176:179], 0\n\t" \
          "v_mov_b32 v170, %[one]\n\t" \
          "v_mov_b32 v171, %[one]\n\t" \
          "v_smfmac_f32_16x16x64_bf16 v[192:195], v[144:147], a[160:167], %[idx]\n\t" \
          "ds_write_b64 %[own], v[206:207] offset:37896\n\t" \
          "v_smfmac_f32_16x16x64_bf16 v[196:199], v[144:147], a[168:175], %[idx]\n\t" \
          "v_cndmask_b32 v208, v208, v212, %[twin]\n\t" \
          "v_cndmask_b32 v209, v209, v213, %[twin]\n\t" \
          "v_smfmac_f32_16x16x64_bf16 v[192:195], v[148:151], a[176:183], %[idx]\n\t" \
          "ds_write_b64 %[own], v[208:209] offset:38912\n\t" \
          "v_cndmask_b32 v210, v210, v214, %[twin]\n\t" \
          "v_cndmask_b32 v211, v211, v215, %[twin]\n\t" \
          "v_smfmac_f32_16x16x64_bf16 v[196:199], v[148:151], a[184:191], %[idx]\n\t" \
          "ds_write_b64 %[own], v[210:211] offset:38920\n\t" \
          "v_smfmac_f32_16x16x64_bf16 v[192:195], v[152:155], a[192:199], %[idx]\n\t" \
          "v_mul_f32 v212, %[qs], v208\n\t" \
          "v_smfmac_f32_16x16x64_bf16 v[196:199], v[152:155], a[200:207], %[idx]\n\t" \
          "buffer_load_dword v232, %[ho], %[rsm], %[vb] offen sc1\n\t" \
          "v_mul_f32 v213, %[qs], v209\n\t" \
          "v_smfmac_f32_16x16x64_bf16 v[192:195], v[156:159], a[208:215], %[idx]\n\t" \
          "buffer_load_dword v234, %[ho], %[rsm], %[vb] offen offset:256 sc1\n\t" \
          "v_mul_f32 v214, %[qs], v210\n\t" \
          "v_smfmac_f32_16x16x64_bf16 v[196:199], v[156:159], a[216:223], %[idx]\n\t" \
          "s_waitcnt lgkmcnt(0)\n\t" \
          "s_barrier\n\t" \
          "v_mul_f32 v215, %[qs], v211\n\t" \
          "v_smfmac_f32_16x16x64_bf16 v[192:195], v[160:163], a[224:231], %[idx]\n\t" \
          "ds_read_b128 v[138:141], %[p1] offset:37888\n\t" \
          "v_smfmac_f32_16x16x64_bf16 v[196:199], v[160:163], a[232:239], %[idx]\n\t" \
          "ds_read_b128 v[216:219], %[p1] offset:38912\n\t" \
          "v_smfmac_f32_16x16x64_bf16 v[192:195], v[164:167], a[240:247], %[idx]\n\t" \
          "ds_read_b128 v[220:223], %[p2] offset:38912\n\t" \
          "v_smfmac_f32_16x16x64_bf16 v[196:199], v[164:167], a[248:255], %[idx]\n\t" \
          "ds_read_b128 v[200:203], %[p3] offset:38912\n\t" \
          "s_nop 0\n\t" \
          "s_waitcnt lgkmcnt(2)\n\t" \
          "v_cvt_pk_bf16_f32 v136, v192, v193\n\t" \
          "v_cvt_pk_bf16_f32 v137, v194, v195\n\t" \
          "v_fmac_f32 v212, %[qs], v216\n\t" \
          "v_fmac_f32 v213, %[qs], v217\n\t" \
          "v_cvt_pk_bf16_f32 v142, v196, v197\n\t" \
          "v_cvt_pk_bf16_f32 v143, v198, v199\n\t" \
          "v_mfma_f32_16x16x32_bf16 v[144:147], v[136:139], %[jk], 0\n\t" \
          "v_fmac_f32 v214, %[qs], v218\n\t" \
          "v_mfma_f32_16x16x32_bf16 v[148:151], v[140:143], %[jk], 0\n\t" \
          "v_fmac_f32 v215, %[qs], v219\n\t" \
          "s_waitcnt lgkmcnt(0)\n\t" \
          "v_fmac_f32 v212, %[qs], v220\n\t" \
          "v_fmac_f32 v213, %[qs], v221\n\t" \
          "v_cvt_pk_bf16_f32 v152, v144, v145\n\t" \
          "v_cvt_pk_bf16_f32 v153, v146, v147\n\t" \
          "v_fmac_f32 v214, %[qs], v222\n\t" \
          "v_fmac_f32 v215, %[qs], v223\n\t" \
          "v_cvt_pk_bf16_f32 v154, v148, v149\n\t" \
          "v_cvt_pk_bf16_f32 v155, v150, v151\n\t" \
          "v_pk_fma_f32 v[212:213], v[200:201], %[qs2], v[212:213] op_sel_hi:[1,0,1]\n\t" \
          "v_pk_fma_f32 v[214:215], v[202:203], %[qs2], v[214:215] op_sel_hi:[1,0,1]\n\t" \
          "v_mfma_f32_16x16x32_bf16 v[156:159], v[152:155], v[176:179], 0\n\t" \
          "v_exp_f32 v180, v212\n\t" \
          "v_mfma_f32_16x16x32_bf16 v[196:199], v[168:171], v[168:171], 0\n\t" \
          "v_exp_f32 v181, v213\n\t" \
          "v_mfma_f32_16x16x32_bf16 v[196:199], v[168:171], v[168:171], 0\n\t" \
          "ds_write_b64 %[own], v[156:157] offset:39936\n\t" \
          "v_exp_f32 v182, v214\n\t" \
          "v_mov_b32 v137, 0\n\t" \
          "v_exp_f32 v183, v215\n\t" \
          "v_cvt_pk_bf16_f32 v172, v180, v181\n\t" \
          "v_cvt_pk_bf16_f32 v173, v182, v183\n\t" \
          "v_pk_mul_f32 v[160:161], v[156:157], %[qs2] op_sel_hi:[1,0]\n\t" \
          "v_mov_b32_dpp v174, v172 quad_perm:[2,3,0,1] row_mask:0xf bank_mask:0xf\n\t" \
          "v_mov_b32_dpp v175, v173 quad_perm:[2,3,0,1] row_mask:0xf bank_mask:0xf\n\t" \
          "s_nop 0\n\t" \
          "s_waitcnt lgkmcnt(0)\n\t" \
          "v_mfma_f32_16x16x32_bf16 v[184:187], v[172:175], v[168:171], 0\n\t" \
          "s_barrier\n\t" \
          "ds_read_b64 v[144:145], %[p1] offset:39936\n\t" \
          "ds_read_b64 v[146:147], %[p2] offset:39936\n\t" \
          "v_mfma_f32_16x16x32_bf16 v[188:191], v[172:175], v[224:227], 0\n\t" \
          "ds_read_b64 v[148:149], %[p3] offset:39936\n\t" \
          "v_mfma_f32_16x16x32_bf16 v[192:195], v[172:175], v[228:231], 0\n\t" \
          "s_waitcnt lgkmcnt(2)\n\t" \
          "v_fmac_f32 v160, %[qs], v144\n\t" \
          "v_fmac_f32 v161, %[qs], v145\n\t" \
          "s_waitcnt lgkmcnt(1)\n\t" \
          "v_fmac_f32 v160, %[qs], v146\n\t" \
          "v_fmac_f32 v161, %[qs], v147\n\t" \
          "s_waitcnt lgkmcnt(0)\n\t" \
          "v_fmac_f32 v160, %[qs], v148\n\t" \
          "v_fmac_f32 v161, %[qs], v149\n\t" \
          "v_exp_f32 v162, v160\n\t" \
          "v_exp_f32 v163, v161\n\t" \
          "v_max_f32 v182, v160, v161\n\t" \
          "v_cvt_pk_bf16_f32 v136, v162, v163\n\t" \
          "v_cmp_nge_f32 vcc, %[th], v182\n\t" \
          "s_waitcnt vmcnt(0)\n\t" \
          "v_mfma_f32_16x16x16_bf16 v[164:167], v[136:137], v[168:169], v[184:187]\n\t" \
          "v_max_f32 v180, v184, v185\n\t" \
          "v_min_f32 v181, v184, v185\n\t" \
          "v_mfma_f32_16x16x16_bf16 v[188:191], v[136:137], v[232:233], v[188:191]\n\t" \
          "v_cmp_nge_f32 %[b2], %[hi], v180\n\t" \
          "v_cmp_nle_f32 %[b3], %[lo], v181\n\t" \
          "v_mfma_f32_16x16x16_bf16 v[192:195], v[136:137], v[234:235], v[192:195]\n\t" \
          "v_max_u32 v183, v232, v234\n\t" \
          "v_cmp_eq_u32 %[bad], -1, v183\n\t" \
          "v_rcp_f32 v162, v164\n\t" \
          "s_or_b64 %[b2], %[b2], %[b3]\n\t" \
          "v_rcp_f32 v163, v165\n\t" \
          "s_or_b64 %[b2], %[b2], vcc\n\t" \
          "v_pk_mul_f32 v[156:157], v[188:189], v[162:163]\n\t" \
          "s_or_b64 %[bad], %[bad], %[b2]\n\t" \
          "s_cselect_b64 exec, 0, -1\n\t" \
          "v_pk_mul_f32 v[158:159], v[192:193], v[162:163]\n\t" \
          "s_nop 0\n\t" \
          "v_cvt_pk_bf16_f32 v156, v156, v158\n\t" \
          "v_cvt_pk_bf16_f32 v157, v157, v159\n\t" \
          "v_min_u32 v156, -2, v156\n\t" \
          "v_min_u32 v157, -2, v157\n\t" \
          "buffer_store_dword v156, %[ho], %[rsm], %[ob] offen sc1\n\t" \
          "buffer_store_dword v157, %[ho], %[rsm], %[ob] offen offset:256 sc1\n\t" \
          "s_memrealtime %[po]\n\t" \
          "s_mov_b64 exec, -1\n\t" \
          "s_sleep 1\n\t" \
          "buffer_load_dword %[al], %[alo], %[rsm], %[mb] offen sc1\n\t" \
          "s_waitcnt lgkmcnt(0)\n\t"
constexpr int QK_SX = 12, QK_SM = 24, QK_KM = 16, QK_VX = 24, QK_KV = 32768, QK_EX = 36864;
#define VO_BODY \
          "s_mov_b64 %[en], -1\n\t" \
          "s_cmp_eq_u32 %[pw], 0\n\t" \
          "s_cbranch_scc1 Lo_c%=\n\t" \
          "v_mov_b64 v[224:225], 0\n\t" \
          "v_mov_b64 v[226:227], 0\n\t" \
          "v_mov_b64 v[228:229], 0\n\t" \
          "v_mov_b64 v[230:231], 0\n\t" \
          "v_mov_b64 v[232:233], 0\n\t" \
          "v_mov_b64 v[234:235], 0\n\t" \
          "v_mov_b64 v[236:237], 0\n\t" \
          "v_mov_b64 v[238:239], 0\n\t" \
          "v_mov_b64 v[176:177], 0\n\t" \
          "v_mov_b64 v[178:179], 0\n\t" \
          "v_mov_b64 v[180:181], 0\n\t" \
          "v_mov_b64 v[182:183], 0\n\t" \
          "v_mov_b64 v[184:185], 0\n\t" \
          "v_mov_b64 v[186:187], 0\n\t" \
          "v_mov_b64 v[188:189], 0\n\t" \
          "v_mov_b64 v[190:191], 0\n\t" \
          "s_cmp_eq_u32 %[s1], 0\n\t" \
          "s_cbranch_scc1 Lv_n%=\n\t" \
          "s_branch Lo_z%=\n\t" \
          "Lo_f0_%=:\n\t" \
          "buffer_load_dwordx4 v[192:195], %[oo0], %[rsm], %[ob] offen sc1\n\t" \
          "Lo_f1_%=:\n\t" \
          "buffer_load_dwordx4 v[196:199], %[oo1], %[rsm], %[ob] offen sc1\n\t" \
          "Lo_f2_%=:\n\t" \
          "buffer_load_dwordx4 v[200:203], %[oo2], %[rsm], %[ob] offen sc1\n\t" \
          "Lo_f3_%=:\n\t" \
          "buffer_load_dwordx4 v[204:207], %[oo3], %[rsm], %[ob] offen sc1\n\t" \
          "Lo_f4_%=:\n\t" \
          "buffer_load_dwordx4 v[208:211], %[oo4], %[rsm], %[ob] offen sc1\n\t" \
          "Lo_f5_%=:\n\t" \
          "buffer_load_dwordx4 v[212:215], %[oo5], %[rsm], %[ob] offen sc1\n\t" \
          "Lo_f6_%=:\n\t" \
          "buffer_load_dwordx4 v[216:219], %[oo6], %[rsm], %[ob] offen sc1\n\t" \
          "Lo_f7_%=:\n\t" \
          "buffer_load_dwordx4 v[220:223], %[oo7], %[rsm], %[ob] offen sc1\n\t" \
          "v_mov_b32 v242, 0x10000\n\t" \
          "s_mov_b64 exec, %[alx]\n\t" \
          "buffer_store_dword v242, %[l4], %[rsm], %[als] offen sc1\n\t" \
          "s_mov_b64 exec, -1\n\t" \
          "v_mov_b32 v243, %[s1]\n\t" \
          "ds_write_b32 %[fa], v243\n\t" \
          "v_mov_b64 v[224:225], 0\n\t" \
          "v_mov_b64 v[226:227], 0\n\t" \
          "v_mov_b64 v[228:229], 0\n\t" \
          "v_mov_b64 v[230:231], 0\n\t" \
          "v_mov_b64 v[232:233], 0\n\t" \
          "v_mov_b64 v[234:235], 0\n\t" \
          "v_mov_b64 v[236:237], 0\n\t" \
          "v_mov_b64 v[238:239], 0\n\t" \
          "s_nop 1\n\t" \
          "s_mov_b32 %[cnt], 0\n\t" \
          "s_waitcnt vmcnt(7)\n\t" \
          "v_max3_u32 v240, v192, v193, v194\n\t" \
          "v_max_u32 v240, v240, v195\n\t" \
          "v_cmp_eq_u32 vcc, -1, v240\n\t" \
          "s_cbranch_vccnz Lo_p0_%=\n\t" \
          "Lo_k0_%=:\n\t" \
          "v_smfmac_f32_16x16x64_bf16 v[224:227], v[192:195], a[0:7], %[idx]\n\t" \
          "v_smfmac_f32_16x16x64_bf16 v[228:231], v[192:195], a[8:15], %[idx]\n\t" \
          "v_smfmac_f32_16x16x64_bf16 v[232:235], v[192:195], a[16:23], %[idx]\n\t" \
          "v_smfmac_f32_16x16x64_bf16 v[236:239], v[192:195], a[24:31], %[idx]\n\t" \
          "s_waitcnt vmcnt(6)\n\t" \
          "v_max3_u32 v240, v196, v197, v198\n\t" \
          "v_max_u32 v240, v240, v199\n\t" \
          "v_cmp_eq_u32 vcc, -1, v240\n\t" \
          "s_cbranch_vccnz Lo_p1_%=\n\t" \
          "Lo_k1_%=:\n\t" \
          "v_smfmac_f32_16x16x64_bf16 v[224:227], v[196:199], a[32:39], %[idx]\n\t" \
          "v_smfmac_f32_16x16x64_bf16 v[228:231], v[196:199], a[40:47], %[idx]\n\t" \
          "v_smfmac_f32_16x16x64_bf16 v[232:235], v[196:199], a[48:55], %[idx]\n\t" \
          "v_smfmac_f32_16x16x64_bf16 v[236:239], v[196:199], a[56:63], %[idx]\n\t" \
          "s_waitcnt vmcnt(5)\n\t" \
          "v_max3_u32 v240, v200, v201, v202\n\t" \
          "v_max_u32 v240, v240, v203\n\t" \
          "v_cmp_eq_u32 vcc, -1, v240\n\t" \
          "s_cbranch_vccnz Lo_p2_%=\n\t" \
          "Lo_k2_%=:\n\t" \
          "v_smfmac_f32_16x16x64_bf16 v[224:227], v[200:203], a[64:71], %[idx]\n\t" \
          "v_smfmac_f32_16x16x64_bf16 v[228:231], v[200:203], a[72:79], %[idx]\n\t" \
          "v_smfmac_f32_16x16x64_bf16 v[232:235], v[200:203], a[80:87], %[idx]\n\t" \
          "v_smfmac_f32_16x16x64_bf16 v[236:239], v[200:203], a[88:95], %[idx]\n\t" \
          "s_waitcnt vmcnt(4)\n\t" \
          "v_max3_u32 v240, v204, v205, v206\n\t" \
          "v_max_u32 v240, v240, v207\n\t" \
          "v_cmp_eq_u32 vcc, -1, v240\n\t" \
          "s_cbranch_vccnz Lo_p3_%=\n\t" \
          "Lo_k3_%=:\n\t" \
          "v_smfmac_f32_16x16x64_bf16 v[224:227], v[204:207], a[96:103], %[idx]\n\t" \
          "v_smfmac_f32_16x16x64_bf16 v[228:231], v[204:207], a[104:111], %[idx]\n\t" \
          "v_smfmac_f32_16x16x64_bf16 v[232:235], v[204:207], a[112:119], %[idx]\n\t" \
          "v_smfmac_f32_16x16x64_bf16 v[236:239], v[204:207], a[120:127], %[idx]\n\t" \
          "s_waitcnt vmcnt(3)\n\t" \
          "v_max3_u32 v240, v208, v209, v210\n\t" \
          "v_max_u32 v240, v240, v211\n\t" \
          "v_cmp_eq_u32 vcc, -1, v240\n\t" \
          "s_cbranch_vccnz Lo_p4_%=\n\t" \
          "Lo_k4_%=:\n\t" \
          "v_smfmac_f32_16x16x64_bf16 v[224:227], v[208:211], a[128:135], %[idx]\n\t" \
          "v_smfmac_f32_16x16x64_bf16 v[228:231], v[208:211], a[136:143], %[idx]\n\t" \
          "v_smfmac_f32_16x16x64_bf16 v[232:235], v[208:211], a[144:151], %[idx]\n\t" \
          "v_smfmac_f32_16x16x64_bf16 v[236:239], v[208:211], a[152:159], %[idx]\n\t" \
          "s_waitcnt vmcnt(2)\n\t" \
          "v_max3_u32 v240, v212, v213, v214\n\t" \
          "v_max_u32 v240, v240, v215\n\t" \
          "v_cmp_eq_u32 vcc, -1, v240\n\t" \
          "s_cbranch_vccnz Lo_p5_%=\n\t" \
          "Lo_k5_%=:\n\t" \
          "v_smfmac_f32_16x16x64_bf16 v[224:227], v[212:215], a[160:167], %[idx]\n\t" \
          "v_smfmac_f32_16x16x64_bf16 v[228:231], v[212:215], a[168:175], %[idx]\n\t" \
          "v_smfmac_f32_16x16x64_bf16 v[232:235], v[212:215], a[176:183], %[idx]\n\t" \
          "v_smfmac_f32_16x16x64_bf16 v[236:239], v[212:215], a[184:191], %[idx]\n\t" \
          "s_waitcnt vmcnt(1)\n\t" \
          "v_max3_u32 v240, v216, v217, v218\n\t" \
          "v_max_u32 v240, v240, v219\n\t" \
          "v_cmp_eq_u32 vcc, -1, v240\n\t" \
          "s_cbranch_vccnz Lo_p6_%=\n\t" \
          "Lo_k6_%=:\n\t" \
          "v_smfmac_f32_16x16x64_bf16 v[224:227], v[216:219], a[192:199], %[idx]\n\t" \
          "v_smfmac_f32_16x16x64_bf16 v[228:231], v[216:219], a[200:207], %[idx]\n\t" \
          "v_smfmac_f32_16x16x64_bf16 v[232:235], v[216:219], a[208:215], %[idx]\n\t" \
          "v_smfmac_f32_16x16x64_bf16 v[236:239], v[216:219], a[216:223], %[idx]\n\t" \
          "s_waitcnt vmcnt(0)\n\t" \
          "v_max3_u32 v240, v220, v221, v222\n\t" \
          "v_max_u32 v240, v240, v223\n\t" \
          "v_cmp_eq_u32 vcc, -1, v240\n\t" \
          "s_cbranch_vccnz Lo_p7_%=\n\t" \
          "Lo_k7_%=:\n\t" \
          "v_smfmac_f32_16x16x64_bf16 v[224:227], v[220:223], a[224:231], %[idx]\n\t" \
          "v_smfmac_f32_16x16x64_bf16 v[228:231], v[220:223], a[232:239], %[idx]\n\t" \
          "v_smfmac_f32_16x16x64_bf16 v[232:235], v[220:223], a[240:247], %[idx]\n\t" \
          "v_smfmac_f32_16x16x64_bf16 v[236:239], v[220:223], a[248:255], %[idx]\n\t" \
          "s_nop 7\n\t" \
          "v_add_f32 v242, v224, v225\n\t" \
          "v_add_f32 v244, v234, v235\n\t" \
          "s_cmp_lg_u32 %[w], 0\n\t" \
          "s_waitcnt lgkmcnt(0)\n\t" \
          "s_branch Lo_tail%=\n\t" \
          "Lo_p0_%=:\n\t" \
          "s_cmp_lt_i32 %[cnt], 0\n\t" \
          "s_cbranch_scc1 Lo_k0_%=\n\t" \
          "s_add_u32 %[cnt], %[cnt], 1\n\t" \
          "s_and_b32 vcc_lo, %[cnt], 3\n\t" \
          "s_cbranch_scc1 Lo_q0_%=\n\t" \
          "s_getpc_b64 s[48:49]\n\t" \
          "s_branch Lo_spin%=\n\t" \
          "Lo_q0_%=:\n\t" \
          "buffer_load_dwordx4 v[192:195], %[oo0], %[rsm], %[ob] offen sc1\n\t" \
          "s_waitcnt vmcnt(0)\n\t" \
          "v_max3_u32 v240, v192, v193, v194\n\t" \
          "v_max_u32 v240, v240, v195\n\t" \
          "v_cmp_eq_u32 vcc, -1, v240\n\t" \
          "s_cbranch_vccnz Lo_p0_%=\n\t" \
          "buffer_load_dwordx4 v[196:199], %[oo1], %[rsm], %[ob] offen sc1\n\t" \
          "buffer_load_dwordx4 v[200:203], %[oo2], %[rsm], %[ob] offen sc1\n\t" \
          "buffer_load_dwordx4 v[204:207], %[oo3], %[rsm], %[ob] offen sc1\n\t" \
          "buffer_load_dwordx4 v[208:211], %[oo4], %[rsm], %[ob] offen sc1\n\t" \
          "buffer_load_dwordx4 v[212:215], %[oo5], %[rsm], %[ob] offen sc1\n\t" \
          "buffer_load_dwordx4 v[216:219], %[oo6], %[rsm], %[ob] offen sc1\n\t" \
          "buffer_load_dwordx4 v[220:223], %[oo7], %[rsm], %[ob] offen sc1\n\t" \
          "s_branch Lo_k0_%=\n\t" \
          "Lo_p1_%=:\n\t" \
          "s_cmp_lt_i32 %[cnt], 0\n\t" \
          "s_cbranch_scc1 Lo_k1_%=\n\t" \
          "s_add_u32 %[cnt], %[cnt], 1\n\t" \
          "s_and_b32 vcc_lo, %[cnt], 3\n\t" \
          "s_cbranch_scc1 Lo_q1_%=\n\t" \
          "s_getpc_b64 s[48:49]\n\t" \
          "s_branch Lo_spin%=\n\t" \
          "Lo_q1_%=:\n\t" \
          "buffer_load_dwordx4 v[196:199], %[oo1], %[rsm], %[ob] offen sc1\n\t" \
          "s_waitcnt vmcnt(0)\n\t" \
          "v_max3_u32 v240, v196, v197, v198\n\t" \
          "v_max_u32 v240, v240, v199\n\t" \
          "v_cmp_eq_u32 vcc, -1, v240\n\t" \
          "s_cbranch_vccnz Lo_p1_%=\n\t" \
          "buffer_load_dwordx4 v[200:203], %[oo2], %[rsm], %[ob] offen sc1\n\t" \
          "buffer_load_dwordx4 v[204:207], %[oo3], %[rsm], %[ob] offen sc1\n\t" \
          "buffer_load_dwordx4 v[208:211], %[oo4], %[rsm], %[ob] offen sc1\n\t" \
          "buffer_load_dwordx4 v[212:215], %[oo5], %[rsm], %[ob] offen sc1\n\t" \
          "buffer_load_dwordx4 v[216:219], %[oo6], %[rsm], %[ob] offen sc1\n\t" \
          "buffer_load_dwordx4 v[220:223], %[oo7], %[rsm], %[ob] offen sc1\n\t" \
          "s_branch Lo_k1_%=\n\t" \
          "Lo_p2_%=:\n\t" \
          "s_cmp_lt_i32 %[cnt], 0\n\t" \
          "s_cbranch_scc1 Lo_k2_%=\n\t" \
          "s_add_u32 %[cnt], %[cnt], 1\n\t" \
          "s_and_b32 vcc_lo, %[cnt], 3\n\t" \
          "s_cbranch_scc1 Lo_q2_%=\n\t" \
          "s_getpc_b64 s[48:49]\n\t" \
          "s_branch Lo_spin%=\n\t" \
          "Lo_q2_%=:\n\t" \
          "buffer_load_dwordx4 v[200:203], %[oo2], %[rsm], %[ob] offen sc1\n\t" \
          "s_waitcnt vmcnt(0)\n\t" \
          "v_max3_u32 v240, v200, v201, v202\n\t" \
          "v_max_u32 v240, v240, v203\n\t" \
          "v_cmp_eq_u32 vcc, -1, v240\n\t" \
          "s_cbranch_vccnz Lo_p2_%=\n\t" \
          "buffer_load_dwordx4 v[204:207], %[oo3], %[rsm], %[ob] offen sc1\n\t" \
          "buffer_load_dwordx4 v[208:211], %[oo4], %[rsm], %[ob] offen sc1\n\t" \
          "buffer_load_dwordx4 v[212:215], %[oo5], %[rsm], %[ob] offen sc1\n\t" \
          "buffer_load_dwordx4 v[216:219], %[oo6], %[rsm], %[ob] offen sc1\n\t" \
          "buffer_load_dwordx4 v[220:223], %[oo7], %[rsm], %[ob] offen sc1\n\t" \
          "s_branch Lo_k2_%=\n\t" \
          "Lo_p3_%=:\n\t" \
          "s_cmp_lt_i32 %[cnt], 0\n\t" \
          "s_cbranch_scc1 Lo_k3_%=\n\t" \
          "s_add_u32 %[cnt], %[cnt], 1\n\t" \
          "s_and_b32 vcc_lo, %[cnt], 3\n\t" \
          "s_cbranch_scc1 Lo_q3_%=\n\t" \
          "s_getpc_b64 s[48:49]\n\t" \
          "s_branch Lo_spin%=\n\t" \
          "Lo_q3_%=:\n\t" \
          "buffer_load_dwordx4 v[204:207], %[oo3], %[rsm], %[ob] offen sc1\n\t" \
          "s_waitcnt vmcnt(0)\n\t" \
          "v_max3_u32 v240, v204, v205, v206\n\t" \
          "v_max_u32 v240, v240, v207\n\t" \
          "v_cmp_eq_u32 vcc, -1, v240\n\t" \
          "s_cbranch_vccnz Lo_p3_%=\n\t" \
          "buffer_load_dwordx4 v[208:211], %[oo4], %[rsm], %[ob] offen sc1\n\t" \
          "buffer_load_dwordx4 v[212:215], %[oo5], %[rsm], %[ob] offen sc1\n\t" \
          "buffer_load_dwordx4 v[216:219], %[oo6], %[rsm], %[ob] offen sc1\n\t" \
          "buffer_load_dwordx4 v[220:223], %[oo7], %[rsm], %[ob] offen sc1\n\t" \
          "s_branch Lo_k3_%=\n\t" \
          "Lo_p4_%=:\n\t" \
          "s_cmp_lt_i32 %[cnt], 0\n\t" \
          "s_cbranch_scc1 Lo_k4_%=\n\t" \
          "s_add_u32 %[cnt], %[cnt], 1\n\t" \
          "s_and_b32 vcc_lo, %[cnt], 3\n\t" \
          "s_cbranch_scc1 Lo_q4_%=\n\t" \
          "s_getpc_b64 s[48:49]\n\t" \
          "s_branch Lo_spin%=\n\t" \
          "Lo_q4_%=:\n\t" \
          "buffer_load_dwordx4 v[208:211], %[oo4], %[rsm], %[ob] offen sc1\n\t" \
          "s_waitcnt vmcnt(0)\n\t" \
          "v_max3_u32 v240, v208, v209, v210\n\t" \
          "v_max_u32 v240, v240, v211\n\t" \
          "v_cmp_eq_u32 vcc, -1, v240\n\t" \
          "s_cbranch_vccnz Lo_p4_%=\n\t" \
          "buffer_load_dwordx4 v[212:215], %[oo5], %[rsm], %[ob] offen sc1\n\t" \
          "buffer_load_dwordx4 v[216:219], %[oo6], %[rsm], %[ob] offen sc1\n\t" \
          "buffer_load_dwordx4 v[220:223], %[oo7], %[rsm], %[ob] offen sc1\n\t" \
          "s_branch Lo_k4_%=\n\t" \
          "Lo_p5_%=:\n\t" \
          "s_cmp_lt_i32 %[cnt], 0\n\t" \
          "s_cbranch_scc1 Lo_k5_%=\n\t" \
          "s_add_u32 %[cnt], %[cnt], 1\n\t" \
          "s_and_b32 vcc_lo, %[cnt], 3\n\t" \
          "s_cbranch_scc1 Lo_q5_%=\n\t" \
          "s_getpc_b64 s[48:49]\n\t" \
          "s_branch Lo_spin%=\n\t" \
          "Lo_q5_%=:\n\t" \
          "buffer_load_dwordx4 v[212:215], %[oo5], %[rsm], %[ob] offen sc1\n\t" \
          "s_waitcnt vmcnt(0)\n\t" \
          "v_max3_u32 v240, v212, v213, v214\n\t" \
          "v_max_u32 v240, v240, v215\n\t" \
          "v_cmp_eq_u32 vcc, -1, v240\n\t" \
          "s_cbranch_vccnz Lo_p5_%=\n\t" \
          "buffer_load_dwordx4 v[216:219], %[oo6], %[rsm], %[ob] offen sc1\n\t" \
          "buffer_load_dwordx4 v[220:223], %[oo7], %[rsm], %[ob] offen sc1\n\t" \
          "s_branch Lo_k5_%=\n\t" \
          "Lo_p6_%=:\n\t" \
          "s_cmp_lt_i32 %[cnt], 0\n\t" \
          "s_cbranch_scc1 Lo_k6_%=\n\t" \
          "s_add_u32 %[cnt], %[cnt], 1\n\t" \
          "s_and_b32 vcc_lo, %[cnt], 3\n\t" \
          "s_cbranch_scc1 Lo_q6_%=\n\t" \
          "s_getpc_b64 s[48:49]\n\t" \
          "s_branch Lo_spin%=\n\t" \
          "Lo_q6_%=:\n\t" \
          "buffer_load_dwordx4 v[216:219], %[oo6], %[rsm], %[ob] offen sc1\n\t" \
          "s_waitcnt vmcnt(0)\n\t" \
          "v_max3_u32 v240, v216, v217, v218\n\t" \
          "v_max_u32 v240, v240, v219\n\t" \
          "v_cmp_eq_u32 vcc, -1, v240\n\t" \
          "s_cbranch_vccnz Lo_p6_%=\n\t" \
          "buffer_load_dwordx4 v[220:223], %[oo7], %[rsm], %[ob] offen sc1\n\t" \
          "s_branch Lo_k6_%=\n\t" \
          "Lo_p7_%=:\n\t" \
          "s_cmp_lt_i32 %[cnt], 0\n\t" \
          "s_cbranch_scc1 Lo_k7_%=\n\t" \
          "s_add_u32 %[cnt], %[cnt], 1\n\t" \
          "s_and_b32 vcc_lo, %[cnt], 3\n\t" \
          "s_cbranch_scc1 Lo_q7_%=\n\t" \
          "s_getpc_b64 s[48:49]\n\t" \
          "s_branch Lo_spin%=\n\t" \
          "Lo_q7_%=:\n\t" \
          "buffer_load_dwordx4 v[220:223], %[oo7], %[rsm], %[ob] offen sc1\n\t" \
          "s_waitcnt vmcnt(0)\n\t" \
          "v_max3_u32 v240, v220, v221, v222\n\t" \
          "v_max_u32 v240, v240, v223\n\t" \
          "v_cmp_eq_u32 vcc, -1, v240\n\t" \
          "s_cbranch_vccnz Lo_p7_%=\n\t" \
          "s_branch Lo_k7_%=\n\t" \
          "Lo_spin%=:\n\t" \
          "buffer_load_dword v241, off, %[rsc], %[tmo] sc1\n\t" \
          "s_add_u32 s48, s48, 4\n\t" \
          "s_addc_u32 s49, s49, 0\n\t" \
          "s_waitcnt vmcnt(0)\n\t" \
          "v_readfirstlane_b32 vcc_lo, v241\n\t" \
          "s_lshr_b32 vcc_hi, %[cnt], 16\n\t" \
          "s_or_b32 vcc_lo, vcc_lo, vcc_hi\n\t" \
          "s_cbranch_scc0 Lo_back%=\n\t" \
          "v_mov_b32 v241, 1\n\t" \
          "buffer_store_dword v241, off, %[rsc], %[tmo] sc1\n\t" \
          "s_mov_b32 %[cnt], -1\n\t" \
          "Lo_back%=:\n\t" \
          "s_setpc_b64 s[48:49]\n\t" \
          "Lo_re%=:\n\t" \
          "s_mov_b32 %[cnt], 0\n\t" \
          "Lo_rl%=:\n\t" \
          "s_sleep 1\n\t" \
          "ds_read_b64 v[244:245], %[rw] offset:512\n\t" \
          "ds_read_b64 v[246:247], %[rw] offset:1024\n\t" \
          "ds_read_b64 v[248:249], %[rw] offset:1536\n\t" \
          "s_waitcnt lgkmcnt(2)\n\t" \
          "v_pk_add_f32 v[250:251], v[242:243], v[244:245]\n\t" \
          "s_waitcnt lgkmcnt(1)\n\t" \
          "v_pk_add_f32 v[250:251], v[250:251], v[246:247]\n\t" \
          "s_waitcnt lgkmcnt(0)\n\t" \
          "v_pk_add_f32 v[250:251], v[250:251], v[248:249]\n\t" \
          "s_nop 0\n\t" \
          "v_max3_u32 v240, v244, v246, v248\n\t" \
          "v_cmp_eq_u32 vcc, -1, v240\n\t" \
          "s_add_u32 %[cnt], %[cnt], 0x4000000\n\t" \
          "s_cbranch_scc1 Lo_rk%=\n\t" \
          "s_cbranch_vccnz Lo_rl%=\n\t" \
          "Lo_rk%=:\n\t" \
          "s_waitcnt vmcnt(0)\n\t" \
          "v_cvt_pk_bf16_f32 v250, v250, v251\n\t" \
          "v_min_u32 v250, -2, v250\n\t" \
          "buffer_store_dword v250, %[mo], %[rsm], %[mbn] offen sc1\n\t" \
          "s_branch Lo_rb%=\n\t" \
          "Lo_fa%=:\n\t" \
          "s_add_u32 %[ta], %[pm], %[shf]\n\t" \
          "s_branch Lo_fb%=\n\t" \
          "Lo_ka%=:\n\t" \
          "v_add_u32 v252, %[ta], v252\n\t" \
          "v_min_u32 v252, -2, v252\n\t" \
          "s_mov_b64 exec, 1\n\t" \
          "buffer_store_dwordx2 v[252:253], off, %[rsm], %[rco] sc1\n\t" \
          "v_mov_b32 v248, %[pm]\n\t" \
          "v_mov_b32 v249, %[t]\n\t" \
          "v_mov_b32 v250, %[fw]\n\t" \
          "v_mov_b32 v251, %[ta]\n\t" \
          "ds_write_b128 %[fa], v[248:251] offset:144\n\t" \
          "s_mov_b64 exec, -1\n\t" \
          "s_branch Lo_kb%=\n\t" \
          "Lv_fail%=:\n\t" \
          "buffer_load_dwordx4 v[192:195], %[ga0], %[rsm], %[mbn] offen sc1\n\t" \
          "buffer_load_dwordx4 v[196:199], %[ga1], %[rsm], %[mbn] offen sc1\n\t" \
          "buffer_load_dwordx4 v[200:203], %[ga2], %[rsm], %[mbn] offen sc1\n\t" \
          "buffer_load_dwordx4 v[204:207], %[ga3], %[rsm], %[mbn] offen sc1\n\t" \
          "s_nop 3\n\t" \
          "v_mov_b64 v[176:177], 0\n\t" \
          "v_mov_b64 v[178:179], 0\n\t" \
          "v_mov_b64 v[180:181], 0\n\t" \
          "v_mov_b64 v[182:183], 0\n\t" \
          "v_mov_b64 v[184:185], 0\n\t" \
          "v_mov_b64 v[186:187], 0\n\t" \
          "v_mov_b64 v[188:189], 0\n\t" \
          "v_mov_b64 v[190:191], 0\n\t" \
          "s_mov_b64 %[en], 0\n\t" \
          "s_mov_b32 %[cnt], 0\n\t" \
          "s_waitcnt vmcnt(3)\n\t" \
          "v_max3_u32 v240, v192, v193, v194\n\t" \
          "v_max_u32 v240, v240, v195\n\t" \
          "v_cmp_eq_u32 vcc, -1, v240\n\t" \
          "s_cbranch_vccnz Lv_p0_%=\n\t" \
          "Lv_k0_%=:\n\t" \
          "s_waitcnt vmcnt(2)\n\t" \
          "v_max3_u32 v240, v196, v197, v198\n\t" \
          "v_max_u32 v240, v240, v199\n\t" \
          "v_cmp_eq_u32 vcc, -1, v240\n\t" \
          "s_cbranch_vccnz Lv_p1_%=\n\t" \
          "Lv_k1_%=:\n\t" \
          "s_waitcnt vmcnt(1)\n\t" \
          "v_max3_u32 v240, v200, v201, v202\n\t" \
          "v_max_u32 v240, v240, v203\n\t" \
          "v_cmp_eq_u32 vcc, -1, v240\n\t" \
          "s_cbranch_vccnz Lv_p2_%=\n\t" \
          "Lv_k2_%=:\n\t" \
          "s_waitcnt vmcnt(0)\n\t" \
          "v_max3_u32 v240, v204, v205, v206\n\t" \
          "v_max_u32 v240, v240, v207\n\t" \
          "v_cmp_eq_u32 vcc, -1, v240\n\t" \
          "s_cbranch_vccnz Lv_p3_%=\n\t" \
          "Lv_k3_%=:\n\t" \
          "s_branch Lv_go%=\n\t" \
          "Lv_p0_%=:\n\t" \
          "s_cmp_lt_i32 %[cnt], 0\n\t" \
          "s_cbranch_scc1 Lv_k0_%=\n\t" \
          "s_add_u32 %[cnt], %[cnt], 1\n\t" \
          "s_and_b32 vcc_lo, %[cnt], 3\n\t" \
          "s_cbranch_scc1 Lv_q0_%=\n\t" \
          "s_getpc_b64 s[48:49]\n\t" \
          "s_branch Lv_spin%=\n\t" \
          "Lv_q0_%=:\n\t" \
          "buffer_load_dwordx4 v[192:195], %[ga0], %[rsm], %[mbn] offen sc1\n\t" \
          "s_waitcnt vmcnt(0)\n\t" \
          "v_max3_u32 v240, v192, v193, v194\n\t" \
          "v_max_u32 v240, v240, v195\n\t" \
          "v_cmp_eq_u32 vcc, -1, v240\n\t" \
          "s_cbranch_vccnz Lv_p0_%=\n\t" \
          "buffer_load_dwordx4 v[196:199], %[ga1], %[rsm], %[mbn] offen sc1\n\t" \
          "buffer_load_dwordx4 v[200:203], %[ga2], %[rsm], %[mbn] offen sc1\n\t" \
          "buffer_load_dwordx4 v[204:207], %[ga3], %[rsm], %[mbn] offen sc1\n\t" \
          "s_branch Lv_k0_%=\n\t" \
          "Lv_p1_%=:\n\t" \
          "s_cmp_lt_i32 %[cnt], 0\n\t" \
          "s_cbranch_scc1 Lv_k1_%=\n\t" \
          "s_add_u32 %[cnt], %[cnt], 1\n\t" \
          "s_and_b32 vcc_lo, %[cnt], 3\n\t" \
          "s_cbranch_scc1 Lv_q1_%=\n\t" \
          "s_getpc_b64 s[48:49]\n\t" \
          "s_branch Lv_spin%=\n\t" \
          "Lv_q1_%=:\n\t" \
          "buffer_load_dwordx4 v[196:199], %[ga1], %[rsm], %[mbn] offen sc1\n\t" \
          "s_waitcnt vmcnt(0)\n\t" \
          "v_max3_u32 v240, v196, v197, v198\n\t" \
          "v_max_u32 v240, v240, v199\n\t" \
          "v_cmp_eq_u32 vcc, -1, v240\n\t" \
          "s_cbranch_vccnz Lv_p1_%=\n\t" \
          "buffer_load_dwordx4 v[200:203], %[ga2], %[rsm], %[mbn] offen sc1\n\t" \
          "buffer_load_dwordx4 v[204:207], %[ga3], %[rsm], %[mbn] offen sc1\n\t" \
          "s_branch Lv_k1_%=\n\t" \
          "Lv_p2_%=:\n\t" \
          "s_cmp_lt_i32 %[cnt], 0\n\t" \
          "s_cbranch_scc1 Lv_k2_%=\n\t" \
          "s_add_u32 %[cnt], %[cnt], 1\n\t" \
          "s_and_b32 vcc_lo, %[cnt], 3\n\t" \
          "s_cbranch_scc1 Lv_q2_%=\n\t" \
          "s_getpc_b64 s[48:49]\n\t" \
          "s_branch Lv_spin%=\n\t" \
          "Lv_q2_%=:\n\t" \
          "buffer_load_dwordx4 v[200:203], %[ga2], %[rsm], %[mbn] offen sc1\n\t" \
          "s_waitcnt vmcnt(0)\n\t" \
          "v_max3_u32 v240, v200, v201, v202\n\t" \
          "v_max_u32 v240, v240, v203\n\t" \
          "v_cmp_eq_u32 vcc, -1, v240\n\t" \
          "s_cbranch_vccnz Lv_p2_%=\n\t" \
          "buffer_load_dwordx4 v[204:207], %[ga3], %[rsm], %[mbn] offen sc1\n\t" \
          "s_branch Lv_k2_%=\n\t" \
          "Lv_p3_%=:\n\t" \
          "s_cmp_lt_i32 %[cnt], 0\n\t" \
          "s_cbranch_scc1 Lv_k3_%=\n\t" \
          "s_add_u32 %[cnt], %[cnt], 1\n\t" \
          "s_and_b32 vcc_lo, %[cnt], 3\n\t" \
          "s_cbranch_scc1 Lv_q3_%=\n\t" \
          "s_getpc_b64 s[48:49]\n\t" \
          "s_branch Lv_spin%=\n\t" \
          "Lv_q3_%=:\n\t" \
          "buffer_load_dwordx4 v[204:207], %[ga3], %[rsm], %[mbn] offen sc1\n\t" \
          "s_waitcnt vmcnt(0)\n\t" \
          "v_max3_u32 v240, v204, v205, v206\n\t" \
          "v_max_u32 v240, v240, v207\n\t" \
          "v_cmp_eq_u32 vcc, -1, v240\n\t" \
          "s_cbranch_vccnz Lv_p3_%=\n\t" \
          "s_branch Lv_k3_%=\n\t" \
          "Lv_spin%=:\n\t" \
          "buffer_load_dword v241, off, %[rsc], %[tmo] sc1\n\t" \
          "s_add_u32 s48, s48, 4\n\t" \
          "s_addc_u32 s49, s49, 0\n\t" \
          "s_waitcnt vmcnt(0)\n\t" \
          "v_readfirstlane_b32 vcc_lo, v241\n\t" \
          "s_lshr_b32 vcc_hi, %[cnt], 16\n\t" \
          "s_or_b32 vcc_lo, vcc_lo, vcc_hi\n\t" \
          "s_cbranch_scc0 Lv_back%=\n\t" \
          "v_mov_b32 v241, 1\n\t" \
          "buffer_store_dword v241, off, %[rsc], %[tmo] sc1\n\t" \
          "s_mov_b32 %[cnt], -1\n\t" \
          "Lv_back%=:\n\t" \
          "s_setpc_b64 s[48:49]\n\t" \
          ".p2align 6\n\t" \
          "Lo_al%=:\n\t" \
          "v_subrev_u32 v252, %[t], v255\n\t" \
          "v_bfe_i32 v252, v252, 0, 16\n\t" \
          "v_cndmask_b32 v252, 0, v252, vcc\n\t" \
          "v_cmp_gt_u32 vcc, 1000, v252\n\t" \
          "s_nop 1\n\t" \
          "v_cndmask_b32 v252, 0, v252, vcc\n\t" \
          "s_nop 1\n\t" \
          "v_max_i32_dpp v252, v252, v252 row_ror:8 row_mask:0xf bank_mask:0xf\n\t" \
          "s_nop 1\n\t" \
          "v_max_i32_dpp v252, v252, v252 row_ror:4 row_mask:0xf bank_mask:0xf\n\t" \
          "s_nop 1\n\t" \
          "v_max_i32_dpp v252, v252, v252 row_ror:2 row_mask:0xf bank_mask:0xf\n\t" \
          "s_nop 1\n\t" \
          "v_max_i32_dpp v252, v252, v252 row_ror:1 row_mask:0xf bank_mask:0xf\n\t" \
          "s_nop 0\n\t" \
          "v_readfirstlane_b32 vcc_lo, v252\n\t" \
          "s_add_u32 %[t], %[t], vcc_lo\n\t" \
          "s_memrealtime vcc\n\t" \
          "s_waitcnt lgkmcnt(0)\n\t" \
          "s_sub_i32 vcc_lo, %[t], vcc_lo\n\t" \
          "s_cmp_gt_i32 vcc_lo, %[lk]\n\t" \
          "s_cbranch_scc0 Lo_w%=\n\t" \
          "Lo_s%=:\n\t" \
          "s_sleep 1\n\t" \
          "Lo_c%=:\n\t" \
          "s_memrealtime vcc\n\t" \
          "s_waitcnt lgkmcnt(0)\n\t" \
          "s_sub_i32 vcc_lo, %[t], vcc_lo\n\t" \
          "s_cmp_gt_i32 vcc_lo, %[lk]\n\t" \
          "s_cbranch_scc1 Lo_s%=\n\t" \
          "buffer_load_dword v255, %[al1], %[rsm], %[mb] offen sc1\n\t" \
          "v_mov_b64 v[224:225], 0\n\t" \
          "v_mov_b64 v[226:227], 0\n\t" \
          "v_mov_b64 v[228:229], 0\n\t" \
          "v_mov_b64 v[230:231], 0\n\t" \
          "v_mov_b64 v[232:233], 0\n\t" \
          "v_mov_b64 v[234:235], 0\n\t" \
          "v_mov_b64 v[236:237], 0\n\t" \
          "v_mov_b64 v[238:239], 0\n\t" \
          "v_mov_b64 v[176:177], 0\n\t" \
          "v_mov_b64 v[178:179], 0\n\t" \
          "v_mov_b64 v[180:181], 0\n\t" \
          "v_mov_b64 v[182:183], 0\n\t" \
          "v_mov_b64 v[184:185], 0\n\t" \
          "v_mov_b64 v[186:187], 0\n\t" \
          "v_mov_b64 v[188:189], 0\n\t" \
          "v_mov_b64 v[190:191], 0\n\t" \
          "s_waitcnt vmcnt(0)\n\t" \
          "v_lshrrev_b32 v252, 16, v255\n\t" \
          "v_cmp_eq_u32 vcc, 1, v252\n\t" \
          "s_cbranch_vccnz Lo_al%=\n\t" \
          "Lo_w%=:\n\t" \
          "s_memrealtime s[50:51]\n\t" \
          "s_getpc_b64 s[48:49]\n\t" \
          "Lo_y%=:\n\t" \
          "s_add_u32 s48, s48, Lo_z%=-Lo_y%=\n\t" \
          "s_addc_u32 s49, s49, 0\n\t" \
          "Lo_v%=:\n\t" \
          "s_memrealtime s[52:53]\n\t" \
          "s_waitcnt lgkmcnt(1)\n\t" \
          "s_sub_i32 vcc_lo, %[t], s50\n\t" \
          "s_cmp_gt_i32 vcc_lo, 3\n\t" \
          "s_cbranch_scc0 Lo_x%=\n\t" \
          "s_memrealtime s[50:51]\n\t" \
          "s_waitcnt lgkmcnt(1)\n\t" \
          "s_sub_i32 vcc_lo, %[t], s52\n\t" \
          "s_cmp_gt_i32 vcc_lo, 3\n\t" \
          "s_cbranch_scc0 Lo_x%=\n\t" \
          "s_memrealtime s[52:53]\n\t" \
          "s_waitcnt lgkmcnt(1)\n\t" \
          "s_sub_i32 vcc_lo, %[t], s50\n\t" \
          "s_cmp_gt_i32 vcc_lo, 3\n\t" \
          "s_cbranch_scc0 Lo_x%=\n\t" \
          "s_memrealtime s[50:51]\n\t" \
          "s_waitcnt lgkmcnt(1)\n\t" \
          "s_sub_i32 vcc_lo, %[t], s52\n\t" \
          "s_cmp_gt_i32 vcc_lo, 3\n\t" \
          "s_cbranch_scc0 Lo_x%=\n\t" \
          "s_memrealtime s[52:53]\n\t" \
          "s_waitcnt lgkmcnt(1)\n\t" \
          "s_sub_i32 vcc_lo, %[t], s50\n\t" \
          "s_cmp_gt_i32 vcc_lo, 3\n\t" \
          "s_cbranch_scc0 Lo_x%=\n\t" \
          "s_memrealtime s[50:51]\n\t" \
          "s_waitcnt lgkmcnt(1)\n\t" \
          "s_sub_i32 vcc_lo, %[t], s52\n\t" \
          "s_cmp_gt_i32 vcc_lo, 3\n\t" \
          "s_cbranch_scc0 Lo_x%=\n\t" \
          "s_memrealtime s[52:53]\n\t" \
          "s_waitcnt lgkmcnt(1)\n\t" \
          "s_sub_i32 vcc_lo, %[t], s50\n\t" \
          "s_cmp_gt_i32 vcc_lo, 3\n\t" \
          "s_cbranch_scc0 Lo_x%=\n\t" \
          "s_memrealtime s[50:51]\n\t" \
          "s_waitcnt lgkmcnt(1)\n\t" \
          "s_sub_i32 vcc_lo, %[t], s52\n\t" \
          "s_cmp_gt_i32 vcc_lo, 3\n\t" \
          "s_cbranch_scc0 Lo_x%=\n\t" \
          "s_branch Lo_v%=\n\t" \
          "Lo_x%=:\n\t" \
          "s_max_i32 vcc_lo, vcc_lo, 0\n\t" \
          "s_lshl_b32 vcc_lo, vcc_lo, 2\n\t" \
          "s_sub_u32 s48, s48, vcc_lo\n\t" \
          "s_subb_u32 s49, s49, 0\n\t" \
          "s_setpc_b64 s[48:49]\n\t" \
          "s_nop 5\n\t" \
          "s_nop 5\n\t" \
          "s_nop 5\n\t" \
          "Lo_z%=:\n\t" \
          "s_barrier\n\t" \
          VO_ST0 \
          "buffer_load_dwordx4 v[192:195], %[oo0], %[rsm], %[ob] offen sc1\n\t" \
          "s_nop 9\n\t" \
          "buffer_load_dwordx4 v[196:199], %[oo1], %[rsm], %[ob] offen sc1\n\t" \
          "s_nop 9\n\t" \
          "buffer_load_dwordx4 v[200:203], %[oo2], %[rsm], %[ob] offen sc1\n\t" \
          "s_nop 9\n\t" \
          "buffer_load_dwordx4 v[204:207], %[oo3], %[rsm], %[ob] offen sc1\n\t" \
          "s_nop 9\n\t" \
          "buffer_load_dwordx4 v[208:211], %[oo4], %[rsm], %[ob] offen sc1\n\t" \
          "s_nop 9\n\t" \
          "buffer_load_dwordx4 v[212:215], %[oo5], %[rsm], %[ob] offen sc1\n\t" \
          "s_nop 9\n\t" \
          "buffer_load_dwordx4 v[216:219], %[oo6], %[rsm], %[ob] offen sc1\n\t" \
          "s_nop 9\n\t" \
          "buffer_load_dwordx4 v[220:223], %[oo7], %[rsm], %[ob] offen sc1\n\t" \
          "s_waitcnt vmcnt(7)\n\t" \
          "v_smfmac_f32_16x16x64_bf16 v[224:227], v[192:195], a[0:7], %[idx]\n\t" \
          "v_max3_u32 v240, v192, v193, v194\n\t" \
          "v_smfmac_f32_16x16x64_bf16 v[228:231], v[192:195], a[8:15], %[idx]\n\t" \
          "v_max3_u32 v240, v194, v195, v240\n\t" \
          "v_smfmac_f32_16x16x64_bf16 v[232:235], v[192:195], a[16:23], %[idx]\n\t" \
          "v_cmp_eq_u32 vcc, -1, v240\n\t" \
          "v_smfmac_f32_16x16x64_bf16 v[236:239], v[192:195], a[24:31], %[idx]\n\t" \
          "s_waitcnt vmcnt(6)\n\t" \
          "v_smfmac_f32_16x16x64_bf16 v[224:227], v[196:199], a[32:39], %[idx]\n\t" \
          "s_cbranch_vccnz Lo_f0_%=\n\t" \
          "v_max3_u32 v240, v196, v197, v198\n\t" \
          "v_smfmac_f32_16x16x64_bf16 v[228:231], v[196:199], a[40:47], %[idx]\n\t" \
          "v_max3_u32 v240, v198, v199, v240\n\t" \
          "v_smfmac_f32_16x16x64_bf16 v[232:235], v[196:199], a[48:55], %[idx]\n\t" \
          "v_cmp_eq_u32 vcc, -1, v240\n\t" \
          "v_smfmac_f32_16x16x64_bf16 v[236:239], v[196:199], a[56:63], %[idx]\n\t" \
          "s_waitcnt vmcnt(5)\n\t" \
          "v_smfmac_f32_16x16x64_bf16 v[224:227], v[200:203], a[64:71], %[idx]\n\t" \
          "s_cbranch_vccnz Lo_f1_%=\n\t" \
          "v_max3_u32 v240, v200, v201, v202\n\t" \
          "v_smfmac_f32_16x16x64_bf16 v[228:231], v[200:203], a[72:79], %[idx]\n\t" \
          "v_max3_u32 v240, v202, v203, v240\n\t" \
          "v_smfmac_f32_16x16x64_bf16 v[232:235], v[200:203], a[80:87], %[idx]\n\t" \
          "v_cmp_eq_u32 vcc, -1, v240\n\t" \
          "v_smfmac_f32_16x16x64_bf16 v[236:239], v[200:203], a[88:95], %[idx]\n\t" \
          "s_waitcnt vmcnt(4)\n\t" \
          "v_smfmac_f32_16x16x64_bf16 v[224:227], v[204:207], a[96:103], %[idx]\n\t" \
          "s_cbranch_vccnz Lo_f2_%=\n\t" \
          "v_max3_u32 v240, v204, v205, v206\n\t" \
          "v_smfmac_f32_16x16x64_bf16 v[228:231], v[204:207], a[104:111], %[idx]\n\t" \
          "v_max3_u32 v240, v206, v207, v240\n\t" \
          "v_smfmac_f32_16x16x64_bf16 v[232:235], v[204:207], a[112:119], %[idx]\n\t" \
          "v_cmp_eq_u32 vcc, -1, v240\n\t" \
          "v_smfmac_f32_16x16x64_bf16 v[236:239], v[204:207], a[120:127], %[idx]\n\t" \
          "s_waitcnt vmcnt(3)\n\t" \
          "v_smfmac_f32_16x16x64_bf16 v[224:227], v[208:211], a[128:135], %[idx]\n\t" \
          "s_cbranch_vccnz Lo_f3_%=\n\t" \
          "v_max3_u32 v240, v208, v209, v210\n\t" \
          "v_smfmac_f32_16x16x64_bf16 v[228:231], v[208:211], a[136:143], %[idx]\n\t" \
          "v_max3_u32 v240, v210, v211, v240\n\t" \
          "v_smfmac_f32_16x16x64_bf16 v[232:235], v[208:211], a[144:151], %[idx]\n\t" \
          "v_cmp_eq_u32 vcc, -1, v240\n\t" \
          "v_smfmac_f32_16x16x64_bf16 v[236:239], v[208:211], a[152:159], %[idx]\n\t" \
          "s_waitcnt vmcnt(2)\n\t" \
          "v_smfmac_f32_16x16x64_bf16 v[224:227], v[212:215], a[160:167], %[idx]\n\t" \
          "s_cbranch_vccnz Lo_f4_%=\n\t" \
          "v_max3_u32 v240, v212, v213, v214\n\t" \
          "v_smfmac_f32_16x16x64_bf16 v[228:231], v[212:215], a[168:175], %[idx]\n\t" \
          "v_max3_u32 v240, v214, v215, v240\n\t" \
          "v_smfmac_f32_16x16x64_bf16 v[232:235], v[212:215], a[176:183], %[idx]\n\t" \
          "v_cmp_eq_u32 vcc, -1, v240\n\t" \
          "v_smfmac_f32_16x16x64_bf16 v[236:239], v[212:215], a[184:191], %[idx]\n\t" \
          "s_waitcnt vmcnt(1)\n\t" \
          "v_smfmac_f32_16x16x64_bf16 v[224:227], v[216:219], a[192:199], %[idx]\n\t" \
          "s_cbranch_vccnz Lo_f5_%=\n\t" \
          "v_max3_u32 v240, v216, v217, v218\n\t" \
          "v_smfmac_f32_16x16x64_bf16 v[228:231], v[216:219], a[200:207], %[idx]\n\t" \
          "v_max3_u32 v240, v218, v219, v240\n\t" \
          "v_smfmac_f32_16x16x64_bf16 v[232:235], v[216:219], a[208:215], %[idx]\n\t" \
          "v_cmp_eq_u32 vcc, -1, v240\n\t" \
          "v_smfmac_f32_16x16x64_bf16 v[236:239], v[216:219], a[216:223], %[idx]\n\t" \
          "s_waitcnt vmcnt(0)\n\t" \
          "v_smfmac_f32_16x16x64_bf16 v[224:227], v[220:223], a[224:231], %[idx]\n\t" \
          "s_cbranch_vccnz Lo_f6_%=\n\t" \
          "v_max3_u32 v240, v220, v221, v222\n\t" \
          "v_smfmac_f32_16x16x64_bf16 v[232:235], v[220:223], a[240:247], %[idx]\n\t" \
          "v_max3_u32 v240, v222, v223, v240\n\t" \
          "v_smfmac_f32_16x16x64_bf16 v[228:231], v[220:223], a[232:239], %[idx]\n\t" \
          "v_cmp_eq_u32 vcc, -1, v240\n\t" \
          "v_smfmac_f32_16x16x64_bf16 v[236:239], v[220:223], a[248:255], %[idx]\n\t" \
          VO_ST1 \
          "s_cmp_lg_u32 %[w], 0\n\t" \
          "v_add_f32 v242, v224, v225\n\t" \
          "v_add_f32 v244, v234, v235\n\t" \
          "s_cbranch_vccnz Lo_f7_%=\n\t" \
          "Lo_tail%=:\n\t" \
          "v_add_f32 v243, v228, v229\n\t" \
          "v_add_f32 v242, v242, v244\n\t" \
          "s_nop 0\n\t" \
          "v_add_f32 v243, v243, v238\n\t" \
          "s_cbranch_scc0 Lo_w0%=\n\t" \
          "v_add_f32 v243, v243, v239\n\t" \
          "ds_write_b64 %[rw], v[242:243]\n\t" \
          "s_barrier\n\t" \
          "s_cmp_eq_u32 %[w], 3\n\t" \
          "s_cbranch_scc0 Lv_z%=\n\t" \
          "s_sleep 1\n\t" \
          "s_branch Lo_end%=\n\t" \
          "Lo_w0%=:\n\t" \
          "v_add_f32 v243, v243, v239\n\t" \
          "s_barrier\n\t" \
          "ds_read_b64 v[244:245], %[rw] offset:512\n\t" \
          "ds_read_b64 v[246:247], %[rw] offset:1024\n\t" \
          "ds_read_b64 v[248:249], %[rw] offset:1536\n\t" \
          "s_waitcnt lgkmcnt(2)\n\t" \
          "v_pk_add_f32 v[250:251], v[242:243], v[244:245]\n\t" \
          "s_waitcnt lgkmcnt(1)\n\t" \
          "v_pk_add_f32 v[250:251], v[250:251], v[246:247]\n\t" \
          "s_waitcnt lgkmcnt(0)\n\t" \
          "v_pk_add_f32 v[250:251], v[250:251], v[248:249]\n\t" \
          "s_nop 0\n\t" \
          "v_cmp_o_f32 vcc, v250, v251\n\t" \
          "v_cvt_pk_bf16_f32 v250, v250, v251\n\t" \
          "v_min_u32 v250, -2, v250\n\t" \
          "v_cndmask_b32 v250, -1, v250, vcc\n\t" \
          "buffer_store_dword v250, %[mo], %[rsm], %[mbn] offen sc1\n\t" \
          "s_cmp_eq_u64 vcc, -1\n\t" \
          "s_cbranch_scc0 Lo_re%=\n\t" \
          "Lo_rb%=:\n\t" \
          VO_ST3 \
          "s_branch Lv_z%=\n\t" \
          "Lo_end%=:\n\t" \
          VO_ST3 \
          "s_memrealtime vcc\n\t" \
          "ds_read_b32 v241, %[fa]\n\t" \
          "ds_read_b64 v[252:253], %[fa] offset:128\n\t" \
          "s_add_u32 %[ta], %[t], %[d2]\n\t" \
          "s_waitcnt lgkmcnt(0)\n\t" \
          "s_mov_b32 %[pm], vcc_lo\n\t" \
          "v_readfirstlane_b32 %[fw], v241\n\t" \
          "s_add_u32 vcc_lo, vcc_lo, %[mn]\n\t" \
          "s_sub_i32 vcc_hi, vcc_lo, %[ta]\n\t" \
          "s_cmp_gt_i32 vcc_hi, 0\n\t" \
          "s_cselect_b32 %[ta], vcc_lo, %[ta]\n\t" \
          "s_cmp_eq_u32 %[fw], %[s1]\n\t" \
          "s_cbranch_scc1 Lo_fa%=\n\t" \
          "Lo_fb%=:\n\t" \
          "s_cmp_eq_u32 %[c], 0\n\t" \
          "s_cbranch_scc1 Lo_ka%=\n\t" \
          "Lo_kb%=:\n\t" \
          "s_add_u32 %[cnt], %[ta], %[dv]\n\t" \
          "Lv_w%=:\n\t" \
          "s_memrealtime s[50:51]\n\t" \
          "s_getpc_b64 s[48:49]\n\t" \
          "Lv_y%=:\n\t" \
          "s_add_u32 s48, s48, Lv_z%=-Lv_y%=\n\t" \
          "s_addc_u32 s49, s49, 0\n\t" \
          "Lv_v%=:\n\t" \
          "s_memrealtime s[52:53]\n\t" \
          "s_waitcnt lgkmcnt(1)\n\t" \
          "s_sub_i32 vcc_lo, %[cnt], s50\n\t" \
          "s_cmp_gt_i32 vcc_lo, 3\n\t" \
          "s_cbranch_scc0 Lv_x%=\n\t" \
          "s_memrealtime s[50:51]\n\t" \
          "s_waitcnt lgkmcnt(1)\n\t" \
          "s_sub_i32 vcc_lo, %[cnt], s52\n\t" \
          "s_cmp_gt_i32 vcc_lo, 3\n\t" \
          "s_cbranch_scc0 Lv_x%=\n\t" \
          "s_memrealtime s[52:53]\n\t" \
          "s_waitcnt lgkmcnt(1)\n\t" \
          "s_sub_i32 vcc_lo, %[cnt], s50\n\t" \
          "s_cmp_gt_i32 vcc_lo, 3\n\t" \
          "s_cbranch_scc0 Lv_x%=\n\t" \
          "s_memrealtime s[50:51]\n\t" \
          "s_waitcnt lgkmcnt(1)\n\t" \
          "s_sub_i32 vcc_lo, %[cnt], s52\n\t" \
          "s_cmp_gt_i32 vcc_lo, 3\n\t" \
          "s_cbranch_scc0 Lv_x%=\n\t" \
          "s_memrealtime s[52:53]\n\t" \
          "s_waitcnt lgkmcnt(1)\n\t" \
          "s_sub_i32 vcc_lo, %[cnt], s50\n\t" \
          "s_cmp_gt_i32 vcc_lo, 3\n\t" \
          "s_cbranch_scc0 Lv_x%=\n\t" \
          "s_memrealtime s[50:51]\n\t" \
          "s_waitcnt lgkmcnt(1)\n\t" \
          "s_sub_i32 vcc_lo, %[cnt], s52\n\t" \
          "s_cmp_gt_i32 vcc_lo, 3\n\t" \
          "s_cbranch_scc0 Lv_x%=\n\t" \
          "s_memrealtime s[52:53]\n\t" \
          "s_waitcnt lgkmcnt(1)\n\t" \
          "s_sub_i32 vcc_lo, %[cnt], s50\n\t" \
          "s_cmp_gt_i32 vcc_lo, 3\n\t" \
          "s_cbranch_scc0 Lv_x%=\n\t" \
          "s_memrealtime s[50:51]\n\t" \
          "s_waitcnt lgkmcnt(1)\n\t" \
          "s_sub_i32 vcc_lo, %[cnt], s52\n\t" \
          "s_cmp_gt_i32 vcc_lo, 3\n\t" \
          "s_cbranch_scc0 Lv_x%=\n\t" \
          "s_branch Lv_v%=\n\t" \
          "Lv_x%=:\n\t" \
          "s_max_i32 vcc_lo, vcc_lo, 0\n\t" \
          "s_lshl_b32 vcc_lo, vcc_lo, 2\n\t" \
          "s_sub_u32 s48, s48, vcc_lo\n\t" \
          "s_subb_u32 s49, s49, 0\n\t" \
          "s_setpc_b64 s[48:49]\n\t" \
          "s_nop 5\n\t" \
          "s_nop 5\n\t" \
          "s_nop 5\n\t" \
          "Lv_z%=:\n\t" \
          "s_barrier\n\t" \
          "Lv_n%=:\n\t" \
          "buffer_load_dwordx4 v[192:195], %[ga0], %[rsm], %[mbn] offen sc1\n\t" \
          "s_nop 15\n\t" \
          "s_nop 5\n\t" \
          "buffer_load_dwordx4 v[196:199], %[ga1], %[rsm], %[mbn] offen sc1\n\t" \
          "s_nop 15\n\t" \
          "s_nop 5\n\t" \
          "buffer_load_dwordx4 v[200:203], %[ga2], %[rsm], %[mbn] offen sc1\n\t" \
          "s_nop 15\n\t" \
          "s_nop 5\n\t" \
          "buffer_load_dwordx4 v[204:207], %[ga3], %[rsm], %[mbn] offen sc1\n\t" \
          "buffer_load_dword %[al], %[alo], %[rsm], %[mb] offen sc1\n\t" \
          "v_mov_b64 v[252:253], -1\n\t" \
          "ds_write_b64 %[rw], v[252:253]\n\t" \
          "Lv_go%=:\n\t" \
          "s_waitcnt vmcnt(4)\n\t" \
          "v_smfmac_f32_16x16x64_bf16 v[176:179], v[192:195], v[8:15], %[idx]\n\t" \
          "v_max3_u32 v240, v192, v193, v194\n\t" \
          "v_smfmac_f32_16x16x64_bf16 v[180:183], v[192:195], v[16:23], %[idx]\n\t" \
          "v_max3_u32 v240, v194, v195, v240\n\t" \
          "v_smfmac_f32_16x16x64_bf16 v[184:187], v[192:195], v[24:31], %[idx]\n\t" \
          "buffer_load_dwordx2 %[rec], off, %[rsm], %[rco] sc1\n\t" \
          "v_smfmac_f32_16x16x64_bf16 v[188:191], v[192:195], v[32:39], %[idx]\n\t" \
          "s_waitcnt vmcnt(4)\n\t" \
          "v_smfmac_f32_16x16x64_bf16 v[176:179], v[196:199], v[40:47], %[idx]\n\t" \
          "v_max3_u32 v240, v196, v197, v240\n\t" \
          "v_smfmac_f32_16x16x64_bf16 v[180:183], v[196:199], v[48:55], %[idx]\n\t" \
          "v_max3_u32 v240, v198, v199, v240\n\t" \
          "v_smfmac_f32_16x16x64_bf16 v[184:187], v[196:199], v[56:63], %[idx]\n\t" \
          "v_smfmac_f32_16x16x64_bf16 v[188:191], v[196:199], v[64:71], %[idx]\n\t" \
          "s_waitcnt vmcnt(3)\n\t" \
          "v_smfmac_f32_16x16x64_bf16 v[176:179], v[200:203], v[72:79], %[idx]\n\t" \
          "v_max3_u32 v240, v200, v201, v240\n\t" \
          "v_smfmac_f32_16x16x64_bf16 v[180:183], v[200:203], v[80:87], %[idx]\n\t" \
          "v_max3_u32 v240, v202, v203, v240\n\t" \
          "v_smfmac_f32_16x16x64_bf16 v[184:187], v[200:203], v[88:95], %[idx]\n\t" \
          "v_smfmac_f32_16x16x64_bf16 v[188:191], v[200:203], v[96:103], %[idx]\n\t" \
          "s_waitcnt vmcnt(2)\n\t" \
          "v_smfmac_f32_16x16x64_bf16 v[176:179], v[204:207], v[104:111], %[idx]\n\t" \
          "v_max3_u32 v240, v204, v205, v240\n\t" \
          "v_smfmac_f32_16x16x64_bf16 v[180:183], v[204:207], v[112:119], %[idx]\n\t" \
          "v_max3_u32 v240, v206, v207, v240\n\t" \
          "v_smfmac_f32_16x16x64_bf16 v[184:187], v[204:207], v[120:127], %[idx]\n\t" \
          "v_cmp_eq_u32 vcc, -1, v240\n\t" \
          "v_smfmac_f32_16x16x64_bf16 v[188:191], v[204:207], v[128:135], %[idx]\n\t" \
          "s_and_b64 vcc, vcc, %[en]\n\t" \
          "v_add_f32 v242, v176, v177\n\t" \
          "v_add_f32 v243, v178, v179\n\t" \
          "v_add_f32 v244, v180, v181\n\t" \
          "v_add_f32 v245, v182, v183\n\t" \
          "s_cbranch_scc1 Lv_fail%=\n\t" \
          "v_add_f32 v246, v184, v185\n\t" \
          "v_add_f32 v247, v186, v187\n\t" \
          "ds_write_b64 %[vw0], v[242:243]\n\t" \
          "ds_write_b64 %[vw1], v[244:245]\n\t" \
          "ds_write_b64 %[vw2], v[246:247]\n\t" \
          "v_add_f32 v248, v188, v189\n\t" \
          "v_add_f32 v249, v190, v191\n\t" \
          "v_mfma_f32_16x16x32_bf16 v[208:211], v[212:215], v[212:215], 0\n\t" \
          "v_mfma_f32_16x16x32_bf16 v[208:211], v[212:215], v[212:215], 0\n\t" \
          "s_waitcnt lgkmcnt(0)\n\t" \
          "s_barrier\n\t" \
          "ds_read_b64 v[242:243], %[vr] offset:0\n\t" \
          "v_mfma_f32_16x16x32_bf16 v[208:211], v[212:215], v[212:215], 0\n\t" \
          "ds_read_b64 v[244:245], %[vr] offset:2064\n\t" \
          "ds_read_b64 v[246:247], %[vr] offset:4128\n\t" \
          "v_mfma_f32_16x16x32_bf16 v[208:211], v[212:215], v[212:215], 0\n\t" \
          "s_waitcnt lgkmcnt(2)\n\t" \
          "v_add_f32 v248, v248, v242\n\t" \
          "v_add_f32 v249, v249, v243\n\t" \
          "s_waitcnt lgkmcnt(1)\n\t" \
          "v_add_f32 v248, v248, v244\n\t" \
          "v_add_f32 v249, v249, v245\n\t" \
          "s_waitcnt lgkmcnt(0)\n\t" \
          "v_add_f32 v248, v248, v246\n\t" \
          "v_add_f32 v249, v249, v247\n\t" \
          "v_cvt_pk_bf16_f32 v242, v248, v249\n\t" \
          "v_min_u32 v242, -2, v242\n\t" \
          "buffer_store_dword v242, %[vo], %[rsm], %[vb] offen sc1\n\t" \
          "s_memrealtime %[te]\n\t" \
          "v_mov_b64 v[252:253], -1\n\t" \
          "v_mov_b64 v[254:255], -1\n\t" \
          "s_sleep 1\n\t" \
          "buffer_store_dwordx4 v[252:255], %[rso], %[rsm], %[rb] offen sc1\n\t" \
          "s_waitcnt vmcnt(2) lgkmcnt(0)\n\t"
#define VO_W "{a[0:31]}"(wa[0]), "{a[32:63]}"(wa[1]), "{a[64:95]}"(wa[2]), "{a[96:127]}"(wa[3]), "{a[128:159]}"(wa[4]), "{a[160:191]}"(wa[5]), "{a[192:223]}"(wa[6]), "{a[224:255]}"(wa[7]), "{v[8:39]}"(wv_[0]), "{v[40:71]}"(wv_[1]), "{v[72:103]}"(wv_[2]), "{v[104:135]}"(wv_[3])
#define VO_DEFW "=&{a[0:31]}"(wa[0]), "=&{a[32:63]}"(wa[1]), "=&{a[64:95]}"(wa[2]), "=&{a[96:127]}"(wa[3]), "=&{a[128:159]}"(wa[4]), "=&{a[160:191]}"(wa[5]), "=&{a[192:223]}"(wa[6]), "=&{a[224:255]}"(wa[7]), "=&{v[8:39]}"(wv_[0]), "=&{v[40:71]}"(wv_[1]), "=&{v[72:103]}"(wv_[2]), "=&{v[104:135]}"(wv_[3])
#define VO_LOADER \
          "s_mov_b64 s[48:49], %[wo]\n\ts_mov_b32 s50, %[wsz]\n\ts_mov_b32 s51, 0x20000\n\ts_nop 4\n\ts_add_u32 m0, %[stg], 0\n\ts_xor_b32 %[t], %[vk], 0x0\n\t" \
          "buffer_load_dwordx4 %[sv], %[rv], %[t] offen lds\n\ts_add_u32 m0, %[stg], 1024\n\ts_xor_b32 %[t], %[vk], 0x10000\n\tbuffer_load_dwordx4 %[sv], %[rv], %[t] offen lds\n\ts_add_u32 m0, %[stg], 2048\n\ts_xor_b32 %[t], %[vk], 0x20000\n\t" \
          "buffer_load_dwordx4 %[sv], %[rv], %[t] offen lds\n\ts_add_u32 m0, %[stg], 3072\n\ts_xor_b32 %[t], %[vk], 0x30000\n\tbuffer_load_dwordx4 %[sv], %[rv], %[t] offen lds\n\ts_add_u32 m0, %[stg], 4096\n\ts_xor_b32 %[t], %[vk], 0x40000\n\t" \
          "buffer_load_dwordx4 %[sv], %[rv], %[t] offen lds\n\ts_add_u32 m0, %[stg], 5120\n\ts_xor_b32 %[t], %[vk], 0x50000\n\tbuffer_load_dwordx4 %[sv], %[rv], %[t] offen lds\n\ts_add_u32 m0, %[stg], 6144\n\ts_xor_b32 %[t], %[vk], 0x60000\n\t" \
          "buffer_load_dwordx4 %[sv], %[rv], %[t] offen lds\n\ts_add_u32 m0, %[stg], 7168\n\ts_xor_b32 %[t], %[vk], 0x70000\n\tbuffer_load_dwordx4 %[sv], %[rv], %[t] offen lds\n\ts_add_u32 m0, %[stg], 8192\n\ts_xor_b32 %[t], %[vk], 0x100000\n\t" \
          "buffer_load_dwordx4 %[sv], %[rv], %[t] offen lds\n\ts_add_u32 m0, %[stg], 9216\n\ts_xor_b32 %[t], %[vk], 0x110000\n\tbuffer_load_dwordx4 %[sv], %[rv], %[t] offen lds\n\ts_add_u32 m0, %[stg], 10240\n\ts_xor_b32 %[t], %[vk], 0x120000\n\t" \
          "buffer_load_dwordx4 %[sv], %[rv], %[t] offen lds\n\ts_add_u32 m0, %[stg], 11264\n\ts_xor_b32 %[t], %[vk], 0x130000\n\tbuffer_load_dwordx4 %[sv], %[rv], %[t] offen lds\n\ts_add_u32 m0, %[stg], 12288\n\ts_xor_b32 %[t], %[vk], 0x140000\n\t" \
          "buffer_load_dwordx4 %[sv], %[rv], %[t] offen lds\n\ts_add_u32 m0, %[stg], 13312\n\ts_xor_b32 %[t], %[vk], 0x150000\n\tbuffer_load_dwordx4 %[sv], %[rv], %[t] offen lds\n\ts_add_u32 m0, %[stg], 14336\n\ts_xor_b32 %[t], %[vk], 0x160000\n\t" \
          "buffer_load_dwordx4 %[sv], %[rv], %[t] offen lds\n\ts_add_u32 m0, %[stg], 15360\n\ts_xor_b32 %[t], %[vk], 0x170000\n\tbuffer_load_dwordx4 %[sv], %[rv], %[t] offen lds\n\ts_add_u32 m0, %[stg], 16384\n\ts_xor_b32 %[t], %[vk], 0x200000\n\t" \
          "buffer_load_dwordx4 %[sv], %[rv], %[t] offen lds\n\ts_add_u32 m0, %[stg], 17408\n\ts_xor_b32 %[t], %[vk], 0x210000\n\tbuffer_load_dwordx4 %[sv], %[rv], %[t] offen lds\n\ts_add_u32 m0, %[stg], 18432\n\ts_xor_b32 %[t], %[vk], 0x220000\n\t" \
          "buffer_load_dwordx4 %[sv], %[rv], %[t] offen lds\n\ts_add_u32 m0, %[stg], 19456\n\ts_xor_b32 %[t], %[vk], 0x230000\n\tbuffer_load_dwordx4 %[sv], %[rv], %[t] offen lds\n\ts_add_u32 m0, %[stg], 20480\n\ts_xor_b32 %[t], %[vk], 0x240000\n\t" \
          "buffer_load_dwordx4 %[sv], %[rv], %[t] offen lds\n\ts_add_u32 m0, %[stg], 21504\n\ts_xor_b32 %[t], %[vk], 0x250000\n\tbuffer_load_dwordx4 %[sv], %[rv], %[t] offen lds\n\ts_add_u32 m0, %[stg], 22528\n\ts_xor_b32 %[t], %[vk], 0x260000\n\t" \
          "buffer_load_dwordx4 %[sv], %[rv], %[t] offen lds\n\ts_add_u32 m0, %[stg], 23552\n\ts_xor_b32 %[t], %[vk], 0x270000\n\tbuffer_load_dwordx4 %[sv], %[rv], %[t] offen lds\n\ts_add_u32 m0, %[stg], 24576\n\ts_xor_b32 %[t], %[vk], 0x300000\n\t" \
          "buffer_load_dwordx4 %[sv], %[rv], %[t] offen lds\n\ts_add_u32 m0, %[stg], 25600\n\ts_xor_b32 %[t], %[vk], 0x310000\n\tbuffer_load_dwordx4 %[sv], %[rv], %[t] offen lds\n\ts_add_u32 m0, %[stg], 26624\n\ts_xor_b32 %[t], %[vk], 0x320000\n\t" \
          "buffer_load_dwordx4 %[sv], %[rv], %[t] offen lds\n\ts_add_u32 m0, %[stg], 27648\n\ts_xor_b32 %[t], %[vk], 0x330000\n\tbuffer_load_dwordx4 %[sv], %[rv], %[t] offen lds\n\ts_add_u32 m0, %[stg], 28672\n\ts_xor_b32 %[t], %[vk], 0x340000\n\t" \
          "buffer_load_dwordx4 %[sv], %[rv], %[t] offen lds\n\ts_add_u32 m0, %[stg], 29696\n\ts_xor_b32 %[t], %[vk], 0x350000\n\tbuffer_load_dwordx4 %[sv], %[rv], %[t] offen lds\n\ts_add_u32 m0, %[stg], 30720\n\ts_xor_b32 %[t], %[vk], 0x360000\n\t" \
          "buffer_load_dwordx4 %[sv], %[rv], %[t] offen lds\n\ts_add_u32 m0, %[stg], 31744\n\ts_xor_b32 %[t], %[vk], 0x370000\n\tbuffer_load_dwordx4 %[sv], %[rv], %[t] offen lds\n\ts_waitcnt vmcnt(16)\n\tds_read_b64_tr_b16 v[8:9], %[vb0] offset:0\n\t" \
          "ds_read_b64_tr_b16 v[10:11], %[vb0] offset:128\n\tds_read_b64_tr_b16 v[12:13], %[vb0] offset:4096\n\tds_read_b64_tr_b16 v[14:15], %[vb0] offset:4224\n\tds_read_b64_tr_b16 v[16:17], %[vb1] offset:0\n\tds_read_b64_tr_b16 v[18:19], %[vb1] offset:128\n\tds_read_b64_tr_b16 v[20:21], %[vb1] offset:4096\n\t" \
          "ds_read_b64_tr_b16 v[22:23], %[vb1] offset:4224\n\tds_read_b64_tr_b16 v[24:25], %[vb2] offset:0\n\tds_read_b64_tr_b16 v[26:27], %[vb2] offset:128\n\tds_read_b64_tr_b16 v[28:29], %[vb2] offset:4096\n\tds_read_b64_tr_b16 v[30:31], %[vb2] offset:4224\n\tds_read_b64_tr_b16 v[32:33], %[vb3] offset:0\n\t" \
          "ds_read_b64_tr_b16 v[34:35], %[vb3] offset:128\n\tds_read_b64_tr_b16 v[36:37], %[vb3] offset:4096\n\tds_read_b64_tr_b16 v[38:39], %[vb3] offset:4224\n\tds_read_b64_tr_b16 v[40:41], %[vb0] offset:8192\n\tds_read_b64_tr_b16 v[42:43], %[vb0] offset:8320\n\tds_read_b64_tr_b16 v[44:45], %[vb0] offset:12288\n\t" \
          "ds_read_b64_tr_b16 v[46:47], %[vb0] offset:12416\n\tds_read_b64_tr_b16 v[48:49], %[vb1] offset:8192\n\tds_read_b64_tr_b16 v[50:51], %[vb1] offset:8320\n\tds_read_b64_tr_b16 v[52:53], %[vb1] offset:12288\n\tds_read_b64_tr_b16 v[54:55], %[vb1] offset:12416\n\tds_read_b64_tr_b16 v[56:57], %[vb2] offset:8192\n\t" \
          "ds_read_b64_tr_b16 v[58:59], %[vb2] offset:8320\n\tds_read_b64_tr_b16 v[60:61], %[vb2] offset:12288\n\tds_read_b64_tr_b16 v[62:63], %[vb2] offset:12416\n\tds_read_b64_tr_b16 v[64:65], %[vb3] offset:8192\n\tds_read_b64_tr_b16 v[66:67], %[vb3] offset:8320\n\tds_read_b64_tr_b16 v[68:69], %[vb3] offset:12288\n\t" \
          "ds_read_b64_tr_b16 v[70:71], %[vb3] offset:12416\n\ts_waitcnt lgkmcnt(0)\n\ts_add_u32 %[ho], %[k0], 1\n\ts_and_b32 %[ho], %[ho], 2\n\ts_or_b32 %[ho], %[ho], 0\n\ts_lshl_b32 %[ho], %[ho], 20\n\t" \
          "s_xor_b32 %[ho], %[ho], %[hx]\n\ts_add_u32 m0, %[stg], 0\n\ts_add_u32 %[t], %[ho], 0\n\tbuffer_load_dwordx4 %[so], s[48:51], %[t] offen lds\n\ts_add_u32 m0, %[stg], 1024\n\ts_add_u32 %[t], %[ho], 16384\n\t" \
          "buffer_load_dwordx4 %[so], s[48:51], %[t] offen lds\n\ts_add_u32 m0, %[stg], 2048\n\ts_add_u32 %[t], %[ho], 32768\n\tbuffer_load_dwordx4 %[so], s[48:51], %[t] offen lds\n\ts_add_u32 m0, %[stg], 3072\n\ts_add_u32 %[t], %[ho], 49152\n\t" \
          "buffer_load_dwordx4 %[so], s[48:51], %[t] offen lds\n\ts_add_u32 m0, %[stg], 4096\n\ts_add_u32 %[t], %[ho], 65536\n\tbuffer_load_dwordx4 %[so], s[48:51], %[t] offen lds\n\ts_add_u32 m0, %[stg], 5120\n\ts_add_u32 %[t], %[ho], 81920\n\t" \
          "buffer_load_dwordx4 %[so], s[48:51], %[t] offen lds\n\ts_add_u32 m0, %[stg], 6144\n\ts_add_u32 %[t], %[ho], 98304\n\tbuffer_load_dwordx4 %[so], s[48:51], %[t] offen lds\n\ts_add_u32 m0, %[stg], 7168\n\ts_add_u32 %[t], %[ho], 114688\n\t" \
          "buffer_load_dwordx4 %[so], s[48:51], %[t] offen lds\n\ts_add_u32 m0, %[stg], 8192\n\ts_add_u32 %[t], %[ho], 131072\n\tbuffer_load_dwordx4 %[so], s[48:51], %[t] offen lds\n\ts_add_u32 m0, %[stg], 9216\n\ts_add_u32 %[t], %[ho], 147456\n\t" \
          "buffer_load_dwordx4 %[so], s[48:51], %[t] offen lds\n\ts_add_u32 m0, %[stg], 10240\n\ts_add_u32 %[t], %[ho], 163840\n\tbuffer_load_dwordx4 %[so], s[48:51], %[t] offen lds\n\ts_add_u32 m0, %[stg], 11264\n\ts_add_u32 %[t], %[ho], 180224\n\t" \
          "buffer_load_dwordx4 %[so], s[48:51], %[t] offen lds\n\ts_add_u32 m0, %[stg], 12288\n\ts_add_u32 %[t], %[ho], 196608\n\tbuffer_load_dwordx4 %[so], s[48:51], %[t] offen lds\n\ts_add_u32 m0, %[stg], 13312\n\ts_add_u32 %[t], %[ho], 212992\n\t" \
          "buffer_load_dwordx4 %[so], s[48:51], %[t] offen lds\n\ts_add_u32 m0, %[stg], 14336\n\ts_add_u32 %[t], %[ho], 229376\n\tbuffer_load_dwordx4 %[so], s[48:51], %[t] offen lds\n\ts_add_u32 m0, %[stg], 15360\n\ts_add_u32 %[t], %[ho], 245760\n\t" \
          "buffer_load_dwordx4 %[so], s[48:51], %[t] offen lds\n\ts_waitcnt vmcnt(16)\n\tds_read_b64_tr_b16 v[72:73], %[vb0] offset:16384\n\tds_read_b64_tr_b16 v[74:75], %[vb0] offset:16512\n\tds_read_b64_tr_b16 v[76:77], %[vb0] offset:20480\n\tds_read_b64_tr_b16 v[78:79], %[vb0] offset:20608\n\t" \
          "ds_read_b64_tr_b16 v[80:81], %[vb1] offset:16384\n\tds_read_b64_tr_b16 v[82:83], %[vb1] offset:16512\n\tds_read_b64_tr_b16 v[84:85], %[vb1] offset:20480\n\tds_read_b64_tr_b16 v[86:87], %[vb1] offset:20608\n\tds_read_b64_tr_b16 v[88:89], %[vb2] offset:16384\n\tds_read_b64_tr_b16 v[90:91], %[vb2] offset:16512\n\t" \
          "ds_read_b64_tr_b16 v[92:93], %[vb2] offset:20480\n\tds_read_b64_tr_b16 v[94:95], %[vb2] offset:20608\n\tds_read_b64_tr_b16 v[96:97], %[vb3] offset:16384\n\tds_read_b64_tr_b16 v[98:99], %[vb3] offset:16512\n\tds_read_b64_tr_b16 v[100:101], %[vb3] offset:20480\n\tds_read_b64_tr_b16 v[102:103], %[vb3] offset:20608\n\t" \
          "ds_read_b64_tr_b16 v[104:105], %[vb0] offset:24576\n\tds_read_b64_tr_b16 v[106:107], %[vb0] offset:24704\n\tds_read_b64_tr_b16 v[108:109], %[vb0] offset:28672\n\tds_read_b64_tr_b16 v[110:111], %[vb0] offset:28800\n\tds_read_b64_tr_b16 v[112:113], %[vb1] offset:24576\n\tds_read_b64_tr_b16 v[114:115], %[vb1] offset:24704\n\t" \
          "ds_read_b64_tr_b16 v[116:117], %[vb1] offset:28672\n\tds_read_b64_tr_b16 v[118:119], %[vb1] offset:28800\n\tds_read_b64_tr_b16 v[120:121], %[vb2] offset:24576\n\tds_read_b64_tr_b16 v[122:123], %[vb2] offset:24704\n\tds_read_b64_tr_b16 v[124:125], %[vb2] offset:28672\n\tds_read_b64_tr_b16 v[126:127], %[vb2] offset:28800\n\t" \
          "ds_read_b64_tr_b16 v[128:129], %[vb3] offset:24576\n\tds_read_b64_tr_b16 v[130:131], %[vb3] offset:24704\n\tds_read_b64_tr_b16 v[132:133], %[vb3] offset:28672\n\tds_read_b64_tr_b16 v[134:135], %[vb3] offset:28800\n\ts_waitcnt lgkmcnt(0)\n\ts_add_u32 %[ho], %[k0], 0\n\t" \
          "s_and_b32 %[ho], %[ho], 2\n\ts_or_b32 %[ho], %[ho], 1\n\ts_lshl_b32 %[ho], %[ho], 20\n\ts_xor_b32 %[ho], %[ho], %[hx]\n\ts_add_u32 m0, %[stg], 16384\n\ts_add_u32 %[t], %[ho], 0\n\t" \
          "buffer_load_dwordx4 %[so], s[48:51], %[t] offen lds\n\ts_add_u32 m0, %[stg], 17408\n\ts_add_u32 %[t], %[ho], 16384\n\tbuffer_load_dwordx4 %[so], s[48:51], %[t] offen lds\n\ts_add_u32 m0, %[stg], 18432\n\ts_add_u32 %[t], %[ho], 32768\n\t" \
          "buffer_load_dwordx4 %[so], s[48:51], %[t] offen lds\n\ts_add_u32 m0, %[stg], 19456\n\ts_add_u32 %[t], %[ho], 49152\n\tbuffer_load_dwordx4 %[so], s[48:51], %[t] offen lds\n\ts_add_u32 m0, %[stg], 20480\n\ts_add_u32 %[t], %[ho], 65536\n\t" \
          "buffer_load_dwordx4 %[so], s[48:51], %[t] offen lds\n\ts_add_u32 m0, %[stg], 21504\n\ts_add_u32 %[t], %[ho], 81920\n\tbuffer_load_dwordx4 %[so], s[48:51], %[t] offen lds\n\ts_add_u32 m0, %[stg], 22528\n\ts_add_u32 %[t], %[ho], 98304\n\t" \
          "buffer_load_dwordx4 %[so], s[48:51], %[t] offen lds\n\ts_add_u32 m0, %[stg], 23552\n\ts_add_u32 %[t], %[ho], 114688\n\tbuffer_load_dwordx4 %[so], s[48:51], %[t] offen lds\n\ts_add_u32 m0, %[stg], 24576\n\ts_add_u32 %[t], %[ho], 131072\n\t" \
          "buffer_load_dwordx4 %[so], s[48:51], %[t] offen lds\n\ts_add_u32 m0, %[stg], 25600\n\ts_add_u32 %[t], %[ho], 147456\n\tbuffer_load_dwordx4 %[so], s[48:51], %[t] offen lds\n\ts_add_u32 m0, %[stg], 26624\n\ts_add_u32 %[t], %[ho], 163840\n\t" \
          "buffer_load_dwordx4 %[so], s[48:51], %[t] offen lds\n\ts_add_u32 m0, %[stg], 27648\n\ts_add_u32 %[t], %[ho], 180224\n\tbuffer_load_dwordx4 %[so], s[48:51], %[t] offen lds\n\ts_add_u32 m0, %[stg], 28672\n\ts_add_u32 %[t], %[ho], 196608\n\t" \
          "buffer_load_dwordx4 %[so], s[48:51], %[t] offen lds\n\ts_add_u32 m0, %[stg], 29696\n\ts_add_u32 %[t], %[ho], 212992\n\tbuffer_load_dwordx4 %[so], s[48:51], %[t] offen lds\n\ts_add_u32 m0, %[stg], 30720\n\ts_add_u32 %[t], %[ho], 229376\n\t" \
          "buffer_load_dwordx4 %[so], s[48:51], %[t] offen lds\n\ts_add_u32 m0, %[stg], 31744\n\ts_add_u32 %[t], %[ho], 245760\n\tbuffer_load_dwordx4 %[so], s[48:51], %[t] offen lds\n\ts_mov_b32 %[n], 8\n\ts_cmp_eq_u32 %[k0], 2\n\t" \
          "s_cbranch_scc1 Lw_2_%=\n\ts_cmp_eq_u32 %[k0], 0\n\ts_cbranch_scc1 Lw_0_%=\n\ts_waitcnt vmcnt(0)\n\ts_cmp_eq_u32 %[k0], 1\n\ts_cbranch_scc1 Lw_1_%=\n\t" \
          "s_branch Lw_3_%=\n\tLw_0_%=:\n\ts_waitcnt vmcnt(16)\n\tds_read_b64_tr_b16 a[0:1], %[ob] offset:0\n\tds_read_b64_tr_b16 a[2:3], %[ob] offset:128\n\tds_read_b64_tr_b16 a[4:5], %[ob] offset:8192\n\t" \
          "ds_read_b64_tr_b16 a[6:7], %[ob] offset:8320\n\tds_read_b64_tr_b16 a[8:9], %[ob] offset:32\n\tds_read_b64_tr_b16 a[10:11], %[ob] offset:160\n\tds_read_b64_tr_b16 a[12:13], %[ob] offset:8224\n\tds_read_b64_tr_b16 a[14:15], %[ob] offset:8352\n\tds_read_b64_tr_b16 a[16:17], %[ob] offset:1024\n\t" \
          "ds_read_b64_tr_b16 a[18:19], %[ob] offset:1152\n\tds_read_b64_tr_b16 a[20:21], %[ob] offset:9216\n\tds_read_b64_tr_b16 a[22:23], %[ob] offset:9344\n\tds_read_b64_tr_b16 a[24:25], %[ob] offset:1056\n\tds_read_b64_tr_b16 a[26:27], %[ob] offset:1184\n\tds_read_b64_tr_b16 a[28:29], %[ob] offset:9248\n\t" \
          "ds_read_b64_tr_b16 a[30:31], %[ob] offset:9376\n\ts_cmp_gt_u32 %[n], 2\n\ts_cselect_b32 s50, s50, 0\n\ts_cmp_gt_u32 %[n], 6\n\ts_cselect_b32 %[ho], 0x2, 0x6\n\ts_lshl_b32 %[ho], %[ho], 20\n\t" \
          "s_xor_b32 %[ho], %[ho], %[hx]\n\ts_waitcnt lgkmcnt(0)\n\ts_add_u32 m0, %[stg], 0\n\ts_add_u32 %[t], %[ho], 0\n\tbuffer_load_dwordx4 %[so], s[48:51], %[t] offen lds\n\ts_add_u32 m0, %[stg], 1024\n\t" \
          "s_add_u32 %[t], %[ho], 16384\n\tbuffer_load_dwordx4 %[so], s[48:51], %[t] offen lds\n\ts_add_u32 m0, %[stg], 2048\n\ts_add_u32 %[t], %[ho], 32768\n\tbuffer_load_dwordx4 %[so], s[48:51], %[t] offen lds\n\ts_add_u32 m0, %[stg], 3072\n\t" \
          "s_add_u32 %[t], %[ho], 49152\n\tbuffer_load_dwordx4 %[so], s[48:51], %[t] offen lds\n\ts_add_u32 m0, %[stg], 4096\n\ts_add_u32 %[t], %[ho], 65536\n\tbuffer_load_dwordx4 %[so], s[48:51], %[t] offen lds\n\ts_add_u32 m0, %[stg], 5120\n\t" \
          "s_add_u32 %[t], %[ho], 81920\n\tbuffer_load_dwordx4 %[so], s[48:51], %[t] offen lds\n\ts_add_u32 m0, %[stg], 6144\n\ts_add_u32 %[t], %[ho], 98304\n\tbuffer_load_dwordx4 %[so], s[48:51], %[t] offen lds\n\ts_add_u32 m0, %[stg], 7168\n\t" \
          "s_add_u32 %[t], %[ho], 114688\n\tbuffer_load_dwordx4 %[so], s[48:51], %[t] offen lds\n\ts_add_u32 m0, %[stg], 8192\n\ts_add_u32 %[t], %[ho], 131072\n\tbuffer_load_dwordx4 %[so], s[48:51], %[t] offen lds\n\ts_add_u32 m0, %[stg], 9216\n\t" \
          "s_add_u32 %[t], %[ho], 147456\n\tbuffer_load_dwordx4 %[so], s[48:51], %[t] offen lds\n\ts_add_u32 m0, %[stg], 10240\n\ts_add_u32 %[t], %[ho], 163840\n\tbuffer_load_dwordx4 %[so], s[48:51], %[t] offen lds\n\ts_add_u32 m0, %[stg], 11264\n\t" \
          "s_add_u32 %[t], %[ho], 180224\n\tbuffer_load_dwordx4 %[so], s[48:51], %[t] offen lds\n\ts_add_u32 m0, %[stg], 12288\n\ts_add_u32 %[t], %[ho], 196608\n\tbuffer_load_dwordx4 %[so], s[48:51], %[t] offen lds\n\ts_add_u32 m0, %[stg], 13312\n\t" \
          "s_add_u32 %[t], %[ho], 212992\n\tbuffer_load_dwordx4 %[so], s[48:51], %[t] offen lds\n\ts_add_u32 m0, %[stg], 14336\n\ts_add_u32 %[t], %[ho], 229376\n\tbuffer_load_dwordx4 %[so], s[48:51], %[t] offen lds\n\ts_add_u32 m0, %[stg], 15360\n\t" \
          "s_add_u32 %[t], %[ho], 245760\n\tbuffer_load_dwordx4 %[so], s[48:51], %[t] offen lds\n\ts_sub_u32 %[n], %[n], 1\n\ts_cmp_eq_u32 %[n], 4\n\ts_cbranch_scc1 Lw_5_%=\n\tLw_1_%=:\n\t" \
          "s_waitcnt vmcnt(16)\n\tds_read_b64_tr_b16 a[32:33], %[ob] offset:16384\n\tds_read_b64_tr_b16 a[34:35], %[ob] offset:16512\n\tds_read_b64_tr_b16 a[36:37], %[ob] offset:24576\n\tds_read_b64_tr_b16 a[38:39], %[ob] offset:24704\n\tds_read_b64_tr_b16 a[40:41], %[ob] offset:16416\n\t" \
          "ds_read_b64_tr_b16 a[42:43], %[ob] offset:16544\n\tds_read_b64_tr_b16 a[44:45], %[ob] offset:24608\n\tds_read_b64_tr_b16 a[46:47], %[ob] offset:24736\n\tds_read_b64_tr_b16 a[48:49], %[ob] offset:17408\n\tds_read_b64_tr_b16 a[50:51], %[ob] offset:17536\n\tds_read_b64_tr_b16 a[52:53], %[ob] offset:25600\n\t" \
          "ds_read_b64_tr_b16 a[54:55], %[ob] offset:25728\n\tds_read_b64_tr_b16 a[56:57], %[ob] offset:17440\n\tds_read_b64_tr_b16 a[58:59], %[ob] offset:17568\n\tds_read_b64_tr_b16 a[60:61], %[ob] offset:25632\n\tds_read_b64_tr_b16 a[62:63], %[ob] offset:25760\n\ts_cmp_gt_u32 %[n], 2\n\t" \
          "s_cselect_b32 s50, s50, 0\n\ts_cmp_gt_u32 %[n], 6\n\ts_cselect_b32 %[ho], 0x3, 0x7\n\ts_lshl_b32 %[ho], %[ho], 20\n\ts_xor_b32 %[ho], %[ho], %[hx]\n\ts_waitcnt lgkmcnt(0)\n\t" \
          "s_add_u32 m0, %[stg], 16384\n\ts_add_u32 %[t], %[ho], 0\n\tbuffer_load_dwordx4 %[so], s[48:51], %[t] offen lds\n\ts_add_u32 m0, %[stg], 17408\n\ts_add_u32 %[t], %[ho], 16384\n\tbuffer_load_dwordx4 %[so], s[48:51], %[t] offen lds\n\t" \
          "s_add_u32 m0, %[stg], 18432\n\ts_add_u32 %[t], %[ho], 32768\n\tbuffer_load_dwordx4 %[so], s[48:51], %[t] offen lds\n\ts_add_u32 m0, %[stg], 19456\n\ts_add_u32 %[t], %[ho], 49152\n\tbuffer_load_dwordx4 %[so], s[48:51], %[t] offen lds\n\t" \
          "s_add_u32 m0, %[stg], 20480\n\ts_add_u32 %[t], %[ho], 65536\n\tbuffer_load_dwordx4 %[so], s[48:51], %[t] offen lds\n\ts_add_u32 m0, %[stg], 21504\n\ts_add_u32 %[t], %[ho], 81920\n\tbuffer_load_dwordx4 %[so], s[48:51], %[t] offen lds\n\t" \
          "s_add_u32 m0, %[stg], 22528\n\ts_add_u32 %[t], %[ho], 98304\n\tbuffer_load_dwordx4 %[so], s[48:51], %[t] offen lds\n\ts_add_u32 m0, %[stg], 23552\n\ts_add_u32 %[t], %[ho], 114688\n\tbuffer_load_dwordx4 %[so], s[48:51], %[t] offen lds\n\t" \
          "s_add_u32 m0, %[stg], 24576\n\ts_add_u32 %[t], %[ho], 131072\n\tbuffer_load_dwordx4 %[so], s[48:51], %[t] offen lds\n\ts_add_u32 m0, %[stg], 25600\n\ts_add_u32 %[t], %[ho], 147456\n\tbuffer_load_dwordx4 %[so], s[48:51], %[t] offen lds\n\t" \
          "s_add_u32 m0, %[stg], 26624\n\ts_add_u32 %[t], %[ho], 163840\n\tbuffer_load_dwordx4 %[so], s[48:51], %[t] offen lds\n\ts_add_u32 m0, %[stg], 27648\n\ts_add_u32 %[t], %[ho], 180224\n\tbuffer_load_dwordx4 %[so], s[48:51], %[t] offen lds\n\t" \
          "s_add_u32 m0, %[stg], 28672\n\ts_add_u32 %[t], %[ho], 196608\n\tbuffer_load_dwordx4 %[so], s[48:51], %[t] offen lds\n\ts_add_u32 m0, %[stg], 29696\n\ts_add_u32 %[t], %[ho], 212992\n\tbuffer_load_dwordx4 %[so], s[48:51], %[t] offen lds\n\t" \
          "s_add_u32 m0, %[stg], 30720\n\ts_add_u32 %[t], %[ho], 229376\n\tbuffer_load_dwordx4 %[so], s[48:51], %[t] offen lds\n\ts_add_u32 m0, %[stg], 31744\n\ts_add_u32 %[t], %[ho], 245760\n\tbuffer_load_dwordx4 %[so], s[48:51], %[t] offen lds\n\t" \
          "s_sub_u32 %[n], %[n], 1\n\ts_cmp_eq_u32 %[n], 4\n\ts_cbranch_scc1 Lw_6_%=\n\tLw_2_%=:\n\ts_waitcnt vmcnt(16)\n\tds_read_b64_tr_b16 a[64:65], %[ob] offset:0\n\t" \
          "ds_read_b64_tr_b16 a[66:67], %[ob] offset:128\n\tds_read_b64_tr_b16 a[68:69], %[ob] offset:8192\n\tds_read_b64_tr_b16 a[70:71], %[ob] offset:8320\n\tds_read_b64_tr_b16 a[72:73], %[ob] offset:32\n\tds_read_b64_tr_b16 a[74:75], %[ob] offset:160\n\tds_read_b64_tr_b16 a[76:77], %[ob] offset:8224\n\t" \
          "ds_read_b64_tr_b16 a[78:79], %[ob] offset:8352\n\tds_read_b64_tr_b16 a[80:81], %[ob] offset:1024\n\tds_read_b64_tr_b16 a[82:83], %[ob] offset:1152\n\tds_read_b64_tr_b16 a[84:85], %[ob] offset:9216\n\tds_read_b64_tr_b16 a[86:87], %[ob] offset:9344\n\tds_read_b64_tr_b16 a[88:89], %[ob] offset:1056\n\t" \
          "ds_read_b64_tr_b16 a[90:91], %[ob] offset:1184\n\tds_read_b64_tr_b16 a[92:93], %[ob] offset:9248\n\tds_read_b64_tr_b16 a[94:95], %[ob] offset:9376\n\ts_cmp_gt_u32 %[n], 2\n\ts_cselect_b32 s50, s50, 0\n\ts_cmp_gt_u32 %[n], 6\n\t" \
          "s_cselect_b32 %[ho], 0x0, 0x4\n\ts_lshl_b32 %[ho], %[ho], 20\n\ts_xor_b32 %[ho], %[ho], %[hx]\n\ts_waitcnt lgkmcnt(0)\n\ts_add_u32 m0, %[stg], 0\n\ts_add_u32 %[t], %[ho], 0\n\t" \
          "buffer_load_dwordx4 %[so], s[48:51], %[t] offen lds\n\ts_add_u32 m0, %[stg], 1024\n\ts_add_u32 %[t], %[ho], 16384\n\tbuffer_load_dwordx4 %[so], s[48:51], %[t] offen lds\n\ts_add_u32 m0, %[stg], 2048\n\ts_add_u32 %[t], %[ho], 32768\n\t" \
          "buffer_load_dwordx4 %[so], s[48:51], %[t] offen lds\n\ts_add_u32 m0, %[stg], 3072\n\ts_add_u32 %[t], %[ho], 49152\n\tbuffer_load_dwordx4 %[so], s[48:51], %[t] offen lds\n\ts_add_u32 m0, %[stg], 4096\n\ts_add_u32 %[t], %[ho], 65536\n\t" \
          "buffer_load_dwordx4 %[so], s[48:51], %[t] offen lds\n\ts_add_u32 m0, %[stg], 5120\n\ts_add_u32 %[t], %[ho], 81920\n\tbuffer_load_dwordx4 %[so], s[48:51], %[t] offen lds\n\ts_add_u32 m0, %[stg], 6144\n\ts_add_u32 %[t], %[ho], 98304\n\t" \
          "buffer_load_dwordx4 %[so], s[48:51], %[t] offen lds\n\ts_add_u32 m0, %[stg], 7168\n\ts_add_u32 %[t], %[ho], 114688\n\tbuffer_load_dwordx4 %[so], s[48:51], %[t] offen lds\n\ts_add_u32 m0, %[stg], 8192\n\ts_add_u32 %[t], %[ho], 131072\n\t" \
          "buffer_load_dwordx4 %[so], s[48:51], %[t] offen lds\n\ts_add_u32 m0, %[stg], 9216\n\ts_add_u32 %[t], %[ho], 147456\n\tbuffer_load_dwordx4 %[so], s[48:51], %[t] offen lds\n\ts_add_u32 m0, %[stg], 10240\n\ts_add_u32 %[t], %[ho], 163840\n\t" \
          "buffer_load_dwordx4 %[so], s[48:51], %[t] offen lds\n\ts_add_u32 m0, %[stg], 11264\n\ts_add_u32 %[t], %[ho], 180224\n\tbuffer_load_dwordx4 %[so], s[48:51], %[t] offen lds\n\ts_add_u32 m0, %[stg], 12288\n\ts_add_u32 %[t], %[ho], 196608\n\t" \
          "buffer_load_dwordx4 %[so], s[48:51], %[t] offen lds\n\ts_add_u32 m0, %[stg], 13312\n\ts_add_u32 %[t], %[ho], 212992\n\tbuffer_load_dwordx4 %[so], s[48:51], %[t] offen lds\n\ts_add_u32 m0, %[stg], 14336\n\ts_add_u32 %[t], %[ho], 229376\n\t" \
          "buffer_load_dwordx4 %[so], s[48:51], %[t] offen lds\n\ts_add_u32 m0, %[stg], 15360\n\ts_add_u32 %[t], %[ho], 245760\n\tbuffer_load_dwordx4 %[so], s[48:51], %[t] offen lds\n\ts_sub_u32 %[n], %[n], 1\n\ts_cmp_eq_u32 %[n], 4\n\t" \
          "s_cbranch_scc1 Lw_7_%=\n\tLw_3_%=:\n\ts_waitcnt vmcnt(16)\n\tds_read_b64_tr_b16 a[96:97], %[ob] offset:16384\n\tds_read_b64_tr_b16 a[98:99], %[ob] offset:16512\n\tds_read_b64_tr_b16 a[100:101], %[ob] offset:24576\n\t" \
          "ds_read_b64_tr_b16 a[102:103], %[ob] offset:24704\n\tds_read_b64_tr_b16 a[104:105], %[ob] offset:16416\n\tds_read_b64_tr_b16 a[106:107], %[ob] offset:16544\n\tds_read_b64_tr_b16 a[108:109], %[ob] offset:24608\n\tds_read_b64_tr_b16 a[110:111], %[ob] offset:24736\n\tds_read_b64_tr_b16 a[112:113], %[ob] offset:17408\n\t" \
          "ds_read_b64_tr_b16 a[114:115], %[ob] offset:17536\n\tds_read_b64_tr_b16 a[116:117], %[ob] offset:25600\n\tds_read_b64_tr_b16 a[118:119], %[ob] offset:25728\n\tds_read_b64_tr_b16 a[120:121], %[ob] offset:17440\n\tds_read_b64_tr_b16 a[122:123], %[ob] offset:17568\n\tds_read_b64_tr_b16 a[124:125], %[ob] offset:25632\n\t" \
          "ds_read_b64_tr_b16 a[126:127], %[ob] offset:25760\n\ts_cmp_gt_u32 %[n], 2\n\ts_cselect_b32 s50, s50, 0\n\ts_cmp_gt_u32 %[n], 6\n\ts_cselect_b32 %[ho], 0x1, 0x5\n\ts_lshl_b32 %[ho], %[ho], 20\n\t" \
          "s_xor_b32 %[ho], %[ho], %[hx]\n\ts_waitcnt lgkmcnt(0)\n\ts_add_u32 m0, %[stg], 16384\n\ts_add_u32 %[t], %[ho], 0\n\tbuffer_load_dwordx4 %[so], s[48:51], %[t] offen lds\n\ts_add_u32 m0, %[stg], 17408\n\t" \
          "s_add_u32 %[t], %[ho], 16384\n\tbuffer_load_dwordx4 %[so], s[48:51], %[t] offen lds\n\ts_add_u32 m0, %[stg], 18432\n\ts_add_u32 %[t], %[ho], 32768\n\tbuffer_load_dwordx4 %[so], s[48:51], %[t] offen lds\n\ts_add_u32 m0, %[stg], 19456\n\t" \
          "s_add_u32 %[t], %[ho], 49152\n\tbuffer_load_dwordx4 %[so], s[48:51], %[t] offen lds\n\ts_add_u32 m0, %[stg], 20480\n\ts_add_u32 %[t], %[ho], 65536\n\tbuffer_load_dwordx4 %[so], s[48:51], %[t] offen lds\n\ts_add_u32 m0, %[stg], 21504\n\t" \
          "s_add_u32 %[t], %[ho], 81920\n\tbuffer_load_dwordx4 %[so], s[48:51], %[t] offen lds\n\ts_add_u32 m0, %[stg], 22528\n\ts_add_u32 %[t], %[ho], 98304\n\tbuffer_load_dwordx4 %[so], s[48:51], %[t] offen lds\n\ts_add_u32 m0, %[stg], 23552\n\t" \
          "s_add_u32 %[t], %[ho], 114688\n\tbuffer_load_dwordx4 %[so], s[48:51], %[t] offen lds\n\ts_add_u32 m0, %[stg], 24576\n\ts_add_u32 %[t], %[ho], 131072\n\tbuffer_load_dwordx4 %[so], s[48:51], %[t] offen lds\n\ts_add_u32 m0, %[stg], 25600\n\t" \
          "s_add_u32 %[t], %[ho], 147456\n\tbuffer_load_dwordx4 %[so], s[48:51], %[t] offen lds\n\ts_add_u32 m0, %[stg], 26624\n\ts_add_u32 %[t], %[ho], 163840\n\tbuffer_load_dwordx4 %[so], s[48:51], %[t] offen lds\n\ts_add_u32 m0, %[stg], 27648\n\t" \
          "s_add_u32 %[t], %[ho], 180224\n\tbuffer_load_dwordx4 %[so], s[48:51], %[t] offen lds\n\ts_add_u32 m0, %[stg], 28672\n\ts_add_u32 %[t], %[ho], 196608\n\tbuffer_load_dwordx4 %[so], s[48:51], %[t] offen lds\n\ts_add_u32 m0, %[stg], 29696\n\t" \
          "s_add_u32 %[t], %[ho], 212992\n\tbuffer_load_dwordx4 %[so], s[48:51], %[t] offen lds\n\ts_add_u32 m0, %[stg], 30720\n\ts_add_u32 %[t], %[ho], 229376\n\tbuffer_load_dwordx4 %[so], s[48:51], %[t] offen lds\n\ts_add_u32 m0, %[stg], 31744\n\t" \
          "s_add_u32 %[t], %[ho], 245760\n\tbuffer_load_dwordx4 %[so], s[48:51], %[t] offen lds\n\ts_sub_u32 %[n], %[n], 1\n\ts_cmp_eq_u32 %[n], 4\n\ts_cbranch_scc1 Lw_4_%=\n\ts_branch Lw_0_%=\n\t" \
          "Lw_4_%=:\n\ts_waitcnt vmcnt(16)\n\tds_read_b64_tr_b16 a[128:129], %[ob] offset:0\n\tds_read_b64_tr_b16 a[130:131], %[ob] offset:128\n\tds_read_b64_tr_b16 a[132:133], %[ob] offset:8192\n\tds_read_b64_tr_b16 a[134:135], %[ob] offset:8320\n\t" \
          "ds_read_b64_tr_b16 a[136:137], %[ob] offset:32\n\tds_read_b64_tr_b16 a[138:139], %[ob] offset:160\n\tds_read_b64_tr_b16 a[140:141], %[ob] offset:8224\n\tds_read_b64_tr_b16 a[142:143], %[ob] offset:8352\n\tds_read_b64_tr_b16 a[144:145], %[ob] offset:1024\n\tds_read_b64_tr_b16 a[146:147], %[ob] offset:1152\n\t" \
          "ds_read_b64_tr_b16 a[148:149], %[ob] offset:9216\n\tds_read_b64_tr_b16 a[150:151], %[ob] offset:9344\n\tds_read_b64_tr_b16 a[152:153], %[ob] offset:1056\n\tds_read_b64_tr_b16 a[154:155], %[ob] offset:1184\n\tds_read_b64_tr_b16 a[156:157], %[ob] offset:9248\n\tds_read_b64_tr_b16 a[158:159], %[ob] offset:9376\n\t" \
          "s_cmp_gt_u32 %[n], 2\n\ts_cselect_b32 s50, s50, 0\n\ts_cmp_gt_u32 %[n], 6\n\ts_cselect_b32 %[ho], 0x2, 0x6\n\ts_lshl_b32 %[ho], %[ho], 20\n\ts_xor_b32 %[ho], %[ho], %[hx]\n\t" \
          "s_waitcnt lgkmcnt(0)\n\ts_add_u32 m0, %[stg], 0\n\ts_add_u32 %[t], %[ho], 0\n\tbuffer_load_dwordx4 %[so], s[48:51], %[t] offen lds\n\ts_add_u32 m0, %[stg], 1024\n\ts_add_u32 %[t], %[ho], 16384\n\t" \
          "buffer_load_dwordx4 %[so], s[48:51], %[t] offen lds\n\ts_add_u32 m0, %[stg], 2048\n\ts_add_u32 %[t], %[ho], 32768\n\tbuffer_load_dwordx4 %[so], s[48:51], %[t] offen lds\n\ts_add_u32 m0, %[stg], 3072\n\ts_add_u32 %[t], %[ho], 49152\n\t" \
          "buffer_load_dwordx4 %[so], s[48:51], %[t] offen lds\n\ts_add_u32 m0, %[stg], 4096\n\ts_add_u32 %[t], %[ho], 65536\n\tbuffer_load_dwordx4 %[so], s[48:51], %[t] offen lds\n\ts_add_u32 m0, %[stg], 5120\n\ts_add_u32 %[t], %[ho], 81920\n\t" \
          "buffer_load_dwordx4 %[so], s[48:51], %[t] offen lds\n\ts_add_u32 m0, %[stg], 6144\n\ts_add_u32 %[t], %[ho], 98304\n\tbuffer_load_dwordx4 %[so], s[48:51], %[t] offen lds\n\ts_add_u32 m0, %[stg], 7168\n\ts_add_u32 %[t], %[ho], 114688\n\t" \
          "buffer_load_dwordx4 %[so], s[48:51], %[t] offen lds\n\ts_add_u32 m0, %[stg], 8192\n\ts_add_u32 %[t], %[ho], 131072\n\tbuffer_load_dwordx4 %[so], s[48:51], %[t] offen lds\n\ts_add_u32 m0, %[stg], 9216\n\ts_add_u32 %[t], %[ho], 147456\n\t" \
          "buffer_load_dwordx4 %[so], s[48:51], %[t] offen lds\n\ts_add_u32 m0, %[stg], 10240\n\ts_add_u32 %[t], %[ho], 163840\n\tbuffer_load_dwordx4 %[so], s[48:51], %[t] offen lds\n\ts_add_u32 m0, %[stg], 11264\n\ts_add_u32 %[t], %[ho], 180224\n\t" \
          "buffer_load_dwordx4 %[so], s[48:51], %[t] offen lds\n\ts_add_u32 m0, %[stg], 12288\n\ts_add_u32 %[t], %[ho], 196608\n\tbuffer_load_dwordx4 %[so], s[48:51], %[t] offen lds\n\ts_add_u32 m0, %[stg], 13312\n\ts_add_u32 %[t], %[ho], 212992\n\t" \
          "buffer_load_dwordx4 %[so], s[48:51], %[t] offen lds\n\ts_add_u32 m0, %[stg], 14336\n\ts_add_u32 %[t], %[ho], 229376\n\tbuffer_load_dwordx4 %[so], s[48:51], %[t] offen lds\n\ts_add_u32 m0, %[stg], 15360\n\ts_add_u32 %[t], %[ho], 245760\n\t" \
          "buffer_load_dwordx4 %[so], s[48:51], %[t] offen lds\n\ts_sub_u32 %[n], %[n], 1\n\ts_cmp_eq_u32 %[n], 0\n\ts_cbranch_scc1 Lw_end%=\n\tLw_5_%=:\n\ts_waitcnt vmcnt(16)\n\t" \
          "ds_read_b64_tr_b16 a[160:161], %[ob] offset:16384\n\tds_read_b64_tr_b16 a[162:163], %[ob] offset:16512\n\tds_read_b64_tr_b16 a[164:165], %[ob] offset:24576\n\tds_read_b64_tr_b16 a[166:167], %[ob] offset:24704\n\tds_read_b64_tr_b16 a[168:169], %[ob] offset:16416\n\tds_read_b64_tr_b16 a[170:171], %[ob] offset:16544\n\t" \
          "ds_read_b64_tr_b16 a[172:173], %[ob] offset:24608\n\tds_read_b64_tr_b16 a[174:175], %[ob] offset:24736\n\tds_read_b64_tr_b16 a[176:177], %[ob] offset:17408\n\tds_read_b64_tr_b16 a[178:179], %[ob] offset:17536\n\tds_read_b64_tr_b16 a[180:181], %[ob] offset:25600\n\tds_read_b64_tr_b16 a[182:183], %[ob] offset:25728\n\t" \
          "ds_read_b64_tr_b16 a[184:185], %[ob] offset:17440\n\tds_read_b64_tr_b16 a[186:187], %[ob] offset:17568\n\tds_read_b64_tr_b16 a[188:189], %[ob] offset:25632\n\tds_read_b64_tr_b16 a[190:191], %[ob] offset:25760\n\ts_cmp_gt_u32 %[n], 2\n\ts_cselect_b32 s50, s50, 0\n\t" \
          "s_cmp_gt_u32 %[n], 6\n\ts_cselect_b32 %[ho], 0x3, 0x7\n\ts_lshl_b32 %[ho], %[ho], 20\n\ts_xor_b32 %[ho], %[ho], %[hx]\n\ts_waitcnt lgkmcnt(0)\n\ts_add_u32 m0, %[stg], 16384\n\t" \
          "s_add_u32 %[t], %[ho], 0\n\tbuffer_load_dwordx4 %[so], s[48:51], %[t] offen lds\n\ts_add_u32 m0, %[stg], 17408\n\ts_add_u32 %[t], %[ho], 16384\n\tbuffer_load_dwordx4 %[so], s[48:51], %[t] offen lds\n\ts_add_u32 m0, %[stg], 18432\n\t" \
          "s_add_u32 %[t], %[ho], 32768\n\tbuffer_load_dwordx4 %[so], s[48:51], %[t] offen lds\n\ts_add_u32 m0, %[stg], 19456\n\ts_add_u32 %[t], %[ho], 49152\n\tbuffer_load_dwordx4 %[so], s[48:51], %[t] offen lds\n\ts_add_u32 m0, %[stg], 20480\n\t" \
          "s_add_u32 %[t], %[ho], 65536\n\tbuffer_load_dwordx4 %[so], s[48:51], %[t] offen lds\n\ts_add_u32 m0, %[stg], 21504\n\ts_add_u32 %[t], %[ho], 81920\n\tbuffer_load_dwordx4 %[so], s[48:51], %[t] offen lds\n\ts_add_u32 m0, %[stg], 22528\n\t" \
          "s_add_u32 %[t], %[ho], 98304\n\tbuffer_load_dwordx4 %[so], s[48:51], %[t] offen lds\n\ts_add_u32 m0, %[stg], 23552\n\ts_add_u32 %[t], %[ho], 114688\n\tbuffer_load_dwordx4 %[so], s[48:51], %[t] offen lds\n\ts_add_u32 m0, %[stg], 24576\n\t" \
          "s_add_u32 %[t], %[ho], 131072\n\tbuffer_load_dwordx4 %[so], s[48:51], %[t] offen lds\n\ts_add_u32 m0, %[stg], 25600\n\ts_add_u32 %[t], %[ho], 147456\n\tbuffer_load_dwordx4 %[so], s[48:51], %[t] offen lds\n\ts_add_u32 m0, %[stg], 26624\n\t" \
          "s_add_u32 %[t], %[ho], 163840\n\tbuffer_load_dwordx4 %[so], s[48:51], %[t] offen lds\n\ts_add_u32 m0, %[stg], 27648\n\ts_add_u32 %[t], %[ho], 180224\n\tbuffer_load_dwordx4 %[so], s[48:51], %[t] offen lds\n\ts_add_u32 m0, %[stg], 28672\n\t" \
          "s_add_u32 %[t], %[ho], 196608\n\tbuffer_load_dwordx4 %[so], s[48:51], %[t] offen lds\n\ts_add_u32 m0, %[stg], 29696\n\ts_add_u32 %[t], %[ho], 212992\n\tbuffer_load_dwordx4 %[so], s[48:51], %[t] offen lds\n\ts_add_u32 m0, %[stg], 30720\n\t" \
          "s_add_u32 %[t], %[ho], 229376\n\tbuffer_load_dwordx4 %[so], s[48:51], %[t] offen lds\n\ts_add_u32 m0, %[stg], 31744\n\ts_add_u32 %[t], %[ho], 245760\n\tbuffer_load_dwordx4 %[so], s[48:51], %[t] offen lds\n\ts_sub_u32 %[n], %[n], 1\n\t" \
          "s_cmp_eq_u32 %[n], 0\n\ts_cbranch_scc1 Lw_end%=\n\tLw_6_%=:\n\ts_waitcnt vmcnt(16)\n\tbuffer_load_dwordx4 %[rec], %[ro], %[rr], 0 offen sc1\n\tds_read_b64_tr_b16 a[192:193], %[ob] offset:0\n\t" \
          "ds_read_b64_tr_b16 a[194:195], %[ob] offset:128\n\tds_read_b64_tr_b16 a[196:197], %[ob] offset:8192\n\tds_read_b64_tr_b16 a[198:199], %[ob] offset:8320\n\tds_read_b64_tr_b16 a[200:201], %[ob] offset:32\n\tds_read_b64_tr_b16 a[202:203], %[ob] offset:160\n\tds_read_b64_tr_b16 a[204:205], %[ob] offset:8224\n\t" \
          "ds_read_b64_tr_b16 a[206:207], %[ob] offset:8352\n\tds_read_b64_tr_b16 a[208:209], %[ob] offset:1024\n\tds_read_b64_tr_b16 a[210:211], %[ob] offset:1152\n\tds_read_b64_tr_b16 a[212:213], %[ob] offset:9216\n\tds_read_b64_tr_b16 a[214:215], %[ob] offset:9344\n\tds_read_b64_tr_b16 a[216:217], %[ob] offset:1056\n\t" \
          "ds_read_b64_tr_b16 a[218:219], %[ob] offset:1184\n\tds_read_b64_tr_b16 a[220:221], %[ob] offset:9248\n\tds_read_b64_tr_b16 a[222:223], %[ob] offset:9376\n\ts_cmp_gt_u32 %[n], 2\n\ts_cselect_b32 s50, s50, 0\n\ts_cmp_gt_u32 %[n], 6\n\t" \
          "s_cselect_b32 %[ho], 0x0, 0x4\n\ts_lshl_b32 %[ho], %[ho], 20\n\ts_xor_b32 %[ho], %[ho], %[hx]\n\ts_waitcnt lgkmcnt(0)\n\ts_add_u32 m0, %[stg], 0\n\ts_add_u32 %[t], %[ho], 0\n\t" \
          "buffer_load_dwordx4 %[so], s[48:51], %[t] offen lds\n\ts_add_u32 m0, %[stg], 1024\n\ts_add_u32 %[t], %[ho], 16384\n\tbuffer_load_dwordx4 %[so], s[48:51], %[t] offen lds\n\ts_add_u32 m0, %[stg], 2048\n\ts_add_u32 %[t], %[ho], 32768\n\t" \
          "buffer_load_dwordx4 %[so], s[48:51], %[t] offen lds\n\ts_add_u32 m0, %[stg], 3072\n\ts_add_u32 %[t], %[ho], 49152\n\tbuffer_load_dwordx4 %[so], s[48:51], %[t] offen lds\n\ts_add_u32 m0, %[stg], 4096\n\ts_add_u32 %[t], %[ho], 65536\n\t" \
          "buffer_load_dwordx4 %[so], s[48:51], %[t] offen lds\n\ts_add_u32 m0, %[stg], 5120\n\ts_add_u32 %[t], %[ho], 81920\n\tbuffer_load_dwordx4 %[so], s[48:51], %[t] offen lds\n\ts_add_u32 m0, %[stg], 6144\n\ts_add_u32 %[t], %[ho], 98304\n\t" \
          "buffer_load_dwordx4 %[so], s[48:51], %[t] offen lds\n\ts_add_u32 m0, %[stg], 7168\n\ts_add_u32 %[t], %[ho], 114688\n\tbuffer_load_dwordx4 %[so], s[48:51], %[t] offen lds\n\ts_add_u32 m0, %[stg], 8192\n\ts_add_u32 %[t], %[ho], 131072\n\t" \
          "buffer_load_dwordx4 %[so], s[48:51], %[t] offen lds\n\ts_add_u32 m0, %[stg], 9216\n\ts_add_u32 %[t], %[ho], 147456\n\tbuffer_load_dwordx4 %[so], s[48:51], %[t] offen lds\n\ts_add_u32 m0, %[stg], 10240\n\ts_add_u32 %[t], %[ho], 163840\n\t" \
          "buffer_load_dwordx4 %[so], s[48:51], %[t] offen lds\n\ts_add_u32 m0, %[stg], 11264\n\ts_add_u32 %[t], %[ho], 180224\n\tbuffer_load_dwordx4 %[so], s[48:51], %[t] offen lds\n\ts_add_u32 m0, %[stg], 12288\n\ts_add_u32 %[t], %[ho], 196608\n\t" \
          "buffer_load_dwordx4 %[so], s[48:51], %[t] offen lds\n\ts_add_u32 m0, %[stg], 13312\n\ts_add_u32 %[t], %[ho], 212992\n\tbuffer_load_dwordx4 %[so], s[48:51], %[t] offen lds\n\ts_add_u32 m0, %[stg], 14336\n\ts_add_u32 %[t], %[ho], 229376\n\t" \
          "buffer_load_dwordx4 %[so], s[48:51], %[t] offen lds\n\ts_add_u32 m0, %[stg], 15360\n\ts_add_u32 %[t], %[ho], 245760\n\tbuffer_load_dwordx4 %[so], s[48:51], %[t] offen lds\n\ts_sub_u32 %[n], %[n], 1\n\ts_cmp_eq_u32 %[n], 0\n\t" \
          "s_cbranch_scc1 Lw_end%=\n\tLw_7_%=:\n\ts_waitcnt vmcnt(16)\n\tds_read_b64_tr_b16 a[224:225], %[ob] offset:16384\n\tds_read_b64_tr_b16 a[226:227], %[ob] offset:16512\n\tds_read_b64_tr_b16 a[228:229], %[ob] offset:24576\n\t" \
          "ds_read_b64_tr_b16 a[230:231], %[ob] offset:24704\n\tds_read_b64_tr_b16 a[232:233], %[ob] offset:16416\n\tds_read_b64_tr_b16 a[234:235], %[ob] offset:16544\n\tds_read_b64_tr_b16 a[236:237], %[ob] offset:24608\n\tds_read_b64_tr_b16 a[238:239], %[ob] offset:24736\n\tds_read_b64_tr_b16 a[240:241], %[ob] offset:17408\n\t" \
          "ds_read_b64_tr_b16 a[242:243], %[ob] offset:17536\n\tds_read_b64_tr_b16 a[244:245], %[ob] offset:25600\n\tds_read_b64_tr_b16 a[246:247], %[ob] offset:25728\n\tds_read_b64_tr_b16 a[248:249], %[ob] offset:17440\n\tds_read_b64_tr_b16 a[250:251], %[ob] offset:17568\n\tds_read_b64_tr_b16 a[252:253], %[ob] offset:25632\n\t" \
          "ds_read_b64_tr_b16 a[254:255], %[ob] offset:25760\n\ts_cmp_gt_u32 %[n], 2\n\ts_cselect_b32 s50, s50, 0\n\ts_cmp_gt_u32 %[n], 6\n\ts_cselect_b32 %[ho], 0x1, 0x5\n\ts_lshl_b32 %[ho], %[ho], 20\n\t" \
          "s_xor_b32 %[ho], %[ho], %[hx]\n\ts_waitcnt lgkmcnt(0)\n\ts_add_u32 m0, %[stg], 16384\n\ts_add_u32 %[t], %[ho], 0\n\tbuffer_load_dwordx4 %[so], s[48:51], %[t] offen lds\n\ts_add_u32 m0, %[stg], 17408\n\t" \
          "s_add_u32 %[t], %[ho], 16384\n\tbuffer_load_dwordx4 %[so], s[48:51], %[t] offen lds\n\ts_add_u32 m0, %[stg], 18432\n\ts_add_u32 %[t], %[ho], 32768\n\tbuffer_load_dwordx4 %[so], s[48:51], %[t] offen lds\n\ts_add_u32 m0, %[stg], 19456\n\t" \
          "s_add_u32 %[t], %[ho], 49152\n\tbuffer_load_dwordx4 %[so], s[48:51], %[t] offen lds\n\ts_add_u32 m0, %[stg], 20480\n\ts_add_u32 %[t], %[ho], 65536\n\tbuffer_load_dwordx4 %[so], s[48:51], %[t] offen lds\n\ts_add_u32 m0, %[stg], 21504\n\t" \
          "s_add_u32 %[t], %[ho], 81920\n\tbuffer_load_dwordx4 %[so], s[48:51], %[t] offen lds\n\ts_add_u32 m0, %[stg], 22528\n\ts_add_u32 %[t], %[ho], 98304\n\tbuffer_load_dwordx4 %[so], s[48:51], %[t] offen lds\n\ts_add_u32 m0, %[stg], 23552\n\t" \
          "s_add_u32 %[t], %[ho], 114688\n\tbuffer_load_dwordx4 %[so], s[48:51], %[t] offen lds\n\ts_add_u32 m0, %[stg], 24576\n\ts_add_u32 %[t], %[ho], 131072\n\tbuffer_load_dwordx4 %[so], s[48:51], %[t] offen lds\n\ts_add_u32 m0, %[stg], 25600\n\t" \
          "s_add_u32 %[t], %[ho], 147456\n\tbuffer_load_dwordx4 %[so], s[48:51], %[t] offen lds\n\ts_add_u32 m0, %[stg], 26624\n\ts_add_u32 %[t], %[ho], 163840\n\tbuffer_load_dwordx4 %[so], s[48:51], %[t] offen lds\n\ts_add_u32 m0, %[stg], 27648\n\t" \
          "s_add_u32 %[t], %[ho], 180224\n\tbuffer_load_dwordx4 %[so], s[48:51], %[t] offen lds\n\ts_add_u32 m0, %[stg], 28672\n\ts_add_u32 %[t], %[ho], 196608\n\tbuffer_load_dwordx4 %[so], s[48:51], %[t] offen lds\n\ts_add_u32 m0, %[stg], 29696\n\t" \
          "s_add_u32 %[t], %[ho], 212992\n\tbuffer_load_dwordx4 %[so], s[48:51], %[t] offen lds\n\ts_add_u32 m0, %[stg], 30720\n\ts_add_u32 %[t], %[ho], 229376\n\tbuffer_load_dwordx4 %[so], s[48:51], %[t] offen lds\n\ts_add_u32 m0, %[stg], 31744\n\t" \
          "s_add_u32 %[t], %[ho], 245760\n\tbuffer_load_dwordx4 %[so], s[48:51], %[t] offen lds\n\ts_sub_u32 %[n], %[n], 1\n\ts_cmp_eq_u32 %[n], 0\n\ts_cbranch_scc1 Lw_end%=\n\ts_branch Lw_4_%=\n\t" \
          "Lw_end%=:\n\ts_waitcnt vmcnt(0)\n\ts_barrier\n\t"
#define VO_CLOB "v176", "v177", "v178", "v179", "v180", "v181", "v182", "v183", "v184", "v185", "v186", "v187", "v188", "v189", "v190", "v191", "v192", "v193", "v194", "v195", "v196", "v197", "v198", "v199", "v200", "v201", "v202", "v203", "v204", "v205", "v206", "v207", "v208", "v209", "v210", "v211", "v212", "v213", "v214", "v215", "v216", "v217", "v218", "v219", "v220", "v221", "v222", "v223", "v224", "v225", "v226", "v227", "v228", "v229", "v230", "v231", "v232", "v233", "v234", "v235", "v236", "v237", "v238", "v239", "v240", "v241", "v242", "v243", "v244", "v245", "v246", "v247", "v248", "v249", "v250", "v251", "v252", "v253", "v254", "v255", "s48", "s49", "s50", "s51", "s52", "s53"
__global__ void __launch_bounds__(256, 1) k_fused(FParams P) {
  __shared__ __attribute__((aligned(16))) char Lraw[LDS_ALL];
  unsigned long long pc0;
  asm volatile("s_getpc_b64 %0\nLfused_pc0:" : "=s"(pc0));
  const int w = __builtin_amdgcn_readfirstlane(threadIdx.x >> 6);
  unsigned cz, pk; unsigned long long tag;
  {
    int clen; asm volatile("s_mov_b32 %0, Lfused_end-Lfused_pc0" : "=s"(clen));
    const int o = ((blockIdx.x >> 3) * 4 + w) * 1024 + (threadIdx.x & 7) * 128;
    asm volatile("s_nop 4\n\tbuffer_load_dword %0, %1, %2, 0 offen" : "=v"(cz) : "v"(o), "s"(__builtin_amdgcn_make_buffer_rsrc((void*)pc0, 0, clen, 0x00020000))); }
  if ((blockIdx.x & 7) >= 3) {
    asm volatile("Lfused_worker:");
    worker(P, cz, Lraw, w);
    asm volatile("Lfused_end:");
    return;
  }
  unsigned* tmo = &P.ctl->tmo[0]; unsigned long long* prof = P.prof; (void)prof;
  const int xq = blockIdx.x & 7, slot = blockIdx.x >> 3, nq = xq < 2 ? 12 : 8;
  const int b = slot < nq ? 12 * xq + slot : NQK + 20 * xq + (slot - nq);
  const int alw = b < NQK ? (b % 9 == 0 ? (b / 9) * 4 + w : -1) : (b == 33 ? 16 + w : (b == 95 ? 20 + w : -1));
  const int xr = xq;
  const unsigned long long alx = (unsigned)__builtin_amdgcn_readfirstlane(alw >= 0 ? -1 : 0);
  const __amdgpu_buffer_rsrc_t rs_m = __builtin_amdgcn_make_buffer_rsrc(P.ring, 0, RSIZE, 0x00020000), rs_r = __builtin_amdgcn_make_buffer_rsrc(P.rdy, 0, RDY_SIZE, 0x00020000);
  const __amdgpu_buffer_rsrc_t rs_c = __builtin_amdgcn_make_buffer_rsrc(P.ctl, 0, sizeof(Ctl), 0x00020000);
  v4u rdy;
#define LANE_CONSTS const int lane = opaque_lane(), tid = 64 * w + lane, l15 = lane & 15, lq = lane >> 4, idx = (lane & 1) ? 0xEEEE : 0x4444; \
  const int mem_lane_off = (((lq >> 1) * 2 + (l15 >> 3)) * 16 + (lq & 1) * 8 + (l15 & 7)) * 16 + dbl(w * 4096); \
  const int wrow = 8 * (lq >> 1) + 2 * (lq & 1) + 4 * (l15 >> 3) + 16 * ((l15 >> 2) & 1), wsw = (wrow >> 1) & 3
  unsigned tA = 0;
  if (b < NQK) {
    const int h = b;
    LANE_CONSTS;
    v32u wa[8], wv_[7];
    const unsigned lb0 = (unsigned)(size_t)Lraw;
    const unsigned own = lb0 + w * 40960 + lane * 16;
    unsigned kfl;
    {
      const __amdgpu_buffer_rsrc_t rq = __builtin_amdgcn_make_buffer_rsrc((void*)(P.Wq + h * DHA + 64 * (w >> 1)), 0, 0x7fffffff, 0x00020000), rk = __builtin_amdgcn_make_buffer_rsrc((void*)(P.Wk + h * DHA + 64 * (w >> 1)), 0, 0x7fffffff, 0x00020000);
      const int go = (lane >> 3) * HDA * 2 + (((((lane & 7) >> 1) ^ ((lane >> 4) & 3)) << 1) | (lane & 1)) * 16;
      unsigned rd[4];
#pragma unroll
      for (int t = 0; t < 4; ++t) rd[t] = lb0 + w * 40960 + wrow * 128 + (l15 & 3) * 8 + ((t ^ (2 * (~w & 1)) ^ wsw) << 5);
#define QK_NTq ""
#define QK_NTk ""
      int t_;
      asm volatile(QK_LOADER : QK_DEFW, [t] "=&s"(t_), [rec] "=&v"(rdy), [kf] "=&v"(kfl)
                   : [ko] "v"(lane < 2 ? (int)offsetof(Ctl, prg) + lane * 4096 + h * 128 : 0x7ffffff0), [rc] "s"(rs_c), [rd0] "v"(rd[0]), [rd1] "v"(rd[1]), [rd2] "v"(rd[2]), [rd3] "v"(rd[3]), [own] "v"(own), [go] "v"(go), [ro] "v"(lane * 128), [rr] "s"(rs_r), [rq] "s"(rq), [rk] "s"(rk), [lw] "s"(lb0 + w * 40960), [kb] "s"((8 * (w & 1) + 2 * xr + (w >> 1)) * 0x80000) : "memory", "scc");
    }
    asm volatile("" :: "v"(cz));
    { const unsigned long long t0 = rdy_tag(rdy); if ((~__ballot(rdy.x == (unsigned)t0 && rdy.y == (unsigned)(t0 >> 32)) << 4) != 0) kfl = 0; }
    const unsigned long long parq = rdy_wait(P, rdy, tag, pk);
    const int rpl = (int)(parq >> 63) * 8192;
    const __amdgpu_buffer_rsrc_t rs_x = __builtin_amdgcn_make_buffer_rsrc(P.qkvA, 0, QSIZE, 0x00020000);
    const int xk = qoff(parq, 16 + (h >> 1)) + (h & 1) * 4096, xv = qoff(parq, 32 + (h >> 1)) + (h & 1) * 4096;
    const int xm = kvoff(pk, h) + w * 512;
#define PF_ADDR const int tz = opaque_lane()
#define KV_SEGS(v) (min(__builtin_amdgcn_readlane(v, 0), __builtin_amdgcn_readlane(v, 1)) >> 1)
#define WAIT_KV(sg) do { for (unsigned spins = 0;; ++spins) { const unsigned v_ = ld_agent(&P.ctl->prg[(tz & 1) * 32 + h][0]); kvok = KV_SEGS(v_); if (kvok > (sg)) break; \
        if (spin_fail(spins, tmo, 3u)) break; __builtin_amdgcn_s_sleep(8); } } while (0)
    int kvok = 0;
#define LOAD_KV(sg) do { const int so_ = (sg) * 2 * QBLK + w * 1024; \
        _Pragma("unroll") for (int q = 0; q < 4; ++q) \
          asm volatile("s_mov_b32 m0, %0\n\ts_nop 4\n\tbuffer_load_dwordx4 %1, %2, %3 offen sc1 lds" :: "s"(lb0 + w * 40960 + QK_KV + q * 1024), "v"((q < 2 ? tz : tz ^ ((tz >> 1) & 8)) * 16), "s"(rs_x), \
                       "s"(so_ + (q & 1) * QBLK + ((q >> 1) ? xv : xk)) : "memory"); } while (0)
    unsigned pz = 0;
#define LD_ASYNC(dst, voff, rs, soff) asm volatile("s_nop 4\n\tbuffer_load_dword %0, %1, %2, %3 offen sc1" : "=v"(dst) : "v"(voff), "s"(rs), "s"(soff) : "memory")
    {
      PF_ADDR;
      kvok = KV_SEGS(kfl);
      if (kvok <= 0) WAIT_KV(0);
      LOAD_KV(0);
      LD_ASYNC(pz, kvok > 1 && tz < 32 ? tz * 128 : 0x7ffffff0, rs_x, (2 + (w & 1)) * QBLK + ((w >> 1) ? xv : xk));
    }
    const float QS = 0.08838834764831845f * 1.4426950408889634f;
    const unsigned j0 = lq == (l15 >> 2) && !(l15 & 1) ? 0x3f803f80u : 0u, j1 = lq == (l15 >> 2) && (l15 & 1) ? 0x3f803f80u : 0u;
    const v4u JK = {j0, j1, j0, j1};
    const unsigned vxp = lb0 + w * 40960 + QK_KV + 2048 + ((l15 & 3) * 16 + ((4 * lq + (l15 >> 2)) ^ ((l15 & 1) << 3))) * 16;
    const unsigned p1 = lb0 + (w ^ 1) * 40960 + lane * 16, p2 = lb0 + (w ^ 2) * 40960 + lane * 16, p3 = lb0 + (w ^ 3) * 40960 + lane * 16;
    unsigned pot = 0, d1p = 140;
    const int mlo = mem_lane_off - dbl(w * 4096) + (w & 1) * 16384 + (w >> 1) * 1024, hoff = dbl(h * 2048) + w * 512 + lane * 4;
    v4u vm = {0u, 0u, 0u, 0u};
    const int alo = lane < 16 && w == 3 ? ALM + lane * 128 : (lane >= 62 ? RECO + (lane - 62) * 4 : 0x7ffffff0);
    for (int s = 0; s < NSEG; ++s) {
      int mb = RBASE(s) + ROFF_M, vb = RBASE(s) + ROFF_V, ob = RBASE(s) + ROFF_O, mb0 = mb + xr * 2048;
      unsigned al, ann, fl; unsigned long long bad, bad1, b2, b3, po_;
      if (__builtin_expect(s == 1 && w == 3, 0)) {
        unsigned recD; rec_get(tA, recD, rs_m, mb + RECO, tmo); tA -= (recD & 0xffffu) + (recD >> 16);
      }
      { int rem = (int)(tA - rtnow());
        if (s == 0 || rem > 20000) tA -= rem; }
      tA = __builtin_amdgcn_readfirstlane(tA); d1p = __builtin_amdgcn_readfirstlane(d1p);
#define QK_STO
      { unsigned cnt; unsigned long long en;
        asm volatile(QK_STEP
          : "=&{v[136:167]}"(wv_[4]), "=&{v[168:199]}"(wv_[5]), "=&{v[200:231]}"(wv_[6]), "+{v[232:235]}"(vm), [m] "+v"(pz), "+v"(kfl), [al] "=&v"(al),
            [bad] "=&s"(bad), [bad1] "=&s"(bad1), [b2] "=&s"(b2), [b3] "=&s"(b3), [po] "=&s"(po_), [cnt] "=&s"(cnt), [en] "=&s"(en), [ann] "=&s"(ann), [fl] "=&s"(fl) QK_STO
          : "{a[0:31]}"(wa[0]), "{a[32:63]}"(wa[1]), "{a[64:95]}"(wa[2]), "{a[96:127]}"(wa[3]), "{a[128:159]}"(wa[4]), "{a[160:191]}"(wa[5]), "{a[192:223]}"(wa[6]), "{a[224:255]}"(wa[7]),
            "{v[8:39]}"(wv_[0]), "{v[40:71]}"(wv_[1]), "{v[72:103]}"(wv_[2]), "{v[104:135]}"(wv_[3]),
            [own] "v"(own), [p1] "v"(p1), [p2] "v"(p2), [p3] "v"(p3), [vxp] "v"(vxp), [ml0] "v"(mlo), [ml1] "v"(mlo ^ 1024), [jk] "v"(JK), [ho] "v"(hoff), [idx] "v"(idx), [alo] "v"(alo),
            [rsm] "s"(rs_m), [rsc] "s"(rs_c), [mb] "s"(mb), [vb] "s"(vb), [ob] "s"(ob), [rb] "s"(RBASE(s + 2) + ROFF_O), [twin] "s"(0xccccccccccccccccull),
            [one] "s"(0x3f803f80u), [qs] "s"(QS), [qs2] "s"((unsigned long long)__float_as_uint(QS)), [hi] "s"(0x1p40f), [lo] "s"(0x1p-40f), [th] "s"(40.f), [t] "s"(tA), [w] "s"(w), [ald0] "s"(d1p + TP(P.sh0, SH0)), [ald1] "s"(d1p + TP(P.lk4, SH5)),
            [tmo] "s"((int)offsetof(Ctl, tmo)), [mb0] "s"(mb0), [mb1] "s"(mb0 ^ 2048), [mb2] "s"(mb0 ^ 4096), [mb3] "s"(mb0 ^ 6144), [ald] "s"(d1p - TP(P.sh1, SH1)), [als] "s"(mb + ALM + (alw & 15) * 128), [alx] "s"(alx)
          : "vcc", "scc", "s48", "s49", "s50", "s51", "s52", "s53", "memory"); }
      {
        const v4u Km = {wv_[4][QK_KM], wv_[4][QK_KM + 1], wv_[4][QK_KM + 2], wv_[4][QK_KM + 3]};
        if (__builtin_expect(bad != 0, 0)) {
#pragma unroll 1
          for (unsigned fails = 0; !__all((vm.x > vm.z ? vm.x : vm.z) != 0xffffffffu); ++fails) {
            __builtin_amdgcn_s_sleep(2);
            vm.x = __builtin_amdgcn_raw_buffer_load_b32(rs_m, hoff, vb, 16); vm.z = __builtin_amdgcn_raw_buffer_load_b32(rs_m, hoff, vb + 256, 16);
            if (spin_fail(fails, tmo, 1u)) break;
          }
          float sx[4], sm[2];
#pragma unroll
          for (int e = 0; e < 4; ++e) sx[e] = __uint_as_float(wv_[6][QK_SX + e]);
          sm[0] = __uint_as_float(wv_[4][QK_SM]); sm[1] = __uint_as_float(wv_[4][QK_SM + 1]);
          float mx = fmaxf(fmaxf(sx[0], sx[1]), fmaxf(sx[2], sx[3]));
          mx = fmaxf(mx, __int_as_float(__builtin_amdgcn_update_dpp(0, __float_as_int(mx), 0x4E, 0xf, 0xf, false)));
          mx = xmax32(xmax16(fmaxf(mx, fmaxf(sm[0], sm[1]))));
          if (b2 == 0) mx = 0.f;
          v4u px; px.x = pk2(__builtin_amdgcn_exp2f(sx[0] - mx), __builtin_amdgcn_exp2f(sx[1] - mx)); px.y = pk2(__builtin_amdgcn_exp2f(sx[2] - mx), __builtin_amdgcn_exp2f(sx[3] - mx));
          px.z = __builtin_amdgcn_update_dpp(0, px.x, 0x4E, 0xf, 0xf, false); px.w = __builtin_amdgcn_update_dpp(0, px.y, 0x4E, 0xf, 0xf, false);
          const v2u pm = {pk2(__builtin_amdgcn_exp2f(sm[0] - mx), __builtin_amdgcn_exp2f(sm[1] - mx)), 0u};
          const v4u on = {0x3f803f80u, 0x3f803f80u, 0x3f803f80u, 0x3f803f80u};
          const v4u vx0 = {wv_[6][QK_VX], wv_[6][QK_VX + 1], wv_[6][QK_VX + 2], wv_[6][QK_VX + 3]}, vx1 = {wv_[6][QK_VX + 4], wv_[6][QK_VX + 5], wv_[6][QK_VX + 6], wv_[6][QK_VX + 7]};
          f32x4 c0, c1, ls;
          asm volatile("s_nop 1\n\t"
                       "v_mfma_f32_16x16x32_bf16 %0, %3, %4, 0\n\t"
                       "v_mfma_f32_16x16x32_bf16 %1, %3, %5, 0\n\t"
                       "v_mfma_f32_16x16x32_bf16 %2, %3, %6, 0\n\t"
                       "s_nop 7\n\t"
                       "v_mfma_f32_16x16x16_bf16 %0, %7, v[232:233], %0\n\t"
                       "v_mfma_f32_16x16x16_bf16 %1, %7, v[234:235], %1\n\t"
                       "v_mfma_f32_16x16x16_bf16 %2, %7, v[168:169], %2\n\t"
                       "s_nop 7"
                       : "=&v"(c0), "=&v"(c1), "=&v"(ls) : "v"(px), "v"(vx0), "v"(vx1), "{v[168:171]}"(on), "v"(pm), "{v[232:235]}"(vm));
          const float i0 = __builtin_amdgcn_rcpf(ls[0]), i1 = __builtin_amdgcn_rcpf(ls[1]);
          __builtin_amdgcn_raw_buffer_store_b32(nosent(pk2(c0[0] * i0, c1[0] * i0)), rs_m, hoff, ob, 16);
          __builtin_amdgcn_raw_buffer_store_b32(nosent(pk2(c0[1] * i1, c1[1] * i1)), rs_m, hoff + 256, ob, 16);
          po_ = rtnow();
        }
        SB0;
        const unsigned po = __builtin_amdgcn_readfirstlane((unsigned)po_);
        fl = __builtin_amdgcn_readfirstlane(fl | (bad != 0 && b2 == 0));
        if (QGAP) __builtin_amdgcn_s_sleep(QGAP);
        SB0;
        __builtin_amdgcn_raw_buffer_store_b32(po - tA, rs_m, (h | (w ^ 3) | opaque_lane() | fl) == 0 ? RECO + 128 : 0x7ffffff0, mb, 16);
      { const int lz = opaque_lane(), kb = xm + s * 2 * QBLK, rb = RBASE(s + 2);
        __builtin_amdgcn_raw_buffer_store_b32(vm.x, rs_x, lz * 4 + 2048, kb, 16); __builtin_amdgcn_raw_buffer_store_b32(vm.z, rs_x, lz * 4 + 2304, kb, 16);
        __builtin_amdgcn_raw_buffer_store_b128((v4u){nosent(Km.x), nosent(Km.y), nosent(Km.z), nosent(Km.w)}, rs_x, (lz & 2) ? 0x7ffffff0 : (2 * (lz >> 2) + (lz & 1)) * 16, kb, 16);
        __builtin_amdgcn_raw_buffer_store_b128((v4u){~0u, ~0u, ~0u, ~0u}, rs_m, (alw & 3) != 0 ? 0x7ffffff0 : lz < 32 ? ROFF_M + ALM + alw * 128 + lz * 16 : (lz == 32 && b == 0 ? ROFF_M + RECO + 128 : 0x7ffffff0), rb, 16); }
        asm volatile("s_waitcnt vmcnt(%1)" : "+v"(al) : "i"(QK_ALW + 4) : "memory");
        pot = po;
      if (s < NSEG - 1) {
        unsigned recD = __builtin_amdgcn_readlane(al, 63);
        tA = __builtin_amdgcn_readlane(al, 62);
        if (s > 0) {
          if (__builtin_expect(tA == 0xffffffffu || recD == 0xffffffffu, 0)) rec_get(tA, recD, rs_m, mb + RECO, tmo);
          d1p = recD & 0xffffu;
          unsigned tB = tA - (recD >> 16);
          (void)al_eval(al, tB);
          if (__builtin_expect((int)(pot + TP(P.sh3, SH3) - tB) > 0, 0)) tB = pot + TP(P.sh3, SH3);
          tA = tB + (recD >> 16);
        } else tA = pot;
        PF_ADDR;
        kvok = max(kvok, KV_SEGS(kfl));
        if (__builtin_expect(kvok <= s + 1, 0)) WAIT_KV(s + 1);
        LOAD_KV(s + 1);
        LD_ASYNC(kfl, kvok < s + 5 && kvok < NSEG && tz < 2 ? tz * 4096 : 0x7ffffff0, rs_c, (int)offsetof(Ctl, prg) + h * 128);
        const int po2 = s + 2 < kvok && !(tz & 32) ? tz * 128 : 0x7ffffff0;
        LD_ASYNC(pz, po2, rs_x, (2 * (s + 2) + (w & 1)) * QBLK + ((w >> 1) ? xv : xk));
      }
      }
    }
    check_in(P, tag, w);
  } else {
    LANE_CONSTS;
    const unsigned c00 = (unsigned)now(), t00 = rtnow();
    const int c = b - NQK, cg = c >> 1, rh = c & 1;
    v32u wa[8], wv_[4];
#define VCH(j) (8 * (w & 1) + 2 * ((j) ^ xq) + (w >> 1))
#define OHEAD(i) (4 * ((i) ^ (2 * xq)) + w)
    {
      const unsigned stg = (unsigned)(size_t)Lraw + w * 32768;
      const unsigned vb = stg + wrow * 128 + (l15 & 3) * 8 + ((w ^ 3 ^ wsw) << 5);
      const __amdgpu_buffer_rsrc_t rv = __builtin_amdgcn_make_buffer_rsrc((void*)(P.Wv + 64 * c), 0, DM * HDA * 2 - 128 * c, 0x00020000);
      int t_, ho, n_;
      asm volatile(VO_LOADER : VO_DEFW, [t] "=&s"(t_), [ho] "=&s"(ho), [n] "=&s"(n_), [rec] "=&v"(rdy)
        : [sv] "v"((lane >> 3) * HDA * 2 + (((((lane & 7) >> 1) ^ ((lane >> 4) & 3)) << 1) | (lane & 1)) * 16), [so] "v"((lane >> 3) * DM * 2 + (lane & 7) * 16), [vb0] "v"(vb), [vb1] "v"(vb ^ 32), [vb2] "v"(vb ^ 64), [vb3] "v"(vb ^ 96),
          [ob] "v"(stg + (32 * (lq >> 1) + 2 * (lq & 1) + 4 * (l15 >> 3) + 16 * ((l15 >> 2) & 1)) * 128 + (cg & 1) * 64 + (l15 & 3) * 8),
          [ro] "v"(lane * 128), [rr] "s"(rs_r), [stg] "s"(stg), [rv] "s"(rv), [vk] "s"(VCH(0) * 0x80000), [wo] "s"(P.Wo + 64 * (cg >> 1)), [wsz] "s"(HDA * DM * 2 - 128 * (cg >> 1)), [hx] "s"(OHEAD(0) * 0x40000), [k0] "s"(c & 3)
        : "s48", "s49", "s50", "s51", "s52", "s53", "scc", "memory");
    }
    asm volatile("" :: "v"(cz));
    const int rpl = (int)(rdy_wait(P, rdy, tag, pk) >> 63) * 8192;
    const unsigned lb0 = (unsigned)(size_t)Lraw;
    unsigned vw[3];
#pragma unroll
    for (int t = 0; t < 3; ++t) vw[t] = lb0 + t * 2064 + ((t ^ w ^ 3) * 64 + lane) * 8;
    const unsigned vr = lb0 + tid * 8, rw = lb0 + 8192 + tid * 8, fa = lb0 + 16384;
    int oo[8];
#pragma unroll
    for (int i = 0; i < 8; ++i) oo[i] = dbl(OHEAD(i) * 2048) + ((lq * 2 + rh) * 16 + l15) * 16;
    int ga[4];
#pragma unroll
    for (int j = 0; j < 4; ++j) ga[j] = mem_lane_off - dbl(w * 4096) + dbl(VCH(j) * 1024);
    const int vo = dbl(c * 1024) + tid * 4, mo = dbl(c * 256) + (32 * (l15 >> 3) + 8 * lq + (l15 & 7)) * 4;
    const bool keeper = (c == 0 && w == 0);
    int d1 = TP(P.dmv, DMV) * 16, d2 = TP(P.dov, DOV) * 16, m1 = TP(P.im, IMARG) * 16, m2 = TP(P.im, IMARG) * 16;
    int LV = 70;
    bool prev_clean = false; int nbad = 0;
    unsigned recD = 140;
    const float tpc = 16.f * (float)(rtnow() - t00) / (float)((unsigned)now() - c00);
    int e1 = __builtin_amdgcn_readfirstlane((int)(TP(P.dmv, DMV) * tpc)), e2 = __builtin_amdgcn_readfirstlane((int)(TP(P.dov, DOV) * tpc));
    d1 = e1 + m1 + TP(P.dq2, DQ2) * 16; d2 = e2 + m2 + TP(P.dq, DQ) * 16;
    if (tid < 32) ((int*)(Lraw + 16384))[tid] = 0;
    const int alk = !keeper ? 0x7ffffff0 : (lane < 24 ? ALM + lane * 128 : (lane == 63 ? RECO + 128 : 0x7ffffff0));
    const int al1 = lane < 16 ? ALM + lane * 128 : 0x7ffffff0;
    const int rso = w == 1 ? ROFF_V + dbl(c * 1024) + lane * 16 : (w != 2 ? 0x7ffffff0 : (lane < 16 ? ROFF_M + dbl(c * 256) + lane * 16 :
                    (lane >= 32 && alw >= 0 ? ROFF_M + ALM + (alw - 6) * 128 + lane * 16 : (lane == 16 && c == 0 ? ROFF_M + RECO : 0x7ffffff0))));
    const int boot = c == 0 ? TP(P.pad, BOOT) & 255 : TP(P.pad, BOOT) >> 8;
    unsigned tB = 0; int mb = RBASE(0) + ROFF_M, ob = 0;
    for (int s = 0; s < NSEG; ++s) {
      unsigned alr, pm, fw; unsigned long long te; v2u rec;
      const int mbn = RBASE(s) + ROFF_M, d2r = recD >> 16;
#define VO_ST0
#define VO_ST1
#define VO_ST3
#define VO_STO
      { unsigned cnt; unsigned long long en;
        asm volatile(VO_BODY : [al] "=&v"(alr), [rec] "=&v"(rec), [te] "=&s"(te), [cnt] "=&s"(cnt), [en] "=&s"(en), [t] "+s"(tB), [ta] "=&s"(tA), [pm] "=&s"(pm), [fw] "=&s"(fw) VO_STO
          : VO_W, [oo0] "v"(oo[0]), [oo1] "v"(oo[1]), [oo2] "v"(oo[2]), [oo3] "v"(oo[3]), [oo4] "v"(oo[4]), [oo5] "v"(oo[5]), [oo6] "v"(oo[6]), [oo7] "v"(oo[7]), [idx] "v"(idx), [rw] "v"(rw), [mo] "v"(mo), [fa] "v"(fa), [l4] "v"(lane * 4), [al1] "v"(al1),
            [ga0] "v"(ga[0]), [ga1] "v"(ga[1]), [ga2] "v"(ga[2]), [ga3] "v"(ga[3]), [alo] "v"(alk), [vw0] "v"(vw[0]), [vw1] "v"(vw[1]), [vw2] "v"(vw[2]), [vr] "v"(vr), [vo] "v"(vo), [rso] "v"(rso), [rb] "s"(RBASE(s + 2)),
            [w] "s"(w), [rsm] "s"(rs_m), [rsc] "s"(rs_c), [ob] "s"(ob), [mb] "s"(mb), [mbn] "s"(mbn), [vb] "s"(RBASE(s) + ROFF_V), [rco] "s"(mbn + RECO), [tmo] "s"((int)offsetof(Ctl, tmo)), [s1] "s"(s),
            [lk] "s"(TP(P.lk1, LK1)), [als] "s"(mb + ALM + (alw & 31) * 128), [alx] "s"(alx), [dv] "s"(TP(P.dv, DVV)),
            [d2] "s"(d2r), [mn] "s"(s == 1 ? boot : 10), [shf] "s"(s == 1 ? boot : TP(P.sh4, SH4)), [c] "s"(c), [pw] "s"(__builtin_amdgcn_readfirstlane(s == 0 ? 1 : w ^ 3))
          : VO_CLOB, "vcc", "scc", "memory"); }
      const unsigned tv = __builtin_amdgcn_readfirstlane((unsigned)te);
      if (c == 2 && tid == 0 && s > 0) __hip_atomic_store((unsigned long long*)&P.ctl->prog[0], (unsigned)(s - 1) | ((unsigned long long)(rec.x + (rec.y & 0xffffu)) << 32), RLX_AGENT);
      if (keeper && s > 0) {
        const v4u kz = *(const v4u*)(Lraw + 16384 + 144); pm = kz.x; tB = kz.y; fw = kz.z; tA = kz.w;
        const unsigned long long f = __ballot((alr >> 16) == 1u);
        const bool kclean = __builtin_amdgcn_readfirstlane(fw) != s, okA = (f & 0xffff) == 0, okB = kclean && ((f >> 16) & 255) == 0;
        const int kx1 = __builtin_amdgcn_readlane(alr, 63), kx2 = __builtin_amdgcn_readfirstlane(pm - tB);
        {
          const int gain = s < 2 ? 0 : (s < 5 ? TP(P.gain0, GAIN0) : TP(P.gain, GAIN)), inc = s < 2 ? 0 : TP(P.inc, MINC);
          e1 += okA && kx1 != -1 ? (min(kx1 * 16 - e1, 320) * gain) >> 4 : 0;
          e2 += kclean ? (min(kx2 * 16 - e2, 320) * gain) >> 4 : 0;
          nbad = okA && okB ? 0 : nbad + 1;
          const int far = nbad >= 4 ? 2 * inc : 0;
          m1 = min(max(m1 + far + (!okA ? 0 : (okB ? -TP(P.dec, MDEC) - (m1 >> 3) : inc)), 0), 800);
          m2 = min(max(m2 + far + (okA ? -TP(P.dec, MDEC) - (m2 >> 3) : (prev_clean ? inc : 0)), 0), 800);
          d1 = e1 + m1 + TP(P.dq2, DQ2) * 16; d2 = e2 + m2 + TP(P.dq, DQ) * 16;
        }
        prev_clean = okB;
      }
      if (keeper) {
        const unsigned r1 = (d1 >> 4) + (s < 2 ? (TP(P.boot2, BOOT2) >> (8 * s)) & 255 : 0), r2 = (d2 >> 4) + (s < 2 ? (TP(P.boot3, BOOT3) >> (8 * s)) & 255 : 0);
        *(v2u*)(Lraw + 16384 + 128) = (v2u){r1 + r2, r1 | (r2 << 16)};
      }
      tB = tv; recD = 0;
      if (s > 0 && w == 3) {
        unsigned recA = __builtin_amdgcn_readfirstlane(rec.x); recD = __builtin_amdgcn_readfirstlane(rec.y);
        if (__builtin_expect(recA == 0xffffffffu || recD == 0xffffffffu, 0)) rec_get(recA, recD, rs_m, mbn + RECO, tmo);
        tB = recA - (recD >> 16);
        if ((int)(tB - tv) > 20000) tB = tv;
      }
      tB = __builtin_amdgcn_readfirstlane(tB); recD = __builtin_amdgcn_readfirstlane(recD);
      mb = mbn; ob = RBASE(s) + ROFF_O;
    }
    check_in(P, tag, w);
  }
}

extern "C" void kernel_launch(void* const* d_in, const int* in_sizes, int n_in,
                              void* d_out, int out_size, void* d_ws, size_t ws_size,
                              hipStream_t stream) {
  const u16* A_x = (const u16*)d_in[0];
  const u16* A_mem0 = (const u16*)d_in[1];
  const u16* A_Wq = (const u16*)d_in[2];
  const u16* A_Wk = (const u16*)d_in[3];
  const u16* A_Wv = (const u16*)d_in[4];
  const u16* A_Wo = (const u16*)d_in[5];
  const u16* B_x = (const u16*)d_in[7];
  const u16* B_wqkv = (const u16*)d_in[8];
  const u16* B_wo = (const u16*)d_in[9];
  u16* out = (u16*)d_out;

  char* ws = (char*)d_ws;
  size_t off = 0;
  auto alloc = [&](size_t bytes) { char* p = ws + off; off += (bytes + 255) & ~(size_t)255; return p; };
  char* rdy = alloc(RDY_SIZE);
  Ctl* ctl = (Ctl*)alloc(sizeof(Ctl));
  unsigned long long* prof = (unsigned long long*)alloc(96 * 4 * 72 * 8 + 8 * 256 * 16 + 64 * 384 * 8);
  off += (size_t)(-(long long)(size_t)(ws + off)) & (QPAGE - 1);
  char* ring = alloc(QPAGE);
  u16* qkvA = (u16*)alloc(QSIZE);

  FParams cp;
  cp.Ax = A_x; cp.mem0 = A_mem0; cp.Bx = B_x; cp.Wq = A_Wq; cp.Wk = A_Wk; cp.Wv = A_Wv; cp.Wo = A_Wo; cp.wqkvB = B_wqkv; cp.woB = B_wo;
  cp.qkvA = qkvA; cp.out = out; cp.ring = ring; cp.rdy = rdy;
  cp.ctl = ctl; cp.prof = prof;
  k_fused<<<256, 256, 0, stream>>>(cp);
}
```
